```python
import torch

def _softmax(x, axis=-1):
    return torch.softmax(x, dim=axis)

def _gelu(x):
    return torch.nn.functional.gelu(x, approximate='tanh')

def _silu(x):
    return torch.nn.functional.silu(x)

def _sigmoid(x):
    return torch.sigmoid(x)

def _layernorm(x, axis=-1, eps=1e-05):
    m = x.mean(dim=axis, keepdim=True)
    v = x.var(dim=axis, keepdim=True, unbiased=False)
    return (x - m) / torch.sqrt(v + eps)

def _scatter_add(tgt, idx, src, axis=0):
    return tgt.clone().index_add_(axis, idx, src)

def _segment_sum(src, seg, n):
    out = torch.zeros((n,) + src.shape[1:], dtype=src.dtype)
    return out.index_add_(0, seg, src)

def _pad(x, pw):
    flat = tuple((v for pair in reversed(pw) for v in pair))
    return torch.nn.functional.pad(x, flat)
kernel_name = 'adaLN_cond__memory_aug_recurrent_mem_tr__moe_arch_mod__prompt'

def setup_inputs():

    _w = lambda a: (lambda t: t.to(torch.bfloat16) if t.is_floating_point() else t)(torch.as_tensor(a))

    def _A_setup():
        B, S, Dm = (1, 2048, 640)
        H, Dh = (32, 192)
        M, seg = (8, 32)
        seg = min(seg, S)
        S = S // seg * seg
        return {'x': _w((lambda *s: torch.randn(*s, dtype=torch.bfloat16) * 0.05)(B, S, Dm)), 'mem0': _w((lambda *s: torch.randn(*s, dtype=torch.bfloat16) * 0.4)(B, M, Dm)), 'Wq': _w((lambda *s: torch.randn(*s, dtype=torch.bfloat16) * 3.2)(Dm, H, Dh) * Dm ** (-0.5)), 'Wk': _w((lambda *s: torch.randn(*s, dtype=torch.bfloat16) * 3.2)(Dm, H, Dh) * Dm ** (-0.5)), 'Wv': _w((lambda *s: torch.randn(*s, dtype=torch.bfloat16) * 0.4)(Dm, H, Dh) * Dm ** (-0.5)), 'Wo': _w((lambda *s: torch.randn(*s, dtype=torch.bfloat16) * 0.4)(H, Dh, Dm) * (H * Dh) ** (-0.5)), 'seg_len': seg}
    _A_in = _A_setup()

    def _B_setup():
        B, S, D, F, C = (1, 2048, 640, 1536, 8)
        return {'x': _w((lambda *s: torch.randn(*s, dtype=torch.bfloat16) * 0.05)(B, S, D)), 'w_r': _w((lambda *s: torch.randn(*s, dtype=torch.bfloat16) * 0.05)(D)), 'w_gate': _w((lambda *s: torch.randn(*s, dtype=torch.bfloat16) * 0.05)(D, F)), 'w_up': _w((lambda *s: torch.randn(*s, dtype=torch.bfloat16) * 0.05)(D, F)), 'w_down': _w((lambda *s: torch.randn(*s, dtype=torch.bfloat16) * 0.05)(F, D))}
    _B_in = _B_setup()
    return {'A_x': _A_in['x'], 'A_mem0': _A_in['mem0'], 'A_Wq': _A_in['Wq'], 'A_Wk': _A_in['Wk'], 'A_Wv': _A_in['Wv'], 'A_Wo': _A_in['Wo'], 'A_seg_len': _A_in['seg_len'], 'B_x': _B_in['x'], 'B_w_r': _B_in['w_r'], 'B_w_gate': _B_in['w_gate'], 'B_w_up': _B_in['w_up'], 'B_w_down': _B_in['w_down'], 'c': _w((lambda *s: torch.randn(*s, dtype=torch.bfloat16) * 0.05)(1, 640) * 8.0), 'w_mod': _w((lambda *s: torch.randn(*s, dtype=torch.bfloat16) * 0.05)(640, 6 * 640) * 640 ** (-0.5) * 8.0)}

def reference(A_x, A_mem0, A_Wq, A_Wk, A_Wv, A_Wo, A_seg_len, B_x, B_w_r, B_w_gate, B_w_up, B_w_down, c, w_mod):

    def _A_ref(x, mem0, Wq, Wk, Wv, Wo, seg_len):
        B, S, Dm = x.shape
        H, Dh = (Wq.shape[1], Wq.shape[2])
        M = mem0.shape[1]
        scale = Dh ** (-0.5)
        n_seg = S // seg_len
        L = M + seg_len
        idx = torch.arange(L)
        mask = (idx[:, None] < M) | (idx[:, None] >= idx[None, :])
        mem = mem0
        outs = []
        for s in range(n_seg):
            xs = x[:, s * seg_len:(s + 1) * seg_len, :].clone()
            h = torch.concatenate([mem, xs], axis=1)
            q = torch.einsum('btd,dhe->bhte', h, Wq)
            k = torch.einsum('btd,dhe->bhte', h, Wk)
            v = torch.einsum('btd,dhe->bhte', h, Wv)
            scores = torch.einsum('bhqe,bhke->bhqk', q, k) * scale
            scores = torch.where(mask[None, None], scores, -1000000000.0)
            p = _softmax(scores, axis=-1)
            o = torch.einsum('bhqk,bhke->bhqe', p, v)
            o = torch.einsum('bhte,hed->btd', o, Wo)
            mem = o[:, :M, :].clone()
            outs.append(o[:, M:, :])
        return torch.concatenate(outs, axis=1)

    def _B_ref(x, w_r, w_gate, w_up, w_down):
        B, S, D = x.shape
        C = 8
        r = x @ w_r
        idx = torch.argsort(-r, axis=-1)[:, :C]
        idx_e = (lambda x, n, axis: torch.repeat_interleave(x, n, dim=axis))(idx.reshape(B, C, 1), D, axis=2)
        x_sel = (lambda x, i, axis: torch.take_along_dim(x, i, dim=axis))(x, idx_e, axis=1)
        g = _silu(x_sel @ w_gate)
        u = x_sel @ w_up
        y = g * u @ w_down
        r_sel = _sigmoid((lambda x, i, axis: torch.take_along_dim(x, i, dim=axis))(r, idx, axis=1))
        y = y * r_sel.reshape(B, C, 1)
        b_off = (torch.arange(B) * S).reshape(B, 1)
        flat_idx = (idx + b_off).reshape(B * C)
        zero = (x * 0.0).reshape(B * S, D)
        upd = _scatter_add(zero, flat_idx, y.reshape(B * C, D), axis=0)
        return x + upd.reshape(B, S, D)
    x = A_x
    mod = (_silu(c) @ w_mod).reshape(c.shape[0], 6, 1, -1)
    sa, ca, ga, sb, cb, gb = (mod[:, 0].clone(), mod[:, 1].clone(), mod[:, 2].clone(), mod[:, 3].clone(), mod[:, 4].clone(), mod[:, 5].clone())
    h = _layernorm(x, axis=-1) * (1 + ca) + sa
    x = x + ga * _A_ref(h, A_mem0, A_Wq, A_Wk, A_Wv, A_Wo, A_seg_len)
    h = _layernorm(x, axis=-1) * (1 + cb) + sb
    x = x + gb * _B_ref(h, B_w_r, B_w_gate, B_w_up, B_w_down)
    return x
```

```cpp
#define CHAIN_STATS
#include <hip/hip_runtime.h>
#include <hip/hip_bf16.h>
#include <stdint.h>

#define GAS __attribute__((address_space(1)))
#define LAS __attribute__((address_space(3)))
#define RLX_AGENT __ATOMIC_RELAXED, __HIP_MEMORY_SCOPE_AGENT
typedef unsigned short u16;
typedef unsigned v4u __attribute__((ext_vector_type(4)));
typedef unsigned v2u __attribute__((ext_vector_type(2)));
typedef unsigned v8u __attribute__((ext_vector_type(8)));
typedef float f4 __attribute__((ext_vector_type(4)));
typedef __bf16 bf2 __attribute__((ext_vector_type(2)));
typedef __bf16 bf8 __attribute__((ext_vector_type(8)));
typedef __bf16 bf16v __attribute__((ext_vector_type(16)));
typedef short s4 __attribute__((ext_vector_type(4)));
typedef GAS unsigned gu32;
typedef __amdgpu_buffer_rsrc_t rsrc_t;

constexpr int S = 2048, DM = 640, NH = 32, DH = 192, HD = NH * DH, NSEG = 64, FF = 1536, TOPK = 8;

constexpr size_t MB = 1u << 20;
constexpr int RING = 30, CHUNK = 6, XB = 2 * RING;
#define HSV 56
constexpr int HS = HSV;
constexpr size_t HAND_BYTES = 124 * MB;
constexpr size_t CTRL_OFF = HAND_BYTES;
constexpr unsigned CTRL_BYTES = 131072;
constexpr size_t MODB_OFF = CTRL_OFF + CTRL_BYTES;
constexpr size_t HF_OFF = MODB_OFF + 8192;
constexpr size_t HF_BYTES = (size_t)S * DM * 2;
constexpr size_t X1_OFF = HF_OFF + HF_BYTES;
constexpr size_t R_OFF = X1_OFF + 2 * HF_BYTES;
constexpr size_t ACT_OFF = R_OFF + S * 4;
constexpr size_t WS_END = ACT_OFF + TOPK * FF * 2;
static_assert(WS_END + 2 * MB <= 256 * MB, "workspace");

constexpr int C_TICK = 256;
constexpr int C_ANCH = 768;
constexpr int C_ERR = 1088;
constexpr int C_FIN = 1152;
constexpr int C_IDX = 1280;
constexpr int C_LN = 3072;
constexpr int C_STAT = 3584;
constexpr int C_AF = 12288;
constexpr int C_RD = 14336;
constexpr int C_ZERO = 16384;

#define FFN_WAIT 100
#define RPOLL 16
#define RTK 13
#define PREF 0
#define DRY 1
#define RSC 256.f
#define QUIET 0
#define HPOLL 8
constexpr int MCOPY = 4;
constexpr int REC = 2304;
constexpr int M_OFF = 0, MC = 12288, P_OFF = MCOPY * MC, Q_OFF = P_OFF + 80 * 4096, BLK_END = Q_OFF + NH * 4 * REC;
constexpr int MEM0_PHYS = XB << 21;
constexpr int N_PP = 0x80000, N_CF = N_PP + 32768, N_REC = N_CF + 8192, N_END = N_REC + 16384;
static_assert(BLK_END <= 0x100000 && BLK_END % 1024 == 0 && BLK_END / 1024 <= 768 && P_OFF % 8192 == 0, "block");
__host__ __device__ constexpr int lmap(int off) { return ((off >> 13) << 14) | (off & 8191); }
constexpr int OOB = 0x40000000;

__device__ __forceinline__ float u2f(unsigned v) { return __builtin_bit_cast(float, v); }
__device__ __forceinline__ unsigned f2u(float v) { return __builtin_bit_cast(unsigned, v); }
__device__ __forceinline__ float bf2f(u16 v) { return __builtin_bit_cast(float, (unsigned)v << 16); }
__device__ __forceinline__ float lo2f(unsigned v) { return __builtin_bit_cast(float, v << 16); }
__device__ __forceinline__ float hi2f(unsigned v) { return __builtin_bit_cast(float, v & 0xffff0000u); }
__device__ __forceinline__ unsigned pack2(float a, float b) { bf2 v = {(__bf16)a, (__bf16)b}; return __builtin_bit_cast(unsigned, v); }
__device__ __forceinline__ float rbf(float a) { return (float)(__bf16)a; }
__device__ __forceinline__ unsigned long long uni64(unsigned long long a) {
  return (unsigned)__builtin_amdgcn_readfirstlane((unsigned)a) | (unsigned long long)(unsigned)__builtin_amdgcn_readfirstlane((unsigned)(a >> 32)) << 32;
}
__device__ __forceinline__ rsrc_t mkrsrc(const void* p, unsigned bytes) { return __builtin_amdgcn_make_buffer_rsrc((void*)uni64((unsigned long long)p), 0, bytes, 0x00020000); }
__device__ __forceinline__ unsigned xcc_id() { return (unsigned)__builtin_amdgcn_s_getreg(20 | (3 << 11)) & 0xfu; }
__device__ __forceinline__ int rfl(int v) { return __builtin_amdgcn_readfirstlane(v); }
__device__ __forceinline__ f4 mfma32(v4u a, v4u b, f4 c) { return __builtin_amdgcn_mfma_f32_16x16x32_bf16(__builtin_bit_cast(bf8, a), __builtin_bit_cast(bf8, b), c, 0, 0, 0); }
__device__ __forceinline__ f4 mfma16(v2u a, v2u b, f4 c) { return mfma32(v4u{a[0], a[1], 0u, 0u}, v4u{b[0], b[1], 0u, 0u}, c); }
__device__ __forceinline__ f4 smfma(v4u a, v8u b, f4 c, int idx) { return __builtin_amdgcn_smfmac_f32_16x16x64_bf16(__builtin_bit_cast(bf8, a), __builtin_bit_cast(bf16v, b), c, idx, 0, 0); }
__device__ __forceinline__ v2u trread(unsigned addr) { return __builtin_bit_cast(v2u, __builtin_amdgcn_ds_read_tr16_b64_v4i16((LAS s4*)addr)); }
__device__ __forceinline__ v4u lds128(unsigned addr) { return *(LAS v4u*)addr; }
__device__ __forceinline__ v2u lds64(unsigned addr) { return *(LAS v2u*)addr; }
__device__ __forceinline__ void dma16(rsrc_t r, int voff, int soff, unsigned ldsaddr) {
  asm volatile("s_mov_b32 m0, %0\n\ts_nop 3\n\tbuffer_load_dwordx4 %1, %2, %3 offen lds" ::"s"(ldsaddr), "v"(voff), "s"(r), "s"(soff) : "memory");
}
__device__ __forceinline__ void dma16_sc1(rsrc_t r, int voff, int soff, unsigned ldsaddr) {
  asm volatile("s_mov_b32 m0, %0\n\ts_nop 3\n\tbuffer_load_dwordx4 %1, %2, %3 offen sc1 lds" ::"s"(ldsaddr), "v"(voff), "s"(r), "s"(soff) : "memory");
}
#define VMCNT(n) asm volatile("s_waitcnt vmcnt(" #n ")" ::: "memory")
#define LGKM0() asm volatile("s_waitcnt lgkmcnt(0)" ::: "memory")
#define BAR() asm volatile("s_barrier" ::: "memory")
__device__ __forceinline__ float swapsum32(float a, float b) {
  auto r = __builtin_amdgcn_permlane32_swap(__builtin_bit_cast(unsigned, a), __builtin_bit_cast(unsigned, b), false, false);
  return u2f(r[0]) + u2f(r[1]);
}
__device__ __forceinline__ float swapsum16(float a, float b) {
  auto r = __builtin_amdgcn_permlane16_swap(__builtin_bit_cast(unsigned, a), __builtin_bit_cast(unsigned, b), false, false);
  return u2f(r[0]) + u2f(r[1]);
}
__device__ __forceinline__ float max_x32(float a) {
  auto r = __builtin_amdgcn_permlane32_swap(__builtin_bit_cast(unsigned, a), __builtin_bit_cast(unsigned, a), false, false);
  return fmaxf(u2f(r[0]), u2f(r[1]));
}
__device__ __forceinline__ float max_x16(float a) {
  auto r = __builtin_amdgcn_permlane16_swap(__builtin_bit_cast(unsigned, a), __builtin_bit_cast(unsigned, a), false, false);
  return fmaxf(u2f(r[0]), u2f(r[1]));
}
__device__ __forceinline__ unsigned ror8u(unsigned v) { return (unsigned)__builtin_amdgcn_update_dpp(0, (int)v, 0x128, 0xf, 0xf, true); }
__device__ __forceinline__ float ror8f(float v) { return __builtin_bit_cast(float, ror8u(__builtin_bit_cast(unsigned, v))); }
__device__ __forceinline__ float wave_sum(float v) {
  v += u2f(__builtin_amdgcn_update_dpp(0, (int)f2u(v), 0xb1, 0xf, 0xf, true)); v += u2f(__builtin_amdgcn_update_dpp(0, (int)f2u(v), 0x4e, 0xf, 0xf, true));
  v += u2f(__builtin_amdgcn_update_dpp(0, (int)f2u(v), 0x124, 0xf, 0xf, true)); v += ror8f(v);
  v = swapsum16(v, v);
  return swapsum32(v, v);
}
typedef unsigned short us2 __attribute__((ext_vector_type(2)));
__device__ __forceinline__ unsigned unsent(unsigned w) { return __builtin_bit_cast(unsigned, __builtin_elementwise_min(__builtin_bit_cast(us2, w), us2{0xfffe, 0xfffe})); }

constexpr unsigned CODE_BYTES = 81920;
struct Params {
  const u16 *x, *mem0, *wq, *wk, *wv, *wo, *wr, *wg, *wu, *wd, *c, *wmod;
  u16* out;
  unsigned char* ws;
  int dQ, dP, dM, dS, lead, pad;
};

__global__ void __launch_bounds__(256) k_pre(Params p) {
  const int tid = threadIdx.x, b = blockIdx.x;
  gu32* ctrl = (gu32*)(p.ws + CTRL_OFF);
  if (b < 64) {
    if (tid < 64) {
      rsrc_t r = mkrsrc(p.ws, (unsigned)HAND_BYTES);
      unsigned best[2] = {1u << 30, 1u << 30};
#pragma unroll 1
      for (int rep = 0; rep < 6; ++rep) {
        const int off = tid < 8 && b <= 2 * RING ? (b << 21) + 0x1FC000 + (rep & 1) * 8192 + tid * 16 : OOB;
        const unsigned long long t1 = __builtin_amdgcn_s_memtime();
        LGKM0();
        __builtin_amdgcn_raw_buffer_store_b128(v4u{1u, 2u, 3u, 4u}, r, off, 0, 16);
        VMCNT(0);
        const unsigned d = (unsigned)(__builtin_amdgcn_s_memtime() - t1);
        if (rep & 1) best[1] = min(best[1], d); else best[0] = min(best[0], d);
      }
      if (tid == 0) p.ws[CTRL_OFF + b] = (best[1] < best[0] ? 1u : 0u) ^ (xcc_id() >> 2 & 1);
    }
    return;
  }
  if (b < 80) {
    const int i = (b - 64) * 256 + tid;
    {
      unsigned long long pc;
      asm volatile("s_getpc_b64 s[40:41]\n\ts_add_u32 s40, s40, _Z6k_main6Params@rel32@lo+4\n\ts_addc_u32 s41, s41, _Z6k_main6Params@rel32@hi+12" : "={s[40:41]}"(pc) :: "scc");
      const rsrc_t rcode = mkrsrc((const void*)pc, CODE_BYTES);
      unsigned x = 0;
#pragma unroll
      for (int k = 0; k < 2; ++k) x ^= __builtin_amdgcn_raw_buffer_load_b32(rcode, (((b >> 3) & 1) * 512 + k * 256 + tid) * 128, 0, 0);
      asm volatile("" ::"v"(x));
    }
    const v4u z = {0u, 0u, 0u, 0u}, m = {~0u, ~0u, ~0u, ~0u};
    if (i >= 16 && i < C_ZERO / 4) *(v4u*)(p.ws + CTRL_OFF + i * 16) = z;
    for (int k = i; k < (N_END - N_PP) / 8; k += 4096) *(v4u*)(p.ws + MEM0_PHYS + 2 * N_PP + k * 16) = z;
    if (i < S / 4 + TOPK * FF / 8) *(v4u*)(p.ws + R_OFF + i * 16) = m;
    if (i < MCOPY * 640) {
      const int o = i % 640, l = o & 63, t = o >> 6;
      const v4u x = *(const v4u*)(p.mem0 + (l & 7) * DM + 64 * t + 16 * (l >> 4) + 8 * ((l >> 3) & 1));
      const v4u zz = {unsent(x[0]), unsent(x[1]), unsent(x[2]), unsent(x[3])};
      const int mo = lmap((i / 640) * MC + o * 16);
      *(v4u*)(p.ws + MEM0_PHYS + mo) = zz; *(v4u*)(p.ws + MEM0_PHYS + 8192 + mo) = zz;
    }
    return;
  }
  __shared__ float red[64][33];
  const int col0 = (b - 80) * 32, cg = tid & 3, rg = tid >> 2;
  v4u w[10]; u16 cv[10];
#pragma unroll
  for (int i = 0; i < 10; ++i) { w[i] = *(const v4u*)(p.wmod + (size_t)(rg + 64 * i) * 3840 + col0 + cg * 8); cv[i] = p.c[rg + 64 * i]; }
  __builtin_amdgcn_sched_barrier(0);
  float acc[8] = {};
#pragma unroll
  for (int i = 0; i < 10; ++i) {
    const float c = bf2f(cv[i]), sv = rbf(c / (1.f + __expf(-c)));
#pragma unroll
    for (int k = 0; k < 4; ++k) { acc[2 * k] += sv * lo2f(w[i][k]); acc[2 * k + 1] += sv * hi2f(w[i][k]); }
  }
#pragma unroll
  for (int k = 0; k < 8; ++k) red[rg][cg * 8 + k] = acc[k];
  __syncthreads();
  const int c = tid & 31, part = tid >> 5;
  float sum = 0.f;
#pragma unroll
  for (int i = 0; i < 8; ++i) sum += red[part * 8 + i][c];
  __syncthreads();
  red[part][c] = sum;
  __syncthreads();
  if (tid < 32) {
    sum = 0.f;
#pragma unroll
    for (int i = 0; i < 8; ++i) sum += red[i][tid];
    ((u16*)(p.ws + MODB_OFF))[col0 + tid] = (u16)pack2((col0 / DM) % 3 == 1 ? 1.f + rbf(sum) : sum, 0.f);
  }
}

constexpr int L_MEM = 0, L_V = 20480, L_QK = 13312, L_P = 14848, L_DUMP = 17408, L_BG = 32768;
constexpr int L_MISC = 122880;
constexpr int L_LN = 123904;
constexpr int MAIN_LDS = L_LN + 25600;
constexpr int SPIN_CAP = 1 << 18;
typedef unsigned v12u __attribute__((ext_vector_type(12)));
typedef unsigned v16u __attribute__((ext_vector_type(16)));
typedef unsigned v32u __attribute__((ext_vector_type(32)));

#define CHAIN_CLOBBERS \
  "v40", "v41", "v42", "v43", "v44", "v45", "v46", "v47", "v48", "v49", "v50", "v51", "v52", "v53", "v54", "v55", "v56", "v57", "v58", "v59", "v60", \
  "v61", "v62", "v63", "v64", "v65", "v66", "v67", "v68", "v69", "v70", "v71", "v72", "v73", "v74", "v75", "v76", "v77", "v78", "v79", "v80", "v81", \
  "v82", "v83", "v84", "v85", "v86", "v87", "v88", "v89", "v90", "v91", "v92", "v93", "v94", "v95", "v100", "v101", "v102", "v103", "v104", "v105", \
  "v106", "v107", "v108", "v109", "v110", "v111", "v112", "v113", "v114", "v115", "v116", "v117", "v118", "v119", "v120", "v121", "v122", "v123", \
  "v124", "v125", "v126", "v127", "v128", "v129", "v130", "v131", "v132", "v133", "v134", "v135", "v136", "v137", "v138", "v139", "v140", "v141", \
  "v142", "v143", "v144", "v145", "v146", "v147", "v148", "v149", "v150", "v151", "v152", "v153", "v154", "v155", "v156", "v157", "v158", "v159", \
  "v160", "v161", "v162", "v163", "v164", "v165", "v166", "v167", "v168", "v169", "v170", "v171", "v172", "v173", "v174", "v175", "v176", "v177", \
  "v178", "v179", "v180", "v181", "v182", "v183", "s34", "s35", "s44", "s45", "s46", "s47", "s48", "s49", "s50", "s51", "s68", "s69", "s70", "s71", \
  "s72", "s73", "s74", "s75", "s76", "s77", "s78", "s79", "s80", "s81", "s82", "s83", "s84", "s85", "s86", "s87", "s88", "s89", "a0", "a1", "a2", "a3", \
  "a4", "a5", "a6", "a7", "a8", "a9", "a10", "a11", "a12", "a13", "a14", "a15", "a16", "a17", "a18", "a19", "a20", "a21", "a22", "a23", "a24", "a25", \
  "a26", "a27", "a28", "a29", "a30", "a31", "a32", "a33", "a34", "a35", "a36", "a37", "a38", "a39", "a40", "a41", "a42", "a43", "a44", "a45", "a46", \
  "a47", "a48", "a49", "a50", "a51", "a52", "a53", "a54", "a55", "a56", "a57", "a58", "a59", "a60", "a61", "a62", "a63", "a64", "a65", "a66", "a67", \
  "a68", "a69", "a70", "a71", "a72", "a73", "a74", "a75", "a76", "a77", "a78", "a79", "a80", "a81", "a82", "a83", "a84", "a85", "a86", "a87", "a88", \
  "a89", "a90", "a91", "a92", "a93", "a94", "a95", "a96", "a97", "a98", "a99", "a100", "a101", "a102", "a103", "a104", "a105", "a106", "a107", "a108", \
  "a109", "a110", "a111", "a112", "a113", "a114", "a115", "a116", "a117", "a118", "a119", "a120", "a121", "a122", "a123", "a124", "a125", "a126", \
  "a127", "a128", "a129", "a130", "a131", "a132", "a133", "a134", "a135", "a136", "a137", "a138", "a139", "a140", "a141", "a142", "a143", "a144", \
  "a145", "a146", "a147", "a148", "a149", "a150", "a151", "a152", "a153", "a154", "a155", "a156", "a157", "a158", "a159", "a160", "a161", "a162", \
  "a163", "a164", "a165", "a166", "a167", "a168", "a169", "a170", "a171", "a172", "a173", "a174", "a175", "a176", "a177", "a178", "a179", "a180", \
  "a181", "a182", "a183", "a184", "a185", "a186", "a187", "a188", "a189", "a190", "a191", "a192", "a193", "a194", "a195", "a196", "a197", "a198", \
  "a199", "a200", "a201", "a202", "a203", "a204", "a205", "a206", "a207", "a208", "a209", "a210", "a211", "a212", "a213", "a214", "a215", "a216", \
  "a217", "a218", "a219", "a220", "a221", "a222", "a223", "a224", "a225", "a226", "a227", "a228", "a229", "a230", "a231", "a232", "a233", "a234", \
  "a235", "a236", "a237", "a238", "a239"
#define CHAIN_ASM \
  "v_mov_b32 v70, 0\n" \
  "v_mov_b32 v71, 0\n" \
  "v_mov_b32 v78, 0\n" \
  "v_mov_b32 v79, 0\n" \
  "v_mov_b32 v82, 0\n" \
  "v_mov_b32 v83, 0\n" \
  "v_mov_b32 v58, 0\n" \
  "v_mov_b32 v59, 0\n" \
  "v_mov_b32 v86, 0\n" \
  "v_mov_b32 v87, 0\n" \
  "v_mov_b32 v90, 0\n" \
  "v_mov_b32 v91, 0\n" \
  "v_mov_b32 v94, 0\n" \
  "v_mov_b32 v95, 0\n" \
  "v_mov_b32 v48, 0x3f803f80\n" \
  "v_mov_b32 v49, 0x3f803f80\n" \
  "v_mov_b32 v50, 0x3f803f80\n" \
  "v_mov_b32 v51, 0x3f803f80\n" \
  "v_mov_b32 v92, v33\n" \
  "v_mov_b32 v93, v33\n" \
  "s_mov_b32 s34, 0x00ff00ff\n" \
  "s_mov_b32 s35, 0x00ff00ff\n" \
  "v_mov_b32 v60, 0x3dd53b94\n" \
  "v_mov_b32 v61, 0x3dd53b94\n" \
  "v_mov_b32 v62, 16896\n" \
  "s_mov_b32 s68, s63\n" \
  "v_or_b32 v38, 0x4000, v38\n" \
  "v_and_b32 v7, 0xffffbfff, v7\n" \
  "s_and_b32 s69, s52, 1\n" \
  "s_lshl_b32 s69, s69, 13\n" \
  "s_mov_b32 s71, s43\n" \
  "s_mov_b32 s63, 2\n" \
  "s_mov_b32 s72, s40\n" \
  "s_mov_b32 s89, 1024\n" \
  "s_mov_b32 s88, s67\n" \
  "s_mul_i32 s67, s68, 1000\n" \
  "s_mov_b32 s73, 0\n" \
  "s_mul_i32 s44, s55, 24\n" \
  "s_mul_i32 s45, s56, 24\n" \
  "s_mul_i32 s46, s57, 24\n" \
  "s_mul_i32 s47, s88, 24\n" \
  "s_mul_i32 s48, s88, -12\n" \
  "s_cmp_eq_u32 s58, 1\n" \
  "s_cbranch_scc1 LSTART1_%=\n" \
  "s_cmp_eq_u32 s58, 2\n" \
  "s_cbranch_scc1 LSTART2_%=\n" \
  "s_cmp_eq_u32 s58, 3\n" \
  "s_cbranch_scc1 LSTART3_%=\n" \
  "LSTART0_%=:\n" \
  "s_getpc_b64 s[82:83]\n" \
  "LPC0_%=:\n" \
  "ds_read_b128 v[100:103], v7 offset:0\n" \
  "ds_read_b128 v[104:107], v7 offset:4096\n" \
  "ds_read_b128 v[108:111], v7 offset:8192\n" \
  "ds_read_b128 v[112:115], v7 offset:12288\n" \
  "ds_read_b64 v[116:117], v7 offset:2048\n" \
  "ds_read_b64 v[118:119], v7 offset:6144\n" \
  "ds_read_b64 v[120:121], v7 offset:10240\n" \
  "ds_read_b64 v[122:123], v7 offset:14336\n" \
  "s_waitcnt lgkmcnt(7)\n" \
  "v_mfma_f32_16x16x32_bf16 v[124:127], v[100:103], v[184:187], 0\n" \
  "v_mfma_f32_16x16x32_bf16 v[128:131], v[100:103], v[208:211], 0\n" \
  "v_mfma_f32_16x16x32_bf16 v[132:135], v[100:103], v[232:235], 0\n" \
  "s_waitcnt lgkmcnt(6)\n" \
  "v_mfma_f32_16x16x32_bf16 v[124:127], v[104:107], v[188:191], v[124:127]\n" \
  "v_mfma_f32_16x16x32_bf16 v[128:131], v[104:107], v[212:215], v[128:131]\n" \
  "v_mfma_f32_16x16x32_bf16 v[132:135], v[104:107], v[236:239], v[132:135]\n" \
  "s_waitcnt lgkmcnt(5)\n" \
  "v_mfma_f32_16x16x32_bf16 v[124:127], v[108:111], v[192:195], v[124:127]\n" \
  "v_mfma_f32_16x16x32_bf16 v[128:131], v[108:111], v[216:219], v[128:131]\n" \
  "v_mfma_f32_16x16x32_bf16 v[132:135], v[108:111], v[240:243], v[132:135]\n" \
  "s_waitcnt lgkmcnt(4)\n" \
  "v_mfma_f32_16x16x32_bf16 v[124:127], v[112:115], v[196:199], v[124:127]\n" \
  "v_mfma_f32_16x16x32_bf16 v[128:131], v[112:115], v[220:223], v[128:131]\n" \
  "v_mfma_f32_16x16x32_bf16 v[132:135], v[112:115], v[244:247], v[132:135]\n" \
  "s_waitcnt lgkmcnt(2)\n" \
  "v_mfma_f32_16x16x32_bf16 v[124:127], v[116:119], v[200:203], v[124:127]\n" \
  "v_mfma_f32_16x16x32_bf16 v[128:131], v[116:119], v[224:227], v[128:131]\n" \
  "v_mfma_f32_16x16x32_bf16 v[132:135], v[116:119], v[248:251], v[132:135]\n" \
  "s_waitcnt lgkmcnt(0)\n" \
  "v_mfma_f32_16x16x32_bf16 v[124:127], v[120:123], v[204:207], v[124:127]\n" \
  "v_mfma_f32_16x16x32_bf16 v[128:131], v[120:123], v[228:231], v[128:131]\n" \
  "v_mfma_f32_16x16x32_bf16 v[132:135], v[120:123], v[252:255], v[132:135]\n" \
  "s_nop 3\n" \
  "v_cvt_pk_bf16_f32 v40, v124, v125\n" \
  "v_cvt_pk_bf16_f32 v41, v126, v127\n" \
  "v_cvt_pk_bf16_f32 v44, v128, v129\n" \
  "v_cvt_pk_bf16_f32 v45, v130, v131\n" \
  "s_mov_b64 exec, s[34:35]\n" \
  "v_cvt_pk_bf16_f32 v48, v132, v133\n" \
  "v_cvt_pk_bf16_f32 v49, v134, v135\n" \
  "s_mov_b64 exec, -1\n" \
  "LOOP0_%=:\n" \
  "s_barrier\n" \
  "ds_read_b128 v[100:103], v7 offset:1024\n" \
  "ds_read_b128 v[104:107], v7 offset:5120\n" \
  "ds_read_b128 v[108:111], v7 offset:9216\n" \
  "ds_read_b128 v[112:115], v7 offset:13312\n" \
  "ds_read_b64 v[116:117], v7 offset:2056\n" \
  "ds_read_b64 v[118:119], v7 offset:6152\n" \
  "ds_read_b64 v[120:121], v7 offset:10248\n" \
  "ds_read_b64 v[122:123], v7 offset:14344\n" \
  "v_mov_b32 v140, 0\n" \
  "v_mov_b32 v141, 0\n" \
  "v_mov_b32 v142, 0\n" \
  "v_mov_b32 v143, 0\n" \
  "v_mov_b32 v144, 0\n" \
  "v_mov_b32 v145, 0\n" \
  "v_mov_b32 v146, 0\n" \
  "v_mov_b32 v147, 0\n" \
  "v_xor_b32 v7, 0x4000, v7\n" \
  "v_mov_b32 v63, s68\n" \
  "s_mov_b32 s62, s41\n" \
  "s_mov_b32 s41, s71\n" \
  "s_add_u32 s68, s68, 1\n" \
  "s_mov_b32 s70, s42\n" \
  "s_add_u32 s63, s63, 2\n" \
  "s_cmp_eq_u32 s63, 60\n" \
  "s_cselect_b32 s63, 0, s63\n" \
  "s_lshr_b64 s[82:83], s[52:53], s63\n" \
  "s_lshl_b32 s42, s63, 21\n" \
  "s_lshl_b32 s76, s82, 12\n" \
  "s_and_b32 s76, s76, 0x2000\n" \
  "s_or_b32 s71, s42, s76\n" \
  "s_or_b32 s71, s71, 0x200000\n" \
  "s_lshl_b32 s76, s82, 13\n" \
  "s_and_b32 s76, s76, 0x2000\n" \
  "s_or_b32 s42, s42, s76\n" \
  "s_waitcnt lgkmcnt(7)\n" \
  "v_mfma_f32_16x16x32_bf16 v[124:127], v[100:103], v[184:187], 0\n" \
  "v_mfma_f32_16x16x32_bf16 v[128:131], v[100:103], v[208:211], 0\n" \
  "v_mfma_f32_16x16x32_bf16 v[132:135], v[100:103], v[232:235], 0\n" \
  "s_waitcnt lgkmcnt(6)\n" \
  "v_mfma_f32_16x16x32_bf16 v[124:127], v[104:107], v[188:191], v[124:127]\n" \
  "v_mfma_f32_16x16x32_bf16 v[128:131], v[104:107], v[212:215], v[128:131]\n" \
  "v_mfma_f32_16x16x32_bf16 v[132:135], v[104:107], v[236:239], v[132:135]\n" \
  "s_waitcnt lgkmcnt(5)\n" \
  "v_mfma_f32_16x16x32_bf16 v[124:127], v[108:111], v[192:195], v[124:127]\n" \
  "v_mfma_f32_16x16x32_bf16 v[128:131], v[108:111], v[216:219], v[128:131]\n" \
  "v_mfma_f32_16x16x32_bf16 v[132:135], v[108:111], v[240:243], v[132:135]\n" \
  "s_waitcnt lgkmcnt(4)\n" \
  "v_mfma_f32_16x16x32_bf16 v[124:127], v[112:115], v[196:199], v[124:127]\n" \
  "v_mfma_f32_16x16x32_bf16 v[128:131], v[112:115], v[220:223], v[128:131]\n" \
  "v_mfma_f32_16x16x32_bf16 v[132:135], v[112:115], v[244:247], v[132:135]\n" \
  "s_waitcnt lgkmcnt(2)\n" \
  "v_mfma_f32_16x16x32_bf16 v[124:127], v[116:119], v[200:203], v[124:127]\n" \
  "v_mfma_f32_16x16x32_bf16 v[128:131], v[116:119], v[224:227], v[128:131]\n" \
  "v_mfma_f32_16x16x32_bf16 v[132:135], v[116:119], v[248:251], v[132:135]\n" \
  "s_waitcnt lgkmcnt(0)\n" \
  "v_mfma_f32_16x16x32_bf16 v[124:127], v[120:123], v[204:207], v[124:127]\n" \
  "v_mfma_f32_16x16x32_bf16 v[128:131], v[120:123], v[228:231], v[128:131]\n" \
  "v_mfma_f32_16x16x32_bf16 v[132:135], v[120:123], v[252:255], v[132:135]\n" \
  "s_nop 3\n" \
  "v_cvt_pk_bf16_f32 v42, v124, v125\n" \
  "v_cvt_pk_bf16_f32 v43, v126, v127\n" \
  "v_cvt_pk_bf16_f32 v46, v128, v129\n" \
  "v_cvt_pk_bf16_f32 v47, v130, v131\n" \
  "s_mov_b64 exec, s[34:35]\n" \
  "v_cvt_pk_bf16_f32 v50, v132, v133\n" \
  "v_cvt_pk_bf16_f32 v51, v134, v135\n" \
  "s_mov_b64 exec, -1\n" \
  "s_waitcnt lgkmcnt(0)\n" \
  "s_waitcnt vmcnt(0)\n" \
  "LM0_%=:\n" \
  "s_barrier\n" \
  "ds_read_b128 v[100:103], v6 offset:0\n" \
  "ds_read_b128 v[104:107], v6 offset:1024\n" \
  "s_barrier\n" \
  "ds_read_b128 v[108:111], v6 offset:2048\n" \
  "ds_read_b128 v[112:115], v6 offset:3072\n" \
  "s_waitcnt lgkmcnt(3)\n" \
  "v_smfmac_f32_16x16x64_bf16 v[140:143], v[100:103], a[0:7], v0\n" \
  "v_smfmac_f32_16x16x64_bf16 v[144:147], v[100:103], a[80:87], v0\n" \
  "s_waitcnt lgkmcnt(2)\n" \
  "v_smfmac_f32_16x16x64_bf16 v[140:143], v[104:107], a[8:15], v0\n" \
  "v_smfmac_f32_16x16x64_bf16 v[144:147], v[104:107], a[88:95], v0\n" \
  "s_barrier\n" \
  "ds_read_b128 v[116:119], v6 offset:4096\n" \
  "ds_read_b128 v[120:123], v6 offset:5120\n" \
  "s_waitcnt lgkmcnt(3)\n" \
  "v_smfmac_f32_16x16x64_bf16 v[140:143], v[108:111], a[16:23], v0\n" \
  "v_smfmac_f32_16x16x64_bf16 v[144:147], v[108:111], a[96:103], v0\n" \
  "s_waitcnt lgkmcnt(2)\n" \
  "v_smfmac_f32_16x16x64_bf16 v[140:143], v[112:115], a[24:31], v0\n" \
  "v_smfmac_f32_16x16x64_bf16 v[144:147], v[112:115], a[104:111], v0\n" \
  "s_barrier\n" \
  "ds_read_b128 v[124:127], v6 offset:6144\n" \
  "ds_read_b128 v[128:131], v6 offset:7168\n" \
  "s_waitcnt lgkmcnt(3)\n" \
  "v_smfmac_f32_16x16x64_bf16 v[140:143], v[116:119], a[32:39], v0\n" \
  "v_smfmac_f32_16x16x64_bf16 v[144:147], v[116:119], a[112:119], v0\n" \
  "s_waitcnt lgkmcnt(2)\n" \
  "v_smfmac_f32_16x16x64_bf16 v[140:143], v[120:123], a[40:47], v0\n" \
  "v_smfmac_f32_16x16x64_bf16 v[144:147], v[120:123], a[120:127], v0\n" \
  "s_barrier\n" \
  "ds_read_b128 v[132:135], v6 offset:8192\n" \
  "ds_read_b128 v[136:139], v6 offset:9216\n" \
  "s_waitcnt lgkmcnt(3)\n" \
  "v_smfmac_f32_16x16x64_bf16 v[140:143], v[124:127], a[48:55], v0\n" \
  "v_smfmac_f32_16x16x64_bf16 v[144:147], v[124:127], a[128:135], v0\n" \
  "s_waitcnt lgkmcnt(2)\n" \
  "v_smfmac_f32_16x16x64_bf16 v[140:143], v[128:131], a[56:63], v0\n" \
  "v_smfmac_f32_16x16x64_bf16 v[144:147], v[128:131], a[136:143], v0\n" \
  "s_waitcnt lgkmcnt(1)\n" \
  "v_smfmac_f32_16x16x64_bf16 v[140:143], v[132:135], a[64:71], v0\n" \
  "buffer_store_dword v63, v39, s[36:39], 0 offen sc1\n" \
  "v_smfmac_f32_16x16x64_bf16 v[144:147], v[132:135], a[144:151], v0\n" \
  "s_waitcnt lgkmcnt(0)\n" \
  "v_smfmac_f32_16x16x64_bf16 v[140:143], v[136:139], a[72:79], v0\n" \
  "v_smfmac_f32_16x16x64_bf16 v[144:147], v[136:139], a[152:159], v0\n" \
  "s_nop 7\n" \
  "v_permlane32_swap_b32 v140, v144\n" \
  "v_permlane32_swap_b32 v141, v145\n" \
  "v_permlane32_swap_b32 v142, v146\n" \
  "v_permlane32_swap_b32 v143, v147\n" \
  "v_pk_add_f32 v[152:153], v[140:141], v[144:145]\n" \
  "v_pk_add_f32 v[154:155], v[142:143], v[146:147]\n" \
  "v_cvt_pk_bf16_f32 v156, v152, v153\n" \
  "v_cvt_pk_bf16_f32 v157, v154, v155\n" \
  "ds_write_b64 v10, v[156:157]\n" \
  "v_cmp_u_f32 vcc, v152, v153\n" \
  "v_cmp_u_f32 s[82:83], v154, v155\n" \
  "s_or_b64 vcc, vcc, s[82:83]\n" \
  "s_cbranch_vccnz LMF0_%=\n" \
  "LMOK0_%=:\n" \
  "s_waitcnt lgkmcnt(0)\n" \
  "s_barrier\n" \
  "ds_read_b64_tr_b16 v[72:73], v11\n" \
  "ds_read_b64_tr_b16 v[74:75], v11 offset:128\n" \
  "ds_read_b64_tr_b16 v[76:77], v12\n" \
  "s_waitcnt lgkmcnt(1)\n" \
  "v_mfma_f32_16x16x32_bf16 v[152:155], v[72:75], v[72:75], 0\n" \
  "s_waitcnt lgkmcnt(0)\n" \
  "v_mfma_f32_16x16x32_bf16 v[152:155], v[76:79], v[76:79], v[152:155]\n" \
  "s_nop 7\n" \
  "buffer_store_dwordx4 v[152:155], v13, s[36:39], s69 offen sc1\n" \
  "s_cmp_eq_u32 s68, 64\n" \
  "s_cbranch_scc1 LEND0_%=\n" \
  "s_barrier\n" \
  "s_nop 5\n" \
  "buffer_load_dwordx4 v[156:159], v15, s[36:39], s69 offen sc1\n" \
  "buffer_load_dword v160, v16, s[36:39], s69 offen sc1\n" \
  "ds_read_b32 v180, v38\n" \
  "s_mov_b32 s72, s70\n" \
  "v_mov_b32 v136, 0\n" \
  "v_mov_b32 v137, 0\n" \
  "v_mov_b32 v138, 0\n" \
  "v_mov_b32 v139, 0\n" \
  "v_mov_b32 v140, 0\n" \
  "v_mov_b32 v141, 0\n" \
  "v_mov_b32 v142, 0\n" \
  "v_mov_b32 v143, 0\n" \
  "v_mov_b32 v144, 0\n" \
  "v_mov_b32 v145, 0\n" \
  "v_mov_b32 v146, 0\n" \
  "v_mov_b32 v147, 0\n" \
  "v_xor_b32 v38, 0x4000, v38\n" \
  "s_mov_b32 s73, s89\n" \
  "s_add_u32 s76, s68, 2\n" \
  "s_min_u32 s76, s76, 63\n" \
  "s_lshr_b32 s77, s76, 4\n" \
  "s_lshl_b32 s77, s77, 13\n" \
  "s_lshl_b32 s76, s76, 9\n" \
  "s_add_u32 s89, s76, s77\n" \
  "s_waitcnt lgkmcnt(0)\n" \
  "v_cmp_gt_u32 vcc, 24, v180\n" \
  "s_nop 3\n" \
  "s_cbranch_vccnz LFF0_%=\n" \
  "LFOK0_%=:\n" \
  "v_mov_b32 v183, v182\n" \
  "s_nop 0\n" \
  "v_mov_b32 v183, v182\n" \
  "s_nop 0\n" \
  "v_mov_b32 v183, v182\n" \
  "s_nop 0\n" \
  "v_mov_b32 v183, v182\n" \
  "s_nop 0\n" \
  "v_mov_b32 v183, v182\n" \
  "s_nop 0\n" \
  "v_mov_b32 v183, v182\n" \
  "s_nop 0\n" \
  "v_mov_b32 v183, v182\n" \
  "s_nop 0\n" \
  "v_mov_b32 v183, v182\n" \
  "s_nop 0\n" \
  "v_mov_b32 v183, v182\n" \
  "s_nop 0\n" \
  "v_mov_b32 v183, v182\n" \
  "s_nop 0\n" \
  "v_mov_b32 v183, v182\n" \
  "s_nop 0\n" \
  "v_mov_b32 v183, v182\n" \
  "s_nop 0\n" \
  "v_mov_b32 v183, v182\n" \
  "s_nop 0\n" \
  "v_mov_b32 v183, v182\n" \
  "s_nop 0\n" \
  "v_mov_b32 v183, v182\n" \
  "s_nop 0\n" \
  "v_mov_b32 v183, v182\n" \
  "s_nop 0\n" \
  "v_mov_b32 v183, v182\n" \
  "s_nop 0\n" \
  "v_mov_b32 v183, v182\n" \
  "s_nop 0\n" \
  "v_mov_b32 v183, v182\n" \
  "s_nop 0\n" \
  "v_mov_b32 v183, v182\n" \
  "s_nop 0\n" \
  "v_mov_b32 v183, v182\n" \
  "s_nop 0\n" \
  "v_mov_b32 v183, v182\n" \
  "s_nop 0\n" \
  "v_mov_b32 v183, v182\n" \
  "s_nop 0\n" \
  "v_mov_b32 v183, v182\n" \
  "s_nop 0\n" \
  "v_mov_b32 v183, v182\n" \
  "s_nop 0\n" \
  "v_mov_b32 v183, v182\n" \
  "s_nop 0\n" \
  "v_mov_b32 v183, v182\n" \
  "s_nop 0\n" \
  "v_mov_b32 v183, v182\n" \
  "s_nop 0\n" \
  "v_mov_b32 v183, v182\n" \
  "s_nop 0\n" \
  "v_mov_b32 v183, v182\n" \
  "s_nop 0\n" \
  "LQ0_%=:\n" \
  "s_barrier\n" \
  "ds_read_b64_tr_b16 v[100:101], v20\n" \
  "ds_read_b64_tr_b16 v[102:103], v21\n" \
  "ds_read_b64_tr_b16 v[104:105], v22\n" \
  "ds_read_b64_tr_b16 v[106:107], v23\n" \
  "ds_read_b64_tr_b16 v[108:109], v24\n" \
  "ds_read_b64_tr_b16 v[110:111], v25\n" \
  "v_mov_b32 v183, v182\n" \
  "s_nop 0\n" \
  "v_mov_b32 v183, v182\n" \
  "s_nop 0\n" \
  "v_mov_b32 v183, v182\n" \
  "s_nop 0\n" \
  "v_mov_b32 v183, v182\n" \
  "s_nop 0\n" \
  "s_waitcnt lgkmcnt(4)\n" \
  "v_smfmac_f32_16x16x64_bf16 v[144:147], v[100:103], v[232:239], v0\n" \
  "v_mov_b32 v183, v182\n" \
  "v_smfmac_f32_16x16x64_bf16 v[136:139], v[100:103], v[184:191], v0\n" \
  "v_mov_b32 v183, v182\n" \
  "v_smfmac_f32_16x16x64_bf16 v[140:143], v[100:103], v[208:215], v0\n" \
  "v_mov_b32 v183, v182\n" \
  "s_waitcnt lgkmcnt(0)\n" \
  "v_smfmac_f32_16x16x64_bf16 v[144:147], v[104:107], v[240:247], v0\n" \
  "v_mov_b32 v183, v182\n" \
  "v_smfmac_f32_16x16x64_bf16 v[136:139], v[104:107], v[192:199], v0\n" \
  "v_mov_b32 v183, v182\n" \
  "s_getreg_b32 s76, hwreg(HW_REG_IB_STS, 0, 4)\n" \
  "v_mov_b32 v183, v182\n" \
  "s_cmp_le_u32 s76, 0\n" \
  "s_cbranch_scc1 LHXQ0_%=\n" \
  "s_getreg_b32 s76, hwreg(HW_REG_IB_STS, 0, 4)\n" \
  "v_mov_b32 v183, v182\n" \
  "s_cmp_le_u32 s76, 0\n" \
  "s_cbranch_scc1 LHXQ0_%=\n" \
  "s_getreg_b32 s76, hwreg(HW_REG_IB_STS, 0, 4)\n" \
  "v_mov_b32 v183, v182\n" \
  "s_cmp_le_u32 s76, 0\n" \
  "s_cbranch_scc1 LHXQ0_%=\n" \
  "s_getreg_b32 s76, hwreg(HW_REG_IB_STS, 0, 4)\n" \
  "v_mov_b32 v183, v182\n" \
  "s_cmp_le_u32 s76, 0\n" \
  "s_cbranch_scc1 LHXQ0_%=\n" \
  "s_getreg_b32 s76, hwreg(HW_REG_IB_STS, 0, 4)\n" \
  "v_mov_b32 v183, v182\n" \
  "s_cmp_le_u32 s76, 0\n" \
  "s_cbranch_scc1 LHXQ0_%=\n" \
  "s_getreg_b32 s76, hwreg(HW_REG_IB_STS, 0, 4)\n" \
  "v_mov_b32 v183, v182\n" \
  "s_cmp_le_u32 s76, 0\n" \
  "s_cbranch_scc1 LHXQ0_%=\n" \
  "s_getreg_b32 s76, hwreg(HW_REG_IB_STS, 0, 4)\n" \
  "v_mov_b32 v183, v182\n" \
  "s_cmp_le_u32 s76, 0\n" \
  "s_cbranch_scc1 LHXQ0_%=\n" \
  "s_getreg_b32 s76, hwreg(HW_REG_IB_STS, 0, 4)\n" \
  "v_mov_b32 v183, v182\n" \
  "s_cmp_le_u32 s76, 0\n" \
  "s_cbranch_scc1 LHXQ0_%=\n" \
  "s_getreg_b32 s76, hwreg(HW_REG_IB_STS, 0, 4)\n" \
  "v_mov_b32 v183, v182\n" \
  "s_cmp_le_u32 s76, 0\n" \
  "s_cbranch_scc1 LHXQ0_%=\n" \
  "s_getreg_b32 s76, hwreg(HW_REG_IB_STS, 0, 4)\n" \
  "v_mov_b32 v183, v182\n" \
  "s_cmp_le_u32 s76, 0\n" \
  "s_cbranch_scc1 LHXQ0_%=\n" \
  "LHXQ0_%=:\n" \
  "s_waitcnt vmcnt(0)\n" \
  "v_mov_b32 v115, v160\n" \
  "v_mov_b32 v114, v160\n" \
  "v_permlane32_swap_b32 v156, v158\n" \
  "v_permlane32_swap_b32 v157, v159\n" \
  "v_pk_add_f32 v[112:113], v[156:157], v[158:159]\n" \
  "v_permlane32_swap_b32 v114, v115\n" \
  "v_add_f32 v117, v114, v115\n" \
  "v_permlane16_swap_b32 v112, v113\n" \
  "v_add_f32 v114, v114, v115\n" \
  "v_add_f32 v116, v112, v113\n" \
  "v_smfmac_f32_16x16x64_bf16 v[140:143], v[104:107], v[216:223], v0\n" \
  "v_permlane16_swap_b32 v117, v114\n" \
  "v_mul_f32 v116, v116, v60\n" \
  "v_add_f32 v117, v117, v114\n" \
  "v_exp_f32 v112, v116\n" \
  "v_mul_f32 v117, v117, v60\n" \
  "v_max_f32 v118, |v116|, |v117|\n" \
  "v_exp_f32 v113, v117\n" \
  "v_cmp_lt_f32 vcc, 0x42800000, v118\n" \
  "v_cvt_pk_bf16_f32 v112, v112, v113\n" \
  "v_smfmac_f32_16x16x64_bf16 v[144:147], v[108:111], v[248:255], v0\n" \
  "s_cbranch_vccnz LMX0_%=\n" \
  "LMY0_%=:\n" \
  "ds_write_b16 v18, v112\n" \
  "ds_write_b16_d16_hi v19, v112\n" \
  "v_smfmac_f32_16x16x64_bf16 v[136:139], v[108:111], v[200:207], v0\n" \
  "v_smfmac_f32_16x16x64_bf16 v[140:143], v[108:111], v[224:231], v0\n" \
  "s_mov_b64 exec, s[34:35]\n" \
  "v_cvt_pk_bf16_f32 v92, v144, v145\n" \
  "v_cvt_pk_bf16_f32 v93, v146, v147\n" \
  "s_mov_b64 exec, -1\n" \
  "v_cvt_pk_bf16_f32 v84, v136, v137\n" \
  "v_cvt_pk_bf16_f32 v85, v138, v139\n" \
  "s_nop 1\n" \
  "v_cvt_pk_bf16_f32 v88, v140, v141\n" \
  "v_cvt_pk_bf16_f32 v89, v142, v143\n" \
  "s_waitcnt lgkmcnt(0)\n" \
  "s_barrier\n" \
  "ds_read_b128 v[120:123], v28\n" \
  "ds_read_b64 v[80:81], v36\n" \
  "v_mov_b32 v183, v182\n" \
  "s_nop 0\n" \
  "v_mov_b32 v183, v182\n" \
  "s_nop 0\n" \
  "v_mov_b32 v183, v182\n" \
  "s_nop 0\n" \
  "v_mov_b32 v183, v182\n" \
  "s_nop 0\n" \
  "s_waitcnt lgkmcnt(1)\n" \
  "v_mfma_f32_16x16x32_bf16 v[172:175], v[48:51], v[120:123], 0\n" \
  "v_mfma_f32_16x16x32_bf16 v[164:167], v[40:43], v[120:123], 0\n" \
  "s_waitcnt lgkmcnt(0)\n" \
  "v_mfma_f32_16x16x32_bf16 v[172:175], v[92:95], v[80:83], v[172:175]\n" \
  "ds_read_b32 v180, v62\n" \
  "v_mfma_f32_16x16x32_bf16 v[168:171], v[44:47], v[120:123], 0\n" \
  "v_mfma_f32_16x16x32_bf16 v[164:167], v[84:87], v[80:83], v[164:167]\n" \
  "v_rcp_f32 v177, v172\n" \
  "v_mfma_f32_16x16x32_bf16 v[168:171], v[88:91], v[80:83], v[168:171]\n" \
  "v_mov_b32 v176, v177\n" \
  "v_cmp_u_f32 vcc, v172, v172\n" \
  "s_nop 0\n" \
  "v_permlane32_swap_b32 v177, v176\n" \
  "v_pk_mul_f32 v[164:165], v[164:165], v[176:177] op_sel_hi:[1,0]\n" \
  "v_pk_mul_f32 v[166:167], v[166:167], v[176:177] op_sel_hi:[1,0]\n" \
  "v_pk_mul_f32 v[172:173], v[172:173], v[176:177] op_sel_hi:[1,0]\n" \
  "v_pk_mul_f32 v[174:175], v[174:175], v[176:177] op_sel_hi:[1,0]\n" \
  "v_pk_mul_f32 v[168:169], v[168:169], v[176:177] op_sel_hi:[1,0]\n" \
  "v_pk_mul_f32 v[170:171], v[170:171], v[176:177] op_sel_hi:[1,0]\n" \
  "v_cvt_pk_bf16_f32 v164, v164, v165\n" \
  "v_cvt_pk_bf16_f32 v165, v166, v167\n" \
  "v_cvt_pk_bf16_f32 v166, v168, v169\n" \
  "v_cvt_pk_bf16_f32 v167, v170, v171\n" \
  "s_cbranch_vccnz LQF0_%=\n" \
  "LQOK0_%=:\n" \
  "buffer_store_dwordx4 v[164:167], v26, s[36:39], s69 offen sc1\n" \
  "v_cvt_pk_bf16_f32 v172, v172, v173\n" \
  "v_cvt_pk_bf16_f32 v173, v174, v175\n" \
  "buffer_store_dwordx2 v[172:173], v27, s[36:39], s69 offen sc1\n" \
  "buffer_store_dwordx4 v[72:75], v31, s[36:39], s62 offen sc1\n" \
  "buffer_store_dwordx2 v[76:77], v32, s[36:39], s62 offen sc1\n" \
  "s_waitcnt lgkmcnt(0)\n" \
  "v_readfirstlane_b32 s50, v180\n" \
  "ds_read_b128 v[100:103], v7 offset:0\n" \
  "ds_read_b128 v[104:107], v7 offset:4096\n" \
  "ds_read_b128 v[108:111], v7 offset:8192\n" \
  "ds_read_b128 v[112:115], v7 offset:12288\n" \
  "ds_read_b64 v[116:117], v7 offset:2048\n" \
  "ds_read_b64 v[118:119], v7 offset:6144\n" \
  "ds_read_b64 v[120:121], v7 offset:10240\n" \
  "ds_read_b64 v[122:123], v7 offset:14336\n" \
  "s_waitcnt lgkmcnt(7)\n" \
  "v_mfma_f32_16x16x32_bf16 v[124:127], v[100:103], v[184:187], 0\n" \
  "v_mfma_f32_16x16x32_bf16 v[128:131], v[100:103], v[208:211], 0\n" \
  "v_mfma_f32_16x16x32_bf16 v[132:135], v[100:103], v[232:235], 0\n" \
  "s_waitcnt lgkmcnt(6)\n" \
  "v_mfma_f32_16x16x32_bf16 v[124:127], v[104:107], v[188:191], v[124:127]\n" \
  "v_mfma_f32_16x16x32_bf16 v[128:131], v[104:107], v[212:215], v[128:131]\n" \
  "v_mfma_f32_16x16x32_bf16 v[132:135], v[104:107], v[236:239], v[132:135]\n" \
  "s_waitcnt lgkmcnt(5)\n" \
  "v_mfma_f32_16x16x32_bf16 v[124:127], v[108:111], v[192:195], v[124:127]\n" \
  "v_mfma_f32_16x16x32_bf16 v[128:131], v[108:111], v[216:219], v[128:131]\n" \
  "v_mfma_f32_16x16x32_bf16 v[132:135], v[108:111], v[240:243], v[132:135]\n" \
  "s_waitcnt lgkmcnt(4)\n" \
  "v_mfma_f32_16x16x32_bf16 v[124:127], v[112:115], v[196:199], v[124:127]\n" \
  "v_mfma_f32_16x16x32_bf16 v[128:131], v[112:115], v[220:223], v[128:131]\n" \
  "v_mfma_f32_16x16x32_bf16 v[132:135], v[112:115], v[244:247], v[132:135]\n" \
  "s_waitcnt lgkmcnt(2)\n" \
  "v_mfma_f32_16x16x32_bf16 v[124:127], v[116:119], v[200:203], v[124:127]\n" \
  "v_mfma_f32_16x16x32_bf16 v[128:131], v[116:119], v[224:227], v[128:131]\n" \
  "v_mfma_f32_16x16x32_bf16 v[132:135], v[116:119], v[248:251], v[132:135]\n" \
  "s_waitcnt lgkmcnt(0)\n" \
  "v_mfma_f32_16x16x32_bf16 v[124:127], v[120:123], v[204:207], v[124:127]\n" \
  "v_mfma_f32_16x16x32_bf16 v[128:131], v[120:123], v[228:231], v[128:131]\n" \
  "v_mfma_f32_16x16x32_bf16 v[132:135], v[120:123], v[252:255], v[132:135]\n" \
  "s_nop 3\n" \
  "v_cvt_pk_bf16_f32 v40, v124, v125\n" \
  "v_cvt_pk_bf16_f32 v41, v126, v127\n" \
  "v_cvt_pk_bf16_f32 v44, v128, v129\n" \
  "v_cvt_pk_bf16_f32 v45, v130, v131\n" \
  "s_mov_b64 exec, s[34:35]\n" \
  "v_cvt_pk_bf16_f32 v48, v132, v133\n" \
  "v_cvt_pk_bf16_f32 v49, v134, v135\n" \
  "s_mov_b64 exec, -1\n" \
  "LUB0_%=:\n" \
  "s_memtime s[80:81]\n" \
  "s_waitcnt lgkmcnt(0)\n" \
  "s_sub_i32 s76, s50, s80\n" \
  "s_cmp_gt_i32 s76, -160\n" \
  "s_cbranch_scc1 LUB0_%=\n" \
  "s_mov_b32 m0, s60\n" \
  "s_add_u32 s77, s71, 16384\n" \
  "buffer_load_dwordx4 v4, s[36:39], s71 offen sc1 lds\n" \
  "buffer_load_dwordx4 v4, s[36:39], s71 offen offset:1024 sc1 lds\n" \
  "buffer_load_dwordx4 v4, s[36:39], s71 offen offset:2048 sc1 lds\n" \
  "buffer_load_dwordx4 v5, s[36:39], s71 offen offset:3072 sc1 lds\n" \
  "s_add_u32 m0, s60, 4096\n" \
  "s_add_u32 s76, s71, 1024\n" \
  "buffer_load_dwordx4 v4, s[36:39], s77 offen sc1 lds\n" \
  "buffer_load_dwordx4 v4, s[36:39], s77 offen offset:1024 sc1 lds\n" \
  "buffer_load_dwordx4 v4, s[36:39], s77 offen offset:2048 sc1 lds\n" \
  "buffer_load_dwordx4 v5, s[36:39], s76 offen offset:3072 sc1 lds\n" \
  "s_xor_b32 s60, s60, 0x4000\n" \
  "s_mov_b32 s69, s70\n" \
  "s_branch LOOP0_%=\n" \
  "LFF0_%=:\n" \
  "buffer_load_dword v180, v37, s[36:39], s73 offen offset:3072 sc1\n" \
  "s_waitcnt vmcnt(0)\n" \
  "v_cmp_gt_u32 vcc, 24, v180\n" \
  "s_nop 3\n" \
  "s_cbranch_vccz LFOK0_%=\n" \
  "s_add_u32 s67, s67, 1\n" \
  "s_cmp_gt_u32 s67, 20000\n" \
  "s_cbranch_scc1 LFOK0_%=\n" \
  "s_sleep 16\n" \
  "s_branch LFF0_%=\n" \
  "LMX0_%=:\n" \
  "v_max_f32 v118, v116, v117\n" \
  "v_mov_b32 v119, v118\n" \
  "s_nop 1\n" \
  "v_permlane16_swap_b32 v118, v119\n" \
  "v_max_f32 v118, v118, v119\n" \
  "v_mov_b32 v119, v118\n" \
  "s_nop 1\n" \
  "v_permlane32_swap_b32 v118, v119\n" \
  "v_max_f32 v118, v118, v119\n" \
  "s_nop 1\n" \
  "v_max_f32_dpp v118, v118, v118 row_ror:8 row_mask:0xf bank_mask:0xf\n" \
  "s_nop 1\n" \
  "v_max_f32_dpp v118, v118, v118 quad_perm:[2,3,0,1] row_mask:0xf bank_mask:0xf\n" \
  "s_nop 1\n" \
  "v_max_f32_dpp v118, v118, v118 row_ror:4 row_mask:0xf bank_mask:0xf\n" \
  "v_sub_f32 v116, v116, v118\n" \
  "v_sub_f32 v117, v117, v118\n" \
  "v_exp_f32 v112, v116\n" \
  "v_exp_f32 v113, v117\n" \
  "s_nop 0\n" \
  "v_cvt_pk_bf16_f32 v112, v112, v113\n" \
  "s_branch LMY0_%=\n" \
  "LMF0_%=:\n" \
  "v_mov_b32 v180, 0\n" \
  "v_pk_max_u16 v180, v180, v100\n" \
  "v_pk_max_u16 v180, v180, v101\n" \
  "v_pk_max_u16 v180, v180, v102\n" \
  "v_pk_max_u16 v180, v180, v103\n" \
  "v_pk_max_u16 v180, v180, v104\n" \
  "v_pk_max_u16 v180, v180, v105\n" \
  "v_pk_max_u16 v180, v180, v106\n" \
  "v_pk_max_u16 v180, v180, v107\n" \
  "v_pk_max_u16 v180, v180, v108\n" \
  "v_pk_max_u16 v180, v180, v109\n" \
  "v_pk_max_u16 v180, v180, v110\n" \
  "v_pk_max_u16 v180, v180, v111\n" \
  "v_pk_max_u16 v180, v180, v112\n" \
  "v_pk_max_u16 v180, v180, v113\n" \
  "v_pk_max_u16 v180, v180, v114\n" \
  "v_pk_max_u16 v180, v180, v115\n" \
  "v_pk_max_u16 v180, v180, v116\n" \
  "v_pk_max_u16 v180, v180, v117\n" \
  "v_pk_max_u16 v180, v180, v118\n" \
  "v_pk_max_u16 v180, v180, v119\n" \
  "v_pk_max_u16 v180, v180, v120\n" \
  "v_pk_max_u16 v180, v180, v121\n" \
  "v_pk_max_u16 v180, v180, v122\n" \
  "v_pk_max_u16 v180, v180, v123\n" \
  "v_pk_max_u16 v180, v180, v124\n" \
  "v_pk_max_u16 v180, v180, v125\n" \
  "v_pk_max_u16 v180, v180, v126\n" \
  "v_pk_max_u16 v180, v180, v127\n" \
  "v_pk_max_u16 v180, v180, v128\n" \
  "v_pk_max_u16 v180, v180, v129\n" \
  "v_pk_max_u16 v180, v180, v130\n" \
  "v_pk_max_u16 v180, v180, v131\n" \
  "v_pk_max_u16 v180, v180, v132\n" \
  "v_pk_max_u16 v180, v180, v133\n" \
  "v_pk_max_u16 v180, v180, v134\n" \
  "v_pk_max_u16 v180, v180, v135\n" \
  "v_pk_max_u16 v180, v180, v136\n" \
  "v_pk_max_u16 v180, v180, v137\n" \
  "v_pk_max_u16 v180, v180, v138\n" \
  "v_pk_max_u16 v180, v180, v139\n" \
  "v_lshrrev_b32 v181, 16, v180\n" \
  "v_and_b32 v180, 0xffff, v180\n" \
  "v_max_u32 v180, v180, v181\n" \
  "v_cmp_eq_u32 vcc, 0xffff, v180\n" \
  "s_nop 3\n" \
  "s_cbranch_vccz LMOK0_%=\n" \
  "s_add_u32 s67, s67, 1\n" \
  "s_cmp_gt_u32 s67, 20000\n" \
  "s_cbranch_scc1 LMOK0_%=\n" \
  "s_add_u32 s64, s64, 1\n" \
  "s_waitcnt vmcnt(0) lgkmcnt(0)\n" \
  "s_barrier\n" \
  "v_mov_b32 v140, 0\n" \
  "v_mov_b32 v141, 0\n" \
  "v_mov_b32 v142, 0\n" \
  "v_mov_b32 v143, 0\n" \
  "v_mov_b32 v144, 0\n" \
  "v_mov_b32 v145, 0\n" \
  "v_mov_b32 v146, 0\n" \
  "v_mov_b32 v147, 0\n" \
  "v_mov_b32 v148, 0\n" \
  "v_mov_b32 v149, 0\n" \
  "v_mov_b32 v150, 0\n" \
  "v_mov_b32 v151, 0\n" \
  "s_branch LM0_%=\n" \
  "LQF0_%=:\n" \
  "s_waitcnt lgkmcnt(0)\n" \
  "v_mov_b32 v116, 0\n" \
  "v_pk_max_u16 v116, v116, v100\n" \
  "v_pk_max_u16 v116, v116, v101\n" \
  "v_pk_max_u16 v116, v116, v102\n" \
  "v_pk_max_u16 v116, v116, v103\n" \
  "v_pk_max_u16 v116, v116, v104\n" \
  "v_pk_max_u16 v116, v116, v105\n" \
  "v_pk_max_u16 v116, v116, v106\n" \
  "v_pk_max_u16 v116, v116, v107\n" \
  "v_pk_max_u16 v116, v116, v108\n" \
  "v_pk_max_u16 v116, v116, v109\n" \
  "v_pk_max_u16 v116, v116, v110\n" \
  "v_pk_max_u16 v116, v116, v111\n" \
  "v_lshrrev_b32 v117, 16, v116\n" \
  "v_and_b32 v116, 0xffff, v116\n" \
  "v_max_u32 v116, v116, v117\n" \
  "v_cmp_eq_u32 vcc, 0xffff, v116\n" \
  "v_max3_u32 v118, v156, v157, v158\n" \
  "v_max3_u32 v118, v118, v159, v160\n" \
  "v_cmp_eq_u32 s[82:83], -1, v118\n" \
  "s_nop 3\n" \
  "s_or_b64 vcc, vcc, s[82:83]\n" \
  "s_cselect_b32 s76, 1, 0\n" \
  "v_mov_b32 v116, s76\n" \
  "ds_write_b32 v62, v116 offset:16\n" \
  "s_waitcnt lgkmcnt(0)\n" \
  "s_barrier\n" \
  "ds_read_b128 v[116:119], v62 offset:16\n" \
  "s_waitcnt lgkmcnt(0)\n" \
  "s_barrier\n" \
  "v_or3_b32 v116, v116, v117, v118\n" \
  "v_or_b32 v116, v116, v119\n" \
  "v_cmp_ne_u32 vcc, 0, v116\n" \
  "s_nop 3\n" \
  "s_cbranch_vccz LQOK0_%=\n" \
  "s_add_u32 s67, s67, 1\n" \
  "s_cmp_gt_u32 s67, 20000\n" \
  "s_cbranch_scc1 LQOK0_%=\n" \
  "s_add_u32 s65, s65, 1\n" \
  "s_barrier\n" \
  "s_nop 5\n" \
  "buffer_load_dwordx4 v[156:159], v15, s[36:39], s69 offen sc1\n" \
  "buffer_load_dword v160, v16, s[36:39], s69 offen sc1\n" \
  "v_mov_b32 v136, 0\n" \
  "v_mov_b32 v137, 0\n" \
  "v_mov_b32 v138, 0\n" \
  "v_mov_b32 v139, 0\n" \
  "v_mov_b32 v140, 0\n" \
  "v_mov_b32 v141, 0\n" \
  "v_mov_b32 v142, 0\n" \
  "v_mov_b32 v143, 0\n" \
  "v_mov_b32 v144, 0\n" \
  "v_mov_b32 v145, 0\n" \
  "v_mov_b32 v146, 0\n" \
  "v_mov_b32 v147, 0\n" \
  "s_waitcnt vmcnt(0) lgkmcnt(0)\n" \
  "s_branch LQ0_%=\n" \
  "LEND0_%=:\n" \
  "buffer_store_dwordx4 v[72:75], v31, s[36:39], s62 offen sc1\n" \
  "buffer_store_dwordx2 v[76:77], v32, s[36:39], s62 offen sc1\n" \
  "s_branch LDONE_%=\n" \
  "LSTART1_%=:\n" \
  "s_getpc_b64 s[82:83]\n" \
  "LPC1_%=:\n" \
  "ds_read_b128 v[100:103], v7 offset:0\n" \
  "ds_read_b128 v[104:107], v7 offset:4096\n" \
  "ds_read_b128 v[108:111], v7 offset:8192\n" \
  "ds_read_b128 v[112:115], v7 offset:12288\n" \
  "ds_read_b64 v[116:117], v7 offset:2048\n" \
  "ds_read_b64 v[118:119], v7 offset:6144\n" \
  "ds_read_b64 v[120:121], v7 offset:10240\n" \
  "ds_read_b64 v[122:123], v7 offset:14336\n" \
  "s_waitcnt lgkmcnt(7)\n" \
  "v_mfma_f32_16x16x32_bf16 v[124:127], v[100:103], v[184:187], 0\n" \
  "v_mfma_f32_16x16x32_bf16 v[128:131], v[100:103], v[208:211], 0\n" \
  "v_mfma_f32_16x16x32_bf16 v[132:135], v[100:103], v[232:235], 0\n" \
  "s_waitcnt lgkmcnt(6)\n" \
  "v_mfma_f32_16x16x32_bf16 v[124:127], v[104:107], v[188:191], v[124:127]\n" \
  "v_mfma_f32_16x16x32_bf16 v[128:131], v[104:107], v[212:215], v[128:131]\n" \
  "v_mfma_f32_16x16x32_bf16 v[132:135], v[104:107], v[236:239], v[132:135]\n" \
  "s_waitcnt lgkmcnt(5)\n" \
  "v_mfma_f32_16x16x32_bf16 v[124:127], v[108:111], v[192:195], v[124:127]\n" \
  "v_mfma_f32_16x16x32_bf16 v[128:131], v[108:111], v[216:219], v[128:131]\n" \
  "v_mfma_f32_16x16x32_bf16 v[132:135], v[108:111], v[240:243], v[132:135]\n" \
  "s_waitcnt lgkmcnt(4)\n" \
  "v_mfma_f32_16x16x32_bf16 v[124:127], v[112:115], v[196:199], v[124:127]\n" \
  "v_mfma_f32_16x16x32_bf16 v[128:131], v[112:115], v[220:223], v[128:131]\n" \
  "v_mfma_f32_16x16x32_bf16 v[132:135], v[112:115], v[244:247], v[132:135]\n" \
  "s_waitcnt lgkmcnt(2)\n" \
  "v_mfma_f32_16x16x32_bf16 v[124:127], v[116:119], v[200:203], v[124:127]\n" \
  "v_mfma_f32_16x16x32_bf16 v[128:131], v[116:119], v[224:227], v[128:131]\n" \
  "v_mfma_f32_16x16x32_bf16 v[132:135], v[116:119], v[248:251], v[132:135]\n" \
  "s_waitcnt lgkmcnt(0)\n" \
  "v_mfma_f32_16x16x32_bf16 v[124:127], v[120:123], v[204:207], v[124:127]\n" \
  "v_mfma_f32_16x16x32_bf16 v[128:131], v[120:123], v[228:231], v[128:131]\n" \
  "v_mfma_f32_16x16x32_bf16 v[132:135], v[120:123], v[252:255], v[132:135]\n" \
  "s_nop 3\n" \
  "v_cvt_pk_bf16_f32 v40, v124, v125\n" \
  "v_cvt_pk_bf16_f32 v41, v126, v127\n" \
  "v_cvt_pk_bf16_f32 v44, v128, v129\n" \
  "v_cvt_pk_bf16_f32 v45, v130, v131\n" \
  "s_mov_b64 exec, s[34:35]\n" \
  "v_cvt_pk_bf16_f32 v48, v132, v133\n" \
  "v_cvt_pk_bf16_f32 v49, v134, v135\n" \
  "s_mov_b64 exec, -1\n" \
  "LOOP1_%=:\n" \
  "s_barrier\n" \
  "ds_read_b128 v[64:67], v8\n" \
  "ds_read_b64 v[68:69], v9\n" \
  "ds_read_b128 v[100:103], v7 offset:1024\n" \
  "ds_read_b128 v[104:107], v7 offset:5120\n" \
  "ds_read_b128 v[108:111], v7 offset:9216\n" \
  "ds_read_b128 v[112:115], v7 offset:13312\n" \
  "ds_read_b64 v[116:117], v7 offset:2056\n" \
  "ds_read_b64 v[118:119], v7 offset:6152\n" \
  "ds_read_b64 v[120:121], v7 offset:10248\n" \
  "ds_read_b64 v[122:123], v7 offset:14344\n" \
  "v_mov_b32 v140, 0\n" \
  "v_mov_b32 v141, 0\n" \
  "v_mov_b32 v142, 0\n" \
  "v_mov_b32 v143, 0\n" \
  "v_mov_b32 v144, 0\n" \
  "v_mov_b32 v145, 0\n" \
  "v_mov_b32 v146, 0\n" \
  "v_mov_b32 v147, 0\n" \
  "v_xor_b32 v8, 0x4000, v8\n" \
  "v_xor_b32 v9, 0x4000, v9\n" \
  "v_xor_b32 v7, 0x4000, v7\n" \
  "v_mov_b32 v63, s68\n" \
  "s_mov_b32 s62, s41\n" \
  "s_mov_b32 s41, s71\n" \
  "s_add_u32 s68, s68, 1\n" \
  "s_mov_b32 s70, s42\n" \
  "s_add_u32 s63, s63, 2\n" \
  "s_cmp_eq_u32 s63, 60\n" \
  "s_cselect_b32 s63, 0, s63\n" \
  "s_lshr_b64 s[82:83], s[52:53], s63\n" \
  "s_lshl_b32 s42, s63, 21\n" \
  "s_lshl_b32 s76, s82, 12\n" \
  "s_and_b32 s76, s76, 0x2000\n" \
  "s_or_b32 s71, s42, s76\n" \
  "s_or_b32 s71, s71, 0x200000\n" \
  "s_lshl_b32 s76, s82, 13\n" \
  "s_and_b32 s76, s76, 0x2000\n" \
  "s_or_b32 s42, s42, s76\n" \
  "s_waitcnt lgkmcnt(7)\n" \
  "v_mfma_f32_16x16x32_bf16 v[124:127], v[100:103], v[184:187], 0\n" \
  "v_mfma_f32_16x16x32_bf16 v[128:131], v[100:103], v[208:211], 0\n" \
  "v_mfma_f32_16x16x32_bf16 v[132:135], v[100:103], v[232:235], 0\n" \
  "s_waitcnt lgkmcnt(6)\n" \
  "v_mfma_f32_16x16x32_bf16 v[124:127], v[104:107], v[188:191], v[124:127]\n" \
  "v_mfma_f32_16x16x32_bf16 v[128:131], v[104:107], v[212:215], v[128:131]\n" \
  "v_mfma_f32_16x16x32_bf16 v[132:135], v[104:107], v[236:239], v[132:135]\n" \
  "s_waitcnt lgkmcnt(5)\n" \
  "v_mfma_f32_16x16x32_bf16 v[124:127], v[108:111], v[192:195], v[124:127]\n" \
  "v_mfma_f32_16x16x32_bf16 v[128:131], v[108:111], v[216:219], v[128:131]\n" \
  "v_mfma_f32_16x16x32_bf16 v[132:135], v[108:111], v[240:243], v[132:135]\n" \
  "s_waitcnt lgkmcnt(4)\n" \
  "v_mfma_f32_16x16x32_bf16 v[124:127], v[112:115], v[196:199], v[124:127]\n" \
  "v_mfma_f32_16x16x32_bf16 v[128:131], v[112:115], v[220:223], v[128:131]\n" \
  "v_mfma_f32_16x16x32_bf16 v[132:135], v[112:115], v[244:247], v[132:135]\n" \
  "s_waitcnt lgkmcnt(2)\n" \
  "v_mfma_f32_16x16x32_bf16 v[124:127], v[116:119], v[200:203], v[124:127]\n" \
  "v_mfma_f32_16x16x32_bf16 v[128:131], v[116:119], v[224:227], v[128:131]\n" \
  "v_mfma_f32_16x16x32_bf16 v[132:135], v[116:119], v[248:251], v[132:135]\n" \
  "s_waitcnt lgkmcnt(0)\n" \
  "v_mfma_f32_16x16x32_bf16 v[124:127], v[120:123], v[204:207], v[124:127]\n" \
  "v_mfma_f32_16x16x32_bf16 v[128:131], v[120:123], v[228:231], v[128:131]\n" \
  "v_mfma_f32_16x16x32_bf16 v[132:135], v[120:123], v[252:255], v[132:135]\n" \
  "s_nop 3\n" \
  "v_cvt_pk_bf16_f32 v42, v124, v125\n" \
  "v_cvt_pk_bf16_f32 v43, v126, v127\n" \
  "v_cvt_pk_bf16_f32 v46, v128, v129\n" \
  "v_cvt_pk_bf16_f32 v47, v130, v131\n" \
  "s_mov_b64 exec, s[34:35]\n" \
  "v_cvt_pk_bf16_f32 v50, v132, v133\n" \
  "v_cvt_pk_bf16_f32 v51, v134, v135\n" \
  "s_mov_b64 exec, -1\n" \
  "s_waitcnt lgkmcnt(0)\n" \
  "s_waitcnt vmcnt(0)\n" \
  "LM1_%=:\n" \
  "s_barrier\n" \
  "ds_read_b128 v[100:103], v6 offset:0\n" \
  "ds_read_b128 v[104:107], v6 offset:1024\n" \
  "s_barrier\n" \
  "ds_read_b128 v[108:111], v6 offset:2048\n" \
  "ds_read_b128 v[112:115], v6 offset:3072\n" \
  "s_waitcnt lgkmcnt(3)\n" \
  "v_smfmac_f32_16x16x64_bf16 v[140:143], v[100:103], a[0:7], v0\n" \
  "v_smfmac_f32_16x16x64_bf16 v[144:147], v[100:103], a[80:87], v0\n" \
  "s_waitcnt lgkmcnt(2)\n" \
  "v_smfmac_f32_16x16x64_bf16 v[140:143], v[104:107], a[8:15], v0\n" \
  "v_smfmac_f32_16x16x64_bf16 v[144:147], v[104:107], a[88:95], v0\n" \
  "s_barrier\n" \
  "ds_read_b128 v[116:119], v6 offset:4096\n" \
  "ds_read_b128 v[120:123], v6 offset:5120\n" \
  "s_waitcnt lgkmcnt(3)\n" \
  "v_smfmac_f32_16x16x64_bf16 v[140:143], v[108:111], a[16:23], v0\n" \
  "v_smfmac_f32_16x16x64_bf16 v[144:147], v[108:111], a[96:103], v0\n" \
  "s_waitcnt lgkmcnt(2)\n" \
  "v_smfmac_f32_16x16x64_bf16 v[140:143], v[112:115], a[24:31], v0\n" \
  "v_smfmac_f32_16x16x64_bf16 v[144:147], v[112:115], a[104:111], v0\n" \
  "s_barrier\n" \
  "ds_read_b128 v[124:127], v6 offset:6144\n" \
  "ds_read_b128 v[128:131], v6 offset:7168\n" \
  "s_waitcnt lgkmcnt(3)\n" \
  "v_smfmac_f32_16x16x64_bf16 v[140:143], v[116:119], a[32:39], v0\n" \
  "v_smfmac_f32_16x16x64_bf16 v[144:147], v[116:119], a[112:119], v0\n" \
  "s_waitcnt lgkmcnt(2)\n" \
  "v_smfmac_f32_16x16x64_bf16 v[140:143], v[120:123], a[40:47], v0\n" \
  "v_smfmac_f32_16x16x64_bf16 v[144:147], v[120:123], a[120:127], v0\n" \
  "s_barrier\n" \
  "ds_read_b128 v[132:135], v6 offset:8192\n" \
  "ds_read_b128 v[136:139], v6 offset:9216\n" \
  "s_waitcnt lgkmcnt(3)\n" \
  "v_smfmac_f32_16x16x64_bf16 v[140:143], v[124:127], a[48:55], v0\n" \
  "v_smfmac_f32_16x16x64_bf16 v[144:147], v[124:127], a[128:135], v0\n" \
  "s_waitcnt lgkmcnt(2)\n" \
  "v_smfmac_f32_16x16x64_bf16 v[140:143], v[128:131], a[56:63], v0\n" \
  "v_smfmac_f32_16x16x64_bf16 v[144:147], v[128:131], a[136:143], v0\n" \
  "s_waitcnt lgkmcnt(1)\n" \
  "v_smfmac_f32_16x16x64_bf16 v[140:143], v[132:135], a[64:71], v0\n" \
  "v_smfmac_f32_16x16x64_bf16 v[144:147], v[132:135], a[144:151], v0\n" \
  "s_waitcnt lgkmcnt(0)\n" \
  "v_smfmac_f32_16x16x64_bf16 v[140:143], v[136:139], a[72:79], v0\n" \
  "v_smfmac_f32_16x16x64_bf16 v[144:147], v[136:139], a[152:159], v0\n" \
  "s_nop 7\n" \
  "v_permlane32_swap_b32 v140, v144\n" \
  "v_permlane32_swap_b32 v141, v145\n" \
  "v_permlane32_swap_b32 v142, v146\n" \
  "v_permlane32_swap_b32 v143, v147\n" \
  "v_pk_add_f32 v[152:153], v[140:141], v[144:145]\n" \
  "v_pk_add_f32 v[154:155], v[142:143], v[146:147]\n" \
  "v_cvt_pk_bf16_f32 v156, v152, v153\n" \
  "v_cvt_pk_bf16_f32 v157, v154, v155\n" \
  "ds_write_b64 v10, v[156:157]\n" \
  "v_cmp_u_f32 vcc, v152, v153\n" \
  "v_cmp_u_f32 s[82:83], v154, v155\n" \
  "s_or_b64 vcc, vcc, s[82:83]\n" \
  "s_cbranch_vccnz LMF1_%=\n" \
  "LMOK1_%=:\n" \
  "s_waitcnt lgkmcnt(0)\n" \
  "s_barrier\n" \
  "ds_read_b64_tr_b16 v[72:73], v11\n" \
  "ds_read_b64_tr_b16 v[74:75], v11 offset:128\n" \
  "ds_read_b64_tr_b16 v[76:77], v12\n" \
  "s_waitcnt lgkmcnt(1)\n" \
  "v_mfma_f32_16x16x32_bf16 v[152:155], v[64:67], v[72:75], 0\n" \
  "s_waitcnt lgkmcnt(0)\n" \
  "v_mfma_f32_16x16x32_bf16 v[152:155], v[68:71], v[76:79], v[152:155]\n" \
  "s_nop 7\n" \
  "buffer_store_dwordx4 v[152:155], v13, s[36:39], s69 offen sc1\n" \
  "s_cmp_eq_u32 s68, 64\n" \
  "s_cbranch_scc1 LEND1_%=\n" \
  "s_barrier\n" \
  "s_nop 5\n" \
  "buffer_load_dwordx4 v[156:159], v15, s[36:39], s69 offen sc1\n" \
  "buffer_load_dword v160, v16, s[36:39], s69 offen sc1\n" \
  "ds_read_b32 v180, v38\n" \
  "s_mov_b32 s72, s70\n" \
  "v_mov_b32 v136, 0\n" \
  "v_mov_b32 v137, 0\n" \
  "v_mov_b32 v138, 0\n" \
  "v_mov_b32 v139, 0\n" \
  "v_mov_b32 v140, 0\n" \
  "v_mov_b32 v141, 0\n" \
  "v_mov_b32 v142, 0\n" \
  "v_mov_b32 v143, 0\n" \
  "v_mov_b32 v144, 0\n" \
  "v_mov_b32 v145, 0\n" \
  "v_mov_b32 v146, 0\n" \
  "v_mov_b32 v147, 0\n" \
  "v_xor_b32 v38, 0x4000, v38\n" \
  "s_mov_b32 s73, s89\n" \
  "s_add_u32 s76, s68, 2\n" \
  "s_min_u32 s76, s76, 63\n" \
  "s_lshr_b32 s77, s76, 4\n" \
  "s_lshl_b32 s77, s77, 13\n" \
  "s_lshl_b32 s76, s76, 9\n" \
  "s_add_u32 s89, s76, s77\n" \
  "s_waitcnt lgkmcnt(0)\n" \
  "v_cmp_gt_u32 vcc, 24, v180\n" \
  "s_nop 3\n" \
  "s_cbranch_vccnz LFF1_%=\n" \
  "LFOK1_%=:\n" \
  "v_mov_b32 v183, v182\n" \
  "s_nop 0\n" \
  "v_mov_b32 v183, v182\n" \
  "s_nop 0\n" \
  "v_mov_b32 v183, v182\n" \
  "s_nop 0\n" \
  "v_mov_b32 v183, v182\n" \
  "s_nop 0\n" \
  "v_mov_b32 v183, v182\n" \
  "s_nop 0\n" \
  "v_mov_b32 v183, v182\n" \
  "s_nop 0\n" \
  "v_mov_b32 v183, v182\n" \
  "s_nop 0\n" \
  "v_mov_b32 v183, v182\n" \
  "s_nop 0\n" \
  "v_mov_b32 v183, v182\n" \
  "s_nop 0\n" \
  "v_mov_b32 v183, v182\n" \
  "s_nop 0\n" \
  "v_mov_b32 v183, v182\n" \
  "s_nop 0\n" \
  "v_mov_b32 v183, v182\n" \
  "s_nop 0\n" \
  "v_mov_b32 v183, v182\n" \
  "s_nop 0\n" \
  "v_mov_b32 v183, v182\n" \
  "s_nop 0\n" \
  "v_mov_b32 v183, v182\n" \
  "s_nop 0\n" \
  "v_mov_b32 v183, v182\n" \
  "s_nop 0\n" \
  "v_mov_b32 v183, v182\n" \
  "s_nop 0\n" \
  "v_mov_b32 v183, v182\n" \
  "s_nop 0\n" \
  "v_mov_b32 v183, v182\n" \
  "s_nop 0\n" \
  "v_mov_b32 v183, v182\n" \
  "s_nop 0\n" \
  "v_mov_b32 v183, v182\n" \
  "s_nop 0\n" \
  "v_mov_b32 v183, v182\n" \
  "s_nop 0\n" \
  "v_mov_b32 v183, v182\n" \
  "s_nop 0\n" \
  "v_mov_b32 v183, v182\n" \
  "s_nop 0\n" \
  "v_mov_b32 v183, v182\n" \
  "s_nop 0\n" \
  "v_mov_b32 v183, v182\n" \
  "s_nop 0\n" \
  "v_mov_b32 v183, v182\n" \
  "s_nop 0\n" \
  "v_mov_b32 v183, v182\n" \
  "s_nop 0\n" \
  "v_mov_b32 v183, v182\n" \
  "s_nop 0\n" \
  "v_mov_b32 v183, v182\n" \
  "s_nop 0\n" \
  "LQ1_%=:\n" \
  "s_barrier\n" \
  "ds_read_b64_tr_b16 v[100:101], v20\n" \
  "ds_read_b64_tr_b16 v[102:103], v21\n" \
  "ds_read_b64_tr_b16 v[104:105], v22\n" \
  "ds_read_b64_tr_b16 v[106:107], v23\n" \
  "ds_read_b64_tr_b16 v[108:109], v24\n" \
  "ds_read_b64_tr_b16 v[110:111], v25\n" \
  "v_mov_b32 v183, v182\n" \
  "s_nop 0\n" \
  "v_mov_b32 v183, v182\n" \
  "s_nop 0\n" \
  "v_mov_b32 v183, v182\n" \
  "s_nop 0\n" \
  "v_mov_b32 v183, v182\n" \
  "s_nop 0\n" \
  "s_waitcnt lgkmcnt(4)\n" \
  "v_smfmac_f32_16x16x64_bf16 v[144:147], v[100:103], v[232:239], v0\n" \
  "v_mov_b32 v183, v182\n" \
  "v_smfmac_f32_16x16x64_bf16 v[136:139], v[100:103], v[184:191], v0\n" \
  "v_mov_b32 v183, v182\n" \
  "v_smfmac_f32_16x16x64_bf16 v[140:143], v[100:103], v[208:215], v0\n" \
  "v_mov_b32 v183, v182\n" \
  "s_waitcnt lgkmcnt(0)\n" \
  "v_smfmac_f32_16x16x64_bf16 v[144:147], v[104:107], v[240:247], v0\n" \
  "v_mov_b32 v183, v182\n" \
  "v_smfmac_f32_16x16x64_bf16 v[136:139], v[104:107], v[192:199], v0\n" \
  "v_mov_b32 v183, v182\n" \
  "s_getreg_b32 s76, hwreg(HW_REG_IB_STS, 0, 4)\n" \
  "v_mov_b32 v183, v182\n" \
  "s_cmp_le_u32 s76, 0\n" \
  "s_cbranch_scc1 LHXQ1_%=\n" \
  "s_getreg_b32 s76, hwreg(HW_REG_IB_STS, 0, 4)\n" \
  "v_mov_b32 v183, v182\n" \
  "s_cmp_le_u32 s76, 0\n" \
  "s_cbranch_scc1 LHXQ1_%=\n" \
  "s_getreg_b32 s76, hwreg(HW_REG_IB_STS, 0, 4)\n" \
  "v_mov_b32 v183, v182\n" \
  "s_cmp_le_u32 s76, 0\n" \
  "s_cbranch_scc1 LHXQ1_%=\n" \
  "s_getreg_b32 s76, hwreg(HW_REG_IB_STS, 0, 4)\n" \
  "v_mov_b32 v183, v182\n" \
  "s_cmp_le_u32 s76, 0\n" \
  "s_cbranch_scc1 LHXQ1_%=\n" \
  "s_getreg_b32 s76, hwreg(HW_REG_IB_STS, 0, 4)\n" \
  "v_mov_b32 v183, v182\n" \
  "s_cmp_le_u32 s76, 0\n" \
  "s_cbranch_scc1 LHXQ1_%=\n" \
  "s_getreg_b32 s76, hwreg(HW_REG_IB_STS, 0, 4)\n" \
  "v_mov_b32 v183, v182\n" \
  "s_cmp_le_u32 s76, 0\n" \
  "s_cbranch_scc1 LHXQ1_%=\n" \
  "s_getreg_b32 s76, hwreg(HW_REG_IB_STS, 0, 4)\n" \
  "v_mov_b32 v183, v182\n" \
  "s_cmp_le_u32 s76, 0\n" \
  "s_cbranch_scc1 LHXQ1_%=\n" \
  "s_getreg_b32 s76, hwreg(HW_REG_IB_STS, 0, 4)\n" \
  "v_mov_b32 v183, v182\n" \
  "s_cmp_le_u32 s76, 0\n" \
  "s_cbranch_scc1 LHXQ1_%=\n" \
  "s_getreg_b32 s76, hwreg(HW_REG_IB_STS, 0, 4)\n" \
  "v_mov_b32 v183, v182\n" \
  "s_cmp_le_u32 s76, 0\n" \
  "s_cbranch_scc1 LHXQ1_%=\n" \
  "s_getreg_b32 s76, hwreg(HW_REG_IB_STS, 0, 4)\n" \
  "v_mov_b32 v183, v182\n" \
  "s_cmp_le_u32 s76, 0\n" \
  "s_cbranch_scc1 LHXQ1_%=\n" \
  "LHXQ1_%=:\n" \
  "s_waitcnt vmcnt(0)\n" \
  "v_mov_b32 v115, v160\n" \
  "v_mov_b32 v114, v160\n" \
  "v_permlane32_swap_b32 v156, v158\n" \
  "v_permlane32_swap_b32 v157, v159\n" \
  "v_pk_add_f32 v[112:113], v[156:157], v[158:159]\n" \
  "v_permlane32_swap_b32 v114, v115\n" \
  "v_add_f32 v117, v114, v115\n" \
  "v_permlane16_swap_b32 v112, v113\n" \
  "v_add_f32 v114, v114, v115\n" \
  "v_add_f32 v116, v112, v113\n" \
  "v_smfmac_f32_16x16x64_bf16 v[140:143], v[104:107], v[216:223], v0\n" \
  "v_permlane16_swap_b32 v117, v114\n" \
  "v_mul_f32 v116, v116, v60\n" \
  "v_add_f32 v117, v117, v114\n" \
  "v_exp_f32 v112, v116\n" \
  "v_mul_f32 v117, v117, v60\n" \
  "v_max_f32 v118, |v116|, |v117|\n" \
  "v_exp_f32 v113, v117\n" \
  "v_cmp_lt_f32 vcc, 0x42800000, v118\n" \
  "v_cvt_pk_bf16_f32 v112, v112, v113\n" \
  "v_smfmac_f32_16x16x64_bf16 v[144:147], v[108:111], v[248:255], v0\n" \
  "s_cbranch_vccnz LMX1_%=\n" \
  "LMY1_%=:\n" \
  "ds_write_b16 v18, v112\n" \
  "ds_write_b16_d16_hi v19, v112\n" \
  "v_smfmac_f32_16x16x64_bf16 v[136:139], v[108:111], v[200:207], v0\n" \
  "v_smfmac_f32_16x16x64_bf16 v[140:143], v[108:111], v[224:231], v0\n" \
  "s_mov_b64 exec, s[34:35]\n" \
  "v_cvt_pk_bf16_f32 v92, v144, v145\n" \
  "v_cvt_pk_bf16_f32 v93, v146, v147\n" \
  "s_mov_b64 exec, -1\n" \
  "v_cvt_pk_bf16_f32 v84, v136, v137\n" \
  "v_cvt_pk_bf16_f32 v85, v138, v139\n" \
  "s_nop 1\n" \
  "v_cvt_pk_bf16_f32 v88, v140, v141\n" \
  "v_cvt_pk_bf16_f32 v89, v142, v143\n" \
  "s_waitcnt lgkmcnt(0)\n" \
  "s_barrier\n" \
  "ds_read_b128 v[120:123], v28\n" \
  "ds_read_b64 v[80:81], v36\n" \
  "v_mov_b32 v183, v182\n" \
  "s_nop 0\n" \
  "v_mov_b32 v183, v182\n" \
  "s_nop 0\n" \
  "v_mov_b32 v183, v182\n" \
  "s_nop 0\n" \
  "v_mov_b32 v183, v182\n" \
  "s_nop 0\n" \
  "s_waitcnt lgkmcnt(1)\n" \
  "v_mfma_f32_16x16x32_bf16 v[172:175], v[48:51], v[120:123], 0\n" \
  "v_mfma_f32_16x16x32_bf16 v[164:167], v[40:43], v[120:123], 0\n" \
  "s_waitcnt lgkmcnt(0)\n" \
  "v_mfma_f32_16x16x32_bf16 v[172:175], v[92:95], v[80:83], v[172:175]\n" \
  "ds_read_b32 v180, v62\n" \
  "v_mfma_f32_16x16x32_bf16 v[168:171], v[44:47], v[120:123], 0\n" \
  "v_mfma_f32_16x16x32_bf16 v[164:167], v[84:87], v[80:83], v[164:167]\n" \
  "v_rcp_f32 v177, v172\n" \
  "v_mfma_f32_16x16x32_bf16 v[168:171], v[88:91], v[80:83], v[168:171]\n" \
  "v_mov_b32 v176, v177\n" \
  "v_cmp_u_f32 vcc, v172, v172\n" \
  "s_nop 0\n" \
  "v_permlane32_swap_b32 v177, v176\n" \
  "v_pk_mul_f32 v[164:165], v[164:165], v[176:177] op_sel_hi:[1,0]\n" \
  "v_pk_mul_f32 v[166:167], v[166:167], v[176:177] op_sel_hi:[1,0]\n" \
  "v_pk_mul_f32 v[172:173], v[172:173], v[176:177] op_sel_hi:[1,0]\n" \
  "v_pk_mul_f32 v[174:175], v[174:175], v[176:177] op_sel_hi:[1,0]\n" \
  "v_pk_mul_f32 v[168:169], v[168:169], v[176:177] op_sel_hi:[1,0]\n" \
  "v_pk_mul_f32 v[170:171], v[170:171], v[176:177] op_sel_hi:[1,0]\n" \
  "v_cvt_pk_bf16_f32 v164, v164, v165\n" \
  "v_cvt_pk_bf16_f32 v165, v166, v167\n" \
  "v_cvt_pk_bf16_f32 v166, v168, v169\n" \
  "v_cvt_pk_bf16_f32 v167, v170, v171\n" \
  "s_cbranch_vccnz LQF1_%=\n" \
  "LQOK1_%=:\n" \
  "buffer_store_dwordx4 v[164:167], v26, s[36:39], s69 offen sc1\n" \
  "v_cvt_pk_bf16_f32 v172, v172, v173\n" \
  "v_cvt_pk_bf16_f32 v173, v174, v175\n" \
  "buffer_store_dwordx2 v[172:173], v27, s[36:39], s69 offen sc1\n" \
  "s_waitcnt lgkmcnt(0)\n" \
  "v_readfirstlane_b32 s50, v180\n" \
  "ds_read_b128 v[100:103], v7 offset:0\n" \
  "ds_read_b128 v[104:107], v7 offset:4096\n" \
  "ds_read_b128 v[108:111], v7 offset:8192\n" \
  "ds_read_b128 v[112:115], v7 offset:12288\n" \
  "ds_read_b64 v[116:117], v7 offset:2048\n" \
  "ds_read_b64 v[118:119], v7 offset:6144\n" \
  "ds_read_b64 v[120:121], v7 offset:10240\n" \
  "ds_read_b64 v[122:123], v7 offset:14336\n" \
  "s_waitcnt lgkmcnt(7)\n" \
  "v_mfma_f32_16x16x32_bf16 v[124:127], v[100:103], v[184:187], 0\n" \
  "v_mfma_f32_16x16x32_bf16 v[128:131], v[100:103], v[208:211], 0\n" \
  "v_mfma_f32_16x16x32_bf16 v[132:135], v[100:103], v[232:235], 0\n" \
  "s_waitcnt lgkmcnt(6)\n" \
  "v_mfma_f32_16x16x32_bf16 v[124:127], v[104:107], v[188:191], v[124:127]\n" \
  "v_mfma_f32_16x16x32_bf16 v[128:131], v[104:107], v[212:215], v[128:131]\n" \
  "v_mfma_f32_16x16x32_bf16 v[132:135], v[104:107], v[236:239], v[132:135]\n" \
  "s_waitcnt lgkmcnt(5)\n" \
  "v_mfma_f32_16x16x32_bf16 v[124:127], v[108:111], v[192:195], v[124:127]\n" \
  "v_mfma_f32_16x16x32_bf16 v[128:131], v[108:111], v[216:219], v[128:131]\n" \
  "v_mfma_f32_16x16x32_bf16 v[132:135], v[108:111], v[240:243], v[132:135]\n" \
  "s_waitcnt lgkmcnt(4)\n" \
  "v_mfma_f32_16x16x32_bf16 v[124:127], v[112:115], v[196:199], v[124:127]\n" \
  "v_mfma_f32_16x16x32_bf16 v[128:131], v[112:115], v[220:223], v[128:131]\n" \
  "v_mfma_f32_16x16x32_bf16 v[132:135], v[112:115], v[244:247], v[132:135]\n" \
  "s_waitcnt lgkmcnt(2)\n" \
  "v_mfma_f32_16x16x32_bf16 v[124:127], v[116:119], v[200:203], v[124:127]\n" \
  "v_mfma_f32_16x16x32_bf16 v[128:131], v[116:119], v[224:227], v[128:131]\n" \
  "v_mfma_f32_16x16x32_bf16 v[132:135], v[116:119], v[248:251], v[132:135]\n" \
  "s_waitcnt lgkmcnt(0)\n" \
  "v_mfma_f32_16x16x32_bf16 v[124:127], v[120:123], v[204:207], v[124:127]\n" \
  "v_mfma_f32_16x16x32_bf16 v[128:131], v[120:123], v[228:231], v[128:131]\n" \
  "v_mfma_f32_16x16x32_bf16 v[132:135], v[120:123], v[252:255], v[132:135]\n" \
  "s_nop 3\n" \
  "v_cvt_pk_bf16_f32 v40, v124, v125\n" \
  "v_cvt_pk_bf16_f32 v41, v126, v127\n" \
  "v_cvt_pk_bf16_f32 v44, v128, v129\n" \
  "v_cvt_pk_bf16_f32 v45, v130, v131\n" \
  "s_mov_b64 exec, s[34:35]\n" \
  "v_cvt_pk_bf16_f32 v48, v132, v133\n" \
  "v_cvt_pk_bf16_f32 v49, v134, v135\n" \
  "s_mov_b64 exec, -1\n" \
  "LUB1_%=:\n" \
  "s_memtime s[80:81]\n" \
  "s_waitcnt lgkmcnt(0)\n" \
  "s_sub_i32 s76, s50, s80\n" \
  "s_cmp_gt_i32 s76, -160\n" \
  "s_cbranch_scc1 LUB1_%=\n" \
  "s_mov_b32 m0, s60\n" \
  "s_add_u32 s77, s71, 16384\n" \
  "buffer_load_dwordx4 v4, s[36:39], s71 offen sc1 lds\n" \
  "buffer_load_dwordx4 v4, s[36:39], s71 offen offset:1024 sc1 lds\n" \
  "buffer_load_dwordx4 v4, s[36:39], s71 offen offset:2048 sc1 lds\n" \
  "buffer_load_dwordx4 v5, s[36:39], s71 offen offset:3072 sc1 lds\n" \
  "s_add_u32 m0, s60, 4096\n" \
  "s_add_u32 s76, s71, 1024\n" \
  "buffer_load_dwordx4 v4, s[36:39], s77 offen sc1 lds\n" \
  "buffer_load_dwordx4 v4, s[36:39], s77 offen offset:1024 sc1 lds\n" \
  "buffer_load_dwordx4 v4, s[36:39], s77 offen offset:2048 sc1 lds\n" \
  "buffer_load_dwordx4 v37, s[36:39], s89 offen offset:3072 sc1 lds\n" \
  "s_xor_b32 s60, s60, 0x4000\n" \
  "s_mov_b32 s69, s70\n" \
  "s_branch LOOP1_%=\n" \
  "LFF1_%=:\n" \
  "buffer_load_dword v180, v37, s[36:39], s73 offen offset:3072 sc1\n" \
  "s_waitcnt vmcnt(0)\n" \
  "v_cmp_gt_u32 vcc, 24, v180\n" \
  "s_nop 3\n" \
  "s_cbranch_vccz LFOK1_%=\n" \
  "s_add_u32 s67, s67, 1\n" \
  "s_cmp_gt_u32 s67, 20000\n" \
  "s_cbranch_scc1 LFOK1_%=\n" \
  "s_sleep 16\n" \
  "s_branch LFF1_%=\n" \
  "LMX1_%=:\n" \
  "v_max_f32 v118, v116, v117\n" \
  "v_mov_b32 v119, v118\n" \
  "s_nop 1\n" \
  "v_permlane16_swap_b32 v118, v119\n" \
  "v_max_f32 v118, v118, v119\n" \
  "v_mov_b32 v119, v118\n" \
  "s_nop 1\n" \
  "v_permlane32_swap_b32 v118, v119\n" \
  "v_max_f32 v118, v118, v119\n" \
  "s_nop 1\n" \
  "v_max_f32_dpp v118, v118, v118 row_ror:8 row_mask:0xf bank_mask:0xf\n" \
  "s_nop 1\n" \
  "v_max_f32_dpp v118, v118, v118 quad_perm:[2,3,0,1] row_mask:0xf bank_mask:0xf\n" \
  "s_nop 1\n" \
  "v_max_f32_dpp v118, v118, v118 row_ror:4 row_mask:0xf bank_mask:0xf\n" \
  "v_sub_f32 v116, v116, v118\n" \
  "v_sub_f32 v117, v117, v118\n" \
  "v_exp_f32 v112, v116\n" \
  "v_exp_f32 v113, v117\n" \
  "s_nop 0\n" \
  "v_cvt_pk_bf16_f32 v112, v112, v113\n" \
  "s_branch LMY1_%=\n" \
  "LMF1_%=:\n" \
  "v_mov_b32 v180, 0\n" \
  "v_pk_max_u16 v180, v180, v100\n" \
  "v_pk_max_u16 v180, v180, v101\n" \
  "v_pk_max_u16 v180, v180, v102\n" \
  "v_pk_max_u16 v180, v180, v103\n" \
  "v_pk_max_u16 v180, v180, v104\n" \
  "v_pk_max_u16 v180, v180, v105\n" \
  "v_pk_max_u16 v180, v180, v106\n" \
  "v_pk_max_u16 v180, v180, v107\n" \
  "v_pk_max_u16 v180, v180, v108\n" \
  "v_pk_max_u16 v180, v180, v109\n" \
  "v_pk_max_u16 v180, v180, v110\n" \
  "v_pk_max_u16 v180, v180, v111\n" \
  "v_pk_max_u16 v180, v180, v112\n" \
  "v_pk_max_u16 v180, v180, v113\n" \
  "v_pk_max_u16 v180, v180, v114\n" \
  "v_pk_max_u16 v180, v180, v115\n" \
  "v_pk_max_u16 v180, v180, v116\n" \
  "v_pk_max_u16 v180, v180, v117\n" \
  "v_pk_max_u16 v180, v180, v118\n" \
  "v_pk_max_u16 v180, v180, v119\n" \
  "v_pk_max_u16 v180, v180, v120\n" \
  "v_pk_max_u16 v180, v180, v121\n" \
  "v_pk_max_u16 v180, v180, v122\n" \
  "v_pk_max_u16 v180, v180, v123\n" \
  "v_pk_max_u16 v180, v180, v124\n" \
  "v_pk_max_u16 v180, v180, v125\n" \
  "v_pk_max_u16 v180, v180, v126\n" \
  "v_pk_max_u16 v180, v180, v127\n" \
  "v_pk_max_u16 v180, v180, v128\n" \
  "v_pk_max_u16 v180, v180, v129\n" \
  "v_pk_max_u16 v180, v180, v130\n" \
  "v_pk_max_u16 v180, v180, v131\n" \
  "v_pk_max_u16 v180, v180, v132\n" \
  "v_pk_max_u16 v180, v180, v133\n" \
  "v_pk_max_u16 v180, v180, v134\n" \
  "v_pk_max_u16 v180, v180, v135\n" \
  "v_pk_max_u16 v180, v180, v136\n" \
  "v_pk_max_u16 v180, v180, v137\n" \
  "v_pk_max_u16 v180, v180, v138\n" \
  "v_pk_max_u16 v180, v180, v139\n" \
  "v_lshrrev_b32 v181, 16, v180\n" \
  "v_and_b32 v180, 0xffff, v180\n" \
  "v_max_u32 v180, v180, v181\n" \
  "v_cmp_eq_u32 vcc, 0xffff, v180\n" \
  "s_nop 3\n" \
  "s_cbranch_vccz LMOK1_%=\n" \
  "s_add_u32 s67, s67, 1\n" \
  "s_cmp_gt_u32 s67, 20000\n" \
  "s_cbranch_scc1 LMOK1_%=\n" \
  "s_add_u32 s64, s64, 1\n" \
  "s_waitcnt vmcnt(0) lgkmcnt(0)\n" \
  "s_barrier\n" \
  "v_mov_b32 v140, 0\n" \
  "v_mov_b32 v141, 0\n" \
  "v_mov_b32 v142, 0\n" \
  "v_mov_b32 v143, 0\n" \
  "v_mov_b32 v144, 0\n" \
  "v_mov_b32 v145, 0\n" \
  "v_mov_b32 v146, 0\n" \
  "v_mov_b32 v147, 0\n" \
  "v_mov_b32 v148, 0\n" \
  "v_mov_b32 v149, 0\n" \
  "v_mov_b32 v150, 0\n" \
  "v_mov_b32 v151, 0\n" \
  "s_branch LM1_%=\n" \
  "LQF1_%=:\n" \
  "s_waitcnt lgkmcnt(0)\n" \
  "v_mov_b32 v116, 0\n" \
  "v_pk_max_u16 v116, v116, v100\n" \
  "v_pk_max_u16 v116, v116, v101\n" \
  "v_pk_max_u16 v116, v116, v102\n" \
  "v_pk_max_u16 v116, v116, v103\n" \
  "v_pk_max_u16 v116, v116, v104\n" \
  "v_pk_max_u16 v116, v116, v105\n" \
  "v_pk_max_u16 v116, v116, v106\n" \
  "v_pk_max_u16 v116, v116, v107\n" \
  "v_pk_max_u16 v116, v116, v108\n" \
  "v_pk_max_u16 v116, v116, v109\n" \
  "v_pk_max_u16 v116, v116, v110\n" \
  "v_pk_max_u16 v116, v116, v111\n" \
  "v_lshrrev_b32 v117, 16, v116\n" \
  "v_and_b32 v116, 0xffff, v116\n" \
  "v_max_u32 v116, v116, v117\n" \
  "v_cmp_eq_u32 vcc, 0xffff, v116\n" \
  "v_max3_u32 v118, v156, v157, v158\n" \
  "v_max3_u32 v118, v118, v159, v160\n" \
  "v_cmp_eq_u32 s[82:83], -1, v118\n" \
  "s_nop 3\n" \
  "s_or_b64 vcc, vcc, s[82:83]\n" \
  "s_cselect_b32 s76, 1, 0\n" \
  "v_mov_b32 v116, s76\n" \
  "ds_write_b32 v62, v116 offset:20\n" \
  "s_waitcnt lgkmcnt(0)\n" \
  "s_barrier\n" \
  "ds_read_b128 v[116:119], v62 offset:16\n" \
  "s_waitcnt lgkmcnt(0)\n" \
  "s_barrier\n" \
  "v_or3_b32 v116, v116, v117, v118\n" \
  "v_or_b32 v116, v116, v119\n" \
  "v_cmp_ne_u32 vcc, 0, v116\n" \
  "s_nop 3\n" \
  "s_cbranch_vccz LQOK1_%=\n" \
  "s_add_u32 s67, s67, 1\n" \
  "s_cmp_gt_u32 s67, 20000\n" \
  "s_cbranch_scc1 LQOK1_%=\n" \
  "s_add_u32 s65, s65, 1\n" \
  "s_barrier\n" \
  "s_nop 5\n" \
  "buffer_load_dwordx4 v[156:159], v15, s[36:39], s69 offen sc1\n" \
  "buffer_load_dword v160, v16, s[36:39], s69 offen sc1\n" \
  "v_mov_b32 v136, 0\n" \
  "v_mov_b32 v137, 0\n" \
  "v_mov_b32 v138, 0\n" \
  "v_mov_b32 v139, 0\n" \
  "v_mov_b32 v140, 0\n" \
  "v_mov_b32 v141, 0\n" \
  "v_mov_b32 v142, 0\n" \
  "v_mov_b32 v143, 0\n" \
  "v_mov_b32 v144, 0\n" \
  "v_mov_b32 v145, 0\n" \
  "v_mov_b32 v146, 0\n" \
  "v_mov_b32 v147, 0\n" \
  "s_waitcnt vmcnt(0) lgkmcnt(0)\n" \
  "s_branch LQ1_%=\n" \
  "LEND1_%=:\n" \
  "s_branch LDONE_%=\n" \
  "LSTART2_%=:\n" \
  "s_getpc_b64 s[82:83]\n" \
  "LPC2_%=:\n" \
  "s_add_u32 s84, s82, LEA2_%=-LPC2_%=+24\n" \
  "s_addc_u32 s85, s83, 0\n" \
  "s_add_u32 s86, s82, LEQ2_%=-LPC2_%=+24\n" \
  "s_addc_u32 s87, s83, 0\n" \
  "s_memrealtime s[74:75]\n" \
  "s_memtime s[80:81]\n" \
  "s_waitcnt lgkmcnt(0)\n" \
  "s_add_u32 s49, s80, 0x1000000\n" \
  "s_mov_b32 s59, s74\n" \
  "s_mov_b32 s60, s80\n" \
  "s_mov_b32 s51, s61\n" \
  "s_mov_b32 s61, 64\n" \
  "s_mov_b32 s73, 0\n" \
  "s_mov_b32 s89, 0\n" \
  "s_mov_b32 s62, 3\n" \
  "s_mov_b32 s50, 16\n" \
  "buffer_load_dword v63, v29, s[36:39], 0 offen sc1\n" \
  "s_add_u32 s55, s55, 16\n" \
  "s_mul_i32 s44, s55, 24\n" \
  "s_add_u32 s56, s56, 16\n" \
  "s_mul_i32 s45, s56, 24\n" \
  "s_add_u32 s57, s57, 16\n" \
  "s_mul_i32 s46, s57, 24\n" \
  "ds_read_b128 v[100:103], v7 offset:0\n" \
  "ds_read_b128 v[104:107], v7 offset:4096\n" \
  "ds_read_b128 v[108:111], v7 offset:8192\n" \
  "ds_read_b128 v[112:115], v7 offset:12288\n" \
  "ds_read_b64 v[116:117], v7 offset:2048\n" \
  "ds_read_b64 v[118:119], v7 offset:6144\n" \
  "ds_read_b64 v[120:121], v7 offset:10240\n" \
  "ds_read_b64 v[122:123], v7 offset:14336\n" \
  "s_waitcnt lgkmcnt(7)\n" \
  "v_mfma_f32_16x16x32_bf16 v[124:127], v[100:103], v[184:187], 0\n" \
  "v_mfma_f32_16x16x32_bf16 v[128:131], v[100:103], v[208:211], 0\n" \
  "v_mfma_f32_16x16x32_bf16 v[132:135], v[100:103], v[232:235], 0\n" \
  "s_waitcnt lgkmcnt(6)\n" \
  "v_mfma_f32_16x16x32_bf16 v[124:127], v[104:107], v[188:191], v[124:127]\n" \
  "v_mfma_f32_16x16x32_bf16 v[128:131], v[104:107], v[212:215], v[128:131]\n" \
  "v_mfma_f32_16x16x32_bf16 v[132:135], v[104:107], v[236:239], v[132:135]\n" \
  "s_waitcnt lgkmcnt(5)\n" \
  "v_mfma_f32_16x16x32_bf16 v[124:127], v[108:111], v[192:195], v[124:127]\n" \
  "v_mfma_f32_16x16x32_bf16 v[128:131], v[108:111], v[216:219], v[128:131]\n" \
  "v_mfma_f32_16x16x32_bf16 v[132:135], v[108:111], v[240:243], v[132:135]\n" \
  "s_waitcnt lgkmcnt(4)\n" \
  "v_mfma_f32_16x16x32_bf16 v[124:127], v[112:115], v[196:199], v[124:127]\n" \
  "v_mfma_f32_16x16x32_bf16 v[128:131], v[112:115], v[220:223], v[128:131]\n" \
  "v_mfma_f32_16x16x32_bf16 v[132:135], v[112:115], v[244:247], v[132:135]\n" \
  "s_waitcnt lgkmcnt(2)\n" \
  "v_mfma_f32_16x16x32_bf16 v[124:127], v[116:119], v[200:203], v[124:127]\n" \
  "v_mfma_f32_16x16x32_bf16 v[128:131], v[116:119], v[224:227], v[128:131]\n" \
  "v_mfma_f32_16x16x32_bf16 v[132:135], v[116:119], v[248:251], v[132:135]\n" \
  "s_waitcnt lgkmcnt(0)\n" \
  "v_mfma_f32_16x16x32_bf16 v[124:127], v[120:123], v[204:207], v[124:127]\n" \
  "v_mfma_f32_16x16x32_bf16 v[128:131], v[120:123], v[228:231], v[128:131]\n" \
  "v_mfma_f32_16x16x32_bf16 v[132:135], v[120:123], v[252:255], v[132:135]\n" \
  "s_nop 3\n" \
  "v_cvt_pk_bf16_f32 v40, v124, v125\n" \
  "v_cvt_pk_bf16_f32 v41, v126, v127\n" \
  "v_cvt_pk_bf16_f32 v44, v128, v129\n" \
  "v_cvt_pk_bf16_f32 v45, v130, v131\n" \
  "s_mov_b64 exec, s[34:35]\n" \
  "v_cvt_pk_bf16_f32 v48, v132, v133\n" \
  "v_cvt_pk_bf16_f32 v49, v134, v135\n" \
  "s_mov_b64 exec, -1\n" \
  "ds_read_b128 v[100:103], v7 offset:1024\n" \
  "ds_read_b128 v[104:107], v7 offset:5120\n" \
  "ds_read_b128 v[108:111], v7 offset:9216\n" \
  "ds_read_b128 v[112:115], v7 offset:13312\n" \
  "ds_read_b64 v[116:117], v7 offset:2056\n" \
  "ds_read_b64 v[118:119], v7 offset:6152\n" \
  "ds_read_b64 v[120:121], v7 offset:10248\n" \
  "ds_read_b64 v[122:123], v7 offset:14344\n" \
  "s_waitcnt lgkmcnt(7)\n" \
  "v_mfma_f32_16x16x32_bf16 v[124:127], v[100:103], v[184:187], 0\n" \
  "v_mfma_f32_16x16x32_bf16 v[128:131], v[100:103], v[208:211], 0\n" \
  "v_mfma_f32_16x16x32_bf16 v[132:135], v[100:103], v[232:235], 0\n" \
  "s_waitcnt lgkmcnt(6)\n" \
  "v_mfma_f32_16x16x32_bf16 v[124:127], v[104:107], v[188:191], v[124:127]\n" \
  "v_mfma_f32_16x16x32_bf16 v[128:131], v[104:107], v[212:215], v[128:131]\n" \
  "v_mfma_f32_16x16x32_bf16 v[132:135], v[104:107], v[236:239], v[132:135]\n" \
  "s_waitcnt lgkmcnt(5)\n" \
  "v_mfma_f32_16x16x32_bf16 v[124:127], v[108:111], v[192:195], v[124:127]\n" \
  "v_mfma_f32_16x16x32_bf16 v[128:131], v[108:111], v[216:219], v[128:131]\n" \
  "v_mfma_f32_16x16x32_bf16 v[132:135], v[108:111], v[240:243], v[132:135]\n" \
  "s_waitcnt lgkmcnt(4)\n" \
  "v_mfma_f32_16x16x32_bf16 v[124:127], v[112:115], v[196:199], v[124:127]\n" \
  "v_mfma_f32_16x16x32_bf16 v[128:131], v[112:115], v[220:223], v[128:131]\n" \
  "v_mfma_f32_16x16x32_bf16 v[132:135], v[112:115], v[244:247], v[132:135]\n" \
  "s_waitcnt lgkmcnt(2)\n" \
  "v_mfma_f32_16x16x32_bf16 v[124:127], v[116:119], v[200:203], v[124:127]\n" \
  "v_mfma_f32_16x16x32_bf16 v[128:131], v[116:119], v[224:227], v[128:131]\n" \
  "v_mfma_f32_16x16x32_bf16 v[132:135], v[116:119], v[248:251], v[132:135]\n" \
  "s_waitcnt lgkmcnt(0)\n" \
  "v_mfma_f32_16x16x32_bf16 v[124:127], v[120:123], v[204:207], v[124:127]\n" \
  "v_mfma_f32_16x16x32_bf16 v[128:131], v[120:123], v[228:231], v[128:131]\n" \
  "v_mfma_f32_16x16x32_bf16 v[132:135], v[120:123], v[252:255], v[132:135]\n" \
  "s_nop 3\n" \
  "v_cvt_pk_bf16_f32 v42, v124, v125\n" \
  "v_cvt_pk_bf16_f32 v43, v126, v127\n" \
  "v_cvt_pk_bf16_f32 v46, v128, v129\n" \
  "v_cvt_pk_bf16_f32 v47, v130, v131\n" \
  "s_mov_b64 exec, s[34:35]\n" \
  "v_cvt_pk_bf16_f32 v50, v132, v133\n" \
  "v_cvt_pk_bf16_f32 v51, v134, v135\n" \
  "s_mov_b64 exec, -1\n" \
  "LOOP2_%=:\n" \
  "s_mov_b32 m0, 0\n" \
  "LWP2_%=:\n" \
  "s_memrealtime s[74:75]\n" \
  "s_memtime s[80:81]\n" \
  "s_waitcnt lgkmcnt(0)\n" \
  "s_sub_i32 s77, s54, s74\n" \
  "s_mul_i32 s77, s77, s51\n" \
  "s_ashr_i32 s77, s77, 8\n" \
  "s_sub_i32 s76, s49, s80\n" \
  "s_min_i32 s76, s76, s77\n" \
  "s_sub_i32 s77, s77, 92\n" \
  "s_max_i32 s76, s76, s77\n" \
  "s_add_u32 s49, s76, s80\n" \
  "s_cmp_gt_i32 s76, 500\n" \
  "s_cbranch_scc1 LWP2_%=\n" \
  "s_barrier\n" \
  "LWA2_%=:\n" \
  "s_memrealtime s[74:75]\n" \
  "s_memtime s[80:81]\n" \
  "s_waitcnt lgkmcnt(0)\n" \
  "s_sub_i32 s77, s54, s74\n" \
  "s_mul_i32 s77, s77, s51\n" \
  "s_ashr_i32 s77, s77, 8\n" \
  "s_sub_i32 s76, s49, s80\n" \
  "s_min_i32 s76, s76, s77\n" \
  "s_sub_i32 s77, s77, 92\n" \
  "s_max_i32 s76, s76, s77\n" \
  "s_add_u32 s49, s76, s80\n" \
  "s_cmp_gt_i32 s76, 168\n" \
  "s_cbranch_scc1 LWA2_%=\n" \
  "s_cmp_lt_i32 s76, s48\n" \
  "s_cbranch_scc1 LSA2_%=\n" \
  "s_max_i32 s76, s76, 48\n" \
  "s_lshr_b32 s76, s76, 3\n" \
  "s_lshl_b32 s76, s76, 2\n" \
  "s_sub_u32 s78, s84, s76\n" \
  "s_subb_u32 s79, s85, 0\n" \
  "s_setpc_b64 s[78:79]\n" \
  "s_nop 1\n" \
  "s_nop 1\n" \
  "s_nop 1\n" \
  "s_nop 1\n" \
  "s_nop 1\n" \
  "s_nop 1\n" \
  "s_nop 1\n" \
  "s_nop 1\n" \
  "s_nop 1\n" \
  "s_nop 1\n" \
  "s_nop 1\n" \
  "s_nop 1\n" \
  "s_nop 1\n" \
  "s_nop 1\n" \
  "s_nop 1\n" \
  "s_nop 1\n" \
  "LEA2_%=:\n" \
  "buffer_load_dwordx4 v1, s[36:39], s72 offen sc1 lds\n" \
  "buffer_load_dwordx4 v1, s[36:39], s72 offen offset:1024 sc1 lds\n" \
  "buffer_load_dwordx4 v1, s[36:39], s72 offen offset:2048 sc1 lds\n" \
  "buffer_load_dwordx4 v1, s[36:39], s72 offen offset:3072 sc1 lds\n" \
  "s_mov_b32 m0, 4096\n" \
  "s_nop 0\n" \
  "buffer_load_dwordx4 v2, s[36:39], s72 offen sc1 lds\n" \
  "buffer_load_dwordx4 v2, s[36:39], s72 offen offset:1024 sc1 lds\n" \
  "buffer_load_dwordx4 v2, s[36:39], s72 offen offset:2048 sc1 lds\n" \
  "buffer_load_dwordx4 v2, s[36:39], s72 offen offset:3072 sc1 lds\n" \
  "s_mov_b32 m0, 8192\n" \
  "s_nop 0\n" \
  "buffer_load_dwordx4 v3, s[36:39], s72 offen sc1 lds\n" \
  "buffer_load_dwordx4 v3, s[36:39], s72 offen offset:1024 sc1 lds\n" \
  "ds_read_b128 v[64:67], v8\n" \
  "ds_read_b64 v[68:69], v9\n" \
  "v_mov_b32 v140, 0\n" \
  "v_mov_b32 v141, 0\n" \
  "v_mov_b32 v142, 0\n" \
  "v_mov_b32 v143, 0\n" \
  "v_mov_b32 v144, 0\n" \
  "v_mov_b32 v145, 0\n" \
  "v_mov_b32 v146, 0\n" \
  "v_mov_b32 v147, 0\n" \
  "v_xor_b32 v8, 0x4000, v8\n" \
  "v_xor_b32 v9, 0x4000, v9\n" \
  "v_xor_b32 v7, 0x4000, v7\n" \
  "s_add_u32 s68, s68, 1\n" \
  "s_mov_b32 s70, s42\n" \
  "s_add_u32 s63, s63, 2\n" \
  "s_cmp_eq_u32 s63, 60\n" \
  "s_cselect_b32 s63, 0, s63\n" \
  "s_lshr_b64 s[82:83], s[52:53], s63\n" \
  "s_lshl_b32 s42, s63, 21\n" \
  "s_lshl_b32 s76, s82, 12\n" \
  "s_and_b32 s76, s76, 0x2000\n" \
  "s_or_b32 s71, s42, s76\n" \
  "s_or_b32 s71, s71, 0x200000\n" \
  "s_lshl_b32 s76, s82, 13\n" \
  "s_and_b32 s76, s76, 0x2000\n" \
  "s_or_b32 s42, s42, s76\n" \
  "s_memrealtime s[74:75]\n" \
  "s_memtime s[80:81]\n" \
  "s_waitcnt lgkmcnt(0)\n" \
  "s_sub_i32 s77, s54, s74\n" \
  "s_mul_i32 s77, s77, s51\n" \
  "s_ashr_i32 s77, s77, 8\n" \
  "s_sub_i32 s76, s49, s80\n" \
  "s_min_i32 s76, s76, s77\n" \
  "s_sub_i32 s77, s77, 92\n" \
  "s_max_i32 s76, s76, s77\n" \
  "s_add_u32 s49, s76, s80\n" \
  "s_waitcnt lgkmcnt(0)\n" \
  "LM2_%=:\n" \
  "s_waitcnt vmcnt(8)\n" \
  "s_barrier\n" \
  "ds_read_b128 v[100:103], v6 offset:0\n" \
  "ds_read_b128 v[104:107], v6 offset:1024\n" \
  "s_waitcnt vmcnt(6)\n" \
  "s_barrier\n" \
  "ds_read_b128 v[108:111], v6 offset:2048\n" \
  "ds_read_b128 v[112:115], v6 offset:3072\n" \
  "s_waitcnt lgkmcnt(3)\n" \
  "v_smfmac_f32_16x16x64_bf16 v[140:143], v[100:103], a[0:7], v0\n" \
  "v_smfmac_f32_16x16x64_bf16 v[144:147], v[100:103], a[80:87], v0\n" \
  "s_waitcnt lgkmcnt(2)\n" \
  "v_smfmac_f32_16x16x64_bf16 v[140:143], v[104:107], a[8:15], v0\n" \
  "v_smfmac_f32_16x16x64_bf16 v[144:147], v[104:107], a[88:95], v0\n" \
  "s_waitcnt vmcnt(4)\n" \
  "s_barrier\n" \
  "ds_read_b128 v[116:119], v6 offset:4096\n" \
  "ds_read_b128 v[120:123], v6 offset:5120\n" \
  "s_waitcnt lgkmcnt(3)\n" \
  "v_smfmac_f32_16x16x64_bf16 v[140:143], v[108:111], a[16:23], v0\n" \
  "v_smfmac_f32_16x16x64_bf16 v[144:147], v[108:111], a[96:103], v0\n" \
  "s_waitcnt lgkmcnt(2)\n" \
  "v_smfmac_f32_16x16x64_bf16 v[140:143], v[112:115], a[24:31], v0\n" \
  "v_smfmac_f32_16x16x64_bf16 v[144:147], v[112:115], a[104:111], v0\n" \
  "s_waitcnt vmcnt(2)\n" \
  "s_barrier\n" \
  "ds_read_b128 v[124:127], v6 offset:6144\n" \
  "ds_read_b128 v[128:131], v6 offset:7168\n" \
  "s_waitcnt lgkmcnt(3)\n" \
  "v_smfmac_f32_16x16x64_bf16 v[140:143], v[116:119], a[32:39], v0\n" \
  "v_smfmac_f32_16x16x64_bf16 v[144:147], v[116:119], a[112:119], v0\n" \
  "s_waitcnt lgkmcnt(2)\n" \
  "v_smfmac_f32_16x16x64_bf16 v[140:143], v[120:123], a[40:47], v0\n" \
  "v_smfmac_f32_16x16x64_bf16 v[144:147], v[120:123], a[120:127], v0\n" \
  "s_waitcnt vmcnt(0)\n" \
  "s_barrier\n" \
  "ds_read_b128 v[132:135], v6 offset:8192\n" \
  "ds_read_b128 v[136:139], v6 offset:9216\n" \
  "s_waitcnt lgkmcnt(3)\n" \
  "v_smfmac_f32_16x16x64_bf16 v[140:143], v[124:127], a[48:55], v0\n" \
  "v_smfmac_f32_16x16x64_bf16 v[144:147], v[124:127], a[128:135], v0\n" \
  "s_waitcnt lgkmcnt(2)\n" \
  "v_smfmac_f32_16x16x64_bf16 v[140:143], v[128:131], a[56:63], v0\n" \
  "v_smfmac_f32_16x16x64_bf16 v[144:147], v[128:131], a[136:143], v0\n" \
  "s_waitcnt lgkmcnt(1)\n" \
  "v_smfmac_f32_16x16x64_bf16 v[140:143], v[132:135], a[64:71], v0\n" \
  "v_smfmac_f32_16x16x64_bf16 v[144:147], v[132:135], a[144:151], v0\n" \
  "s_waitcnt lgkmcnt(0)\n" \
  "v_smfmac_f32_16x16x64_bf16 v[140:143], v[136:139], a[72:79], v0\n" \
  "v_smfmac_f32_16x16x64_bf16 v[144:147], v[136:139], a[152:159], v0\n" \
  "s_nop 7\n" \
  "v_permlane32_swap_b32 v140, v144\n" \
  "v_permlane32_swap_b32 v141, v145\n" \
  "v_permlane32_swap_b32 v142, v146\n" \
  "v_permlane32_swap_b32 v143, v147\n" \
  "v_pk_add_f32 v[152:153], v[140:141], v[144:145]\n" \
  "v_pk_add_f32 v[154:155], v[142:143], v[146:147]\n" \
  "v_cvt_pk_bf16_f32 v156, v152, v153\n" \
  "v_cvt_pk_bf16_f32 v157, v154, v155\n" \
  "ds_write_b64 v10, v[156:157]\n" \
  "v_cmp_u_f32 vcc, v152, v153\n" \
  "v_cmp_u_f32 s[82:83], v154, v155\n" \
  "s_or_b64 vcc, vcc, s[82:83]\n" \
  "s_cbranch_vccnz LMF2_%=\n" \
  "LMOK2_%=:\n" \
  "s_waitcnt lgkmcnt(0)\n" \
  "s_barrier\n" \
  "ds_read_b64_tr_b16 v[72:73], v11\n" \
  "ds_read_b64_tr_b16 v[74:75], v11 offset:128\n" \
  "ds_read_b64_tr_b16 v[76:77], v12\n" \
  "s_waitcnt lgkmcnt(1)\n" \
  "v_mfma_f32_16x16x32_bf16 v[152:155], v[64:67], v[72:75], 0\n" \
  "s_waitcnt lgkmcnt(0)\n" \
  "v_mfma_f32_16x16x32_bf16 v[152:155], v[68:71], v[76:79], v[152:155]\n" \
  "s_add_u32 s54, s54, s55\n" \
  "s_add_u32 s49, s49, s44\n" \
  "s_nop 5\n" \
  "buffer_store_dwordx4 v[152:155], v13, s[36:39], s69 offen sc1\n" \
  "s_cmp_eq_u32 s68, 64\n" \
  "s_cbranch_scc1 LEND2_%=\n" \
  "LWQ2_%=:\n" \
  "s_memrealtime s[74:75]\n" \
  "s_memtime s[80:81]\n" \
  "s_waitcnt lgkmcnt(0)\n" \
  "s_sub_i32 s77, s54, s74\n" \
  "s_mul_i32 s77, s77, s51\n" \
  "s_ashr_i32 s77, s77, 8\n" \
  "s_sub_i32 s76, s49, s80\n" \
  "s_min_i32 s76, s76, s77\n" \
  "s_sub_i32 s77, s77, 92\n" \
  "s_max_i32 s76, s76, s77\n" \
  "s_add_u32 s49, s76, s80\n" \
  "s_cmp_gt_i32 s76, 168\n" \
  "s_cbranch_scc1 LWQ2_%=\n" \
  "s_cmp_lt_i32 s76, s48\n" \
  "s_cbranch_scc1 LSQ2_%=\n" \
  "s_max_i32 s76, s76, 48\n" \
  "s_lshr_b32 s76, s76, 3\n" \
  "s_lshl_b32 s76, s76, 2\n" \
  "s_sub_u32 s78, s86, s76\n" \
  "s_subb_u32 s79, s87, 0\n" \
  "s_setpc_b64 s[78:79]\n" \
  "s_nop 1\n" \
  "s_nop 1\n" \
  "s_nop 1\n" \
  "s_nop 1\n" \
  "s_nop 1\n" \
  "s_nop 1\n" \
  "s_nop 1\n" \
  "s_nop 1\n" \
  "s_nop 1\n" \
  "s_nop 1\n" \
  "s_nop 1\n" \
  "s_nop 1\n" \
  "s_nop 1\n" \
  "s_nop 1\n" \
  "s_nop 1\n" \
  "s_nop 1\n" \
  "LEQ2_%=:\n" \
  "s_barrier\n" \
  "s_nop 5\n" \
  "buffer_load_dwordx4 v[156:159], v15, s[36:39], s69 offen sc1\n" \
  "buffer_load_dword v160, v16, s[36:39], s69 offen sc1\n" \
  "s_mov_b32 s72, s70\n" \
  "v_mov_b32 v136, 0\n" \
  "v_mov_b32 v137, 0\n" \
  "v_mov_b32 v138, 0\n" \
  "v_mov_b32 v139, 0\n" \
  "v_mov_b32 v140, 0\n" \
  "v_mov_b32 v141, 0\n" \
  "v_mov_b32 v142, 0\n" \
  "v_mov_b32 v143, 0\n" \
  "v_mov_b32 v144, 0\n" \
  "v_mov_b32 v145, 0\n" \
  "v_mov_b32 v146, 0\n" \
  "v_mov_b32 v147, 0\n" \
  "s_memrealtime s[74:75]\n" \
  "s_memtime s[80:81]\n" \
  "s_waitcnt lgkmcnt(0)\n" \
  "s_sub_i32 s77, s54, s74\n" \
  "s_mul_i32 s77, s77, s51\n" \
  "s_ashr_i32 s77, s77, 8\n" \
  "s_sub_i32 s76, s49, s80\n" \
  "s_min_i32 s76, s76, s77\n" \
  "s_sub_i32 s77, s77, 92\n" \
  "s_max_i32 s76, s76, s77\n" \
  "s_add_u32 s49, s76, s80\n" \
  "s_add_u32 s76, s49, s45\n" \
  "v_mov_b32 v180, s76\n" \
  "ds_write_b32 v62, v180\n" \
  "v_readfirstlane_b32 s76, v63\n" \
  "s_max_u32 s66, s66, s76\n" \
  "s_and_b32 s77, s76, 0xff\n" \
  "s_lshr_b32 s76, s76, 8\n" \
  "s_cselect_b32 s76, s76, s61\n" \
  "s_sub_i32 s77, s68, s77\n" \
  "s_sub_i32 s76, s76, s61\n" \
  "s_cmp_ge_i32 s77, 0\n" \
  "s_cselect_b32 s76, s76, 0\n" \
  "s_add_u32 s61, s61, s76\n" \
  "s_mul_i32 s77, s77, s76\n" \
  "s_mul_i32 s77, s77, 3\n" \
  "s_add_u32 s41, s56, s57\n" \
  "s_add_u32 s41, s41, s77\n" \
  "s_mul_i32 s43, s41, s51\n" \
  "s_lshr_b32 s43, s43, 8\n" \
  "s_lshr_b32 s77, s50, 1\n" \
  "s_sub_u32 s82, s77, s50\n" \
  "s_mov_b32 s50, s77\n" \
  "s_add_u32 s76, s76, s82\n" \
  "s_add_u32 s55, s55, s76\n" \
  "s_mul_i32 s44, s55, s51\n" \
  "s_lshr_b32 s44, s44, 8\n" \
  "s_add_u32 s56, s56, s76\n" \
  "s_mul_i32 s45, s56, s51\n" \
  "s_lshr_b32 s45, s45, 8\n" \
  "s_add_u32 s57, s57, s76\n" \
  "s_mul_i32 s46, s57, s51\n" \
  "s_lshr_b32 s46, s46, 8\n" \
  "v_mov_b32 v183, v182\n" \
  "s_nop 0\n" \
  "v_mov_b32 v183, v182\n" \
  "s_nop 0\n" \
  "v_mov_b32 v183, v182\n" \
  "s_nop 0\n" \
  "v_mov_b32 v183, v182\n" \
  "s_nop 0\n" \
  "LQ2_%=:\n" \
  "s_barrier\n" \
  "ds_read_b64_tr_b16 v[100:101], v20\n" \
  "ds_read_b64_tr_b16 v[102:103], v21\n" \
  "ds_read_b64_tr_b16 v[104:105], v22\n" \
  "ds_read_b64_tr_b16 v[106:107], v23\n" \
  "ds_read_b64_tr_b16 v[108:109], v24\n" \
  "ds_read_b64_tr_b16 v[110:111], v25\n" \
  "v_mov_b32 v183, v182\n" \
  "s_nop 0\n" \
  "v_mov_b32 v183, v182\n" \
  "s_nop 0\n" \
  "v_mov_b32 v183, v182\n" \
  "s_nop 0\n" \
  "v_mov_b32 v183, v182\n" \
  "s_nop 0\n" \
  "s_waitcnt lgkmcnt(4)\n" \
  "v_smfmac_f32_16x16x64_bf16 v[144:147], v[100:103], v[232:239], v0\n" \
  "v_mov_b32 v183, v182\n" \
  "v_smfmac_f32_16x16x64_bf16 v[136:139], v[100:103], v[184:191], v0\n" \
  "v_mov_b32 v183, v182\n" \
  "v_smfmac_f32_16x16x64_bf16 v[140:143], v[100:103], v[208:215], v0\n" \
  "v_mov_b32 v183, v182\n" \
  "s_waitcnt lgkmcnt(0)\n" \
  "v_smfmac_f32_16x16x64_bf16 v[144:147], v[104:107], v[240:247], v0\n" \
  "v_mov_b32 v183, v182\n" \
  "v_smfmac_f32_16x16x64_bf16 v[136:139], v[104:107], v[192:199], v0\n" \
  "v_mov_b32 v183, v182\n" \
  "s_getreg_b32 s76, hwreg(HW_REG_IB_STS, 0, 4)\n" \
  "v_mov_b32 v183, v182\n" \
  "s_cmp_le_u32 s76, 0\n" \
  "s_cbranch_scc1 LHXQ2_%=\n" \
  "s_getreg_b32 s76, hwreg(HW_REG_IB_STS, 0, 4)\n" \
  "v_mov_b32 v183, v182\n" \
  "s_cmp_le_u32 s76, 0\n" \
  "s_cbranch_scc1 LHXQ2_%=\n" \
  "s_getreg_b32 s76, hwreg(HW_REG_IB_STS, 0, 4)\n" \
  "v_mov_b32 v183, v182\n" \
  "s_cmp_le_u32 s76, 0\n" \
  "s_cbranch_scc1 LHXQ2_%=\n" \
  "s_getreg_b32 s76, hwreg(HW_REG_IB_STS, 0, 4)\n" \
  "v_mov_b32 v183, v182\n" \
  "s_cmp_le_u32 s76, 0\n" \
  "s_cbranch_scc1 LHXQ2_%=\n" \
  "s_getreg_b32 s76, hwreg(HW_REG_IB_STS, 0, 4)\n" \
  "v_mov_b32 v183, v182\n" \
  "s_cmp_le_u32 s76, 0\n" \
  "s_cbranch_scc1 LHXQ2_%=\n" \
  "s_getreg_b32 s76, hwreg(HW_REG_IB_STS, 0, 4)\n" \
  "v_mov_b32 v183, v182\n" \
  "s_cmp_le_u32 s76, 0\n" \
  "s_cbranch_scc1 LHXQ2_%=\n" \
  "s_getreg_b32 s76, hwreg(HW_REG_IB_STS, 0, 4)\n" \
  "v_mov_b32 v183, v182\n" \
  "s_cmp_le_u32 s76, 0\n" \
  "s_cbranch_scc1 LHXQ2_%=\n" \
  "s_getreg_b32 s76, hwreg(HW_REG_IB_STS, 0, 4)\n" \
  "v_mov_b32 v183, v182\n" \
  "s_cmp_le_u32 s76, 0\n" \
  "s_cbranch_scc1 LHXQ2_%=\n" \
  "s_getreg_b32 s76, hwreg(HW_REG_IB_STS, 0, 4)\n" \
  "v_mov_b32 v183, v182\n" \
  "s_cmp_le_u32 s76, 0\n" \
  "s_cbranch_scc1 LHXQ2_%=\n" \
  "s_getreg_b32 s76, hwreg(HW_REG_IB_STS, 0, 4)\n" \
  "v_mov_b32 v183, v182\n" \
  "s_cmp_le_u32 s76, 0\n" \
  "s_cbranch_scc1 LHXQ2_%=\n" \
  "LHXQ2_%=:\n" \
  "s_waitcnt vmcnt(0)\n" \
  "v_mov_b32 v115, v160\n" \
  "v_mov_b32 v114, v160\n" \
  "v_permlane32_swap_b32 v156, v158\n" \
  "v_permlane32_swap_b32 v157, v159\n" \
  "v_pk_add_f32 v[112:113], v[156:157], v[158:159]\n" \
  "v_permlane32_swap_b32 v114, v115\n" \
  "v_add_f32 v117, v114, v115\n" \
  "v_permlane16_swap_b32 v112, v113\n" \
  "v_add_f32 v114, v114, v115\n" \
  "v_add_f32 v116, v112, v113\n" \
  "v_smfmac_f32_16x16x64_bf16 v[140:143], v[104:107], v[216:223], v0\n" \
  "v_permlane16_swap_b32 v117, v114\n" \
  "v_mul_f32 v116, v116, v60\n" \
  "v_add_f32 v117, v117, v114\n" \
  "v_exp_f32 v112, v116\n" \
  "v_mul_f32 v117, v117, v60\n" \
  "v_max_f32 v118, |v116|, |v117|\n" \
  "v_exp_f32 v113, v117\n" \
  "v_cmp_lt_f32 vcc, 0x42800000, v118\n" \
  "v_cvt_pk_bf16_f32 v112, v112, v113\n" \
  "v_smfmac_f32_16x16x64_bf16 v[144:147], v[108:111], v[248:255], v0\n" \
  "s_cbranch_vccnz LMX2_%=\n" \
  "LMY2_%=:\n" \
  "ds_write_b16 v18, v112\n" \
  "ds_write_b16_d16_hi v19, v112\n" \
  "v_smfmac_f32_16x16x64_bf16 v[136:139], v[108:111], v[200:207], v0\n" \
  "v_smfmac_f32_16x16x64_bf16 v[140:143], v[108:111], v[224:231], v0\n" \
  "s_mov_b64 exec, s[34:35]\n" \
  "v_cvt_pk_bf16_f32 v92, v144, v145\n" \
  "v_cvt_pk_bf16_f32 v93, v146, v147\n" \
  "s_mov_b64 exec, -1\n" \
  "v_cvt_pk_bf16_f32 v84, v136, v137\n" \
  "v_cvt_pk_bf16_f32 v85, v138, v139\n" \
  "s_nop 1\n" \
  "v_cvt_pk_bf16_f32 v88, v140, v141\n" \
  "v_cvt_pk_bf16_f32 v89, v142, v143\n" \
  "s_waitcnt lgkmcnt(0)\n" \
  "s_barrier\n" \
  "ds_read_b128 v[120:123], v28\n" \
  "ds_read_b64 v[80:81], v36\n" \
  "v_mov_b32 v183, v182\n" \
  "s_nop 0\n" \
  "v_mov_b32 v183, v182\n" \
  "s_nop 0\n" \
  "v_mov_b32 v183, v182\n" \
  "s_nop 0\n" \
  "v_mov_b32 v183, v182\n" \
  "s_nop 0\n" \
  "s_waitcnt lgkmcnt(1)\n" \
  "v_mfma_f32_16x16x32_bf16 v[172:175], v[48:51], v[120:123], 0\n" \
  "v_mfma_f32_16x16x32_bf16 v[164:167], v[40:43], v[120:123], 0\n" \
  "s_waitcnt lgkmcnt(0)\n" \
  "v_mfma_f32_16x16x32_bf16 v[172:175], v[92:95], v[80:83], v[172:175]\n" \
  "v_mfma_f32_16x16x32_bf16 v[168:171], v[44:47], v[120:123], 0\n" \
  "v_mfma_f32_16x16x32_bf16 v[164:167], v[84:87], v[80:83], v[164:167]\n" \
  "v_rcp_f32 v177, v172\n" \
  "v_mfma_f32_16x16x32_bf16 v[168:171], v[88:91], v[80:83], v[168:171]\n" \
  "v_mov_b32 v176, v177\n" \
  "v_cmp_u_f32 vcc, v172, v172\n" \
  "s_nop 0\n" \
  "v_permlane32_swap_b32 v177, v176\n" \
  "v_pk_mul_f32 v[164:165], v[164:165], v[176:177] op_sel_hi:[1,0]\n" \
  "v_pk_mul_f32 v[166:167], v[166:167], v[176:177] op_sel_hi:[1,0]\n" \
  "v_pk_mul_f32 v[172:173], v[172:173], v[176:177] op_sel_hi:[1,0]\n" \
  "v_pk_mul_f32 v[174:175], v[174:175], v[176:177] op_sel_hi:[1,0]\n" \
  "v_pk_mul_f32 v[168:169], v[168:169], v[176:177] op_sel_hi:[1,0]\n" \
  "v_pk_mul_f32 v[170:171], v[170:171], v[176:177] op_sel_hi:[1,0]\n" \
  "v_cvt_pk_bf16_f32 v164, v164, v165\n" \
  "v_cvt_pk_bf16_f32 v165, v166, v167\n" \
  "v_cvt_pk_bf16_f32 v166, v168, v169\n" \
  "v_cvt_pk_bf16_f32 v167, v170, v171\n" \
  "s_cbranch_vccnz LQF2_%=\n" \
  "LQOK2_%=:\n" \
  "buffer_store_dwordx4 v[164:167], v26, s[36:39], s69 offen sc1\n" \
  "v_cvt_pk_bf16_f32 v172, v172, v173\n" \
  "v_cvt_pk_bf16_f32 v173, v174, v175\n" \
  "buffer_store_dwordx2 v[172:173], v27, s[36:39], s69 offen sc1\n" \
  "s_add_u32 s54, s54, s41\n" \
  "s_add_u32 s49, s49, s43\n" \
  "ds_read_b128 v[100:103], v7 offset:0\n" \
  "ds_read_b128 v[104:107], v7 offset:4096\n" \
  "ds_read_b128 v[108:111], v7 offset:8192\n" \
  "ds_read_b128 v[112:115], v7 offset:12288\n" \
  "ds_read_b64 v[116:117], v7 offset:2048\n" \
  "ds_read_b64 v[118:119], v7 offset:6144\n" \
  "ds_read_b64 v[120:121], v7 offset:10240\n" \
  "ds_read_b64 v[122:123], v7 offset:14336\n" \
  "s_waitcnt lgkmcnt(7)\n" \
  "v_mfma_f32_16x16x32_bf16 v[124:127], v[100:103], v[184:187], 0\n" \
  "v_mfma_f32_16x16x32_bf16 v[128:131], v[100:103], v[208:211], 0\n" \
  "v_mfma_f32_16x16x32_bf16 v[132:135], v[100:103], v[232:235], 0\n" \
  "s_waitcnt lgkmcnt(6)\n" \
  "v_mfma_f32_16x16x32_bf16 v[124:127], v[104:107], v[188:191], v[124:127]\n" \
  "v_mfma_f32_16x16x32_bf16 v[128:131], v[104:107], v[212:215], v[128:131]\n" \
  "v_mfma_f32_16x16x32_bf16 v[132:135], v[104:107], v[236:239], v[132:135]\n" \
  "s_waitcnt lgkmcnt(5)\n" \
  "v_mfma_f32_16x16x32_bf16 v[124:127], v[108:111], v[192:195], v[124:127]\n" \
  "v_mfma_f32_16x16x32_bf16 v[128:131], v[108:111], v[216:219], v[128:131]\n" \
  "v_mfma_f32_16x16x32_bf16 v[132:135], v[108:111], v[240:243], v[132:135]\n" \
  "s_waitcnt lgkmcnt(4)\n" \
  "v_mfma_f32_16x16x32_bf16 v[124:127], v[112:115], v[196:199], v[124:127]\n" \
  "v_mfma_f32_16x16x32_bf16 v[128:131], v[112:115], v[220:223], v[128:131]\n" \
  "v_mfma_f32_16x16x32_bf16 v[132:135], v[112:115], v[244:247], v[132:135]\n" \
  "s_waitcnt lgkmcnt(2)\n" \
  "v_mfma_f32_16x16x32_bf16 v[124:127], v[116:119], v[200:203], v[124:127]\n" \
  "v_mfma_f32_16x16x32_bf16 v[128:131], v[116:119], v[224:227], v[128:131]\n" \
  "v_mfma_f32_16x16x32_bf16 v[132:135], v[116:119], v[248:251], v[132:135]\n" \
  "s_waitcnt lgkmcnt(0)\n" \
  "v_mfma_f32_16x16x32_bf16 v[124:127], v[120:123], v[204:207], v[124:127]\n" \
  "v_mfma_f32_16x16x32_bf16 v[128:131], v[120:123], v[228:231], v[128:131]\n" \
  "v_mfma_f32_16x16x32_bf16 v[132:135], v[120:123], v[252:255], v[132:135]\n" \
  "s_nop 3\n" \
  "v_cvt_pk_bf16_f32 v40, v124, v125\n" \
  "v_cvt_pk_bf16_f32 v41, v126, v127\n" \
  "v_cvt_pk_bf16_f32 v44, v128, v129\n" \
  "v_cvt_pk_bf16_f32 v45, v130, v131\n" \
  "s_mov_b64 exec, s[34:35]\n" \
  "v_cvt_pk_bf16_f32 v48, v132, v133\n" \
  "v_cvt_pk_bf16_f32 v49, v134, v135\n" \
  "s_mov_b64 exec, -1\n" \
  "ds_read_b128 v[100:103], v7 offset:1024\n" \
  "ds_read_b128 v[104:107], v7 offset:5120\n" \
  "ds_read_b128 v[108:111], v7 offset:9216\n" \
  "ds_read_b128 v[112:115], v7 offset:13312\n" \
  "ds_read_b64 v[116:117], v7 offset:2056\n" \
  "ds_read_b64 v[118:119], v7 offset:6152\n" \
  "ds_read_b64 v[120:121], v7 offset:10248\n" \
  "ds_read_b64 v[122:123], v7 offset:14344\n" \
  "s_waitcnt lgkmcnt(7)\n" \
  "v_mfma_f32_16x16x32_bf16 v[124:127], v[100:103], v[184:187], 0\n" \
  "v_mfma_f32_16x16x32_bf16 v[128:131], v[100:103], v[208:211], 0\n" \
  "v_mfma_f32_16x16x32_bf16 v[132:135], v[100:103], v[232:235], 0\n" \
  "s_waitcnt lgkmcnt(6)\n" \
  "v_mfma_f32_16x16x32_bf16 v[124:127], v[104:107], v[188:191], v[124:127]\n" \
  "v_mfma_f32_16x16x32_bf16 v[128:131], v[104:107], v[212:215], v[128:131]\n" \
  "v_mfma_f32_16x16x32_bf16 v[132:135], v[104:107], v[236:239], v[132:135]\n" \
  "s_waitcnt lgkmcnt(5)\n" \
  "v_mfma_f32_16x16x32_bf16 v[124:127], v[108:111], v[192:195], v[124:127]\n" \
  "v_mfma_f32_16x16x32_bf16 v[128:131], v[108:111], v[216:219], v[128:131]\n" \
  "v_mfma_f32_16x16x32_bf16 v[132:135], v[108:111], v[240:243], v[132:135]\n" \
  "s_waitcnt lgkmcnt(4)\n" \
  "v_mfma_f32_16x16x32_bf16 v[124:127], v[112:115], v[196:199], v[124:127]\n" \
  "v_mfma_f32_16x16x32_bf16 v[128:131], v[112:115], v[220:223], v[128:131]\n" \
  "v_mfma_f32_16x16x32_bf16 v[132:135], v[112:115], v[244:247], v[132:135]\n" \
  "s_waitcnt lgkmcnt(2)\n" \
  "v_mfma_f32_16x16x32_bf16 v[124:127], v[116:119], v[200:203], v[124:127]\n" \
  "v_mfma_f32_16x16x32_bf16 v[128:131], v[116:119], v[224:227], v[128:131]\n" \
  "v_mfma_f32_16x16x32_bf16 v[132:135], v[116:119], v[248:251], v[132:135]\n" \
  "s_waitcnt lgkmcnt(0)\n" \
  "v_mfma_f32_16x16x32_bf16 v[124:127], v[120:123], v[204:207], v[124:127]\n" \
  "v_mfma_f32_16x16x32_bf16 v[128:131], v[120:123], v[228:231], v[128:131]\n" \
  "v_mfma_f32_16x16x32_bf16 v[132:135], v[120:123], v[252:255], v[132:135]\n" \
  "s_nop 3\n" \
  "v_cvt_pk_bf16_f32 v42, v124, v125\n" \
  "v_cvt_pk_bf16_f32 v43, v126, v127\n" \
  "v_cvt_pk_bf16_f32 v46, v128, v129\n" \
  "v_cvt_pk_bf16_f32 v47, v130, v131\n" \
  "s_mov_b64 exec, s[34:35]\n" \
  "v_cvt_pk_bf16_f32 v50, v132, v133\n" \
  "v_cvt_pk_bf16_f32 v51, v134, v135\n" \
  "s_mov_b64 exec, -1\n" \
  "s_sub_u32 s76, s80, s60\n" \
  "s_sub_u32 s77, s74, s59\n" \
  "v_cvt_f32_u32 v180, s76\n" \
  "v_cvt_f32_u32 v181, s77\n" \
  "v_rcp_f32 v181, v181\n" \
  "v_mul_f32 v180, 0x43820000, v180\n" \
  "v_mul_f32 v180, v180, v181\n" \
  "v_cvt_u32_f32 v180, v180\n" \
  "s_nop 0\n" \
  "v_readfirstlane_b32 s76, v180\n" \
  "s_min_u32 s76, s76, 6400\n" \
  "s_cmp_gt_u32 s77, 900\n" \
  "s_cselect_b32 s51, s76, s51\n" \
  "s_cselect_b32 s59, s74, s59\n" \
  "s_cselect_b32 s60, s80, s60\n" \
  "s_cmp_lt_u32 s68, 3\n" \
  "s_cselect_b32 s59, s74, s59\n" \
  "s_cselect_b32 s60, s80, s60\n" \
  "v_cvt_f32_u32 v180, s51\n" \
  "v_rcp_f32 v180, v180\n" \
  "v_mov_b32 v181, 0xc0b00000\n" \
  "v_fmac_f32 v181, 0x48cc6500, v180\n" \
  "v_cvt_u32_f32 v181, v181\n" \
  "s_cmp_lt_u32 s68, s62\n" \
  "s_cselect_b32 s89, s73, s89\n" \
  "s_cselect_b32 s82, 0, -1\n" \
  "s_cmp_lg_u32 s89, s73\n" \
  "s_addc_u32 s77, s61, 0\n" \
  "s_mov_b32 s89, s73\n" \
  "v_readfirstlane_b32 s76, v181\n" \
  "s_max_u32 s76, s76, s77\n" \
  "s_and_b32 s76, s76, s82\n" \
  "s_add_u32 s77, s68, 2\n" \
  "s_lshl_b32 s82, s76, 8\n" \
  "s_or_b32 s82, s82, s77\n" \
  "s_add_u32 s77, s68, 3\n" \
  "s_cmp_gt_u32 s76, s61\n" \
  "s_cselect_b32 s66, s82, s66\n" \
  "s_cselect_b32 s62, s77, s62\n" \
  "v_mov_b32 v180, s66\n" \
  "s_lshl_b32 s76, s68, 7\n" \
  "buffer_store_dword v180, v30, s[36:39], s76 offen offset:128 sc1\n" \
  "buffer_load_dword v63, v29, s[36:39], s76 offen sc1\n" \
  "s_mov_b32 s69, s70\n" \
  "s_branch LOOP2_%=\n" \
  "LSA2_%=:\n" \
  "s_add_u32 s54, s54, s88\n" \
  "s_add_u32 s49, s49, s47\n" \
  "s_add_u32 s73, s73, 1\n" \
  "s_branch LWA2_%=\n" \
  "LSQ2_%=:\n" \
  "s_add_u32 s54, s54, s88\n" \
  "s_add_u32 s49, s49, s47\n" \
  "s_add_u32 s73, s73, 1\n" \
  "s_branch LWQ2_%=\n" \
  "LMX2_%=:\n" \
  "v_max_f32 v118, v116, v117\n" \
  "v_mov_b32 v119, v118\n" \
  "s_nop 1\n" \
  "v_permlane16_swap_b32 v118, v119\n" \
  "v_max_f32 v118, v118, v119\n" \
  "v_mov_b32 v119, v118\n" \
  "s_nop 1\n" \
  "v_permlane32_swap_b32 v118, v119\n" \
  "v_max_f32 v118, v118, v119\n" \
  "s_nop 1\n" \
  "v_max_f32_dpp v118, v118, v118 row_ror:8 row_mask:0xf bank_mask:0xf\n" \
  "s_nop 1\n" \
  "v_max_f32_dpp v118, v118, v118 quad_perm:[2,3,0,1] row_mask:0xf bank_mask:0xf\n" \
  "s_nop 1\n" \
  "v_max_f32_dpp v118, v118, v118 row_ror:4 row_mask:0xf bank_mask:0xf\n" \
  "v_sub_f32 v116, v116, v118\n" \
  "v_sub_f32 v117, v117, v118\n" \
  "v_exp_f32 v112, v116\n" \
  "v_exp_f32 v113, v117\n" \
  "s_nop 0\n" \
  "v_cvt_pk_bf16_f32 v112, v112, v113\n" \
  "s_branch LMY2_%=\n" \
  "LMF2_%=:\n" \
  "v_mov_b32 v180, 0\n" \
  "v_pk_max_u16 v180, v180, v100\n" \
  "v_pk_max_u16 v180, v180, v101\n" \
  "v_pk_max_u16 v180, v180, v102\n" \
  "v_pk_max_u16 v180, v180, v103\n" \
  "v_pk_max_u16 v180, v180, v104\n" \
  "v_pk_max_u16 v180, v180, v105\n" \
  "v_pk_max_u16 v180, v180, v106\n" \
  "v_pk_max_u16 v180, v180, v107\n" \
  "v_pk_max_u16 v180, v180, v108\n" \
  "v_pk_max_u16 v180, v180, v109\n" \
  "v_pk_max_u16 v180, v180, v110\n" \
  "v_pk_max_u16 v180, v180, v111\n" \
  "v_pk_max_u16 v180, v180, v112\n" \
  "v_pk_max_u16 v180, v180, v113\n" \
  "v_pk_max_u16 v180, v180, v114\n" \
  "v_pk_max_u16 v180, v180, v115\n" \
  "v_pk_max_u16 v180, v180, v116\n" \
  "v_pk_max_u16 v180, v180, v117\n" \
  "v_pk_max_u16 v180, v180, v118\n" \
  "v_pk_max_u16 v180, v180, v119\n" \
  "v_pk_max_u16 v180, v180, v120\n" \
  "v_pk_max_u16 v180, v180, v121\n" \
  "v_pk_max_u16 v180, v180, v122\n" \
  "v_pk_max_u16 v180, v180, v123\n" \
  "v_pk_max_u16 v180, v180, v124\n" \
  "v_pk_max_u16 v180, v180, v125\n" \
  "v_pk_max_u16 v180, v180, v126\n" \
  "v_pk_max_u16 v180, v180, v127\n" \
  "v_pk_max_u16 v180, v180, v128\n" \
  "v_pk_max_u16 v180, v180, v129\n" \
  "v_pk_max_u16 v180, v180, v130\n" \
  "v_pk_max_u16 v180, v180, v131\n" \
  "v_pk_max_u16 v180, v180, v132\n" \
  "v_pk_max_u16 v180, v180, v133\n" \
  "v_pk_max_u16 v180, v180, v134\n" \
  "v_pk_max_u16 v180, v180, v135\n" \
  "v_pk_max_u16 v180, v180, v136\n" \
  "v_pk_max_u16 v180, v180, v137\n" \
  "v_pk_max_u16 v180, v180, v138\n" \
  "v_pk_max_u16 v180, v180, v139\n" \
  "v_lshrrev_b32 v181, 16, v180\n" \
  "v_and_b32 v180, 0xffff, v180\n" \
  "v_max_u32 v180, v180, v181\n" \
  "v_cmp_eq_u32 vcc, 0xffff, v180\n" \
  "s_nop 3\n" \
  "s_cbranch_vccz LMOK2_%=\n" \
  "s_add_u32 s67, s67, 1\n" \
  "s_cmp_gt_u32 s67, 20000\n" \
  "s_cbranch_scc1 LMOK2_%=\n" \
  "s_add_u32 s64, s64, 1\n" \
  "s_waitcnt vmcnt(0) lgkmcnt(0)\n" \
  "s_barrier\n" \
  "s_mov_b32 m0, 0\n" \
  "LHM2_%=:\n" \
  "s_add_u32 s54, s54, s88\n" \
  "s_add_u32 s49, s49, s47\n" \
  "s_add_u32 s73, s73, 1\n" \
  "LGM2_%=:\n" \
  "s_memrealtime s[74:75]\n" \
  "s_memtime s[80:81]\n" \
  "s_waitcnt lgkmcnt(0)\n" \
  "s_sub_i32 s77, s54, s74\n" \
  "s_mul_i32 s77, s77, s51\n" \
  "s_ashr_i32 s77, s77, 8\n" \
  "s_sub_i32 s76, s49, s80\n" \
  "s_min_i32 s76, s76, s77\n" \
  "s_sub_i32 s77, s77, 92\n" \
  "s_max_i32 s76, s76, s77\n" \
  "s_add_u32 s49, s76, s80\n" \
  "s_cmp_gt_i32 s76, 60\n" \
  "s_cbranch_scc1 LGM2_%=\n" \
  "s_cmp_lt_i32 s76, s48\n" \
  "s_cbranch_scc1 LHM2_%=\n" \
  "buffer_load_dwordx4 v1, s[36:39], s72 offen sc1 lds\n" \
  "buffer_load_dwordx4 v1, s[36:39], s72 offen offset:1024 sc1 lds\n" \
  "buffer_load_dwordx4 v1, s[36:39], s72 offen offset:2048 sc1 lds\n" \
  "buffer_load_dwordx4 v1, s[36:39], s72 offen offset:3072 sc1 lds\n" \
  "s_mov_b32 m0, 4096\n" \
  "s_nop 0\n" \
  "buffer_load_dwordx4 v2, s[36:39], s72 offen sc1 lds\n" \
  "buffer_load_dwordx4 v2, s[36:39], s72 offen offset:1024 sc1 lds\n" \
  "buffer_load_dwordx4 v2, s[36:39], s72 offen offset:2048 sc1 lds\n" \
  "buffer_load_dwordx4 v2, s[36:39], s72 offen offset:3072 sc1 lds\n" \
  "s_mov_b32 m0, 8192\n" \
  "s_nop 0\n" \
  "buffer_load_dwordx4 v3, s[36:39], s72 offen sc1 lds\n" \
  "buffer_load_dwordx4 v3, s[36:39], s72 offen offset:1024 sc1 lds\n" \
  "v_mov_b32 v140, 0\n" \
  "v_mov_b32 v141, 0\n" \
  "v_mov_b32 v142, 0\n" \
  "v_mov_b32 v143, 0\n" \
  "v_mov_b32 v144, 0\n" \
  "v_mov_b32 v145, 0\n" \
  "v_mov_b32 v146, 0\n" \
  "v_mov_b32 v147, 0\n" \
  "v_mov_b32 v148, 0\n" \
  "v_mov_b32 v149, 0\n" \
  "v_mov_b32 v150, 0\n" \
  "v_mov_b32 v151, 0\n" \
  "s_branch LM2_%=\n" \
  "LQF2_%=:\n" \
  "s_waitcnt lgkmcnt(0)\n" \
  "v_mov_b32 v116, 0\n" \
  "v_pk_max_u16 v116, v116, v100\n" \
  "v_pk_max_u16 v116, v116, v101\n" \
  "v_pk_max_u16 v116, v116, v102\n" \
  "v_pk_max_u16 v116, v116, v103\n" \
  "v_pk_max_u16 v116, v116, v104\n" \
  "v_pk_max_u16 v116, v116, v105\n" \
  "v_pk_max_u16 v116, v116, v106\n" \
  "v_pk_max_u16 v116, v116, v107\n" \
  "v_pk_max_u16 v116, v116, v108\n" \
  "v_pk_max_u16 v116, v116, v109\n" \
  "v_pk_max_u16 v116, v116, v110\n" \
  "v_pk_max_u16 v116, v116, v111\n" \
  "v_lshrrev_b32 v117, 16, v116\n" \
  "v_and_b32 v116, 0xffff, v116\n" \
  "v_max_u32 v116, v116, v117\n" \
  "v_cmp_eq_u32 vcc, 0xffff, v116\n" \
  "v_max3_u32 v118, v156, v157, v158\n" \
  "v_max3_u32 v118, v118, v159, v160\n" \
  "v_cmp_eq_u32 s[82:83], -1, v118\n" \
  "s_nop 3\n" \
  "s_or_b64 vcc, vcc, s[82:83]\n" \
  "s_cselect_b32 s76, 1, 0\n" \
  "v_mov_b32 v116, s76\n" \
  "ds_write_b32 v62, v116 offset:24\n" \
  "s_waitcnt lgkmcnt(0)\n" \
  "s_barrier\n" \
  "ds_read_b128 v[116:119], v62 offset:16\n" \
  "s_waitcnt lgkmcnt(0)\n" \
  "s_barrier\n" \
  "v_or3_b32 v116, v116, v117, v118\n" \
  "v_or_b32 v116, v116, v119\n" \
  "v_cmp_ne_u32 vcc, 0, v116\n" \
  "s_nop 3\n" \
  "s_cbranch_vccz LQOK2_%=\n" \
  "s_add_u32 s67, s67, 1\n" \
  "s_cmp_gt_u32 s67, 20000\n" \
  "s_cbranch_scc1 LQOK2_%=\n" \
  "s_add_u32 s65, s65, 1\n" \
  "LHQ2_%=:\n" \
  "s_add_u32 s54, s54, s88\n" \
  "s_add_u32 s49, s49, s47\n" \
  "s_add_u32 s73, s73, 1\n" \
  "LGQ2_%=:\n" \
  "s_memrealtime s[74:75]\n" \
  "s_memtime s[80:81]\n" \
  "s_waitcnt lgkmcnt(0)\n" \
  "s_sub_i32 s77, s54, s74\n" \
  "s_mul_i32 s77, s77, s51\n" \
  "s_ashr_i32 s77, s77, 8\n" \
  "s_sub_i32 s76, s49, s80\n" \
  "s_min_i32 s76, s76, s77\n" \
  "s_sub_i32 s77, s77, 92\n" \
  "s_max_i32 s76, s76, s77\n" \
  "s_add_u32 s49, s76, s80\n" \
  "s_cmp_gt_i32 s76, 60\n" \
  "s_cbranch_scc1 LGQ2_%=\n" \
  "s_cmp_lt_i32 s76, s48\n" \
  "s_cbranch_scc1 LHQ2_%=\n" \
  "s_barrier\n" \
  "s_nop 5\n" \
  "buffer_load_dwordx4 v[156:159], v15, s[36:39], s69 offen sc1\n" \
  "buffer_load_dword v160, v16, s[36:39], s69 offen sc1\n" \
  "s_add_u32 s76, s49, s45\n" \
  "v_mov_b32 v180, s76\n" \
  "ds_write_b32 v62, v180\n" \
  "v_mov_b32 v136, 0\n" \
  "v_mov_b32 v137, 0\n" \
  "v_mov_b32 v138, 0\n" \
  "v_mov_b32 v139, 0\n" \
  "v_mov_b32 v140, 0\n" \
  "v_mov_b32 v141, 0\n" \
  "v_mov_b32 v142, 0\n" \
  "v_mov_b32 v143, 0\n" \
  "v_mov_b32 v144, 0\n" \
  "v_mov_b32 v145, 0\n" \
  "v_mov_b32 v146, 0\n" \
  "v_mov_b32 v147, 0\n" \
  "s_waitcnt vmcnt(0) lgkmcnt(0)\n" \
  "s_branch LQ2_%=\n" \
  "LEND2_%=:\n" \
  "s_branch LDONE_%=\n" \
  "LSTART3_%=:\n" \
  "s_getpc_b64 s[82:83]\n" \
  "LPC3_%=:\n" \
  "s_add_u32 s84, s82, LEP3_%=-LPC3_%=+24\n" \
  "s_addc_u32 s85, s83, 0\n" \
  "v_mov_b32 v64, v8\n" \
  "v_mov_b32 v65, v9\n" \
  "v_mov_b32 v66, v10\n" \
  "v_mov_b32 v67, v11\n" \
  "ds_read_b128 v[100:103], v7 offset:0\n" \
  "ds_read_b128 v[104:107], v7 offset:4096\n" \
  "ds_read_b128 v[108:111], v7 offset:8192\n" \
  "ds_read_b128 v[112:115], v7 offset:12288\n" \
  "ds_read_b64 v[116:117], v7 offset:2048\n" \
  "ds_read_b64 v[118:119], v7 offset:6144\n" \
  "ds_read_b64 v[120:121], v7 offset:10240\n" \
  "ds_read_b64 v[122:123], v7 offset:14336\n" \
  "s_waitcnt lgkmcnt(7)\n" \
  "v_mfma_f32_16x16x32_bf16 v[124:127], v[100:103], v[184:187], 0\n" \
  "v_mfma_f32_16x16x32_bf16 v[128:131], v[100:103], v[208:211], 0\n" \
  "v_mfma_f32_16x16x32_bf16 v[132:135], v[100:103], v[232:235], 0\n" \
  "s_waitcnt lgkmcnt(6)\n" \
  "v_mfma_f32_16x16x32_bf16 v[124:127], v[104:107], v[188:191], v[124:127]\n" \
  "v_mfma_f32_16x16x32_bf16 v[128:131], v[104:107], v[212:215], v[128:131]\n" \
  "v_mfma_f32_16x16x32_bf16 v[132:135], v[104:107], v[236:239], v[132:135]\n" \
  "s_waitcnt lgkmcnt(5)\n" \
  "v_mfma_f32_16x16x32_bf16 v[124:127], v[108:111], v[192:195], v[124:127]\n" \
  "v_mfma_f32_16x16x32_bf16 v[128:131], v[108:111], v[216:219], v[128:131]\n" \
  "v_mfma_f32_16x16x32_bf16 v[132:135], v[108:111], v[240:243], v[132:135]\n" \
  "s_waitcnt lgkmcnt(4)\n" \
  "v_mfma_f32_16x16x32_bf16 v[124:127], v[112:115], v[196:199], v[124:127]\n" \
  "v_mfma_f32_16x16x32_bf16 v[128:131], v[112:115], v[220:223], v[128:131]\n" \
  "v_mfma_f32_16x16x32_bf16 v[132:135], v[112:115], v[244:247], v[132:135]\n" \
  "s_waitcnt lgkmcnt(2)\n" \
  "v_mfma_f32_16x16x32_bf16 v[124:127], v[116:119], v[200:203], v[124:127]\n" \
  "v_mfma_f32_16x16x32_bf16 v[128:131], v[116:119], v[224:227], v[128:131]\n" \
  "v_mfma_f32_16x16x32_bf16 v[132:135], v[116:119], v[248:251], v[132:135]\n" \
  "s_waitcnt lgkmcnt(0)\n" \
  "v_mfma_f32_16x16x32_bf16 v[124:127], v[120:123], v[204:207], v[124:127]\n" \
  "v_mfma_f32_16x16x32_bf16 v[128:131], v[120:123], v[228:231], v[128:131]\n" \
  "v_mfma_f32_16x16x32_bf16 v[132:135], v[120:123], v[252:255], v[132:135]\n" \
  "s_nop 3\n" \
  "v_cvt_pk_bf16_f32 v40, v124, v125\n" \
  "v_cvt_pk_bf16_f32 v41, v126, v127\n" \
  "v_cvt_pk_bf16_f32 v44, v128, v129\n" \
  "v_cvt_pk_bf16_f32 v45, v130, v131\n" \
  "s_mov_b64 exec, s[34:35]\n" \
  "v_cvt_pk_bf16_f32 v48, v132, v133\n" \
  "v_cvt_pk_bf16_f32 v49, v134, v135\n" \
  "s_mov_b64 exec, -1\n" \
  "LOOP3_%=:\n" \
  "s_barrier\n" \
  "ds_read_b128 v[100:103], v7 offset:1024\n" \
  "ds_read_b128 v[104:107], v7 offset:5120\n" \
  "ds_read_b128 v[108:111], v7 offset:9216\n" \
  "ds_read_b128 v[112:115], v7 offset:13312\n" \
  "ds_read_b64 v[116:117], v7 offset:2056\n" \
  "ds_read_b64 v[118:119], v7 offset:6152\n" \
  "ds_read_b64 v[120:121], v7 offset:10248\n" \
  "ds_read_b64 v[122:123], v7 offset:14344\n" \
  "v_mov_b32 v140, 0\n" \
  "v_mov_b32 v141, 0\n" \
  "v_mov_b32 v142, 0\n" \
  "v_mov_b32 v143, 0\n" \
  "v_mov_b32 v144, 0\n" \
  "v_mov_b32 v145, 0\n" \
  "v_mov_b32 v146, 0\n" \
  "v_mov_b32 v147, 0\n" \
  "v_mov_b32 v148, 0\n" \
  "v_mov_b32 v149, 0\n" \
  "v_mov_b32 v150, 0\n" \
  "v_mov_b32 v151, 0\n" \
  "v_xor_b32 v7, 0x4000, v7\n" \
  "v_mov_b32 v63, s68\n" \
  "s_mov_b32 s62, s41\n" \
  "s_mov_b32 s41, s71\n" \
  "s_add_u32 s68, s68, 1\n" \
  "s_mov_b32 s70, s42\n" \
  "s_add_u32 s63, s63, 2\n" \
  "s_cmp_eq_u32 s63, 60\n" \
  "s_cselect_b32 s63, 0, s63\n" \
  "s_lshr_b64 s[82:83], s[52:53], s63\n" \
  "s_lshl_b32 s42, s63, 21\n" \
  "s_lshl_b32 s76, s82, 12\n" \
  "s_and_b32 s76, s76, 0x2000\n" \
  "s_or_b32 s71, s42, s76\n" \
  "s_or_b32 s71, s71, 0x200000\n" \
  "s_lshl_b32 s76, s82, 13\n" \
  "s_and_b32 s76, s76, 0x2000\n" \
  "s_or_b32 s42, s42, s76\n" \
  "s_waitcnt lgkmcnt(7)\n" \
  "v_mfma_f32_16x16x32_bf16 v[124:127], v[100:103], v[184:187], 0\n" \
  "v_mfma_f32_16x16x32_bf16 v[128:131], v[100:103], v[208:211], 0\n" \
  "v_mfma_f32_16x16x32_bf16 v[132:135], v[100:103], v[232:235], 0\n" \
  "s_waitcnt lgkmcnt(6)\n" \
  "v_mfma_f32_16x16x32_bf16 v[124:127], v[104:107], v[188:191], v[124:127]\n" \
  "v_mfma_f32_16x16x32_bf16 v[128:131], v[104:107], v[212:215], v[128:131]\n" \
  "v_mfma_f32_16x16x32_bf16 v[132:135], v[104:107], v[236:239], v[132:135]\n" \
  "s_waitcnt lgkmcnt(5)\n" \
  "v_mfma_f32_16x16x32_bf16 v[124:127], v[108:111], v[192:195], v[124:127]\n" \
  "v_mfma_f32_16x16x32_bf16 v[128:131], v[108:111], v[216:219], v[128:131]\n" \
  "v_mfma_f32_16x16x32_bf16 v[132:135], v[108:111], v[240:243], v[132:135]\n" \
  "s_waitcnt lgkmcnt(4)\n" \
  "v_mfma_f32_16x16x32_bf16 v[124:127], v[112:115], v[196:199], v[124:127]\n" \
  "v_mfma_f32_16x16x32_bf16 v[128:131], v[112:115], v[220:223], v[128:131]\n" \
  "v_mfma_f32_16x16x32_bf16 v[132:135], v[112:115], v[244:247], v[132:135]\n" \
  "s_waitcnt lgkmcnt(2)\n" \
  "v_mfma_f32_16x16x32_bf16 v[124:127], v[116:119], v[200:203], v[124:127]\n" \
  "v_mfma_f32_16x16x32_bf16 v[128:131], v[116:119], v[224:227], v[128:131]\n" \
  "v_mfma_f32_16x16x32_bf16 v[132:135], v[116:119], v[248:251], v[132:135]\n" \
  "s_waitcnt lgkmcnt(0)\n" \
  "v_mfma_f32_16x16x32_bf16 v[124:127], v[120:123], v[204:207], v[124:127]\n" \
  "v_mfma_f32_16x16x32_bf16 v[128:131], v[120:123], v[228:231], v[128:131]\n" \
  "v_mfma_f32_16x16x32_bf16 v[132:135], v[120:123], v[252:255], v[132:135]\n" \
  "s_nop 3\n" \
  "v_cvt_pk_bf16_f32 v42, v124, v125\n" \
  "v_cvt_pk_bf16_f32 v43, v126, v127\n" \
  "v_cvt_pk_bf16_f32 v46, v128, v129\n" \
  "v_cvt_pk_bf16_f32 v47, v130, v131\n" \
  "s_mov_b64 exec, s[34:35]\n" \
  "v_cvt_pk_bf16_f32 v50, v132, v133\n" \
  "v_cvt_pk_bf16_f32 v51, v134, v135\n" \
  "s_mov_b64 exec, -1\n" \
  "s_waitcnt lgkmcnt(0)\n" \
  "s_waitcnt vmcnt(0)\n" \
  "LM3_%=:\n" \
  "s_barrier\n" \
  "ds_read_b128 v[100:103], v6 offset:0\n" \
  "ds_read_b128 v[104:107], v6 offset:1024\n" \
  "s_barrier\n" \
  "ds_read_b128 v[108:111], v6 offset:2048\n" \
  "ds_read_b128 v[112:115], v6 offset:3072\n" \
  "s_waitcnt lgkmcnt(3)\n" \
  "v_smfmac_f32_16x16x64_bf16 v[140:143], v[100:103], a[0:7], v0\n" \
  "v_smfmac_f32_16x16x64_bf16 v[144:147], v[100:103], a[80:87], v0\n" \
  "s_waitcnt lgkmcnt(2)\n" \
  "v_smfmac_f32_16x16x64_bf16 v[140:143], v[104:107], a[8:15], v0\n" \
  "v_smfmac_f32_16x16x64_bf16 v[144:147], v[104:107], a[88:95], v0\n" \
  "s_barrier\n" \
  "ds_read_b128 v[116:119], v6 offset:4096\n" \
  "ds_read_b128 v[120:123], v6 offset:5120\n" \
  "s_waitcnt lgkmcnt(3)\n" \
  "v_smfmac_f32_16x16x64_bf16 v[140:143], v[108:111], a[16:23], v0\n" \
  "v_smfmac_f32_16x16x64_bf16 v[144:147], v[108:111], a[96:103], v0\n" \
  "s_waitcnt lgkmcnt(2)\n" \
  "v_smfmac_f32_16x16x64_bf16 v[140:143], v[112:115], a[24:31], v0\n" \
  "v_smfmac_f32_16x16x64_bf16 v[144:147], v[112:115], a[104:111], v0\n" \
  "s_barrier\n" \
  "ds_read_b128 v[124:127], v6 offset:6144\n" \
  "ds_read_b128 v[128:131], v6 offset:7168\n" \
  "s_waitcnt lgkmcnt(3)\n" \
  "v_smfmac_f32_16x16x64_bf16 v[140:143], v[116:119], a[32:39], v0\n" \
  "v_smfmac_f32_16x16x64_bf16 v[144:147], v[116:119], a[112:119], v0\n" \
  "s_waitcnt lgkmcnt(2)\n" \
  "v_smfmac_f32_16x16x64_bf16 v[140:143], v[120:123], a[40:47], v0\n" \
  "v_smfmac_f32_16x16x64_bf16 v[144:147], v[120:123], a[120:127], v0\n" \
  "s_barrier\n" \
  "ds_read_b128 v[132:135], v6 offset:8192\n" \
  "ds_read_b128 v[136:139], v6 offset:9216\n" \
  "s_waitcnt lgkmcnt(3)\n" \
  "v_smfmac_f32_16x16x64_bf16 v[140:143], v[124:127], a[48:55], v0\n" \
  "v_smfmac_f32_16x16x64_bf16 v[144:147], v[124:127], a[128:135], v0\n" \
  "s_waitcnt lgkmcnt(2)\n" \
  "v_smfmac_f32_16x16x64_bf16 v[140:143], v[128:131], a[56:63], v0\n" \
  "v_smfmac_f32_16x16x64_bf16 v[144:147], v[128:131], a[136:143], v0\n" \
  "s_waitcnt lgkmcnt(1)\n" \
  "v_smfmac_f32_16x16x64_bf16 v[140:143], v[132:135], a[64:71], v0\n" \
  "v_smfmac_f32_16x16x64_bf16 v[144:147], v[132:135], a[144:151], v0\n" \
  "s_waitcnt lgkmcnt(0)\n" \
  "v_smfmac_f32_16x16x64_bf16 v[140:143], v[136:139], a[72:79], v0\n" \
  "v_smfmac_f32_16x16x64_bf16 v[144:147], v[136:139], a[152:159], v0\n" \
  "v_smfmac_f32_16x16x64_bf16 v[148:151], v[100:103], a[160:167], v0\n" \
  "v_smfmac_f32_16x16x64_bf16 v[148:151], v[104:107], a[168:175], v0\n" \
  "s_nop 4\n" \
  "v_cmp_u_f32 vcc, v140, v141\n" \
  "v_cmp_u_f32 s[82:83], v142, v143\n" \
  "v_smfmac_f32_16x16x64_bf16 v[148:151], v[108:111], a[176:183], v0\n" \
  "s_or_b64 vcc, vcc, s[82:83]\n" \
  "v_smfmac_f32_16x16x64_bf16 v[148:151], v[112:115], a[184:191], v0\n" \
  "s_cbranch_vccnz LMF3_%=\n" \
  "LMOK3_%=:\n" \
  "v_permlane32_swap_b32 v140, v144\n" \
  "v_permlane32_swap_b32 v141, v145\n" \
  "v_smfmac_f32_16x16x64_bf16 v[148:151], v[116:119], a[192:199], v0\n" \
  "v_permlane32_swap_b32 v142, v146\n" \
  "v_permlane32_swap_b32 v143, v147\n" \
  "v_smfmac_f32_16x16x64_bf16 v[148:151], v[120:123], a[200:207], v0\n" \
  "v_pk_add_f32 v[152:153], v[140:141], v[144:145]\n" \
  "v_smfmac_f32_16x16x64_bf16 v[148:151], v[124:127], a[208:215], v0\n" \
  "v_pk_add_f32 v[154:155], v[142:143], v[146:147]\n" \
  "v_smfmac_f32_16x16x64_bf16 v[148:151], v[128:131], a[216:223], v0\n" \
  "s_barrier\n" \
  "v_cvt_pk_bf16_f32 v152, v152, v153\n" \
  "v_smfmac_f32_16x16x64_bf16 v[148:151], v[132:135], a[224:231], v0\n" \
  "v_cvt_pk_bf16_f32 v153, v154, v155\n" \
  "v_smfmac_f32_16x16x64_bf16 v[148:151], v[136:139], a[232:239], v0\n" \
  "v_pk_min_u16 v152, v152, -2 op_sel_hi:[1,0]\n" \
  "v_pk_min_u16 v153, v153, -2 op_sel_hi:[1,0]\n" \
  "buffer_store_dwordx2 v[152:153], v13, s[36:39], s69 offen sc1\n" \
  "ds_write_b64 v35, v[152:153]\n" \
  "s_nop 3\n" \
  "v_permlane32_swap_b32 v148, v150\n" \
  "v_permlane32_swap_b32 v149, v151\n" \
  "v_pk_add_f32 v[154:155], v[148:149], v[150:151]\n" \
  "v_cvt_pk_bf16_f32 v154, v154, v155\n" \
  "v_pk_min_u16 v154, v154, -2 op_sel_hi:[1,0]\n" \
  "buffer_store_dword v154, v14, s[36:39], s69 offen sc1\n" \
  "ds_write_b32 v12, v154\n" \
  "s_cmp_eq_u32 s68, 64\n" \
  "s_cbranch_scc1 LEND3_%=\n" \
  "s_mov_b32 m0, 20480\n" \
  "s_barrier\n" \
  "buffer_load_dwordx4 v1, s[36:39], s69 offen sc1 lds\n" \
  "buffer_load_dwordx4 v2, s[36:39], s69 offen offset:1024 sc1 lds\n" \
  "buffer_load_dwordx4 v3, s[36:39], s69 offen offset:2048 sc1 lds\n" \
  "buffer_load_dwordx4 v[156:159], v15, s[36:39], s69 offen sc1\n" \
  "buffer_load_dword v160, v16, s[36:39], s69 offen sc1\n" \
  "s_mov_b32 s72, s70\n" \
  "v_mov_b32 v136, 0\n" \
  "v_mov_b32 v137, 0\n" \
  "v_mov_b32 v138, 0\n" \
  "v_mov_b32 v139, 0\n" \
  "v_mov_b32 v140, 0\n" \
  "v_mov_b32 v141, 0\n" \
  "v_mov_b32 v142, 0\n" \
  "v_mov_b32 v143, 0\n" \
  "v_mov_b32 v144, 0\n" \
  "v_mov_b32 v145, 0\n" \
  "v_mov_b32 v146, 0\n" \
  "v_mov_b32 v147, 0\n" \
  "s_waitcnt lgkmcnt(0)\n" \
  "s_nop 15\n" \
  "s_nop 15\n" \
  "s_getreg_b32 s76, hwreg(HW_REG_IB_STS, 0, 4)\n" \
  "v_mov_b32 v183, v182\n" \
  "s_cmp_le_u32 s76, 2\n" \
  "s_cbranch_scc1 LHXV3_%=\n" \
  "s_getreg_b32 s76, hwreg(HW_REG_IB_STS, 0, 4)\n" \
  "v_mov_b32 v183, v182\n" \
  "s_cmp_le_u32 s76, 2\n" \
  "s_cbranch_scc1 LHXV3_%=\n" \
  "s_getreg_b32 s76, hwreg(HW_REG_IB_STS, 0, 4)\n" \
  "v_mov_b32 v183, v182\n" \
  "s_cmp_le_u32 s76, 2\n" \
  "s_cbranch_scc1 LHXV3_%=\n" \
  "s_getreg_b32 s76, hwreg(HW_REG_IB_STS, 0, 4)\n" \
  "v_mov_b32 v183, v182\n" \
  "s_cmp_le_u32 s76, 2\n" \
  "s_cbranch_scc1 LHXV3_%=\n" \
  "s_getreg_b32 s76, hwreg(HW_REG_IB_STS, 0, 4)\n" \
  "v_mov_b32 v183, v182\n" \
  "s_cmp_le_u32 s76, 2\n" \
  "s_cbranch_scc1 LHXV3_%=\n" \
  "s_getreg_b32 s76, hwreg(HW_REG_IB_STS, 0, 4)\n" \
  "v_mov_b32 v183, v182\n" \
  "s_cmp_le_u32 s76, 2\n" \
  "s_cbranch_scc1 LHXV3_%=\n" \
  "s_getreg_b32 s76, hwreg(HW_REG_IB_STS, 0, 4)\n" \
  "v_mov_b32 v183, v182\n" \
  "s_cmp_le_u32 s76, 2\n" \
  "s_cbranch_scc1 LHXV3_%=\n" \
  "s_getreg_b32 s76, hwreg(HW_REG_IB_STS, 0, 4)\n" \
  "v_mov_b32 v183, v182\n" \
  "s_cmp_le_u32 s76, 2\n" \
  "s_cbranch_scc1 LHXV3_%=\n" \
  "s_getreg_b32 s76, hwreg(HW_REG_IB_STS, 0, 4)\n" \
  "v_mov_b32 v183, v182\n" \
  "s_cmp_le_u32 s76, 2\n" \
  "s_cbranch_scc1 LHXV3_%=\n" \
  "s_getreg_b32 s76, hwreg(HW_REG_IB_STS, 0, 4)\n" \
  "v_mov_b32 v183, v182\n" \
  "s_cmp_le_u32 s76, 2\n" \
  "s_cbranch_scc1 LHXV3_%=\n" \
  "s_getreg_b32 s76, hwreg(HW_REG_IB_STS, 0, 4)\n" \
  "v_mov_b32 v183, v182\n" \
  "s_cmp_le_u32 s76, 2\n" \
  "s_cbranch_scc1 LHXV3_%=\n" \
  "s_getreg_b32 s76, hwreg(HW_REG_IB_STS, 0, 4)\n" \
  "v_mov_b32 v183, v182\n" \
  "s_cmp_le_u32 s76, 2\n" \
  "s_cbranch_scc1 LHXV3_%=\n" \
  "s_getreg_b32 s76, hwreg(HW_REG_IB_STS, 0, 4)\n" \
  "v_mov_b32 v183, v182\n" \
  "s_cmp_le_u32 s76, 2\n" \
  "s_cbranch_scc1 LHXV3_%=\n" \
  "s_getreg_b32 s76, hwreg(HW_REG_IB_STS, 0, 4)\n" \
  "v_mov_b32 v183, v182\n" \
  "s_cmp_le_u32 s76, 2\n" \
  "s_cbranch_scc1 LHXV3_%=\n" \
  "s_getreg_b32 s76, hwreg(HW_REG_IB_STS, 0, 4)\n" \
  "v_mov_b32 v183, v182\n" \
  "s_cmp_le_u32 s76, 2\n" \
  "s_cbranch_scc1 LHXV3_%=\n" \
  "s_getreg_b32 s76, hwreg(HW_REG_IB_STS, 0, 4)\n" \
  "v_mov_b32 v183, v182\n" \
  "s_cmp_le_u32 s76, 2\n" \
  "s_cbranch_scc1 LHXV3_%=\n" \
  "s_getreg_b32 s76, hwreg(HW_REG_IB_STS, 0, 4)\n" \
  "v_mov_b32 v183, v182\n" \
  "s_cmp_le_u32 s76, 2\n" \
  "s_cbranch_scc1 LHXV3_%=\n" \
  "s_getreg_b32 s76, hwreg(HW_REG_IB_STS, 0, 4)\n" \
  "v_mov_b32 v183, v182\n" \
  "s_cmp_le_u32 s76, 2\n" \
  "s_cbranch_scc1 LHXV3_%=\n" \
  "s_getreg_b32 s76, hwreg(HW_REG_IB_STS, 0, 4)\n" \
  "v_mov_b32 v183, v182\n" \
  "s_cmp_le_u32 s76, 2\n" \
  "s_cbranch_scc1 LHXV3_%=\n" \
  "s_getreg_b32 s76, hwreg(HW_REG_IB_STS, 0, 4)\n" \
  "v_mov_b32 v183, v182\n" \
  "s_cmp_le_u32 s76, 2\n" \
  "s_cbranch_scc1 LHXV3_%=\n" \
  "LHXV3_%=:\n" \
  "s_waitcnt vmcnt(2)\n" \
  "LQ3_%=:\n" \
  "s_barrier\n" \
  "ds_read_b64_tr_b16 v[100:101], v20\n" \
  "ds_read_b64_tr_b16 v[102:103], v21\n" \
  "ds_read_b64_tr_b16 v[104:105], v22\n" \
  "ds_read_b64_tr_b16 v[106:107], v23\n" \
  "ds_read_b64_tr_b16 v[108:109], v24\n" \
  "ds_read_b64_tr_b16 v[110:111], v25\n" \
  "v_mov_b32 v183, v182\n" \
  "s_nop 0\n" \
  "v_mov_b32 v183, v182\n" \
  "s_nop 0\n" \
  "v_mov_b32 v183, v182\n" \
  "s_nop 0\n" \
  "v_mov_b32 v183, v182\n" \
  "s_nop 0\n" \
  "s_waitcnt lgkmcnt(4)\n" \
  "v_smfmac_f32_16x16x64_bf16 v[144:147], v[100:103], v[232:239], v0\n" \
  "v_mov_b32 v183, v182\n" \
  "v_smfmac_f32_16x16x64_bf16 v[136:139], v[100:103], v[184:191], v0\n" \
  "v_mov_b32 v183, v182\n" \
  "v_smfmac_f32_16x16x64_bf16 v[140:143], v[100:103], v[208:215], v0\n" \
  "v_mov_b32 v183, v182\n" \
  "s_waitcnt lgkmcnt(0)\n" \
  "v_smfmac_f32_16x16x64_bf16 v[144:147], v[104:107], v[240:247], v0\n" \
  "v_mov_b32 v183, v182\n" \
  "v_smfmac_f32_16x16x64_bf16 v[136:139], v[104:107], v[192:199], v0\n" \
  "v_mov_b32 v183, v182\n" \
  "s_getreg_b32 s76, hwreg(HW_REG_IB_STS, 0, 4)\n" \
  "v_mov_b32 v183, v182\n" \
  "s_cmp_le_u32 s76, 0\n" \
  "s_cbranch_scc1 LHXQ3_%=\n" \
  "s_getreg_b32 s76, hwreg(HW_REG_IB_STS, 0, 4)\n" \
  "v_mov_b32 v183, v182\n" \
  "s_cmp_le_u32 s76, 0\n" \
  "s_cbranch_scc1 LHXQ3_%=\n" \
  "s_getreg_b32 s76, hwreg(HW_REG_IB_STS, 0, 4)\n" \
  "v_mov_b32 v183, v182\n" \
  "s_cmp_le_u32 s76, 0\n" \
  "s_cbranch_scc1 LHXQ3_%=\n" \
  "s_getreg_b32 s76, hwreg(HW_REG_IB_STS, 0, 4)\n" \
  "v_mov_b32 v183, v182\n" \
  "s_cmp_le_u32 s76, 0\n" \
  "s_cbranch_scc1 LHXQ3_%=\n" \
  "s_getreg_b32 s76, hwreg(HW_REG_IB_STS, 0, 4)\n" \
  "v_mov_b32 v183, v182\n" \
  "s_cmp_le_u32 s76, 0\n" \
  "s_cbranch_scc1 LHXQ3_%=\n" \
  "s_getreg_b32 s76, hwreg(HW_REG_IB_STS, 0, 4)\n" \
  "v_mov_b32 v183, v182\n" \
  "s_cmp_le_u32 s76, 0\n" \
  "s_cbranch_scc1 LHXQ3_%=\n" \
  "s_getreg_b32 s76, hwreg(HW_REG_IB_STS, 0, 4)\n" \
  "v_mov_b32 v183, v182\n" \
  "s_cmp_le_u32 s76, 0\n" \
  "s_cbranch_scc1 LHXQ3_%=\n" \
  "s_getreg_b32 s76, hwreg(HW_REG_IB_STS, 0, 4)\n" \
  "v_mov_b32 v183, v182\n" \
  "s_cmp_le_u32 s76, 0\n" \
  "s_cbranch_scc1 LHXQ3_%=\n" \
  "s_getreg_b32 s76, hwreg(HW_REG_IB_STS, 0, 4)\n" \
  "v_mov_b32 v183, v182\n" \
  "s_cmp_le_u32 s76, 0\n" \
  "s_cbranch_scc1 LHXQ3_%=\n" \
  "s_getreg_b32 s76, hwreg(HW_REG_IB_STS, 0, 4)\n" \
  "v_mov_b32 v183, v182\n" \
  "s_cmp_le_u32 s76, 0\n" \
  "s_cbranch_scc1 LHXQ3_%=\n" \
  "LHXQ3_%=:\n" \
  "s_waitcnt vmcnt(0)\n" \
  "v_mov_b32 v115, v160\n" \
  "v_mov_b32 v114, v160\n" \
  "v_permlane32_swap_b32 v156, v158\n" \
  "v_permlane32_swap_b32 v157, v159\n" \
  "v_pk_add_f32 v[112:113], v[156:157], v[158:159]\n" \
  "v_permlane32_swap_b32 v114, v115\n" \
  "v_add_f32 v117, v114, v115\n" \
  "v_permlane16_swap_b32 v112, v113\n" \
  "v_add_f32 v114, v114, v115\n" \
  "v_add_f32 v116, v112, v113\n" \
  "v_smfmac_f32_16x16x64_bf16 v[140:143], v[104:107], v[216:223], v0\n" \
  "v_permlane16_swap_b32 v117, v114\n" \
  "v_mul_f32 v116, v116, v60\n" \
  "v_add_f32 v117, v117, v114\n" \
  "v_exp_f32 v112, v116\n" \
  "v_mul_f32 v117, v117, v60\n" \
  "v_max_f32 v118, |v116|, |v117|\n" \
  "v_exp_f32 v113, v117\n" \
  "v_cmp_lt_f32 vcc, 0x42800000, v118\n" \
  "v_cvt_pk_bf16_f32 v112, v112, v113\n" \
  "v_smfmac_f32_16x16x64_bf16 v[144:147], v[108:111], v[248:255], v0\n" \
  "s_cbranch_vccnz LMX3_%=\n" \
  "LMY3_%=:\n" \
  "ds_write_b16 v18, v112\n" \
  "ds_write_b16_d16_hi v19, v112\n" \
  "v_smfmac_f32_16x16x64_bf16 v[136:139], v[108:111], v[200:207], v0\n" \
  "v_smfmac_f32_16x16x64_bf16 v[140:143], v[108:111], v[224:231], v0\n" \
  "s_mov_b64 exec, s[34:35]\n" \
  "v_cvt_pk_bf16_f32 v92, v144, v145\n" \
  "v_cvt_pk_bf16_f32 v93, v146, v147\n" \
  "s_mov_b64 exec, -1\n" \
  "v_cvt_pk_bf16_f32 v84, v136, v137\n" \
  "v_cvt_pk_bf16_f32 v85, v138, v139\n" \
  "s_nop 1\n" \
  "v_cvt_pk_bf16_f32 v88, v140, v141\n" \
  "v_cvt_pk_bf16_f32 v89, v142, v143\n" \
  "s_waitcnt lgkmcnt(0)\n" \
  "s_barrier\n" \
  "ds_read_b128 v[120:123], v28\n" \
  "ds_read_b64 v[80:81], v36\n" \
  "v_mov_b32 v183, v182\n" \
  "s_nop 0\n" \
  "v_mov_b32 v183, v182\n" \
  "s_nop 0\n" \
  "v_mov_b32 v183, v182\n" \
  "s_nop 0\n" \
  "v_mov_b32 v183, v182\n" \
  "s_nop 0\n" \
  "s_waitcnt lgkmcnt(1)\n" \
  "v_mfma_f32_16x16x32_bf16 v[172:175], v[48:51], v[120:123], 0\n" \
  "v_mfma_f32_16x16x32_bf16 v[164:167], v[40:43], v[120:123], 0\n" \
  "s_waitcnt lgkmcnt(0)\n" \
  "v_mfma_f32_16x16x32_bf16 v[172:175], v[92:95], v[80:83], v[172:175]\n" \
  "ds_read_b32 v180, v62\n" \
  "v_mfma_f32_16x16x32_bf16 v[168:171], v[44:47], v[120:123], 0\n" \
  "v_mfma_f32_16x16x32_bf16 v[164:167], v[84:87], v[80:83], v[164:167]\n" \
  "v_rcp_f32 v177, v172\n" \
  "v_mfma_f32_16x16x32_bf16 v[168:171], v[88:91], v[80:83], v[168:171]\n" \
  "v_mov_b32 v176, v177\n" \
  "v_cmp_u_f32 vcc, v172, v172\n" \
  "s_nop 0\n" \
  "v_permlane32_swap_b32 v177, v176\n" \
  "v_pk_mul_f32 v[164:165], v[164:165], v[176:177] op_sel_hi:[1,0]\n" \
  "v_pk_mul_f32 v[166:167], v[166:167], v[176:177] op_sel_hi:[1,0]\n" \
  "v_pk_mul_f32 v[172:173], v[172:173], v[176:177] op_sel_hi:[1,0]\n" \
  "v_pk_mul_f32 v[174:175], v[174:175], v[176:177] op_sel_hi:[1,0]\n" \
  "v_pk_mul_f32 v[168:169], v[168:169], v[176:177] op_sel_hi:[1,0]\n" \
  "v_pk_mul_f32 v[170:171], v[170:171], v[176:177] op_sel_hi:[1,0]\n" \
  "v_cvt_pk_bf16_f32 v164, v164, v165\n" \
  "v_cvt_pk_bf16_f32 v165, v166, v167\n" \
  "v_cvt_pk_bf16_f32 v166, v168, v169\n" \
  "v_cvt_pk_bf16_f32 v167, v170, v171\n" \
  "s_cbranch_vccnz LQF3_%=\n" \
  "LQOK3_%=:\n" \
  "buffer_store_dwordx4 v[164:167], v26, s[36:39], s69 offen sc1\n" \
  "v_cvt_pk_bf16_f32 v172, v172, v173\n" \
  "v_cvt_pk_bf16_f32 v173, v174, v175\n" \
  "buffer_store_dwordx2 v[172:173], v27, s[36:39], s69 offen sc1\n" \
  "buffer_store_dwordx2 v[152:153], v31, s[36:39], s62 offen sc1\n" \
  "buffer_store_dword v154, v32, s[36:39], s62 offen sc1\n" \
  "s_waitcnt lgkmcnt(0)\n" \
  "v_readfirstlane_b32 s50, v180\n" \
  "LUP3_%=:\n" \
  "s_memtime s[80:81]\n" \
  "s_waitcnt lgkmcnt(0)\n" \
  "s_sub_i32 s76, s50, s80\n" \
  "s_cmp_gt_i32 s76, 168\n" \
  "s_cbranch_scc1 LUP3_%=\n" \
  "s_max_i32 s76, s76, 48\n" \
  "s_lshr_b32 s76, s76, 3\n" \
  "s_lshl_b32 s76, s76, 2\n" \
  "s_sub_u32 s78, s84, s76\n" \
  "s_subb_u32 s79, s85, 0\n" \
  "s_setpc_b64 s[78:79]\n" \
  "s_nop 1\n" \
  "s_nop 1\n" \
  "s_nop 1\n" \
  "s_nop 1\n" \
  "s_nop 1\n" \
  "s_nop 1\n" \
  "s_nop 1\n" \
  "s_nop 1\n" \
  "s_nop 1\n" \
  "s_nop 1\n" \
  "s_nop 1\n" \
  "s_nop 1\n" \
  "s_nop 1\n" \
  "s_nop 1\n" \
  "s_nop 1\n" \
  "s_nop 1\n" \
  "LEP3_%=:\n" \
  "buffer_load_dwordx4 v[140:143], v29, s[36:39], s69 offen sc1\n" \
  "buffer_load_dwordx4 v[144:147], v29, s[36:39], s69 offen offset:1024 sc1\n" \
  "buffer_load_dwordx4 v[148:151], v29, s[36:39], s69 offen offset:2048 sc1\n" \
  "buffer_load_dwordx4 v[152:155], v29, s[36:39], s69 offen offset:3072 sc1\n" \
  "ds_read_b128 v[100:103], v7 offset:0\n" \
  "ds_read_b128 v[104:107], v7 offset:4096\n" \
  "ds_read_b128 v[108:111], v7 offset:8192\n" \
  "ds_read_b128 v[112:115], v7 offset:12288\n" \
  "ds_read_b64 v[116:117], v7 offset:2048\n" \
  "ds_read_b64 v[118:119], v7 offset:6144\n" \
  "ds_read_b64 v[120:121], v7 offset:10240\n" \
  "ds_read_b64 v[122:123], v7 offset:14336\n" \
  "s_waitcnt lgkmcnt(7)\n" \
  "v_mfma_f32_16x16x32_bf16 v[124:127], v[100:103], v[184:187], 0\n" \
  "v_mfma_f32_16x16x32_bf16 v[128:131], v[100:103], v[208:211], 0\n" \
  "v_mfma_f32_16x16x32_bf16 v[132:135], v[100:103], v[232:235], 0\n" \
  "s_waitcnt lgkmcnt(6)\n" \
  "v_mfma_f32_16x16x32_bf16 v[124:127], v[104:107], v[188:191], v[124:127]\n" \
  "v_mfma_f32_16x16x32_bf16 v[128:131], v[104:107], v[212:215], v[128:131]\n" \
  "v_mfma_f32_16x16x32_bf16 v[132:135], v[104:107], v[236:239], v[132:135]\n" \
  "s_waitcnt lgkmcnt(5)\n" \
  "v_mfma_f32_16x16x32_bf16 v[124:127], v[108:111], v[192:195], v[124:127]\n" \
  "v_mfma_f32_16x16x32_bf16 v[128:131], v[108:111], v[216:219], v[128:131]\n" \
  "v_mfma_f32_16x16x32_bf16 v[132:135], v[108:111], v[240:243], v[132:135]\n" \
  "s_waitcnt lgkmcnt(4)\n" \
  "v_mfma_f32_16x16x32_bf16 v[124:127], v[112:115], v[196:199], v[124:127]\n" \
  "v_mfma_f32_16x16x32_bf16 v[128:131], v[112:115], v[220:223], v[128:131]\n" \
  "v_mfma_f32_16x16x32_bf16 v[132:135], v[112:115], v[244:247], v[132:135]\n" \
  "s_waitcnt lgkmcnt(2)\n" \
  "v_mfma_f32_16x16x32_bf16 v[124:127], v[116:119], v[200:203], v[124:127]\n" \
  "v_mfma_f32_16x16x32_bf16 v[128:131], v[116:119], v[224:227], v[128:131]\n" \
  "v_mfma_f32_16x16x32_bf16 v[132:135], v[116:119], v[248:251], v[132:135]\n" \
  "s_waitcnt lgkmcnt(0)\n" \
  "v_mfma_f32_16x16x32_bf16 v[124:127], v[120:123], v[204:207], v[124:127]\n" \
  "v_mfma_f32_16x16x32_bf16 v[128:131], v[120:123], v[228:231], v[128:131]\n" \
  "v_mfma_f32_16x16x32_bf16 v[132:135], v[120:123], v[252:255], v[132:135]\n" \
  "s_nop 3\n" \
  "v_cvt_pk_bf16_f32 v40, v124, v125\n" \
  "v_cvt_pk_bf16_f32 v41, v126, v127\n" \
  "v_cvt_pk_bf16_f32 v44, v128, v129\n" \
  "v_cvt_pk_bf16_f32 v45, v130, v131\n" \
  "s_mov_b64 exec, s[34:35]\n" \
  "v_cvt_pk_bf16_f32 v48, v132, v133\n" \
  "v_cvt_pk_bf16_f32 v49, v134, v135\n" \
  "s_mov_b64 exec, -1\n" \
  "LP3_%=:\n" \
  "s_waitcnt vmcnt(3)\n" \
  "v_mfma_f32_16x16x32_bf16 v[156:159], v[64:67], v[140:143], 0\n" \
  "s_waitcnt vmcnt(2)\n" \
  "v_mfma_f32_16x16x32_bf16 v[156:159], v[64:67], v[144:147], v[156:159]\n" \
  "s_waitcnt vmcnt(1)\n" \
  "v_mfma_f32_16x16x32_bf16 v[156:159], v[64:67], v[148:151], v[156:159]\n" \
  "s_waitcnt vmcnt(0)\n" \
  "v_mfma_f32_16x16x32_bf16 v[156:159], v[64:67], v[152:155], v[156:159]\n" \
  "s_nop 7\n" \
  "v_add_f32_dpp v156, v156, v156 row_ror:8 row_mask:0xf bank_mask:0xf\n" \
  "v_add_f32_dpp v157, v157, v157 row_ror:8 row_mask:0xf bank_mask:0xf\n" \
  "v_add_f32_dpp v158, v158, v158 row_ror:8 row_mask:0xf bank_mask:0xf\n" \
  "v_add_f32_dpp v159, v159, v159 row_ror:8 row_mask:0xf bank_mask:0xf\n" \
  "v_cmp_u_f32 vcc, v156, v156\n" \
  "v_cvt_pk_bf16_f32 v160, v156, v157\n" \
  "v_cvt_pk_bf16_f32 v161, v158, v159\n" \
  "s_cbranch_vccnz LPF3_%=\n" \
  "LPOK3_%=:\n" \
  "buffer_store_dwordx2 v[160:161], v30, s[36:39], s70 offen sc1\n" \
  "s_mov_b32 s69, s70\n" \
  "s_branch LOOP3_%=\n" \
  "LMX3_%=:\n" \
  "v_max_f32 v118, v116, v117\n" \
  "v_mov_b32 v119, v118\n" \
  "s_nop 1\n" \
  "v_permlane16_swap_b32 v118, v119\n" \
  "v_max_f32 v118, v118, v119\n" \
  "v_mov_b32 v119, v118\n" \
  "s_nop 1\n" \
  "v_permlane32_swap_b32 v118, v119\n" \
  "v_max_f32 v118, v118, v119\n" \
  "s_nop 1\n" \
  "v_max_f32_dpp v118, v118, v118 row_ror:8 row_mask:0xf bank_mask:0xf\n" \
  "s_nop 1\n" \
  "v_max_f32_dpp v118, v118, v118 quad_perm:[2,3,0,1] row_mask:0xf bank_mask:0xf\n" \
  "s_nop 1\n" \
  "v_max_f32_dpp v118, v118, v118 row_ror:4 row_mask:0xf bank_mask:0xf\n" \
  "v_sub_f32 v116, v116, v118\n" \
  "v_sub_f32 v117, v117, v118\n" \
  "v_exp_f32 v112, v116\n" \
  "v_exp_f32 v113, v117\n" \
  "s_nop 0\n" \
  "v_cvt_pk_bf16_f32 v112, v112, v113\n" \
  "s_branch LMY3_%=\n" \
  "LMF3_%=:\n" \
  "v_mov_b32 v180, 0\n" \
  "v_pk_max_u16 v180, v180, v100\n" \
  "v_pk_max_u16 v180, v180, v101\n" \
  "v_pk_max_u16 v180, v180, v102\n" \
  "v_pk_max_u16 v180, v180, v103\n" \
  "v_pk_max_u16 v180, v180, v104\n" \
  "v_pk_max_u16 v180, v180, v105\n" \
  "v_pk_max_u16 v180, v180, v106\n" \
  "v_pk_max_u16 v180, v180, v107\n" \
  "v_pk_max_u16 v180, v180, v108\n" \
  "v_pk_max_u16 v180, v180, v109\n" \
  "v_pk_max_u16 v180, v180, v110\n" \
  "v_pk_max_u16 v180, v180, v111\n" \
  "v_pk_max_u16 v180, v180, v112\n" \
  "v_pk_max_u16 v180, v180, v113\n" \
  "v_pk_max_u16 v180, v180, v114\n" \
  "v_pk_max_u16 v180, v180, v115\n" \
  "v_pk_max_u16 v180, v180, v116\n" \
  "v_pk_max_u16 v180, v180, v117\n" \
  "v_pk_max_u16 v180, v180, v118\n" \
  "v_pk_max_u16 v180, v180, v119\n" \
  "v_pk_max_u16 v180, v180, v120\n" \
  "v_pk_max_u16 v180, v180, v121\n" \
  "v_pk_max_u16 v180, v180, v122\n" \
  "v_pk_max_u16 v180, v180, v123\n" \
  "v_pk_max_u16 v180, v180, v124\n" \
  "v_pk_max_u16 v180, v180, v125\n" \
  "v_pk_max_u16 v180, v180, v126\n" \
  "v_pk_max_u16 v180, v180, v127\n" \
  "v_pk_max_u16 v180, v180, v128\n" \
  "v_pk_max_u16 v180, v180, v129\n" \
  "v_pk_max_u16 v180, v180, v130\n" \
  "v_pk_max_u16 v180, v180, v131\n" \
  "v_pk_max_u16 v180, v180, v132\n" \
  "v_pk_max_u16 v180, v180, v133\n" \
  "v_pk_max_u16 v180, v180, v134\n" \
  "v_pk_max_u16 v180, v180, v135\n" \
  "v_pk_max_u16 v180, v180, v136\n" \
  "v_pk_max_u16 v180, v180, v137\n" \
  "v_pk_max_u16 v180, v180, v138\n" \
  "v_pk_max_u16 v180, v180, v139\n" \
  "v_lshrrev_b32 v181, 16, v180\n" \
  "v_and_b32 v180, 0xffff, v180\n" \
  "v_max_u32 v180, v180, v181\n" \
  "v_cmp_eq_u32 vcc, 0xffff, v180\n" \
  "s_nop 3\n" \
  "s_cbranch_vccz LMOK3_%=\n" \
  "s_add_u32 s67, s67, 1\n" \
  "s_cmp_gt_u32 s67, 20000\n" \
  "s_cbranch_scc1 LMOK3_%=\n" \
  "s_add_u32 s64, s64, 1\n" \
  "s_waitcnt vmcnt(0) lgkmcnt(0)\n" \
  "s_barrier\n" \
  "v_mov_b32 v140, 0\n" \
  "v_mov_b32 v141, 0\n" \
  "v_mov_b32 v142, 0\n" \
  "v_mov_b32 v143, 0\n" \
  "v_mov_b32 v144, 0\n" \
  "v_mov_b32 v145, 0\n" \
  "v_mov_b32 v146, 0\n" \
  "v_mov_b32 v147, 0\n" \
  "v_mov_b32 v148, 0\n" \
  "v_mov_b32 v149, 0\n" \
  "v_mov_b32 v150, 0\n" \
  "v_mov_b32 v151, 0\n" \
  "s_branch LM3_%=\n" \
  "LQF3_%=:\n" \
  "s_waitcnt lgkmcnt(0)\n" \
  "v_mov_b32 v116, 0\n" \
  "v_pk_max_u16 v116, v116, v100\n" \
  "v_pk_max_u16 v116, v116, v101\n" \
  "v_pk_max_u16 v116, v116, v102\n" \
  "v_pk_max_u16 v116, v116, v103\n" \
  "v_pk_max_u16 v116, v116, v104\n" \
  "v_pk_max_u16 v116, v116, v105\n" \
  "v_pk_max_u16 v116, v116, v106\n" \
  "v_pk_max_u16 v116, v116, v107\n" \
  "v_pk_max_u16 v116, v116, v108\n" \
  "v_pk_max_u16 v116, v116, v109\n" \
  "v_pk_max_u16 v116, v116, v110\n" \
  "v_pk_max_u16 v116, v116, v111\n" \
  "v_lshrrev_b32 v117, 16, v116\n" \
  "v_and_b32 v116, 0xffff, v116\n" \
  "v_max_u32 v116, v116, v117\n" \
  "v_cmp_eq_u32 vcc, 0xffff, v116\n" \
  "v_max3_u32 v118, v156, v157, v158\n" \
  "v_max3_u32 v118, v118, v159, v160\n" \
  "v_cmp_eq_u32 s[82:83], -1, v118\n" \
  "s_nop 3\n" \
  "s_or_b64 vcc, vcc, s[82:83]\n" \
  "s_cselect_b32 s76, 1, 0\n" \
  "v_mov_b32 v116, s76\n" \
  "ds_write_b32 v62, v116 offset:28\n" \
  "s_waitcnt lgkmcnt(0)\n" \
  "s_barrier\n" \
  "ds_read_b128 v[116:119], v62 offset:16\n" \
  "s_waitcnt lgkmcnt(0)\n" \
  "s_barrier\n" \
  "v_or3_b32 v116, v116, v117, v118\n" \
  "v_or_b32 v116, v116, v119\n" \
  "v_cmp_ne_u32 vcc, 0, v116\n" \
  "s_nop 3\n" \
  "s_cbranch_vccz LQOK3_%=\n" \
  "s_add_u32 s67, s67, 1\n" \
  "s_cmp_gt_u32 s67, 20000\n" \
  "s_cbranch_scc1 LQOK3_%=\n" \
  "s_add_u32 s65, s65, 1\n" \
  "s_barrier\n" \
  "buffer_load_dwordx4 v1, s[36:39], s69 offen sc1 lds\n" \
  "buffer_load_dwordx4 v2, s[36:39], s69 offen offset:1024 sc1 lds\n" \
  "buffer_load_dwordx4 v3, s[36:39], s69 offen offset:2048 sc1 lds\n" \
  "buffer_load_dwordx4 v[156:159], v15, s[36:39], s69 offen sc1\n" \
  "buffer_load_dword v160, v16, s[36:39], s69 offen sc1\n" \
  "v_mov_b32 v136, 0\n" \
  "v_mov_b32 v137, 0\n" \
  "v_mov_b32 v138, 0\n" \
  "v_mov_b32 v139, 0\n" \
  "v_mov_b32 v140, 0\n" \
  "v_mov_b32 v141, 0\n" \
  "v_mov_b32 v142, 0\n" \
  "v_mov_b32 v143, 0\n" \
  "v_mov_b32 v144, 0\n" \
  "v_mov_b32 v145, 0\n" \
  "v_mov_b32 v146, 0\n" \
  "v_mov_b32 v147, 0\n" \
  "s_waitcnt vmcnt(0) lgkmcnt(0)\n" \
  "s_branch LQ3_%=\n" \
  "LPF3_%=:\n" \
  "v_pk_min_u16 v160, v160, -2 op_sel_hi:[1,0]\n" \
  "v_pk_min_u16 v161, v161, -2 op_sel_hi:[1,0]\n" \
  "v_mov_b32 v180, 0\n" \
  "v_pk_max_u16 v180, v180, v140\n" \
  "v_pk_max_u16 v180, v180, v141\n" \
  "v_pk_max_u16 v180, v180, v142\n" \
  "v_pk_max_u16 v180, v180, v143\n" \
  "v_pk_max_u16 v180, v180, v144\n" \
  "v_pk_max_u16 v180, v180, v145\n" \
  "v_pk_max_u16 v180, v180, v146\n" \
  "v_pk_max_u16 v180, v180, v147\n" \
  "v_pk_max_u16 v180, v180, v148\n" \
  "v_pk_max_u16 v180, v180, v149\n" \
  "v_pk_max_u16 v180, v180, v150\n" \
  "v_pk_max_u16 v180, v180, v151\n" \
  "v_pk_max_u16 v180, v180, v152\n" \
  "v_pk_max_u16 v180, v180, v153\n" \
  "v_pk_max_u16 v180, v180, v154\n" \
  "v_pk_max_u16 v180, v180, v155\n" \
  "v_lshrrev_b32 v181, 16, v180\n" \
  "v_and_b32 v180, 0xffff, v180\n" \
  "v_max_u32 v180, v180, v181\n" \
  "v_cmp_eq_u32 vcc, 0xffff, v180\n" \
  "s_nop 3\n" \
  "s_cbranch_vccz LPOK3_%=\n" \
  "s_add_u32 s67, s67, 1\n" \
  "s_cmp_gt_u32 s67, 20000\n" \
  "s_cbranch_scc1 LPOK3_%=\n" \
  "s_add_u32 s66, s66, 1\n" \
  "LPS3_%=:\n" \
  "s_add_u32 s50, s50, s47\n" \
  "LUR3_%=:\n" \
  "s_memtime s[80:81]\n" \
  "s_waitcnt lgkmcnt(0)\n" \
  "s_sub_i32 s76, s50, s80\n" \
  "s_cmp_gt_i32 s76, 60\n" \
  "s_cbranch_scc1 LUR3_%=\n" \
  "s_cmp_lt_i32 s76, s48\n" \
  "s_cbranch_scc1 LPS3_%=\n" \
  "buffer_load_dwordx4 v[140:143], v29, s[36:39], s69 offen sc1\n" \
  "buffer_load_dwordx4 v[144:147], v29, s[36:39], s69 offen offset:1024 sc1\n" \
  "buffer_load_dwordx4 v[148:151], v29, s[36:39], s69 offen offset:2048 sc1\n" \
  "buffer_load_dwordx4 v[152:155], v29, s[36:39], s69 offen offset:3072 sc1\n" \
  "s_branch LP3_%=\n" \
  "LEND3_%=:\n" \
  "buffer_store_dwordx2 v[152:153], v31, s[36:39], s62 offen sc1\n" \
  "buffer_store_dword v154, v32, s[36:39], s62 offen sc1\n" \
  "s_branch LDONE_%=\n" \
  "LDONE_%=:\n" \
  "s_waitcnt vmcnt(0) lgkmcnt(0)\n" \
  "s_barrier\n" \
  "s_mov_b32 s63, s61\n" \
  "s_cmp_eq_u32 s58, 2\n" \
  "s_cselect_b32 s66, 0, s66\n" \
  "v_mov_b32 v63, 64\n" \
  "buffer_store_dword v63, v39, s[36:39], 0 offen sc1\n"
#define PROJKV_CLOBBERS \
  "v12", "v13", "v14", "v15", "v16", "v17", "v18", "v19", "v20", "v21", "v22", "v23", "v24", "v25", "v26", "v27", "v28", "v29", "v30", "v31", "v32", \
  "v33", "v34", "v35", "v36", "v37", "v38", "v39", "v40", "v41", "v42", "v43", "v44", "v45", "v46", "v47", "v48", "v49", "v50", "v51", "v52", "v53", \
  "v54", "v55", "v56", "v57", "v58", "v59", "v60", "v61", "v62", "v63", "v64", "v65", "v66", "v67", "v68", "v69", "v70", "v71", "v72", "v73", "v74", \
  "v75", "v76", "v77", "v78", "v79", "v100", "v101", "v102", "v103", "v104", "v105", "v106", "v107", "v108", "v109", "v110", "v111", "v112", "v113", \
  "v114", "v115", "v116", "v117", "v118", "v119", "v120", "v121", "v122", "v123", "s68", "s69", "s70", "s71", "s72", "s73", "s74", "s75", "s76", "s77"
#define PROJKV_ASM \
  "s_waitcnt vmcnt(0)\n" \
  "s_barrier\n" \
  "ds_read_b128 v[32:35], v0 offset:0\n" \
  "ds_read_b128 v[36:39], v0 offset:20480\n" \
  "ds_read_b128 v[40:43], v0 offset:1024\n" \
  "ds_read_b128 v[44:47], v0 offset:21504\n" \
  "v_mov_b32 v12, -1\n" \
  "v_mov_b32 v13, -1\n" \
  "v_mov_b32 v14, -1\n" \
  "v_mov_b32 v15, -1\n" \
  "v_mov_b32 v11, 1\n" \
  "v_add_u32 v9, 40960, v0\n" \
  "v_add_u32 v10, 81920, v0\n" \
  "s_mul_i32 s68, s54, 40960\n" \
  "s_add_u32 s68, s68, 81920\n" \
  "s_lshr_b32 s69, s54, 4\n" \
  "s_lshl_b32 s69, s69, 13\n" \
  "s_lshl_b32 s74, s54, 9\n" \
  "s_add_u32 s74, s74, s69\n" \
  "s_waitcnt lgkmcnt(2)\n" \
  "v_mfma_f32_16x16x32_bf16 v[56:59], a[0:3], v[32:35], 0\n" \
  "ds_read_b128 v[48:51], v0 offset:2048\n" \
  "v_mfma_f32_16x16x32_bf16 v[68:71], a[0:3], v[36:39], 0\n" \
  "ds_read_b128 v[52:55], v0 offset:22528\n" \
  "v_mfma_f32_16x16x32_bf16 v[60:63], a[80:83], v[32:35], 0\n" \
  "v_mfma_f32_16x16x32_bf16 v[72:75], a[80:83], v[36:39], 0\n" \
  "v_mfma_f32_16x16x32_bf16 v[64:67], a[160:163], v[32:35], 0\n" \
  "v_mfma_f32_16x16x32_bf16 v[76:79], a[160:163], v[36:39], 0\n" \
  "s_waitcnt lgkmcnt(2)\n" \
  "v_mfma_f32_16x16x32_bf16 v[56:59], a[4:7], v[40:43], v[56:59]\n" \
  "ds_read_b128 v[32:35], v0 offset:3072\n" \
  "v_mfma_f32_16x16x32_bf16 v[68:71], a[4:7], v[44:47], v[68:71]\n" \
  "ds_read_b128 v[36:39], v0 offset:23552\n" \
  "v_mfma_f32_16x16x32_bf16 v[60:63], a[84:87], v[40:43], v[60:63]\n" \
  "v_mfma_f32_16x16x32_bf16 v[72:75], a[84:87], v[44:47], v[72:75]\n" \
  "v_mfma_f32_16x16x32_bf16 v[64:67], a[164:167], v[40:43], v[64:67]\n" \
  "v_mfma_f32_16x16x32_bf16 v[76:79], a[164:167], v[44:47], v[76:79]\n" \
  "s_waitcnt lgkmcnt(2)\n" \
  "v_mfma_f32_16x16x32_bf16 v[56:59], a[8:11], v[48:51], v[56:59]\n" \
  "ds_read_b128 v[40:43], v0 offset:4096\n" \
  "v_mfma_f32_16x16x32_bf16 v[68:71], a[8:11], v[52:55], v[68:71]\n" \
  "ds_read_b128 v[44:47], v0 offset:24576\n" \
  "v_mfma_f32_16x16x32_bf16 v[60:63], a[88:91], v[48:51], v[60:63]\n" \
  "v_mfma_f32_16x16x32_bf16 v[72:75], a[88:91], v[52:55], v[72:75]\n" \
  "v_mfma_f32_16x16x32_bf16 v[64:67], a[168:171], v[48:51], v[64:67]\n" \
  "v_mfma_f32_16x16x32_bf16 v[76:79], a[168:171], v[52:55], v[76:79]\n" \
  "s_waitcnt lgkmcnt(2)\n" \
  "v_mfma_f32_16x16x32_bf16 v[56:59], a[12:15], v[32:35], v[56:59]\n" \
  "ds_read_b128 v[48:51], v0 offset:5120\n" \
  "v_mfma_f32_16x16x32_bf16 v[68:71], a[12:15], v[36:39], v[68:71]\n" \
  "ds_read_b128 v[52:55], v0 offset:25600\n" \
  "v_mfma_f32_16x16x32_bf16 v[60:63], a[92:95], v[32:35], v[60:63]\n" \
  "v_mfma_f32_16x16x32_bf16 v[72:75], a[92:95], v[36:39], v[72:75]\n" \
  "v_mfma_f32_16x16x32_bf16 v[64:67], a[172:175], v[32:35], v[64:67]\n" \
  "v_mfma_f32_16x16x32_bf16 v[76:79], a[172:175], v[36:39], v[76:79]\n" \
  "s_waitcnt lgkmcnt(2)\n" \
  "v_mfma_f32_16x16x32_bf16 v[56:59], a[16:19], v[40:43], v[56:59]\n" \
  "ds_read_b128 v[32:35], v0 offset:6144\n" \
  "v_mfma_f32_16x16x32_bf16 v[68:71], a[16:19], v[44:47], v[68:71]\n" \
  "ds_read_b128 v[36:39], v0 offset:26624\n" \
  "v_mfma_f32_16x16x32_bf16 v[60:63], a[96:99], v[40:43], v[60:63]\n" \
  "v_mfma_f32_16x16x32_bf16 v[72:75], a[96:99], v[44:47], v[72:75]\n" \
  "v_mfma_f32_16x16x32_bf16 v[64:67], a[176:179], v[40:43], v[64:67]\n" \
  "v_mfma_f32_16x16x32_bf16 v[76:79], a[176:179], v[44:47], v[76:79]\n" \
  "s_waitcnt lgkmcnt(2)\n" \
  "v_mfma_f32_16x16x32_bf16 v[56:59], a[20:23], v[48:51], v[56:59]\n" \
  "ds_read_b128 v[40:43], v0 offset:7168\n" \
  "v_mfma_f32_16x16x32_bf16 v[68:71], a[20:23], v[52:55], v[68:71]\n" \
  "ds_read_b128 v[44:47], v0 offset:27648\n" \
  "v_mfma_f32_16x16x32_bf16 v[60:63], a[100:103], v[48:51], v[60:63]\n" \
  "v_mfma_f32_16x16x32_bf16 v[72:75], a[100:103], v[52:55], v[72:75]\n" \
  "v_mfma_f32_16x16x32_bf16 v[64:67], a[180:183], v[48:51], v[64:67]\n" \
  "v_mfma_f32_16x16x32_bf16 v[76:79], a[180:183], v[52:55], v[76:79]\n" \
  "s_waitcnt lgkmcnt(2)\n" \
  "v_mfma_f32_16x16x32_bf16 v[56:59], a[24:27], v[32:35], v[56:59]\n" \
  "ds_read_b128 v[48:51], v0 offset:8192\n" \
  "v_mfma_f32_16x16x32_bf16 v[68:71], a[24:27], v[36:39], v[68:71]\n" \
  "ds_read_b128 v[52:55], v0 offset:28672\n" \
  "v_mfma_f32_16x16x32_bf16 v[60:63], a[104:107], v[32:35], v[60:63]\n" \
  "v_mfma_f32_16x16x32_bf16 v[72:75], a[104:107], v[36:39], v[72:75]\n" \
  "v_mfma_f32_16x16x32_bf16 v[64:67], a[184:187], v[32:35], v[64:67]\n" \
  "v_mfma_f32_16x16x32_bf16 v[76:79], a[184:187], v[36:39], v[76:79]\n" \
  "s_waitcnt lgkmcnt(2)\n" \
  "v_mfma_f32_16x16x32_bf16 v[56:59], a[28:31], v[40:43], v[56:59]\n" \
  "ds_read_b128 v[32:35], v0 offset:9216\n" \
  "v_mfma_f32_16x16x32_bf16 v[68:71], a[28:31], v[44:47], v[68:71]\n" \
  "ds_read_b128 v[36:39], v0 offset:29696\n" \
  "v_mfma_f32_16x16x32_bf16 v[60:63], a[108:111], v[40:43], v[60:63]\n" \
  "v_mfma_f32_16x16x32_bf16 v[72:75], a[108:111], v[44:47], v[72:75]\n" \
  "v_mfma_f32_16x16x32_bf16 v[64:67], a[188:191], v[40:43], v[64:67]\n" \
  "v_mfma_f32_16x16x32_bf16 v[76:79], a[188:191], v[44:47], v[76:79]\n" \
  "s_waitcnt lgkmcnt(2)\n" \
  "v_mfma_f32_16x16x32_bf16 v[56:59], a[32:35], v[48:51], v[56:59]\n" \
  "ds_read_b128 v[40:43], v0 offset:10240\n" \
  "v_mfma_f32_16x16x32_bf16 v[68:71], a[32:35], v[52:55], v[68:71]\n" \
  "ds_read_b128 v[44:47], v0 offset:30720\n" \
  "v_mfma_f32_16x16x32_bf16 v[60:63], a[112:115], v[48:51], v[60:63]\n" \
  "s_add_u32 s72, s68, 4096\n" \
  "v_mfma_f32_16x16x32_bf16 v[72:75], a[112:115], v[52:55], v[72:75]\n" \
  "s_add_u32 s73, s68, 8192\n" \
  "v_mfma_f32_16x16x32_bf16 v[64:67], a[192:195], v[48:51], v[64:67]\n" \
  "s_add_u32 m0, s55, 81920\n" \
  "v_mfma_f32_16x16x32_bf16 v[76:79], a[192:195], v[52:55], v[76:79]\n" \
  "buffer_load_dwordx4 v1, s[36:39], s68 offen lds\n" \
  "s_waitcnt lgkmcnt(2)\n" \
  "v_mfma_f32_16x16x32_bf16 v[56:59], a[36:39], v[32:35], v[56:59]\n" \
  "ds_read_b128 v[48:51], v0 offset:11264\n" \
  "v_mfma_f32_16x16x32_bf16 v[68:71], a[36:39], v[36:39], v[68:71]\n" \
  "ds_read_b128 v[52:55], v0 offset:31744\n" \
  "v_mfma_f32_16x16x32_bf16 v[60:63], a[116:119], v[32:35], v[60:63]\n" \
  "buffer_load_dwordx4 v1, s[36:39], s68 offen offset:1024 lds\n" \
  "v_mfma_f32_16x16x32_bf16 v[72:75], a[116:119], v[36:39], v[72:75]\n" \
  "s_lshr_b64 s[76:77], s[52:53], s56\n" \
  "v_mfma_f32_16x16x32_bf16 v[64:67], a[196:199], v[32:35], v[64:67]\n" \
  "s_lshl_b32 s69, s56, 8\n" \
  "v_mfma_f32_16x16x32_bf16 v[76:79], a[196:199], v[36:39], v[76:79]\n" \
  "s_and_b32 s70, s76, 1\n" \
  "s_waitcnt lgkmcnt(2)\n" \
  "v_mfma_f32_16x16x32_bf16 v[56:59], a[40:43], v[40:43], v[56:59]\n" \
  "ds_read_b128 v[32:35], v0 offset:12288\n" \
  "v_mfma_f32_16x16x32_bf16 v[68:71], a[40:43], v[44:47], v[68:71]\n" \
  "ds_read_b128 v[36:39], v0 offset:32768\n" \
  "v_mfma_f32_16x16x32_bf16 v[60:63], a[120:123], v[40:43], v[60:63]\n" \
  "buffer_load_dwordx4 v1, s[36:39], s68 offen offset:2048 lds\n" \
  "v_mfma_f32_16x16x32_bf16 v[72:75], a[120:123], v[44:47], v[72:75]\n" \
  "s_bfe_u32 s71, s76, 0x10001\n" \
  "v_mfma_f32_16x16x32_bf16 v[64:67], a[200:203], v[40:43], v[64:67]\n" \
  "s_add_u32 s71, s71, s69\n" \
  "v_mfma_f32_16x16x32_bf16 v[76:79], a[200:203], v[44:47], v[76:79]\n" \
  "s_add_u32 s69, s69, s70\n" \
  "s_waitcnt lgkmcnt(2)\n" \
  "v_mfma_f32_16x16x32_bf16 v[56:59], a[44:47], v[48:51], v[56:59]\n" \
  "ds_read_b128 v[40:43], v0 offset:13312\n" \
  "v_mfma_f32_16x16x32_bf16 v[68:71], a[44:47], v[52:55], v[68:71]\n" \
  "ds_read_b128 v[44:47], v0 offset:33792\n" \
  "v_mfma_f32_16x16x32_bf16 v[60:63], a[124:127], v[48:51], v[60:63]\n" \
  "buffer_load_dwordx4 v1, s[36:39], s68 offen offset:3072 lds\n" \
  "v_mfma_f32_16x16x32_bf16 v[72:75], a[124:127], v[52:55], v[72:75]\n" \
  "s_add_u32 s71, s71, 0x100\n" \
  "v_mfma_f32_16x16x32_bf16 v[64:67], a[204:207], v[48:51], v[64:67]\n" \
  "s_lshl_b32 s69, s69, 13\n" \
  "v_mfma_f32_16x16x32_bf16 v[76:79], a[204:207], v[52:55], v[76:79]\n" \
  "s_lshl_b32 s71, s71, 13\n" \
  "s_waitcnt lgkmcnt(2)\n" \
  "v_mfma_f32_16x16x32_bf16 v[56:59], a[48:51], v[32:35], v[56:59]\n" \
  "ds_read_b128 v[48:51], v0 offset:14336\n" \
  "v_mfma_f32_16x16x32_bf16 v[68:71], a[48:51], v[36:39], v[68:71]\n" \
  "ds_read_b128 v[52:55], v0 offset:34816\n" \
  "v_mfma_f32_16x16x32_bf16 v[60:63], a[128:131], v[32:35], v[60:63]\n" \
  "s_add_u32 m0, s55, 86016\n" \
  "v_mfma_f32_16x16x32_bf16 v[72:75], a[128:131], v[36:39], v[72:75]\n" \
  "buffer_load_dwordx4 v1, s[36:39], s72 offen lds\n" \
  "v_mfma_f32_16x16x32_bf16 v[64:67], a[208:211], v[32:35], v[64:67]\n" \
  "v_mfma_f32_16x16x32_bf16 v[76:79], a[208:211], v[36:39], v[76:79]\n" \
  "s_waitcnt lgkmcnt(2)\n" \
  "v_mfma_f32_16x16x32_bf16 v[56:59], a[52:55], v[40:43], v[56:59]\n" \
  "ds_read_b128 v[32:35], v0 offset:15360\n" \
  "v_mfma_f32_16x16x32_bf16 v[68:71], a[52:55], v[44:47], v[68:71]\n" \
  "ds_read_b128 v[36:39], v0 offset:35840\n" \
  "v_mfma_f32_16x16x32_bf16 v[60:63], a[132:135], v[40:43], v[60:63]\n" \
  "buffer_load_dwordx4 v1, s[36:39], s72 offen offset:1024 lds\n" \
  "v_mfma_f32_16x16x32_bf16 v[72:75], a[132:135], v[44:47], v[72:75]\n" \
  "s_cmp_eq_u32 s57, 0\n" \
  "v_mfma_f32_16x16x32_bf16 v[64:67], a[212:215], v[40:43], v[64:67]\n" \
  "s_cselect_b32 s70, s69, s71\n" \
  "v_mfma_f32_16x16x32_bf16 v[76:79], a[212:215], v[44:47], v[76:79]\n" \
  "s_xor_b32 s70, s70, 0x2000\n" \
  "s_waitcnt lgkmcnt(2)\n" \
  "v_mfma_f32_16x16x32_bf16 v[56:59], a[56:59], v[48:51], v[56:59]\n" \
  "ds_read_b128 v[40:43], v0 offset:16384\n" \
  "v_mfma_f32_16x16x32_bf16 v[68:71], a[56:59], v[52:55], v[68:71]\n" \
  "ds_read_b128 v[44:47], v0 offset:36864\n" \
  "v_mfma_f32_16x16x32_bf16 v[60:63], a[136:139], v[48:51], v[60:63]\n" \
  "buffer_load_dwordx4 v1, s[36:39], s72 offen offset:2048 lds\n" \
  "v_mfma_f32_16x16x32_bf16 v[72:75], a[136:139], v[52:55], v[72:75]\n" \
  "s_add_u32 s56, s56, 2\n" \
  "v_mfma_f32_16x16x32_bf16 v[64:67], a[216:219], v[48:51], v[64:67]\n" \
  "v_mfma_f32_16x16x32_bf16 v[76:79], a[216:219], v[52:55], v[76:79]\n" \
  "s_waitcnt lgkmcnt(2)\n" \
  "v_mfma_f32_16x16x32_bf16 v[56:59], a[60:63], v[32:35], v[56:59]\n" \
  "ds_read_b128 v[48:51], v0 offset:17408\n" \
  "v_mfma_f32_16x16x32_bf16 v[68:71], a[60:63], v[36:39], v[68:71]\n" \
  "ds_read_b128 v[52:55], v0 offset:37888\n" \
  "v_mfma_f32_16x16x32_bf16 v[60:63], a[140:143], v[32:35], v[60:63]\n" \
  "buffer_load_dwordx4 v1, s[36:39], s72 offen offset:3072 lds\n" \
  "v_mfma_f32_16x16x32_bf16 v[72:75], a[140:143], v[36:39], v[72:75]\n" \
  "v_mfma_f32_16x16x32_bf16 v[64:67], a[220:223], v[32:35], v[64:67]\n" \
  "v_mfma_f32_16x16x32_bf16 v[76:79], a[220:223], v[36:39], v[76:79]\n" \
  "s_waitcnt lgkmcnt(2)\n" \
  "v_mfma_f32_16x16x32_bf16 v[56:59], a[64:67], v[40:43], v[56:59]\n" \
  "ds_read_b128 v[32:35], v0 offset:18432\n" \
  "v_mfma_f32_16x16x32_bf16 v[68:71], a[64:67], v[44:47], v[68:71]\n" \
  "ds_read_b128 v[36:39], v0 offset:38912\n" \
  "v_mfma_f32_16x16x32_bf16 v[60:63], a[144:147], v[40:43], v[60:63]\n" \
  "s_add_u32 m0, s55, 90112\n" \
  "v_mfma_f32_16x16x32_bf16 v[72:75], a[144:147], v[44:47], v[72:75]\n" \
  "buffer_load_dwordx4 v1, s[36:39], s73 offen lds\n" \
  "v_mfma_f32_16x16x32_bf16 v[64:67], a[224:227], v[40:43], v[64:67]\n" \
  "v_mfma_f32_16x16x32_bf16 v[76:79], a[224:227], v[44:47], v[76:79]\n" \
  "s_waitcnt lgkmcnt(2)\n" \
  "v_mfma_f32_16x16x32_bf16 v[56:59], a[68:71], v[48:51], v[56:59]\n" \
  "ds_read_b128 v[40:43], v0 offset:19456\n" \
  "v_mfma_f32_16x16x32_bf16 v[68:71], a[68:71], v[52:55], v[68:71]\n" \
  "ds_read_b128 v[44:47], v0 offset:39936\n" \
  "v_mfma_f32_16x16x32_bf16 v[60:63], a[148:151], v[48:51], v[60:63]\n" \
  "buffer_load_dwordx4 v1, s[36:39], s73 offen offset:1024 lds\n" \
  "v_mfma_f32_16x16x32_bf16 v[72:75], a[148:151], v[52:55], v[72:75]\n" \
  "s_add_u32 s68, s68, 40960\n" \
  "v_mfma_f32_16x16x32_bf16 v[64:67], a[228:231], v[48:51], v[64:67]\n" \
  "v_mfma_f32_16x16x32_bf16 v[76:79], a[228:231], v[52:55], v[76:79]\n" \
  "s_waitcnt vmcnt(10) lgkmcnt(0)\n" \
  "s_barrier\n" \
  "v_mfma_f32_16x16x32_bf16 v[56:59], a[72:75], v[32:35], v[56:59]\n" \
  "ds_read_b128 v[48:51], v9 offset:0\n" \
  "v_mfma_f32_16x16x32_bf16 v[68:71], a[72:75], v[36:39], v[68:71]\n" \
  "ds_read_b128 v[52:55], v9 offset:20480\n" \
  "v_mfma_f32_16x16x32_bf16 v[60:63], a[152:155], v[32:35], v[60:63]\n" \
  "v_mfma_f32_16x16x32_bf16 v[72:75], a[152:155], v[36:39], v[72:75]\n" \
  "v_mfma_f32_16x16x32_bf16 v[64:67], a[232:235], v[32:35], v[64:67]\n" \
  "v_mfma_f32_16x16x32_bf16 v[76:79], a[232:235], v[36:39], v[76:79]\n" \
  "v_mfma_f32_16x16x32_bf16 v[56:59], a[76:79], v[40:43], v[56:59]\n" \
  "ds_read_b128 v[32:35], v9 offset:1024\n" \
  "v_mfma_f32_16x16x32_bf16 v[68:71], a[76:79], v[44:47], v[68:71]\n" \
  "ds_read_b128 v[36:39], v9 offset:21504\n" \
  "v_mfma_f32_16x16x32_bf16 v[60:63], a[156:159], v[40:43], v[60:63]\n" \
  "s_cmp_eq_u32 s54, 60\n" \
  "v_mfma_f32_16x16x32_bf16 v[72:75], a[156:159], v[44:47], v[72:75]\n" \
  "v_mfma_f32_16x16x32_bf16 v[64:67], a[236:239], v[40:43], v[64:67]\n" \
  "v_mfma_f32_16x16x32_bf16 v[76:79], a[236:239], v[44:47], v[76:79]\n" \
  "s_waitcnt lgkmcnt(2)\n" \
  "v_mfma_f32_16x16x32_bf16 v[100:103], a[0:3], v[48:51], 0\n" \
  "ds_read_b128 v[40:43], v9 offset:2048\n" \
  "v_mfma_f32_16x16x32_bf16 v[112:115], a[0:3], v[52:55], 0\n" \
  "ds_read_b128 v[44:47], v9 offset:22528\n" \
  "v_mfma_f32_16x16x32_bf16 v[104:107], a[80:83], v[48:51], 0\n" \
  "v_mfma_f32_16x16x32_bf16 v[116:119], a[80:83], v[52:55], 0\n" \
  "v_mfma_f32_16x16x32_bf16 v[108:111], a[160:163], v[48:51], 0\n" \
  "v_mfma_f32_16x16x32_bf16 v[120:123], a[160:163], v[52:55], 0\n" \
  "s_waitcnt lgkmcnt(2)\n" \
  "v_mfma_f32_16x16x32_bf16 v[100:103], a[4:7], v[32:35], v[100:103]\n" \
  "ds_read_b128 v[48:51], v9 offset:3072\n" \
  "v_mfma_f32_16x16x32_bf16 v[112:115], a[4:7], v[36:39], v[112:115]\n" \
  "ds_read_b128 v[52:55], v9 offset:23552\n" \
  "v_mfma_f32_16x16x32_bf16 v[104:107], a[84:87], v[32:35], v[104:107]\n" \
  "buffer_store_dwordx4 v[12:15], v2, s[40:43], s69 offen sc1\n" \
  "v_mfma_f32_16x16x32_bf16 v[116:119], a[84:87], v[36:39], v[116:119]\n" \
  "v_cvt_pk_bf16_f32 v16, v56, v57\n" \
  "v_mfma_f32_16x16x32_bf16 v[108:111], a[164:167], v[32:35], v[108:111]\n" \
  "v_cvt_pk_bf16_f32 v17, v58, v59\n" \
  "v_mfma_f32_16x16x32_bf16 v[120:123], a[164:167], v[36:39], v[120:123]\n" \
  "v_cvt_pk_bf16_f32 v18, v60, v61\n" \
  "s_waitcnt lgkmcnt(2)\n" \
  "v_mfma_f32_16x16x32_bf16 v[100:103], a[8:11], v[40:43], v[100:103]\n" \
  "ds_read_b128 v[32:35], v9 offset:4096\n" \
  "v_mfma_f32_16x16x32_bf16 v[112:115], a[8:11], v[44:47], v[112:115]\n" \
  "ds_read_b128 v[36:39], v9 offset:24576\n" \
  "v_mfma_f32_16x16x32_bf16 v[104:107], a[88:91], v[40:43], v[104:107]\n" \
  "v_cvt_pk_bf16_f32 v19, v62, v63\n" \
  "v_mfma_f32_16x16x32_bf16 v[116:119], a[88:91], v[44:47], v[116:119]\n" \
  "buffer_store_dwordx4 v[16:19], v3, s[40:43], s71 offen offset:0 sc1\n" \
  "v_mfma_f32_16x16x32_bf16 v[108:111], a[168:171], v[40:43], v[108:111]\n" \
  "v_cvt_pk_bf16_f32 v20, v68, v69\n" \
  "v_mfma_f32_16x16x32_bf16 v[120:123], a[168:171], v[44:47], v[120:123]\n" \
  "v_cvt_pk_bf16_f32 v21, v70, v71\n" \
  "s_waitcnt lgkmcnt(2)\n" \
  "v_mfma_f32_16x16x32_bf16 v[100:103], a[12:15], v[48:51], v[100:103]\n" \
  "ds_read_b128 v[40:43], v9 offset:5120\n" \
  "v_mfma_f32_16x16x32_bf16 v[112:115], a[12:15], v[52:55], v[112:115]\n" \
  "ds_read_b128 v[44:47], v9 offset:25600\n" \
  "v_mfma_f32_16x16x32_bf16 v[104:107], a[92:95], v[48:51], v[104:107]\n" \
  "v_cvt_pk_bf16_f32 v22, v72, v73\n" \
  "v_mfma_f32_16x16x32_bf16 v[116:119], a[92:95], v[52:55], v[116:119]\n" \
  "v_cvt_pk_bf16_f32 v23, v74, v75\n" \
  "v_mfma_f32_16x16x32_bf16 v[108:111], a[172:175], v[48:51], v[108:111]\n" \
  "buffer_store_dwordx4 v[20:23], v3, s[40:43], s71 offen offset:1024 sc1\n" \
  "v_mfma_f32_16x16x32_bf16 v[120:123], a[172:175], v[52:55], v[120:123]\n" \
  "v_cvt_pk_bf16_f32 v24, v64, v65\n" \
  "s_waitcnt lgkmcnt(2)\n" \
  "v_mfma_f32_16x16x32_bf16 v[100:103], a[16:19], v[32:35], v[100:103]\n" \
  "ds_read_b128 v[48:51], v9 offset:6144\n" \
  "v_mfma_f32_16x16x32_bf16 v[112:115], a[16:19], v[36:39], v[112:115]\n" \
  "ds_read_b128 v[52:55], v9 offset:26624\n" \
  "v_mfma_f32_16x16x32_bf16 v[104:107], a[96:99], v[32:35], v[104:107]\n" \
  "v_cvt_pk_bf16_f32 v25, v66, v67\n" \
  "v_mfma_f32_16x16x32_bf16 v[116:119], a[96:99], v[36:39], v[116:119]\n" \
  "v_cvt_pk_bf16_f32 v26, v76, v77\n" \
  "v_mfma_f32_16x16x32_bf16 v[108:111], a[176:179], v[32:35], v[108:111]\n" \
  "v_cvt_pk_bf16_f32 v27, v78, v79\n" \
  "v_mfma_f32_16x16x32_bf16 v[120:123], a[176:179], v[36:39], v[120:123]\n" \
  "buffer_store_dwordx4 v[24:27], v3, s[40:43], s71 offen offset:2048 sc1\n" \
  "s_waitcnt lgkmcnt(2)\n" \
  "v_mfma_f32_16x16x32_bf16 v[100:103], a[20:23], v[40:43], v[100:103]\n" \
  "ds_read_b128 v[32:35], v9 offset:7168\n" \
  "v_mfma_f32_16x16x32_bf16 v[112:115], a[20:23], v[44:47], v[112:115]\n" \
  "ds_read_b128 v[36:39], v9 offset:27648\n" \
  "v_mfma_f32_16x16x32_bf16 v[104:107], a[100:103], v[40:43], v[104:107]\n" \
  "buffer_store_dwordx4 v[16:19], v6, s[40:43], s70 offen sc1\n" \
  "v_mfma_f32_16x16x32_bf16 v[116:119], a[100:103], v[44:47], v[116:119]\n" \
  "v_mfma_f32_16x16x32_bf16 v[108:111], a[180:183], v[40:43], v[108:111]\n" \
  "v_mfma_f32_16x16x32_bf16 v[120:123], a[180:183], v[44:47], v[120:123]\n" \
  "s_waitcnt lgkmcnt(2)\n" \
  "v_mfma_f32_16x16x32_bf16 v[100:103], a[24:27], v[48:51], v[100:103]\n" \
  "ds_read_b128 v[40:43], v9 offset:8192\n" \
  "v_mfma_f32_16x16x32_bf16 v[112:115], a[24:27], v[52:55], v[112:115]\n" \
  "ds_read_b128 v[44:47], v9 offset:28672\n" \
  "v_mfma_f32_16x16x32_bf16 v[104:107], a[104:107], v[48:51], v[104:107]\n" \
  "buffer_store_dwordx4 v[20:23], v6, s[40:43], s70 offen offset:1024 sc1\n" \
  "v_mfma_f32_16x16x32_bf16 v[116:119], a[104:107], v[52:55], v[116:119]\n" \
  "v_mfma_f32_16x16x32_bf16 v[108:111], a[184:187], v[48:51], v[108:111]\n" \
  "v_mfma_f32_16x16x32_bf16 v[120:123], a[184:187], v[52:55], v[120:123]\n" \
  "s_waitcnt lgkmcnt(2)\n" \
  "v_mfma_f32_16x16x32_bf16 v[100:103], a[28:31], v[32:35], v[100:103]\n" \
  "ds_read_b128 v[48:51], v9 offset:9216\n" \
  "v_mfma_f32_16x16x32_bf16 v[112:115], a[28:31], v[36:39], v[112:115]\n" \
  "ds_read_b128 v[52:55], v9 offset:29696\n" \
  "v_mfma_f32_16x16x32_bf16 v[104:107], a[108:111], v[32:35], v[104:107]\n" \
  "buffer_store_dwordx4 v[24:27], v7, s[40:43], s70 offen sc1\n" \
  "v_mfma_f32_16x16x32_bf16 v[116:119], a[108:111], v[36:39], v[116:119]\n" \
  "v_mfma_f32_16x16x32_bf16 v[108:111], a[188:191], v[32:35], v[108:111]\n" \
  "v_mfma_f32_16x16x32_bf16 v[120:123], a[188:191], v[36:39], v[120:123]\n" \
  "s_waitcnt lgkmcnt(2)\n" \
  "v_mfma_f32_16x16x32_bf16 v[100:103], a[32:35], v[40:43], v[100:103]\n" \
  "ds_read_b128 v[32:35], v9 offset:10240\n" \
  "v_mfma_f32_16x16x32_bf16 v[112:115], a[32:35], v[44:47], v[112:115]\n" \
  "ds_read_b128 v[36:39], v9 offset:30720\n" \
  "v_mfma_f32_16x16x32_bf16 v[104:107], a[112:115], v[40:43], v[104:107]\n" \
  "s_add_u32 s72, s68, 4096\n" \
  "v_mfma_f32_16x16x32_bf16 v[116:119], a[112:115], v[44:47], v[116:119]\n" \
  "s_add_u32 s73, s68, 8192\n" \
  "v_mfma_f32_16x16x32_bf16 v[108:111], a[192:195], v[40:43], v[108:111]\n" \
  "s_add_u32 m0, s55, 0\n" \
  "v_mfma_f32_16x16x32_bf16 v[120:123], a[192:195], v[44:47], v[120:123]\n" \
  "buffer_load_dwordx4 v1, s[36:39], s68 offen lds\n" \
  "s_waitcnt lgkmcnt(2)\n" \
  "v_mfma_f32_16x16x32_bf16 v[100:103], a[36:39], v[48:51], v[100:103]\n" \
  "ds_read_b128 v[40:43], v9 offset:11264\n" \
  "v_mfma_f32_16x16x32_bf16 v[112:115], a[36:39], v[52:55], v[112:115]\n" \
  "ds_read_b128 v[44:47], v9 offset:31744\n" \
  "v_mfma_f32_16x16x32_bf16 v[104:107], a[116:119], v[48:51], v[104:107]\n" \
  "buffer_load_dwordx4 v1, s[36:39], s68 offen offset:1024 lds\n" \
  "v_mfma_f32_16x16x32_bf16 v[116:119], a[116:119], v[52:55], v[116:119]\n" \
  "s_lshr_b64 s[76:77], s[52:53], s56\n" \
  "v_mfma_f32_16x16x32_bf16 v[108:111], a[196:199], v[48:51], v[108:111]\n" \
  "s_lshl_b32 s69, s56, 8\n" \
  "v_mfma_f32_16x16x32_bf16 v[120:123], a[196:199], v[52:55], v[120:123]\n" \
  "s_and_b32 s70, s76, 1\n" \
  "s_waitcnt lgkmcnt(2)\n" \
  "v_mfma_f32_16x16x32_bf16 v[100:103], a[40:43], v[32:35], v[100:103]\n" \
  "ds_read_b128 v[48:51], v9 offset:12288\n" \
  "v_mfma_f32_16x16x32_bf16 v[112:115], a[40:43], v[36:39], v[112:115]\n" \
  "ds_read_b128 v[52:55], v9 offset:32768\n" \
  "v_mfma_f32_16x16x32_bf16 v[104:107], a[120:123], v[32:35], v[104:107]\n" \
  "buffer_load_dwordx4 v1, s[36:39], s68 offen offset:2048 lds\n" \
  "v_mfma_f32_16x16x32_bf16 v[116:119], a[120:123], v[36:39], v[116:119]\n" \
  "s_bfe_u32 s71, s76, 0x10001\n" \
  "v_mfma_f32_16x16x32_bf16 v[108:111], a[200:203], v[32:35], v[108:111]\n" \
  "s_add_u32 s71, s71, s69\n" \
  "v_mfma_f32_16x16x32_bf16 v[120:123], a[200:203], v[36:39], v[120:123]\n" \
  "s_add_u32 s69, s69, s70\n" \
  "s_waitcnt lgkmcnt(2)\n" \
  "v_mfma_f32_16x16x32_bf16 v[100:103], a[44:47], v[40:43], v[100:103]\n" \
  "ds_read_b128 v[32:35], v9 offset:13312\n" \
  "v_mfma_f32_16x16x32_bf16 v[112:115], a[44:47], v[44:47], v[112:115]\n" \
  "ds_read_b128 v[36:39], v9 offset:33792\n" \
  "v_mfma_f32_16x16x32_bf16 v[104:107], a[124:127], v[40:43], v[104:107]\n" \
  "buffer_load_dwordx4 v1, s[36:39], s68 offen offset:3072 lds\n" \
  "v_mfma_f32_16x16x32_bf16 v[116:119], a[124:127], v[44:47], v[116:119]\n" \
  "s_add_u32 s71, s71, 0x100\n" \
  "v_mfma_f32_16x16x32_bf16 v[108:111], a[204:207], v[40:43], v[108:111]\n" \
  "s_lshl_b32 s69, s69, 13\n" \
  "v_mfma_f32_16x16x32_bf16 v[120:123], a[204:207], v[44:47], v[120:123]\n" \
  "s_lshl_b32 s71, s71, 13\n" \
  "s_waitcnt lgkmcnt(2)\n" \
  "v_mfma_f32_16x16x32_bf16 v[100:103], a[48:51], v[48:51], v[100:103]\n" \
  "ds_read_b128 v[40:43], v9 offset:14336\n" \
  "v_mfma_f32_16x16x32_bf16 v[112:115], a[48:51], v[52:55], v[112:115]\n" \
  "ds_read_b128 v[44:47], v9 offset:34816\n" \
  "v_mfma_f32_16x16x32_bf16 v[104:107], a[128:131], v[48:51], v[104:107]\n" \
  "s_add_u32 m0, s55, 4096\n" \
  "v_mfma_f32_16x16x32_bf16 v[116:119], a[128:131], v[52:55], v[116:119]\n" \
  "buffer_load_dwordx4 v1, s[36:39], s72 offen lds\n" \
  "v_mfma_f32_16x16x32_bf16 v[108:111], a[208:211], v[48:51], v[108:111]\n" \
  "v_mfma_f32_16x16x32_bf16 v[120:123], a[208:211], v[52:55], v[120:123]\n" \
  "s_waitcnt lgkmcnt(2)\n" \
  "v_mfma_f32_16x16x32_bf16 v[100:103], a[52:55], v[32:35], v[100:103]\n" \
  "ds_read_b128 v[48:51], v9 offset:15360\n" \
  "v_mfma_f32_16x16x32_bf16 v[112:115], a[52:55], v[36:39], v[112:115]\n" \
  "ds_read_b128 v[52:55], v9 offset:35840\n" \
  "v_mfma_f32_16x16x32_bf16 v[104:107], a[132:135], v[32:35], v[104:107]\n" \
  "buffer_load_dwordx4 v1, s[36:39], s72 offen offset:1024 lds\n" \
  "v_mfma_f32_16x16x32_bf16 v[116:119], a[132:135], v[36:39], v[116:119]\n" \
  "s_cmp_eq_u32 s57, 0\n" \
  "v_mfma_f32_16x16x32_bf16 v[108:111], a[212:215], v[32:35], v[108:111]\n" \
  "s_cselect_b32 s70, s69, s71\n" \
  "v_mfma_f32_16x16x32_bf16 v[120:123], a[212:215], v[36:39], v[120:123]\n" \
  "s_xor_b32 s70, s70, 0x2000\n" \
  "s_waitcnt lgkmcnt(2)\n" \
  "v_mfma_f32_16x16x32_bf16 v[100:103], a[56:59], v[40:43], v[100:103]\n" \
  "ds_read_b128 v[32:35], v9 offset:16384\n" \
  "v_mfma_f32_16x16x32_bf16 v[112:115], a[56:59], v[44:47], v[112:115]\n" \
  "ds_read_b128 v[36:39], v9 offset:36864\n" \
  "v_mfma_f32_16x16x32_bf16 v[104:107], a[136:139], v[40:43], v[104:107]\n" \
  "buffer_load_dwordx4 v1, s[36:39], s72 offen offset:2048 lds\n" \
  "v_mfma_f32_16x16x32_bf16 v[116:119], a[136:139], v[44:47], v[116:119]\n" \
  "s_add_u32 s56, s56, 2\n" \
  "v_mfma_f32_16x16x32_bf16 v[108:111], a[216:219], v[40:43], v[108:111]\n" \
  "v_mfma_f32_16x16x32_bf16 v[120:123], a[216:219], v[44:47], v[120:123]\n" \
  "s_waitcnt lgkmcnt(2)\n" \
  "v_mfma_f32_16x16x32_bf16 v[100:103], a[60:63], v[48:51], v[100:103]\n" \
  "ds_read_b128 v[40:43], v9 offset:17408\n" \
  "v_mfma_f32_16x16x32_bf16 v[112:115], a[60:63], v[52:55], v[112:115]\n" \
  "ds_read_b128 v[44:47], v9 offset:37888\n" \
  "v_mfma_f32_16x16x32_bf16 v[104:107], a[140:143], v[48:51], v[104:107]\n" \
  "buffer_load_dwordx4 v1, s[36:39], s72 offen offset:3072 lds\n" \
  "v_mfma_f32_16x16x32_bf16 v[116:119], a[140:143], v[52:55], v[116:119]\n" \
  "v_mfma_f32_16x16x32_bf16 v[108:111], a[220:223], v[48:51], v[108:111]\n" \
  "v_mfma_f32_16x16x32_bf16 v[120:123], a[220:223], v[52:55], v[120:123]\n" \
  "s_waitcnt lgkmcnt(2)\n" \
  "v_mfma_f32_16x16x32_bf16 v[100:103], a[64:67], v[32:35], v[100:103]\n" \
  "ds_read_b128 v[48:51], v9 offset:18432\n" \
  "v_mfma_f32_16x16x32_bf16 v[112:115], a[64:67], v[36:39], v[112:115]\n" \
  "ds_read_b128 v[52:55], v9 offset:38912\n" \
  "v_mfma_f32_16x16x32_bf16 v[104:107], a[144:147], v[32:35], v[104:107]\n" \
  "s_add_u32 m0, s55, 8192\n" \
  "v_mfma_f32_16x16x32_bf16 v[116:119], a[144:147], v[36:39], v[116:119]\n" \
  "buffer_load_dwordx4 v1, s[36:39], s73 offen lds\n" \
  "v_mfma_f32_16x16x32_bf16 v[108:111], a[224:227], v[32:35], v[108:111]\n" \
  "v_mfma_f32_16x16x32_bf16 v[120:123], a[224:227], v[36:39], v[120:123]\n" \
  "s_waitcnt lgkmcnt(2)\n" \
  "v_mfma_f32_16x16x32_bf16 v[100:103], a[68:71], v[40:43], v[100:103]\n" \
  "ds_read_b128 v[32:35], v9 offset:19456\n" \
  "v_mfma_f32_16x16x32_bf16 v[112:115], a[68:71], v[44:47], v[112:115]\n" \
  "ds_read_b128 v[36:39], v9 offset:39936\n" \
  "v_mfma_f32_16x16x32_bf16 v[104:107], a[148:151], v[40:43], v[104:107]\n" \
  "buffer_load_dwordx4 v1, s[36:39], s73 offen offset:1024 lds\n" \
  "v_mfma_f32_16x16x32_bf16 v[116:119], a[148:151], v[44:47], v[116:119]\n" \
  "s_add_u32 s68, s68, 40960\n" \
  "v_mfma_f32_16x16x32_bf16 v[108:111], a[228:231], v[40:43], v[108:111]\n" \
  "v_mfma_f32_16x16x32_bf16 v[120:123], a[228:231], v[44:47], v[120:123]\n" \
  "s_waitcnt vmcnt(10) lgkmcnt(0)\n" \
  "s_barrier\n" \
  "v_mfma_f32_16x16x32_bf16 v[100:103], a[72:75], v[48:51], v[100:103]\n" \
  "ds_read_b128 v[40:43], v10 offset:0\n" \
  "v_mfma_f32_16x16x32_bf16 v[112:115], a[72:75], v[52:55], v[112:115]\n" \
  "ds_read_b128 v[44:47], v10 offset:20480\n" \
  "v_mfma_f32_16x16x32_bf16 v[104:107], a[152:155], v[48:51], v[104:107]\n" \
  "v_mfma_f32_16x16x32_bf16 v[116:119], a[152:155], v[52:55], v[116:119]\n" \
  "v_mfma_f32_16x16x32_bf16 v[108:111], a[232:235], v[48:51], v[108:111]\n" \
  "v_mfma_f32_16x16x32_bf16 v[120:123], a[232:235], v[52:55], v[120:123]\n" \
  "v_mfma_f32_16x16x32_bf16 v[100:103], a[76:79], v[32:35], v[100:103]\n" \
  "ds_read_b128 v[48:51], v10 offset:1024\n" \
  "v_mfma_f32_16x16x32_bf16 v[112:115], a[76:79], v[36:39], v[112:115]\n" \
  "ds_read_b128 v[52:55], v10 offset:21504\n" \
  "v_mfma_f32_16x16x32_bf16 v[104:107], a[156:159], v[32:35], v[104:107]\n" \
  "s_cmp_eq_u32 s54, 60\n" \
  "v_mfma_f32_16x16x32_bf16 v[116:119], a[156:159], v[36:39], v[116:119]\n" \
  "v_mfma_f32_16x16x32_bf16 v[108:111], a[236:239], v[32:35], v[108:111]\n" \
  "v_mfma_f32_16x16x32_bf16 v[120:123], a[236:239], v[36:39], v[120:123]\n" \
  "s_waitcnt lgkmcnt(2)\n" \
  "v_mfma_f32_16x16x32_bf16 v[56:59], a[0:3], v[40:43], 0\n" \
  "ds_read_b128 v[32:35], v10 offset:2048\n" \
  "v_mfma_f32_16x16x32_bf16 v[68:71], a[0:3], v[44:47], 0\n" \
  "ds_read_b128 v[36:39], v10 offset:22528\n" \
  "v_mfma_f32_16x16x32_bf16 v[60:63], a[80:83], v[40:43], 0\n" \
  "buffer_atomic_add v11, v8, s[40:43], s74 offen\n" \
  "v_mfma_f32_16x16x32_bf16 v[72:75], a[80:83], v[44:47], 0\n" \
  "s_add_u32 s74, s74, 0x2200\n" \
  "v_mfma_f32_16x16x32_bf16 v[64:67], a[160:163], v[40:43], 0\n" \
  "s_and_b32 s74, s74, 0xffffdfff\n" \
  "v_mfma_f32_16x16x32_bf16 v[76:79], a[160:163], v[44:47], 0\n" \
  "s_waitcnt lgkmcnt(2)\n" \
  "v_mfma_f32_16x16x32_bf16 v[56:59], a[4:7], v[48:51], v[56:59]\n" \
  "ds_read_b128 v[40:43], v10 offset:3072\n" \
  "v_mfma_f32_16x16x32_bf16 v[68:71], a[4:7], v[52:55], v[68:71]\n" \
  "ds_read_b128 v[44:47], v10 offset:23552\n" \
  "v_mfma_f32_16x16x32_bf16 v[60:63], a[84:87], v[48:51], v[60:63]\n" \
  "buffer_store_dwordx4 v[12:15], v2, s[40:43], s69 offen sc1\n" \
  "v_mfma_f32_16x16x32_bf16 v[72:75], a[84:87], v[52:55], v[72:75]\n" \
  "v_cvt_pk_bf16_f32 v16, v100, v101\n" \
  "v_mfma_f32_16x16x32_bf16 v[64:67], a[164:167], v[48:51], v[64:67]\n" \
  "v_cvt_pk_bf16_f32 v17, v102, v103\n" \
  "v_mfma_f32_16x16x32_bf16 v[76:79], a[164:167], v[52:55], v[76:79]\n" \
  "v_cvt_pk_bf16_f32 v18, v104, v105\n" \
  "s_waitcnt lgkmcnt(2)\n" \
  "v_mfma_f32_16x16x32_bf16 v[56:59], a[8:11], v[32:35], v[56:59]\n" \
  "ds_read_b128 v[48:51], v10 offset:4096\n" \
  "v_mfma_f32_16x16x32_bf16 v[68:71], a[8:11], v[36:39], v[68:71]\n" \
  "ds_read_b128 v[52:55], v10 offset:24576\n" \
  "v_mfma_f32_16x16x32_bf16 v[60:63], a[88:91], v[32:35], v[60:63]\n" \
  "v_cvt_pk_bf16_f32 v19, v106, v107\n" \
  "v_mfma_f32_16x16x32_bf16 v[72:75], a[88:91], v[36:39], v[72:75]\n" \
  "buffer_store_dwordx4 v[16:19], v3, s[40:43], s71 offen offset:0 sc1\n" \
  "v_mfma_f32_16x16x32_bf16 v[64:67], a[168:171], v[32:35], v[64:67]\n" \
  "v_cvt_pk_bf16_f32 v20, v112, v113\n" \
  "v_mfma_f32_16x16x32_bf16 v[76:79], a[168:171], v[36:39], v[76:79]\n" \
  "v_cvt_pk_bf16_f32 v21, v114, v115\n" \
  "s_waitcnt lgkmcnt(2)\n" \
  "v_mfma_f32_16x16x32_bf16 v[56:59], a[12:15], v[40:43], v[56:59]\n" \
  "ds_read_b128 v[32:35], v10 offset:5120\n" \
  "v_mfma_f32_16x16x32_bf16 v[68:71], a[12:15], v[44:47], v[68:71]\n" \
  "ds_read_b128 v[36:39], v10 offset:25600\n" \
  "v_mfma_f32_16x16x32_bf16 v[60:63], a[92:95], v[40:43], v[60:63]\n" \
  "v_cvt_pk_bf16_f32 v22, v116, v117\n" \
  "v_mfma_f32_16x16x32_bf16 v[72:75], a[92:95], v[44:47], v[72:75]\n" \
  "v_cvt_pk_bf16_f32 v23, v118, v119\n" \
  "v_mfma_f32_16x16x32_bf16 v[64:67], a[172:175], v[40:43], v[64:67]\n" \
  "buffer_store_dwordx4 v[20:23], v3, s[40:43], s71 offen offset:1024 sc1\n" \
  "v_mfma_f32_16x16x32_bf16 v[76:79], a[172:175], v[44:47], v[76:79]\n" \
  "v_cvt_pk_bf16_f32 v24, v108, v109\n" \
  "s_waitcnt lgkmcnt(2)\n" \
  "v_mfma_f32_16x16x32_bf16 v[56:59], a[16:19], v[48:51], v[56:59]\n" \
  "ds_read_b128 v[40:43], v10 offset:6144\n" \
  "v_mfma_f32_16x16x32_bf16 v[68:71], a[16:19], v[52:55], v[68:71]\n" \
  "ds_read_b128 v[44:47], v10 offset:26624\n" \
  "v_mfma_f32_16x16x32_bf16 v[60:63], a[96:99], v[48:51], v[60:63]\n" \
  "v_cvt_pk_bf16_f32 v25, v110, v111\n" \
  "v_mfma_f32_16x16x32_bf16 v[72:75], a[96:99], v[52:55], v[72:75]\n" \
  "v_cvt_pk_bf16_f32 v26, v120, v121\n" \
  "v_mfma_f32_16x16x32_bf16 v[64:67], a[176:179], v[48:51], v[64:67]\n" \
  "v_cvt_pk_bf16_f32 v27, v122, v123\n" \
  "v_mfma_f32_16x16x32_bf16 v[76:79], a[176:179], v[52:55], v[76:79]\n" \
  "buffer_store_dwordx4 v[24:27], v3, s[40:43], s71 offen offset:2048 sc1\n" \
  "s_waitcnt lgkmcnt(2)\n" \
  "v_mfma_f32_16x16x32_bf16 v[56:59], a[20:23], v[32:35], v[56:59]\n" \
  "ds_read_b128 v[48:51], v10 offset:7168\n" \
  "v_mfma_f32_16x16x32_bf16 v[68:71], a[20:23], v[36:39], v[68:71]\n" \
  "ds_read_b128 v[52:55], v10 offset:27648\n" \
  "v_mfma_f32_16x16x32_bf16 v[60:63], a[100:103], v[32:35], v[60:63]\n" \
  "buffer_store_dwordx4 v[16:19], v6, s[40:43], s70 offen sc1\n" \
  "v_mfma_f32_16x16x32_bf16 v[72:75], a[100:103], v[36:39], v[72:75]\n" \
  "v_mfma_f32_16x16x32_bf16 v[64:67], a[180:183], v[32:35], v[64:67]\n" \
  "v_mfma_f32_16x16x32_bf16 v[76:79], a[180:183], v[36:39], v[76:79]\n" \
  "s_waitcnt lgkmcnt(2)\n" \
  "v_mfma_f32_16x16x32_bf16 v[56:59], a[24:27], v[40:43], v[56:59]\n" \
  "ds_read_b128 v[32:35], v10 offset:8192\n" \
  "v_mfma_f32_16x16x32_bf16 v[68:71], a[24:27], v[44:47], v[68:71]\n" \
  "ds_read_b128 v[36:39], v10 offset:28672\n" \
  "v_mfma_f32_16x16x32_bf16 v[60:63], a[104:107], v[40:43], v[60:63]\n" \
  "buffer_store_dwordx4 v[20:23], v6, s[40:43], s70 offen offset:1024 sc1\n" \
  "v_mfma_f32_16x16x32_bf16 v[72:75], a[104:107], v[44:47], v[72:75]\n" \
  "v_mfma_f32_16x16x32_bf16 v[64:67], a[184:187], v[40:43], v[64:67]\n" \
  "v_mfma_f32_16x16x32_bf16 v[76:79], a[184:187], v[44:47], v[76:79]\n" \
  "s_waitcnt lgkmcnt(2)\n" \
  "v_mfma_f32_16x16x32_bf16 v[56:59], a[28:31], v[48:51], v[56:59]\n" \
  "ds_read_b128 v[40:43], v10 offset:9216\n" \
  "v_mfma_f32_16x16x32_bf16 v[68:71], a[28:31], v[52:55], v[68:71]\n" \
  "ds_read_b128 v[44:47], v10 offset:29696\n" \
  "v_mfma_f32_16x16x32_bf16 v[60:63], a[108:111], v[48:51], v[60:63]\n" \
  "buffer_store_dwordx4 v[24:27], v7, s[40:43], s70 offen sc1\n" \
  "v_mfma_f32_16x16x32_bf16 v[72:75], a[108:111], v[52:55], v[72:75]\n" \
  "v_mfma_f32_16x16x32_bf16 v[64:67], a[188:191], v[48:51], v[64:67]\n" \
  "v_mfma_f32_16x16x32_bf16 v[76:79], a[188:191], v[52:55], v[76:79]\n" \
  "s_waitcnt lgkmcnt(2)\n" \
  "v_mfma_f32_16x16x32_bf16 v[56:59], a[32:35], v[32:35], v[56:59]\n" \
  "ds_read_b128 v[48:51], v10 offset:10240\n" \
  "v_mfma_f32_16x16x32_bf16 v[68:71], a[32:35], v[36:39], v[68:71]\n" \
  "ds_read_b128 v[52:55], v10 offset:30720\n" \
  "v_mfma_f32_16x16x32_bf16 v[60:63], a[112:115], v[32:35], v[60:63]\n" \
  "s_add_u32 s72, s68, 4096\n" \
  "v_mfma_f32_16x16x32_bf16 v[72:75], a[112:115], v[36:39], v[72:75]\n" \
  "s_add_u32 s73, s68, 8192\n" \
  "v_mfma_f32_16x16x32_bf16 v[64:67], a[192:195], v[32:35], v[64:67]\n" \
  "s_add_u32 m0, s55, 40960\n" \
  "v_mfma_f32_16x16x32_bf16 v[76:79], a[192:195], v[36:39], v[76:79]\n" \
  "buffer_load_dwordx4 v1, s[36:39], s68 offen lds\n" \
  "s_waitcnt lgkmcnt(2)\n" \
  "v_mfma_f32_16x16x32_bf16 v[56:59], a[36:39], v[40:43], v[56:59]\n" \
  "ds_read_b128 v[32:35], v10 offset:11264\n" \
  "v_mfma_f32_16x16x32_bf16 v[68:71], a[36:39], v[44:47], v[68:71]\n" \
  "ds_read_b128 v[36:39], v10 offset:31744\n" \
  "v_mfma_f32_16x16x32_bf16 v[60:63], a[116:119], v[40:43], v[60:63]\n" \
  "buffer_load_dwordx4 v1, s[36:39], s68 offen offset:1024 lds\n" \
  "v_mfma_f32_16x16x32_bf16 v[72:75], a[116:119], v[44:47], v[72:75]\n" \
  "s_lshr_b64 s[76:77], s[52:53], s56\n" \
  "v_mfma_f32_16x16x32_bf16 v[64:67], a[196:199], v[40:43], v[64:67]\n" \
  "s_lshl_b32 s69, s56, 8\n" \
  "v_mfma_f32_16x16x32_bf16 v[76:79], a[196:199], v[44:47], v[76:79]\n" \
  "s_and_b32 s70, s76, 1\n" \
  "s_waitcnt lgkmcnt(2)\n" \
  "v_mfma_f32_16x16x32_bf16 v[56:59], a[40:43], v[48:51], v[56:59]\n" \
  "ds_read_b128 v[40:43], v10 offset:12288\n" \
  "v_mfma_f32_16x16x32_bf16 v[68:71], a[40:43], v[52:55], v[68:71]\n" \
  "ds_read_b128 v[44:47], v10 offset:32768\n" \
  "v_mfma_f32_16x16x32_bf16 v[60:63], a[120:123], v[48:51], v[60:63]\n" \
  "buffer_load_dwordx4 v1, s[36:39], s68 offen offset:2048 lds\n" \
  "v_mfma_f32_16x16x32_bf16 v[72:75], a[120:123], v[52:55], v[72:75]\n" \
  "s_bfe_u32 s71, s76, 0x10001\n" \
  "v_mfma_f32_16x16x32_bf16 v[64:67], a[200:203], v[48:51], v[64:67]\n" \
  "s_add_u32 s71, s71, s69\n" \
  "v_mfma_f32_16x16x32_bf16 v[76:79], a[200:203], v[52:55], v[76:79]\n" \
  "s_add_u32 s69, s69, s70\n" \
  "s_waitcnt lgkmcnt(2)\n" \
  "v_mfma_f32_16x16x32_bf16 v[56:59], a[44:47], v[32:35], v[56:59]\n" \
  "ds_read_b128 v[48:51], v10 offset:13312\n" \
  "v_mfma_f32_16x16x32_bf16 v[68:71], a[44:47], v[36:39], v[68:71]\n" \
  "ds_read_b128 v[52:55], v10 offset:33792\n" \
  "v_mfma_f32_16x16x32_bf16 v[60:63], a[124:127], v[32:35], v[60:63]\n" \
  "buffer_load_dwordx4 v1, s[36:39], s68 offen offset:3072 lds\n" \
  "v_mfma_f32_16x16x32_bf16 v[72:75], a[124:127], v[36:39], v[72:75]\n" \
  "s_add_u32 s71, s71, 0x100\n" \
  "v_mfma_f32_16x16x32_bf16 v[64:67], a[204:207], v[32:35], v[64:67]\n" \
  "s_lshl_b32 s69, s69, 13\n" \
  "v_mfma_f32_16x16x32_bf16 v[76:79], a[204:207], v[36:39], v[76:79]\n" \
  "s_lshl_b32 s71, s71, 13\n" \
  "s_waitcnt lgkmcnt(2)\n" \
  "v_mfma_f32_16x16x32_bf16 v[56:59], a[48:51], v[40:43], v[56:59]\n" \
  "ds_read_b128 v[32:35], v10 offset:14336\n" \
  "v_mfma_f32_16x16x32_bf16 v[68:71], a[48:51], v[44:47], v[68:71]\n" \
  "ds_read_b128 v[36:39], v10 offset:34816\n" \
  "v_mfma_f32_16x16x32_bf16 v[60:63], a[128:131], v[40:43], v[60:63]\n" \
  "s_add_u32 m0, s55, 45056\n" \
  "v_mfma_f32_16x16x32_bf16 v[72:75], a[128:131], v[44:47], v[72:75]\n" \
  "buffer_load_dwordx4 v1, s[36:39], s72 offen lds\n" \
  "v_mfma_f32_16x16x32_bf16 v[64:67], a[208:211], v[40:43], v[64:67]\n" \
  "v_mfma_f32_16x16x32_bf16 v[76:79], a[208:211], v[44:47], v[76:79]\n" \
  "s_waitcnt lgkmcnt(2)\n" \
  "v_mfma_f32_16x16x32_bf16 v[56:59], a[52:55], v[48:51], v[56:59]\n" \
  "ds_read_b128 v[40:43], v10 offset:15360\n" \
  "v_mfma_f32_16x16x32_bf16 v[68:71], a[52:55], v[52:55], v[68:71]\n" \
  "ds_read_b128 v[44:47], v10 offset:35840\n" \
  "v_mfma_f32_16x16x32_bf16 v[60:63], a[132:135], v[48:51], v[60:63]\n" \
  "buffer_load_dwordx4 v1, s[36:39], s72 offen offset:1024 lds\n" \
  "v_mfma_f32_16x16x32_bf16 v[72:75], a[132:135], v[52:55], v[72:75]\n" \
  "s_cmp_eq_u32 s57, 0\n" \
  "v_mfma_f32_16x16x32_bf16 v[64:67], a[212:215], v[48:51], v[64:67]\n" \
  "s_cselect_b32 s70, s69, s71\n" \
  "v_mfma_f32_16x16x32_bf16 v[76:79], a[212:215], v[52:55], v[76:79]\n" \
  "s_xor_b32 s70, s70, 0x2000\n" \
  "s_waitcnt lgkmcnt(2)\n" \
  "v_mfma_f32_16x16x32_bf16 v[56:59], a[56:59], v[32:35], v[56:59]\n" \
  "ds_read_b128 v[48:51], v10 offset:16384\n" \
  "v_mfma_f32_16x16x32_bf16 v[68:71], a[56:59], v[36:39], v[68:71]\n" \
  "ds_read_b128 v[52:55], v10 offset:36864\n" \
  "v_mfma_f32_16x16x32_bf16 v[60:63], a[136:139], v[32:35], v[60:63]\n" \
  "buffer_load_dwordx4 v1, s[36:39], s72 offen offset:2048 lds\n" \
  "v_mfma_f32_16x16x32_bf16 v[72:75], a[136:139], v[36:39], v[72:75]\n" \
  "s_add_u32 s56, s56, 2\n" \
  "v_mfma_f32_16x16x32_bf16 v[64:67], a[216:219], v[32:35], v[64:67]\n" \
  "v_mfma_f32_16x16x32_bf16 v[76:79], a[216:219], v[36:39], v[76:79]\n" \
  "s_waitcnt lgkmcnt(2)\n" \
  "v_mfma_f32_16x16x32_bf16 v[56:59], a[60:63], v[40:43], v[56:59]\n" \
  "ds_read_b128 v[32:35], v10 offset:17408\n" \
  "v_mfma_f32_16x16x32_bf16 v[68:71], a[60:63], v[44:47], v[68:71]\n" \
  "ds_read_b128 v[36:39], v10 offset:37888\n" \
  "v_mfma_f32_16x16x32_bf16 v[60:63], a[140:143], v[40:43], v[60:63]\n" \
  "buffer_load_dwordx4 v1, s[36:39], s72 offen offset:3072 lds\n" \
  "v_mfma_f32_16x16x32_bf16 v[72:75], a[140:143], v[44:47], v[72:75]\n" \
  "v_mfma_f32_16x16x32_bf16 v[64:67], a[220:223], v[40:43], v[64:67]\n" \
  "v_mfma_f32_16x16x32_bf16 v[76:79], a[220:223], v[44:47], v[76:79]\n" \
  "s_waitcnt lgkmcnt(2)\n" \
  "v_mfma_f32_16x16x32_bf16 v[56:59], a[64:67], v[48:51], v[56:59]\n" \
  "ds_read_b128 v[40:43], v10 offset:18432\n" \
  "v_mfma_f32_16x16x32_bf16 v[68:71], a[64:67], v[52:55], v[68:71]\n" \
  "ds_read_b128 v[44:47], v10 offset:38912\n" \
  "v_mfma_f32_16x16x32_bf16 v[60:63], a[144:147], v[48:51], v[60:63]\n" \
  "s_add_u32 m0, s55, 49152\n" \
  "v_mfma_f32_16x16x32_bf16 v[72:75], a[144:147], v[52:55], v[72:75]\n" \
  "buffer_load_dwordx4 v1, s[36:39], s73 offen lds\n" \
  "v_mfma_f32_16x16x32_bf16 v[64:67], a[224:227], v[48:51], v[64:67]\n" \
  "v_mfma_f32_16x16x32_bf16 v[76:79], a[224:227], v[52:55], v[76:79]\n" \
  "s_waitcnt lgkmcnt(2)\n" \
  "v_mfma_f32_16x16x32_bf16 v[56:59], a[68:71], v[32:35], v[56:59]\n" \
  "ds_read_b128 v[48:51], v10 offset:19456\n" \
  "v_mfma_f32_16x16x32_bf16 v[68:71], a[68:71], v[36:39], v[68:71]\n" \
  "ds_read_b128 v[52:55], v10 offset:39936\n" \
  "v_mfma_f32_16x16x32_bf16 v[60:63], a[148:151], v[32:35], v[60:63]\n" \
  "buffer_load_dwordx4 v1, s[36:39], s73 offen offset:1024 lds\n" \
  "v_mfma_f32_16x16x32_bf16 v[72:75], a[148:151], v[36:39], v[72:75]\n" \
  "s_add_u32 s68, s68, 40960\n" \
  "v_mfma_f32_16x16x32_bf16 v[64:67], a[228:231], v[32:35], v[64:67]\n" \
  "v_mfma_f32_16x16x32_bf16 v[76:79], a[228:231], v[36:39], v[76:79]\n" \
  "s_waitcnt vmcnt(10) lgkmcnt(0)\n" \
  "s_barrier\n" \
  "v_mfma_f32_16x16x32_bf16 v[56:59], a[72:75], v[40:43], v[56:59]\n" \
  "ds_read_b128 v[32:35], v0 offset:0\n" \
  "v_mfma_f32_16x16x32_bf16 v[68:71], a[72:75], v[44:47], v[68:71]\n" \
  "ds_read_b128 v[36:39], v0 offset:20480\n" \
  "v_mfma_f32_16x16x32_bf16 v[60:63], a[152:155], v[40:43], v[60:63]\n" \
  "v_mfma_f32_16x16x32_bf16 v[72:75], a[152:155], v[44:47], v[72:75]\n" \
  "v_mfma_f32_16x16x32_bf16 v[64:67], a[232:235], v[40:43], v[64:67]\n" \
  "v_mfma_f32_16x16x32_bf16 v[76:79], a[232:235], v[44:47], v[76:79]\n" \
  "v_mfma_f32_16x16x32_bf16 v[56:59], a[76:79], v[48:51], v[56:59]\n" \
  "ds_read_b128 v[40:43], v0 offset:1024\n" \
  "v_mfma_f32_16x16x32_bf16 v[68:71], a[76:79], v[52:55], v[68:71]\n" \
  "ds_read_b128 v[44:47], v0 offset:21504\n" \
  "v_mfma_f32_16x16x32_bf16 v[60:63], a[156:159], v[48:51], v[60:63]\n" \
  "s_cmp_eq_u32 s54, 60\n" \
  "v_mfma_f32_16x16x32_bf16 v[72:75], a[156:159], v[52:55], v[72:75]\n" \
  "v_mfma_f32_16x16x32_bf16 v[64:67], a[236:239], v[48:51], v[64:67]\n" \
  "v_mfma_f32_16x16x32_bf16 v[76:79], a[236:239], v[52:55], v[76:79]\n" \
  "s_waitcnt lgkmcnt(2)\n" \
  "v_mfma_f32_16x16x32_bf16 v[100:103], a[0:3], v[32:35], 0\n" \
  "ds_read_b128 v[48:51], v0 offset:2048\n" \
  "v_mfma_f32_16x16x32_bf16 v[112:115], a[0:3], v[36:39], 0\n" \
  "ds_read_b128 v[52:55], v0 offset:22528\n" \
  "v_mfma_f32_16x16x32_bf16 v[104:107], a[80:83], v[32:35], 0\n" \
  "buffer_atomic_add v11, v8, s[40:43], s74 offen\n" \
  "v_mfma_f32_16x16x32_bf16 v[116:119], a[80:83], v[36:39], 0\n" \
  "s_add_u32 s74, s74, 0x2200\n" \
  "v_mfma_f32_16x16x32_bf16 v[108:111], a[160:163], v[32:35], 0\n" \
  "s_and_b32 s74, s74, 0xffffdfff\n" \
  "v_mfma_f32_16x16x32_bf16 v[120:123], a[160:163], v[36:39], 0\n" \
  "s_waitcnt lgkmcnt(2)\n" \
  "v_mfma_f32_16x16x32_bf16 v[100:103], a[4:7], v[40:43], v[100:103]\n" \
  "ds_read_b128 v[32:35], v0 offset:3072\n" \
  "v_mfma_f32_16x16x32_bf16 v[112:115], a[4:7], v[44:47], v[112:115]\n" \
  "ds_read_b128 v[36:39], v0 offset:23552\n" \
  "v_mfma_f32_16x16x32_bf16 v[104:107], a[84:87], v[40:43], v[104:107]\n" \
  "buffer_store_dwordx4 v[12:15], v2, s[40:43], s69 offen sc1\n" \
  "v_mfma_f32_16x16x32_bf16 v[116:119], a[84:87], v[44:47], v[116:119]\n" \
  "v_cvt_pk_bf16_f32 v16, v56, v57\n" \
  "v_mfma_f32_16x16x32_bf16 v[108:111], a[164:167], v[40:43], v[108:111]\n" \
  "v_cvt_pk_bf16_f32 v17, v58, v59\n" \
  "v_mfma_f32_16x16x32_bf16 v[120:123], a[164:167], v[44:47], v[120:123]\n" \
  "v_cvt_pk_bf16_f32 v18, v60, v61\n" \
  "s_waitcnt lgkmcnt(2)\n" \
  "v_mfma_f32_16x16x32_bf16 v[100:103], a[8:11], v[48:51], v[100:103]\n" \
  "ds_read_b128 v[40:43], v0 offset:4096\n" \
  "v_mfma_f32_16x16x32_bf16 v[112:115], a[8:11], v[52:55], v[112:115]\n" \
  "ds_read_b128 v[44:47], v0 offset:24576\n" \
  "v_mfma_f32_16x16x32_bf16 v[104:107], a[88:91], v[48:51], v[104:107]\n" \
  "v_cvt_pk_bf16_f32 v19, v62, v63\n" \
  "v_mfma_f32_16x16x32_bf16 v[116:119], a[88:91], v[52:55], v[116:119]\n" \
  "buffer_store_dwordx4 v[16:19], v3, s[40:43], s71 offen offset:0 sc1\n" \
  "v_mfma_f32_16x16x32_bf16 v[108:111], a[168:171], v[48:51], v[108:111]\n" \
  "v_cvt_pk_bf16_f32 v20, v68, v69\n" \
  "v_mfma_f32_16x16x32_bf16 v[120:123], a[168:171], v[52:55], v[120:123]\n" \
  "v_cvt_pk_bf16_f32 v21, v70, v71\n" \
  "s_waitcnt lgkmcnt(2)\n" \
  "v_mfma_f32_16x16x32_bf16 v[100:103], a[12:15], v[32:35], v[100:103]\n" \
  "ds_read_b128 v[48:51], v0 offset:5120\n" \
  "v_mfma_f32_16x16x32_bf16 v[112:115], a[12:15], v[36:39], v[112:115]\n" \
  "ds_read_b128 v[52:55], v0 offset:25600\n" \
  "v_mfma_f32_16x16x32_bf16 v[104:107], a[92:95], v[32:35], v[104:107]\n" \
  "v_cvt_pk_bf16_f32 v22, v72, v73\n" \
  "v_mfma_f32_16x16x32_bf16 v[116:119], a[92:95], v[36:39], v[116:119]\n" \
  "v_cvt_pk_bf16_f32 v23, v74, v75\n" \
  "v_mfma_f32_16x16x32_bf16 v[108:111], a[172:175], v[32:35], v[108:111]\n" \
  "buffer_store_dwordx4 v[20:23], v3, s[40:43], s71 offen offset:1024 sc1\n" \
  "v_mfma_f32_16x16x32_bf16 v[120:123], a[172:175], v[36:39], v[120:123]\n" \
  "v_cvt_pk_bf16_f32 v24, v64, v65\n" \
  "s_waitcnt lgkmcnt(2)\n" \
  "v_mfma_f32_16x16x32_bf16 v[100:103], a[16:19], v[40:43], v[100:103]\n" \
  "ds_read_b128 v[32:35], v0 offset:6144\n" \
  "v_mfma_f32_16x16x32_bf16 v[112:115], a[16:19], v[44:47], v[112:115]\n" \
  "ds_read_b128 v[36:39], v0 offset:26624\n" \
  "v_mfma_f32_16x16x32_bf16 v[104:107], a[96:99], v[40:43], v[104:107]\n" \
  "v_cvt_pk_bf16_f32 v25, v66, v67\n" \
  "v_mfma_f32_16x16x32_bf16 v[116:119], a[96:99], v[44:47], v[116:119]\n" \
  "v_cvt_pk_bf16_f32 v26, v76, v77\n" \
  "v_mfma_f32_16x16x32_bf16 v[108:111], a[176:179], v[40:43], v[108:111]\n" \
  "v_cvt_pk_bf16_f32 v27, v78, v79\n" \
  "v_mfma_f32_16x16x32_bf16 v[120:123], a[176:179], v[44:47], v[120:123]\n" \
  "buffer_store_dwordx4 v[24:27], v3, s[40:43], s71 offen offset:2048 sc1\n" \
  "s_waitcnt lgkmcnt(2)\n" \
  "v_mfma_f32_16x16x32_bf16 v[100:103], a[20:23], v[48:51], v[100:103]\n" \
  "ds_read_b128 v[40:43], v0 offset:7168\n" \
  "v_mfma_f32_16x16x32_bf16 v[112:115], a[20:23], v[52:55], v[112:115]\n" \
  "ds_read_b128 v[44:47], v0 offset:27648\n" \
  "v_mfma_f32_16x16x32_bf16 v[104:107], a[100:103], v[48:51], v[104:107]\n" \
  "buffer_store_dwordx4 v[16:19], v6, s[40:43], s70 offen sc1\n" \
  "v_mfma_f32_16x16x32_bf16 v[116:119], a[100:103], v[52:55], v[116:119]\n" \
  "v_mfma_f32_16x16x32_bf16 v[108:111], a[180:183], v[48:51], v[108:111]\n" \
  "v_mfma_f32_16x16x32_bf16 v[120:123], a[180:183], v[52:55], v[120:123]\n" \
  "s_waitcnt lgkmcnt(2)\n" \
  "v_mfma_f32_16x16x32_bf16 v[100:103], a[24:27], v[32:35], v[100:103]\n" \
  "ds_read_b128 v[48:51], v0 offset:8192\n" \
  "v_mfma_f32_16x16x32_bf16 v[112:115], a[24:27], v[36:39], v[112:115]\n" \
  "ds_read_b128 v[52:55], v0 offset:28672\n" \
  "v_mfma_f32_16x16x32_bf16 v[104:107], a[104:107], v[32:35], v[104:107]\n" \
  "buffer_store_dwordx4 v[20:23], v6, s[40:43], s70 offen offset:1024 sc1\n" \
  "v_mfma_f32_16x16x32_bf16 v[116:119], a[104:107], v[36:39], v[116:119]\n" \
  "v_mfma_f32_16x16x32_bf16 v[108:111], a[184:187], v[32:35], v[108:111]\n" \
  "v_mfma_f32_16x16x32_bf16 v[120:123], a[184:187], v[36:39], v[120:123]\n" \
  "s_waitcnt lgkmcnt(2)\n" \
  "v_mfma_f32_16x16x32_bf16 v[100:103], a[28:31], v[40:43], v[100:103]\n" \
  "ds_read_b128 v[32:35], v0 offset:9216\n" \
  "v_mfma_f32_16x16x32_bf16 v[112:115], a[28:31], v[44:47], v[112:115]\n" \
  "ds_read_b128 v[36:39], v0 offset:29696\n" \
  "v_mfma_f32_16x16x32_bf16 v[104:107], a[108:111], v[40:43], v[104:107]\n" \
  "buffer_store_dwordx4 v[24:27], v7, s[40:43], s70 offen sc1\n" \
  "v_mfma_f32_16x16x32_bf16 v[116:119], a[108:111], v[44:47], v[116:119]\n" \
  "v_mfma_f32_16x16x32_bf16 v[108:111], a[188:191], v[40:43], v[108:111]\n" \
  "v_mfma_f32_16x16x32_bf16 v[120:123], a[188:191], v[44:47], v[120:123]\n" \
  "s_waitcnt lgkmcnt(2)\n" \
  "v_mfma_f32_16x16x32_bf16 v[100:103], a[32:35], v[48:51], v[100:103]\n" \
  "ds_read_b128 v[40:43], v0 offset:10240\n" \
  "v_mfma_f32_16x16x32_bf16 v[112:115], a[32:35], v[52:55], v[112:115]\n" \
  "ds_read_b128 v[44:47], v0 offset:30720\n" \
  "v_mfma_f32_16x16x32_bf16 v[104:107], a[112:115], v[48:51], v[104:107]\n" \
  "s_add_u32 s72, s68, 4096\n" \
  "v_mfma_f32_16x16x32_bf16 v[116:119], a[112:115], v[52:55], v[116:119]\n" \
  "s_add_u32 s73, s68, 8192\n" \
  "v_mfma_f32_16x16x32_bf16 v[108:111], a[192:195], v[48:51], v[108:111]\n" \
  "s_add_u32 m0, s55, 81920\n" \
  "v_mfma_f32_16x16x32_bf16 v[120:123], a[192:195], v[52:55], v[120:123]\n" \
  "buffer_load_dwordx4 v1, s[36:39], s68 offen lds\n" \
  "s_waitcnt lgkmcnt(2)\n" \
  "v_mfma_f32_16x16x32_bf16 v[100:103], a[36:39], v[32:35], v[100:103]\n" \
  "ds_read_b128 v[48:51], v0 offset:11264\n" \
  "v_mfma_f32_16x16x32_bf16 v[112:115], a[36:39], v[36:39], v[112:115]\n" \
  "ds_read_b128 v[52:55], v0 offset:31744\n" \
  "v_mfma_f32_16x16x32_bf16 v[104:107], a[116:119], v[32:35], v[104:107]\n" \
  "buffer_load_dwordx4 v1, s[36:39], s68 offen offset:1024 lds\n" \
  "v_mfma_f32_16x16x32_bf16 v[116:119], a[116:119], v[36:39], v[116:119]\n" \
  "s_lshr_b64 s[76:77], s[52:53], s56\n" \
  "v_mfma_f32_16x16x32_bf16 v[108:111], a[196:199], v[32:35], v[108:111]\n" \
  "s_lshl_b32 s69, s56, 8\n" \
  "v_mfma_f32_16x16x32_bf16 v[120:123], a[196:199], v[36:39], v[120:123]\n" \
  "s_and_b32 s70, s76, 1\n" \
  "s_waitcnt lgkmcnt(2)\n" \
  "v_mfma_f32_16x16x32_bf16 v[100:103], a[40:43], v[40:43], v[100:103]\n" \
  "ds_read_b128 v[32:35], v0 offset:12288\n" \
  "v_mfma_f32_16x16x32_bf16 v[112:115], a[40:43], v[44:47], v[112:115]\n" \
  "ds_read_b128 v[36:39], v0 offset:32768\n" \
  "v_mfma_f32_16x16x32_bf16 v[104:107], a[120:123], v[40:43], v[104:107]\n" \
  "buffer_load_dwordx4 v1, s[36:39], s68 offen offset:2048 lds\n" \
  "v_mfma_f32_16x16x32_bf16 v[116:119], a[120:123], v[44:47], v[116:119]\n" \
  "s_bfe_u32 s71, s76, 0x10001\n" \
  "v_mfma_f32_16x16x32_bf16 v[108:111], a[200:203], v[40:43], v[108:111]\n" \
  "s_add_u32 s71, s71, s69\n" \
  "v_mfma_f32_16x16x32_bf16 v[120:123], a[200:203], v[44:47], v[120:123]\n" \
  "s_add_u32 s69, s69, s70\n" \
  "s_waitcnt lgkmcnt(2)\n" \
  "v_mfma_f32_16x16x32_bf16 v[100:103], a[44:47], v[48:51], v[100:103]\n" \
  "ds_read_b128 v[40:43], v0 offset:13312\n" \
  "v_mfma_f32_16x16x32_bf16 v[112:115], a[44:47], v[52:55], v[112:115]\n" \
  "ds_read_b128 v[44:47], v0 offset:33792\n" \
  "v_mfma_f32_16x16x32_bf16 v[104:107], a[124:127], v[48:51], v[104:107]\n" \
  "buffer_load_dwordx4 v1, s[36:39], s68 offen offset:3072 lds\n" \
  "v_mfma_f32_16x16x32_bf16 v[116:119], a[124:127], v[52:55], v[116:119]\n" \
  "s_add_u32 s71, s71, 0x100\n" \
  "v_mfma_f32_16x16x32_bf16 v[108:111], a[204:207], v[48:51], v[108:111]\n" \
  "s_lshl_b32 s69, s69, 13\n" \
  "v_mfma_f32_16x16x32_bf16 v[120:123], a[204:207], v[52:55], v[120:123]\n" \
  "s_lshl_b32 s71, s71, 13\n" \
  "s_waitcnt lgkmcnt(2)\n" \
  "v_mfma_f32_16x16x32_bf16 v[100:103], a[48:51], v[32:35], v[100:103]\n" \
  "ds_read_b128 v[48:51], v0 offset:14336\n" \
  "v_mfma_f32_16x16x32_bf16 v[112:115], a[48:51], v[36:39], v[112:115]\n" \
  "ds_read_b128 v[52:55], v0 offset:34816\n" \
  "v_mfma_f32_16x16x32_bf16 v[104:107], a[128:131], v[32:35], v[104:107]\n" \
  "s_add_u32 m0, s55, 86016\n" \
  "v_mfma_f32_16x16x32_bf16 v[116:119], a[128:131], v[36:39], v[116:119]\n" \
  "buffer_load_dwordx4 v1, s[36:39], s72 offen lds\n" \
  "v_mfma_f32_16x16x32_bf16 v[108:111], a[208:211], v[32:35], v[108:111]\n" \
  "v_mfma_f32_16x16x32_bf16 v[120:123], a[208:211], v[36:39], v[120:123]\n" \
  "s_waitcnt lgkmcnt(2)\n" \
  "v_mfma_f32_16x16x32_bf16 v[100:103], a[52:55], v[40:43], v[100:103]\n" \
  "ds_read_b128 v[32:35], v0 offset:15360\n" \
  "v_mfma_f32_16x16x32_bf16 v[112:115], a[52:55], v[44:47], v[112:115]\n" \
  "ds_read_b128 v[36:39], v0 offset:35840\n" \
  "v_mfma_f32_16x16x32_bf16 v[104:107], a[132:135], v[40:43], v[104:107]\n" \
  "buffer_load_dwordx4 v1, s[36:39], s72 offen offset:1024 lds\n" \
  "v_mfma_f32_16x16x32_bf16 v[116:119], a[132:135], v[44:47], v[116:119]\n" \
  "s_cmp_eq_u32 s57, 0\n" \
  "v_mfma_f32_16x16x32_bf16 v[108:111], a[212:215], v[40:43], v[108:111]\n" \
  "s_cselect_b32 s70, s69, s71\n" \
  "v_mfma_f32_16x16x32_bf16 v[120:123], a[212:215], v[44:47], v[120:123]\n" \
  "s_xor_b32 s70, s70, 0x2000\n" \
  "s_waitcnt lgkmcnt(2)\n" \
  "v_mfma_f32_16x16x32_bf16 v[100:103], a[56:59], v[48:51], v[100:103]\n" \
  "ds_read_b128 v[40:43], v0 offset:16384\n" \
  "v_mfma_f32_16x16x32_bf16 v[112:115], a[56:59], v[52:55], v[112:115]\n" \
  "ds_read_b128 v[44:47], v0 offset:36864\n" \
  "v_mfma_f32_16x16x32_bf16 v[104:107], a[136:139], v[48:51], v[104:107]\n" \
  "buffer_load_dwordx4 v1, s[36:39], s72 offen offset:2048 lds\n" \
  "v_mfma_f32_16x16x32_bf16 v[116:119], a[136:139], v[52:55], v[116:119]\n" \
  "s_add_u32 s56, s56, 2\n" \
  "v_mfma_f32_16x16x32_bf16 v[108:111], a[216:219], v[48:51], v[108:111]\n" \
  "v_mfma_f32_16x16x32_bf16 v[120:123], a[216:219], v[52:55], v[120:123]\n" \
  "s_waitcnt lgkmcnt(2)\n" \
  "v_mfma_f32_16x16x32_bf16 v[100:103], a[60:63], v[32:35], v[100:103]\n" \
  "ds_read_b128 v[48:51], v0 offset:17408\n" \
  "v_mfma_f32_16x16x32_bf16 v[112:115], a[60:63], v[36:39], v[112:115]\n" \
  "ds_read_b128 v[52:55], v0 offset:37888\n" \
  "v_mfma_f32_16x16x32_bf16 v[104:107], a[140:143], v[32:35], v[104:107]\n" \
  "buffer_load_dwordx4 v1, s[36:39], s72 offen offset:3072 lds\n" \
  "v_mfma_f32_16x16x32_bf16 v[116:119], a[140:143], v[36:39], v[116:119]\n" \
  "v_mfma_f32_16x16x32_bf16 v[108:111], a[220:223], v[32:35], v[108:111]\n" \
  "v_mfma_f32_16x16x32_bf16 v[120:123], a[220:223], v[36:39], v[120:123]\n" \
  "s_waitcnt lgkmcnt(2)\n" \
  "v_mfma_f32_16x16x32_bf16 v[100:103], a[64:67], v[40:43], v[100:103]\n" \
  "ds_read_b128 v[32:35], v0 offset:18432\n" \
  "v_mfma_f32_16x16x32_bf16 v[112:115], a[64:67], v[44:47], v[112:115]\n" \
  "ds_read_b128 v[36:39], v0 offset:38912\n" \
  "v_mfma_f32_16x16x32_bf16 v[104:107], a[144:147], v[40:43], v[104:107]\n" \
  "s_add_u32 m0, s55, 90112\n" \
  "v_mfma_f32_16x16x32_bf16 v[116:119], a[144:147], v[44:47], v[116:119]\n" \
  "buffer_load_dwordx4 v1, s[36:39], s73 offen lds\n" \
  "v_mfma_f32_16x16x32_bf16 v[108:111], a[224:227], v[40:43], v[108:111]\n" \
  "v_mfma_f32_16x16x32_bf16 v[120:123], a[224:227], v[44:47], v[120:123]\n" \
  "s_waitcnt lgkmcnt(2)\n" \
  "v_mfma_f32_16x16x32_bf16 v[100:103], a[68:71], v[48:51], v[100:103]\n" \
  "ds_read_b128 v[40:43], v0 offset:19456\n" \
  "v_mfma_f32_16x16x32_bf16 v[112:115], a[68:71], v[52:55], v[112:115]\n" \
  "ds_read_b128 v[44:47], v0 offset:39936\n" \
  "v_mfma_f32_16x16x32_bf16 v[104:107], a[148:151], v[48:51], v[104:107]\n" \
  "buffer_load_dwordx4 v1, s[36:39], s73 offen offset:1024 lds\n" \
  "v_mfma_f32_16x16x32_bf16 v[116:119], a[148:151], v[52:55], v[116:119]\n" \
  "s_add_u32 s68, s68, 40960\n" \
  "v_mfma_f32_16x16x32_bf16 v[108:111], a[228:231], v[48:51], v[108:111]\n" \
  "v_mfma_f32_16x16x32_bf16 v[120:123], a[228:231], v[52:55], v[120:123]\n" \
  "s_waitcnt vmcnt(10) lgkmcnt(0)\n" \
  "s_barrier\n" \
  "v_mfma_f32_16x16x32_bf16 v[100:103], a[72:75], v[32:35], v[100:103]\n" \
  "ds_read_b128 v[48:51], v9 offset:0\n" \
  "v_mfma_f32_16x16x32_bf16 v[112:115], a[72:75], v[36:39], v[112:115]\n" \
  "ds_read_b128 v[52:55], v9 offset:20480\n" \
  "v_mfma_f32_16x16x32_bf16 v[104:107], a[152:155], v[32:35], v[104:107]\n" \
  "v_mfma_f32_16x16x32_bf16 v[116:119], a[152:155], v[36:39], v[116:119]\n" \
  "v_mfma_f32_16x16x32_bf16 v[108:111], a[232:235], v[32:35], v[108:111]\n" \
  "v_mfma_f32_16x16x32_bf16 v[120:123], a[232:235], v[36:39], v[120:123]\n" \
  "v_mfma_f32_16x16x32_bf16 v[100:103], a[76:79], v[40:43], v[100:103]\n" \
  "ds_read_b128 v[32:35], v9 offset:1024\n" \
  "v_mfma_f32_16x16x32_bf16 v[112:115], a[76:79], v[44:47], v[112:115]\n" \
  "ds_read_b128 v[36:39], v9 offset:21504\n" \
  "v_mfma_f32_16x16x32_bf16 v[104:107], a[156:159], v[40:43], v[104:107]\n" \
  "s_cmp_eq_u32 s54, 60\n" \
  "v_mfma_f32_16x16x32_bf16 v[116:119], a[156:159], v[44:47], v[116:119]\n" \
  "v_mfma_f32_16x16x32_bf16 v[108:111], a[236:239], v[40:43], v[108:111]\n" \
  "v_mfma_f32_16x16x32_bf16 v[120:123], a[236:239], v[44:47], v[120:123]\n" \
  "s_cbranch_scc1 LPE_%=\n" \
  "s_waitcnt lgkmcnt(2)\n" \
  "v_mfma_f32_16x16x32_bf16 v[56:59], a[0:3], v[48:51], 0\n" \
  "ds_read_b128 v[40:43], v9 offset:2048\n" \
  "v_mfma_f32_16x16x32_bf16 v[68:71], a[0:3], v[52:55], 0\n" \
  "ds_read_b128 v[44:47], v9 offset:22528\n" \
  "v_mfma_f32_16x16x32_bf16 v[60:63], a[80:83], v[48:51], 0\n" \
  "buffer_atomic_add v11, v8, s[40:43], s74 offen\n" \
  "v_mfma_f32_16x16x32_bf16 v[72:75], a[80:83], v[52:55], 0\n" \
  "s_add_u32 s74, s74, 0x2200\n" \
  "v_mfma_f32_16x16x32_bf16 v[64:67], a[160:163], v[48:51], 0\n" \
  "s_and_b32 s74, s74, 0xffffdfff\n" \
  "v_mfma_f32_16x16x32_bf16 v[76:79], a[160:163], v[52:55], 0\n" \
  "s_waitcnt lgkmcnt(2)\n" \
  "v_mfma_f32_16x16x32_bf16 v[56:59], a[4:7], v[32:35], v[56:59]\n" \
  "ds_read_b128 v[48:51], v9 offset:3072\n" \
  "v_mfma_f32_16x16x32_bf16 v[68:71], a[4:7], v[36:39], v[68:71]\n" \
  "ds_read_b128 v[52:55], v9 offset:23552\n" \
  "v_mfma_f32_16x16x32_bf16 v[60:63], a[84:87], v[32:35], v[60:63]\n" \
  "buffer_store_dwordx4 v[12:15], v2, s[40:43], s69 offen sc1\n" \
  "v_mfma_f32_16x16x32_bf16 v[72:75], a[84:87], v[36:39], v[72:75]\n" \
  "v_cvt_pk_bf16_f32 v16, v100, v101\n" \
  "v_mfma_f32_16x16x32_bf16 v[64:67], a[164:167], v[32:35], v[64:67]\n" \
  "v_cvt_pk_bf16_f32 v17, v102, v103\n" \
  "v_mfma_f32_16x16x32_bf16 v[76:79], a[164:167], v[36:39], v[76:79]\n" \
  "v_cvt_pk_bf16_f32 v18, v104, v105\n" \
  "s_waitcnt lgkmcnt(2)\n" \
  "v_mfma_f32_16x16x32_bf16 v[56:59], a[8:11], v[40:43], v[56:59]\n" \
  "ds_read_b128 v[32:35], v9 offset:4096\n" \
  "v_mfma_f32_16x16x32_bf16 v[68:71], a[8:11], v[44:47], v[68:71]\n" \
  "ds_read_b128 v[36:39], v9 offset:24576\n" \
  "v_mfma_f32_16x16x32_bf16 v[60:63], a[88:91], v[40:43], v[60:63]\n" \
  "v_cvt_pk_bf16_f32 v19, v106, v107\n" \
  "v_mfma_f32_16x16x32_bf16 v[72:75], a[88:91], v[44:47], v[72:75]\n" \
  "buffer_store_dwordx4 v[16:19], v3, s[40:43], s71 offen offset:0 sc1\n" \
  "v_mfma_f32_16x16x32_bf16 v[64:67], a[168:171], v[40:43], v[64:67]\n" \
  "v_cvt_pk_bf16_f32 v20, v112, v113\n" \
  "v_mfma_f32_16x16x32_bf16 v[76:79], a[168:171], v[44:47], v[76:79]\n" \
  "v_cvt_pk_bf16_f32 v21, v114, v115\n" \
  "s_waitcnt lgkmcnt(2)\n" \
  "v_mfma_f32_16x16x32_bf16 v[56:59], a[12:15], v[48:51], v[56:59]\n" \
  "ds_read_b128 v[40:43], v9 offset:5120\n" \
  "v_mfma_f32_16x16x32_bf16 v[68:71], a[12:15], v[52:55], v[68:71]\n" \
  "ds_read_b128 v[44:47], v9 offset:25600\n" \
  "v_mfma_f32_16x16x32_bf16 v[60:63], a[92:95], v[48:51], v[60:63]\n" \
  "v_cvt_pk_bf16_f32 v22, v116, v117\n" \
  "v_mfma_f32_16x16x32_bf16 v[72:75], a[92:95], v[52:55], v[72:75]\n" \
  "v_cvt_pk_bf16_f32 v23, v118, v119\n" \
  "v_mfma_f32_16x16x32_bf16 v[64:67], a[172:175], v[48:51], v[64:67]\n" \
  "buffer_store_dwordx4 v[20:23], v3, s[40:43], s71 offen offset:1024 sc1\n" \
  "v_mfma_f32_16x16x32_bf16 v[76:79], a[172:175], v[52:55], v[76:79]\n" \
  "v_cvt_pk_bf16_f32 v24, v108, v109\n" \
  "s_waitcnt lgkmcnt(2)\n" \
  "v_mfma_f32_16x16x32_bf16 v[56:59], a[16:19], v[32:35], v[56:59]\n" \
  "ds_read_b128 v[48:51], v9 offset:6144\n" \
  "v_mfma_f32_16x16x32_bf16 v[68:71], a[16:19], v[36:39], v[68:71]\n" \
  "ds_read_b128 v[52:55], v9 offset:26624\n" \
  "v_mfma_f32_16x16x32_bf16 v[60:63], a[96:99], v[32:35], v[60:63]\n" \
  "v_cvt_pk_bf16_f32 v25, v110, v111\n" \
  "v_mfma_f32_16x16x32_bf16 v[72:75], a[96:99], v[36:39], v[72:75]\n" \
  "v_cvt_pk_bf16_f32 v26, v120, v121\n" \
  "v_mfma_f32_16x16x32_bf16 v[64:67], a[176:179], v[32:35], v[64:67]\n" \
  "v_cvt_pk_bf16_f32 v27, v122, v123\n" \
  "v_mfma_f32_16x16x32_bf16 v[76:79], a[176:179], v[36:39], v[76:79]\n" \
  "buffer_store_dwordx4 v[24:27], v3, s[40:43], s71 offen offset:2048 sc1\n" \
  "s_waitcnt lgkmcnt(2)\n" \
  "v_mfma_f32_16x16x32_bf16 v[56:59], a[20:23], v[40:43], v[56:59]\n" \
  "ds_read_b128 v[32:35], v9 offset:7168\n" \
  "v_mfma_f32_16x16x32_bf16 v[68:71], a[20:23], v[44:47], v[68:71]\n" \
  "ds_read_b128 v[36:39], v9 offset:27648\n" \
  "v_mfma_f32_16x16x32_bf16 v[60:63], a[100:103], v[40:43], v[60:63]\n" \
  "buffer_store_dwordx4 v[16:19], v6, s[40:43], s70 offen sc1\n" \
  "v_mfma_f32_16x16x32_bf16 v[72:75], a[100:103], v[44:47], v[72:75]\n" \
  "v_mfma_f32_16x16x32_bf16 v[64:67], a[180:183], v[40:43], v[64:67]\n" \
  "v_mfma_f32_16x16x32_bf16 v[76:79], a[180:183], v[44:47], v[76:79]\n" \
  "s_waitcnt lgkmcnt(2)\n" \
  "v_mfma_f32_16x16x32_bf16 v[56:59], a[24:27], v[48:51], v[56:59]\n" \
  "ds_read_b128 v[40:43], v9 offset:8192\n" \
  "v_mfma_f32_16x16x32_bf16 v[68:71], a[24:27], v[52:55], v[68:71]\n" \
  "ds_read_b128 v[44:47], v9 offset:28672\n" \
  "v_mfma_f32_16x16x32_bf16 v[60:63], a[104:107], v[48:51], v[60:63]\n" \
  "buffer_store_dwordx4 v[20:23], v6, s[40:43], s70 offen offset:1024 sc1\n" \
  "v_mfma_f32_16x16x32_bf16 v[72:75], a[104:107], v[52:55], v[72:75]\n" \
  "v_mfma_f32_16x16x32_bf16 v[64:67], a[184:187], v[48:51], v[64:67]\n" \
  "v_mfma_f32_16x16x32_bf16 v[76:79], a[184:187], v[52:55], v[76:79]\n" \
  "s_waitcnt lgkmcnt(2)\n" \
  "v_mfma_f32_16x16x32_bf16 v[56:59], a[28:31], v[32:35], v[56:59]\n" \
  "ds_read_b128 v[48:51], v9 offset:9216\n" \
  "v_mfma_f32_16x16x32_bf16 v[68:71], a[28:31], v[36:39], v[68:71]\n" \
  "ds_read_b128 v[52:55], v9 offset:29696\n" \
  "v_mfma_f32_16x16x32_bf16 v[60:63], a[108:111], v[32:35], v[60:63]\n" \
  "buffer_store_dwordx4 v[24:27], v7, s[40:43], s70 offen sc1\n" \
  "v_mfma_f32_16x16x32_bf16 v[72:75], a[108:111], v[36:39], v[72:75]\n" \
  "v_mfma_f32_16x16x32_bf16 v[64:67], a[188:191], v[32:35], v[64:67]\n" \
  "v_mfma_f32_16x16x32_bf16 v[76:79], a[188:191], v[36:39], v[76:79]\n" \
  "s_waitcnt lgkmcnt(2)\n" \
  "v_mfma_f32_16x16x32_bf16 v[56:59], a[32:35], v[40:43], v[56:59]\n" \
  "ds_read_b128 v[32:35], v9 offset:10240\n" \
  "v_mfma_f32_16x16x32_bf16 v[68:71], a[32:35], v[44:47], v[68:71]\n" \
  "ds_read_b128 v[36:39], v9 offset:30720\n" \
  "v_mfma_f32_16x16x32_bf16 v[60:63], a[112:115], v[40:43], v[60:63]\n" \
  "s_add_u32 s72, s68, 4096\n" \
  "v_mfma_f32_16x16x32_bf16 v[72:75], a[112:115], v[44:47], v[72:75]\n" \
  "s_add_u32 s73, s68, 8192\n" \
  "v_mfma_f32_16x16x32_bf16 v[64:67], a[192:195], v[40:43], v[64:67]\n" \
  "s_add_u32 m0, s55, 0\n" \
  "v_mfma_f32_16x16x32_bf16 v[76:79], a[192:195], v[44:47], v[76:79]\n" \
  "buffer_load_dwordx4 v1, s[36:39], s68 offen lds\n" \
  "s_waitcnt lgkmcnt(2)\n" \
  "v_mfma_f32_16x16x32_bf16 v[56:59], a[36:39], v[48:51], v[56:59]\n" \
  "ds_read_b128 v[40:43], v9 offset:11264\n" \
  "v_mfma_f32_16x16x32_bf16 v[68:71], a[36:39], v[52:55], v[68:71]\n" \
  "ds_read_b128 v[44:47], v9 offset:31744\n" \
  "v_mfma_f32_16x16x32_bf16 v[60:63], a[116:119], v[48:51], v[60:63]\n" \
  "buffer_load_dwordx4 v1, s[36:39], s68 offen offset:1024 lds\n" \
  "v_mfma_f32_16x16x32_bf16 v[72:75], a[116:119], v[52:55], v[72:75]\n" \
  "s_lshr_b64 s[76:77], s[52:53], s56\n" \
  "v_mfma_f32_16x16x32_bf16 v[64:67], a[196:199], v[48:51], v[64:67]\n" \
  "s_lshl_b32 s69, s56, 8\n" \
  "v_mfma_f32_16x16x32_bf16 v[76:79], a[196:199], v[52:55], v[76:79]\n" \
  "s_and_b32 s70, s76, 1\n" \
  "s_waitcnt lgkmcnt(2)\n" \
  "v_mfma_f32_16x16x32_bf16 v[56:59], a[40:43], v[32:35], v[56:59]\n" \
  "ds_read_b128 v[48:51], v9 offset:12288\n" \
  "v_mfma_f32_16x16x32_bf16 v[68:71], a[40:43], v[36:39], v[68:71]\n" \
  "ds_read_b128 v[52:55], v9 offset:32768\n" \
  "v_mfma_f32_16x16x32_bf16 v[60:63], a[120:123], v[32:35], v[60:63]\n" \
  "buffer_load_dwordx4 v1, s[36:39], s68 offen offset:2048 lds\n" \
  "v_mfma_f32_16x16x32_bf16 v[72:75], a[120:123], v[36:39], v[72:75]\n" \
  "s_bfe_u32 s71, s76, 0x10001\n" \
  "v_mfma_f32_16x16x32_bf16 v[64:67], a[200:203], v[32:35], v[64:67]\n" \
  "s_add_u32 s71, s71, s69\n" \
  "v_mfma_f32_16x16x32_bf16 v[76:79], a[200:203], v[36:39], v[76:79]\n" \
  "s_add_u32 s69, s69, s70\n" \
  "s_waitcnt lgkmcnt(2)\n" \
  "v_mfma_f32_16x16x32_bf16 v[56:59], a[44:47], v[40:43], v[56:59]\n" \
  "ds_read_b128 v[32:35], v9 offset:13312\n" \
  "v_mfma_f32_16x16x32_bf16 v[68:71], a[44:47], v[44:47], v[68:71]\n" \
  "ds_read_b128 v[36:39], v9 offset:33792\n" \
  "v_mfma_f32_16x16x32_bf16 v[60:63], a[124:127], v[40:43], v[60:63]\n" \
  "buffer_load_dwordx4 v1, s[36:39], s68 offen offset:3072 lds\n" \
  "v_mfma_f32_16x16x32_bf16 v[72:75], a[124:127], v[44:47], v[72:75]\n" \
  "s_add_u32 s71, s71, 0x100\n" \
  "v_mfma_f32_16x16x32_bf16 v[64:67], a[204:207], v[40:43], v[64:67]\n" \
  "s_lshl_b32 s69, s69, 13\n" \
  "v_mfma_f32_16x16x32_bf16 v[76:79], a[204:207], v[44:47], v[76:79]\n" \
  "s_lshl_b32 s71, s71, 13\n" \
  "s_waitcnt lgkmcnt(2)\n" \
  "v_mfma_f32_16x16x32_bf16 v[56:59], a[48:51], v[48:51], v[56:59]\n" \
  "ds_read_b128 v[40:43], v9 offset:14336\n" \
  "v_mfma_f32_16x16x32_bf16 v[68:71], a[48:51], v[52:55], v[68:71]\n" \
  "ds_read_b128 v[44:47], v9 offset:34816\n" \
  "v_mfma_f32_16x16x32_bf16 v[60:63], a[128:131], v[48:51], v[60:63]\n" \
  "s_add_u32 m0, s55, 4096\n" \
  "v_mfma_f32_16x16x32_bf16 v[72:75], a[128:131], v[52:55], v[72:75]\n" \
  "buffer_load_dwordx4 v1, s[36:39], s72 offen lds\n" \
  "v_mfma_f32_16x16x32_bf16 v[64:67], a[208:211], v[48:51], v[64:67]\n" \
  "v_mfma_f32_16x16x32_bf16 v[76:79], a[208:211], v[52:55], v[76:79]\n" \
  "s_waitcnt lgkmcnt(2)\n" \
  "v_mfma_f32_16x16x32_bf16 v[56:59], a[52:55], v[32:35], v[56:59]\n" \
  "ds_read_b128 v[48:51], v9 offset:15360\n" \
  "v_mfma_f32_16x16x32_bf16 v[68:71], a[52:55], v[36:39], v[68:71]\n" \
  "ds_read_b128 v[52:55], v9 offset:35840\n" \
  "v_mfma_f32_16x16x32_bf16 v[60:63], a[132:135], v[32:35], v[60:63]\n" \
  "buffer_load_dwordx4 v1, s[36:39], s72 offen offset:1024 lds\n" \
  "v_mfma_f32_16x16x32_bf16 v[72:75], a[132:135], v[36:39], v[72:75]\n" \
  "s_cmp_eq_u32 s57, 0\n" \
  "v_mfma_f32_16x16x32_bf16 v[64:67], a[212:215], v[32:35], v[64:67]\n" \
  "s_cselect_b32 s70, s69, s71\n" \
  "v_mfma_f32_16x16x32_bf16 v[76:79], a[212:215], v[36:39], v[76:79]\n" \
  "s_xor_b32 s70, s70, 0x2000\n" \
  "s_waitcnt lgkmcnt(2)\n" \
  "v_mfma_f32_16x16x32_bf16 v[56:59], a[56:59], v[40:43], v[56:59]\n" \
  "ds_read_b128 v[32:35], v9 offset:16384\n" \
  "v_mfma_f32_16x16x32_bf16 v[68:71], a[56:59], v[44:47], v[68:71]\n" \
  "ds_read_b128 v[36:39], v9 offset:36864\n" \
  "v_mfma_f32_16x16x32_bf16 v[60:63], a[136:139], v[40:43], v[60:63]\n" \
  "buffer_load_dwordx4 v1, s[36:39], s72 offen offset:2048 lds\n" \
  "v_mfma_f32_16x16x32_bf16 v[72:75], a[136:139], v[44:47], v[72:75]\n" \
  "s_add_u32 s56, s56, 2\n" \
  "v_mfma_f32_16x16x32_bf16 v[64:67], a[216:219], v[40:43], v[64:67]\n" \
  "v_mfma_f32_16x16x32_bf16 v[76:79], a[216:219], v[44:47], v[76:79]\n" \
  "s_waitcnt lgkmcnt(2)\n" \
  "v_mfma_f32_16x16x32_bf16 v[56:59], a[60:63], v[48:51], v[56:59]\n" \
  "ds_read_b128 v[40:43], v9 offset:17408\n" \
  "v_mfma_f32_16x16x32_bf16 v[68:71], a[60:63], v[52:55], v[68:71]\n" \
  "ds_read_b128 v[44:47], v9 offset:37888\n" \
  "v_mfma_f32_16x16x32_bf16 v[60:63], a[140:143], v[48:51], v[60:63]\n" \
  "buffer_load_dwordx4 v1, s[36:39], s72 offen offset:3072 lds\n" \
  "v_mfma_f32_16x16x32_bf16 v[72:75], a[140:143], v[52:55], v[72:75]\n" \
  "v_mfma_f32_16x16x32_bf16 v[64:67], a[220:223], v[48:51], v[64:67]\n" \
  "v_mfma_f32_16x16x32_bf16 v[76:79], a[220:223], v[52:55], v[76:79]\n" \
  "s_waitcnt lgkmcnt(2)\n" \
  "v_mfma_f32_16x16x32_bf16 v[56:59], a[64:67], v[32:35], v[56:59]\n" \
  "ds_read_b128 v[48:51], v9 offset:18432\n" \
  "v_mfma_f32_16x16x32_bf16 v[68:71], a[64:67], v[36:39], v[68:71]\n" \
  "ds_read_b128 v[52:55], v9 offset:38912\n" \
  "v_mfma_f32_16x16x32_bf16 v[60:63], a[144:147], v[32:35], v[60:63]\n" \
  "s_add_u32 m0, s55, 8192\n" \
  "v_mfma_f32_16x16x32_bf16 v[72:75], a[144:147], v[36:39], v[72:75]\n" \
  "buffer_load_dwordx4 v1, s[36:39], s73 offen lds\n" \
  "v_mfma_f32_16x16x32_bf16 v[64:67], a[224:227], v[32:35], v[64:67]\n" \
  "v_mfma_f32_16x16x32_bf16 v[76:79], a[224:227], v[36:39], v[76:79]\n" \
  "s_waitcnt lgkmcnt(2)\n" \
  "v_mfma_f32_16x16x32_bf16 v[56:59], a[68:71], v[40:43], v[56:59]\n" \
  "ds_read_b128 v[32:35], v9 offset:19456\n" \
  "v_mfma_f32_16x16x32_bf16 v[68:71], a[68:71], v[44:47], v[68:71]\n" \
  "ds_read_b128 v[36:39], v9 offset:39936\n" \
  "v_mfma_f32_16x16x32_bf16 v[60:63], a[148:151], v[40:43], v[60:63]\n" \
  "buffer_load_dwordx4 v1, s[36:39], s73 offen offset:1024 lds\n" \
  "v_mfma_f32_16x16x32_bf16 v[72:75], a[148:151], v[44:47], v[72:75]\n" \
  "s_add_u32 s68, s68, 40960\n" \
  "v_mfma_f32_16x16x32_bf16 v[64:67], a[228:231], v[40:43], v[64:67]\n" \
  "v_mfma_f32_16x16x32_bf16 v[76:79], a[228:231], v[44:47], v[76:79]\n" \
  "s_waitcnt vmcnt(10) lgkmcnt(0)\n" \
  "s_barrier\n" \
  "v_mfma_f32_16x16x32_bf16 v[56:59], a[72:75], v[48:51], v[56:59]\n" \
  "ds_read_b128 v[40:43], v10 offset:0\n" \
  "v_mfma_f32_16x16x32_bf16 v[68:71], a[72:75], v[52:55], v[68:71]\n" \
  "ds_read_b128 v[44:47], v10 offset:20480\n" \
  "v_mfma_f32_16x16x32_bf16 v[60:63], a[152:155], v[48:51], v[60:63]\n" \
  "v_mfma_f32_16x16x32_bf16 v[72:75], a[152:155], v[52:55], v[72:75]\n" \
  "v_mfma_f32_16x16x32_bf16 v[64:67], a[232:235], v[48:51], v[64:67]\n" \
  "v_mfma_f32_16x16x32_bf16 v[76:79], a[232:235], v[52:55], v[76:79]\n" \
  "v_mfma_f32_16x16x32_bf16 v[56:59], a[76:79], v[32:35], v[56:59]\n" \
  "ds_read_b128 v[48:51], v10 offset:1024\n" \
  "v_mfma_f32_16x16x32_bf16 v[68:71], a[76:79], v[36:39], v[68:71]\n" \
  "ds_read_b128 v[52:55], v10 offset:21504\n" \
  "v_mfma_f32_16x16x32_bf16 v[60:63], a[156:159], v[32:35], v[60:63]\n" \
  "s_cmp_eq_u32 s54, 60\n" \
  "v_mfma_f32_16x16x32_bf16 v[72:75], a[156:159], v[36:39], v[72:75]\n" \
  "v_mfma_f32_16x16x32_bf16 v[64:67], a[236:239], v[32:35], v[64:67]\n" \
  "v_mfma_f32_16x16x32_bf16 v[76:79], a[236:239], v[36:39], v[76:79]\n" \
  "s_waitcnt lgkmcnt(2)\n" \
  "v_mfma_f32_16x16x32_bf16 v[100:103], a[0:3], v[40:43], 0\n" \
  "ds_read_b128 v[32:35], v10 offset:2048\n" \
  "v_mfma_f32_16x16x32_bf16 v[112:115], a[0:3], v[44:47], 0\n" \
  "ds_read_b128 v[36:39], v10 offset:22528\n" \
  "v_mfma_f32_16x16x32_bf16 v[104:107], a[80:83], v[40:43], 0\n" \
  "buffer_atomic_add v11, v8, s[40:43], s74 offen\n" \
  "v_mfma_f32_16x16x32_bf16 v[116:119], a[80:83], v[44:47], 0\n" \
  "s_add_u32 s74, s74, 0x2200\n" \
  "v_mfma_f32_16x16x32_bf16 v[108:111], a[160:163], v[40:43], 0\n" \
  "s_and_b32 s74, s74, 0xffffdfff\n" \
  "v_mfma_f32_16x16x32_bf16 v[120:123], a[160:163], v[44:47], 0\n" \
  "s_waitcnt lgkmcnt(2)\n" \
  "v_mfma_f32_16x16x32_bf16 v[100:103], a[4:7], v[48:51], v[100:103]\n" \
  "ds_read_b128 v[40:43], v10 offset:3072\n" \
  "v_mfma_f32_16x16x32_bf16 v[112:115], a[4:7], v[52:55], v[112:115]\n" \
  "ds_read_b128 v[44:47], v10 offset:23552\n" \
  "v_mfma_f32_16x16x32_bf16 v[104:107], a[84:87], v[48:51], v[104:107]\n" \
  "buffer_store_dwordx4 v[12:15], v2, s[40:43], s69 offen sc1\n" \
  "v_mfma_f32_16x16x32_bf16 v[116:119], a[84:87], v[52:55], v[116:119]\n" \
  "v_cvt_pk_bf16_f32 v16, v56, v57\n" \
  "v_mfma_f32_16x16x32_bf16 v[108:111], a[164:167], v[48:51], v[108:111]\n" \
  "v_cvt_pk_bf16_f32 v17, v58, v59\n" \
  "v_mfma_f32_16x16x32_bf16 v[120:123], a[164:167], v[52:55], v[120:123]\n" \
  "v_cvt_pk_bf16_f32 v18, v60, v61\n" \
  "s_waitcnt lgkmcnt(2)\n" \
  "v_mfma_f32_16x16x32_bf16 v[100:103], a[8:11], v[32:35], v[100:103]\n" \
  "ds_read_b128 v[48:51], v10 offset:4096\n" \
  "v_mfma_f32_16x16x32_bf16 v[112:115], a[8:11], v[36:39], v[112:115]\n" \
  "ds_read_b128 v[52:55], v10 offset:24576\n" \
  "v_mfma_f32_16x16x32_bf16 v[104:107], a[88:91], v[32:35], v[104:107]\n" \
  "v_cvt_pk_bf16_f32 v19, v62, v63\n" \
  "v_mfma_f32_16x16x32_bf16 v[116:119], a[88:91], v[36:39], v[116:119]\n" \
  "buffer_store_dwordx4 v[16:19], v3, s[40:43], s71 offen offset:0 sc1\n" \
  "v_mfma_f32_16x16x32_bf16 v[108:111], a[168:171], v[32:35], v[108:111]\n" \
  "v_cvt_pk_bf16_f32 v20, v68, v69\n" \
  "v_mfma_f32_16x16x32_bf16 v[120:123], a[168:171], v[36:39], v[120:123]\n" \
  "v_cvt_pk_bf16_f32 v21, v70, v71\n" \
  "s_waitcnt lgkmcnt(2)\n" \
  "v_mfma_f32_16x16x32_bf16 v[100:103], a[12:15], v[40:43], v[100:103]\n" \
  "ds_read_b128 v[32:35], v10 offset:5120\n" \
  "v_mfma_f32_16x16x32_bf16 v[112:115], a[12:15], v[44:47], v[112:115]\n" \
  "ds_read_b128 v[36:39], v10 offset:25600\n" \
  "v_mfma_f32_16x16x32_bf16 v[104:107], a[92:95], v[40:43], v[104:107]\n" \
  "v_cvt_pk_bf16_f32 v22, v72, v73\n" \
  "v_mfma_f32_16x16x32_bf16 v[116:119], a[92:95], v[44:47], v[116:119]\n" \
  "v_cvt_pk_bf16_f32 v23, v74, v75\n" \
  "v_mfma_f32_16x16x32_bf16 v[108:111], a[172:175], v[40:43], v[108:111]\n" \
  "buffer_store_dwordx4 v[20:23], v3, s[40:43], s71 offen offset:1024 sc1\n" \
  "v_mfma_f32_16x16x32_bf16 v[120:123], a[172:175], v[44:47], v[120:123]\n" \
  "v_cvt_pk_bf16_f32 v24, v64, v65\n" \
  "s_waitcnt lgkmcnt(2)\n" \
  "v_mfma_f32_16x16x32_bf16 v[100:103], a[16:19], v[48:51], v[100:103]\n" \
  "ds_read_b128 v[40:43], v10 offset:6144\n" \
  "v_mfma_f32_16x16x32_bf16 v[112:115], a[16:19], v[52:55], v[112:115]\n" \
  "ds_read_b128 v[44:47], v10 offset:26624\n" \
  "v_mfma_f32_16x16x32_bf16 v[104:107], a[96:99], v[48:51], v[104:107]\n" \
  "v_cvt_pk_bf16_f32 v25, v66, v67\n" \
  "v_mfma_f32_16x16x32_bf16 v[116:119], a[96:99], v[52:55], v[116:119]\n" \
  "v_cvt_pk_bf16_f32 v26, v76, v77\n" \
  "v_mfma_f32_16x16x32_bf16 v[108:111], a[176:179], v[48:51], v[108:111]\n" \
  "v_cvt_pk_bf16_f32 v27, v78, v79\n" \
  "v_mfma_f32_16x16x32_bf16 v[120:123], a[176:179], v[52:55], v[120:123]\n" \
  "buffer_store_dwordx4 v[24:27], v3, s[40:43], s71 offen offset:2048 sc1\n" \
  "s_waitcnt lgkmcnt(2)\n" \
  "v_mfma_f32_16x16x32_bf16 v[100:103], a[20:23], v[32:35], v[100:103]\n" \
  "ds_read_b128 v[48:51], v10 offset:7168\n" \
  "v_mfma_f32_16x16x32_bf16 v[112:115], a[20:23], v[36:39], v[112:115]\n" \
  "ds_read_b128 v[52:55], v10 offset:27648\n" \
  "v_mfma_f32_16x16x32_bf16 v[104:107], a[100:103], v[32:35], v[104:107]\n" \
  "buffer_store_dwordx4 v[16:19], v6, s[40:43], s70 offen sc1\n" \
  "v_mfma_f32_16x16x32_bf16 v[116:119], a[100:103], v[36:39], v[116:119]\n" \
  "v_mfma_f32_16x16x32_bf16 v[108:111], a[180:183], v[32:35], v[108:111]\n" \
  "v_mfma_f32_16x16x32_bf16 v[120:123], a[180:183], v[36:39], v[120:123]\n" \
  "s_waitcnt lgkmcnt(2)\n" \
  "v_mfma_f32_16x16x32_bf16 v[100:103], a[24:27], v[40:43], v[100:103]\n" \
  "ds_read_b128 v[32:35], v10 offset:8192\n" \
  "v_mfma_f32_16x16x32_bf16 v[112:115], a[24:27], v[44:47], v[112:115]\n" \
  "ds_read_b128 v[36:39], v10 offset:28672\n" \
  "v_mfma_f32_16x16x32_bf16 v[104:107], a[104:107], v[40:43], v[104:107]\n" \
  "buffer_store_dwordx4 v[20:23], v6, s[40:43], s70 offen offset:1024 sc1\n" \
  "v_mfma_f32_16x16x32_bf16 v[116:119], a[104:107], v[44:47], v[116:119]\n" \
  "v_mfma_f32_16x16x32_bf16 v[108:111], a[184:187], v[40:43], v[108:111]\n" \
  "v_mfma_f32_16x16x32_bf16 v[120:123], a[184:187], v[44:47], v[120:123]\n" \
  "s_waitcnt lgkmcnt(2)\n" \
  "v_mfma_f32_16x16x32_bf16 v[100:103], a[28:31], v[48:51], v[100:103]\n" \
  "ds_read_b128 v[40:43], v10 offset:9216\n" \
  "v_mfma_f32_16x16x32_bf16 v[112:115], a[28:31], v[52:55], v[112:115]\n" \
  "ds_read_b128 v[44:47], v10 offset:29696\n" \
  "v_mfma_f32_16x16x32_bf16 v[104:107], a[108:111], v[48:51], v[104:107]\n" \
  "buffer_store_dwordx4 v[24:27], v7, s[40:43], s70 offen sc1\n" \
  "v_mfma_f32_16x16x32_bf16 v[116:119], a[108:111], v[52:55], v[116:119]\n" \
  "v_mfma_f32_16x16x32_bf16 v[108:111], a[188:191], v[48:51], v[108:111]\n" \
  "v_mfma_f32_16x16x32_bf16 v[120:123], a[188:191], v[52:55], v[120:123]\n" \
  "s_waitcnt lgkmcnt(2)\n" \
  "v_mfma_f32_16x16x32_bf16 v[100:103], a[32:35], v[32:35], v[100:103]\n" \
  "ds_read_b128 v[48:51], v10 offset:10240\n" \
  "v_mfma_f32_16x16x32_bf16 v[112:115], a[32:35], v[36:39], v[112:115]\n" \
  "ds_read_b128 v[52:55], v10 offset:30720\n" \
  "v_mfma_f32_16x16x32_bf16 v[104:107], a[112:115], v[32:35], v[104:107]\n" \
  "s_add_u32 s72, s68, 4096\n" \
  "v_mfma_f32_16x16x32_bf16 v[116:119], a[112:115], v[36:39], v[116:119]\n" \
  "s_add_u32 s73, s68, 8192\n" \
  "v_mfma_f32_16x16x32_bf16 v[108:111], a[192:195], v[32:35], v[108:111]\n" \
  "s_add_u32 m0, s55, 40960\n" \
  "v_mfma_f32_16x16x32_bf16 v[120:123], a[192:195], v[36:39], v[120:123]\n" \
  "buffer_load_dwordx4 v1, s[36:39], s68 offen lds\n" \
  "s_waitcnt lgkmcnt(2)\n" \
  "v_mfma_f32_16x16x32_bf16 v[100:103], a[36:39], v[40:43], v[100:103]\n" \
  "ds_read_b128 v[32:35], v10 offset:11264\n" \
  "v_mfma_f32_16x16x32_bf16 v[112:115], a[36:39], v[44:47], v[112:115]\n" \
  "ds_read_b128 v[36:39], v10 offset:31744\n" \
  "v_mfma_f32_16x16x32_bf16 v[104:107], a[116:119], v[40:43], v[104:107]\n" \
  "buffer_load_dwordx4 v1, s[36:39], s68 offen offset:1024 lds\n" \
  "v_mfma_f32_16x16x32_bf16 v[116:119], a[116:119], v[44:47], v[116:119]\n" \
  "s_lshr_b64 s[76:77], s[52:53], s56\n" \
  "v_mfma_f32_16x16x32_bf16 v[108:111], a[196:199], v[40:43], v[108:111]\n" \
  "s_lshl_b32 s69, s56, 8\n" \
  "v_mfma_f32_16x16x32_bf16 v[120:123], a[196:199], v[44:47], v[120:123]\n" \
  "s_and_b32 s70, s76, 1\n" \
  "s_waitcnt lgkmcnt(2)\n" \
  "v_mfma_f32_16x16x32_bf16 v[100:103], a[40:43], v[48:51], v[100:103]\n" \
  "ds_read_b128 v[40:43], v10 offset:12288\n" \
  "v_mfma_f32_16x16x32_bf16 v[112:115], a[40:43], v[52:55], v[112:115]\n" \
  "ds_read_b128 v[44:47], v10 offset:32768\n" \
  "v_mfma_f32_16x16x32_bf16 v[104:107], a[120:123], v[48:51], v[104:107]\n" \
  "buffer_load_dwordx4 v1, s[36:39], s68 offen offset:2048 lds\n" \
  "v_mfma_f32_16x16x32_bf16 v[116:119], a[120:123], v[52:55], v[116:119]\n" \
  "s_bfe_u32 s71, s76, 0x10001\n" \
  "v_mfma_f32_16x16x32_bf16 v[108:111], a[200:203], v[48:51], v[108:111]\n" \
  "s_add_u32 s71, s71, s69\n" \
  "v_mfma_f32_16x16x32_bf16 v[120:123], a[200:203], v[52:55], v[120:123]\n" \
  "s_add_u32 s69, s69, s70\n" \
  "s_waitcnt lgkmcnt(2)\n" \
  "v_mfma_f32_16x16x32_bf16 v[100:103], a[44:47], v[32:35], v[100:103]\n" \
  "ds_read_b128 v[48:51], v10 offset:13312\n" \
  "v_mfma_f32_16x16x32_bf16 v[112:115], a[44:47], v[36:39], v[112:115]\n" \
  "ds_read_b128 v[52:55], v10 offset:33792\n" \
  "v_mfma_f32_16x16x32_bf16 v[104:107], a[124:127], v[32:35], v[104:107]\n" \
  "buffer_load_dwordx4 v1, s[36:39], s68 offen offset:3072 lds\n" \
  "v_mfma_f32_16x16x32_bf16 v[116:119], a[124:127], v[36:39], v[116:119]\n" \
  "s_add_u32 s71, s71, 0x100\n" \
  "v_mfma_f32_16x16x32_bf16 v[108:111], a[204:207], v[32:35], v[108:111]\n" \
  "s_lshl_b32 s69, s69, 13\n" \
  "v_mfma_f32_16x16x32_bf16 v[120:123], a[204:207], v[36:39], v[120:123]\n" \
  "s_lshl_b32 s71, s71, 13\n" \
  "s_waitcnt lgkmcnt(2)\n" \
  "v_mfma_f32_16x16x32_bf16 v[100:103], a[48:51], v[40:43], v[100:103]\n" \
  "ds_read_b128 v[32:35], v10 offset:14336\n" \
  "v_mfma_f32_16x16x32_bf16 v[112:115], a[48:51], v[44:47], v[112:115]\n" \
  "ds_read_b128 v[36:39], v10 offset:34816\n" \
  "v_mfma_f32_16x16x32_bf16 v[104:107], a[128:131], v[40:43], v[104:107]\n" \
  "s_add_u32 m0, s55, 45056\n" \
  "v_mfma_f32_16x16x32_bf16 v[116:119], a[128:131], v[44:47], v[116:119]\n" \
  "buffer_load_dwordx4 v1, s[36:39], s72 offen lds\n" \
  "v_mfma_f32_16x16x32_bf16 v[108:111], a[208:211], v[40:43], v[108:111]\n" \
  "v_mfma_f32_16x16x32_bf16 v[120:123], a[208:211], v[44:47], v[120:123]\n" \
  "s_waitcnt lgkmcnt(2)\n" \
  "v_mfma_f32_16x16x32_bf16 v[100:103], a[52:55], v[48:51], v[100:103]\n" \
  "ds_read_b128 v[40:43], v10 offset:15360\n" \
  "v_mfma_f32_16x16x32_bf16 v[112:115], a[52:55], v[52:55], v[112:115]\n" \
  "ds_read_b128 v[44:47], v10 offset:35840\n" \
  "v_mfma_f32_16x16x32_bf16 v[104:107], a[132:135], v[48:51], v[104:107]\n" \
  "buffer_load_dwordx4 v1, s[36:39], s72 offen offset:1024 lds\n" \
  "v_mfma_f32_16x16x32_bf16 v[116:119], a[132:135], v[52:55], v[116:119]\n" \
  "s_cmp_eq_u32 s57, 0\n" \
  "v_mfma_f32_16x16x32_bf16 v[108:111], a[212:215], v[48:51], v[108:111]\n" \
  "s_cselect_b32 s70, s69, s71\n" \
  "v_mfma_f32_16x16x32_bf16 v[120:123], a[212:215], v[52:55], v[120:123]\n" \
  "s_xor_b32 s70, s70, 0x2000\n" \
  "s_waitcnt lgkmcnt(2)\n" \
  "v_mfma_f32_16x16x32_bf16 v[100:103], a[56:59], v[32:35], v[100:103]\n" \
  "ds_read_b128 v[48:51], v10 offset:16384\n" \
  "v_mfma_f32_16x16x32_bf16 v[112:115], a[56:59], v[36:39], v[112:115]\n" \
  "ds_read_b128 v[52:55], v10 offset:36864\n" \
  "v_mfma_f32_16x16x32_bf16 v[104:107], a[136:139], v[32:35], v[104:107]\n" \
  "buffer_load_dwordx4 v1, s[36:39], s72 offen offset:2048 lds\n" \
  "v_mfma_f32_16x16x32_bf16 v[116:119], a[136:139], v[36:39], v[116:119]\n" \
  "s_add_u32 s56, s56, 2\n" \
  "v_mfma_f32_16x16x32_bf16 v[108:111], a[216:219], v[32:35], v[108:111]\n" \
  "v_mfma_f32_16x16x32_bf16 v[120:123], a[216:219], v[36:39], v[120:123]\n" \
  "s_waitcnt lgkmcnt(2)\n" \
  "v_mfma_f32_16x16x32_bf16 v[100:103], a[60:63], v[40:43], v[100:103]\n" \
  "ds_read_b128 v[32:35], v10 offset:17408\n" \
  "v_mfma_f32_16x16x32_bf16 v[112:115], a[60:63], v[44:47], v[112:115]\n" \
  "ds_read_b128 v[36:39], v10 offset:37888\n" \
  "v_mfma_f32_16x16x32_bf16 v[104:107], a[140:143], v[40:43], v[104:107]\n" \
  "buffer_load_dwordx4 v1, s[36:39], s72 offen offset:3072 lds\n" \
  "v_mfma_f32_16x16x32_bf16 v[116:119], a[140:143], v[44:47], v[116:119]\n" \
  "v_mfma_f32_16x16x32_bf16 v[108:111], a[220:223], v[40:43], v[108:111]\n" \
  "v_mfma_f32_16x16x32_bf16 v[120:123], a[220:223], v[44:47], v[120:123]\n" \
  "s_waitcnt lgkmcnt(2)\n" \
  "v_mfma_f32_16x16x32_bf16 v[100:103], a[64:67], v[48:51], v[100:103]\n" \
  "ds_read_b128 v[40:43], v10 offset:18432\n" \
  "v_mfma_f32_16x16x32_bf16 v[112:115], a[64:67], v[52:55], v[112:115]\n" \
  "ds_read_b128 v[44:47], v10 offset:38912\n" \
  "v_mfma_f32_16x16x32_bf16 v[104:107], a[144:147], v[48:51], v[104:107]\n" \
  "s_add_u32 m0, s55, 49152\n" \
  "v_mfma_f32_16x16x32_bf16 v[116:119], a[144:147], v[52:55], v[116:119]\n" \
  "buffer_load_dwordx4 v1, s[36:39], s73 offen lds\n" \
  "v_mfma_f32_16x16x32_bf16 v[108:111], a[224:227], v[48:51], v[108:111]\n" \
  "v_mfma_f32_16x16x32_bf16 v[120:123], a[224:227], v[52:55], v[120:123]\n" \
  "s_waitcnt lgkmcnt(2)\n" \
  "v_mfma_f32_16x16x32_bf16 v[100:103], a[68:71], v[32:35], v[100:103]\n" \
  "ds_read_b128 v[48:51], v10 offset:19456\n" \
  "v_mfma_f32_16x16x32_bf16 v[112:115], a[68:71], v[36:39], v[112:115]\n" \
  "ds_read_b128 v[52:55], v10 offset:39936\n" \
  "v_mfma_f32_16x16x32_bf16 v[104:107], a[148:151], v[32:35], v[104:107]\n" \
  "buffer_load_dwordx4 v1, s[36:39], s73 offen offset:1024 lds\n" \
  "v_mfma_f32_16x16x32_bf16 v[116:119], a[148:151], v[36:39], v[116:119]\n" \
  "s_add_u32 s68, s68, 40960\n" \
  "v_mfma_f32_16x16x32_bf16 v[108:111], a[228:231], v[32:35], v[108:111]\n" \
  "v_mfma_f32_16x16x32_bf16 v[120:123], a[228:231], v[36:39], v[120:123]\n" \
  "s_waitcnt vmcnt(10) lgkmcnt(0)\n" \
  "s_barrier\n" \
  "v_mfma_f32_16x16x32_bf16 v[100:103], a[72:75], v[40:43], v[100:103]\n" \
  "v_mfma_f32_16x16x32_bf16 v[112:115], a[72:75], v[44:47], v[112:115]\n" \
  "v_mfma_f32_16x16x32_bf16 v[104:107], a[152:155], v[40:43], v[104:107]\n" \
  "v_mfma_f32_16x16x32_bf16 v[116:119], a[152:155], v[44:47], v[116:119]\n" \
  "v_mfma_f32_16x16x32_bf16 v[108:111], a[232:235], v[40:43], v[108:111]\n" \
  "v_mfma_f32_16x16x32_bf16 v[120:123], a[232:235], v[44:47], v[120:123]\n" \
  "v_mfma_f32_16x16x32_bf16 v[100:103], a[76:79], v[48:51], v[100:103]\n" \
  "s_cmp_eq_u32 s54, 60\n" \
  "v_mfma_f32_16x16x32_bf16 v[112:115], a[76:79], v[52:55], v[112:115]\n" \
  "v_mfma_f32_16x16x32_bf16 v[104:107], a[156:159], v[48:51], v[104:107]\n" \
  "v_mfma_f32_16x16x32_bf16 v[116:119], a[156:159], v[52:55], v[116:119]\n" \
  "v_mfma_f32_16x16x32_bf16 v[108:111], a[236:239], v[48:51], v[108:111]\n" \
  "v_mfma_f32_16x16x32_bf16 v[120:123], a[236:239], v[52:55], v[120:123]\n" \
  "LPE_%=:\n" \
  "buffer_atomic_add v11, v8, s[40:43], s74 offen\n" \
  "s_add_u32 s74, s74, 0x2200\n" \
  "s_and_b32 s74, s74, 0xffffdfff\n" \
  "buffer_store_dwordx4 v[12:15], v2, s[40:43], s69 offen sc1\n" \
  "s_nop 3\n" \
  "v_cvt_pk_bf16_f32 v16, v100, v101\n" \
  "v_cvt_pk_bf16_f32 v17, v102, v103\n" \
  "v_cvt_pk_bf16_f32 v18, v104, v105\n" \
  "v_cvt_pk_bf16_f32 v19, v106, v107\n" \
  "v_cvt_pk_bf16_f32 v20, v112, v113\n" \
  "v_cvt_pk_bf16_f32 v21, v114, v115\n" \
  "v_cvt_pk_bf16_f32 v22, v116, v117\n" \
  "v_cvt_pk_bf16_f32 v23, v118, v119\n" \
  "v_cvt_pk_bf16_f32 v24, v108, v109\n" \
  "v_cvt_pk_bf16_f32 v25, v110, v111\n" \
  "v_cvt_pk_bf16_f32 v26, v120, v121\n" \
  "v_cvt_pk_bf16_f32 v27, v122, v123\n" \
  "buffer_store_dwordx4 v[16:19], v3, s[40:43], s71 offen offset:0 sc1\n" \
  "buffer_store_dwordx4 v[20:23], v3, s[40:43], s71 offen offset:1024 sc1\n" \
  "buffer_store_dwordx4 v[24:27], v3, s[40:43], s71 offen offset:2048 sc1\n" \
  "buffer_store_dwordx4 v[16:19], v6, s[40:43], s70 offen sc1\n" \
  "buffer_store_dwordx4 v[20:23], v6, s[40:43], s70 offen offset:1024 sc1\n" \
  "buffer_store_dwordx4 v[24:27], v7, s[40:43], s70 offen sc1\n" \
  "s_waitcnt vmcnt(0) lgkmcnt(0)\n" \
  "s_barrier\n" \
  "buffer_atomic_add v11, v8, s[40:43], s74 offen\n"
#define PROJQ_CLOBBERS \
  "v12", "v13", "v14", "v15", "v16", "v17", "v18", "v19", "v20", "v21", "v22", "v23", "v24", "v25", "v26", "v27", "v28", "v29", "v30", "v31", "v32", \
  "v33", "v34", "v35", "v36", "v37", "v38", "v39", "v40", "v41", "v42", "v43", "v44", "v45", "v46", "v47", "v48", "v49", "v50", "v51", "v52", "v53", \
  "v54", "v55", "v56", "v57", "v58", "v59", "v60", "v61", "v62", "v63", "v64", "v65", "v66", "v67", "v68", "v69", "v70", "v71", "v72", "v73", "v74", \
  "v75", "v76", "v77", "v78", "v79", "v100", "v101", "v102", "v103", "v104", "v105", "v106", "v107", "v108", "v109", "v110", "v111", "v112", "v113", \
  "v114", "v115", "v116", "v117", "v118", "v119", "v120", "v121", "v122", "v123", "s68", "s69", "s70", "s71", "s72", "s73", "s74", "s75", "s76", "s77"
#define PROJQ_ASM \
  "s_waitcnt vmcnt(0)\n" \
  "s_barrier\n" \
  "ds_read_b128 v[32:35], v0 offset:0\n" \
  "ds_read_b128 v[36:39], v0 offset:20480\n" \
  "ds_read_b128 v[40:43], v0 offset:1024\n" \
  "ds_read_b128 v[44:47], v0 offset:21504\n" \
  "v_mov_b32 v12, -1\n" \
  "v_mov_b32 v13, -1\n" \
  "v_mov_b32 v14, -1\n" \
  "v_mov_b32 v15, -1\n" \
  "v_mov_b32 v11, 1\n" \
  "v_add_u32 v9, 40960, v0\n" \
  "v_add_u32 v10, 81920, v0\n" \
  "s_mul_i32 s68, s54, 40960\n" \
  "s_add_u32 s68, s68, 81920\n" \
  "s_lshr_b32 s69, s54, 4\n" \
  "s_lshl_b32 s69, s69, 13\n" \
  "s_lshl_b32 s74, s54, 9\n" \
  "s_add_u32 s74, s74, s69\n" \
  "s_waitcnt lgkmcnt(2)\n" \
  "v_mfma_f32_16x16x32_bf16 v[56:59], a[0:3], v[32:35], 0\n" \
  "ds_read_b128 v[48:51], v0 offset:2048\n" \
  "v_mfma_f32_16x16x32_bf16 v[68:71], a[0:3], v[36:39], 0\n" \
  "ds_read_b128 v[52:55], v0 offset:22528\n" \
  "v_mfma_f32_16x16x32_bf16 v[60:63], a[80:83], v[32:35], 0\n" \
  "v_mfma_f32_16x16x32_bf16 v[72:75], a[80:83], v[36:39], 0\n" \
  "v_mfma_f32_16x16x32_bf16 v[64:67], a[160:163], v[32:35], 0\n" \
  "v_mfma_f32_16x16x32_bf16 v[76:79], a[160:163], v[36:39], 0\n" \
  "s_waitcnt lgkmcnt(2)\n" \
  "v_mfma_f32_16x16x32_bf16 v[56:59], a[4:7], v[40:43], v[56:59]\n" \
  "ds_read_b128 v[32:35], v0 offset:3072\n" \
  "v_mfma_f32_16x16x32_bf16 v[68:71], a[4:7], v[44:47], v[68:71]\n" \
  "ds_read_b128 v[36:39], v0 offset:23552\n" \
  "v_mfma_f32_16x16x32_bf16 v[60:63], a[84:87], v[40:43], v[60:63]\n" \
  "v_mfma_f32_16x16x32_bf16 v[72:75], a[84:87], v[44:47], v[72:75]\n" \
  "v_mfma_f32_16x16x32_bf16 v[64:67], a[164:167], v[40:43], v[64:67]\n" \
  "v_mfma_f32_16x16x32_bf16 v[76:79], a[164:167], v[44:47], v[76:79]\n" \
  "s_waitcnt lgkmcnt(2)\n" \
  "v_mfma_f32_16x16x32_bf16 v[56:59], a[8:11], v[48:51], v[56:59]\n" \
  "ds_read_b128 v[40:43], v0 offset:4096\n" \
  "v_mfma_f32_16x16x32_bf16 v[68:71], a[8:11], v[52:55], v[68:71]\n" \
  "ds_read_b128 v[44:47], v0 offset:24576\n" \
  "v_mfma_f32_16x16x32_bf16 v[60:63], a[88:91], v[48:51], v[60:63]\n" \
  "v_mfma_f32_16x16x32_bf16 v[72:75], a[88:91], v[52:55], v[72:75]\n" \
  "v_mfma_f32_16x16x32_bf16 v[64:67], a[168:171], v[48:51], v[64:67]\n" \
  "v_mfma_f32_16x16x32_bf16 v[76:79], a[168:171], v[52:55], v[76:79]\n" \
  "s_waitcnt lgkmcnt(2)\n" \
  "v_mfma_f32_16x16x32_bf16 v[56:59], a[12:15], v[32:35], v[56:59]\n" \
  "ds_read_b128 v[48:51], v0 offset:5120\n" \
  "v_mfma_f32_16x16x32_bf16 v[68:71], a[12:15], v[36:39], v[68:71]\n" \
  "ds_read_b128 v[52:55], v0 offset:25600\n" \
  "v_mfma_f32_16x16x32_bf16 v[60:63], a[92:95], v[32:35], v[60:63]\n" \
  "v_mfma_f32_16x16x32_bf16 v[72:75], a[92:95], v[36:39], v[72:75]\n" \
  "v_mfma_f32_16x16x32_bf16 v[64:67], a[172:175], v[32:35], v[64:67]\n" \
  "v_mfma_f32_16x16x32_bf16 v[76:79], a[172:175], v[36:39], v[76:79]\n" \
  "s_waitcnt lgkmcnt(2)\n" \
  "v_mfma_f32_16x16x32_bf16 v[56:59], a[16:19], v[40:43], v[56:59]\n" \
  "ds_read_b128 v[32:35], v0 offset:6144\n" \
  "v_mfma_f32_16x16x32_bf16 v[68:71], a[16:19], v[44:47], v[68:71]\n" \
  "ds_read_b128 v[36:39], v0 offset:26624\n" \
  "v_mfma_f32_16x16x32_bf16 v[60:63], a[96:99], v[40:43], v[60:63]\n" \
  "v_mfma_f32_16x16x32_bf16 v[72:75], a[96:99], v[44:47], v[72:75]\n" \
  "v_mfma_f32_16x16x32_bf16 v[64:67], a[176:179], v[40:43], v[64:67]\n" \
  "v_mfma_f32_16x16x32_bf16 v[76:79], a[176:179], v[44:47], v[76:79]\n" \
  "s_waitcnt lgkmcnt(2)\n" \
  "v_mfma_f32_16x16x32_bf16 v[56:59], a[20:23], v[48:51], v[56:59]\n" \
  "ds_read_b128 v[40:43], v0 offset:7168\n" \
  "v_mfma_f32_16x16x32_bf16 v[68:71], a[20:23], v[52:55], v[68:71]\n" \
  "ds_read_b128 v[44:47], v0 offset:27648\n" \
  "v_mfma_f32_16x16x32_bf16 v[60:63], a[100:103], v[48:51], v[60:63]\n" \
  "v_mfma_f32_16x16x32_bf16 v[72:75], a[100:103], v[52:55], v[72:75]\n" \
  "v_mfma_f32_16x16x32_bf16 v[64:67], a[180:183], v[48:51], v[64:67]\n" \
  "v_mfma_f32_16x16x32_bf16 v[76:79], a[180:183], v[52:55], v[76:79]\n" \
  "s_waitcnt lgkmcnt(2)\n" \
  "v_mfma_f32_16x16x32_bf16 v[56:59], a[24:27], v[32:35], v[56:59]\n" \
  "ds_read_b128 v[48:51], v0 offset:8192\n" \
  "v_mfma_f32_16x16x32_bf16 v[68:71], a[24:27], v[36:39], v[68:71]\n" \
  "ds_read_b128 v[52:55], v0 offset:28672\n" \
  "v_mfma_f32_16x16x32_bf16 v[60:63], a[104:107], v[32:35], v[60:63]\n" \
  "v_mfma_f32_16x16x32_bf16 v[72:75], a[104:107], v[36:39], v[72:75]\n" \
  "v_mfma_f32_16x16x32_bf16 v[64:67], a[184:187], v[32:35], v[64:67]\n" \
  "v_mfma_f32_16x16x32_bf16 v[76:79], a[184:187], v[36:39], v[76:79]\n" \
  "s_waitcnt lgkmcnt(2)\n" \
  "v_mfma_f32_16x16x32_bf16 v[56:59], a[28:31], v[40:43], v[56:59]\n" \
  "ds_read_b128 v[32:35], v0 offset:9216\n" \
  "v_mfma_f32_16x16x32_bf16 v[68:71], a[28:31], v[44:47], v[68:71]\n" \
  "ds_read_b128 v[36:39], v0 offset:29696\n" \
  "v_mfma_f32_16x16x32_bf16 v[60:63], a[108:111], v[40:43], v[60:63]\n" \
  "v_mfma_f32_16x16x32_bf16 v[72:75], a[108:111], v[44:47], v[72:75]\n" \
  "v_mfma_f32_16x16x32_bf16 v[64:67], a[188:191], v[40:43], v[64:67]\n" \
  "v_mfma_f32_16x16x32_bf16 v[76:79], a[188:191], v[44:47], v[76:79]\n" \
  "s_waitcnt lgkmcnt(2)\n" \
  "v_mfma_f32_16x16x32_bf16 v[56:59], a[32:35], v[48:51], v[56:59]\n" \
  "ds_read_b128 v[40:43], v0 offset:10240\n" \
  "v_mfma_f32_16x16x32_bf16 v[68:71], a[32:35], v[52:55], v[68:71]\n" \
  "ds_read_b128 v[44:47], v0 offset:30720\n" \
  "v_mfma_f32_16x16x32_bf16 v[60:63], a[112:115], v[48:51], v[60:63]\n" \
  "s_add_u32 s72, s68, 4096\n" \
  "v_mfma_f32_16x16x32_bf16 v[72:75], a[112:115], v[52:55], v[72:75]\n" \
  "s_add_u32 s73, s68, 8192\n" \
  "v_mfma_f32_16x16x32_bf16 v[64:67], a[192:195], v[48:51], v[64:67]\n" \
  "s_add_u32 m0, s55, 81920\n" \
  "v_mfma_f32_16x16x32_bf16 v[76:79], a[192:195], v[52:55], v[76:79]\n" \
  "buffer_load_dwordx4 v1, s[36:39], s68 offen lds\n" \
  "s_waitcnt lgkmcnt(2)\n" \
  "v_mfma_f32_16x16x32_bf16 v[56:59], a[36:39], v[32:35], v[56:59]\n" \
  "ds_read_b128 v[48:51], v0 offset:11264\n" \
  "v_mfma_f32_16x16x32_bf16 v[68:71], a[36:39], v[36:39], v[68:71]\n" \
  "ds_read_b128 v[52:55], v0 offset:31744\n" \
  "v_mfma_f32_16x16x32_bf16 v[60:63], a[116:119], v[32:35], v[60:63]\n" \
  "buffer_load_dwordx4 v1, s[36:39], s68 offen offset:1024 lds\n" \
  "v_mfma_f32_16x16x32_bf16 v[72:75], a[116:119], v[36:39], v[72:75]\n" \
  "s_lshr_b64 s[76:77], s[52:53], s56\n" \
  "v_mfma_f32_16x16x32_bf16 v[64:67], a[196:199], v[32:35], v[64:67]\n" \
  "s_lshl_b32 s69, s56, 8\n" \
  "v_mfma_f32_16x16x32_bf16 v[76:79], a[196:199], v[36:39], v[76:79]\n" \
  "s_and_b32 s70, s76, 1\n" \
  "s_waitcnt lgkmcnt(2)\n" \
  "v_mfma_f32_16x16x32_bf16 v[56:59], a[40:43], v[40:43], v[56:59]\n" \
  "ds_read_b128 v[32:35], v0 offset:12288\n" \
  "v_mfma_f32_16x16x32_bf16 v[68:71], a[40:43], v[44:47], v[68:71]\n" \
  "ds_read_b128 v[36:39], v0 offset:32768\n" \
  "v_mfma_f32_16x16x32_bf16 v[60:63], a[120:123], v[40:43], v[60:63]\n" \
  "buffer_load_dwordx4 v1, s[36:39], s68 offen offset:2048 lds\n" \
  "v_mfma_f32_16x16x32_bf16 v[72:75], a[120:123], v[44:47], v[72:75]\n" \
  "s_bfe_u32 s71, s76, 0x10001\n" \
  "v_mfma_f32_16x16x32_bf16 v[64:67], a[200:203], v[40:43], v[64:67]\n" \
  "s_add_u32 s71, s71, s69\n" \
  "v_mfma_f32_16x16x32_bf16 v[76:79], a[200:203], v[44:47], v[76:79]\n" \
  "s_add_u32 s69, s69, s70\n" \
  "s_waitcnt lgkmcnt(2)\n" \
  "v_mfma_f32_16x16x32_bf16 v[56:59], a[44:47], v[48:51], v[56:59]\n" \
  "ds_read_b128 v[40:43], v0 offset:13312\n" \
  "v_mfma_f32_16x16x32_bf16 v[68:71], a[44:47], v[52:55], v[68:71]\n" \
  "ds_read_b128 v[44:47], v0 offset:33792\n" \
  "v_mfma_f32_16x16x32_bf16 v[60:63], a[124:127], v[48:51], v[60:63]\n" \
  "buffer_load_dwordx4 v1, s[36:39], s68 offen offset:3072 lds\n" \
  "v_mfma_f32_16x16x32_bf16 v[72:75], a[124:127], v[52:55], v[72:75]\n" \
  "s_add_u32 s71, s71, 0x100\n" \
  "v_mfma_f32_16x16x32_bf16 v[64:67], a[204:207], v[48:51], v[64:67]\n" \
  "s_lshl_b32 s69, s69, 13\n" \
  "v_mfma_f32_16x16x32_bf16 v[76:79], a[204:207], v[52:55], v[76:79]\n" \
  "s_lshl_b32 s71, s71, 13\n" \
  "s_waitcnt lgkmcnt(2)\n" \
  "v_mfma_f32_16x16x32_bf16 v[56:59], a[48:51], v[32:35], v[56:59]\n" \
  "ds_read_b128 v[48:51], v0 offset:14336\n" \
  "v_mfma_f32_16x16x32_bf16 v[68:71], a[48:51], v[36:39], v[68:71]\n" \
  "ds_read_b128 v[52:55], v0 offset:34816\n" \
  "v_mfma_f32_16x16x32_bf16 v[60:63], a[128:131], v[32:35], v[60:63]\n" \
  "s_add_u32 m0, s55, 86016\n" \
  "v_mfma_f32_16x16x32_bf16 v[72:75], a[128:131], v[36:39], v[72:75]\n" \
  "buffer_load_dwordx4 v1, s[36:39], s72 offen lds\n" \
  "v_mfma_f32_16x16x32_bf16 v[64:67], a[208:211], v[32:35], v[64:67]\n" \
  "v_mfma_f32_16x16x32_bf16 v[76:79], a[208:211], v[36:39], v[76:79]\n" \
  "s_waitcnt lgkmcnt(2)\n" \
  "v_mfma_f32_16x16x32_bf16 v[56:59], a[52:55], v[40:43], v[56:59]\n" \
  "ds_read_b128 v[32:35], v0 offset:15360\n" \
  "v_mfma_f32_16x16x32_bf16 v[68:71], a[52:55], v[44:47], v[68:71]\n" \
  "ds_read_b128 v[36:39], v0 offset:35840\n" \
  "v_mfma_f32_16x16x32_bf16 v[60:63], a[132:135], v[40:43], v[60:63]\n" \
  "buffer_load_dwordx4 v1, s[36:39], s72 offen offset:1024 lds\n" \
  "v_mfma_f32_16x16x32_bf16 v[72:75], a[132:135], v[44:47], v[72:75]\n" \
  "s_cmp_eq_u32 s57, 0\n" \
  "v_mfma_f32_16x16x32_bf16 v[64:67], a[212:215], v[40:43], v[64:67]\n" \
  "s_cselect_b32 s70, s69, s71\n" \
  "v_mfma_f32_16x16x32_bf16 v[76:79], a[212:215], v[44:47], v[76:79]\n" \
  "s_xor_b32 s70, s70, 0x2000\n" \
  "s_waitcnt lgkmcnt(2)\n" \
  "v_mfma_f32_16x16x32_bf16 v[56:59], a[56:59], v[48:51], v[56:59]\n" \
  "ds_read_b128 v[40:43], v0 offset:16384\n" \
  "v_mfma_f32_16x16x32_bf16 v[68:71], a[56:59], v[52:55], v[68:71]\n" \
  "ds_read_b128 v[44:47], v0 offset:36864\n" \
  "v_mfma_f32_16x16x32_bf16 v[60:63], a[136:139], v[48:51], v[60:63]\n" \
  "buffer_load_dwordx4 v1, s[36:39], s72 offen offset:2048 lds\n" \
  "v_mfma_f32_16x16x32_bf16 v[72:75], a[136:139], v[52:55], v[72:75]\n" \
  "s_add_u32 s56, s56, 2\n" \
  "v_mfma_f32_16x16x32_bf16 v[64:67], a[216:219], v[48:51], v[64:67]\n" \
  "v_mfma_f32_16x16x32_bf16 v[76:79], a[216:219], v[52:55], v[76:79]\n" \
  "s_waitcnt lgkmcnt(2)\n" \
  "v_mfma_f32_16x16x32_bf16 v[56:59], a[60:63], v[32:35], v[56:59]\n" \
  "ds_read_b128 v[48:51], v0 offset:17408\n" \
  "v_mfma_f32_16x16x32_bf16 v[68:71], a[60:63], v[36:39], v[68:71]\n" \
  "ds_read_b128 v[52:55], v0 offset:37888\n" \
  "v_mfma_f32_16x16x32_bf16 v[60:63], a[140:143], v[32:35], v[60:63]\n" \
  "buffer_load_dwordx4 v1, s[36:39], s72 offen offset:3072 lds\n" \
  "v_mfma_f32_16x16x32_bf16 v[72:75], a[140:143], v[36:39], v[72:75]\n" \
  "v_mfma_f32_16x16x32_bf16 v[64:67], a[220:223], v[32:35], v[64:67]\n" \
  "v_mfma_f32_16x16x32_bf16 v[76:79], a[220:223], v[36:39], v[76:79]\n" \
  "s_waitcnt lgkmcnt(2)\n" \
  "v_mfma_f32_16x16x32_bf16 v[56:59], a[64:67], v[40:43], v[56:59]\n" \
  "ds_read_b128 v[32:35], v0 offset:18432\n" \
  "v_mfma_f32_16x16x32_bf16 v[68:71], a[64:67], v[44:47], v[68:71]\n" \
  "ds_read_b128 v[36:39], v0 offset:38912\n" \
  "v_mfma_f32_16x16x32_bf16 v[60:63], a[144:147], v[40:43], v[60:63]\n" \
  "s_add_u32 m0, s55, 90112\n" \
  "v_mfma_f32_16x16x32_bf16 v[72:75], a[144:147], v[44:47], v[72:75]\n" \
  "buffer_load_dwordx4 v1, s[36:39], s73 offen lds\n" \
  "v_mfma_f32_16x16x32_bf16 v[64:67], a[224:227], v[40:43], v[64:67]\n" \
  "v_mfma_f32_16x16x32_bf16 v[76:79], a[224:227], v[44:47], v[76:79]\n" \
  "s_waitcnt lgkmcnt(2)\n" \
  "v_mfma_f32_16x16x32_bf16 v[56:59], a[68:71], v[48:51], v[56:59]\n" \
  "ds_read_b128 v[40:43], v0 offset:19456\n" \
  "v_mfma_f32_16x16x32_bf16 v[68:71], a[68:71], v[52:55], v[68:71]\n" \
  "ds_read_b128 v[44:47], v0 offset:39936\n" \
  "v_mfma_f32_16x16x32_bf16 v[60:63], a[148:151], v[48:51], v[60:63]\n" \
  "buffer_load_dwordx4 v1, s[36:39], s73 offen offset:1024 lds\n" \
  "v_mfma_f32_16x16x32_bf16 v[72:75], a[148:151], v[52:55], v[72:75]\n" \
  "s_add_u32 s68, s68, 40960\n" \
  "v_mfma_f32_16x16x32_bf16 v[64:67], a[228:231], v[48:51], v[64:67]\n" \
  "v_mfma_f32_16x16x32_bf16 v[76:79], a[228:231], v[52:55], v[76:79]\n" \
  "s_waitcnt vmcnt(10) lgkmcnt(0)\n" \
  "s_barrier\n" \
  "v_mfma_f32_16x16x32_bf16 v[56:59], a[72:75], v[32:35], v[56:59]\n" \
  "ds_read_b128 v[48:51], v9 offset:0\n" \
  "v_mfma_f32_16x16x32_bf16 v[68:71], a[72:75], v[36:39], v[68:71]\n" \
  "ds_read_b128 v[52:55], v9 offset:20480\n" \
  "v_mfma_f32_16x16x32_bf16 v[60:63], a[152:155], v[32:35], v[60:63]\n" \
  "v_mfma_f32_16x16x32_bf16 v[72:75], a[152:155], v[36:39], v[72:75]\n" \
  "v_mfma_f32_16x16x32_bf16 v[64:67], a[232:235], v[32:35], v[64:67]\n" \
  "v_mfma_f32_16x16x32_bf16 v[76:79], a[232:235], v[36:39], v[76:79]\n" \
  "v_mfma_f32_16x16x32_bf16 v[56:59], a[76:79], v[40:43], v[56:59]\n" \
  "ds_read_b128 v[32:35], v9 offset:1024\n" \
  "v_mfma_f32_16x16x32_bf16 v[68:71], a[76:79], v[44:47], v[68:71]\n" \
  "ds_read_b128 v[36:39], v9 offset:21504\n" \
  "v_mfma_f32_16x16x32_bf16 v[60:63], a[156:159], v[40:43], v[60:63]\n" \
  "s_cmp_eq_u32 s54, 60\n" \
  "v_mfma_f32_16x16x32_bf16 v[72:75], a[156:159], v[44:47], v[72:75]\n" \
  "v_mfma_f32_16x16x32_bf16 v[64:67], a[236:239], v[40:43], v[64:67]\n" \
  "v_mfma_f32_16x16x32_bf16 v[76:79], a[236:239], v[44:47], v[76:79]\n" \
  "s_waitcnt lgkmcnt(2)\n" \
  "v_mfma_f32_16x16x32_bf16 v[100:103], a[0:3], v[48:51], 0\n" \
  "ds_read_b128 v[40:43], v9 offset:2048\n" \
  "v_mfma_f32_16x16x32_bf16 v[112:115], a[0:3], v[52:55], 0\n" \
  "ds_read_b128 v[44:47], v9 offset:22528\n" \
  "v_mfma_f32_16x16x32_bf16 v[104:107], a[80:83], v[48:51], 0\n" \
  "v_mfma_f32_16x16x32_bf16 v[116:119], a[80:83], v[52:55], 0\n" \
  "v_mfma_f32_16x16x32_bf16 v[108:111], a[160:163], v[48:51], 0\n" \
  "v_mfma_f32_16x16x32_bf16 v[120:123], a[160:163], v[52:55], 0\n" \
  "s_waitcnt lgkmcnt(2)\n" \
  "v_mfma_f32_16x16x32_bf16 v[100:103], a[4:7], v[32:35], v[100:103]\n" \
  "ds_read_b128 v[48:51], v9 offset:3072\n" \
  "v_mfma_f32_16x16x32_bf16 v[112:115], a[4:7], v[36:39], v[112:115]\n" \
  "ds_read_b128 v[52:55], v9 offset:23552\n" \
  "v_mfma_f32_16x16x32_bf16 v[104:107], a[84:87], v[32:35], v[104:107]\n" \
  "buffer_store_dwordx4 v[12:15], v2, s[40:43], s69 offen sc1\n" \
  "v_mfma_f32_16x16x32_bf16 v[116:119], a[84:87], v[36:39], v[116:119]\n" \
  "v_cvt_pk_bf16_f32 v16, v56, v57\n" \
  "v_mfma_f32_16x16x32_bf16 v[108:111], a[164:167], v[32:35], v[108:111]\n" \
  "v_cvt_pk_bf16_f32 v17, v58, v59\n" \
  "v_mfma_f32_16x16x32_bf16 v[120:123], a[164:167], v[36:39], v[120:123]\n" \
  "v_cvt_pk_bf16_f32 v18, v60, v61\n" \
  "s_waitcnt lgkmcnt(2)\n" \
  "v_mfma_f32_16x16x32_bf16 v[100:103], a[8:11], v[40:43], v[100:103]\n" \
  "ds_read_b128 v[32:35], v9 offset:4096\n" \
  "v_mfma_f32_16x16x32_bf16 v[112:115], a[8:11], v[44:47], v[112:115]\n" \
  "ds_read_b128 v[36:39], v9 offset:24576\n" \
  "v_mfma_f32_16x16x32_bf16 v[104:107], a[88:91], v[40:43], v[104:107]\n" \
  "v_cvt_pk_bf16_f32 v19, v62, v63\n" \
  "v_mfma_f32_16x16x32_bf16 v[116:119], a[88:91], v[44:47], v[116:119]\n" \
  "buffer_store_dwordx4 v[12:15], v3, s[40:43], s69 offen sc1\n" \
  "v_mfma_f32_16x16x32_bf16 v[108:111], a[168:171], v[40:43], v[108:111]\n" \
  "v_cvt_pk_bf16_f32 v20, v68, v69\n" \
  "v_mfma_f32_16x16x32_bf16 v[120:123], a[168:171], v[44:47], v[120:123]\n" \
  "v_cvt_pk_bf16_f32 v21, v70, v71\n" \
  "s_waitcnt lgkmcnt(2)\n" \
  "v_mfma_f32_16x16x32_bf16 v[100:103], a[12:15], v[48:51], v[100:103]\n" \
  "ds_read_b128 v[40:43], v9 offset:5120\n" \
  "v_mfma_f32_16x16x32_bf16 v[112:115], a[12:15], v[52:55], v[112:115]\n" \
  "ds_read_b128 v[44:47], v9 offset:25600\n" \
  "v_mfma_f32_16x16x32_bf16 v[104:107], a[92:95], v[48:51], v[104:107]\n" \
  "v_cvt_pk_bf16_f32 v22, v72, v73\n" \
  "v_mfma_f32_16x16x32_bf16 v[116:119], a[92:95], v[52:55], v[116:119]\n" \
  "v_cvt_pk_bf16_f32 v23, v74, v75\n" \
  "v_mfma_f32_16x16x32_bf16 v[108:111], a[172:175], v[48:51], v[108:111]\n" \
  "buffer_store_dwordx4 v[12:15], v4, s[40:43], s69 offen sc1\n" \
  "v_mfma_f32_16x16x32_bf16 v[120:123], a[172:175], v[52:55], v[120:123]\n" \
  "v_cvt_pk_bf16_f32 v24, v64, v65\n" \
  "s_waitcnt lgkmcnt(2)\n" \
  "v_mfma_f32_16x16x32_bf16 v[100:103], a[16:19], v[32:35], v[100:103]\n" \
  "ds_read_b128 v[48:51], v9 offset:6144\n" \
  "v_mfma_f32_16x16x32_bf16 v[112:115], a[16:19], v[36:39], v[112:115]\n" \
  "ds_read_b128 v[52:55], v9 offset:26624\n" \
  "v_mfma_f32_16x16x32_bf16 v[104:107], a[96:99], v[32:35], v[104:107]\n" \
  "v_cvt_pk_bf16_f32 v25, v66, v67\n" \
  "v_mfma_f32_16x16x32_bf16 v[116:119], a[96:99], v[36:39], v[116:119]\n" \
  "v_cvt_pk_bf16_f32 v26, v76, v77\n" \
  "v_mfma_f32_16x16x32_bf16 v[108:111], a[176:179], v[32:35], v[108:111]\n" \
  "v_cvt_pk_bf16_f32 v27, v78, v79\n" \
  "v_mfma_f32_16x16x32_bf16 v[120:123], a[176:179], v[36:39], v[120:123]\n" \
  "buffer_store_dwordx4 v[12:15], v5, s[40:43], s69 offen sc1\n" \
  "s_waitcnt lgkmcnt(2)\n" \
  "v_mfma_f32_16x16x32_bf16 v[100:103], a[20:23], v[40:43], v[100:103]\n" \
  "ds_read_b128 v[32:35], v9 offset:7168\n" \
  "v_mfma_f32_16x16x32_bf16 v[112:115], a[20:23], v[44:47], v[112:115]\n" \
  "ds_read_b128 v[36:39], v9 offset:27648\n" \
  "v_mfma_f32_16x16x32_bf16 v[104:107], a[100:103], v[40:43], v[104:107]\n" \
  "buffer_store_dwordx4 v[16:19], v6, s[40:43], s70 offen sc1\n" \
  "v_mfma_f32_16x16x32_bf16 v[116:119], a[100:103], v[44:47], v[116:119]\n" \
  "v_mfma_f32_16x16x32_bf16 v[108:111], a[180:183], v[40:43], v[108:111]\n" \
  "v_mfma_f32_16x16x32_bf16 v[120:123], a[180:183], v[44:47], v[120:123]\n" \
  "s_waitcnt lgkmcnt(2)\n" \
  "v_mfma_f32_16x16x32_bf16 v[100:103], a[24:27], v[48:51], v[100:103]\n" \
  "ds_read_b128 v[40:43], v9 offset:8192\n" \
  "v_mfma_f32_16x16x32_bf16 v[112:115], a[24:27], v[52:55], v[112:115]\n" \
  "ds_read_b128 v[44:47], v9 offset:28672\n" \
  "v_mfma_f32_16x16x32_bf16 v[104:107], a[104:107], v[48:51], v[104:107]\n" \
  "buffer_store_dwordx4 v[20:23], v6, s[40:43], s70 offen offset:1024 sc1\n" \
  "v_mfma_f32_16x16x32_bf16 v[116:119], a[104:107], v[52:55], v[116:119]\n" \
  "v_mfma_f32_16x16x32_bf16 v[108:111], a[184:187], v[48:51], v[108:111]\n" \
  "v_mfma_f32_16x16x32_bf16 v[120:123], a[184:187], v[52:55], v[120:123]\n" \
  "s_waitcnt lgkmcnt(2)\n" \
  "v_mfma_f32_16x16x32_bf16 v[100:103], a[28:31], v[32:35], v[100:103]\n" \
  "ds_read_b128 v[48:51], v9 offset:9216\n" \
  "v_mfma_f32_16x16x32_bf16 v[112:115], a[28:31], v[36:39], v[112:115]\n" \
  "ds_read_b128 v[52:55], v9 offset:29696\n" \
  "v_mfma_f32_16x16x32_bf16 v[104:107], a[108:111], v[32:35], v[104:107]\n" \
  "buffer_store_dwordx4 v[24:27], v7, s[40:43], s70 offen sc1\n" \
  "v_mfma_f32_16x16x32_bf16 v[116:119], a[108:111], v[36:39], v[116:119]\n" \
  "v_mfma_f32_16x16x32_bf16 v[108:111], a[188:191], v[32:35], v[108:111]\n" \
  "v_mfma_f32_16x16x32_bf16 v[120:123], a[188:191], v[36:39], v[120:123]\n" \
  "s_waitcnt lgkmcnt(2)\n" \
  "v_mfma_f32_16x16x32_bf16 v[100:103], a[32:35], v[40:43], v[100:103]\n" \
  "ds_read_b128 v[32:35], v9 offset:10240\n" \
  "v_mfma_f32_16x16x32_bf16 v[112:115], a[32:35], v[44:47], v[112:115]\n" \
  "ds_read_b128 v[36:39], v9 offset:30720\n" \
  "v_mfma_f32_16x16x32_bf16 v[104:107], a[112:115], v[40:43], v[104:107]\n" \
  "s_add_u32 s72, s68, 4096\n" \
  "v_mfma_f32_16x16x32_bf16 v[116:119], a[112:115], v[44:47], v[116:119]\n" \
  "s_add_u32 s73, s68, 8192\n" \
  "v_mfma_f32_16x16x32_bf16 v[108:111], a[192:195], v[40:43], v[108:111]\n" \
  "s_add_u32 m0, s55, 0\n" \
  "v_mfma_f32_16x16x32_bf16 v[120:123], a[192:195], v[44:47], v[120:123]\n" \
  "buffer_load_dwordx4 v1, s[36:39], s68 offen lds\n" \
  "s_waitcnt lgkmcnt(2)\n" \
  "v_mfma_f32_16x16x32_bf16 v[100:103], a[36:39], v[48:51], v[100:103]\n" \
  "ds_read_b128 v[40:43], v9 offset:11264\n" \
  "v_mfma_f32_16x16x32_bf16 v[112:115], a[36:39], v[52:55], v[112:115]\n" \
  "ds_read_b128 v[44:47], v9 offset:31744\n" \
  "v_mfma_f32_16x16x32_bf16 v[104:107], a[116:119], v[48:51], v[104:107]\n" \
  "buffer_load_dwordx4 v1, s[36:39], s68 offen offset:1024 lds\n" \
  "v_mfma_f32_16x16x32_bf16 v[116:119], a[116:119], v[52:55], v[116:119]\n" \
  "s_lshr_b64 s[76:77], s[52:53], s56\n" \
  "v_mfma_f32_16x16x32_bf16 v[108:111], a[196:199], v[48:51], v[108:111]\n" \
  "s_lshl_b32 s69, s56, 8\n" \
  "v_mfma_f32_16x16x32_bf16 v[120:123], a[196:199], v[52:55], v[120:123]\n" \
  "s_and_b32 s70, s76, 1\n" \
  "s_waitcnt lgkmcnt(2)\n" \
  "v_mfma_f32_16x16x32_bf16 v[100:103], a[40:43], v[32:35], v[100:103]\n" \
  "ds_read_b128 v[48:51], v9 offset:12288\n" \
  "v_mfma_f32_16x16x32_bf16 v[112:115], a[40:43], v[36:39], v[112:115]\n" \
  "ds_read_b128 v[52:55], v9 offset:32768\n" \
  "v_mfma_f32_16x16x32_bf16 v[104:107], a[120:123], v[32:35], v[104:107]\n" \
  "buffer_load_dwordx4 v1, s[36:39], s68 offen offset:2048 lds\n" \
  "v_mfma_f32_16x16x32_bf16 v[116:119], a[120:123], v[36:39], v[116:119]\n" \
  "s_bfe_u32 s71, s76, 0x10001\n" \
  "v_mfma_f32_16x16x32_bf16 v[108:111], a[200:203], v[32:35], v[108:111]\n" \
  "s_add_u32 s71, s71, s69\n" \
  "v_mfma_f32_16x16x32_bf16 v[120:123], a[200:203], v[36:39], v[120:123]\n" \
  "s_add_u32 s69, s69, s70\n" \
  "s_waitcnt lgkmcnt(2)\n" \
  "v_mfma_f32_16x16x32_bf16 v[100:103], a[44:47], v[40:43], v[100:103]\n" \
  "ds_read_b128 v[32:35], v9 offset:13312\n" \
  "v_mfma_f32_16x16x32_bf16 v[112:115], a[44:47], v[44:47], v[112:115]\n" \
  "ds_read_b128 v[36:39], v9 offset:33792\n" \
  "v_mfma_f32_16x16x32_bf16 v[104:107], a[124:127], v[40:43], v[104:107]\n" \
  "buffer_load_dwordx4 v1, s[36:39], s68 offen offset:3072 lds\n" \
  "v_mfma_f32_16x16x32_bf16 v[116:119], a[124:127], v[44:47], v[116:119]\n" \
  "s_add_u32 s71, s71, 0x100\n" \
  "v_mfma_f32_16x16x32_bf16 v[108:111], a[204:207], v[40:43], v[108:111]\n" \
  "s_lshl_b32 s69, s69, 13\n" \
  "v_mfma_f32_16x16x32_bf16 v[120:123], a[204:207], v[44:47], v[120:123]\n" \
  "s_lshl_b32 s71, s71, 13\n" \
  "s_waitcnt lgkmcnt(2)\n" \
  "v_mfma_f32_16x16x32_bf16 v[100:103], a[48:51], v[48:51], v[100:103]\n" \
  "ds_read_b128 v[40:43], v9 offset:14336\n" \
  "v_mfma_f32_16x16x32_bf16 v[112:115], a[48:51], v[52:55], v[112:115]\n" \
  "ds_read_b128 v[44:47], v9 offset:34816\n" \
  "v_mfma_f32_16x16x32_bf16 v[104:107], a[128:131], v[48:51], v[104:107]\n" \
  "s_add_u32 m0, s55, 4096\n" \
  "v_mfma_f32_16x16x32_bf16 v[116:119], a[128:131], v[52:55], v[116:119]\n" \
  "buffer_load_dwordx4 v1, s[36:39], s72 offen lds\n" \
  "v_mfma_f32_16x16x32_bf16 v[108:111], a[208:211], v[48:51], v[108:111]\n" \
  "v_mfma_f32_16x16x32_bf16 v[120:123], a[208:211], v[52:55], v[120:123]\n" \
  "s_waitcnt lgkmcnt(2)\n" \
  "v_mfma_f32_16x16x32_bf16 v[100:103], a[52:55], v[32:35], v[100:103]\n" \
  "ds_read_b128 v[48:51], v9 offset:15360\n" \
  "v_mfma_f32_16x16x32_bf16 v[112:115], a[52:55], v[36:39], v[112:115]\n" \
  "ds_read_b128 v[52:55], v9 offset:35840\n" \
  "v_mfma_f32_16x16x32_bf16 v[104:107], a[132:135], v[32:35], v[104:107]\n" \
  "buffer_load_dwordx4 v1, s[36:39], s72 offen offset:1024 lds\n" \
  "v_mfma_f32_16x16x32_bf16 v[116:119], a[132:135], v[36:39], v[116:119]\n" \
  "s_cmp_eq_u32 s57, 0\n" \
  "v_mfma_f32_16x16x32_bf16 v[108:111], a[212:215], v[32:35], v[108:111]\n" \
  "s_cselect_b32 s70, s69, s71\n" \
  "v_mfma_f32_16x16x32_bf16 v[120:123], a[212:215], v[36:39], v[120:123]\n" \
  "s_xor_b32 s70, s70, 0x2000\n" \
  "s_waitcnt lgkmcnt(2)\n" \
  "v_mfma_f32_16x16x32_bf16 v[100:103], a[56:59], v[40:43], v[100:103]\n" \
  "ds_read_b128 v[32:35], v9 offset:16384\n" \
  "v_mfma_f32_16x16x32_bf16 v[112:115], a[56:59], v[44:47], v[112:115]\n" \
  "ds_read_b128 v[36:39], v9 offset:36864\n" \
  "v_mfma_f32_16x16x32_bf16 v[104:107], a[136:139], v[40:43], v[104:107]\n" \
  "buffer_load_dwordx4 v1, s[36:39], s72 offen offset:2048 lds\n" \
  "v_mfma_f32_16x16x32_bf16 v[116:119], a[136:139], v[44:47], v[116:119]\n" \
  "s_add_u32 s56, s56, 2\n" \
  "v_mfma_f32_16x16x32_bf16 v[108:111], a[216:219], v[40:43], v[108:111]\n" \
  "v_mfma_f32_16x16x32_bf16 v[120:123], a[216:219], v[44:47], v[120:123]\n" \
  "s_waitcnt lgkmcnt(2)\n" \
  "v_mfma_f32_16x16x32_bf16 v[100:103], a[60:63], v[48:51], v[100:103]\n" \
  "ds_read_b128 v[40:43], v9 offset:17408\n" \
  "v_mfma_f32_16x16x32_bf16 v[112:115], a[60:63], v[52:55], v[112:115]\n" \
  "ds_read_b128 v[44:47], v9 offset:37888\n" \
  "v_mfma_f32_16x16x32_bf16 v[104:107], a[140:143], v[48:51], v[104:107]\n" \
  "buffer_load_dwordx4 v1, s[36:39], s72 offen offset:3072 lds\n" \
  "v_mfma_f32_16x16x32_bf16 v[116:119], a[140:143], v[52:55], v[116:119]\n" \
  "v_mfma_f32_16x16x32_bf16 v[108:111], a[220:223], v[48:51], v[108:111]\n" \
  "v_mfma_f32_16x16x32_bf16 v[120:123], a[220:223], v[52:55], v[120:123]\n" \
  "s_waitcnt lgkmcnt(2)\n" \
  "v_mfma_f32_16x16x32_bf16 v[100:103], a[64:67], v[32:35], v[100:103]\n" \
  "ds_read_b128 v[48:51], v9 offset:18432\n" \
  "v_mfma_f32_16x16x32_bf16 v[112:115], a[64:67], v[36:39], v[112:115]\n" \
  "ds_read_b128 v[52:55], v9 offset:38912\n" \
  "v_mfma_f32_16x16x32_bf16 v[104:107], a[144:147], v[32:35], v[104:107]\n" \
  "s_add_u32 m0, s55, 8192\n" \
  "v_mfma_f32_16x16x32_bf16 v[116:119], a[144:147], v[36:39], v[116:119]\n" \
  "buffer_load_dwordx4 v1, s[36:39], s73 offen lds\n" \
  "v_mfma_f32_16x16x32_bf16 v[108:111], a[224:227], v[32:35], v[108:111]\n" \
  "v_mfma_f32_16x16x32_bf16 v[120:123], a[224:227], v[36:39], v[120:123]\n" \
  "s_waitcnt lgkmcnt(2)\n" \
  "v_mfma_f32_16x16x32_bf16 v[100:103], a[68:71], v[40:43], v[100:103]\n" \
  "ds_read_b128 v[32:35], v9 offset:19456\n" \
  "v_mfma_f32_16x16x32_bf16 v[112:115], a[68:71], v[44:47], v[112:115]\n" \
  "ds_read_b128 v[36:39], v9 offset:39936\n" \
  "v_mfma_f32_16x16x32_bf16 v[104:107], a[148:151], v[40:43], v[104:107]\n" \
  "buffer_load_dwordx4 v1, s[36:39], s73 offen offset:1024 lds\n" \
  "v_mfma_f32_16x16x32_bf16 v[116:119], a[148:151], v[44:47], v[116:119]\n" \
  "s_add_u32 s68, s68, 40960\n" \
  "v_mfma_f32_16x16x32_bf16 v[108:111], a[228:231], v[40:43], v[108:111]\n" \
  "v_mfma_f32_16x16x32_bf16 v[120:123], a[228:231], v[44:47], v[120:123]\n" \
  "s_waitcnt vmcnt(10) lgkmcnt(0)\n" \
  "s_barrier\n" \
  "v_mfma_f32_16x16x32_bf16 v[100:103], a[72:75], v[48:51], v[100:103]\n" \
  "ds_read_b128 v[40:43], v10 offset:0\n" \
  "v_mfma_f32_16x16x32_bf16 v[112:115], a[72:75], v[52:55], v[112:115]\n" \
  "ds_read_b128 v[44:47], v10 offset:20480\n" \
  "v_mfma_f32_16x16x32_bf16 v[104:107], a[152:155], v[48:51], v[104:107]\n" \
  "v_mfma_f32_16x16x32_bf16 v[116:119], a[152:155], v[52:55], v[116:119]\n" \
  "v_mfma_f32_16x16x32_bf16 v[108:111], a[232:235], v[48:51], v[108:111]\n" \
  "v_mfma_f32_16x16x32_bf16 v[120:123], a[232:235], v[52:55], v[120:123]\n" \
  "v_mfma_f32_16x16x32_bf16 v[100:103], a[76:79], v[32:35], v[100:103]\n" \
  "ds_read_b128 v[48:51], v10 offset:1024\n" \
  "v_mfma_f32_16x16x32_bf16 v[112:115], a[76:79], v[36:39], v[112:115]\n" \
  "ds_read_b128 v[52:55], v10 offset:21504\n" \
  "v_mfma_f32_16x16x32_bf16 v[104:107], a[156:159], v[32:35], v[104:107]\n" \
  "s_cmp_eq_u32 s54, 60\n" \
  "v_mfma_f32_16x16x32_bf16 v[116:119], a[156:159], v[36:39], v[116:119]\n" \
  "v_mfma_f32_16x16x32_bf16 v[108:111], a[236:239], v[32:35], v[108:111]\n" \
  "v_mfma_f32_16x16x32_bf16 v[120:123], a[236:239], v[36:39], v[120:123]\n" \
  "s_waitcnt lgkmcnt(2)\n" \
  "v_mfma_f32_16x16x32_bf16 v[56:59], a[0:3], v[40:43], 0\n" \
  "ds_read_b128 v[32:35], v10 offset:2048\n" \
  "v_mfma_f32_16x16x32_bf16 v[68:71], a[0:3], v[44:47], 0\n" \
  "ds_read_b128 v[36:39], v10 offset:22528\n" \
  "v_mfma_f32_16x16x32_bf16 v[60:63], a[80:83], v[40:43], 0\n" \
  "buffer_atomic_add v11, v8, s[40:43], s74 offen\n" \
  "v_mfma_f32_16x16x32_bf16 v[72:75], a[80:83], v[44:47], 0\n" \
  "s_add_u32 s74, s74, 0x2200\n" \
  "v_mfma_f32_16x16x32_bf16 v[64:67], a[160:163], v[40:43], 0\n" \
  "s_and_b32 s74, s74, 0xffffdfff\n" \
  "v_mfma_f32_16x16x32_bf16 v[76:79], a[160:163], v[44:47], 0\n" \
  "s_waitcnt lgkmcnt(2)\n" \
  "v_mfma_f32_16x16x32_bf16 v[56:59], a[4:7], v[48:51], v[56:59]\n" \
  "ds_read_b128 v[40:43], v10 offset:3072\n" \
  "v_mfma_f32_16x16x32_bf16 v[68:71], a[4:7], v[52:55], v[68:71]\n" \
  "ds_read_b128 v[44:47], v10 offset:23552\n" \
  "v_mfma_f32_16x16x32_bf16 v[60:63], a[84:87], v[48:51], v[60:63]\n" \
  "buffer_store_dwordx4 v[12:15], v2, s[40:43], s69 offen sc1\n" \
  "v_mfma_f32_16x16x32_bf16 v[72:75], a[84:87], v[52:55], v[72:75]\n" \
  "v_cvt_pk_bf16_f32 v16, v100, v101\n" \
  "v_mfma_f32_16x16x32_bf16 v[64:67], a[164:167], v[48:51], v[64:67]\n" \
  "v_cvt_pk_bf16_f32 v17, v102, v103\n" \
  "v_mfma_f32_16x16x32_bf16 v[76:79], a[164:167], v[52:55], v[76:79]\n" \
  "v_cvt_pk_bf16_f32 v18, v104, v105\n" \
  "s_waitcnt lgkmcnt(2)\n" \
  "v_mfma_f32_16x16x32_bf16 v[56:59], a[8:11], v[32:35], v[56:59]\n" \
  "ds_read_b128 v[48:51], v10 offset:4096\n" \
  "v_mfma_f32_16x16x32_bf16 v[68:71], a[8:11], v[36:39], v[68:71]\n" \
  "ds_read_b128 v[52:55], v10 offset:24576\n" \
  "v_mfma_f32_16x16x32_bf16 v[60:63], a[88:91], v[32:35], v[60:63]\n" \
  "v_cvt_pk_bf16_f32 v19, v106, v107\n" \
  "v_mfma_f32_16x16x32_bf16 v[72:75], a[88:91], v[36:39], v[72:75]\n" \
  "buffer_store_dwordx4 v[12:15], v3, s[40:43], s69 offen sc1\n" \
  "v_mfma_f32_16x16x32_bf16 v[64:67], a[168:171], v[32:35], v[64:67]\n" \
  "v_cvt_pk_bf16_f32 v20, v112, v113\n" \
  "v_mfma_f32_16x16x32_bf16 v[76:79], a[168:171], v[36:39], v[76:79]\n" \
  "v_cvt_pk_bf16_f32 v21, v114, v115\n" \
  "s_waitcnt lgkmcnt(2)\n" \
  "v_mfma_f32_16x16x32_bf16 v[56:59], a[12:15], v[40:43], v[56:59]\n" \
  "ds_read_b128 v[32:35], v10 offset:5120\n" \
  "v_mfma_f32_16x16x32_bf16 v[68:71], a[12:15], v[44:47], v[68:71]\n" \
  "ds_read_b128 v[36:39], v10 offset:25600\n" \
  "v_mfma_f32_16x16x32_bf16 v[60:63], a[92:95], v[40:43], v[60:63]\n" \
  "v_cvt_pk_bf16_f32 v22, v116, v117\n" \
  "v_mfma_f32_16x16x32_bf16 v[72:75], a[92:95], v[44:47], v[72:75]\n" \
  "v_cvt_pk_bf16_f32 v23, v118, v119\n" \
  "v_mfma_f32_16x16x32_bf16 v[64:67], a[172:175], v[40:43], v[64:67]\n" \
  "buffer_store_dwordx4 v[12:15], v4, s[40:43], s69 offen sc1\n" \
  "v_mfma_f32_16x16x32_bf16 v[76:79], a[172:175], v[44:47], v[76:79]\n" \
  "v_cvt_pk_bf16_f32 v24, v108, v109\n" \
  "s_waitcnt lgkmcnt(2)\n" \
  "v_mfma_f32_16x16x32_bf16 v[56:59], a[16:19], v[48:51], v[56:59]\n" \
  "ds_read_b128 v[40:43], v10 offset:6144\n" \
  "v_mfma_f32_16x16x32_bf16 v[68:71], a[16:19], v[52:55], v[68:71]\n" \
  "ds_read_b128 v[44:47], v10 offset:26624\n" \
  "v_mfma_f32_16x16x32_bf16 v[60:63], a[96:99], v[48:51], v[60:63]\n" \
  "v_cvt_pk_bf16_f32 v25, v110, v111\n" \
  "v_mfma_f32_16x16x32_bf16 v[72:75], a[96:99], v[52:55], v[72:75]\n" \
  "v_cvt_pk_bf16_f32 v26, v120, v121\n" \
  "v_mfma_f32_16x16x32_bf16 v[64:67], a[176:179], v[48:51], v[64:67]\n" \
  "v_cvt_pk_bf16_f32 v27, v122, v123\n" \
  "v_mfma_f32_16x16x32_bf16 v[76:79], a[176:179], v[52:55], v[76:79]\n" \
  "buffer_store_dwordx4 v[12:15], v5, s[40:43], s69 offen sc1\n" \
  "s_waitcnt lgkmcnt(2)\n" \
  "v_mfma_f32_16x16x32_bf16 v[56:59], a[20:23], v[32:35], v[56:59]\n" \
  "ds_read_b128 v[48:51], v10 offset:7168\n" \
  "v_mfma_f32_16x16x32_bf16 v[68:71], a[20:23], v[36:39], v[68:71]\n" \
  "ds_read_b128 v[52:55], v10 offset:27648\n" \
  "v_mfma_f32_16x16x32_bf16 v[60:63], a[100:103], v[32:35], v[60:63]\n" \
  "buffer_store_dwordx4 v[16:19], v6, s[40:43], s70 offen sc1\n" \
  "v_mfma_f32_16x16x32_bf16 v[72:75], a[100:103], v[36:39], v[72:75]\n" \
  "v_mfma_f32_16x16x32_bf16 v[64:67], a[180:183], v[32:35], v[64:67]\n" \
  "v_mfma_f32_16x16x32_bf16 v[76:79], a[180:183], v[36:39], v[76:79]\n" \
  "s_waitcnt lgkmcnt(2)\n" \
  "v_mfma_f32_16x16x32_bf16 v[56:59], a[24:27], v[40:43], v[56:59]\n" \
  "ds_read_b128 v[32:35], v10 offset:8192\n" \
  "v_mfma_f32_16x16x32_bf16 v[68:71], a[24:27], v[44:47], v[68:71]\n" \
  "ds_read_b128 v[36:39], v10 offset:28672\n" \
  "v_mfma_f32_16x16x32_bf16 v[60:63], a[104:107], v[40:43], v[60:63]\n" \
  "buffer_store_dwordx4 v[20:23], v6, s[40:43], s70 offen offset:1024 sc1\n" \
  "v_mfma_f32_16x16x32_bf16 v[72:75], a[104:107], v[44:47], v[72:75]\n" \
  "v_mfma_f32_16x16x32_bf16 v[64:67], a[184:187], v[40:43], v[64:67]\n" \
  "v_mfma_f32_16x16x32_bf16 v[76:79], a[184:187], v[44:47], v[76:79]\n" \
  "s_waitcnt lgkmcnt(2)\n" \
  "v_mfma_f32_16x16x32_bf16 v[56:59], a[28:31], v[48:51], v[56:59]\n" \
  "ds_read_b128 v[40:43], v10 offset:9216\n" \
  "v_mfma_f32_16x16x32_bf16 v[68:71], a[28:31], v[52:55], v[68:71]\n" \
  "ds_read_b128 v[44:47], v10 offset:29696\n" \
  "v_mfma_f32_16x16x32_bf16 v[60:63], a[108:111], v[48:51], v[60:63]\n" \
  "buffer_store_dwordx4 v[24:27], v7, s[40:43], s70 offen sc1\n" \
  "v_mfma_f32_16x16x32_bf16 v[72:75], a[108:111], v[52:55], v[72:75]\n" \
  "v_mfma_f32_16x16x32_bf16 v[64:67], a[188:191], v[48:51], v[64:67]\n" \
  "v_mfma_f32_16x16x32_bf16 v[76:79], a[188:191], v[52:55], v[76:79]\n" \
  "s_waitcnt lgkmcnt(2)\n" \
  "v_mfma_f32_16x16x32_bf16 v[56:59], a[32:35], v[32:35], v[56:59]\n" \
  "ds_read_b128 v[48:51], v10 offset:10240\n" \
  "v_mfma_f32_16x16x32_bf16 v[68:71], a[32:35], v[36:39], v[68:71]\n" \
  "ds_read_b128 v[52:55], v10 offset:30720\n" \
  "v_mfma_f32_16x16x32_bf16 v[60:63], a[112:115], v[32:35], v[60:63]\n" \
  "s_add_u32 s72, s68, 4096\n" \
  "v_mfma_f32_16x16x32_bf16 v[72:75], a[112:115], v[36:39], v[72:75]\n" \
  "s_add_u32 s73, s68, 8192\n" \
  "v_mfma_f32_16x16x32_bf16 v[64:67], a[192:195], v[32:35], v[64:67]\n" \
  "s_add_u32 m0, s55, 40960\n" \
  "v_mfma_f32_16x16x32_bf16 v[76:79], a[192:195], v[36:39], v[76:79]\n" \
  "buffer_load_dwordx4 v1, s[36:39], s68 offen lds\n" \
  "s_waitcnt lgkmcnt(2)\n" \
  "v_mfma_f32_16x16x32_bf16 v[56:59], a[36:39], v[40:43], v[56:59]\n" \
  "ds_read_b128 v[32:35], v10 offset:11264\n" \
  "v_mfma_f32_16x16x32_bf16 v[68:71], a[36:39], v[44:47], v[68:71]\n" \
  "ds_read_b128 v[36:39], v10 offset:31744\n" \
  "v_mfma_f32_16x16x32_bf16 v[60:63], a[116:119], v[40:43], v[60:63]\n" \
  "buffer_load_dwordx4 v1, s[36:39], s68 offen offset:1024 lds\n" \
  "v_mfma_f32_16x16x32_bf16 v[72:75], a[116:119], v[44:47], v[72:75]\n" \
  "s_lshr_b64 s[76:77], s[52:53], s56\n" \
  "v_mfma_f32_16x16x32_bf16 v[64:67], a[196:199], v[40:43], v[64:67]\n" \
  "s_lshl_b32 s69, s56, 8\n" \
  "v_mfma_f32_16x16x32_bf16 v[76:79], a[196:199], v[44:47], v[76:79]\n" \
  "s_and_b32 s70, s76, 1\n" \
  "s_waitcnt lgkmcnt(2)\n" \
  "v_mfma_f32_16x16x32_bf16 v[56:59], a[40:43], v[48:51], v[56:59]\n" \
  "ds_read_b128 v[40:43], v10 offset:12288\n" \
  "v_mfma_f32_16x16x32_bf16 v[68:71], a[40:43], v[52:55], v[68:71]\n" \
  "ds_read_b128 v[44:47], v10 offset:32768\n" \
  "v_mfma_f32_16x16x32_bf16 v[60:63], a[120:123], v[48:51], v[60:63]\n" \
  "buffer_load_dwordx4 v1, s[36:39], s68 offen offset:2048 lds\n" \
  "v_mfma_f32_16x16x32_bf16 v[72:75], a[120:123], v[52:55], v[72:75]\n" \
  "s_bfe_u32 s71, s76, 0x10001\n" \
  "v_mfma_f32_16x16x32_bf16 v[64:67], a[200:203], v[48:51], v[64:67]\n" \
  "s_add_u32 s71, s71, s69\n" \
  "v_mfma_f32_16x16x32_bf16 v[76:79], a[200:203], v[52:55], v[76:79]\n" \
  "s_add_u32 s69, s69, s70\n" \
  "s_waitcnt lgkmcnt(2)\n" \
  "v_mfma_f32_16x16x32_bf16 v[56:59], a[44:47], v[32:35], v[56:59]\n" \
  "ds_read_b128 v[48:51], v10 offset:13312\n" \
  "v_mfma_f32_16x16x32_bf16 v[68:71], a[44:47], v[36:39], v[68:71]\n" \
  "ds_read_b128 v[52:55], v10 offset:33792\n" \
  "v_mfma_f32_16x16x32_bf16 v[60:63], a[124:127], v[32:35], v[60:63]\n" \
  "buffer_load_dwordx4 v1, s[36:39], s68 offen offset:3072 lds\n" \
  "v_mfma_f32_16x16x32_bf16 v[72:75], a[124:127], v[36:39], v[72:75]\n" \
  "s_add_u32 s71, s71, 0x100\n" \
  "v_mfma_f32_16x16x32_bf16 v[64:67], a[204:207], v[32:35], v[64:67]\n" \
  "s_lshl_b32 s69, s69, 13\n" \
  "v_mfma_f32_16x16x32_bf16 v[76:79], a[204:207], v[36:39], v[76:79]\n" \
  "s_lshl_b32 s71, s71, 13\n" \
  "s_waitcnt lgkmcnt(2)\n" \
  "v_mfma_f32_16x16x32_bf16 v[56:59], a[48:51], v[40:43], v[56:59]\n" \
  "ds_read_b128 v[32:35], v10 offset:14336\n" \
  "v_mfma_f32_16x16x32_bf16 v[68:71], a[48:51], v[44:47], v[68:71]\n" \
  "ds_read_b128 v[36:39], v10 offset:34816\n" \
  "v_mfma_f32_16x16x32_bf16 v[60:63], a[128:131], v[40:43], v[60:63]\n" \
  "s_add_u32 m0, s55, 45056\n" \
  "v_mfma_f32_16x16x32_bf16 v[72:75], a[128:131], v[44:47], v[72:75]\n" \
  "buffer_load_dwordx4 v1, s[36:39], s72 offen lds\n" \
  "v_mfma_f32_16x16x32_bf16 v[64:67], a[208:211], v[40:43], v[64:67]\n" \
  "v_mfma_f32_16x16x32_bf16 v[76:79], a[208:211], v[44:47], v[76:79]\n" \
  "s_waitcnt lgkmcnt(2)\n" \
  "v_mfma_f32_16x16x32_bf16 v[56:59], a[52:55], v[48:51], v[56:59]\n" \
  "ds_read_b128 v[40:43], v10 offset:15360\n" \
  "v_mfma_f32_16x16x32_bf16 v[68:71], a[52:55], v[52:55], v[68:71]\n" \
  "ds_read_b128 v[44:47], v10 offset:35840\n" \
  "v_mfma_f32_16x16x32_bf16 v[60:63], a[132:135], v[48:51], v[60:63]\n" \
  "buffer_load_dwordx4 v1, s[36:39], s72 offen offset:1024 lds\n" \
  "v_mfma_f32_16x16x32_bf16 v[72:75], a[132:135], v[52:55], v[72:75]\n" \
  "s_cmp_eq_u32 s57, 0\n" \
  "v_mfma_f32_16x16x32_bf16 v[64:67], a[212:215], v[48:51], v[64:67]\n" \
  "s_cselect_b32 s70, s69, s71\n" \
  "v_mfma_f32_16x16x32_bf16 v[76:79], a[212:215], v[52:55], v[76:79]\n" \
  "s_xor_b32 s70, s70, 0x2000\n" \
  "s_waitcnt lgkmcnt(2)\n" \
  "v_mfma_f32_16x16x32_bf16 v[56:59], a[56:59], v[32:35], v[56:59]\n" \
  "ds_read_b128 v[48:51], v10 offset:16384\n" \
  "v_mfma_f32_16x16x32_bf16 v[68:71], a[56:59], v[36:39], v[68:71]\n" \
  "ds_read_b128 v[52:55], v10 offset:36864\n" \
  "v_mfma_f32_16x16x32_bf16 v[60:63], a[136:139], v[32:35], v[60:63]\n" \
  "buffer_load_dwordx4 v1, s[36:39], s72 offen offset:2048 lds\n" \
  "v_mfma_f32_16x16x32_bf16 v[72:75], a[136:139], v[36:39], v[72:75]\n" \
  "s_add_u32 s56, s56, 2\n" \
  "v_mfma_f32_16x16x32_bf16 v[64:67], a[216:219], v[32:35], v[64:67]\n" \
  "v_mfma_f32_16x16x32_bf16 v[76:79], a[216:219], v[36:39], v[76:79]\n" \
  "s_waitcnt lgkmcnt(2)\n" \
  "v_mfma_f32_16x16x32_bf16 v[56:59], a[60:63], v[40:43], v[56:59]\n" \
  "ds_read_b128 v[32:35], v10 offset:17408\n" \
  "v_mfma_f32_16x16x32_bf16 v[68:71], a[60:63], v[44:47], v[68:71]\n" \
  "ds_read_b128 v[36:39], v10 offset:37888\n" \
  "v_mfma_f32_16x16x32_bf16 v[60:63], a[140:143], v[40:43], v[60:63]\n" \
  "buffer_load_dwordx4 v1, s[36:39], s72 offen offset:3072 lds\n" \
  "v_mfma_f32_16x16x32_bf16 v[72:75], a[140:143], v[44:47], v[72:75]\n" \
  "v_mfma_f32_16x16x32_bf16 v[64:67], a[220:223], v[40:43], v[64:67]\n" \
  "v_mfma_f32_16x16x32_bf16 v[76:79], a[220:223], v[44:47], v[76:79]\n" \
  "s_waitcnt lgkmcnt(2)\n" \
  "v_mfma_f32_16x16x32_bf16 v[56:59], a[64:67], v[48:51], v[56:59]\n" \
  "ds_read_b128 v[40:43], v10 offset:18432\n" \
  "v_mfma_f32_16x16x32_bf16 v[68:71], a[64:67], v[52:55], v[68:71]\n" \
  "ds_read_b128 v[44:47], v10 offset:38912\n" \
  "v_mfma_f32_16x16x32_bf16 v[60:63], a[144:147], v[48:51], v[60:63]\n" \
  "s_add_u32 m0, s55, 49152\n" \
  "v_mfma_f32_16x16x32_bf16 v[72:75], a[144:147], v[52:55], v[72:75]\n" \
  "buffer_load_dwordx4 v1, s[36:39], s73 offen lds\n" \
  "v_mfma_f32_16x16x32_bf16 v[64:67], a[224:227], v[48:51], v[64:67]\n" \
  "v_mfma_f32_16x16x32_bf16 v[76:79], a[224:227], v[52:55], v[76:79]\n" \
  "s_waitcnt lgkmcnt(2)\n" \
  "v_mfma_f32_16x16x32_bf16 v[56:59], a[68:71], v[32:35], v[56:59]\n" \
  "ds_read_b128 v[48:51], v10 offset:19456\n" \
  "v_mfma_f32_16x16x32_bf16 v[68:71], a[68:71], v[36:39], v[68:71]\n" \
  "ds_read_b128 v[52:55], v10 offset:39936\n" \
  "v_mfma_f32_16x16x32_bf16 v[60:63], a[148:151], v[32:35], v[60:63]\n" \
  "buffer_load_dwordx4 v1, s[36:39], s73 offen offset:1024 lds\n" \
  "v_mfma_f32_16x16x32_bf16 v[72:75], a[148:151], v[36:39], v[72:75]\n" \
  "s_add_u32 s68, s68, 40960\n" \
  "v_mfma_f32_16x16x32_bf16 v[64:67], a[228:231], v[32:35], v[64:67]\n" \
  "v_mfma_f32_16x16x32_bf16 v[76:79], a[228:231], v[36:39], v[76:79]\n" \
  "s_waitcnt vmcnt(10) lgkmcnt(0)\n" \
  "s_barrier\n" \
  "v_mfma_f32_16x16x32_bf16 v[56:59], a[72:75], v[40:43], v[56:59]\n" \
  "ds_read_b128 v[32:35], v0 offset:0\n" \
  "v_mfma_f32_16x16x32_bf16 v[68:71], a[72:75], v[44:47], v[68:71]\n" \
  "ds_read_b128 v[36:39], v0 offset:20480\n" \
  "v_mfma_f32_16x16x32_bf16 v[60:63], a[152:155], v[40:43], v[60:63]\n" \
  "v_mfma_f32_16x16x32_bf16 v[72:75], a[152:155], v[44:47], v[72:75]\n" \
  "v_mfma_f32_16x16x32_bf16 v[64:67], a[232:235], v[40:43], v[64:67]\n" \
  "v_mfma_f32_16x16x32_bf16 v[76:79], a[232:235], v[44:47], v[76:79]\n" \
  "v_mfma_f32_16x16x32_bf16 v[56:59], a[76:79], v[48:51], v[56:59]\n" \
  "ds_read_b128 v[40:43], v0 offset:1024\n" \
  "v_mfma_f32_16x16x32_bf16 v[68:71], a[76:79], v[52:55], v[68:71]\n" \
  "ds_read_b128 v[44:47], v0 offset:21504\n" \
  "v_mfma_f32_16x16x32_bf16 v[60:63], a[156:159], v[48:51], v[60:63]\n" \
  "s_cmp_eq_u32 s54, 60\n" \
  "v_mfma_f32_16x16x32_bf16 v[72:75], a[156:159], v[52:55], v[72:75]\n" \
  "v_mfma_f32_16x16x32_bf16 v[64:67], a[236:239], v[48:51], v[64:67]\n" \
  "v_mfma_f32_16x16x32_bf16 v[76:79], a[236:239], v[52:55], v[76:79]\n" \
  "s_waitcnt lgkmcnt(2)\n" \
  "v_mfma_f32_16x16x32_bf16 v[100:103], a[0:3], v[32:35], 0\n" \
  "ds_read_b128 v[48:51], v0 offset:2048\n" \
  "v_mfma_f32_16x16x32_bf16 v[112:115], a[0:3], v[36:39], 0\n" \
  "ds_read_b128 v[52:55], v0 offset:22528\n" \
  "v_mfma_f32_16x16x32_bf16 v[104:107], a[80:83], v[32:35], 0\n" \
  "buffer_atomic_add v11, v8, s[40:43], s74 offen\n" \
  "v_mfma_f32_16x16x32_bf16 v[116:119], a[80:83], v[36:39], 0\n" \
  "s_add_u32 s74, s74, 0x2200\n" \
  "v_mfma_f32_16x16x32_bf16 v[108:111], a[160:163], v[32:35], 0\n" \
  "s_and_b32 s74, s74, 0xffffdfff\n" \
  "v_mfma_f32_16x16x32_bf16 v[120:123], a[160:163], v[36:39], 0\n" \
  "s_waitcnt lgkmcnt(2)\n" \
  "v_mfma_f32_16x16x32_bf16 v[100:103], a[4:7], v[40:43], v[100:103]\n" \
  "ds_read_b128 v[32:35], v0 offset:3072\n" \
  "v_mfma_f32_16x16x32_bf16 v[112:115], a[4:7], v[44:47], v[112:115]\n" \
  "ds_read_b128 v[36:39], v0 offset:23552\n" \
  "v_mfma_f32_16x16x32_bf16 v[104:107], a[84:87], v[40:43], v[104:107]\n" \
  "buffer_store_dwordx4 v[12:15], v2, s[40:43], s69 offen sc1\n" \
  "v_mfma_f32_16x16x32_bf16 v[116:119], a[84:87], v[44:47], v[116:119]\n" \
  "v_cvt_pk_bf16_f32 v16, v56, v57\n" \
  "v_mfma_f32_16x16x32_bf16 v[108:111], a[164:167], v[40:43], v[108:111]\n" \
  "v_cvt_pk_bf16_f32 v17, v58, v59\n" \
  "v_mfma_f32_16x16x32_bf16 v[120:123], a[164:167], v[44:47], v[120:123]\n" \
  "v_cvt_pk_bf16_f32 v18, v60, v61\n" \
  "s_waitcnt lgkmcnt(2)\n" \
  "v_mfma_f32_16x16x32_bf16 v[100:103], a[8:11], v[48:51], v[100:103]\n" \
  "ds_read_b128 v[40:43], v0 offset:4096\n" \
  "v_mfma_f32_16x16x32_bf16 v[112:115], a[8:11], v[52:55], v[112:115]\n" \
  "ds_read_b128 v[44:47], v0 offset:24576\n" \
  "v_mfma_f32_16x16x32_bf16 v[104:107], a[88:91], v[48:51], v[104:107]\n" \
  "v_cvt_pk_bf16_f32 v19, v62, v63\n" \
  "v_mfma_f32_16x16x32_bf16 v[116:119], a[88:91], v[52:55], v[116:119]\n" \
  "buffer_store_dwordx4 v[12:15], v3, s[40:43], s69 offen sc1\n" \
  "v_mfma_f32_16x16x32_bf16 v[108:111], a[168:171], v[48:51], v[108:111]\n" \
  "v_cvt_pk_bf16_f32 v20, v68, v69\n" \
  "v_mfma_f32_16x16x32_bf16 v[120:123], a[168:171], v[52:55], v[120:123]\n" \
  "v_cvt_pk_bf16_f32 v21, v70, v71\n" \
  "s_waitcnt lgkmcnt(2)\n" \
  "v_mfma_f32_16x16x32_bf16 v[100:103], a[12:15], v[32:35], v[100:103]\n" \
  "ds_read_b128 v[48:51], v0 offset:5120\n" \
  "v_mfma_f32_16x16x32_bf16 v[112:115], a[12:15], v[36:39], v[112:115]\n" \
  "ds_read_b128 v[52:55], v0 offset:25600\n" \
  "v_mfma_f32_16x16x32_bf16 v[104:107], a[92:95], v[32:35], v[104:107]\n" \
  "v_cvt_pk_bf16_f32 v22, v72, v73\n" \
  "v_mfma_f32_16x16x32_bf16 v[116:119], a[92:95], v[36:39], v[116:119]\n" \
  "v_cvt_pk_bf16_f32 v23, v74, v75\n" \
  "v_mfma_f32_16x16x32_bf16 v[108:111], a[172:175], v[32:35], v[108:111]\n" \
  "buffer_store_dwordx4 v[12:15], v4, s[40:43], s69 offen sc1\n" \
  "v_mfma_f32_16x16x32_bf16 v[120:123], a[172:175], v[36:39], v[120:123]\n" \
  "v_cvt_pk_bf16_f32 v24, v64, v65\n" \
  "s_waitcnt lgkmcnt(2)\n" \
  "v_mfma_f32_16x16x32_bf16 v[100:103], a[16:19], v[40:43], v[100:103]\n" \
  "ds_read_b128 v[32:35], v0 offset:6144\n" \
  "v_mfma_f32_16x16x32_bf16 v[112:115], a[16:19], v[44:47], v[112:115]\n" \
  "ds_read_b128 v[36:39], v0 offset:26624\n" \
  "v_mfma_f32_16x16x32_bf16 v[104:107], a[96:99], v[40:43], v[104:107]\n" \
  "v_cvt_pk_bf16_f32 v25, v66, v67\n" \
  "v_mfma_f32_16x16x32_bf16 v[116:119], a[96:99], v[44:47], v[116:119]\n" \
  "v_cvt_pk_bf16_f32 v26, v76, v77\n" \
  "v_mfma_f32_16x16x32_bf16 v[108:111], a[176:179], v[40:43], v[108:111]\n" \
  "v_cvt_pk_bf16_f32 v27, v78, v79\n" \
  "v_mfma_f32_16x16x32_bf16 v[120:123], a[176:179], v[44:47], v[120:123]\n" \
  "buffer_store_dwordx4 v[12:15], v5, s[40:43], s69 offen sc1\n" \
  "s_waitcnt lgkmcnt(2)\n" \
  "v_mfma_f32_16x16x32_bf16 v[100:103], a[20:23], v[48:51], v[100:103]\n" \
  "ds_read_b128 v[40:43], v0 offset:7168\n" \
  "v_mfma_f32_16x16x32_bf16 v[112:115], a[20:23], v[52:55], v[112:115]\n" \
  "ds_read_b128 v[44:47], v0 offset:27648\n" \
  "v_mfma_f32_16x16x32_bf16 v[104:107], a[100:103], v[48:51], v[104:107]\n" \
  "buffer_store_dwordx4 v[16:19], v6, s[40:43], s70 offen sc1\n" \
  "v_mfma_f32_16x16x32_bf16 v[116:119], a[100:103], v[52:55], v[116:119]\n" \
  "v_mfma_f32_16x16x32_bf16 v[108:111], a[180:183], v[48:51], v[108:111]\n" \
  "v_mfma_f32_16x16x32_bf16 v[120:123], a[180:183], v[52:55], v[120:123]\n" \
  "s_waitcnt lgkmcnt(2)\n" \
  "v_mfma_f32_16x16x32_bf16 v[100:103], a[24:27], v[32:35], v[100:103]\n" \
  "ds_read_b128 v[48:51], v0 offset:8192\n" \
  "v_mfma_f32_16x16x32_bf16 v[112:115], a[24:27], v[36:39], v[112:115]\n" \
  "ds_read_b128 v[52:55], v0 offset:28672\n" \
  "v_mfma_f32_16x16x32_bf16 v[104:107], a[104:107], v[32:35], v[104:107]\n" \
  "buffer_store_dwordx4 v[20:23], v6, s[40:43], s70 offen offset:1024 sc1\n" \
  "v_mfma_f32_16x16x32_bf16 v[116:119], a[104:107], v[36:39], v[116:119]\n" \
  "v_mfma_f32_16x16x32_bf16 v[108:111], a[184:187], v[32:35], v[108:111]\n" \
  "v_mfma_f32_16x16x32_bf16 v[120:123], a[184:187], v[36:39], v[120:123]\n" \
  "s_waitcnt lgkmcnt(2)\n" \
  "v_mfma_f32_16x16x32_bf16 v[100:103], a[28:31], v[40:43], v[100:103]\n" \
  "ds_read_b128 v[32:35], v0 offset:9216\n" \
  "v_mfma_f32_16x16x32_bf16 v[112:115], a[28:31], v[44:47], v[112:115]\n" \
  "ds_read_b128 v[36:39], v0 offset:29696\n" \
  "v_mfma_f32_16x16x32_bf16 v[104:107], a[108:111], v[40:43], v[104:107]\n" \
  "buffer_store_dwordx4 v[24:27], v7, s[40:43], s70 offen sc1\n" \
  "v_mfma_f32_16x16x32_bf16 v[116:119], a[108:111], v[44:47], v[116:119]\n" \
  "v_mfma_f32_16x16x32_bf16 v[108:111], a[188:191], v[40:43], v[108:111]\n" \
  "v_mfma_f32_16x16x32_bf16 v[120:123], a[188:191], v[44:47], v[120:123]\n" \
  "s_waitcnt lgkmcnt(2)\n" \
  "v_mfma_f32_16x16x32_bf16 v[100:103], a[32:35], v[48:51], v[100:103]\n" \
  "ds_read_b128 v[40:43], v0 offset:10240\n" \
  "v_mfma_f32_16x16x32_bf16 v[112:115], a[32:35], v[52:55], v[112:115]\n" \
  "ds_read_b128 v[44:47], v0 offset:30720\n" \
  "v_mfma_f32_16x16x32_bf16 v[104:107], a[112:115], v[48:51], v[104:107]\n" \
  "s_add_u32 s72, s68, 4096\n" \
  "v_mfma_f32_16x16x32_bf16 v[116:119], a[112:115], v[52:55], v[116:119]\n" \
  "s_add_u32 s73, s68, 8192\n" \
  "v_mfma_f32_16x16x32_bf16 v[108:111], a[192:195], v[48:51], v[108:111]\n" \
  "s_add_u32 m0, s55, 81920\n" \
  "v_mfma_f32_16x16x32_bf16 v[120:123], a[192:195], v[52:55], v[120:123]\n" \
  "buffer_load_dwordx4 v1, s[36:39], s68 offen lds\n" \
  "s_waitcnt lgkmcnt(2)\n" \
  "v_mfma_f32_16x16x32_bf16 v[100:103], a[36:39], v[32:35], v[100:103]\n" \
  "ds_read_b128 v[48:51], v0 offset:11264\n" \
  "v_mfma_f32_16x16x32_bf16 v[112:115], a[36:39], v[36:39], v[112:115]\n" \
  "ds_read_b128 v[52:55], v0 offset:31744\n" \
  "v_mfma_f32_16x16x32_bf16 v[104:107], a[116:119], v[32:35], v[104:107]\n" \
  "buffer_load_dwordx4 v1, s[36:39], s68 offen offset:1024 lds\n" \
  "v_mfma_f32_16x16x32_bf16 v[116:119], a[116:119], v[36:39], v[116:119]\n" \
  "s_lshr_b64 s[76:77], s[52:53], s56\n" \
  "v_mfma_f32_16x16x32_bf16 v[108:111], a[196:199], v[32:35], v[108:111]\n" \
  "s_lshl_b32 s69, s56, 8\n" \
  "v_mfma_f32_16x16x32_bf16 v[120:123], a[196:199], v[36:39], v[120:123]\n" \
  "s_and_b32 s70, s76, 1\n" \
  "s_waitcnt lgkmcnt(2)\n" \
  "v_mfma_f32_16x16x32_bf16 v[100:103], a[40:43], v[40:43], v[100:103]\n" \
  "ds_read_b128 v[32:35], v0 offset:12288\n" \
  "v_mfma_f32_16x16x32_bf16 v[112:115], a[40:43], v[44:47], v[112:115]\n" \
  "ds_read_b128 v[36:39], v0 offset:32768\n" \
  "v_mfma_f32_16x16x32_bf16 v[104:107], a[120:123], v[40:43], v[104:107]\n" \
  "buffer_load_dwordx4 v1, s[36:39], s68 offen offset:2048 lds\n" \
  "v_mfma_f32_16x16x32_bf16 v[116:119], a[120:123], v[44:47], v[116:119]\n" \
  "s_bfe_u32 s71, s76, 0x10001\n" \
  "v_mfma_f32_16x16x32_bf16 v[108:111], a[200:203], v[40:43], v[108:111]\n" \
  "s_add_u32 s71, s71, s69\n" \
  "v_mfma_f32_16x16x32_bf16 v[120:123], a[200:203], v[44:47], v[120:123]\n" \
  "s_add_u32 s69, s69, s70\n" \
  "s_waitcnt lgkmcnt(2)\n" \
  "v_mfma_f32_16x16x32_bf16 v[100:103], a[44:47], v[48:51], v[100:103]\n" \
  "ds_read_b128 v[40:43], v0 offset:13312\n" \
  "v_mfma_f32_16x16x32_bf16 v[112:115], a[44:47], v[52:55], v[112:115]\n" \
  "ds_read_b128 v[44:47], v0 offset:33792\n" \
  "v_mfma_f32_16x16x32_bf16 v[104:107], a[124:127], v[48:51], v[104:107]\n" \
  "buffer_load_dwordx4 v1, s[36:39], s68 offen offset:3072 lds\n" \
  "v_mfma_f32_16x16x32_bf16 v[116:119], a[124:127], v[52:55], v[116:119]\n" \
  "s_add_u32 s71, s71, 0x100\n" \
  "v_mfma_f32_16x16x32_bf16 v[108:111], a[204:207], v[48:51], v[108:111]\n" \
  "s_lshl_b32 s69, s69, 13\n" \
  "v_mfma_f32_16x16x32_bf16 v[120:123], a[204:207], v[52:55], v[120:123]\n" \
  "s_lshl_b32 s71, s71, 13\n" \
  "s_waitcnt lgkmcnt(2)\n" \
  "v_mfma_f32_16x16x32_bf16 v[100:103], a[48:51], v[32:35], v[100:103]\n" \
  "ds_read_b128 v[48:51], v0 offset:14336\n" \
  "v_mfma_f32_16x16x32_bf16 v[112:115], a[48:51], v[36:39], v[112:115]\n" \
  "ds_read_b128 v[52:55], v0 offset:34816\n" \
  "v_mfma_f32_16x16x32_bf16 v[104:107], a[128:131], v[32:35], v[104:107]\n" \
  "s_add_u32 m0, s55, 86016\n" \
  "v_mfma_f32_16x16x32_bf16 v[116:119], a[128:131], v[36:39], v[116:119]\n" \
  "buffer_load_dwordx4 v1, s[36:39], s72 offen lds\n" \
  "v_mfma_f32_16x16x32_bf16 v[108:111], a[208:211], v[32:35], v[108:111]\n" \
  "v_mfma_f32_16x16x32_bf16 v[120:123], a[208:211], v[36:39], v[120:123]\n" \
  "s_waitcnt lgkmcnt(2)\n" \
  "v_mfma_f32_16x16x32_bf16 v[100:103], a[52:55], v[40:43], v[100:103]\n" \
  "ds_read_b128 v[32:35], v0 offset:15360\n" \
  "v_mfma_f32_16x16x32_bf16 v[112:115], a[52:55], v[44:47], v[112:115]\n" \
  "ds_read_b128 v[36:39], v0 offset:35840\n" \
  "v_mfma_f32_16x16x32_bf16 v[104:107], a[132:135], v[40:43], v[104:107]\n" \
  "buffer_load_dwordx4 v1, s[36:39], s72 offen offset:1024 lds\n" \
  "v_mfma_f32_16x16x32_bf16 v[116:119], a[132:135], v[44:47], v[116:119]\n" \
  "s_cmp_eq_u32 s57, 0\n" \
  "v_mfma_f32_16x16x32_bf16 v[108:111], a[212:215], v[40:43], v[108:111]\n" \
  "s_cselect_b32 s70, s69, s71\n" \
  "v_mfma_f32_16x16x32_bf16 v[120:123], a[212:215], v[44:47], v[120:123]\n" \
  "s_xor_b32 s70, s70, 0x2000\n" \
  "s_waitcnt lgkmcnt(2)\n" \
  "v_mfma_f32_16x16x32_bf16 v[100:103], a[56:59], v[48:51], v[100:103]\n" \
  "ds_read_b128 v[40:43], v0 offset:16384\n" \
  "v_mfma_f32_16x16x32_bf16 v[112:115], a[56:59], v[52:55], v[112:115]\n" \
  "ds_read_b128 v[44:47], v0 offset:36864\n" \
  "v_mfma_f32_16x16x32_bf16 v[104:107], a[136:139], v[48:51], v[104:107]\n" \
  "buffer_load_dwordx4 v1, s[36:39], s72 offen offset:2048 lds\n" \
  "v_mfma_f32_16x16x32_bf16 v[116:119], a[136:139], v[52:55], v[116:119]\n" \
  "s_add_u32 s56, s56, 2\n" \
  "v_mfma_f32_16x16x32_bf16 v[108:111], a[216:219], v[48:51], v[108:111]\n" \
  "v_mfma_f32_16x16x32_bf16 v[120:123], a[216:219], v[52:55], v[120:123]\n" \
  "s_waitcnt lgkmcnt(2)\n" \
  "v_mfma_f32_16x16x32_bf16 v[100:103], a[60:63], v[32:35], v[100:103]\n" \
  "ds_read_b128 v[48:51], v0 offset:17408\n" \
  "v_mfma_f32_16x16x32_bf16 v[112:115], a[60:63], v[36:39], v[112:115]\n" \
  "ds_read_b128 v[52:55], v0 offset:37888\n" \
  "v_mfma_f32_16x16x32_bf16 v[104:107], a[140:143], v[32:35], v[104:107]\n" \
  "buffer_load_dwordx4 v1, s[36:39], s72 offen offset:3072 lds\n" \
  "v_mfma_f32_16x16x32_bf16 v[116:119], a[140:143], v[36:39], v[116:119]\n" \
  "v_mfma_f32_16x16x32_bf16 v[108:111], a[220:223], v[32:35], v[108:111]\n" \
  "v_mfma_f32_16x16x32_bf16 v[120:123], a[220:223], v[36:39], v[120:123]\n" \
  "s_waitcnt lgkmcnt(2)\n" \
  "v_mfma_f32_16x16x32_bf16 v[100:103], a[64:67], v[40:43], v[100:103]\n" \
  "ds_read_b128 v[32:35], v0 offset:18432\n" \
  "v_mfma_f32_16x16x32_bf16 v[112:115], a[64:67], v[44:47], v[112:115]\n" \
  "ds_read_b128 v[36:39], v0 offset:38912\n" \
  "v_mfma_f32_16x16x32_bf16 v[104:107], a[144:147], v[40:43], v[104:107]\n" \
  "s_add_u32 m0, s55, 90112\n" \
  "v_mfma_f32_16x16x32_bf16 v[116:119], a[144:147], v[44:47], v[116:119]\n" \
  "buffer_load_dwordx4 v1, s[36:39], s73 offen lds\n" \
  "v_mfma_f32_16x16x32_bf16 v[108:111], a[224:227], v[40:43], v[108:111]\n" \
  "v_mfma_f32_16x16x32_bf16 v[120:123], a[224:227], v[44:47], v[120:123]\n" \
  "s_waitcnt lgkmcnt(2)\n" \
  "v_mfma_f32_16x16x32_bf16 v[100:103], a[68:71], v[48:51], v[100:103]\n" \
  "ds_read_b128 v[40:43], v0 offset:19456\n" \
  "v_mfma_f32_16x16x32_bf16 v[112:115], a[68:71], v[52:55], v[112:115]\n" \
  "ds_read_b128 v[44:47], v0 offset:39936\n" \
  "v_mfma_f32_16x16x32_bf16 v[104:107], a[148:151], v[48:51], v[104:107]\n" \
  "buffer_load_dwordx4 v1, s[36:39], s73 offen offset:1024 lds\n" \
  "v_mfma_f32_16x16x32_bf16 v[116:119], a[148:151], v[52:55], v[116:119]\n" \
  "s_add_u32 s68, s68, 40960\n" \
  "v_mfma_f32_16x16x32_bf16 v[108:111], a[228:231], v[48:51], v[108:111]\n" \
  "v_mfma_f32_16x16x32_bf16 v[120:123], a[228:231], v[52:55], v[120:123]\n" \
  "s_waitcnt vmcnt(10) lgkmcnt(0)\n" \
  "s_barrier\n" \
  "v_mfma_f32_16x16x32_bf16 v[100:103], a[72:75], v[32:35], v[100:103]\n" \
  "ds_read_b128 v[48:51], v9 offset:0\n" \
  "v_mfma_f32_16x16x32_bf16 v[112:115], a[72:75], v[36:39], v[112:115]\n" \
  "ds_read_b128 v[52:55], v9 offset:20480\n" \
  "v_mfma_f32_16x16x32_bf16 v[104:107], a[152:155], v[32:35], v[104:107]\n" \
  "v_mfma_f32_16x16x32_bf16 v[116:119], a[152:155], v[36:39], v[116:119]\n" \
  "v_mfma_f32_16x16x32_bf16 v[108:111], a[232:235], v[32:35], v[108:111]\n" \
  "v_mfma_f32_16x16x32_bf16 v[120:123], a[232:235], v[36:39], v[120:123]\n" \
  "v_mfma_f32_16x16x32_bf16 v[100:103], a[76:79], v[40:43], v[100:103]\n" \
  "ds_read_b128 v[32:35], v9 offset:1024\n" \
  "v_mfma_f32_16x16x32_bf16 v[112:115], a[76:79], v[44:47], v[112:115]\n" \
  "ds_read_b128 v[36:39], v9 offset:21504\n" \
  "v_mfma_f32_16x16x32_bf16 v[104:107], a[156:159], v[40:43], v[104:107]\n" \
  "s_cmp_eq_u32 s54, 60\n" \
  "v_mfma_f32_16x16x32_bf16 v[116:119], a[156:159], v[44:47], v[116:119]\n" \
  "v_mfma_f32_16x16x32_bf16 v[108:111], a[236:239], v[40:43], v[108:111]\n" \
  "v_mfma_f32_16x16x32_bf16 v[120:123], a[236:239], v[44:47], v[120:123]\n" \
  "s_cbranch_scc1 LPE_%=\n" \
  "s_waitcnt lgkmcnt(2)\n" \
  "v_mfma_f32_16x16x32_bf16 v[56:59], a[0:3], v[48:51], 0\n" \
  "ds_read_b128 v[40:43], v9 offset:2048\n" \
  "v_mfma_f32_16x16x32_bf16 v[68:71], a[0:3], v[52:55], 0\n" \
  "ds_read_b128 v[44:47], v9 offset:22528\n" \
  "v_mfma_f32_16x16x32_bf16 v[60:63], a[80:83], v[48:51], 0\n" \
  "buffer_atomic_add v11, v8, s[40:43], s74 offen\n" \
  "v_mfma_f32_16x16x32_bf16 v[72:75], a[80:83], v[52:55], 0\n" \
  "s_add_u32 s74, s74, 0x2200\n" \
  "v_mfma_f32_16x16x32_bf16 v[64:67], a[160:163], v[48:51], 0\n" \
  "s_and_b32 s74, s74, 0xffffdfff\n" \
  "v_mfma_f32_16x16x32_bf16 v[76:79], a[160:163], v[52:55], 0\n" \
  "s_waitcnt lgkmcnt(2)\n" \
  "v_mfma_f32_16x16x32_bf16 v[56:59], a[4:7], v[32:35], v[56:59]\n" \
  "ds_read_b128 v[48:51], v9 offset:3072\n" \
  "v_mfma_f32_16x16x32_bf16 v[68:71], a[4:7], v[36:39], v[68:71]\n" \
  "ds_read_b128 v[52:55], v9 offset:23552\n" \
  "v_mfma_f32_16x16x32_bf16 v[60:63], a[84:87], v[32:35], v[60:63]\n" \
  "buffer_store_dwordx4 v[12:15], v2, s[40:43], s69 offen sc1\n" \
  "v_mfma_f32_16x16x32_bf16 v[72:75], a[84:87], v[36:39], v[72:75]\n" \
  "v_cvt_pk_bf16_f32 v16, v100, v101\n" \
  "v_mfma_f32_16x16x32_bf16 v[64:67], a[164:167], v[32:35], v[64:67]\n" \
  "v_cvt_pk_bf16_f32 v17, v102, v103\n" \
  "v_mfma_f32_16x16x32_bf16 v[76:79], a[164:167], v[36:39], v[76:79]\n" \
  "v_cvt_pk_bf16_f32 v18, v104, v105\n" \
  "s_waitcnt lgkmcnt(2)\n" \
  "v_mfma_f32_16x16x32_bf16 v[56:59], a[8:11], v[40:43], v[56:59]\n" \
  "ds_read_b128 v[32:35], v9 offset:4096\n" \
  "v_mfma_f32_16x16x32_bf16 v[68:71], a[8:11], v[44:47], v[68:71]\n" \
  "ds_read_b128 v[36:39], v9 offset:24576\n" \
  "v_mfma_f32_16x16x32_bf16 v[60:63], a[88:91], v[40:43], v[60:63]\n" \
  "v_cvt_pk_bf16_f32 v19, v106, v107\n" \
  "v_mfma_f32_16x16x32_bf16 v[72:75], a[88:91], v[44:47], v[72:75]\n" \
  "buffer_store_dwordx4 v[12:15], v3, s[40:43], s69 offen sc1\n" \
  "v_mfma_f32_16x16x32_bf16 v[64:67], a[168:171], v[40:43], v[64:67]\n" \
  "v_cvt_pk_bf16_f32 v20, v112, v113\n" \
  "v_mfma_f32_16x16x32_bf16 v[76:79], a[168:171], v[44:47], v[76:79]\n" \
  "v_cvt_pk_bf16_f32 v21, v114, v115\n" \
  "s_waitcnt lgkmcnt(2)\n" \
  "v_mfma_f32_16x16x32_bf16 v[56:59], a[12:15], v[48:51], v[56:59]\n" \
  "ds_read_b128 v[40:43], v9 offset:5120\n" \
  "v_mfma_f32_16x16x32_bf16 v[68:71], a[12:15], v[52:55], v[68:71]\n" \
  "ds_read_b128 v[44:47], v9 offset:25600\n" \
  "v_mfma_f32_16x16x32_bf16 v[60:63], a[92:95], v[48:51], v[60:63]\n" \
  "v_cvt_pk_bf16_f32 v22, v116, v117\n" \
  "v_mfma_f32_16x16x32_bf16 v[72:75], a[92:95], v[52:55], v[72:75]\n" \
  "v_cvt_pk_bf16_f32 v23, v118, v119\n" \
  "v_mfma_f32_16x16x32_bf16 v[64:67], a[172:175], v[48:51], v[64:67]\n" \
  "buffer_store_dwordx4 v[12:15], v4, s[40:43], s69 offen sc1\n" \
  "v_mfma_f32_16x16x32_bf16 v[76:79], a[172:175], v[52:55], v[76:79]\n" \
  "v_cvt_pk_bf16_f32 v24, v108, v109\n" \
  "s_waitcnt lgkmcnt(2)\n" \
  "v_mfma_f32_16x16x32_bf16 v[56:59], a[16:19], v[32:35], v[56:59]\n" \
  "ds_read_b128 v[48:51], v9 offset:6144\n" \
  "v_mfma_f32_16x16x32_bf16 v[68:71], a[16:19], v[36:39], v[68:71]\n" \
  "ds_read_b128 v[52:55], v9 offset:26624\n" \
  "v_mfma_f32_16x16x32_bf16 v[60:63], a[96:99], v[32:35], v[60:63]\n" \
  "v_cvt_pk_bf16_f32 v25, v110, v111\n" \
  "v_mfma_f32_16x16x32_bf16 v[72:75], a[96:99], v[36:39], v[72:75]\n" \
  "v_cvt_pk_bf16_f32 v26, v120, v121\n" \
  "v_mfma_f32_16x16x32_bf16 v[64:67], a[176:179], v[32:35], v[64:67]\n" \
  "v_cvt_pk_bf16_f32 v27, v122, v123\n" \
  "v_mfma_f32_16x16x32_bf16 v[76:79], a[176:179], v[36:39], v[76:79]\n" \
  "buffer_store_dwordx4 v[12:15], v5, s[40:43], s69 offen sc1\n" \
  "s_waitcnt lgkmcnt(2)\n" \
  "v_mfma_f32_16x16x32_bf16 v[56:59], a[20:23], v[40:43], v[56:59]\n" \
  "ds_read_b128 v[32:35], v9 offset:7168\n" \
  "v_mfma_f32_16x16x32_bf16 v[68:71], a[20:23], v[44:47], v[68:71]\n" \
  "ds_read_b128 v[36:39], v9 offset:27648\n" \
  "v_mfma_f32_16x16x32_bf16 v[60:63], a[100:103], v[40:43], v[60:63]\n" \
  "buffer_store_dwordx4 v[16:19], v6, s[40:43], s70 offen sc1\n" \
  "v_mfma_f32_16x16x32_bf16 v[72:75], a[100:103], v[44:47], v[72:75]\n" \
  "v_mfma_f32_16x16x32_bf16 v[64:67], a[180:183], v[40:43], v[64:67]\n" \
  "v_mfma_f32_16x16x32_bf16 v[76:79], a[180:183], v[44:47], v[76:79]\n" \
  "s_waitcnt lgkmcnt(2)\n" \
  "v_mfma_f32_16x16x32_bf16 v[56:59], a[24:27], v[48:51], v[56:59]\n" \
  "ds_read_b128 v[40:43], v9 offset:8192\n" \
  "v_mfma_f32_16x16x32_bf16 v[68:71], a[24:27], v[52:55], v[68:71]\n" \
  "ds_read_b128 v[44:47], v9 offset:28672\n" \
  "v_mfma_f32_16x16x32_bf16 v[60:63], a[104:107], v[48:51], v[60:63]\n" \
  "buffer_store_dwordx4 v[20:23], v6, s[40:43], s70 offen offset:1024 sc1\n" \
  "v_mfma_f32_16x16x32_bf16 v[72:75], a[104:107], v[52:55], v[72:75]\n" \
  "v_mfma_f32_16x16x32_bf16 v[64:67], a[184:187], v[48:51], v[64:67]\n" \
  "v_mfma_f32_16x16x32_bf16 v[76:79], a[184:187], v[52:55], v[76:79]\n" \
  "s_waitcnt lgkmcnt(2)\n" \
  "v_mfma_f32_16x16x32_bf16 v[56:59], a[28:31], v[32:35], v[56:59]\n" \
  "ds_read_b128 v[48:51], v9 offset:9216\n" \
  "v_mfma_f32_16x16x32_bf16 v[68:71], a[28:31], v[36:39], v[68:71]\n" \
  "ds_read_b128 v[52:55], v9 offset:29696\n" \
  "v_mfma_f32_16x16x32_bf16 v[60:63], a[108:111], v[32:35], v[60:63]\n" \
  "buffer_store_dwordx4 v[24:27], v7, s[40:43], s70 offen sc1\n" \
  "v_mfma_f32_16x16x32_bf16 v[72:75], a[108:111], v[36:39], v[72:75]\n" \
  "v_mfma_f32_16x16x32_bf16 v[64:67], a[188:191], v[32:35], v[64:67]\n" \
  "v_mfma_f32_16x16x32_bf16 v[76:79], a[188:191], v[36:39], v[76:79]\n" \
  "s_waitcnt lgkmcnt(2)\n" \
  "v_mfma_f32_16x16x32_bf16 v[56:59], a[32:35], v[40:43], v[56:59]\n" \
  "ds_read_b128 v[32:35], v9 offset:10240\n" \
  "v_mfma_f32_16x16x32_bf16 v[68:71], a[32:35], v[44:47], v[68:71]\n" \
  "ds_read_b128 v[36:39], v9 offset:30720\n" \
  "v_mfma_f32_16x16x32_bf16 v[60:63], a[112:115], v[40:43], v[60:63]\n" \
  "s_add_u32 s72, s68, 4096\n" \
  "v_mfma_f32_16x16x32_bf16 v[72:75], a[112:115], v[44:47], v[72:75]\n" \
  "s_add_u32 s73, s68, 8192\n" \
  "v_mfma_f32_16x16x32_bf16 v[64:67], a[192:195], v[40:43], v[64:67]\n" \
  "s_add_u32 m0, s55, 0\n" \
  "v_mfma_f32_16x16x32_bf16 v[76:79], a[192:195], v[44:47], v[76:79]\n" \
  "buffer_load_dwordx4 v1, s[36:39], s68 offen lds\n" \
  "s_waitcnt lgkmcnt(2)\n" \
  "v_mfma_f32_16x16x32_bf16 v[56:59], a[36:39], v[48:51], v[56:59]\n" \
  "ds_read_b128 v[40:43], v9 offset:11264\n" \
  "v_mfma_f32_16x16x32_bf16 v[68:71], a[36:39], v[52:55], v[68:71]\n" \
  "ds_read_b128 v[44:47], v9 offset:31744\n" \
  "v_mfma_f32_16x16x32_bf16 v[60:63], a[116:119], v[48:51], v[60:63]\n" \
  "buffer_load_dwordx4 v1, s[36:39], s68 offen offset:1024 lds\n" \
  "v_mfma_f32_16x16x32_bf16 v[72:75], a[116:119], v[52:55], v[72:75]\n" \
  "s_lshr_b64 s[76:77], s[52:53], s56\n" \
  "v_mfma_f32_16x16x32_bf16 v[64:67], a[196:199], v[48:51], v[64:67]\n" \
  "s_lshl_b32 s69, s56, 8\n" \
  "v_mfma_f32_16x16x32_bf16 v[76:79], a[196:199], v[52:55], v[76:79]\n" \
  "s_and_b32 s70, s76, 1\n" \
  "s_waitcnt lgkmcnt(2)\n" \
  "v_mfma_f32_16x16x32_bf16 v[56:59], a[40:43], v[32:35], v[56:59]\n" \
  "ds_read_b128 v[48:51], v9 offset:12288\n" \
  "v_mfma_f32_16x16x32_bf16 v[68:71], a[40:43], v[36:39], v[68:71]\n" \
  "ds_read_b128 v[52:55], v9 offset:32768\n" \
  "v_mfma_f32_16x16x32_bf16 v[60:63], a[120:123], v[32:35], v[60:63]\n" \
  "buffer_load_dwordx4 v1, s[36:39], s68 offen offset:2048 lds\n" \
  "v_mfma_f32_16x16x32_bf16 v[72:75], a[120:123], v[36:39], v[72:75]\n" \
  "s_bfe_u32 s71, s76, 0x10001\n" \
  "v_mfma_f32_16x16x32_bf16 v[64:67], a[200:203], v[32:35], v[64:67]\n" \
  "s_add_u32 s71, s71, s69\n" \
  "v_mfma_f32_16x16x32_bf16 v[76:79], a[200:203], v[36:39], v[76:79]\n" \
  "s_add_u32 s69, s69, s70\n" \
  "s_waitcnt lgkmcnt(2)\n" \
  "v_mfma_f32_16x16x32_bf16 v[56:59], a[44:47], v[40:43], v[56:59]\n" \
  "ds_read_b128 v[32:35], v9 offset:13312\n" \
  "v_mfma_f32_16x16x32_bf16 v[68:71], a[44:47], v[44:47], v[68:71]\n" \
  "ds_read_b128 v[36:39], v9 offset:33792\n" \
  "v_mfma_f32_16x16x32_bf16 v[60:63], a[124:127], v[40:43], v[60:63]\n" \
  "buffer_load_dwordx4 v1, s[36:39], s68 offen offset:3072 lds\n" \
  "v_mfma_f32_16x16x32_bf16 v[72:75], a[124:127], v[44:47], v[72:75]\n" \
  "s_add_u32 s71, s71, 0x100\n" \
  "v_mfma_f32_16x16x32_bf16 v[64:67], a[204:207], v[40:43], v[64:67]\n" \
  "s_lshl_b32 s69, s69, 13\n" \
  "v_mfma_f32_16x16x32_bf16 v[76:79], a[204:207], v[44:47], v[76:79]\n" \
  "s_lshl_b32 s71, s71, 13\n" \
  "s_waitcnt lgkmcnt(2)\n" \
  "v_mfma_f32_16x16x32_bf16 v[56:59], a[48:51], v[48:51], v[56:59]\n" \
  "ds_read_b128 v[40:43], v9 offset:14336\n" \
  "v_mfma_f32_16x16x32_bf16 v[68:71], a[48:51], v[52:55], v[68:71]\n" \
  "ds_read_b128 v[44:47], v9 offset:34816\n" \
  "v_mfma_f32_16x16x32_bf16 v[60:63], a[128:131], v[48:51], v[60:63]\n" \
  "s_add_u32 m0, s55, 4096\n" \
  "v_mfma_f32_16x16x32_bf16 v[72:75], a[128:131], v[52:55], v[72:75]\n" \
  "buffer_load_dwordx4 v1, s[36:39], s72 offen lds\n" \
  "v_mfma_f32_16x16x32_bf16 v[64:67], a[208:211], v[48:51], v[64:67]\n" \
  "v_mfma_f32_16x16x32_bf16 v[76:79], a[208:211], v[52:55], v[76:79]\n" \
  "s_waitcnt lgkmcnt(2)\n" \
  "v_mfma_f32_16x16x32_bf16 v[56:59], a[52:55], v[32:35], v[56:59]\n" \
  "ds_read_b128 v[48:51], v9 offset:15360\n" \
  "v_mfma_f32_16x16x32_bf16 v[68:71], a[52:55], v[36:39], v[68:71]\n" \
  "ds_read_b128 v[52:55], v9 offset:35840\n" \
  "v_mfma_f32_16x16x32_bf16 v[60:63], a[132:135], v[32:35], v[60:63]\n" \
  "buffer_load_dwordx4 v1, s[36:39], s72 offen offset:1024 lds\n" \
  "v_mfma_f32_16x16x32_bf16 v[72:75], a[132:135], v[36:39], v[72:75]\n" \
  "s_cmp_eq_u32 s57, 0\n" \
  "v_mfma_f32_16x16x32_bf16 v[64:67], a[212:215], v[32:35], v[64:67]\n" \
  "s_cselect_b32 s70, s69, s71\n" \
  "v_mfma_f32_16x16x32_bf16 v[76:79], a[212:215], v[36:39], v[76:79]\n" \
  "s_xor_b32 s70, s70, 0x2000\n" \
  "s_waitcnt lgkmcnt(2)\n" \
  "v_mfma_f32_16x16x32_bf16 v[56:59], a[56:59], v[40:43], v[56:59]\n" \
  "ds_read_b128 v[32:35], v9 offset:16384\n" \
  "v_mfma_f32_16x16x32_bf16 v[68:71], a[56:59], v[44:47], v[68:71]\n" \
  "ds_read_b128 v[36:39], v9 offset:36864\n" \
  "v_mfma_f32_16x16x32_bf16 v[60:63], a[136:139], v[40:43], v[60:63]\n" \
  "buffer_load_dwordx4 v1, s[36:39], s72 offen offset:2048 lds\n" \
  "v_mfma_f32_16x16x32_bf16 v[72:75], a[136:139], v[44:47], v[72:75]\n" \
  "s_add_u32 s56, s56, 2\n" \
  "v_mfma_f32_16x16x32_bf16 v[64:67], a[216:219], v[40:43], v[64:67]\n" \
  "v_mfma_f32_16x16x32_bf16 v[76:79], a[216:219], v[44:47], v[76:79]\n" \
  "s_waitcnt lgkmcnt(2)\n" \
  "v_mfma_f32_16x16x32_bf16 v[56:59], a[60:63], v[48:51], v[56:59]\n" \
  "ds_read_b128 v[40:43], v9 offset:17408\n" \
  "v_mfma_f32_16x16x32_bf16 v[68:71], a[60:63], v[52:55], v[68:71]\n" \
  "ds_read_b128 v[44:47], v9 offset:37888\n" \
  "v_mfma_f32_16x16x32_bf16 v[60:63], a[140:143], v[48:51], v[60:63]\n" \
  "buffer_load_dwordx4 v1, s[36:39], s72 offen offset:3072 lds\n" \
  "v_mfma_f32_16x16x32_bf16 v[72:75], a[140:143], v[52:55], v[72:75]\n" \
  "v_mfma_f32_16x16x32_bf16 v[64:67], a[220:223], v[48:51], v[64:67]\n" \
  "v_mfma_f32_16x16x32_bf16 v[76:79], a[220:223], v[52:55], v[76:79]\n" \
  "s_waitcnt lgkmcnt(2)\n" \
  "v_mfma_f32_16x16x32_bf16 v[56:59], a[64:67], v[32:35], v[56:59]\n" \
  "ds_read_b128 v[48:51], v9 offset:18432\n" \
  "v_mfma_f32_16x16x32_bf16 v[68:71], a[64:67], v[36:39], v[68:71]\n" \
  "ds_read_b128 v[52:55], v9 offset:38912\n" \
  "v_mfma_f32_16x16x32_bf16 v[60:63], a[144:147], v[32:35], v[60:63]\n" \
  "s_add_u32 m0, s55, 8192\n" \
  "v_mfma_f32_16x16x32_bf16 v[72:75], a[144:147], v[36:39], v[72:75]\n" \
  "buffer_load_dwordx4 v1, s[36:39], s73 offen lds\n" \
  "v_mfma_f32_16x16x32_bf16 v[64:67], a[224:227], v[32:35], v[64:67]\n" \
  "v_mfma_f32_16x16x32_bf16 v[76:79], a[224:227], v[36:39], v[76:79]\n" \
  "s_waitcnt lgkmcnt(2)\n" \
  "v_mfma_f32_16x16x32_bf16 v[56:59], a[68:71], v[40:43], v[56:59]\n" \
  "ds_read_b128 v[32:35], v9 offset:19456\n" \
  "v_mfma_f32_16x16x32_bf16 v[68:71], a[68:71], v[44:47], v[68:71]\n" \
  "ds_read_b128 v[36:39], v9 offset:39936\n" \
  "v_mfma_f32_16x16x32_bf16 v[60:63], a[148:151], v[40:43], v[60:63]\n" \
  "buffer_load_dwordx4 v1, s[36:39], s73 offen offset:1024 lds\n" \
  "v_mfma_f32_16x16x32_bf16 v[72:75], a[148:151], v[44:47], v[72:75]\n" \
  "s_add_u32 s68, s68, 40960\n" \
  "v_mfma_f32_16x16x32_bf16 v[64:67], a[228:231], v[40:43], v[64:67]\n" \
  "v_mfma_f32_16x16x32_bf16 v[76:79], a[228:231], v[44:47], v[76:79]\n" \
  "s_waitcnt vmcnt(10) lgkmcnt(0)\n" \
  "s_barrier\n" \
  "v_mfma_f32_16x16x32_bf16 v[56:59], a[72:75], v[48:51], v[56:59]\n" \
  "ds_read_b128 v[40:43], v10 offset:0\n" \
  "v_mfma_f32_16x16x32_bf16 v[68:71], a[72:75], v[52:55], v[68:71]\n" \
  "ds_read_b128 v[44:47], v10 offset:20480\n" \
  "v_mfma_f32_16x16x32_bf16 v[60:63], a[152:155], v[48:51], v[60:63]\n" \
  "v_mfma_f32_16x16x32_bf16 v[72:75], a[152:155], v[52:55], v[72:75]\n" \
  "v_mfma_f32_16x16x32_bf16 v[64:67], a[232:235], v[48:51], v[64:67]\n" \
  "v_mfma_f32_16x16x32_bf16 v[76:79], a[232:235], v[52:55], v[76:79]\n" \
  "v_mfma_f32_16x16x32_bf16 v[56:59], a[76:79], v[32:35], v[56:59]\n" \
  "ds_read_b128 v[48:51], v10 offset:1024\n" \
  "v_mfma_f32_16x16x32_bf16 v[68:71], a[76:79], v[36:39], v[68:71]\n" \
  "ds_read_b128 v[52:55], v10 offset:21504\n" \
  "v_mfma_f32_16x16x32_bf16 v[60:63], a[156:159], v[32:35], v[60:63]\n" \
  "s_cmp_eq_u32 s54, 60\n" \
  "v_mfma_f32_16x16x32_bf16 v[72:75], a[156:159], v[36:39], v[72:75]\n" \
  "v_mfma_f32_16x16x32_bf16 v[64:67], a[236:239], v[32:35], v[64:67]\n" \
  "v_mfma_f32_16x16x32_bf16 v[76:79], a[236:239], v[36:39], v[76:79]\n" \
  "s_waitcnt lgkmcnt(2)\n" \
  "v_mfma_f32_16x16x32_bf16 v[100:103], a[0:3], v[40:43], 0\n" \
  "ds_read_b128 v[32:35], v10 offset:2048\n" \
  "v_mfma_f32_16x16x32_bf16 v[112:115], a[0:3], v[44:47], 0\n" \
  "ds_read_b128 v[36:39], v10 offset:22528\n" \
  "v_mfma_f32_16x16x32_bf16 v[104:107], a[80:83], v[40:43], 0\n" \
  "buffer_atomic_add v11, v8, s[40:43], s74 offen\n" \
  "v_mfma_f32_16x16x32_bf16 v[116:119], a[80:83], v[44:47], 0\n" \
  "s_add_u32 s74, s74, 0x2200\n" \
  "v_mfma_f32_16x16x32_bf16 v[108:111], a[160:163], v[40:43], 0\n" \
  "s_and_b32 s74, s74, 0xffffdfff\n" \
  "v_mfma_f32_16x16x32_bf16 v[120:123], a[160:163], v[44:47], 0\n" \
  "s_waitcnt lgkmcnt(2)\n" \
  "v_mfma_f32_16x16x32_bf16 v[100:103], a[4:7], v[48:51], v[100:103]\n" \
  "ds_read_b128 v[40:43], v10 offset:3072\n" \
  "v_mfma_f32_16x16x32_bf16 v[112:115], a[4:7], v[52:55], v[112:115]\n" \
  "ds_read_b128 v[44:47], v10 offset:23552\n" \
  "v_mfma_f32_16x16x32_bf16 v[104:107], a[84:87], v[48:51], v[104:107]\n" \
  "buffer_store_dwordx4 v[12:15], v2, s[40:43], s69 offen sc1\n" \
  "v_mfma_f32_16x16x32_bf16 v[116:119], a[84:87], v[52:55], v[116:119]\n" \
  "v_cvt_pk_bf16_f32 v16, v56, v57\n" \
  "v_mfma_f32_16x16x32_bf16 v[108:111], a[164:167], v[48:51], v[108:111]\n" \
  "v_cvt_pk_bf16_f32 v17, v58, v59\n" \
  "v_mfma_f32_16x16x32_bf16 v[120:123], a[164:167], v[52:55], v[120:123]\n" \
  "v_cvt_pk_bf16_f32 v18, v60, v61\n" \
  "s_waitcnt lgkmcnt(2)\n" \
  "v_mfma_f32_16x16x32_bf16 v[100:103], a[8:11], v[32:35], v[100:103]\n" \
  "ds_read_b128 v[48:51], v10 offset:4096\n" \
  "v_mfma_f32_16x16x32_bf16 v[112:115], a[8:11], v[36:39], v[112:115]\n" \
  "ds_read_b128 v[52:55], v10 offset:24576\n" \
  "v_mfma_f32_16x16x32_bf16 v[104:107], a[88:91], v[32:35], v[104:107]\n" \
  "v_cvt_pk_bf16_f32 v19, v62, v63\n" \
  "v_mfma_f32_16x16x32_bf16 v[116:119], a[88:91], v[36:39], v[116:119]\n" \
  "buffer_store_dwordx4 v[12:15], v3, s[40:43], s69 offen sc1\n" \
  "v_mfma_f32_16x16x32_bf16 v[108:111], a[168:171], v[32:35], v[108:111]\n" \
  "v_cvt_pk_bf16_f32 v20, v68, v69\n" \
  "v_mfma_f32_16x16x32_bf16 v[120:123], a[168:171], v[36:39], v[120:123]\n" \
  "v_cvt_pk_bf16_f32 v21, v70, v71\n" \
  "s_waitcnt lgkmcnt(2)\n" \
  "v_mfma_f32_16x16x32_bf16 v[100:103], a[12:15], v[40:43], v[100:103]\n" \
  "ds_read_b128 v[32:35], v10 offset:5120\n" \
  "v_mfma_f32_16x16x32_bf16 v[112:115], a[12:15], v[44:47], v[112:115]\n" \
  "ds_read_b128 v[36:39], v10 offset:25600\n" \
  "v_mfma_f32_16x16x32_bf16 v[104:107], a[92:95], v[40:43], v[104:107]\n" \
  "v_cvt_pk_bf16_f32 v22, v72, v73\n" \
  "v_mfma_f32_16x16x32_bf16 v[116:119], a[92:95], v[44:47], v[116:119]\n" \
  "v_cvt_pk_bf16_f32 v23, v74, v75\n" \
  "v_mfma_f32_16x16x32_bf16 v[108:111], a[172:175], v[40:43], v[108:111]\n" \
  "buffer_store_dwordx4 v[12:15], v4, s[40:43], s69 offen sc1\n" \
  "v_mfma_f32_16x16x32_bf16 v[120:123], a[172:175], v[44:47], v[120:123]\n" \
  "v_cvt_pk_bf16_f32 v24, v64, v65\n" \
  "s_waitcnt lgkmcnt(2)\n" \
  "v_mfma_f32_16x16x32_bf16 v[100:103], a[16:19], v[48:51], v[100:103]\n" \
  "ds_read_b128 v[40:43], v10 offset:6144\n" \
  "v_mfma_f32_16x16x32_bf16 v[112:115], a[16:19], v[52:55], v[112:115]\n" \
  "ds_read_b128 v[44:47], v10 offset:26624\n" \
  "v_mfma_f32_16x16x32_bf16 v[104:107], a[96:99], v[48:51], v[104:107]\n" \
  "v_cvt_pk_bf16_f32 v25, v66, v67\n" \
  "v_mfma_f32_16x16x32_bf16 v[116:119], a[96:99], v[52:55], v[116:119]\n" \
  "v_cvt_pk_bf16_f32 v26, v76, v77\n" \
  "v_mfma_f32_16x16x32_bf16 v[108:111], a[176:179], v[48:51], v[108:111]\n" \
  "v_cvt_pk_bf16_f32 v27, v78, v79\n" \
  "v_mfma_f32_16x16x32_bf16 v[120:123], a[176:179], v[52:55], v[120:123]\n" \
  "buffer_store_dwordx4 v[12:15], v5, s[40:43], s69 offen sc1\n" \
  "s_waitcnt lgkmcnt(2)\n" \
  "v_mfma_f32_16x16x32_bf16 v[100:103], a[20:23], v[32:35], v[100:103]\n" \
  "ds_read_b128 v[48:51], v10 offset:7168\n" \
  "v_mfma_f32_16x16x32_bf16 v[112:115], a[20:23], v[36:39], v[112:115]\n" \
  "ds_read_b128 v[52:55], v10 offset:27648\n" \
  "v_mfma_f32_16x16x32_bf16 v[104:107], a[100:103], v[32:35], v[104:107]\n" \
  "buffer_store_dwordx4 v[16:19], v6, s[40:43], s70 offen sc1\n" \
  "v_mfma_f32_16x16x32_bf16 v[116:119], a[100:103], v[36:39], v[116:119]\n" \
  "v_mfma_f32_16x16x32_bf16 v[108:111], a[180:183], v[32:35], v[108:111]\n" \
  "v_mfma_f32_16x16x32_bf16 v[120:123], a[180:183], v[36:39], v[120:123]\n" \
  "s_waitcnt lgkmcnt(2)\n" \
  "v_mfma_f32_16x16x32_bf16 v[100:103], a[24:27], v[40:43], v[100:103]\n" \
  "ds_read_b128 v[32:35], v10 offset:8192\n" \
  "v_mfma_f32_16x16x32_bf16 v[112:115], a[24:27], v[44:47], v[112:115]\n" \
  "ds_read_b128 v[36:39], v10 offset:28672\n" \
  "v_mfma_f32_16x16x32_bf16 v[104:107], a[104:107], v[40:43], v[104:107]\n" \
  "buffer_store_dwordx4 v[20:23], v6, s[40:43], s70 offen offset:1024 sc1\n" \
  "v_mfma_f32_16x16x32_bf16 v[116:119], a[104:107], v[44:47], v[116:119]\n" \
  "v_mfma_f32_16x16x32_bf16 v[108:111], a[184:187], v[40:43], v[108:111]\n" \
  "v_mfma_f32_16x16x32_bf16 v[120:123], a[184:187], v[44:47], v[120:123]\n" \
  "s_waitcnt lgkmcnt(2)\n" \
  "v_mfma_f32_16x16x32_bf16 v[100:103], a[28:31], v[48:51], v[100:103]\n" \
  "ds_read_b128 v[40:43], v10 offset:9216\n" \
  "v_mfma_f32_16x16x32_bf16 v[112:115], a[28:31], v[52:55], v[112:115]\n" \
  "ds_read_b128 v[44:47], v10 offset:29696\n" \
  "v_mfma_f32_16x16x32_bf16 v[104:107], a[108:111], v[48:51], v[104:107]\n" \
  "buffer_store_dwordx4 v[24:27], v7, s[40:43], s70 offen sc1\n" \
  "v_mfma_f32_16x16x32_bf16 v[116:119], a[108:111], v[52:55], v[116:119]\n" \
  "v_mfma_f32_16x16x32_bf16 v[108:111], a[188:191], v[48:51], v[108:111]\n" \
  "v_mfma_f32_16x16x32_bf16 v[120:123], a[188:191], v[52:55], v[120:123]\n" \
  "s_waitcnt lgkmcnt(2)\n" \
  "v_mfma_f32_16x16x32_bf16 v[100:103], a[32:35], v[32:35], v[100:103]\n" \
  "ds_read_b128 v[48:51], v10 offset:10240\n" \
  "v_mfma_f32_16x16x32_bf16 v[112:115], a[32:35], v[36:39], v[112:115]\n" \
  "ds_read_b128 v[52:55], v10 offset:30720\n" \
  "v_mfma_f32_16x16x32_bf16 v[104:107], a[112:115], v[32:35], v[104:107]\n" \
  "s_add_u32 s72, s68, 4096\n" \
  "v_mfma_f32_16x16x32_bf16 v[116:119], a[112:115], v[36:39], v[116:119]\n" \
  "s_add_u32 s73, s68, 8192\n" \
  "v_mfma_f32_16x16x32_bf16 v[108:111], a[192:195], v[32:35], v[108:111]\n" \
  "s_add_u32 m0, s55, 40960\n" \
  "v_mfma_f32_16x16x32_bf16 v[120:123], a[192:195], v[36:39], v[120:123]\n" \
  "buffer_load_dwordx4 v1, s[36:39], s68 offen lds\n" \
  "s_waitcnt lgkmcnt(2)\n" \
  "v_mfma_f32_16x16x32_bf16 v[100:103], a[36:39], v[40:43], v[100:103]\n" \
  "ds_read_b128 v[32:35], v10 offset:11264\n" \
  "v_mfma_f32_16x16x32_bf16 v[112:115], a[36:39], v[44:47], v[112:115]\n" \
  "ds_read_b128 v[36:39], v10 offset:31744\n" \
  "v_mfma_f32_16x16x32_bf16 v[104:107], a[116:119], v[40:43], v[104:107]\n" \
  "buffer_load_dwordx4 v1, s[36:39], s68 offen offset:1024 lds\n" \
  "v_mfma_f32_16x16x32_bf16 v[116:119], a[116:119], v[44:47], v[116:119]\n" \
  "s_lshr_b64 s[76:77], s[52:53], s56\n" \
  "v_mfma_f32_16x16x32_bf16 v[108:111], a[196:199], v[40:43], v[108:111]\n" \
  "s_lshl_b32 s69, s56, 8\n" \
  "v_mfma_f32_16x16x32_bf16 v[120:123], a[196:199], v[44:47], v[120:123]\n" \
  "s_and_b32 s70, s76, 1\n" \
  "s_waitcnt lgkmcnt(2)\n" \
  "v_mfma_f32_16x16x32_bf16 v[100:103], a[40:43], v[48:51], v[100:103]\n" \
  "ds_read_b128 v[40:43], v10 offset:12288\n" \
  "v_mfma_f32_16x16x32_bf16 v[112:115], a[40:43], v[52:55], v[112:115]\n" \
  "ds_read_b128 v[44:47], v10 offset:32768\n" \
  "v_mfma_f32_16x16x32_bf16 v[104:107], a[120:123], v[48:51], v[104:107]\n" \
  "buffer_load_dwordx4 v1, s[36:39], s68 offen offset:2048 lds\n" \
  "v_mfma_f32_16x16x32_bf16 v[116:119], a[120:123], v[52:55], v[116:119]\n" \
  "s_bfe_u32 s71, s76, 0x10001\n" \
  "v_mfma_f32_16x16x32_bf16 v[108:111], a[200:203], v[48:51], v[108:111]\n" \
  "s_add_u32 s71, s71, s69\n" \
  "v_mfma_f32_16x16x32_bf16 v[120:123], a[200:203], v[52:55], v[120:123]\n" \
  "s_add_u32 s69, s69, s70\n" \
  "s_waitcnt lgkmcnt(2)\n" \
  "v_mfma_f32_16x16x32_bf16 v[100:103], a[44:47], v[32:35], v[100:103]\n" \
  "ds_read_b128 v[48:51], v10 offset:13312\n" \
  "v_mfma_f32_16x16x32_bf16 v[112:115], a[44:47], v[36:39], v[112:115]\n" \
  "ds_read_b128 v[52:55], v10 offset:33792\n" \
  "v_mfma_f32_16x16x32_bf16 v[104:107], a[124:127], v[32:35], v[104:107]\n" \
  "buffer_load_dwordx4 v1, s[36:39], s68 offen offset:3072 lds\n" \
  "v_mfma_f32_16x16x32_bf16 v[116:119], a[124:127], v[36:39], v[116:119]\n" \
  "s_add_u32 s71, s71, 0x100\n" \
  "v_mfma_f32_16x16x32_bf16 v[108:111], a[204:207], v[32:35], v[108:111]\n" \
  "s_lshl_b32 s69, s69, 13\n" \
  "v_mfma_f32_16x16x32_bf16 v[120:123], a[204:207], v[36:39], v[120:123]\n" \
  "s_lshl_b32 s71, s71, 13\n" \
  "s_waitcnt lgkmcnt(2)\n" \
  "v_mfma_f32_16x16x32_bf16 v[100:103], a[48:51], v[40:43], v[100:103]\n" \
  "ds_read_b128 v[32:35], v10 offset:14336\n" \
  "v_mfma_f32_16x16x32_bf16 v[112:115], a[48:51], v[44:47], v[112:115]\n" \
  "ds_read_b128 v[36:39], v10 offset:34816\n" \
  "v_mfma_f32_16x16x32_bf16 v[104:107], a[128:131], v[40:43], v[104:107]\n" \
  "s_add_u32 m0, s55, 45056\n" \
  "v_mfma_f32_16x16x32_bf16 v[116:119], a[128:131], v[44:47], v[116:119]\n" \
  "buffer_load_dwordx4 v1, s[36:39], s72 offen lds\n" \
  "v_mfma_f32_16x16x32_bf16 v[108:111], a[208:211], v[40:43], v[108:111]\n" \
  "v_mfma_f32_16x16x32_bf16 v[120:123], a[208:211], v[44:47], v[120:123]\n" \
  "s_waitcnt lgkmcnt(2)\n" \
  "v_mfma_f32_16x16x32_bf16 v[100:103], a[52:55], v[48:51], v[100:103]\n" \
  "ds_read_b128 v[40:43], v10 offset:15360\n" \
  "v_mfma_f32_16x16x32_bf16 v[112:115], a[52:55], v[52:55], v[112:115]\n" \
  "ds_read_b128 v[44:47], v10 offset:35840\n" \
  "v_mfma_f32_16x16x32_bf16 v[104:107], a[132:135], v[48:51], v[104:107]\n" \
  "buffer_load_dwordx4 v1, s[36:39], s72 offen offset:1024 lds\n" \
  "v_mfma_f32_16x16x32_bf16 v[116:119], a[132:135], v[52:55], v[116:119]\n" \
  "s_cmp_eq_u32 s57, 0\n" \
  "v_mfma_f32_16x16x32_bf16 v[108:111], a[212:215], v[48:51], v[108:111]\n" \
  "s_cselect_b32 s70, s69, s71\n" \
  "v_mfma_f32_16x16x32_bf16 v[120:123], a[212:215], v[52:55], v[120:123]\n" \
  "s_xor_b32 s70, s70, 0x2000\n" \
  "s_waitcnt lgkmcnt(2)\n" \
  "v_mfma_f32_16x16x32_bf16 v[100:103], a[56:59], v[32:35], v[100:103]\n" \
  "ds_read_b128 v[48:51], v10 offset:16384\n" \
  "v_mfma_f32_16x16x32_bf16 v[112:115], a[56:59], v[36:39], v[112:115]\n" \
  "ds_read_b128 v[52:55], v10 offset:36864\n" \
  "v_mfma_f32_16x16x32_bf16 v[104:107], a[136:139], v[32:35], v[104:107]\n" \
  "buffer_load_dwordx4 v1, s[36:39], s72 offen offset:2048 lds\n" \
  "v_mfma_f32_16x16x32_bf16 v[116:119], a[136:139], v[36:39], v[116:119]\n" \
  "s_add_u32 s56, s56, 2\n" \
  "v_mfma_f32_16x16x32_bf16 v[108:111], a[216:219], v[32:35], v[108:111]\n" \
  "v_mfma_f32_16x16x32_bf16 v[120:123], a[216:219], v[36:39], v[120:123]\n" \
  "s_waitcnt lgkmcnt(2)\n" \
  "v_mfma_f32_16x16x32_bf16 v[100:103], a[60:63], v[40:43], v[100:103]\n" \
  "ds_read_b128 v[32:35], v10 offset:17408\n" \
  "v_mfma_f32_16x16x32_bf16 v[112:115], a[60:63], v[44:47], v[112:115]\n" \
  "ds_read_b128 v[36:39], v10 offset:37888\n" \
  "v_mfma_f32_16x16x32_bf16 v[104:107], a[140:143], v[40:43], v[104:107]\n" \
  "buffer_load_dwordx4 v1, s[36:39], s72 offen offset:3072 lds\n" \
  "v_mfma_f32_16x16x32_bf16 v[116:119], a[140:143], v[44:47], v[116:119]\n" \
  "v_mfma_f32_16x16x32_bf16 v[108:111], a[220:223], v[40:43], v[108:111]\n" \
  "v_mfma_f32_16x16x32_bf16 v[120:123], a[220:223], v[44:47], v[120:123]\n" \
  "s_waitcnt lgkmcnt(2)\n" \
  "v_mfma_f32_16x16x32_bf16 v[100:103], a[64:67], v[48:51], v[100:103]\n" \
  "ds_read_b128 v[40:43], v10 offset:18432\n" \
  "v_mfma_f32_16x16x32_bf16 v[112:115], a[64:67], v[52:55], v[112:115]\n" \
  "ds_read_b128 v[44:47], v10 offset:38912\n" \
  "v_mfma_f32_16x16x32_bf16 v[104:107], a[144:147], v[48:51], v[104:107]\n" \
  "s_add_u32 m0, s55, 49152\n" \
  "v_mfma_f32_16x16x32_bf16 v[116:119], a[144:147], v[52:55], v[116:119]\n" \
  "buffer_load_dwordx4 v1, s[36:39], s73 offen lds\n" \
  "v_mfma_f32_16x16x32_bf16 v[108:111], a[224:227], v[48:51], v[108:111]\n" \
  "v_mfma_f32_16x16x32_bf16 v[120:123], a[224:227], v[52:55], v[120:123]\n" \
  "s_waitcnt lgkmcnt(2)\n" \
  "v_mfma_f32_16x16x32_bf16 v[100:103], a[68:71], v[32:35], v[100:103]\n" \
  "ds_read_b128 v[48:51], v10 offset:19456\n" \
  "v_mfma_f32_16x16x32_bf16 v[112:115], a[68:71], v[36:39], v[112:115]\n" \
  "ds_read_b128 v[52:55], v10 offset:39936\n" \
  "v_mfma_f32_16x16x32_bf16 v[104:107], a[148:151], v[32:35], v[104:107]\n" \
  "buffer_load_dwordx4 v1, s[36:39], s73 offen offset:1024 lds\n" \
  "v_mfma_f32_16x16x32_bf16 v[116:119], a[148:151], v[36:39], v[116:119]\n" \
  "s_add_u32 s68, s68, 40960\n" \
  "v_mfma_f32_16x16x32_bf16 v[108:111], a[228:231], v[32:35], v[108:111]\n" \
  "v_mfma_f32_16x16x32_bf16 v[120:123], a[228:231], v[36:39], v[120:123]\n" \
  "s_waitcnt vmcnt(10) lgkmcnt(0)\n" \
  "s_barrier\n" \
  "v_mfma_f32_16x16x32_bf16 v[100:103], a[72:75], v[40:43], v[100:103]\n" \
  "v_mfma_f32_16x16x32_bf16 v[112:115], a[72:75], v[44:47], v[112:115]\n" \
  "v_mfma_f32_16x16x32_bf16 v[104:107], a[152:155], v[40:43], v[104:107]\n" \
  "v_mfma_f32_16x16x32_bf16 v[116:119], a[152:155], v[44:47], v[116:119]\n" \
  "v_mfma_f32_16x16x32_bf16 v[108:111], a[232:235], v[40:43], v[108:111]\n" \
  "v_mfma_f32_16x16x32_bf16 v[120:123], a[232:235], v[44:47], v[120:123]\n" \
  "v_mfma_f32_16x16x32_bf16 v[100:103], a[76:79], v[48:51], v[100:103]\n" \
  "s_cmp_eq_u32 s54, 60\n" \
  "v_mfma_f32_16x16x32_bf16 v[112:115], a[76:79], v[52:55], v[112:115]\n" \
  "v_mfma_f32_16x16x32_bf16 v[104:107], a[156:159], v[48:51], v[104:107]\n" \
  "v_mfma_f32_16x16x32_bf16 v[116:119], a[156:159], v[52:55], v[116:119]\n" \
  "v_mfma_f32_16x16x32_bf16 v[108:111], a[236:239], v[48:51], v[108:111]\n" \
  "v_mfma_f32_16x16x32_bf16 v[120:123], a[236:239], v[52:55], v[120:123]\n" \
  "LPE_%=:\n" \
  "buffer_atomic_add v11, v8, s[40:43], s74 offen\n" \
  "s_add_u32 s74, s74, 0x2200\n" \
  "s_and_b32 s74, s74, 0xffffdfff\n" \
  "buffer_store_dwordx4 v[12:15], v2, s[40:43], s69 offen sc1\n" \
  "s_nop 3\n" \
  "v_cvt_pk_bf16_f32 v16, v100, v101\n" \
  "v_cvt_pk_bf16_f32 v17, v102, v103\n" \
  "v_cvt_pk_bf16_f32 v18, v104, v105\n" \
  "v_cvt_pk_bf16_f32 v19, v106, v107\n" \
  "v_cvt_pk_bf16_f32 v20, v112, v113\n" \
  "v_cvt_pk_bf16_f32 v21, v114, v115\n" \
  "v_cvt_pk_bf16_f32 v22, v116, v117\n" \
  "v_cvt_pk_bf16_f32 v23, v118, v119\n" \
  "v_cvt_pk_bf16_f32 v24, v108, v109\n" \
  "v_cvt_pk_bf16_f32 v25, v110, v111\n" \
  "v_cvt_pk_bf16_f32 v26, v120, v121\n" \
  "v_cvt_pk_bf16_f32 v27, v122, v123\n" \
  "buffer_store_dwordx4 v[12:15], v3, s[40:43], s69 offen sc1\n" \
  "buffer_store_dwordx4 v[12:15], v4, s[40:43], s69 offen sc1\n" \
  "buffer_store_dwordx4 v[12:15], v5, s[40:43], s69 offen sc1\n" \
  "buffer_store_dwordx4 v[16:19], v6, s[40:43], s70 offen sc1\n" \
  "buffer_store_dwordx4 v[20:23], v6, s[40:43], s70 offen offset:1024 sc1\n" \
  "buffer_store_dwordx4 v[24:27], v7, s[40:43], s70 offen sc1\n" \
  "s_waitcnt vmcnt(0) lgkmcnt(0)\n" \
  "s_barrier\n" \
  "buffer_atomic_add v11, v8, s[40:43], s74 offen\n"
#define WO1_CLOBBERS \
  "v108", "v109", "v110", "v111", "v112", "v113", "v114", "v115", "v116", "v117", "v118", "v119", "v120", "v121", "v122", "v123", "v124", "v125", \
  "v126", "v127", "v128", "v129", "v130", "v131", "v132", "v133", "v134", "v135", "v136", "v137", "v138", "v139", "v140", "v141", "v142", "v143", \
  "v144", "v145", "v146", "v147", "v148", "v149", "v150", "v151", "v152", "v153", "v154", "v155", "v156", "v157", "v158", "v159", "v160", "v161", \
  "v162", "v163", "v164", "v165", "v166", "v167", "v168", "v169", "v170", "v171", "v172", "v173", "v174", "v175", "v176", "v177", "v178", "v179", \
  "v180", "v181", "v182", "v183", "v184", "v185", "v186", "v187", "v188", "v189", "v190", "v191", "v192", "v193", "v194", "v195", "v196", "v197", \
  "v198", "v199", "v200", "v201", "v202", "v203", "v204", "v205", "v206", "v207", "v208", "v209", "v210", "v211", "v212", "v213", "v214", "v215", \
  "v216", "v217", "v218", "v219", "v220", "v221", "v222", "v223", "v224", "v225", "v226", "v227", "v228", "v229", "v230", "v231", "v232", "v233", \
  "v234", "v235", "v236", "v237", "v238", "v239", "v240", "v241", "v242", "v243", "v244", "v245", "v246", "v247", "v248", "v249", "v250", "v251", \
  "v252", "v253", "scc"
#define WO1_ASM \
  "v_add_u32 v107, 32768, v101\n" \
  "v_add_u32 v108, 4096, v102\n" \
  "ds_read_b128 v[112:115], v100 offset:0\n" \
  "ds_read_b128 v[116:119], v100 offset:3072\n" \
  "ds_read_b128 v[120:123], v100 offset:6144\n" \
  "ds_read_b128 v[124:127], v100 offset:9216\n" \
  "ds_read_b64 v[128:129], v100 offset:2048\n" \
  "ds_read_b64 v[130:131], v100 offset:5120\n" \
  "ds_read_b64 v[132:133], v100 offset:8192\n" \
  "ds_read_b64 v[134:135], v100 offset:11264\n" \
  "s_waitcnt lgkmcnt(7)\n" \
  "v_mfma_f32_16x16x32_bf16 v[160:163], v[112:115], a[0:3], 0\n" \
  "v_mfma_f32_16x16x32_bf16 v[164:167], v[112:115], a[24:27], 0\n" \
  "v_mfma_f32_16x16x32_bf16 v[168:171], v[112:115], a[48:51], 0\n" \
  "v_mfma_f32_16x16x32_bf16 v[172:175], v[112:115], a[72:75], 0\n" \
  "v_mfma_f32_16x16x32_bf16 v[176:179], v[112:115], a[96:99], 0\n" \
  "v_mfma_f32_16x16x32_bf16 v[180:183], v[112:115], a[120:123], 0\n" \
  "v_mfma_f32_16x16x32_bf16 v[184:187], v[112:115], a[144:147], 0\n" \
  "v_mfma_f32_16x16x32_bf16 v[188:191], v[112:115], a[168:171], 0\n" \
  "v_mfma_f32_16x16x32_bf16 v[192:195], v[112:115], a[192:195], 0\n" \
  "v_mfma_f32_16x16x32_bf16 v[196:199], v[112:115], a[216:219], 0\n" \
  "s_waitcnt lgkmcnt(6)\n" \
  "v_mfma_f32_16x16x32_bf16 v[160:163], v[116:119], a[4:7], v[160:163]\n" \
  "v_mfma_f32_16x16x32_bf16 v[164:167], v[116:119], a[28:31], v[164:167]\n" \
  "ds_read_b128 v[136:139], v100 offset:1024\n" \
  "v_mfma_f32_16x16x32_bf16 v[168:171], v[116:119], a[52:55], v[168:171]\n" \
  "ds_read_b128 v[140:143], v100 offset:4096\n" \
  "v_mfma_f32_16x16x32_bf16 v[172:175], v[116:119], a[76:79], v[172:175]\n" \
  "ds_read_b128 v[144:147], v100 offset:7168\n" \
  "v_mfma_f32_16x16x32_bf16 v[176:179], v[116:119], a[100:103], v[176:179]\n" \
  "ds_read_b128 v[148:151], v100 offset:10240\n" \
  "v_mfma_f32_16x16x32_bf16 v[180:183], v[116:119], a[124:127], v[180:183]\n" \
  "ds_read_b64 v[152:153], v100 offset:2056\n" \
  "v_mfma_f32_16x16x32_bf16 v[184:187], v[116:119], a[148:151], v[184:187]\n" \
  "ds_read_b64 v[154:155], v100 offset:5128\n" \
  "v_mfma_f32_16x16x32_bf16 v[188:191], v[116:119], a[172:175], v[188:191]\n" \
  "ds_read_b64 v[156:157], v100 offset:8200\n" \
  "v_mfma_f32_16x16x32_bf16 v[192:195], v[116:119], a[196:199], v[192:195]\n" \
  "ds_read_b64 v[158:159], v100 offset:11272\n" \
  "v_mfma_f32_16x16x32_bf16 v[196:199], v[116:119], a[220:223], v[196:199]\n" \
  "s_waitcnt lgkmcnt(13)\n" \
  "v_mfma_f32_16x16x32_bf16 v[160:163], v[120:123], a[8:11], v[160:163]\n" \
  "v_mfma_f32_16x16x32_bf16 v[164:167], v[120:123], a[32:35], v[164:167]\n" \
  "v_mfma_f32_16x16x32_bf16 v[168:171], v[120:123], a[56:59], v[168:171]\n" \
  "v_mfma_f32_16x16x32_bf16 v[172:175], v[120:123], a[80:83], v[172:175]\n" \
  "v_mfma_f32_16x16x32_bf16 v[176:179], v[120:123], a[104:107], v[176:179]\n" \
  "v_mfma_f32_16x16x32_bf16 v[180:183], v[120:123], a[128:131], v[180:183]\n" \
  "v_mfma_f32_16x16x32_bf16 v[184:187], v[120:123], a[152:155], v[184:187]\n" \
  "v_mfma_f32_16x16x32_bf16 v[188:191], v[120:123], a[176:179], v[188:191]\n" \
  "v_mfma_f32_16x16x32_bf16 v[192:195], v[120:123], a[200:203], v[192:195]\n" \
  "v_mfma_f32_16x16x32_bf16 v[196:199], v[120:123], a[224:227], v[196:199]\n" \
  "s_waitcnt lgkmcnt(12)\n" \
  "v_mfma_f32_16x16x32_bf16 v[160:163], v[124:127], a[12:15], v[160:163]\n" \
  "v_mfma_f32_16x16x32_bf16 v[164:167], v[124:127], a[36:39], v[164:167]\n" \
  "v_mfma_f32_16x16x32_bf16 v[168:171], v[124:127], a[60:63], v[168:171]\n" \
  "v_mfma_f32_16x16x32_bf16 v[172:175], v[124:127], a[84:87], v[172:175]\n" \
  "v_mfma_f32_16x16x32_bf16 v[176:179], v[124:127], a[108:111], v[176:179]\n" \
  "v_mfma_f32_16x16x32_bf16 v[180:183], v[124:127], a[132:135], v[180:183]\n" \
  "v_mfma_f32_16x16x32_bf16 v[184:187], v[124:127], a[156:159], v[184:187]\n" \
  "v_mfma_f32_16x16x32_bf16 v[188:191], v[124:127], a[180:183], v[188:191]\n" \
  "v_mfma_f32_16x16x32_bf16 v[192:195], v[124:127], a[204:207], v[192:195]\n" \
  "v_mfma_f32_16x16x32_bf16 v[196:199], v[124:127], a[228:231], v[196:199]\n" \
  "s_waitcnt lgkmcnt(10)\n" \
  "v_mfma_f32_16x16x32_bf16 v[160:163], v[128:131], a[16:19], v[160:163]\n" \
  "v_mfma_f32_16x16x32_bf16 v[164:167], v[128:131], a[40:43], v[164:167]\n" \
  "v_mfma_f32_16x16x32_bf16 v[168:171], v[128:131], a[64:67], v[168:171]\n" \
  "v_mfma_f32_16x16x32_bf16 v[172:175], v[128:131], a[88:91], v[172:175]\n" \
  "v_mfma_f32_16x16x32_bf16 v[176:179], v[128:131], a[112:115], v[176:179]\n" \
  "v_mfma_f32_16x16x32_bf16 v[180:183], v[128:131], a[136:139], v[180:183]\n" \
  "v_mfma_f32_16x16x32_bf16 v[184:187], v[128:131], a[160:163], v[184:187]\n" \
  "v_mfma_f32_16x16x32_bf16 v[188:191], v[128:131], a[184:187], v[188:191]\n" \
  "v_mfma_f32_16x16x32_bf16 v[192:195], v[128:131], a[208:211], v[192:195]\n" \
  "v_mfma_f32_16x16x32_bf16 v[196:199], v[128:131], a[232:235], v[196:199]\n" \
  "s_waitcnt lgkmcnt(8)\n" \
  "v_mfma_f32_16x16x32_bf16 v[160:163], v[132:135], a[20:23], v[160:163]\n" \
  "v_mfma_f32_16x16x32_bf16 v[164:167], v[132:135], a[44:47], v[164:167]\n" \
  "v_mfma_f32_16x16x32_bf16 v[168:171], v[132:135], a[68:71], v[168:171]\n" \
  "v_mfma_f32_16x16x32_bf16 v[172:175], v[132:135], a[92:95], v[172:175]\n" \
  "v_mfma_f32_16x16x32_bf16 v[176:179], v[132:135], a[116:119], v[176:179]\n" \
  "v_mfma_f32_16x16x32_bf16 v[180:183], v[132:135], a[140:143], v[180:183]\n" \
  "v_mfma_f32_16x16x32_bf16 v[184:187], v[132:135], a[164:167], v[184:187]\n" \
  "v_mfma_f32_16x16x32_bf16 v[188:191], v[132:135], a[188:191], v[188:191]\n" \
  "v_mfma_f32_16x16x32_bf16 v[192:195], v[132:135], a[212:215], v[192:195]\n" \
  "v_mfma_f32_16x16x32_bf16 v[196:199], v[132:135], a[236:239], v[196:199]\n" \
  "s_waitcnt lgkmcnt(7)\n" \
  "v_mfma_f32_16x16x32_bf16 v[200:203], v[136:139], a[0:3], 0\n" \
  "v_mfma_f32_16x16x32_bf16 v[204:207], v[136:139], a[24:27], 0\n" \
  "v_mfma_f32_16x16x32_bf16 v[208:211], v[136:139], a[48:51], 0\n" \
  "v_mfma_f32_16x16x32_bf16 v[212:215], v[136:139], a[72:75], 0\n" \
  "v_mfma_f32_16x16x32_bf16 v[216:219], v[136:139], a[96:99], 0\n" \
  "v_cvt_pk_bf16_f32 v240, v160, v164\n" \
  "v_mfma_f32_16x16x32_bf16 v[220:223], v[136:139], a[120:123], 0\n" \
  "v_cvt_pk_bf16_f32 v241, v168, v172\n" \
  "v_mfma_f32_16x16x32_bf16 v[224:227], v[136:139], a[144:147], 0\n" \
  "v_cvt_pk_bf16_f32 v242, v176, v180\n" \
  "v_mfma_f32_16x16x32_bf16 v[228:231], v[136:139], a[168:171], 0\n" \
  "v_cvt_pk_bf16_f32 v243, v184, v188\n" \
  "v_mfma_f32_16x16x32_bf16 v[232:235], v[136:139], a[192:195], 0\n" \
  "v_cvt_pk_bf16_f32 v248, v192, v196\n" \
  "v_mfma_f32_16x16x32_bf16 v[236:239], v[136:139], a[216:219], 0\n" \
  "buffer_store_dwordx4 v[240:243], v101, s[36:39], s40 offen offset:0 sc1\n" \
  "s_waitcnt lgkmcnt(6)\n" \
  "v_mfma_f32_16x16x32_bf16 v[200:203], v[140:143], a[4:7], v[200:203]\n" \
  "v_mfma_f32_16x16x32_bf16 v[204:207], v[140:143], a[28:31], v[204:207]\n" \
  "v_mfma_f32_16x16x32_bf16 v[208:211], v[140:143], a[52:55], v[208:211]\n" \
  "v_mfma_f32_16x16x32_bf16 v[212:215], v[140:143], a[76:79], v[212:215]\n" \
  "buffer_store_dword v248, v102, s[36:39], s40 offen offset:0 sc1\n" \
  "v_mfma_f32_16x16x32_bf16 v[216:219], v[140:143], a[100:103], v[216:219]\n" \
  "v_cvt_pk_bf16_f32 v244, v161, v165\n" \
  "v_mfma_f32_16x16x32_bf16 v[220:223], v[140:143], a[124:127], v[220:223]\n" \
  "v_cvt_pk_bf16_f32 v245, v169, v173\n" \
  "v_mfma_f32_16x16x32_bf16 v[224:227], v[140:143], a[148:151], v[224:227]\n" \
  "v_cvt_pk_bf16_f32 v246, v177, v181\n" \
  "v_mfma_f32_16x16x32_bf16 v[228:231], v[140:143], a[172:175], v[228:231]\n" \
  "v_cvt_pk_bf16_f32 v247, v185, v189\n" \
  "v_mfma_f32_16x16x32_bf16 v[232:235], v[140:143], a[196:199], v[232:235]\n" \
  "v_cvt_pk_bf16_f32 v249, v193, v197\n" \
  "v_mfma_f32_16x16x32_bf16 v[236:239], v[140:143], a[220:223], v[236:239]\n" \
  "buffer_store_dwordx4 v[244:247], v101, s[36:39], s40 offen offset:1024 sc1\n" \
  "s_waitcnt lgkmcnt(5)\n" \
  "v_mfma_f32_16x16x32_bf16 v[200:203], v[144:147], a[8:11], v[200:203]\n" \
  "v_mfma_f32_16x16x32_bf16 v[204:207], v[144:147], a[32:35], v[204:207]\n" \
  "v_mfma_f32_16x16x32_bf16 v[208:211], v[144:147], a[56:59], v[208:211]\n" \
  "v_mfma_f32_16x16x32_bf16 v[212:215], v[144:147], a[80:83], v[212:215]\n" \
  "buffer_store_dword v249, v102, s[36:39], s40 offen offset:256 sc1\n" \
  "v_mfma_f32_16x16x32_bf16 v[216:219], v[144:147], a[104:107], v[216:219]\n" \
  "v_cvt_pk_bf16_f32 v240, v162, v166\n" \
  "v_mfma_f32_16x16x32_bf16 v[220:223], v[144:147], a[128:131], v[220:223]\n" \
  "v_cvt_pk_bf16_f32 v241, v170, v174\n" \
  "v_mfma_f32_16x16x32_bf16 v[224:227], v[144:147], a[152:155], v[224:227]\n" \
  "v_cvt_pk_bf16_f32 v242, v178, v182\n" \
  "v_mfma_f32_16x16x32_bf16 v[228:231], v[144:147], a[176:179], v[228:231]\n" \
  "v_cvt_pk_bf16_f32 v243, v186, v190\n" \
  "v_mfma_f32_16x16x32_bf16 v[232:235], v[144:147], a[200:203], v[232:235]\n" \
  "v_cvt_pk_bf16_f32 v248, v194, v198\n" \
  "v_mfma_f32_16x16x32_bf16 v[236:239], v[144:147], a[224:227], v[236:239]\n" \
  "buffer_store_dwordx4 v[240:243], v101, s[36:39], s40 offen offset:2048 sc1\n" \
  "s_waitcnt lgkmcnt(4)\n" \
  "v_mfma_f32_16x16x32_bf16 v[200:203], v[148:151], a[12:15], v[200:203]\n" \
  "v_mfma_f32_16x16x32_bf16 v[204:207], v[148:151], a[36:39], v[204:207]\n" \
  "v_mfma_f32_16x16x32_bf16 v[208:211], v[148:151], a[60:63], v[208:211]\n" \
  "v_mfma_f32_16x16x32_bf16 v[212:215], v[148:151], a[84:87], v[212:215]\n" \
  "buffer_store_dword v248, v102, s[36:39], s40 offen offset:512 sc1\n" \
  "v_mfma_f32_16x16x32_bf16 v[216:219], v[148:151], a[108:111], v[216:219]\n" \
  "v_cvt_pk_bf16_f32 v244, v163, v167\n" \
  "v_mfma_f32_16x16x32_bf16 v[220:223], v[148:151], a[132:135], v[220:223]\n" \
  "v_cvt_pk_bf16_f32 v245, v171, v175\n" \
  "v_mfma_f32_16x16x32_bf16 v[224:227], v[148:151], a[156:159], v[224:227]\n" \
  "v_cvt_pk_bf16_f32 v246, v179, v183\n" \
  "v_mfma_f32_16x16x32_bf16 v[228:231], v[148:151], a[180:183], v[228:231]\n" \
  "v_cvt_pk_bf16_f32 v247, v187, v191\n" \
  "v_mfma_f32_16x16x32_bf16 v[232:235], v[148:151], a[204:207], v[232:235]\n" \
  "v_cvt_pk_bf16_f32 v249, v195, v199\n" \
  "v_mfma_f32_16x16x32_bf16 v[236:239], v[148:151], a[228:231], v[236:239]\n" \
  "buffer_store_dwordx4 v[244:247], v101, s[36:39], s40 offen offset:3072 sc1\n" \
  "s_waitcnt lgkmcnt(2)\n" \
  "v_mfma_f32_16x16x32_bf16 v[200:203], v[152:155], a[16:19], v[200:203]\n" \
  "v_mfma_f32_16x16x32_bf16 v[204:207], v[152:155], a[40:43], v[204:207]\n" \
  "v_mfma_f32_16x16x32_bf16 v[208:211], v[152:155], a[64:67], v[208:211]\n" \
  "v_mfma_f32_16x16x32_bf16 v[212:215], v[152:155], a[88:91], v[212:215]\n" \
  "buffer_store_dword v249, v102, s[36:39], s40 offen offset:768 sc1\n" \
  "v_mfma_f32_16x16x32_bf16 v[216:219], v[152:155], a[112:115], v[216:219]\n" \
  "v_mfma_f32_16x16x32_bf16 v[220:223], v[152:155], a[136:139], v[220:223]\n" \
  "v_mfma_f32_16x16x32_bf16 v[224:227], v[152:155], a[160:163], v[224:227]\n" \
  "v_mfma_f32_16x16x32_bf16 v[228:231], v[152:155], a[184:187], v[228:231]\n" \
  "v_mfma_f32_16x16x32_bf16 v[232:235], v[152:155], a[208:211], v[232:235]\n" \
  "v_mfma_f32_16x16x32_bf16 v[236:239], v[152:155], a[232:235], v[236:239]\n" \
  "s_waitcnt lgkmcnt(0)\n" \
  "v_mfma_f32_16x16x32_bf16 v[200:203], v[156:159], a[20:23], v[200:203]\n" \
  "v_mfma_f32_16x16x32_bf16 v[204:207], v[156:159], a[44:47], v[204:207]\n" \
  "v_mfma_f32_16x16x32_bf16 v[208:211], v[156:159], a[68:71], v[208:211]\n" \
  "v_mfma_f32_16x16x32_bf16 v[212:215], v[156:159], a[92:95], v[212:215]\n" \
  "v_mfma_f32_16x16x32_bf16 v[216:219], v[156:159], a[116:119], v[216:219]\n" \
  "v_mfma_f32_16x16x32_bf16 v[220:223], v[156:159], a[140:143], v[220:223]\n" \
  "v_mfma_f32_16x16x32_bf16 v[224:227], v[156:159], a[164:167], v[224:227]\n" \
  "v_mfma_f32_16x16x32_bf16 v[228:231], v[156:159], a[188:191], v[228:231]\n" \
  "v_mfma_f32_16x16x32_bf16 v[232:235], v[156:159], a[212:215], v[232:235]\n" \
  "v_mfma_f32_16x16x32_bf16 v[236:239], v[156:159], a[236:239], v[236:239]\n" \
  "s_nop 7\n" \
  "v_cvt_pk_bf16_f32 v240, v200, v204\n" \
  "v_cvt_pk_bf16_f32 v241, v208, v212\n" \
  "v_cvt_pk_bf16_f32 v242, v216, v220\n" \
  "v_cvt_pk_bf16_f32 v243, v224, v228\n" \
  "v_cvt_pk_bf16_f32 v248, v232, v236\n" \
  "buffer_store_dwordx4 v[240:243], v107, s[36:39], s40 offen offset:0 sc1\n" \
  "buffer_store_dword v248, v108, s[36:39], s40 offen offset:0 sc1\n" \
  "v_cvt_pk_bf16_f32 v244, v201, v205\n" \
  "v_cvt_pk_bf16_f32 v245, v209, v213\n" \
  "v_cvt_pk_bf16_f32 v246, v217, v221\n" \
  "v_cvt_pk_bf16_f32 v247, v225, v229\n" \
  "v_cvt_pk_bf16_f32 v249, v233, v237\n" \
  "buffer_store_dwordx4 v[244:247], v107, s[36:39], s40 offen offset:1024 sc1\n" \
  "buffer_store_dword v249, v108, s[36:39], s40 offen offset:256 sc1\n" \
  "v_cvt_pk_bf16_f32 v240, v202, v206\n" \
  "v_cvt_pk_bf16_f32 v241, v210, v214\n" \
  "v_cvt_pk_bf16_f32 v242, v218, v222\n" \
  "v_cvt_pk_bf16_f32 v243, v226, v230\n" \
  "v_cvt_pk_bf16_f32 v248, v234, v238\n" \
  "buffer_store_dwordx4 v[240:243], v107, s[36:39], s40 offen offset:2048 sc1\n" \
  "buffer_store_dword v248, v108, s[36:39], s40 offen offset:512 sc1\n" \
  "v_cvt_pk_bf16_f32 v244, v203, v207\n" \
  "v_cvt_pk_bf16_f32 v245, v211, v215\n" \
  "v_cvt_pk_bf16_f32 v246, v219, v223\n" \
  "v_cvt_pk_bf16_f32 v247, v227, v231\n" \
  "v_cvt_pk_bf16_f32 v249, v235, v239\n" \
  "buffer_store_dwordx4 v[244:247], v107, s[36:39], s40 offen offset:3072 sc1\n" \
  "buffer_store_dword v249, v108, s[36:39], s40 offen offset:768 sc1\n" \
  "s_nop 1\n"
#define HEAD_CLOBBERS \
  "v44", "v45", "v46", "v47", "v48", "v49", "v50", "v51", "v52", "v53", "v54", "v55", "v56", "v57", "v58", "v59", "v60", "v61", "v62", "v63", "v64", \
  "v65", "v66", "v67", "v68", "v69", "v70", "v71", "v72", "v73", "v74", "v75", "v76", "v77", "v78", "v79", "v80", "v81", "v82", "v83", "v84", "v85", \
  "v86", "v87", "v88", "v89", "v90", "v91", "v92", "v93", "v94", "v95", "v100", "v101", "v102", "v103", "v104", "v105", "v106", "v107", "v108", "v109", \
  "v110", "v111", "v112", "v113", "v114", "v115", "v116", "v117", "v118", "v119", "v120", "v121", "v122", "v123", "v124", "v125", "v126", "v127", \
  "v128", "v129", "v130", "v131", "v132", "v133", "v134", "v135", "v136", "v137", "v138", "v139", "v140", "v141", "v142", "v143", "v144", "v145", \
  "v146", "v147", "v148", "v149", "v150", "v151", "v152", "v153", "v154", "v155", "v156", "v157", "v158", "v159", "v160", "v161", "v162", "v163", \
  "v164", "v165", "v166", "v167", "v168", "v169", "v170", "v171", "v172", "v173", "v174", "v175", "v176", "v177", "v178", "v179", "v180", "v181", \
  "v182", "v183", "v184", "v185", "v186", "v187", "v188", "v189", "v190", "v191", "v192", "v193", "v194", "v195", "v196", "v197", "v198", "v199", \
  "v200", "v201", "v202", "v203", "v204", "v205", "v206", "v207", "v208", "v209", "v210", "v211", "v212", "v213", "v214", "v215", "v216", "v217", \
  "v218", "v219", "v220", "v221", "v222", "v223", "v224", "v225", "v226", "v227", "v228", "v229", "v230", "v231", "v232", "v233", "v234", "v235", \
  "v236", "v237", "v238", "v239", "v240", "v241", "v242", "v243", "v244", "v245", "v246", "v247", "v248", "v249", "v250", "v251", "s52", "s53", "s54", \
  "s55", "s56", "s57", "s58", "s59", "s60", "s61", "s62", "s63", "scc"
#define HEAD_ASM \
  "v_add_u32 v21, 32768, v10\n" \
  "v_add_u32 v22, 4096, v11\n" \
  "v_mov_b32 v24, 1\n" \
  "v_mov_b32 v90, 0\n" \
  "v_mov_b32 v91, 0\n" \
  "v_mov_b32 v94, 0\n" \
  "v_mov_b32 v95, 0\n" \
  "s_mov_b32 s56, 0x40000000\n" \
  "s_mov_b32 s57, 4294918144\n" \
  "s_mov_b32 s63, 0x40000000\n" \
  "s_waitcnt vmcnt(0) lgkmcnt(0)\n" \
  "LHL_%=:\n" \
  "s_waitcnt vmcnt(29)\n" \
  "s_waitcnt lgkmcnt(0)\n" \
  "s_barrier\n" \
  "ds_read_b128 v[44:47], v0 offset:0\n" \
  "ds_read_b128 v[48:51], v0 offset:3072\n" \
  "s_waitcnt lgkmcnt(1)\n" \
  "v_mfma_f32_16x16x32_bf16 v[100:103], v[44:47], a[0:3], 0\n" \
  "buffer_load_dword v25, v17, s[36:39], 0 offen sc1\n" \
  "v_mfma_f32_16x16x32_bf16 v[104:107], v[44:47], a[24:27], 0\n" \
  "ds_read_b128 v[52:55], v0 offset:6144\n" \
  "s_lshr_b64 s[58:59], s[40:41], s50\n" \
  "s_lshl_b32 s60, s50, 8\n" \
  "v_mfma_f32_16x16x32_bf16 v[108:111], v[44:47], a[48:51], 0\n" \
  "s_bfe_u32 s54, s58, 65537\n" \
  "s_add_u32 s54, s54, s60\n" \
  "s_add_u32 s54, s54, 256\n" \
  "s_lshl_b32 s54, s54, 13\n" \
  "v_mfma_f32_16x16x32_bf16 v[112:115], v[44:47], a[72:75], 0\n" \
  "s_bfe_u32 s55, s58, 65539\n" \
  "s_add_u32 s55, s55, s60\n" \
  "s_add_u32 s55, s55, 768\n" \
  "s_lshl_b32 s55, s55, 13\n" \
  "v_mfma_f32_16x16x32_bf16 v[116:119], v[44:47], a[96:99], 0\n" \
  "v_cvt_pk_bf16_f32 v56, v140, v144\n" \
  "v_cvt_pk_bf16_f32 v57, v148, v152\n" \
  "v_mfma_f32_16x16x32_bf16 v[120:123], v[44:47], a[120:123], 0\n" \
  "v_cvt_pk_bf16_f32 v58, v156, v160\n" \
  "v_cvt_pk_bf16_f32 v59, v164, v168\n" \
  "v_mfma_f32_16x16x32_bf16 v[124:127], v[44:47], a[144:147], 0\n" \
  "s_lshr_b32 s58, s58, s43\n" \
  "s_lshl_b32 s61, s43, 8\n" \
  "s_add_u32 s60, s60, s61\n" \
  "s_bfe_u32 s52, s58, 65536\n" \
  "v_mfma_f32_16x16x32_bf16 v[128:131], v[44:47], a[168:171], 0\n" \
  "v_cvt_pk_bf16_f32 v64, v172, v176\n" \
  "buffer_store_dwordx4 v[56:59], v21, s[36:39], s56 offen offset:0 sc1\n" \
  "v_mfma_f32_16x16x32_bf16 v[132:135], v[44:47], a[192:195], 0\n" \
  "s_xor_b32 s52, s52, 1\n" \
  "s_add_u32 s52, s52, s60\n" \
  "s_lshl_b32 s52, s52, 13\n" \
  "s_bfe_u32 s53, s58, 65538\n" \
  "v_mfma_f32_16x16x32_bf16 v[136:139], v[44:47], a[216:219], 0\n" \
  "s_xor_b32 s53, s53, 513\n" \
  "s_add_u32 s53, s53, s60\n" \
  "s_lshl_b32 s53, s53, 13\n" \
  "s_add_u32 s50, s50, 4\n" \
  "s_waitcnt lgkmcnt(1)\n" \
  "v_mfma_f32_16x16x32_bf16 v[100:103], v[48:51], a[4:7], v[100:103]\n" \
  "s_cmp_eq_u32 s50, 60\n" \
  "s_cselect_b32 s50, 0, s50\n" \
  "s_add_u32 m0, s44, 0\n" \
  "s_nop 0\n" \
  "buffer_load_dwordx4 v12, s[36:39], s52 offen offset:0 sc1 lds\n" \
  "v_mfma_f32_16x16x32_bf16 v[104:107], v[48:51], a[28:31], v[104:107]\n" \
  "ds_read_b128 v[44:47], v0 offset:9216\n" \
  "ds_read_b128 v[180:183], v1 offset:0\n" \
  "v_mfma_f32_16x16x32_bf16 v[108:111], v[48:51], a[52:55], v[108:111]\n" \
  "ds_read_b128 v[184:187], v4 offset:0\n" \
  "ds_read_b128 v[188:191], v3 offset:3072\n" \
  "v_mfma_f32_16x16x32_bf16 v[112:115], v[48:51], a[76:79], v[112:115]\n" \
  "buffer_store_dword v64, v22, s[36:39], s56 offen offset:0 sc1\n" \
  "v_mfma_f32_16x16x32_bf16 v[116:119], v[48:51], a[100:103], v[116:119]\n" \
  "ds_read_b128 v[192:195], v3 offset:4096\n" \
  "ds_read_b128 v[196:199], v1 offset:3072\n" \
  "v_mfma_f32_16x16x32_bf16 v[120:123], v[48:51], a[124:127], v[120:123]\n" \
  "v_cvt_pk_bf16_f32 v60, v141, v145\n" \
  "v_cvt_pk_bf16_f32 v61, v149, v153\n" \
  "v_mfma_f32_16x16x32_bf16 v[124:127], v[48:51], a[148:151], v[124:127]\n" \
  "buffer_load_dwordx4 v12, s[36:39], s52 offen offset:1024 sc1 lds\n" \
  "v_mfma_f32_16x16x32_bf16 v[128:131], v[48:51], a[172:175], v[128:131]\n" \
  "v_cvt_pk_bf16_f32 v62, v157, v161\n" \
  "v_cvt_pk_bf16_f32 v63, v165, v169\n" \
  "v_mfma_f32_16x16x32_bf16 v[132:135], v[48:51], a[196:199], v[132:135]\n" \
  "ds_read_b128 v[200:203], v4 offset:8192\n" \
  "ds_read_b128 v[204:207], v3 offset:11264\n" \
  "v_mfma_f32_16x16x32_bf16 v[136:139], v[48:51], a[220:223], v[136:139]\n" \
  "v_cvt_pk_bf16_f32 v65, v173, v177\n" \
  "buffer_store_dwordx4 v[60:63], v21, s[36:39], s56 offen offset:1024 sc1\n" \
  "s_waitcnt lgkmcnt(8)\n" \
  "v_mfma_f32_16x16x32_bf16 v[100:103], v[52:55], a[8:11], v[100:103]\n" \
  "ds_read_b128 v[208:211], v3 offset:12288\n" \
  "ds_read_b128 v[212:215], v1 offset:6144\n" \
  "v_mfma_f32_16x16x32_bf16 v[104:107], v[52:55], a[32:35], v[104:107]\n" \
  "ds_read_b64 v[48:49], v0 offset:2048\n" \
  "ds_read_b64 v[50:51], v0 offset:5120\n" \
  "v_mfma_f32_16x16x32_bf16 v[108:111], v[52:55], a[56:59], v[108:111]\n" \
  "buffer_load_dwordx4 v13, s[36:39], s52 offen offset:2048 sc1 lds\n" \
  "v_mfma_f32_16x16x32_bf16 v[112:115], v[52:55], a[80:83], v[112:115]\n" \
  "s_add_u32 m0, s44, 12288\n" \
  "ds_read_b128 v[216:219], v4 offset:16384\n" \
  "ds_read_b128 v[220:223], v3 offset:19456\n" \
  "v_mfma_f32_16x16x32_bf16 v[116:119], v[52:55], a[104:107], v[116:119]\n" \
  "ds_read_b128 v[224:227], v3 offset:20480\n" \
  "s_waitcnt lgkmcnt(12)\n" \
  "v_mfma_f32_16x16x32_bf16 v[66:69], v[184:187], v[180:183], v[32:35]\n" \
  "s_waitcnt lgkmcnt(11)\n" \
  "v_mfma_f32_16x16x32_bf16 v[70:73], v[188:191], v[180:183], v[36:39]\n" \
  "s_waitcnt lgkmcnt(10)\n" \
  "v_mfma_f32_16x16x32_bf16 v[74:77], v[192:195], v[180:183], v[40:43]\n" \
  "ds_read_b128 v[180:183], v1 offset:9216\n" \
  "ds_read_b128 v[184:187], v4 offset:24576\n" \
  "v_mfma_f32_16x16x32_bf16 v[120:123], v[52:55], a[128:131], v[120:123]\n" \
  "buffer_store_dword v65, v22, s[36:39], s56 offen offset:256 sc1\n" \
  "v_mfma_f32_16x16x32_bf16 v[124:127], v[52:55], a[152:155], v[124:127]\n" \
  "ds_read_b128 v[188:191], v3 offset:27648\n" \
  "ds_read_b128 v[192:195], v3 offset:28672\n" \
  "v_mfma_f32_16x16x32_bf16 v[128:131], v[52:55], a[176:179], v[128:131]\n" \
  "v_cvt_pk_bf16_f32 v56, v142, v146\n" \
  "v_cvt_pk_bf16_f32 v57, v150, v154\n" \
  "v_mfma_f32_16x16x32_bf16 v[132:135], v[52:55], a[200:203], v[132:135]\n" \
  "buffer_load_dwordx4 v12, s[36:39], s53 offen offset:0 sc1 lds\n" \
  "v_mfma_f32_16x16x32_bf16 v[136:139], v[52:55], a[224:227], v[136:139]\n" \
  "v_cvt_pk_bf16_f32 v58, v158, v162\n" \
  "v_cvt_pk_bf16_f32 v59, v166, v170\n" \
  "v_mfma_f32_16x16x32_bf16 v[100:103], v[44:47], a[12:15], v[100:103]\n" \
  "s_waitcnt lgkmcnt(12)\n" \
  "v_mfma_f32_16x16x32_bf16 v[66:69], v[200:203], v[196:199], v[66:69]\n" \
  "s_waitcnt lgkmcnt(11)\n" \
  "v_mfma_f32_16x16x32_bf16 v[70:73], v[204:207], v[196:199], v[70:73]\n" \
  "s_waitcnt lgkmcnt(10)\n" \
  "v_mfma_f32_16x16x32_bf16 v[74:77], v[208:211], v[196:199], v[74:77]\n" \
  "ds_read_b64 v[196:197], v2 offset:2048\n" \
  "ds_read_b64 v[200:201], v5 offset:0\n" \
  "v_mfma_f32_16x16x32_bf16 v[104:107], v[44:47], a[36:39], v[104:107]\n" \
  "ds_read_b64 v[52:53], v0 offset:8192\n" \
  "ds_read_b64 v[54:55], v0 offset:11264\n" \
  "v_mfma_f32_16x16x32_bf16 v[108:111], v[44:47], a[60:63], v[108:111]\n" \
  "buffer_load_dwordx4 v12, s[36:39], s53 offen offset:1024 sc1 lds\n" \
  "v_mfma_f32_16x16x32_bf16 v[112:115], v[44:47], a[84:87], v[112:115]\n" \
  "v_cvt_pk_bf16_f32 v64, v174, v178\n" \
  "ds_read_b64 v[204:205], v3 offset:5120\n" \
  "v_mfma_f32_16x16x32_bf16 v[116:119], v[44:47], a[108:111], v[116:119]\n" \
  "s_waitcnt lgkmcnt(14)\n" \
  "ds_read_b64 v[208:209], v3 offset:5128\n" \
  "s_waitcnt lgkmcnt(14)\n" \
  "ds_read_b64 v[198:199], v2 offset:5120\n" \
  "v_mfma_f32_16x16x32_bf16 v[120:123], v[44:47], a[132:135], v[120:123]\n" \
  "buffer_store_dwordx4 v[56:59], v21, s[36:39], s56 offen offset:2048 sc1\n" \
  "v_mfma_f32_16x16x32_bf16 v[124:127], v[44:47], a[156:159], v[124:127]\n" \
  "s_waitcnt lgkmcnt(14)\n" \
  "ds_read_b64 v[202:203], v5 offset:8192\n" \
  "s_waitcnt lgkmcnt(14)\n" \
  "ds_read_b64 v[206:207], v3 offset:13312\n" \
  "v_mfma_f32_16x16x32_bf16 v[128:131], v[44:47], a[180:183], v[128:131]\n" \
  "s_waitcnt lgkmcnt(14)\n" \
  "ds_read_b64 v[210:211], v3 offset:13320\n" \
  "v_mfma_f32_16x16x32_bf16 v[66:69], v[216:219], v[212:215], v[66:69]\n" \
  "v_mfma_f32_16x16x32_bf16 v[70:73], v[220:223], v[212:215], v[70:73]\n" \
  "s_waitcnt lgkmcnt(14)\n" \
  "v_mfma_f32_16x16x32_bf16 v[74:77], v[224:227], v[212:215], v[74:77]\n" \
  "ds_read_b64 v[212:213], v2 offset:8192\n" \
  "s_waitcnt lgkmcnt(14)\n" \
  "ds_read_b64 v[216:217], v5 offset:16384\n" \
  "v_mfma_f32_16x16x32_bf16 v[132:135], v[44:47], a[204:207], v[132:135]\n" \
  "buffer_load_dwordx4 v13, s[36:39], s53 offen offset:2048 sc1 lds\n" \
  "v_mfma_f32_16x16x32_bf16 v[136:139], v[44:47], a[228:231], v[136:139]\n" \
  "s_waitcnt lgkmcnt(14)\n" \
  "ds_read_b64 v[220:221], v3 offset:21504\n" \
  "s_waitcnt lgkmcnt(14)\n" \
  "ds_read_b64 v[224:225], v3 offset:21512\n" \
  "v_mfma_f32_16x16x32_bf16 v[100:103], v[48:51], a[16:19], v[100:103]\n" \
  "s_waitcnt lgkmcnt(14)\n" \
  "ds_read_b64 v[214:215], v2 offset:11264\n" \
  "s_waitcnt lgkmcnt(14)\n" \
  "ds_read_b64 v[218:219], v5 offset:24576\n" \
  "v_mfma_f32_16x16x32_bf16 v[104:107], v[48:51], a[40:43], v[104:107]\n" \
  "s_waitcnt lgkmcnt(14)\n" \
  "ds_read_b128 v[44:47], v0 offset:1024\n" \
  "buffer_store_dword v64, v22, s[36:39], s56 offen offset:512 sc1\n" \
  "v_mfma_f32_16x16x32_bf16 v[108:111], v[48:51], a[64:67], v[108:111]\n" \
  "s_waitcnt lgkmcnt(14)\n" \
  "ds_read_b64 v[222:223], v3 offset:29696\n" \
  "s_waitcnt lgkmcnt(14)\n" \
  "ds_read_b64 v[226:227], v3 offset:29704\n" \
  "v_mfma_f32_16x16x32_bf16 v[112:115], v[48:51], a[88:91], v[112:115]\n" \
  "v_cvt_pk_bf16_f32 v60, v143, v147\n" \
  "v_cvt_pk_bf16_f32 v61, v151, v155\n" \
  "v_mfma_f32_16x16x32_bf16 v[116:119], v[48:51], a[112:115], v[116:119]\n" \
  "v_mfma_f32_16x16x32_bf16 v[66:69], v[184:187], v[180:183], v[66:69]\n" \
  "v_mfma_f32_16x16x32_bf16 v[70:73], v[188:191], v[180:183], v[70:73]\n" \
  "v_mfma_f32_16x16x32_bf16 v[74:77], v[192:195], v[180:183], v[74:77]\n" \
  "v_mfma_f32_16x16x32_bf16 v[120:123], v[48:51], a[136:139], v[120:123]\n" \
  "v_cvt_pk_bf16_f32 v62, v159, v163\n" \
  "v_cvt_pk_bf16_f32 v63, v167, v171\n" \
  "v_mfma_f32_16x16x32_bf16 v[124:127], v[48:51], a[160:163], v[124:127]\n" \
  "v_mfma_f32_16x16x32_bf16 v[128:131], v[48:51], a[184:187], v[128:131]\n" \
  "v_cvt_pk_bf16_f32 v65, v175, v179\n" \
  "buffer_store_dwordx4 v[60:63], v21, s[36:39], s56 offen offset:3072 sc1\n" \
  "v_mfma_f32_16x16x32_bf16 v[132:135], v[48:51], a[208:211], v[132:135]\n" \
  "s_waitcnt lgkmcnt(11)\n" \
  "v_mfma_f32_16x16x32_bf16 v[66:69], v[200:203], v[196:199], v[66:69]\n" \
  "s_waitcnt lgkmcnt(10)\n" \
  "v_mfma_f32_16x16x32_bf16 v[70:73], v[204:207], v[196:199], v[70:73]\n" \
  "s_waitcnt lgkmcnt(9)\n" \
  "v_mfma_f32_16x16x32_bf16 v[74:77], v[208:211], v[196:199], v[74:77]\n" \
  "v_mfma_f32_16x16x32_bf16 v[136:139], v[48:51], a[232:235], v[136:139]\n" \
  "v_mfma_f32_16x16x32_bf16 v[100:103], v[52:55], a[20:23], v[100:103]\n" \
  "s_waitcnt lgkmcnt(3)\n" \
  "v_mfma_f32_16x16x32_bf16 v[66:69], v[216:219], v[212:215], v[66:69]\n" \
  "s_waitcnt lgkmcnt(1)\n" \
  "v_mfma_f32_16x16x32_bf16 v[70:73], v[220:223], v[212:215], v[70:73]\n" \
  "s_waitcnt lgkmcnt(0)\n" \
  "v_mfma_f32_16x16x32_bf16 v[74:77], v[224:227], v[212:215], v[74:77]\n" \
  "s_waitcnt vmcnt(13)\n" \
  "v_min_u32_dpp v25, v25, v25 quad_perm:[1,0,3,2] row_mask:0xf bank_mask:0xf\n" \
  "v_mfma_f32_16x16x32_bf16 v[104:107], v[52:55], a[44:47], v[104:107]\n" \
  "ds_read_b128 v[48:51], v0 offset:4096\n" \
  "buffer_store_dword v65, v22, s[36:39], s56 offen offset:768 sc1\n" \
  "v_mfma_f32_16x16x32_bf16 v[108:111], v[52:55], a[68:71], v[108:111]\n" \
  "v_mfma_f32_16x16x32_bf16 v[112:115], v[52:55], a[92:95], v[112:115]\n" \
  "v_min_u32_dpp v25, v25, v25 quad_perm:[2,3,0,1] row_mask:0xf bank_mask:0xf\n" \
  "ds_write_b32 v19, v25\n" \
  "v_mfma_f32_16x16x32_bf16 v[116:119], v[52:55], a[116:119], v[116:119]\n" \
  "s_waitcnt lgkmcnt(0)\n" \
  "s_barrier\n" \
  "ds_read_b32 v25, v20\n" \
  "v_mfma_f32_16x16x32_bf16 v[120:123], v[52:55], a[140:143], v[120:123]\n" \
  "s_add_u32 m0, s45, 3072\n" \
  "s_add_u32 s60, s52, 3072\n" \
  "buffer_load_dwordx4 v15, s[36:39], s60 offen offset:0 sc1 lds\n" \
  "v_mfma_f32_16x16x32_bf16 v[124:127], v[52:55], a[164:167], v[124:127]\n" \
  "v_mov_b32 v84, 0x3d93cd3a\n" \
  "v_cvt_pk_bf16_f32 v78, v66, v67\n" \
  "v_mfma_f32_16x16x32_bf16 v[128:131], v[52:55], a[188:191], v[128:131]\n" \
  "v_cvt_pk_bf16_f32 v79, v68, v69\n" \
  "v_cvt_pk_bf16_f32 v80, v70, v71\n" \
  "v_mfma_f32_16x16x32_bf16 v[132:135], v[52:55], a[212:215], v[132:135]\n" \
  "buffer_load_dwordx4 v15, s[36:39], s60 offen offset:1024 sc1 lds\n" \
  "v_mfma_f32_16x16x32_bf16 v[136:139], v[52:55], a[236:239], v[136:139]\n" \
  "ds_read_b64_tr_b16 v[180:181], v6 offset:0\n" \
  "v_cvt_pk_bf16_f32 v81, v72, v73\n" \
  "v_mfma_f32_16x16x32_bf16 v[140:143], v[44:47], a[0:3], 0\n" \
  "v_cvt_pk_bf16_f32 v82, v74, v75\n" \
  "v_cvt_pk_bf16_f32 v83, v76, v77\n" \
  "v_mfma_f32_16x16x32_bf16 v[144:147], v[44:47], a[24:27], 0\n" \
  "ds_read_b128 v[52:55], v0 offset:7168\n" \
  "buffer_load_dwordx4 v15, s[36:39], s60 offen offset:2048 sc1 lds\n" \
  "v_mfma_f32_16x16x32_bf16 v[148:151], v[44:47], a[48:51], 0\n" \
  "s_add_u32 s61, s42, 4\n" \
  "s_waitcnt lgkmcnt(2)\n" \
  "v_readfirstlane_b32 s62, v25\n" \
  "s_min_u32 s62, s62, 56\n" \
  "v_mfma_f32_16x16x32_bf16 v[152:155], v[44:47], a[72:75], 0\n" \
  "s_cmp_ge_u32 s62, s61\n" \
  "s_cselect_b32 s62, 0, 0x40000000\n" \
  "v_or_b32 v23, s62, v16\n" \
  "v_mfma_f32_16x16x32_bf16 v[156:159], v[44:47], a[96:99], 0\n" \
  "v_cvt_pk_bf16_f32 v56, v100, v104\n" \
  "v_cvt_pk_bf16_f32 v57, v108, v112\n" \
  "v_mfma_f32_16x16x32_bf16 v[160:163], v[44:47], a[120:123], 0\n" \
  "v_cvt_pk_bf16_f32 v58, v116, v120\n" \
  "v_cvt_pk_bf16_f32 v59, v124, v128\n" \
  "v_mfma_f32_16x16x32_bf16 v[164:167], v[44:47], a[144:147], 0\n" \
  "buffer_load_dwordx4 v23, s[36:39], s54 offen offset:3072 sc1 lds\n" \
  "v_mfma_f32_16x16x32_bf16 v[168:171], v[44:47], a[168:171], 0\n" \
  "v_cvt_pk_bf16_f32 v64, v132, v136\n" \
  "s_add_u32 m0, s45, 35840\n" \
  "s_add_u32 s60, s53, 3072\n" \
  "v_mfma_f32_16x16x32_bf16 v[172:175], v[44:47], a[192:195], 0\n" \
  "v_lshlrev_b32 v66, 16, v78\n" \
  "ds_read_b64_tr_b16 v[182:183], v6 offset:1024\n" \
  "v_mfma_f32_16x16x32_bf16 v[176:179], v[44:47], a[216:219], 0\n" \
  "buffer_store_dwordx4 v[56:59], v10, s[36:39], s46 offen offset:0 sc1\n" \
  "v_mfma_f32_16x16x32_bf16 v[140:143], v[48:51], a[4:7], v[140:143]\n" \
  "v_and_b32 v67, 0xffff0000, v78\n" \
  "v_mul_f32 v66, v66, v84\n" \
  "v_mfma_f32_16x16x32_bf16 v[144:147], v[48:51], a[28:31], v[144:147]\n" \
  "ds_read_b128 v[44:47], v0 offset:10240\n" \
  "v_mul_f32 v67, v67, v84\n" \
  "v_mfma_f32_16x16x32_bf16 v[148:151], v[48:51], a[52:55], v[148:151]\n" \
  "buffer_load_dwordx4 v15, s[36:39], s60 offen offset:0 sc1 lds\n" \
  "v_mfma_f32_16x16x32_bf16 v[152:155], v[48:51], a[76:79], v[152:155]\n" \
  "v_cvt_pk_bf16_f32 v78, v66, v67\n" \
  "ds_read_b64_tr_b16 v[184:185], v6 offset:8\n" \
  "v_mfma_f32_16x16x32_bf16 v[156:159], v[48:51], a[100:103], v[156:159]\n" \
  "v_lshlrev_b32 v66, 16, v78\n" \
  "v_and_b32 v67, 0xffff0000, v78\n" \
  "v_mfma_f32_16x16x32_bf16 v[160:163], v[48:51], a[124:127], v[160:163]\n" \
  "buffer_store_dword v64, v11, s[36:39], s46 offen offset:0 sc1\n" \
  "v_mfma_f32_16x16x32_bf16 v[164:167], v[48:51], a[148:151], v[164:167]\n" \
  "v_cvt_pk_bf16_f32 v60, v101, v105\n" \
  "v_lshlrev_b32 v68, 16, v79\n" \
  "v_mfma_f32_16x16x32_bf16 v[168:171], v[48:51], a[172:175], v[168:171]\n" \
  "v_cvt_pk_bf16_f32 v61, v109, v113\n" \
  "v_cvt_pk_bf16_f32 v62, v117, v121\n" \
  "v_mfma_f32_16x16x32_bf16 v[172:175], v[48:51], a[196:199], v[172:175]\n" \
  "buffer_load_dwordx4 v15, s[36:39], s60 offen offset:1024 sc1 lds\n" \
  "v_mfma_f32_16x16x32_bf16 v[176:179], v[48:51], a[220:223], v[176:179]\n" \
  "v_cvt_pk_bf16_f32 v63, v125, v129\n" \
  "v_cvt_pk_bf16_f32 v65, v133, v137\n" \
  "s_waitcnt lgkmcnt(3)\n" \
  "v_mfma_f32_16x16x32_bf16 v[140:143], v[52:55], a[8:11], v[140:143]\n" \
  "v_and_b32 v69, 0xffff0000, v79\n" \
  "ds_read_b64_tr_b16 v[186:187], v6 offset:1032\n" \
  "v_mfma_f32_16x16x32_bf16 v[144:147], v[52:55], a[32:35], v[144:147]\n" \
  "ds_read_b64 v[48:49], v0 offset:2056\n" \
  "ds_read_b64 v[50:51], v0 offset:5128\n" \
  "v_mfma_f32_16x16x32_bf16 v[148:151], v[52:55], a[56:59], v[148:151]\n" \
  "buffer_load_dwordx4 v15, s[36:39], s60 offen offset:2048 sc1 lds\n" \
  "v_mfma_f32_16x16x32_bf16 v[152:155], v[52:55], a[80:83], v[152:155]\n" \
  "v_mul_f32 v68, v68, v84\n" \
  "v_mul_f32 v69, v69, v84\n" \
  "v_mfma_f32_16x16x32_bf16 v[156:159], v[52:55], a[104:107], v[156:159]\n" \
  "v_cvt_pk_bf16_f32 v79, v68, v69\n" \
  "v_lshlrev_b32 v68, 16, v79\n" \
  "v_mfma_f32_16x16x32_bf16 v[160:163], v[52:55], a[128:131], v[160:163]\n" \
  "buffer_store_dwordx4 v[60:63], v10, s[36:39], s46 offen offset:1024 sc1\n" \
  "v_mfma_f32_16x16x32_bf16 v[164:167], v[52:55], a[152:155], v[164:167]\n" \
  "ds_read_b64_tr_b16 v[188:189], v6 offset:2048\n" \
  "v_and_b32 v69, 0xffff0000, v79\n" \
  "v_mfma_f32_16x16x32_bf16 v[168:171], v[52:55], a[176:179], v[168:171]\n" \
  "v_lshlrev_b32 v70, 16, v80\n" \
  "v_and_b32 v71, 0xffff0000, v80\n" \
  "v_mfma_f32_16x16x32_bf16 v[172:175], v[52:55], a[200:203], v[172:175]\n" \
  "buffer_store_dword v65, v11, s[36:39], s46 offen offset:256 sc1\n" \
  "v_mfma_f32_16x16x32_bf16 v[176:179], v[52:55], a[224:227], v[176:179]\n" \
  "v_cvt_pk_bf16_f32 v56, v102, v106\n" \
  "v_cvt_pk_bf16_f32 v57, v110, v114\n" \
  "s_waitcnt lgkmcnt(5)\n" \
  "v_mfma_f32_16x16x32_bf16 v[140:143], v[44:47], a[12:15], v[140:143]\n" \
  "v_mul_f32 v70, v70, v84\n" \
  "ds_read_b64_tr_b16 v[190:191], v6 offset:2056\n" \
  "v_mfma_f32_16x16x32_bf16 v[144:147], v[44:47], a[36:39], v[144:147]\n" \
  "ds_read_b64 v[52:53], v0 offset:8200\n" \
  "ds_read_b64 v[54:55], v0 offset:11272\n" \
  "v_mfma_f32_16x16x32_bf16 v[148:151], v[44:47], a[60:63], v[148:151]\n" \
  "buffer_load_dwordx4 v23, s[36:39], s55 offen offset:3072 sc1 lds\n" \
  "v_mfma_f32_16x16x32_bf16 v[152:155], v[44:47], a[84:87], v[152:155]\n" \
  "v_cvt_pk_bf16_f32 v58, v118, v122\n" \
  "v_cvt_pk_bf16_f32 v59, v126, v130\n" \
  "v_mfma_f32_16x16x32_bf16 v[156:159], v[44:47], a[108:111], v[156:159]\n" \
  "v_mul_f32 v71, v71, v84\n" \
  "v_cvt_pk_bf16_f32 v80, v70, v71\n" \
  "v_mfma_f32_16x16x32_bf16 v[160:163], v[44:47], a[132:135], v[160:163]\n" \
  "v_cvt_pk_bf16_f32 v64, v134, v138\n" \
  "buffer_store_dwordx4 v[56:59], v10, s[36:39], s46 offen offset:2048 sc1\n" \
  "v_mfma_f32_16x16x32_bf16 v[164:167], v[44:47], a[156:159], v[164:167]\n" \
  "v_lshlrev_b32 v70, 16, v80\n" \
  "v_and_b32 v71, 0xffff0000, v80\n" \
  "v_mfma_f32_16x16x32_bf16 v[168:171], v[44:47], a[180:183], v[168:171]\n" \
  "ds_read_b64_tr_b16 v[192:193], v6 offset:8192\n" \
  "v_lshlrev_b32 v72, 16, v81\n" \
  "v_mfma_f32_16x16x32_bf16 v[172:175], v[44:47], a[204:207], v[172:175]\n" \
  "v_and_b32 v73, 0xffff0000, v81\n" \
  "v_mul_f32 v72, v72, v84\n" \
  "v_mfma_f32_16x16x32_bf16 v[176:179], v[44:47], a[228:231], v[176:179]\n" \
  "buffer_store_dword v64, v11, s[36:39], s46 offen offset:512 sc1\n" \
  "s_waitcnt lgkmcnt(5)\n" \
  "v_mfma_f32_16x16x32_bf16 v[140:143], v[48:51], a[16:19], v[140:143]\n" \
  "v_mul_f32 v73, v73, v84\n" \
  "ds_read_b64_tr_b16 v[194:195], v6 offset:9216\n" \
  "v_mfma_f32_16x16x32_bf16 v[144:147], v[48:51], a[40:43], v[144:147]\n" \
  "ds_read_b128 v[44:47], v0 offset:12288\n" \
  "v_cvt_pk_bf16_f32 v60, v103, v107\n" \
  "v_mfma_f32_16x16x32_bf16 v[148:151], v[48:51], a[64:67], v[148:151]\n" \
  "v_cvt_pk_bf16_f32 v81, v72, v73\n" \
  "v_lshlrev_b32 v72, 16, v81\n" \
  "v_mfma_f32_16x16x32_bf16 v[152:155], v[48:51], a[88:91], v[152:155]\n" \
  "v_cvt_pk_bf16_f32 v61, v111, v115\n" \
  "v_cvt_pk_bf16_f32 v62, v119, v123\n" \
  "v_mfma_f32_16x16x32_bf16 v[156:159], v[48:51], a[112:115], v[156:159]\n" \
  "v_and_b32 v73, 0xffff0000, v81\n" \
  "v_lshlrev_b32 v74, 16, v82\n" \
  "v_mfma_f32_16x16x32_bf16 v[160:163], v[48:51], a[136:139], v[160:163]\n" \
  "v_cvt_pk_bf16_f32 v63, v127, v131\n" \
  "v_cvt_pk_bf16_f32 v65, v135, v139\n" \
  "v_mfma_f32_16x16x32_bf16 v[164:167], v[48:51], a[160:163], v[164:167]\n" \
  "ds_read_b64_tr_b16 v[196:197], v6 offset:8200\n" \
  "v_and_b32 v75, 0xffff0000, v82\n" \
  "v_mfma_f32_16x16x32_bf16 v[168:171], v[48:51], a[184:187], v[168:171]\n" \
  "buffer_store_dwordx4 v[60:63], v10, s[36:39], s46 offen offset:3072 sc1\n" \
  "v_mfma_f32_16x16x32_bf16 v[172:175], v[48:51], a[208:211], v[172:175]\n" \
  "v_mul_f32 v74, v74, v84\n" \
  "v_mul_f32 v75, v75, v84\n" \
  "v_mfma_f32_16x16x32_bf16 v[176:179], v[48:51], a[232:235], v[176:179]\n" \
  "v_cvt_pk_bf16_f32 v82, v74, v75\n" \
  "ds_read_b64_tr_b16 v[198:199], v6 offset:9224\n" \
  "s_waitcnt lgkmcnt(5)\n" \
  "v_mfma_f32_16x16x32_bf16 v[140:143], v[52:55], a[20:23], v[140:143]\n" \
  "v_lshlrev_b32 v74, 16, v82\n" \
  "v_and_b32 v75, 0xffff0000, v82\n" \
  "v_mfma_f32_16x16x32_bf16 v[144:147], v[52:55], a[44:47], v[144:147]\n" \
  "ds_read_b128 v[48:51], v0 offset:15360\n" \
  "buffer_store_dword v65, v11, s[36:39], s46 offen offset:768 sc1\n" \
  "v_mfma_f32_16x16x32_bf16 v[148:151], v[52:55], a[68:71], v[148:151]\n" \
  "v_lshlrev_b32 v76, 16, v83\n" \
  "v_and_b32 v77, 0xffff0000, v83\n" \
  "v_mfma_f32_16x16x32_bf16 v[152:155], v[52:55], a[92:95], v[152:155]\n" \
  "ds_read_b64_tr_b16 v[200:201], v6 offset:10240\n" \
  "v_mul_f32 v76, v76, v84\n" \
  "v_mfma_f32_16x16x32_bf16 v[156:159], v[52:55], a[116:119], v[156:159]\n" \
  "v_mul_f32 v77, v77, v84\n" \
  "v_cvt_pk_bf16_f32 v83, v76, v77\n" \
  "v_mfma_f32_16x16x32_bf16 v[160:163], v[52:55], a[140:143], v[160:163]\n" \
  "v_lshlrev_b32 v76, 16, v83\n" \
  "ds_read_b64_tr_b16 v[202:203], v6 offset:10248\n" \
  "v_mfma_f32_16x16x32_bf16 v[164:167], v[52:55], a[164:167], v[164:167]\n" \
  "v_and_b32 v77, 0xffff0000, v83\n" \
  "v_max3_f32 v78, v66, v67, v68\n" \
  "v_mfma_f32_16x16x32_bf16 v[168:171], v[52:55], a[188:191], v[168:171]\n" \
  "v_max3_f32 v78, v78, v69, v70\n" \
  "v_max3_f32 v78, v78, v71, v72\n" \
  "v_mfma_f32_16x16x32_bf16 v[172:175], v[52:55], a[212:215], v[172:175]\n" \
  "ds_read_b64 v[204:205], v7 offset:0\n" \
  "v_max3_f32 v78, v78, v73, v74\n" \
  "v_mfma_f32_16x16x32_bf16 v[176:179], v[52:55], a[236:239], v[176:179]\n" \
  "v_max3_f32 v78, v78, v75, v76\n" \
  "v_max_f32 v78, v78, v77\n" \
  "s_waitcnt lgkmcnt(6)\n" \
  "v_mfma_f32_16x16x32_bf16 v[100:103], v[44:47], a[0:3], 0\n" \
  "v_mov_b32 v79, v78\n" \
  "ds_read_b64 v[208:209], v8 offset:0\n" \
  "v_mfma_f32_16x16x32_bf16 v[104:107], v[44:47], a[24:27], 0\n" \
  "ds_read_b128 v[52:55], v0 offset:18432\n" \
  "v_mfma_f32_16x16x32_bf16 v[108:111], v[44:47], a[48:51], 0\n" \
  "v_permlane16_swap_b32 v78, v79\n" \
  "v_mfma_f32_16x16x32_bf16 v[112:115], v[44:47], a[72:75], 0\n" \
  "v_max_f32 v78, v78, v79\n" \
  "v_mov_b32 v79, v78\n" \
  "v_mfma_f32_16x16x32_bf16 v[116:119], v[44:47], a[96:99], 0\n" \
  "v_cvt_pk_bf16_f32 v56, v140, v144\n" \
  "v_cvt_pk_bf16_f32 v57, v148, v152\n" \
  "v_mfma_f32_16x16x32_bf16 v[120:123], v[44:47], a[120:123], 0\n" \
  "v_cvt_pk_bf16_f32 v58, v156, v160\n" \
  "v_cvt_pk_bf16_f32 v59, v164, v168\n" \
  "v_mfma_f32_16x16x32_bf16 v[124:127], v[44:47], a[144:147], 0\n" \
  "ds_read_b64 v[212:213], v9 offset:0\n" \
  "v_mfma_f32_16x16x32_bf16 v[128:131], v[44:47], a[168:171], 0\n" \
  "v_cvt_pk_bf16_f32 v64, v172, v176\n" \
  "buffer_store_dwordx4 v[56:59], v21, s[36:39], s46 offen offset:0 sc1\n" \
  "v_mfma_f32_16x16x32_bf16 v[132:135], v[44:47], a[192:195], 0\n" \
  "v_permlane32_swap_b32 v78, v79\n" \
  "v_mfma_f32_16x16x32_bf16 v[136:139], v[44:47], a[216:219], 0\n" \
  "v_max_f32 v78, v78, v79\n" \
  "v_mul_f32 v78, 0x3fb8aa3b, v78\n" \
  "s_waitcnt lgkmcnt(6)\n" \
  "v_mfma_f32_16x16x32_bf16 v[100:103], v[48:51], a[4:7], v[100:103]\n" \
  "ds_read_b64 v[206:207], v7 offset:8192\n" \
  "v_mov_b32 v80, 0x3fb8aa3b\n" \
  "v_mfma_f32_16x16x32_bf16 v[104:107], v[48:51], a[28:31], v[104:107]\n" \
  "ds_read_b128 v[44:47], v0 offset:21504\n" \
  "buffer_store_dword v64, v22, s[36:39], s46 offen offset:0 sc1\n" \
  "v_mfma_f32_16x16x32_bf16 v[108:111], v[48:51], a[52:55], v[108:111]\n" \
  "v_fma_f32 v66, v66, v80, -v78\n" \
  "v_fma_f32 v67, v67, v80, -v78\n" \
  "v_mfma_f32_16x16x32_bf16 v[112:115], v[48:51], a[76:79], v[112:115]\n" \
  "v_cvt_pk_bf16_f32 v60, v141, v145\n" \
  "v_cvt_pk_bf16_f32 v61, v149, v153\n" \
  "v_mfma_f32_16x16x32_bf16 v[116:119], v[48:51], a[100:103], v[116:119]\n" \
  "v_fma_f32 v68, v68, v80, -v78\n" \
  "ds_read_b64 v[210:211], v8 offset:8192\n" \
  "v_mfma_f32_16x16x32_bf16 v[120:123], v[48:51], a[124:127], v[120:123]\n" \
  "v_cvt_pk_bf16_f32 v62, v157, v161\n" \
  "v_cvt_pk_bf16_f32 v63, v165, v169\n" \
  "v_mfma_f32_16x16x32_bf16 v[124:127], v[48:51], a[148:151], v[124:127]\n" \
  "v_fma_f32 v69, v69, v80, -v78\n" \
  "v_fma_f32 v70, v70, v80, -v78\n" \
  "v_mfma_f32_16x16x32_bf16 v[128:131], v[48:51], a[172:175], v[128:131]\n" \
  "v_cvt_pk_bf16_f32 v65, v173, v177\n" \
  "buffer_store_dwordx4 v[60:63], v21, s[36:39], s46 offen offset:1024 sc1\n" \
  "v_mfma_f32_16x16x32_bf16 v[132:135], v[48:51], a[196:199], v[132:135]\n" \
  "v_fma_f32 v71, v71, v80, -v78\n" \
  "v_fma_f32 v72, v72, v80, -v78\n" \
  "v_mfma_f32_16x16x32_bf16 v[136:139], v[48:51], a[220:223], v[136:139]\n" \
  "ds_read_b64 v[214:215], v9 offset:8192\n" \
  "v_fma_f32 v73, v73, v80, -v78\n" \
  "s_waitcnt lgkmcnt(5)\n" \
  "v_mfma_f32_16x16x32_bf16 v[100:103], v[52:55], a[8:11], v[100:103]\n" \
  "v_fma_f32 v74, v74, v80, -v78\n" \
  "v_fma_f32 v75, v75, v80, -v78\n" \
  "v_mfma_f32_16x16x32_bf16 v[104:107], v[52:55], a[32:35], v[104:107]\n" \
  "ds_read_b64 v[48:49], v0 offset:14336\n" \
  "ds_read_b64 v[50:51], v0 offset:17408\n" \
  "v_mfma_f32_16x16x32_bf16 v[108:111], v[52:55], a[56:59], v[108:111]\n" \
  "v_fma_f32 v76, v76, v80, -v78\n" \
  "ds_read_b64 v[216:217], v7 offset:16384\n" \
  "v_mfma_f32_16x16x32_bf16 v[112:115], v[52:55], a[80:83], v[112:115]\n" \
  "buffer_store_dword v65, v22, s[36:39], s46 offen offset:256 sc1\n" \
  "v_mfma_f32_16x16x32_bf16 v[116:119], v[52:55], a[104:107], v[116:119]\n" \
  "v_fma_f32 v77, v77, v80, -v78\n" \
  "v_exp_f32 v66, v66\n" \
  "v_mfma_f32_16x16x32_bf16 v[120:123], v[52:55], a[128:131], v[120:123]\n" \
  "v_cvt_pk_bf16_f32 v56, v142, v146\n" \
  "v_cvt_pk_bf16_f32 v57, v150, v154\n" \
  "v_mfma_f32_16x16x32_bf16 v[124:127], v[52:55], a[152:155], v[124:127]\n" \
  "v_exp_f32 v67, v67\n" \
  "v_mfma_f32_16x16x32_bf16 v[128:131], v[52:55], a[176:179], v[128:131]\n" \
  "v_cvt_pk_bf16_f32 v58, v158, v162\n" \
  "v_cvt_pk_bf16_f32 v59, v166, v170\n" \
  "v_mfma_f32_16x16x32_bf16 v[132:135], v[52:55], a[200:203], v[132:135]\n" \
  "v_exp_f32 v68, v68\n" \
  "v_mfma_f32_16x16x32_bf16 v[136:139], v[52:55], a[224:227], v[136:139]\n" \
  "v_cvt_pk_bf16_f32 v64, v174, v178\n" \
  "buffer_store_dwordx4 v[56:59], v21, s[36:39], s46 offen offset:2048 sc1\n" \
  "s_waitcnt lgkmcnt(5)\n" \
  "v_mfma_f32_16x16x32_bf16 v[100:103], v[44:47], a[12:15], v[100:103]\n" \
  "ds_read_b64 v[220:221], v8 offset:16384\n" \
  "v_exp_f32 v69, v69\n" \
  "v_mfma_f32_16x16x32_bf16 v[104:107], v[44:47], a[36:39], v[104:107]\n" \
  "ds_read_b64 v[52:53], v0 offset:20480\n" \
  "ds_read_b64 v[54:55], v0 offset:23552\n" \
  "v_mfma_f32_16x16x32_bf16 v[108:111], v[44:47], a[60:63], v[108:111]\n" \
  "v_exp_f32 v70, v70\n" \
  "v_mfma_f32_16x16x32_bf16 v[112:115], v[44:47], a[84:87], v[112:115]\n" \
  "buffer_store_dword v64, v22, s[36:39], s46 offen offset:512 sc1\n" \
  "v_mfma_f32_16x16x32_bf16 v[116:119], v[44:47], a[108:111], v[116:119]\n" \
  "v_exp_f32 v71, v71\n" \
  "v_mfma_f32_16x16x32_bf16 v[120:123], v[44:47], a[132:135], v[120:123]\n" \
  "v_cvt_pk_bf16_f32 v60, v143, v147\n" \
  "v_cvt_pk_bf16_f32 v61, v151, v155\n" \
  "v_mfma_f32_16x16x32_bf16 v[124:127], v[44:47], a[156:159], v[124:127]\n" \
  "v_exp_f32 v72, v72\n" \
  "v_mfma_f32_16x16x32_bf16 v[128:131], v[44:47], a[180:183], v[128:131]\n" \
  "v_cvt_pk_bf16_f32 v62, v159, v163\n" \
  "v_cvt_pk_bf16_f32 v63, v167, v171\n" \
  "v_mfma_f32_16x16x32_bf16 v[132:135], v[44:47], a[204:207], v[132:135]\n" \
  "ds_read_b64 v[224:225], v9 offset:16384\n" \
  "v_exp_f32 v73, v73\n" \
  "v_mfma_f32_16x16x32_bf16 v[136:139], v[44:47], a[228:231], v[136:139]\n" \
  "v_cvt_pk_bf16_f32 v65, v175, v179\n" \
  "buffer_store_dwordx4 v[60:63], v21, s[36:39], s46 offen offset:3072 sc1\n" \
  "s_waitcnt lgkmcnt(5)\n" \
  "v_mfma_f32_16x16x32_bf16 v[100:103], v[48:51], a[16:19], v[100:103]\n" \
  "v_exp_f32 v74, v74\n" \
  "v_mfma_f32_16x16x32_bf16 v[104:107], v[48:51], a[40:43], v[104:107]\n" \
  "ds_read_b128 v[44:47], v0 offset:13312\n" \
  "v_exp_f32 v75, v75\n" \
  "v_mfma_f32_16x16x32_bf16 v[108:111], v[48:51], a[64:67], v[108:111]\n" \
  "v_exp_f32 v76, v76\n" \
  "v_mfma_f32_16x16x32_bf16 v[112:115], v[48:51], a[88:91], v[112:115]\n" \
  "buffer_store_dword v65, v22, s[36:39], s46 offen offset:768 sc1\n" \
  "v_mfma_f32_16x16x32_bf16 v[116:119], v[48:51], a[112:115], v[116:119]\n" \
  "ds_read_b64 v[218:219], v7 offset:24576\n" \
  "v_exp_f32 v77, v77\n" \
  "v_mfma_f32_16x16x32_bf16 v[120:123], v[48:51], a[136:139], v[120:123]\n" \
  "v_add_f32 v78, v66, v67\n" \
  "v_add_f32 v78, v78, v68\n" \
  "v_mfma_f32_16x16x32_bf16 v[124:127], v[48:51], a[160:163], v[124:127]\n" \
  "v_add_f32 v78, v78, v69\n" \
  "ds_read_b64 v[222:223], v8 offset:24576\n" \
  "v_mfma_f32_16x16x32_bf16 v[128:131], v[48:51], a[184:187], v[128:131]\n" \
  "v_add_f32 v78, v78, v70\n" \
  "v_add_f32 v78, v78, v71\n" \
  "v_mfma_f32_16x16x32_bf16 v[132:135], v[48:51], a[208:211], v[132:135]\n" \
  "v_add_f32 v78, v78, v72\n" \
  "v_add_f32 v78, v78, v73\n" \
  "v_mfma_f32_16x16x32_bf16 v[136:139], v[48:51], a[232:235], v[136:139]\n" \
  "ds_read_b64 v[226:227], v9 offset:24576\n" \
  "v_add_f32 v78, v78, v74\n" \
  "s_waitcnt lgkmcnt(5)\n" \
  "v_mfma_f32_16x16x32_bf16 v[100:103], v[52:55], a[20:23], v[100:103]\n" \
  "v_add_f32 v78, v78, v75\n" \
  "v_add_f32 v78, v78, v76\n" \
  "v_mfma_f32_16x16x32_bf16 v[104:107], v[52:55], a[44:47], v[104:107]\n" \
  "ds_read_b128 v[48:51], v0 offset:16384\n" \
  "v_add_f32 v78, v78, v77\n" \
  "v_mfma_f32_16x16x32_bf16 v[108:111], v[52:55], a[68:71], v[108:111]\n" \
  "v_mov_b32 v79, v78\n" \
  "v_mfma_f32_16x16x32_bf16 v[112:115], v[52:55], a[92:95], v[112:115]\n" \
  "s_nop 0\n" \
  "v_permlane16_swap_b32 v78, v79\n" \
  "v_mfma_f32_16x16x32_bf16 v[116:119], v[52:55], a[116:119], v[116:119]\n" \
  "v_add_f32 v78, v78, v79\n" \
  "v_mov_b32 v79, v78\n" \
  "v_mfma_f32_16x16x32_bf16 v[120:123], v[52:55], a[140:143], v[120:123]\n" \
  "v_mfma_f32_16x16x32_bf16 v[124:127], v[52:55], a[164:167], v[124:127]\n" \
  "v_permlane32_swap_b32 v78, v79\n" \
  "v_mfma_f32_16x16x32_bf16 v[128:131], v[52:55], a[188:191], v[128:131]\n" \
  "v_add_f32 v78, v78, v79\n" \
  "v_rcp_f32 v78, v78\n" \
  "v_mfma_f32_16x16x32_bf16 v[132:135], v[52:55], a[212:215], v[132:135]\n" \
  "v_mul_f32 v70, v70, v78\n" \
  "v_mul_f32 v71, v71, v78\n" \
  "v_mfma_f32_16x16x32_bf16 v[136:139], v[52:55], a[236:239], v[136:139]\n" \
  "v_mul_f32 v72, v72, v78\n" \
  "v_mul_f32 v73, v73, v78\n" \
  "s_waitcnt lgkmcnt(4)\n" \
  "v_mfma_f32_16x16x32_bf16 v[140:143], v[44:47], a[0:3], 0\n" \
  "v_mul_f32 v74, v74, v78\n" \
  "v_mul_f32 v75, v75, v78\n" \
  "v_mfma_f32_16x16x32_bf16 v[144:147], v[44:47], a[24:27], 0\n" \
  "ds_read_b128 v[52:55], v0 offset:19456\n" \
  "v_mul_f32 v76, v76, v78\n" \
  "v_mfma_f32_16x16x32_bf16 v[148:151], v[44:47], a[48:51], 0\n" \
  "v_mul_f32 v77, v77, v78\n" \
  "v_mul_f32 v66, v66, v78\n" \
  "v_mfma_f32_16x16x32_bf16 v[152:155], v[44:47], a[72:75], 0\n" \
  "v_mul_f32 v67, v67, v78\n" \
  "v_mul_f32 v68, v68, v78\n" \
  "v_mfma_f32_16x16x32_bf16 v[156:159], v[44:47], a[96:99], 0\n" \
  "v_cvt_pk_bf16_f32 v56, v100, v104\n" \
  "v_cvt_pk_bf16_f32 v57, v108, v112\n" \
  "v_mfma_f32_16x16x32_bf16 v[160:163], v[44:47], a[120:123], 0\n" \
  "v_cvt_pk_bf16_f32 v58, v116, v120\n" \
  "v_cvt_pk_bf16_f32 v59, v124, v128\n" \
  "v_mfma_f32_16x16x32_bf16 v[164:167], v[44:47], a[144:147], 0\n" \
  "v_mul_f32 v69, v69, v78\n" \
  "v_cvt_pk_bf16_f32 v86, v70, v71\n" \
  "v_mfma_f32_16x16x32_bf16 v[168:171], v[44:47], a[168:171], 0\n" \
  "v_cvt_pk_bf16_f32 v64, v132, v136\n" \
  "buffer_store_dwordx4 v[56:59], v10, s[36:39], s47 offen offset:0 sc1\n" \
  "v_mfma_f32_16x16x32_bf16 v[172:175], v[44:47], a[192:195], 0\n" \
  "v_cvt_pk_bf16_f32 v87, v72, v73\n" \
  "v_cvt_pk_bf16_f32 v88, v74, v75\n" \
  "v_mfma_f32_16x16x32_bf16 v[176:179], v[44:47], a[216:219], 0\n" \
  "v_cvt_pk_bf16_f32 v89, v76, v77\n" \
  "v_cvt_pk_bf16_f32 v92, v66, v67\n" \
  "s_waitcnt lgkmcnt(1)\n" \
  "v_mfma_f32_16x16x32_bf16 v[140:143], v[48:51], a[4:7], v[140:143]\n" \
  "v_cvt_pk_bf16_f32 v93, v68, v69\n" \
  "v_mfma_f32_16x16x32_bf16 v[228:231], v[180:183], v[86:89], 0\n" \
  "v_mfma_f32_16x16x32_bf16 v[232:235], v[184:187], v[86:89], 0\n" \
  "v_mfma_f32_16x16x32_bf16 v[236:239], v[188:191], v[86:89], 0\n" \
  "v_mfma_f32_16x16x32_bf16 v[228:231], v[204:207], v[92:95], v[228:231]\n" \
  "v_mfma_f32_16x16x32_bf16 v[232:235], v[208:211], v[92:95], v[232:235]\n" \
  "v_mfma_f32_16x16x32_bf16 v[236:239], v[212:215], v[92:95], v[236:239]\n" \
  "ds_read_b64_tr_b16 v[180:181], v6 offset:16384\n" \
  "ds_read_b64_tr_b16 v[182:183], v6 offset:17408\n" \
  "v_mfma_f32_16x16x32_bf16 v[144:147], v[48:51], a[28:31], v[144:147]\n" \
  "ds_read_b128 v[44:47], v0 offset:22528\n" \
  "buffer_store_dword v64, v11, s[36:39], s47 offen offset:0 sc1\n" \
  "v_mfma_f32_16x16x32_bf16 v[148:151], v[48:51], a[52:55], v[148:151]\n" \
  "ds_read_b64_tr_b16 v[184:185], v6 offset:16392\n" \
  "ds_read_b64_tr_b16 v[186:187], v6 offset:17416\n" \
  "v_mfma_f32_16x16x32_bf16 v[152:155], v[48:51], a[76:79], v[152:155]\n" \
  "v_cvt_pk_bf16_f32 v60, v101, v105\n" \
  "v_cvt_pk_bf16_f32 v61, v109, v113\n" \
  "v_mfma_f32_16x16x32_bf16 v[156:159], v[48:51], a[100:103], v[156:159]\n" \
  "ds_read_b64_tr_b16 v[188:189], v6 offset:18432\n" \
  "ds_read_b64_tr_b16 v[190:191], v6 offset:18440\n" \
  "v_mfma_f32_16x16x32_bf16 v[160:163], v[48:51], a[124:127], v[160:163]\n" \
  "v_cvt_pk_bf16_f32 v62, v117, v121\n" \
  "v_cvt_pk_bf16_f32 v63, v125, v129\n" \
  "v_mfma_f32_16x16x32_bf16 v[164:167], v[48:51], a[148:151], v[164:167]\n" \
  "v_mfma_f32_16x16x32_bf16 v[240:243], v[192:195], v[86:89], 0\n" \
  "v_mfma_f32_16x16x32_bf16 v[244:247], v[196:199], v[86:89], 0\n" \
  "v_mfma_f32_16x16x32_bf16 v[248:251], v[200:203], v[86:89], 0\n" \
  "v_mfma_f32_16x16x32_bf16 v[240:243], v[204:207], v[90:93], v[240:243]\n" \
  "v_mfma_f32_16x16x32_bf16 v[244:247], v[208:211], v[90:93], v[244:247]\n" \
  "v_mfma_f32_16x16x32_bf16 v[248:251], v[212:215], v[90:93], v[248:251]\n" \
  "ds_read_b64_tr_b16 v[192:193], v6 offset:24576\n" \
  "ds_read_b64_tr_b16 v[194:195], v6 offset:25600\n" \
  "v_mfma_f32_16x16x32_bf16 v[168:171], v[48:51], a[172:175], v[168:171]\n" \
  "v_cvt_pk_bf16_f32 v65, v133, v137\n" \
  "buffer_store_dwordx4 v[60:63], v10, s[36:39], s47 offen offset:1024 sc1\n" \
  "v_mfma_f32_16x16x32_bf16 v[172:175], v[48:51], a[196:199], v[172:175]\n" \
  "ds_read_b64_tr_b16 v[196:197], v6 offset:24584\n" \
  "ds_read_b64_tr_b16 v[198:199], v6 offset:25608\n" \
  "v_mfma_f32_16x16x32_bf16 v[176:179], v[48:51], a[220:223], v[176:179]\n" \
  "ds_read_b64_tr_b16 v[200:201], v6 offset:26624\n" \
  "ds_read_b64_tr_b16 v[202:203], v6 offset:26632\n" \
  "s_waitcnt lgkmcnt(13)\n" \
  "v_mfma_f32_16x16x32_bf16 v[140:143], v[52:55], a[8:11], v[140:143]\n" \
  "v_mfma_f32_16x16x32_bf16 v[144:147], v[52:55], a[32:35], v[144:147]\n" \
  "ds_read_b64 v[48:49], v0 offset:14344\n" \
  "ds_read_b64 v[50:51], v0 offset:17416\n" \
  "v_mfma_f32_16x16x32_bf16 v[148:151], v[52:55], a[56:59], v[148:151]\n" \
  "v_mfma_f32_16x16x32_bf16 v[152:155], v[52:55], a[80:83], v[152:155]\n" \
  "buffer_store_dword v65, v11, s[36:39], s47 offen offset:256 sc1\n" \
  "v_mfma_f32_16x16x32_bf16 v[156:159], v[52:55], a[104:107], v[156:159]\n" \
  "s_waitcnt vmcnt(28)\n" \
  "s_waitcnt lgkmcnt(0)\n" \
  "s_barrier\n" \
  "v_mfma_f32_16x16x32_bf16 v[160:163], v[52:55], a[128:131], v[160:163]\n" \
  "v_cvt_pk_bf16_f32 v56, v102, v106\n" \
  "v_cvt_pk_bf16_f32 v57, v110, v114\n" \
  "v_mfma_f32_16x16x32_bf16 v[164:167], v[52:55], a[152:155], v[164:167]\n" \
  "s_add_u32 m0, s45, 0\n" \
  "s_nop 0\n" \
  "buffer_load_dwordx4 v14, s[36:39], s52 offen offset:0 sc1 lds\n" \
  "v_mfma_f32_16x16x32_bf16 v[168:171], v[52:55], a[176:179], v[168:171]\n" \
  "v_cvt_pk_bf16_f32 v58, v118, v122\n" \
  "v_cvt_pk_bf16_f32 v59, v126, v130\n" \
  "v_mfma_f32_16x16x32_bf16 v[172:175], v[52:55], a[200:203], v[172:175]\n" \
  "v_cvt_pk_bf16_f32 v66, v228, v229\n" \
  "v_cvt_pk_bf16_f32 v67, v230, v231\n" \
  "v_mfma_f32_16x16x32_bf16 v[176:179], v[52:55], a[224:227], v[176:179]\n" \
  "v_cvt_pk_bf16_f32 v64, v134, v138\n" \
  "buffer_store_dwordx4 v[56:59], v10, s[36:39], s47 offen offset:2048 sc1\n" \
  "v_mfma_f32_16x16x32_bf16 v[140:143], v[44:47], a[12:15], v[140:143]\n" \
  "v_cvt_pk_bf16_f32 v68, v232, v233\n" \
  "v_cvt_pk_bf16_f32 v69, v234, v235\n" \
  "v_mfma_f32_16x16x32_bf16 v[144:147], v[44:47], a[36:39], v[144:147]\n" \
  "ds_read_b64 v[52:53], v0 offset:20488\n" \
  "ds_read_b64 v[54:55], v0 offset:23560\n" \
  "v_mfma_f32_16x16x32_bf16 v[148:151], v[44:47], a[60:63], v[148:151]\n" \
  "buffer_load_dwordx4 v14, s[36:39], s52 offen offset:1024 sc1 lds\n" \
  "v_mfma_f32_16x16x32_bf16 v[152:155], v[44:47], a[84:87], v[152:155]\n" \
  "v_cvt_pk_bf16_f32 v70, v236, v237\n" \
  "v_cvt_pk_bf16_f32 v71, v238, v239\n" \
  "v_mfma_f32_16x16x32_bf16 v[156:159], v[44:47], a[108:111], v[156:159]\n" \
  "ds_write_b128 v1, v[66:69] offset:0\n" \
  "v_mfma_f32_16x16x32_bf16 v[160:163], v[44:47], a[132:135], v[160:163]\n" \
  "buffer_store_dword v64, v11, s[36:39], s47 offen offset:512 sc1\n" \
  "v_mfma_f32_16x16x32_bf16 v[164:167], v[44:47], a[156:159], v[164:167]\n" \
  "ds_write_b64 v2, v[70:71] offset:2048\n" \
  "v_mfma_f32_16x16x32_bf16 v[168:171], v[44:47], a[180:183], v[168:171]\n" \
  "v_cvt_pk_bf16_f32 v60, v103, v107\n" \
  "v_cvt_pk_bf16_f32 v61, v111, v115\n" \
  "v_mfma_f32_16x16x32_bf16 v[172:175], v[44:47], a[204:207], v[172:175]\n" \
  "buffer_load_dwordx4 v14, s[36:39], s52 offen offset:2048 sc1 lds\n" \
  "v_mfma_f32_16x16x32_bf16 v[176:179], v[44:47], a[228:231], v[176:179]\n" \
  "v_cvt_pk_bf16_f32 v62, v119, v123\n" \
  "v_cvt_pk_bf16_f32 v63, v127, v131\n" \
  "v_mfma_f32_16x16x32_bf16 v[140:143], v[48:51], a[16:19], v[140:143]\n" \
  "s_add_u32 m0, s45, 3328\n" \
  "v_mfma_f32_16x16x32_bf16 v[228:231], v[180:183], v[86:89], 0\n" \
  "v_mfma_f32_16x16x32_bf16 v[232:235], v[184:187], v[86:89], 0\n" \
  "v_mfma_f32_16x16x32_bf16 v[236:239], v[188:191], v[86:89], 0\n" \
  "v_mfma_f32_16x16x32_bf16 v[228:231], v[216:219], v[92:95], v[228:231]\n" \
  "v_mfma_f32_16x16x32_bf16 v[232:235], v[220:223], v[92:95], v[232:235]\n" \
  "v_mfma_f32_16x16x32_bf16 v[236:239], v[224:227], v[92:95], v[236:239]\n" \
  "v_cvt_pk_bf16_f32 v72, v240, v241\n" \
  "v_cvt_pk_bf16_f32 v73, v242, v243\n" \
  "v_mfma_f32_16x16x32_bf16 v[144:147], v[48:51], a[40:43], v[144:147]\n" \
  "v_cvt_pk_bf16_f32 v65, v135, v139\n" \
  "buffer_store_dwordx4 v[60:63], v10, s[36:39], s47 offen offset:3072 sc1\n" \
  "v_mfma_f32_16x16x32_bf16 v[148:151], v[48:51], a[64:67], v[148:151]\n" \
  "v_cvt_pk_bf16_f32 v74, v244, v245\n" \
  "v_cvt_pk_bf16_f32 v75, v246, v247\n" \
  "v_mfma_f32_16x16x32_bf16 v[152:155], v[48:51], a[88:91], v[152:155]\n" \
  "v_cvt_pk_bf16_f32 v76, v248, v249\n" \
  "v_cvt_pk_bf16_f32 v77, v250, v251\n" \
  "v_mfma_f32_16x16x32_bf16 v[156:159], v[48:51], a[112:115], v[156:159]\n" \
  "buffer_load_dwordx4 v23, s[36:39], s54 offen offset:3840 sc1 lds\n" \
  "v_mfma_f32_16x16x32_bf16 v[160:163], v[48:51], a[136:139], v[160:163]\n" \
  "ds_write_b128 v1, v[72:75] offset:3072\n" \
  "v_mfma_f32_16x16x32_bf16 v[164:167], v[48:51], a[160:163], v[164:167]\n" \
  "ds_write_b64 v2, v[76:77] offset:5120\n" \
  "v_mfma_f32_16x16x32_bf16 v[168:171], v[48:51], a[184:187], v[168:171]\n" \
  "buffer_store_dword v65, v11, s[36:39], s47 offen offset:768 sc1\n" \
  "v_mfma_f32_16x16x32_bf16 v[172:175], v[48:51], a[208:211], v[172:175]\n" \
  "v_mfma_f32_16x16x32_bf16 v[240:243], v[192:195], v[86:89], 0\n" \
  "v_mfma_f32_16x16x32_bf16 v[244:247], v[196:199], v[86:89], 0\n" \
  "v_mfma_f32_16x16x32_bf16 v[248:251], v[200:203], v[86:89], 0\n" \
  "v_mfma_f32_16x16x32_bf16 v[240:243], v[216:219], v[90:93], v[240:243]\n" \
  "v_mfma_f32_16x16x32_bf16 v[244:247], v[220:223], v[90:93], v[244:247]\n" \
  "v_mfma_f32_16x16x32_bf16 v[248:251], v[224:227], v[90:93], v[248:251]\n" \
  "v_cvt_pk_bf16_f32 v66, v228, v229\n" \
  "v_cvt_pk_bf16_f32 v67, v230, v231\n" \
  "v_mfma_f32_16x16x32_bf16 v[176:179], v[48:51], a[232:235], v[176:179]\n" \
  "v_cvt_pk_bf16_f32 v68, v232, v233\n" \
  "v_cvt_pk_bf16_f32 v69, v234, v235\n" \
  "s_waitcnt lgkmcnt(4)\n" \
  "v_mfma_f32_16x16x32_bf16 v[140:143], v[52:55], a[20:23], v[140:143]\n" \
  "buffer_atomic_add v24, v18, s[36:39], s63 offen\n" \
  "v_mfma_f32_16x16x32_bf16 v[144:147], v[52:55], a[44:47], v[144:147]\n" \
  "s_add_u32 m0, s45, 32768\n" \
  "v_cvt_pk_bf16_f32 v70, v236, v237\n" \
  "v_cvt_pk_bf16_f32 v71, v238, v239\n" \
  "v_mfma_f32_16x16x32_bf16 v[148:151], v[52:55], a[68:71], v[148:151]\n" \
  "ds_write_b128 v1, v[66:69] offset:6144\n" \
  "v_mfma_f32_16x16x32_bf16 v[152:155], v[52:55], a[92:95], v[152:155]\n" \
  "buffer_load_dwordx4 v14, s[36:39], s53 offen offset:0 sc1 lds\n" \
  "v_mfma_f32_16x16x32_bf16 v[156:159], v[52:55], a[116:119], v[156:159]\n" \
  "ds_write_b64 v2, v[70:71] offset:8192\n" \
  "v_mfma_f32_16x16x32_bf16 v[160:163], v[52:55], a[140:143], v[160:163]\n" \
  "v_mfma_f32_16x16x32_bf16 v[164:167], v[52:55], a[164:167], v[164:167]\n" \
  "buffer_load_dwordx4 v14, s[36:39], s53 offen offset:1024 sc1 lds\n" \
  "v_mfma_f32_16x16x32_bf16 v[168:171], v[52:55], a[188:191], v[168:171]\n" \
  "v_mfma_f32_16x16x32_bf16 v[172:175], v[52:55], a[212:215], v[172:175]\n" \
  "v_mfma_f32_16x16x32_bf16 v[176:179], v[52:55], a[236:239], v[176:179]\n" \
  "buffer_load_dwordx4 v14, s[36:39], s53 offen offset:2048 sc1 lds\n" \
  "s_add_u32 m0, s45, 36096\n" \
  "s_nop 0\n" \
  "buffer_load_dwordx4 v23, s[36:39], s55 offen offset:3840 sc1 lds\n" \
  "v_cvt_pk_bf16_f32 v72, v240, v241\n" \
  "v_cvt_pk_bf16_f32 v73, v242, v243\n" \
  "v_cvt_pk_bf16_f32 v74, v244, v245\n" \
  "v_cvt_pk_bf16_f32 v75, v246, v247\n" \
  "v_cvt_pk_bf16_f32 v76, v248, v249\n" \
  "v_cvt_pk_bf16_f32 v77, v250, v251\n" \
  "ds_write_b128 v1, v[72:75] offset:9216\n" \
  "ds_write_b64 v2, v[76:77] offset:11264\n" \
  "s_mov_b32 s56, s47\n" \
  "s_mov_b32 s46, s48\n" \
  "s_mov_b32 s47, s49\n" \
  "s_mov_b32 s48, s52\n" \
  "s_mov_b32 s49, s53\n" \
  "s_sub_u32 s60, s44, s51\n" \
  "s_cmp_eq_u32 s60, 0\n" \
  "s_cselect_b32 s61, s57, 24576\n" \
  "v_add_u32 v1, s61, v1\n" \
  "v_add_u32 v2, s61, v2\n" \
  "s_cmp_eq_u32 s60, 24576\n" \
  "s_cselect_b32 s61, s57, 24576\n" \
  "v_add_u32 v0, s61, v0\n" \
  "s_cmp_eq_u32 s60, 49152\n" \
  "s_cselect_b32 s61, s57, 24576\n" \
  "s_add_u32 s44, s44, s61\n" \
  "s_add_u32 s42, s42, 2\n" \
  "s_lshl_b32 s63, s42, 7\n" \
  "s_cmp_eq_u32 s62, 0\n" \
  "s_cbranch_scc1 LHL_%=\n" \
  "s_nop 7\n" \
  "v_cvt_pk_bf16_f32 v56, v140, v144\n" \
  "v_cvt_pk_bf16_f32 v57, v148, v152\n" \
  "v_cvt_pk_bf16_f32 v58, v156, v160\n" \
  "v_cvt_pk_bf16_f32 v59, v164, v168\n" \
  "v_cvt_pk_bf16_f32 v64, v172, v176\n" \
  "buffer_store_dwordx4 v[56:59], v21, s[36:39], s56 offen offset:0 sc1\n" \
  "buffer_store_dword v64, v22, s[36:39], s56 offen offset:0 sc1\n" \
  "v_cvt_pk_bf16_f32 v60, v141, v145\n" \
  "v_cvt_pk_bf16_f32 v61, v149, v153\n" \
  "v_cvt_pk_bf16_f32 v62, v157, v161\n" \
  "v_cvt_pk_bf16_f32 v63, v165, v169\n" \
  "v_cvt_pk_bf16_f32 v65, v173, v177\n" \
  "buffer_store_dwordx4 v[60:63], v21, s[36:39], s56 offen offset:1024 sc1\n" \
  "buffer_store_dword v65, v22, s[36:39], s56 offen offset:256 sc1\n" \
  "v_cvt_pk_bf16_f32 v56, v142, v146\n" \
  "v_cvt_pk_bf16_f32 v57, v150, v154\n" \
  "v_cvt_pk_bf16_f32 v58, v158, v162\n" \
  "v_cvt_pk_bf16_f32 v59, v166, v170\n" \
  "v_cvt_pk_bf16_f32 v64, v174, v178\n" \
  "buffer_store_dwordx4 v[56:59], v21, s[36:39], s56 offen offset:2048 sc1\n" \
  "buffer_store_dword v64, v22, s[36:39], s56 offen offset:512 sc1\n" \
  "v_cvt_pk_bf16_f32 v60, v143, v147\n" \
  "v_cvt_pk_bf16_f32 v61, v151, v155\n" \
  "v_cvt_pk_bf16_f32 v62, v159, v163\n" \
  "v_cvt_pk_bf16_f32 v63, v167, v171\n" \
  "v_cvt_pk_bf16_f32 v65, v175, v179\n" \
  "buffer_store_dwordx4 v[60:63], v21, s[36:39], s56 offen offset:3072 sc1\n" \
  "buffer_store_dword v65, v22, s[36:39], s56 offen offset:768 sc1\n" \
  "s_nop 1\n"
#define FILL0_CLOBBERS \
  "a0", "a1", "a2", "a3", "a4", "a5", "a6", "a7", "a8", "a9", "a10", "a11", "a12", "a13", "a14", "a15", "a16", "a17", "a18", "a19", "a20", "a21", "a22", \
  "a23", "a24", "a25", "a26", "a27", "a28", "a29", "a30", "a31", "a32", "a33", "a34", "a35", "a36", "a37", "a38", "a39", "a40", "a41", "a42", "a43", \
  "a44", "a45", "a46", "a47", "a48", "a49", "a50", "a51", "a52", "a53", "a54", "a55", "a56", "a57", "a58", "a59", "a60", "a61", "a62", "a63", "a64", \
  "a65", "a66", "a67", "a68", "a69", "a70", "a71", "a72", "a73", "a74", "a75", "a76", "a77", "a78", "a79"
#define FILL0_ASM \
  "ds_read_b64_tr_b16 a[0:1], %0 offset:0\n" \
  "ds_read_b64_tr_b16 a[2:3], %0 offset:192\n" \
  "ds_read_b64_tr_b16 a[4:5], %0 offset:3072\n" \
  "ds_read_b64_tr_b16 a[6:7], %0 offset:3264\n" \
  "ds_read_b64_tr_b16 a[8:9], %0 offset:6144\n" \
  "ds_read_b64_tr_b16 a[10:11], %0 offset:6336\n" \
  "ds_read_b64_tr_b16 a[12:13], %0 offset:9216\n" \
  "ds_read_b64_tr_b16 a[14:15], %0 offset:9408\n" \
  "ds_read_b64_tr_b16 a[16:17], %0 offset:12288\n" \
  "ds_read_b64_tr_b16 a[18:19], %0 offset:12480\n" \
  "ds_read_b64_tr_b16 a[20:21], %0 offset:15360\n" \
  "ds_read_b64_tr_b16 a[22:23], %0 offset:15552\n" \
  "ds_read_b64_tr_b16 a[24:25], %0 offset:18432\n" \
  "ds_read_b64_tr_b16 a[26:27], %0 offset:18624\n" \
  "ds_read_b64_tr_b16 a[28:29], %0 offset:21504\n" \
  "ds_read_b64_tr_b16 a[30:31], %0 offset:21696\n" \
  "ds_read_b64_tr_b16 a[32:33], %0 offset:24576\n" \
  "ds_read_b64_tr_b16 a[34:35], %0 offset:24768\n" \
  "ds_read_b64_tr_b16 a[36:37], %0 offset:27648\n" \
  "ds_read_b64_tr_b16 a[38:39], %0 offset:27840\n" \
  "ds_read_b64_tr_b16 a[40:41], %0 offset:30720\n" \
  "ds_read_b64_tr_b16 a[42:43], %0 offset:30912\n" \
  "ds_read_b64_tr_b16 a[44:45], %0 offset:33792\n" \
  "ds_read_b64_tr_b16 a[46:47], %0 offset:33984\n" \
  "ds_read_b64_tr_b16 a[48:49], %0 offset:36864\n" \
  "ds_read_b64_tr_b16 a[50:51], %0 offset:37056\n" \
  "ds_read_b64_tr_b16 a[52:53], %0 offset:39936\n" \
  "ds_read_b64_tr_b16 a[54:55], %0 offset:40128\n" \
  "ds_read_b64_tr_b16 a[56:57], %0 offset:43008\n" \
  "ds_read_b64_tr_b16 a[58:59], %0 offset:43200\n" \
  "ds_read_b64_tr_b16 a[60:61], %0 offset:46080\n" \
  "ds_read_b64_tr_b16 a[62:63], %0 offset:46272\n" \
  "ds_read_b64_tr_b16 a[64:65], %0 offset:49152\n" \
  "ds_read_b64_tr_b16 a[66:67], %0 offset:49344\n" \
  "ds_read_b64_tr_b16 a[68:69], %0 offset:52224\n" \
  "ds_read_b64_tr_b16 a[70:71], %0 offset:52416\n" \
  "ds_read_b64_tr_b16 a[72:73], %0 offset:55296\n" \
  "ds_read_b64_tr_b16 a[74:75], %0 offset:55488\n" \
  "ds_read_b64_tr_b16 a[76:77], %0 offset:58368\n" \
  "ds_read_b64_tr_b16 a[78:79], %0 offset:58560\n" \
  "s_waitcnt lgkmcnt(0)\n"
#define FILL80_CLOBBERS \
  "a80", "a81", "a82", "a83", "a84", "a85", "a86", "a87", "a88", "a89", "a90", "a91", "a92", "a93", "a94", "a95", "a96", "a97", "a98", "a99", "a100", \
  "a101", "a102", "a103", "a104", "a105", "a106", "a107", "a108", "a109", "a110", "a111", "a112", "a113", "a114", "a115", "a116", "a117", "a118", \
  "a119", "a120", "a121", "a122", "a123", "a124", "a125", "a126", "a127", "a128", "a129", "a130", "a131", "a132", "a133", "a134", "a135", "a136", \
  "a137", "a138", "a139", "a140", "a141", "a142", "a143", "a144", "a145", "a146", "a147", "a148", "a149", "a150", "a151", "a152", "a153", "a154", \
  "a155", "a156", "a157", "a158", "a159"
#define FILL80_ASM \
  "ds_read_b64_tr_b16 a[80:81], %0 offset:0\n" \
  "ds_read_b64_tr_b16 a[82:83], %0 offset:192\n" \
  "ds_read_b64_tr_b16 a[84:85], %0 offset:3072\n" \
  "ds_read_b64_tr_b16 a[86:87], %0 offset:3264\n" \
  "ds_read_b64_tr_b16 a[88:89], %0 offset:6144\n" \
  "ds_read_b64_tr_b16 a[90:91], %0 offset:6336\n" \
  "ds_read_b64_tr_b16 a[92:93], %0 offset:9216\n" \
  "ds_read_b64_tr_b16 a[94:95], %0 offset:9408\n" \
  "ds_read_b64_tr_b16 a[96:97], %0 offset:12288\n" \
  "ds_read_b64_tr_b16 a[98:99], %0 offset:12480\n" \
  "ds_read_b64_tr_b16 a[100:101], %0 offset:15360\n" \
  "ds_read_b64_tr_b16 a[102:103], %0 offset:15552\n" \
  "ds_read_b64_tr_b16 a[104:105], %0 offset:18432\n" \
  "ds_read_b64_tr_b16 a[106:107], %0 offset:18624\n" \
  "ds_read_b64_tr_b16 a[108:109], %0 offset:21504\n" \
  "ds_read_b64_tr_b16 a[110:111], %0 offset:21696\n" \
  "ds_read_b64_tr_b16 a[112:113], %0 offset:24576\n" \
  "ds_read_b64_tr_b16 a[114:115], %0 offset:24768\n" \
  "ds_read_b64_tr_b16 a[116:117], %0 offset:27648\n" \
  "ds_read_b64_tr_b16 a[118:119], %0 offset:27840\n" \
  "ds_read_b64_tr_b16 a[120:121], %0 offset:30720\n" \
  "ds_read_b64_tr_b16 a[122:123], %0 offset:30912\n" \
  "ds_read_b64_tr_b16 a[124:125], %0 offset:33792\n" \
  "ds_read_b64_tr_b16 a[126:127], %0 offset:33984\n" \
  "ds_read_b64_tr_b16 a[128:129], %0 offset:36864\n" \
  "ds_read_b64_tr_b16 a[130:131], %0 offset:37056\n" \
  "ds_read_b64_tr_b16 a[132:133], %0 offset:39936\n" \
  "ds_read_b64_tr_b16 a[134:135], %0 offset:40128\n" \
  "ds_read_b64_tr_b16 a[136:137], %0 offset:43008\n" \
  "ds_read_b64_tr_b16 a[138:139], %0 offset:43200\n" \
  "ds_read_b64_tr_b16 a[140:141], %0 offset:46080\n" \
  "ds_read_b64_tr_b16 a[142:143], %0 offset:46272\n" \
  "ds_read_b64_tr_b16 a[144:145], %0 offset:49152\n" \
  "ds_read_b64_tr_b16 a[146:147], %0 offset:49344\n" \
  "ds_read_b64_tr_b16 a[148:149], %0 offset:52224\n" \
  "ds_read_b64_tr_b16 a[150:151], %0 offset:52416\n" \
  "ds_read_b64_tr_b16 a[152:153], %0 offset:55296\n" \
  "ds_read_b64_tr_b16 a[154:155], %0 offset:55488\n" \
  "ds_read_b64_tr_b16 a[156:157], %0 offset:58368\n" \
  "ds_read_b64_tr_b16 a[158:159], %0 offset:58560\n" \
  "s_waitcnt lgkmcnt(0)\n"
#define FILL160_CLOBBERS \
  "a160", "a161", "a162", "a163", "a164", "a165", "a166", "a167", "a168", "a169", "a170", "a171", "a172", "a173", "a174", "a175", "a176", "a177", \
  "a178", "a179", "a180", "a181", "a182", "a183", "a184", "a185", "a186", "a187", "a188", "a189", "a190", "a191", "a192", "a193", "a194", "a195", \
  "a196", "a197", "a198", "a199", "a200", "a201", "a202", "a203", "a204", "a205", "a206", "a207", "a208", "a209", "a210", "a211", "a212", "a213", \
  "a214", "a215", "a216", "a217", "a218", "a219", "a220", "a221", "a222", "a223", "a224", "a225", "a226", "a227", "a228", "a229", "a230", "a231", \
  "a232", "a233", "a234", "a235", "a236", "a237", "a238", "a239"
#define FILL160_ASM \
  "ds_read_b64_tr_b16 a[160:161], %0 offset:0\n" \
  "ds_read_b64_tr_b16 a[162:163], %0 offset:192\n" \
  "ds_read_b64_tr_b16 a[164:165], %0 offset:3072\n" \
  "ds_read_b64_tr_b16 a[166:167], %0 offset:3264\n" \
  "ds_read_b64_tr_b16 a[168:169], %0 offset:6144\n" \
  "ds_read_b64_tr_b16 a[170:171], %0 offset:6336\n" \
  "ds_read_b64_tr_b16 a[172:173], %0 offset:9216\n" \
  "ds_read_b64_tr_b16 a[174:175], %0 offset:9408\n" \
  "ds_read_b64_tr_b16 a[176:177], %0 offset:12288\n" \
  "ds_read_b64_tr_b16 a[178:179], %0 offset:12480\n" \
  "ds_read_b64_tr_b16 a[180:181], %0 offset:15360\n" \
  "ds_read_b64_tr_b16 a[182:183], %0 offset:15552\n" \
  "ds_read_b64_tr_b16 a[184:185], %0 offset:18432\n" \
  "ds_read_b64_tr_b16 a[186:187], %0 offset:18624\n" \
  "ds_read_b64_tr_b16 a[188:189], %0 offset:21504\n" \
  "ds_read_b64_tr_b16 a[190:191], %0 offset:21696\n" \
  "ds_read_b64_tr_b16 a[192:193], %0 offset:24576\n" \
  "ds_read_b64_tr_b16 a[194:195], %0 offset:24768\n" \
  "ds_read_b64_tr_b16 a[196:197], %0 offset:27648\n" \
  "ds_read_b64_tr_b16 a[198:199], %0 offset:27840\n" \
  "ds_read_b64_tr_b16 a[200:201], %0 offset:30720\n" \
  "ds_read_b64_tr_b16 a[202:203], %0 offset:30912\n" \
  "ds_read_b64_tr_b16 a[204:205], %0 offset:33792\n" \
  "ds_read_b64_tr_b16 a[206:207], %0 offset:33984\n" \
  "ds_read_b64_tr_b16 a[208:209], %0 offset:36864\n" \
  "ds_read_b64_tr_b16 a[210:211], %0 offset:37056\n" \
  "ds_read_b64_tr_b16 a[212:213], %0 offset:39936\n" \
  "ds_read_b64_tr_b16 a[214:215], %0 offset:40128\n" \
  "ds_read_b64_tr_b16 a[216:217], %0 offset:43008\n" \
  "ds_read_b64_tr_b16 a[218:219], %0 offset:43200\n" \
  "ds_read_b64_tr_b16 a[220:221], %0 offset:46080\n" \
  "ds_read_b64_tr_b16 a[222:223], %0 offset:46272\n" \
  "ds_read_b64_tr_b16 a[224:225], %0 offset:49152\n" \
  "ds_read_b64_tr_b16 a[226:227], %0 offset:49344\n" \
  "ds_read_b64_tr_b16 a[228:229], %0 offset:52224\n" \
  "ds_read_b64_tr_b16 a[230:231], %0 offset:52416\n" \
  "ds_read_b64_tr_b16 a[232:233], %0 offset:55296\n" \
  "ds_read_b64_tr_b16 a[234:235], %0 offset:55488\n" \
  "ds_read_b64_tr_b16 a[236:237], %0 offset:58368\n" \
  "ds_read_b64_tr_b16 a[238:239], %0 offset:58560\n" \
  "s_waitcnt lgkmcnt(0)\n"
#define ATTN_CLOBBERS \
  "v128", "v129", "v130", "v131", "v132", "v133", "v134", "v135", "v136", "v137", "v138", "v139", "v140", "v141", "v142", "v143", "v144", "v145", \
  "v146", "v147", "v148", "v149", "v150", "v151", "v152", "v153", "v154", "v155", "v156", "v157", "v158", "v159", "v160", "v161", "v162", "v163", \
  "v164", "v165", "v166", "v167", "v168", "v169", "v170", "v171", "v172", "v173", "v174", "v175", "v176", "v177", "v178", "v179", "v180", "v181", \
  "v182", "v183", "v184", "v185", "v186", "v187", "v188", "v189", "v190", "v191", "v192", "v193", "v194", "v195", "v196", "v197", "v198", "v199", \
  "v200", "v201", "v202", "v203", "v204", "v205", "v206", "v207", "v208", "v209", "v210", "v211", "v212", "v213", "v214", "v215", "v216", "v217", \
  "v218", "v219", "v220", "v221", "v222", "v223", "v224", "v225", "v226", "v227", "v228", "v229", "v230", "v231", "v232", "v233", "v234", "v235", \
  "v236", "v237", "v238", "v239", "v240", "v241", "v242", "v243", "v244", "v245", "v246", "v247", "v248", "v249", "v250", "v251", "v252", "v253"
#define ATTN_ASM \
  "ds_read_b128 v[128:131], v100 offset:0\n" \
  "ds_read_b128 v[132:135], v103 offset:0\n" \
  "ds_read_b128 v[136:139], v102 offset:3072\n" \
  "ds_read_b128 v[140:143], v102 offset:4096\n" \
  "ds_read_b128 v[144:147], v100 offset:3072\n" \
  "ds_read_b128 v[148:151], v103 offset:8192\n" \
  "ds_read_b128 v[152:155], v102 offset:11264\n" \
  "ds_read_b128 v[156:159], v102 offset:12288\n" \
  "ds_read_b128 v[160:163], v100 offset:6144\n" \
  "ds_read_b128 v[164:167], v103 offset:16384\n" \
  "ds_read_b128 v[168:171], v102 offset:19456\n" \
  "ds_read_b128 v[172:175], v102 offset:20480\n" \
  "v_mov_b32 v248, 0\n" \
  "v_mov_b32 v249, 0\n" \
  "v_mov_b32 v252, 0\n" \
  "v_mov_b32 v253, 0\n" \
  "v_mov_b32 v242, 0x3d93cd3a\n" \
  "v_mov_b32 v243, 0x3d93cd3a\n" \
  "s_waitcnt lgkmcnt(10)\n" \
  "v_mfma_f32_16x16x32_bf16 v[224:227], v[132:135], v[128:131], v[116:119]\n" \
  "s_waitcnt lgkmcnt(9)\n" \
  "v_mfma_f32_16x16x32_bf16 v[228:231], v[136:139], v[128:131], v[120:123]\n" \
  "s_waitcnt lgkmcnt(8)\n" \
  "v_mfma_f32_16x16x32_bf16 v[232:235], v[140:143], v[128:131], v[124:127]\n" \
  "ds_read_b128 v[176:179], v100 offset:9216\n" \
  "ds_read_b128 v[180:183], v103 offset:24576\n" \
  "ds_read_b128 v[184:187], v102 offset:27648\n" \
  "ds_read_b128 v[188:191], v102 offset:28672\n" \
  "s_waitcnt lgkmcnt(10)\n" \
  "v_mfma_f32_16x16x32_bf16 v[224:227], v[148:151], v[144:147], v[224:227]\n" \
  "s_waitcnt lgkmcnt(9)\n" \
  "v_mfma_f32_16x16x32_bf16 v[228:231], v[152:155], v[144:147], v[228:231]\n" \
  "s_waitcnt lgkmcnt(8)\n" \
  "v_mfma_f32_16x16x32_bf16 v[232:235], v[156:159], v[144:147], v[232:235]\n" \
  "ds_read_b64 v[192:193], v101 offset:2048\n" \
  "ds_read_b64 v[196:197], v104 offset:0\n" \
  "ds_read_b64 v[200:201], v102 offset:5120\n" \
  "ds_read_b64 v[204:205], v102 offset:5128\n" \
  "ds_read_b64 v[194:195], v101 offset:5120\n" \
  "ds_read_b64 v[198:199], v104 offset:8192\n" \
  "ds_read_b64 v[202:203], v102 offset:13312\n" \
  "s_waitcnt lgkmcnt(14)\n" \
  "ds_read_b64 v[206:207], v102 offset:13320\n" \
  "s_waitcnt lgkmcnt(14)\n" \
  "v_mfma_f32_16x16x32_bf16 v[224:227], v[164:167], v[160:163], v[224:227]\n" \
  "s_waitcnt lgkmcnt(13)\n" \
  "v_mfma_f32_16x16x32_bf16 v[228:231], v[168:171], v[160:163], v[228:231]\n" \
  "s_waitcnt lgkmcnt(12)\n" \
  "v_mfma_f32_16x16x32_bf16 v[232:235], v[172:175], v[160:163], v[232:235]\n" \
  "ds_read_b64 v[208:209], v101 offset:8192\n" \
  "ds_read_b64 v[212:213], v104 offset:16384\n" \
  "ds_read_b64 v[216:217], v102 offset:21504\n" \
  "s_waitcnt lgkmcnt(14)\n" \
  "ds_read_b64 v[220:221], v102 offset:21512\n" \
  "s_waitcnt lgkmcnt(14)\n" \
  "ds_read_b64 v[210:211], v101 offset:11264\n" \
  "s_waitcnt lgkmcnt(14)\n" \
  "ds_read_b64 v[214:215], v104 offset:24576\n" \
  "s_waitcnt lgkmcnt(14)\n" \
  "ds_read_b64 v[218:219], v102 offset:29696\n" \
  "s_waitcnt lgkmcnt(14)\n" \
  "ds_read_b64 v[222:223], v102 offset:29704\n" \
  "v_mfma_f32_16x16x32_bf16 v[224:227], v[180:183], v[176:179], v[224:227]\n" \
  "v_mfma_f32_16x16x32_bf16 v[228:231], v[184:187], v[176:179], v[228:231]\n" \
  "v_mfma_f32_16x16x32_bf16 v[232:235], v[188:191], v[176:179], v[232:235]\n" \
  "s_waitcnt lgkmcnt(10)\n" \
  "v_mfma_f32_16x16x32_bf16 v[224:227], v[196:199], v[192:195], v[224:227]\n" \
  "s_waitcnt lgkmcnt(9)\n" \
  "v_mfma_f32_16x16x32_bf16 v[228:231], v[200:203], v[192:195], v[228:231]\n" \
  "s_waitcnt lgkmcnt(8)\n" \
  "v_mfma_f32_16x16x32_bf16 v[232:235], v[204:207], v[192:195], v[232:235]\n" \
  "s_waitcnt lgkmcnt(2)\n" \
  "v_mfma_f32_16x16x32_bf16 v[224:227], v[212:215], v[208:211], v[224:227]\n" \
  "s_waitcnt lgkmcnt(1)\n" \
  "v_mfma_f32_16x16x32_bf16 v[228:231], v[216:219], v[208:211], v[228:231]\n" \
  "s_waitcnt lgkmcnt(0)\n" \
  "v_mfma_f32_16x16x32_bf16 v[232:235], v[220:223], v[208:211], v[232:235]\n" \
  "ds_read_b64_tr_b16 v[128:129], v105 offset:0\n" \
  "ds_read_b64_tr_b16 v[130:131], v105 offset:1024\n" \
  "ds_read_b64_tr_b16 v[132:133], v105 offset:8\n" \
  "ds_read_b64_tr_b16 v[134:135], v105 offset:1032\n" \
  "ds_read_b64_tr_b16 v[136:137], v105 offset:2048\n" \
  "ds_read_b64_tr_b16 v[138:139], v105 offset:2056\n" \
  "ds_read_b64_tr_b16 v[140:141], v105 offset:8192\n" \
  "ds_read_b64_tr_b16 v[142:143], v105 offset:9216\n" \
  "v_cvt_pk_bf16_f32 v236, v224, v225\n" \
  "v_cvt_pk_bf16_f32 v237, v226, v227\n" \
  "ds_read_b64_tr_b16 v[144:145], v105 offset:8200\n" \
  "v_cvt_pk_bf16_f32 v238, v228, v229\n" \
  "v_cvt_pk_bf16_f32 v239, v230, v231\n" \
  "ds_read_b64_tr_b16 v[146:147], v105 offset:9224\n" \
  "v_cvt_pk_bf16_f32 v240, v232, v233\n" \
  "v_cvt_pk_bf16_f32 v241, v234, v235\n" \
  "ds_read_b64_tr_b16 v[148:149], v105 offset:10240\n" \
  "v_lshlrev_b32 v224, 16, v236\n" \
  "v_and_b32 v225, 0xffff0000, v236\n" \
  "ds_read_b64_tr_b16 v[150:151], v105 offset:10248\n" \
  "v_pk_mul_f32 v[224:225], v[224:225], v[242:243]\n" \
  "v_cvt_pk_bf16_f32 v236, v224, v225\n" \
  "ds_read_b64_tr_b16 v[152:153], v105 offset:16384\n" \
  "v_lshlrev_b32 v224, 16, v236\n" \
  "v_and_b32 v225, 0xffff0000, v236\n" \
  "ds_read_b64_tr_b16 v[154:155], v105 offset:17408\n" \
  "v_lshlrev_b32 v226, 16, v237\n" \
  "v_and_b32 v227, 0xffff0000, v237\n" \
  "ds_read_b64_tr_b16 v[156:157], v105 offset:16392\n" \
  "v_pk_mul_f32 v[226:227], v[226:227], v[242:243]\n" \
  "v_cvt_pk_bf16_f32 v237, v226, v227\n" \
  "s_waitcnt lgkmcnt(14)\n" \
  "ds_read_b64_tr_b16 v[158:159], v105 offset:17416\n" \
  "v_lshlrev_b32 v226, 16, v237\n" \
  "v_and_b32 v227, 0xffff0000, v237\n" \
  "s_waitcnt lgkmcnt(14)\n" \
  "ds_read_b64_tr_b16 v[160:161], v105 offset:18432\n" \
  "v_lshlrev_b32 v228, 16, v238\n" \
  "v_and_b32 v229, 0xffff0000, v238\n" \
  "s_waitcnt lgkmcnt(14)\n" \
  "ds_read_b64_tr_b16 v[162:163], v105 offset:18440\n" \
  "v_pk_mul_f32 v[228:229], v[228:229], v[242:243]\n" \
  "v_cvt_pk_bf16_f32 v238, v228, v229\n" \
  "s_waitcnt lgkmcnt(14)\n" \
  "ds_read_b64_tr_b16 v[164:165], v105 offset:24576\n" \
  "v_lshlrev_b32 v228, 16, v238\n" \
  "v_and_b32 v229, 0xffff0000, v238\n" \
  "s_waitcnt lgkmcnt(14)\n" \
  "ds_read_b64_tr_b16 v[166:167], v105 offset:25600\n" \
  "v_lshlrev_b32 v230, 16, v239\n" \
  "v_and_b32 v231, 0xffff0000, v239\n" \
  "s_waitcnt lgkmcnt(14)\n" \
  "ds_read_b64_tr_b16 v[168:169], v105 offset:24584\n" \
  "v_pk_mul_f32 v[230:231], v[230:231], v[242:243]\n" \
  "v_cvt_pk_bf16_f32 v239, v230, v231\n" \
  "s_waitcnt lgkmcnt(14)\n" \
  "ds_read_b64_tr_b16 v[170:171], v105 offset:25608\n" \
  "v_lshlrev_b32 v230, 16, v239\n" \
  "v_and_b32 v231, 0xffff0000, v239\n" \
  "s_waitcnt lgkmcnt(14)\n" \
  "ds_read_b64_tr_b16 v[172:173], v105 offset:26624\n" \
  "v_lshlrev_b32 v232, 16, v240\n" \
  "v_and_b32 v233, 0xffff0000, v240\n" \
  "s_waitcnt lgkmcnt(14)\n" \
  "ds_read_b64_tr_b16 v[174:175], v105 offset:26632\n" \
  "v_pk_mul_f32 v[232:233], v[232:233], v[242:243]\n" \
  "v_cvt_pk_bf16_f32 v240, v232, v233\n" \
  "s_waitcnt lgkmcnt(14)\n" \
  "ds_read_b64 v[176:177], v106 offset:0\n" \
  "v_lshlrev_b32 v232, 16, v240\n" \
  "v_and_b32 v233, 0xffff0000, v240\n" \
  "s_waitcnt lgkmcnt(14)\n" \
  "ds_read_b64 v[180:181], v107 offset:0\n" \
  "v_lshlrev_b32 v234, 16, v241\n" \
  "v_and_b32 v235, 0xffff0000, v241\n" \
  "s_waitcnt lgkmcnt(14)\n" \
  "ds_read_b64 v[184:185], v108 offset:0\n" \
  "v_pk_mul_f32 v[234:235], v[234:235], v[242:243]\n" \
  "v_cvt_pk_bf16_f32 v241, v234, v235\n" \
  "s_waitcnt lgkmcnt(14)\n" \
  "ds_read_b64 v[178:179], v106 offset:8192\n" \
  "v_lshlrev_b32 v234, 16, v241\n" \
  "v_and_b32 v235, 0xffff0000, v241\n" \
  "s_waitcnt lgkmcnt(14)\n" \
  "ds_read_b64 v[182:183], v107 offset:8192\n" \
  "v_max3_f32 v236, v224, v225, v226\n" \
  "v_max3_f32 v236, v236, v227, v228\n" \
  "s_waitcnt lgkmcnt(14)\n" \
  "ds_read_b64 v[186:187], v108 offset:8192\n" \
  "v_max3_f32 v236, v236, v229, v230\n" \
  "v_max3_f32 v236, v236, v231, v232\n" \
  "s_waitcnt lgkmcnt(14)\n" \
  "ds_read_b64 v[188:189], v106 offset:16384\n" \
  "v_max3_f32 v236, v236, v233, v234\n" \
  "v_max_f32 v236, v236, v235\n" \
  "s_waitcnt lgkmcnt(14)\n" \
  "ds_read_b64 v[192:193], v107 offset:16384\n" \
  "v_mov_b32 v237, v236\n" \
  "s_nop 1\n" \
  "v_permlane16_swap_b32 v236, v237\n" \
  "v_max_f32 v236, v236, v237\n" \
  "s_waitcnt lgkmcnt(14)\n" \
  "ds_read_b64 v[196:197], v108 offset:16384\n" \
  "v_mov_b32 v237, v236\n" \
  "s_nop 1\n" \
  "v_permlane32_swap_b32 v236, v237\n" \
  "v_max_f32 v236, v236, v237\n" \
  "s_waitcnt lgkmcnt(14)\n" \
  "ds_read_b64 v[190:191], v106 offset:24576\n" \
  "v_mov_b32 v238, 0x3fb8aa3b\n" \
  "v_mov_b32 v239, 0x3fb8aa3b\n" \
  "v_pk_add_f32 v[224:225], v[224:225], v[236:237] op_sel_hi:[1,0] neg_lo:[0,1] neg_hi:[0,1]\n" \
  "v_pk_mul_f32 v[224:225], v[224:225], v[238:239]\n" \
  "s_waitcnt lgkmcnt(14)\n" \
  "ds_read_b64 v[194:195], v107 offset:24576\n" \
  "v_pk_add_f32 v[226:227], v[226:227], v[236:237] op_sel_hi:[1,0] neg_lo:[0,1] neg_hi:[0,1]\n" \
  "v_pk_mul_f32 v[226:227], v[226:227], v[238:239]\n" \
  "s_waitcnt lgkmcnt(14)\n" \
  "ds_read_b64 v[198:199], v108 offset:24576\n" \
  "v_pk_add_f32 v[228:229], v[228:229], v[236:237] op_sel_hi:[1,0] neg_lo:[0,1] neg_hi:[0,1]\n" \
  "v_pk_mul_f32 v[228:229], v[228:229], v[238:239]\n" \
  "v_pk_add_f32 v[230:231], v[230:231], v[236:237] op_sel_hi:[1,0] neg_lo:[0,1] neg_hi:[0,1]\n" \
  "v_pk_mul_f32 v[230:231], v[230:231], v[238:239]\n" \
  "v_pk_add_f32 v[232:233], v[232:233], v[236:237] op_sel_hi:[1,0] neg_lo:[0,1] neg_hi:[0,1]\n" \
  "v_pk_mul_f32 v[232:233], v[232:233], v[238:239]\n" \
  "v_pk_add_f32 v[234:235], v[234:235], v[236:237] op_sel_hi:[1,0] neg_lo:[0,1] neg_hi:[0,1]\n" \
  "v_pk_mul_f32 v[234:235], v[234:235], v[238:239]\n" \
  "v_exp_f32 v224, v224\n" \
  "v_exp_f32 v225, v225\n" \
  "v_exp_f32 v226, v226\n" \
  "v_exp_f32 v227, v227\n" \
  "v_exp_f32 v228, v228\n" \
  "v_exp_f32 v229, v229\n" \
  "v_exp_f32 v230, v230\n" \
  "v_exp_f32 v231, v231\n" \
  "v_exp_f32 v232, v232\n" \
  "v_exp_f32 v233, v233\n" \
  "v_exp_f32 v234, v234\n" \
  "v_exp_f32 v235, v235\n" \
  "v_pk_add_f32 v[236:237], v[224:225], v[226:227]\n" \
  "v_pk_add_f32 v[238:239], v[228:229], v[230:231]\n" \
  "v_pk_add_f32 v[240:241], v[232:233], v[234:235]\n" \
  "v_pk_add_f32 v[236:237], v[236:237], v[238:239]\n" \
  "v_pk_add_f32 v[236:237], v[236:237], v[240:241]\n" \
  "v_add_f32 v236, v236, v237\n" \
  "v_mov_b32 v237, v236\n" \
  "s_nop 1\n" \
  "v_permlane16_swap_b32 v236, v237\n" \
  "v_add_f32 v236, v236, v237\n" \
  "v_mov_b32 v237, v236\n" \
  "s_nop 1\n" \
  "v_permlane32_swap_b32 v236, v237\n" \
  "v_add_f32 v236, v236, v237\n" \
  "v_rcp_f32 v236, v236\n" \
  "s_nop 0\n" \
  "v_pk_mul_f32 v[228:229], v[228:229], v[236:237] op_sel_hi:[1,0]\n" \
  "v_pk_mul_f32 v[230:231], v[230:231], v[236:237] op_sel_hi:[1,0]\n" \
  "v_pk_mul_f32 v[232:233], v[232:233], v[236:237] op_sel_hi:[1,0]\n" \
  "v_pk_mul_f32 v[234:235], v[234:235], v[236:237] op_sel_hi:[1,0]\n" \
  "v_pk_mul_f32 v[224:225], v[224:225], v[236:237] op_sel_hi:[1,0]\n" \
  "v_pk_mul_f32 v[226:227], v[226:227], v[236:237] op_sel_hi:[1,0]\n" \
  "v_cvt_pk_bf16_f32 v244, v228, v229\n" \
  "v_cvt_pk_bf16_f32 v245, v230, v231\n" \
  "v_cvt_pk_bf16_f32 v246, v232, v233\n" \
  "v_cvt_pk_bf16_f32 v247, v234, v235\n" \
  "v_cvt_pk_bf16_f32 v250, v224, v225\n" \
  "v_cvt_pk_bf16_f32 v251, v226, v227\n" \
  "s_nop 1\n" \
  "v_mfma_f32_16x16x32_bf16 v[200:203], v[128:131], v[244:247], 0\n" \
  "v_mfma_f32_16x16x32_bf16 v[204:207], v[132:135], v[244:247], 0\n" \
  "v_mfma_f32_16x16x32_bf16 v[208:211], v[136:139], v[244:247], 0\n" \
  "s_waitcnt lgkmcnt(8)\n" \
  "v_mfma_f32_16x16x32_bf16 v[200:203], v[176:179], v[250:253], v[200:203]\n" \
  "s_waitcnt lgkmcnt(7)\n" \
  "v_mfma_f32_16x16x32_bf16 v[204:207], v[180:183], v[250:253], v[204:207]\n" \
  "s_waitcnt lgkmcnt(6)\n" \
  "v_mfma_f32_16x16x32_bf16 v[208:211], v[184:187], v[250:253], v[208:211]\n" \
  "v_mfma_f32_16x16x32_bf16 v[212:215], v[140:143], v[244:247], 0\n" \
  "v_mfma_f32_16x16x32_bf16 v[216:219], v[144:147], v[244:247], 0\n" \
  "s_nop 3\n" \
  "s_nop 0\n" \
  "v_mfma_f32_16x16x32_bf16 v[220:223], v[148:151], v[244:247], 0\n" \
  "v_cvt_pk_bf16_f32 v224, v200, v201\n" \
  "v_cvt_pk_bf16_f32 v225, v202, v203\n" \
  "v_mfma_f32_16x16x32_bf16 v[212:215], v[176:179], v[248:251], v[212:215]\n" \
  "v_cvt_pk_bf16_f32 v226, v204, v205\n" \
  "v_cvt_pk_bf16_f32 v227, v206, v207\n" \
  "v_mfma_f32_16x16x32_bf16 v[216:219], v[180:183], v[248:251], v[216:219]\n" \
  "v_cvt_pk_bf16_f32 v228, v208, v209\n" \
  "v_cvt_pk_bf16_f32 v229, v210, v211\n" \
  "v_mfma_f32_16x16x32_bf16 v[220:223], v[184:187], v[248:251], v[220:223]\n" \
  "ds_write_b128 v100, v[224:227] offset:0\n" \
  "ds_write_b64 v101, v[228:229] offset:2048\n" \
  "v_mfma_f32_16x16x32_bf16 v[200:203], v[152:155], v[244:247], 0\n" \
  "v_mfma_f32_16x16x32_bf16 v[204:207], v[156:159], v[244:247], 0\n" \
  "s_nop 3\n" \
  "s_nop 0\n" \
  "v_mfma_f32_16x16x32_bf16 v[208:211], v[160:163], v[244:247], 0\n" \
  "v_cvt_pk_bf16_f32 v230, v212, v213\n" \
  "v_cvt_pk_bf16_f32 v231, v214, v215\n" \
  "s_waitcnt lgkmcnt(4)\n" \
  "v_mfma_f32_16x16x32_bf16 v[200:203], v[188:191], v[250:253], v[200:203]\n" \
  "v_cvt_pk_bf16_f32 v232, v216, v217\n" \
  "v_cvt_pk_bf16_f32 v233, v218, v219\n" \
  "s_waitcnt lgkmcnt(3)\n" \
  "v_mfma_f32_16x16x32_bf16 v[204:207], v[192:195], v[250:253], v[204:207]\n" \
  "v_cvt_pk_bf16_f32 v234, v220, v221\n" \
  "v_cvt_pk_bf16_f32 v235, v222, v223\n" \
  "s_waitcnt lgkmcnt(2)\n" \
  "v_mfma_f32_16x16x32_bf16 v[208:211], v[196:199], v[250:253], v[208:211]\n" \
  "ds_write_b128 v100, v[230:233] offset:3072\n" \
  "ds_write_b64 v101, v[234:235] offset:5120\n" \
  "v_mfma_f32_16x16x32_bf16 v[212:215], v[164:167], v[244:247], 0\n" \
  "v_mfma_f32_16x16x32_bf16 v[216:219], v[168:171], v[244:247], 0\n" \
  "s_nop 3\n" \
  "s_nop 0\n" \
  "v_mfma_f32_16x16x32_bf16 v[220:223], v[172:175], v[244:247], 0\n" \
  "v_cvt_pk_bf16_f32 v224, v200, v201\n" \
  "v_cvt_pk_bf16_f32 v225, v202, v203\n" \
  "v_mfma_f32_16x16x32_bf16 v[212:215], v[188:191], v[248:251], v[212:215]\n" \
  "v_cvt_pk_bf16_f32 v226, v204, v205\n" \
  "v_cvt_pk_bf16_f32 v227, v206, v207\n" \
  "v_mfma_f32_16x16x32_bf16 v[216:219], v[192:195], v[248:251], v[216:219]\n" \
  "v_cvt_pk_bf16_f32 v228, v208, v209\n" \
  "v_cvt_pk_bf16_f32 v229, v210, v211\n" \
  "v_mfma_f32_16x16x32_bf16 v[220:223], v[196:199], v[248:251], v[220:223]\n" \
  "ds_write_b128 v100, v[224:227] offset:6144\n" \
  "ds_write_b64 v101, v[228:229] offset:8192\n" \
  "s_nop 3\n" \
  "s_nop 3\n" \
  "s_nop 0\n" \
  "v_cvt_pk_bf16_f32 v230, v212, v213\n" \
  "v_cvt_pk_bf16_f32 v231, v214, v215\n" \
  "v_cvt_pk_bf16_f32 v232, v216, v217\n" \
  "v_cvt_pk_bf16_f32 v233, v218, v219\n" \
  "v_cvt_pk_bf16_f32 v234, v220, v221\n" \
  "v_cvt_pk_bf16_f32 v235, v222, v223\n" \
  "ds_write_b128 v100, v[230:233] offset:9216\n" \
  "ds_write_b64 v101, v[234:235] offset:11264\n"

__device__ __forceinline__ int fresh_lane() {
  int ln;
  asm volatile("v_mbcnt_lo_u32_b32 %0, -1, 0\n\tv_mbcnt_hi_u32_b32 %0, -1, %0" : "=v"(ln));
  __builtin_assume(ln >= 0 && ln < 64);
  return ln;
}
__device__ __forceinline__ unsigned now_ticks() { return (unsigned)__builtin_amdgcn_s_memrealtime(); }
template <int SLEEP>
__device__ __forceinline__ unsigned wait_ge(gu32* ctrl, long word, unsigned need) {
  for (int spin = 0;; ++spin) {
    unsigned x = __hip_atomic_load(ctrl + word, RLX_AGENT);
    if (__builtin_amdgcn_ballot_w64(x < need) == 0) {
#pragma unroll
      for (int o = 32; o; o >>= 1) x = min(x, (unsigned)__shfl_xor((int)x, o));
      return rfl(x);
    }
    if (spin > SPIN_CAP || ((spin & 1023) == 1023 && rfl(__hip_atomic_load(ctrl + C_ERR, RLX_AGENT)))) { __hip_atomic_store(ctrl + C_ERR, 1u, RLX_AGENT); return need; }
    __builtin_amdgcn_s_sleep(SLEEP);
  }
}
template <typename VT>
__device__ __forceinline__ void set_rsrc(VT& sr, int i, const void* ptr, unsigned bytes) {
  const unsigned long long a = uni64((unsigned long long)ptr);
  sr[4 * i] = (unsigned)a; sr[4 * i + 1] = (unsigned)(a >> 32) & 0xffffu; sr[4 * i + 2] = bytes; sr[4 * i + 3] = 0x00020000u;
}
__device__ __forceinline__ int so_near(unsigned long long par, int blk) { return (blk << 21) | ((int)(par >> blk & 1) << 13); }
__device__ __forceinline__ int so_far(unsigned long long par, int blk) { return so_near(par, blk) ^ 0x2000; }
__device__ __forceinline__ unsigned long long load_parmask(const Params& p) {
  const __attribute__((address_space(4))) unsigned* w = (const __attribute__((address_space(4))) unsigned*)uni64((unsigned long long)(p.ws + CTRL_OFF));
  unsigned long long m = 0;
#pragma unroll
  for (int i = 0; i < 16; ++i) m |= (unsigned long long)(((w[i] & 0x01010101u) * 0x10204080u) >> 28) << (4 * i);
  return m;
}
__device__ __forceinline__ long nword(unsigned long long par, int off) { return ((long)so_near(par, XB) + lmap(off) - (long)CTRL_OFF) / 4; }
__device__ __forceinline__ v4u ld16s(rsrc_t r, int vo, int so) { return __builtin_amdgcn_raw_buffer_load_b128(r, vo, so, 16); }
__device__ __forceinline__ v2u ld8s(rsrc_t r, int vo, int so) { return __builtin_amdgcn_raw_buffer_load_b64(r, vo, so, 16); }
#define TS(cond, i) if (cond) ctrl[C_STAT + 100 + (i)] = now_ticks();
#define OPAQUE(v) asm volatile("" : "=v"(v))
#define W240_IN(w) "{a[0:31]}"(w[0]), "{a[32:63]}"(w[1]), "{a[64:95]}"(w[2]), "{a[96:127]}"(w[3]), "{a[128:159]}"(w[4]), "{a[160:191]}"(w[5]), \
                   "{a[192:223]}"(w[6]), "{a[224:239]}"(w##7)
#define W240_SET(w, i, k, val) do { if ((i) < 224) w[(i) >> 5][((i) & 31) + (k)] = (val); else w##7[(i) - 224 + (k)] = (val); } while (0)

__device__ __forceinline__ unsigned wave_max_u32(unsigned m) {
  m = max(m, (unsigned)__builtin_amdgcn_update_dpp(0, (int)m, 0xb1, 0xf, 0xf, true)); m = max(m, (unsigned)__builtin_amdgcn_update_dpp(0, (int)m, 0x4e, 0xf, 0xf, true));
  m = max(m, (unsigned)__builtin_amdgcn_update_dpp(0, (int)m, 0x124, 0xf, 0xf, true)); m = max(m, ror8u(m));
  auto a = __builtin_amdgcn_permlane16_swap(m, m, false, false); m = max(a[0], a[1]);
  auto b = __builtin_amdgcn_permlane32_swap(m, m, false, false); return max(b[0], b[1]);
}
__device__ __forceinline__ void select_rows(gu32* ctrl, rsrc_t rT, int lane) {
  unsigned key[32];
  v4u rr[8];
#pragma unroll
  for (int i = 0; i < 8; ++i) rr[i] = ld16s(rT, lane * 16 + i * 1024, 2 * (int)HF_BYTES);
  __builtin_amdgcn_sched_barrier(0);
#pragma unroll
  for (int i = 0; i < 8; ++i) {
    const v4u r = rr[i];
#pragma unroll
    for (int k = 0; k < 4; ++k) {
      unsigned u = f2u(u2f(r[k]) + 0.f);
      u = (u & 0x80000000u) ? ~u : (u | 0x80000000u);
      key[4 * i + k] = (u & 0xffff0000u) | (unsigned)(S - 1 - (i * 256 + lane * 4 + k));
    }
  }
  unsigned mine = 0, bound = 0;
#pragma unroll 1
  for (int it = 0; it < TOPK; ++it) {
    unsigned m = 0;
#pragma unroll
    for (int i = 0; i < 32; ++i) m = max(m, key[i] - bound);
    bound += wave_max_u32(m);
    if ((lane & 7) == it) mine = S - (bound & 0xffffu);
  }
#pragma unroll
  for (int i = 0; i < 2; ++i) __hip_atomic_store(ctrl + C_IDX + ((lane >> 3) + 8 * i) * 32 + (lane & 7), mine, RLX_AGENT);
}

__device__ __forceinline__ void ffn_role(const Params& p, int role, int lane, int wave) {
  gu32* ctrl = (gu32*)(p.ws + CTRL_OFF);
  const int g = lane >> 4, n = lane & 15, q = (lane >> 2) & 3, pp = lane & 3;
  const bool up = role < 96, down = role >= 88;
  const int c = role - 88;
  v4u wg[5], wu[5], wd[12];
  {
    const rsrc_t rg = mkrsrc(p.wg, DM * FF * 2), ru = mkrsrc(p.wu, DM * FF * 2), rd = mkrsrc(p.wd, FF * DM * 2);
#pragma unroll
    for (int i = 0; i < 5; ++i) {
      const int vo = up ? ((wave * 160 + i * 32 + (lane >> 1)) * FF + role * 16) * 2 + (lane & 1) * 16 : OOB;
      dma16(rg, vo, 0, wave * 22528 + i * 1024); dma16(ru, vo, 0, wave * 22528 + 5120 + i * 1024);
    }
#pragma unroll
    for (int i = 0; i < 12; ++i) dma16(rd, down ? ((wave * 384 + i * 32 + (lane >> 1)) * DM + c * 16) * 2 + (lane & 1) * 16 : OOB, 0, wave * 22528 + 10240 + i * 1024);
    VMCNT(0);
    const unsigned a = wave * 22528 + (8 * g + q) * 32 + pp * 8;
#pragma unroll
    for (int i = 0; i < 22; ++i) {
      const v2u lo = trread(a + i * 1024), hi = trread(a + i * 1024 + 128);
      const v4u x = {lo[0], lo[1], hi[0], hi[1]};
      if (i < 5) wg[i] = x; else if (i < 10) wu[i - 5] = x; else wd[i - 10] = x;
    }
  }
  const rsrc_t rT = mkrsrc(p.ws + X1_OFF, (unsigned)(WS_END - X1_OFF));
  constexpr int SO_H2 = DM * 2, SO_R = 2 * (int)HF_BYTES, SO_ACT = (int)(ACT_OFF - X1_OFF), RED = 98304;
  if (wave == 0) {
    wait_ge<3>(ctrl, C_IDX + (role & 15) * 32 + (lane & 7), 1u);
    if (lane < 8) *(LAS unsigned*)(L_MISC + lane * 4) = __hip_atomic_load(ctrl + C_IDX + (role & 15) * 32 + lane, RLX_AGENT);
  }
  __syncthreads();
  const unsigned t0 = now_ticks();
  const int row = (*(LAS int*)(L_MISC + (n & 7) * 4) - 1) & (S - 1);
  v2u x1, h2, gb; unsigned rv;
  if (down && wave == 0) { gb = ld8s(mkrsrc(p.ws + MODB_OFF, 7680), (5 * DM + c * 16 + 4 * g) * 2, 0); x1 = ld8s(rT, (row * 2 * DM + c * 16 + 4 * g) * 2, 0); h2 = ld8s(rT, (row * 2 * DM + c * 16 + 4 * g) * 2, SO_H2); rv = __builtin_amdgcn_raw_buffer_load_b32(rT, row * 4, SO_R, 16); }
  if (up) {
    v4u xb[5];
#pragma unroll
    for (int i = 0; i < 5; ++i) xb[i] = ld16s(rT, (row * 2 * DM + wave * 160 + i * 32 + 8 * g) * 2, SO_H2);
    __builtin_amdgcn_sched_barrier(0);
    f4 ag = {}, au = {};
#pragma unroll
    for (int i = 0; i < 5; ++i) { ag = mfma32(wg[i], xb[i], ag); au = mfma32(wu[i], xb[i], au); }
    *(LAS f4*)(RED + wave * 2048 + lane * 16) = ag; *(LAS f4*)(RED + wave * 2048 + 1024 + lane * 16) = au;
    __syncthreads();
    if (wave == 0) {
#pragma unroll
      for (int w = 1; w < 4; ++w) { ag += *(LAS f4*)(RED + w * 2048 + lane * 16); au += *(LAS f4*)(RED + w * 2048 + 1024 + lane * 16); }
      float a[4];
#pragma unroll
      for (int r = 0; r < 4; ++r) { const float gs = rbf(ag[r]); a[r] = rbf(gs / (1.f + __expf(-gs))) * rbf(au[r]); }
      __builtin_amdgcn_raw_buffer_store_b64(v2u{unsent(pack2(a[0], a[1])), unsent(pack2(a[2], a[3]))}, rT, n < 8 ? role * 256 + (g >> 1) * 128 + n * 16 + (g & 1) * 8 : OOB, SO_ACT, 16);
    }
  }
  if (!down) return;
  while ((int)(now_ticks() - t0) < FFN_WAIT) __builtin_amdgcn_s_sleep(1);
  v4u ab[12];
  for (int spin = 0;; ++spin) {
    asm volatile("" ::: "memory");
    unsigned m = 0;
#pragma unroll
    for (int i = 0; i < 12; ++i) ab[i] = ld16s(rT, (wave * 12 + i) * 512 + g * 128 + (n & 7) * 16, SO_ACT);
    __builtin_amdgcn_sched_barrier(0);
#pragma unroll
    for (int i = 0; i < 12; ++i) m = max(max(m, max(ab[i][0], ab[i][1])), max(ab[i][2], ab[i][3]));
    if (__builtin_amdgcn_ballot_w64(m == ~0u) == 0) break;
    if (spin > SPIN_CAP) { __hip_atomic_store(ctrl + C_ERR, 1u, RLX_AGENT); break; }
    __builtin_amdgcn_s_sleep(4);
  }
  f4 y = {};
#pragma unroll
  for (int i = 0; i < 12; ++i) y = mfma32(wd[i], ab[i], y);
  *(LAS f4*)(RED + 8192 + wave * 1024 + lane * 16) = y;
  __syncthreads();
  if (wave == 0 && n < 8) {
#pragma unroll
    for (int w = 1; w < 4; ++w) y += *(LAS f4*)(RED + 8192 + w * 1024 + lane * 16);
    const float rs = rbf(1.f / (1.f + __expf(-u2f(rv))));
    float o[4];
#pragma unroll
    for (int r = 0; r < 4; ++r) {
      const float hv = (r & 1) ? hi2f(h2[r >> 1]) : lo2f(h2[r >> 1]), xv = (r & 1) ? hi2f(x1[r >> 1]) : lo2f(x1[r >> 1]);
      o[r] = xv + rbf(((r & 1) ? hi2f(gb[r >> 1]) : lo2f(gb[r >> 1])) * rbf(hv + rbf(rbf(y[r]) * rs)));
    }
    *(v2u*)(p.out + (size_t)row * DM + c * 16 + 4 * g) = v2u{pack2(o[0], o[1]), pack2(o[2], o[3])};
  }
}

__device__ __forceinline__ void ln1_fetch(const Params& p, int tile, int lane, int wave) {
  const rsrc_t rx = mkrsrc(p.x, S * DM * 2), rm = mkrsrc(p.ws + MODB_OFF, 2560);
#pragma unroll
  for (int k = 0; k < 4; ++k) dma16(rx, lane * 16, (tile * 16 + wave * 4 + k) * 1280, L_LN + (wave * 4 + k) * 1040);
  dma16(rx, (lane >> 4) * 1280 + 1024 + (lane & 15) * 16, (tile * 16 + wave * 4) * 1280, L_LN + 16640 + wave * 1024);
  dma16(rm, wave * 1024 + lane * 16, 0, L_LN + 20736 + wave * 1024);
}
__device__ __forceinline__ void ln1_rows(const Params& p, int tile, int lane, int wave) {
  const rsrc_t rh = mkrsrc(p.ws + HF_OFF, (unsigned)HF_BYTES);
  const int r = wave * 8 + (lane & 7);
  const unsigned xa = L_LN + r * 1040 + (lane >> 3) * 16, xt = L_LN + 16640 + r * 256 + (lane >> 3) * 16 - 1024;
  v4u a[10], sh[10], sc[10];
#pragma unroll
  for (int i = 0; i < 10; ++i) a[i] = lds128((i < 8 ? xa : xt) + i * 128);
#pragma unroll
  for (int i = 0; i < 10; ++i) { sh[i] = lds128(L_LN + 20736 + (lane >> 3) * 16 + i * 128); sc[i] = lds128(L_LN + 22016 + (lane >> 3) * 16 + i * 128); }
  __builtin_amdgcn_sched_barrier(0);
  float s = 0.f, q = 0.f;
  const bf2 one = {(__bf16)1.f, (__bf16)1.f};
#pragma unroll
  for (int i = 0; i < 10; ++i)
#pragma unroll
    for (int k = 0; k < 4; ++k) { const unsigned w = a[i][k]; s = __builtin_amdgcn_fdot2_f32_bf16(__builtin_bit_cast(bf2, w), one, s, false); }
  s += ror8f(s); s = swapsum16(s, s); s = swapsum32(s, s);
  const float mean = s * (1.f / DM);
#pragma unroll
  for (int i = 0; i < 10; ++i)
#pragma unroll
    for (int k = 0; k < 4; ++k) { const float d0 = lo2f(a[i][k]) - mean, d1 = hi2f(a[i][k]) - mean; q = fmaf(d0, d0, fmaf(d1, d1, q)); }
  q += ror8f(q); q = swapsum16(q, q); q = swapsum32(q, q);
  const float mb = rbf(mean), isd = __builtin_amdgcn_rcpf(rbf(__builtin_amdgcn_sqrtf(rbf(rbf(q * (1.f / DM)) + 1e-5f))));
#pragma unroll
  for (int i = 0; i < 10; ++i) {
    v4u o;
#pragma unroll
    for (int k = 0; k < 4; ++k) {
      const unsigned d = pack2(lo2f(a[i][k]) - mb, hi2f(a[i][k]) - mb), n = pack2(lo2f(d) * isd, hi2f(d) * isd);
      const unsigned m = pack2(lo2f(n) * lo2f(sc[i][k]), hi2f(n) * hi2f(sc[i][k]));
      o[k] = pack2(lo2f(m) + lo2f(sh[i][k]), hi2f(m) + hi2f(sh[i][k]));
    }
    __builtin_amdgcn_raw_buffer_store_b128(o, rh, tile * 20480 + (lane >> 3) * 256 + r * 16, i * 2048, 16);
  }
}
template <typename I, typename T, typename H>
__device__ __forceinline__ void stage4(I issue, T take, H hook) {
  issue(0, 0); hook(4); issue(1, 1);
  hook(0);
  VMCNT(15); BAR(); hook(5); take(0, 0); LGKM0(); BAR(); issue(2, 0);
  VMCNT(15); BAR(); hook(1); take(1, 1); LGKM0(); BAR(); issue(3, 1);
  hook(3);
  VMCNT(15); BAR(); hook(6); take(2, 0);
  hook(2);
  VMCNT(0); BAR(); take(3, 1); LGKM0(); BAR();
}

__device__ __forceinline__ void chain_role(const Params& p, int role, int lane, int wave, int tid) {
  gu32* ctrl = (gu32*)(p.ws + CTRL_OFF);
  const int G = role & 3, h = role >> 2;
  const int g = lane >> 4, n = lane & 15, q = (lane >> 2) & 3, pp = lane & 3;
  unsigned long long parmask;
  v32u vc; v8u vd;
  auto lane_consts = [&]() {
  parmask = load_parmask(p);
  OPAQUE(vc); OPAQUE(vd);
  vc[0] = n < 8 ? 0x4444 : 0xEEEE;
#pragma unroll
  for (int k = 0; k < 3; ++k)
    vc[1 + k] = wave == 2 ? lmap(M_OFF + (h % MCOPY) * MC + k * 4096 + lane * 16) : lane < 48 ? lmap(Q_OFF + (h * 4 + ((G + 1 + k) & 3)) * REC + 1536 + lane * 16) - 1024 * k : OOB;
  vc[4] = h * 65536 + wave * 32768 + lane * 16;
  vc[5] = h * 65536 + G * 16384 + wave * 2048 + lane * 16;
  vc[6] = lane * 16;
  vc[7] = L_BG + lane * 16;
  const int ktt = (wave - 1) & 1;
  vc[8] = L_BG + ktt * 4096 + 3072 + lane * 16;
  vc[9] = L_BG + 2 * 4096 + 3072 + lane * 16 + 8 * ktt;
  vc[10] = L_QK + (16 * wave + n) * 32 + g * 8;
  vc[11] = L_QK + (8 * g + q) * 32 + pp * 8;
  vc[12] = L_QK + (32 + 4 * g + q) * 32 + pp * 8;
  const int recoff = wave == 0 ? (g >= 2 && n < 8 ? (n >> 1) * 384 + 256 + (g - 2) * 32 + (n & 1) * 16 : -1)
                   : wave < 3 ? (n < 8 ? (n >> 1) * 384 + (wave - 1) * 128 + g * 32 + (n & 1) * 16 : -1)
                              : 1536 + (16 * (lane >> 5) + n) * 16 + (g & 1) * 8;
  const int recoff2 = 1536 + (32 + n) * 16 + (g & 1) * 8 + (lane >> 5) * 4;
  const int hrec = Q_OFF + h * 4 * REC;
  vc[13] = recoff >= 0 ? lmap(hrec + G * REC + recoff) : OOB;
  vc[14] = lmap(hrec + G * REC + recoff2);
  const int qi = 2 * wave + (n & 1), ch = n >> 1;
  vc[15] = lmap(hrec + g * REC + wave * 384 + n * 16);
  vc[16] = lmap(hrec + g * REC + wave * 384 + 256 + (ch >> 2) * 32 + (n & 1) * 16 + (ch & 3) * 4);
  vc[18] = L_P + qi * 64 + (ch & 3) * 16 + (ch >> 2) * 8 + g * 2;
  vc[19] = g == 0 ? L_P + 512 + qi * 16 + ch * 2 : L_DUMP + wave * 256 + lane * 4;
#pragma unroll
  for (int t = 0; t < 3; ++t)
#pragma unroll
    for (int u = 0; u < 2; ++u) {
      const int k = 16 * g + 8 * u + 4 * (q >> 1) + 2 * (pp >> 1) + (q & 1);
      const int f = 2 * t + (k >> 5), gk = (k >> 3) & 3, j = k & 7;
      const int dl = f < 4 ? 48 * f + 8 * gk + j : 48 * (2 * (f - 4) + (j >> 2)) + 32 + 4 * gk + (j & 3);
      vc[20 + 2 * t + u] = L_V + ((dl / 48 - G - 1) & 3) * 1024 + (dl % 48) * 16 + (pp & 1) * 8;
    }
  vc[26] = n < 8 ? lmap(P_OFF + ((20 * G + 4 * wave + g) * 32 + h) * 128 + n * 16) : OOB;
  vc[27] = n < 8 && g < 2 ? lmap(P_OFF + ((20 * G + 16 + wave) * 32 + h) * 128 + n * 16 + g * 8) : OOB;
  vc[28] = L_P + (n & 7) * 64 + g * 16;
  const int rt = (role & 31) < 20 ? (role >> 5) * 20 + (role & 31) : -1;
  vc[29] = rt >= 0 ? lmap(P_OFF + rt * 4096 + lane * 16) : OOB;
  vc[30] = rt >= 0 ? lmap(M_OFF + ((n >> 3) + 2 * (g >> 1)) * MC + rt * 128 + (n & 7) * 16 + (g & 1) * 8) : OOB;
  if (wave == 2) {
    vc[29] = so_near(parmask, XB) + lmap(N_REC);
    vc[30] = role == 0 && lane == 0 ? vc[29] : OOB;
  }
  if (wave == 3) vc[12] = L_V + 3072 + recoff2 - 1536;
  if (wave == 3)
#pragma unroll
    for (int k = 0; k < 4; ++k) vc[8 + k] = (n & 7) == 2 * k ? 0x3f80u : (n & 7) == 2 * k + 1 ? 0x3f800000u : 0u;
  const int kmo = h * 65536 + G * 16384 + 6144;
  vc[31] = wave == 0 ? (n >= 8 ? kmo + (g * 8 + n - 8) * 16 : OOB) : kmo + 768 + recoff - 1536;
  vd[0] = wave == 0 ? (n >= 8 ? kmo + 512 + (g * 8 + n - 8) * 8 : OOB) : kmo + 768 + recoff2 - 1536;
  vd[1] = g < 2 ? 0x3f803f80u : 0u;
  vd[2] = lane == 0 ? (role * 4 + wave) * 16 : OOB;
  vd[3] = L_V + 3072 + recoff - 1536;
  vd[4] = L_P + 512 + (n & 7) * 16 + (g & 1) * 8;
  vd[5] = so_near(parmask, XB) + lmap(N_PP) - 3072 + (lane & 3) * 128;
  vd[6] = L_BG + 16384 + 3 * 4096 + 3072 + lane * 16;
  vd[7] = tid == 0 ? so_near(parmask, XB) + lmap(N_CF) + h * 128 + G * 4 : OOB;
  asm volatile("" : "+v"(vc), "+v"(vd));
  };
  v32u wb[2]; v8u wb2;
  {
    const rsrc_t rv = mkrsrc(p.wv, DM * HD * 2), ro = mkrsrc(p.wo, DM * HD * 2);
    ln1_fetch(p, role, lane, wave);
    const unsigned rb = (16 * (g >> 1) + 8 * (q >> 1) + 4 * (g & 1) + (q & 1)) * 96 + 8 * pp;
    auto tile_at = [&](int t) { return rb + ((q & 1) ? (t == 2 ? 0 : 32 + 32 * t) : 32 * t); };
#define RDW(dst, base) asm volatile(FILL##dst##_ASM ::"v"(base) : "memory", FILL##dst##_CLOBBERS);
    const int tl = wave < 3 ? wave : 0;
    auto issue = [&](rsrc_t r, int slot, int i0, int cnt) {
#pragma unroll 1
      for (int i = i0; i < i0 + cnt; ++i) {
        const int c = i * 64 + lane, row = c / 6, part = (c % 6 + (row & 1) * 4) % 6;
        dma16(r, (row * HD + h * DH) * 2 + (part < 4 ? 64 * G + part * 16 : 192 + 32 * G + part * 16), 0, slot * 61440 + i * 1024);
      }
    };
    VMCNT(0); BAR();
    if (wave < 2) {
      ln1_rows(p, role, lane, wave);
      VMCNT(0);
      if (lane == 0) __hip_atomic_fetch_add(ctrl + C_LN + (role & 7) * 32, 1u, RLX_AGENT);
      TS(tid == 0, 25856 + role)
    } else issue(mkrsrc(wave == 2 ? p.wq : p.wk, DM * HD * 2), wave - 2, 0, 60);
    VMCNT(0); BAR();
    RDW(0, tile_at(tl))
    BAR();
    issue(rv, 0, wave * 15, 15);
    RDW(80, tile_at(tl) + 61440)
    BAR();
#pragma unroll 1
    for (int i = wave * 15; i < wave * 15 + 15; ++i) {
      const int c = i * 64 + lane;
      dma16(ro, ((h * DH + c / 20) * DM + 160 * G) * 2 + (c % 20) * 16, 0, 61440 + i * 1024);
    }
    if (tid == 0) __hip_atomic_fetch_add(ctrl + C_ANCH + 128, 1u, RLX_AGENT);
    lane_consts();
    VMCNT(0); BAR();
    if (wave == 3) { RDW(0, tile_at(0)) RDW(80, tile_at(1)) RDW(160, tile_at(2)) }
#pragma unroll
    for (int i = 0; i < 3; ++i)
#pragma unroll
      for (int t = 0; t < 3; ++t)
#pragma unroll
        for (int u = 0; u < 4; ++u) {
          const int f = 2 * t + (u >> 1), j = 4 * (u & 1) + q;
          const int dl = f < 4 ? 32 * f + 8 * g + j : 128 + 16 * (2 * (f - 4) + (u & 1)) + 4 * g + q;
          const v2u x = trread(61440 + dl * 320 + (i < 2 ? 32 * wave + 8 * pp + 4 * i : 128 + 8 * wave + 4 * (pp & 1)) * 2);
          const int r = (i * 3 + t) * 8 + 2 * u;
          if (r < 64) { wb[r >> 5][r & 31] = x[0]; wb[r >> 5][(r & 31) + 1] = x[1]; } else { wb2[r - 64] = x[0]; wb2[r - 63] = x[1]; }
        }
    LGKM0(); BAR();
  }
  if (role == 0 && tid == 0) ctrl[C_STAT + 10] = now_ticks();
  TS(tid == 0, 25600 + role)
  const rsrc_t rH = mkrsrc(p.ws, (unsigned)HAND_BYTES);
  const unsigned ck0 = now_ticks(), cy0 = (unsigned)__builtin_amdgcn_s_memtime();
  auto ratio = [&]() {
    const unsigned dt = now_ticks() - ck0, dc = (unsigned)__builtin_amdgcn_s_memtime() - cy0;
    return dt < 150u ? 5900u : min((unsigned)rfl((int)(RSC * (float)dc / (float)dt)), 5950u);
  };
  v16u sc;
  unsigned anchor = now_ticks();
#pragma unroll 1
  for (int pass = DRY ? 0 : 1; pass < 2; ++pass) {
  if (pass) {
  const int lane = fresh_lane();
  if (wave == 0) {
    if (role == 0) {
      wait_ge<4>(ctrl, C_ANCH + 128, 128u); TS(lane == 0, 6) wait_ge<2>(ctrl, nword(parmask, N_PP + (lane & 7) * 128), 24u);
      TS(lane == 0, 7)
      const unsigned lev0 = (unsigned)(64.5f - 70.0f + 70.0f * 23.0f * 260.f / (float)ratio());
      if (lane == 0) __hip_atomic_store(ctrl + nword(parmask, N_REC), lev0 << 8 | 1u, RLX_AGENT);
      if (lane < 4) __hip_atomic_store(ctrl + C_ANCH + lane * 32, (now_ticks() + p.lead) | 1u, RLX_AGENT);
    }
    const unsigned an = wait_ge<4>(ctrl, C_ANCH + (role >> 5) * 32, 1u);
    if (lane == 0) *(LAS unsigned*)L_MISC = an;
  }
  __syncthreads();
  anchor = rfl(*(LAS int*)L_MISC);
  if ((unsigned)(anchor - now_ticks() + 100000u) > 200000u) anchor = now_ticks();
#pragma unroll
  for (int j = 0; j < 2; ++j) {
    const int so = so_near(parmask, 2 * j + 1), dst = L_BG + j * 16384 + wave * 4096;
#pragma unroll
    for (int k = 0; k < 3; ++k) dma16_sc1(rH, h * 65536 + wave * 16384 + lane * 16, so + k * 1024, dst + k * 1024);
    if (wave < 3) dma16_sc1(rH, h * 65536 + G * 16384 + 3072 + wave * 1024 + lane * 16, so, dst + 3072);
  }
  VMCNT(0); BAR();
  if (wave == 1) { while ((int)(anchor - now_ticks()) > 70) __builtin_amdgcn_s_sleep(1); dma16_sc1(rH, vd[5], 3072 + 1024, L_BG + 16384 + 3 * 4096 + 3072); }
  }
  v8u sr;
  set_rsrc(sr, 0, p.ws, pass ? (unsigned)HAND_BYTES : 0u);
  sr[4] = so_near(parmask, XB); sr[5] = so_near(parmask, 1); sr[6] = so_near(parmask, 2); sr[7] = so_near(parmask, 3);
  sc[0] = (unsigned)parmask; sc[1] = (unsigned)(parmask >> 32); sc[2] = anchor; sc[3] = pass ? p.dQ : 0; sc[4] = pass ? p.dP : 0; sc[5] = pass ? p.dM : 0;
  sc[6] = wave; sc[7] = L_MEM + wave * 3072; sc[8] = L_BG + wave * 8192; sc[9] = rfl(ratio());
  sc[10] = 0; sc[11] = pass ? 0 : 62;
  sc[12] = 0; sc[13] = 0; sc[14] = 0; sc[15] = pass ? p.dS : 4000;
#pragma unroll
  for (int i = 0; i < 16; ++i) sc[i] = rfl(sc[i]);
#pragma unroll
  for (int i = 0; i < 8; ++i) sr[i] = rfl(sr[i]);
  if ((role & 31) == 0 && tid == 0) { ctrl[C_STAT + 80 + (role >> 5) * 4] = now_ticks(); ctrl[C_STAT + 81 + (role >> 5) * 4] = (unsigned)__builtin_amdgcn_s_memtime(); }
  asm volatile(CHAIN_ASM
               : "+{s[52:67]}"(sc), "+{v[0:31]}"(vc), "+{v[32:39]}"(vd), "+{s[36:43]}"(sr)
               : "{v[184:215]}"(wb[0]), "{v[216:247]}"(wb[1]), "{v[248:255]}"(wb2)
               : "memory", "vcc", "scc", CHAIN_CLOBBERS);
  }
  lane = fresh_lane();
  if (lane == 0) {
    if (sc[15] > 20000u) __hip_atomic_store(ctrl + C_ERR, 1u, RLX_AGENT);
    for (int i = 0; i < 3; ++i) ctrl[24576 + (role * 4 + wave) * 4 + i] = sc[12 + i];
    if (role == 5 && wave == 2) __hip_atomic_store(ctrl + C_STAT + 30, sc[11], RLX_AGENT);
    if ((role & 31) == 0 && wave == 0) { ctrl[C_STAT + 82 + (role >> 5) * 4] = now_ticks(); ctrl[C_STAT + 83 + (role >> 5) * 4] = (unsigned)__builtin_amdgcn_s_memtime(); }
    if (role == 0 && wave == 0) { ctrl[C_STAT + 4] = anchor; __hip_atomic_store(ctrl + C_STAT + 5, now_ticks(), RLX_AGENT); }
  }
}

__device__ __forceinline__ void attn_tile(unsigned LQ, unsigned LK, int qt, int lane) {
  const int g = lane >> 4, n = lane & 15, q = (lane >> 2) & 3, pp = lane & 3;
  v16u a; v12u init;
  OPAQUE(a);
  a[0] = LQ + qt * 1024 + lane * 16; a[1] = LQ + lane * 16 + qt * 8; a[2] = LK + lane * 16;
  a[3] = LK + 6144 + (g * 8 + (n & 7)) * 16; a[4] = LK + 6656 + (g * 8 + (n & 7)) * 8; a[5] = LK + (pp * 16 + ((4 * g + q + 8 * (pp & 1)) & 15)) * 16;
#pragma unroll
  for (int c = 0; c < 3; ++c) a[6 + c] = LK + 7168 + (c < 2 ? 8 * (n >> 2) + 4 * c + (n & 3) : 32 + n) * 16 + (g & 1) * 8;
  const unsigned neg = f2u(-1e30f);
#pragma unroll
  for (int r = 0; r < 4; ++r) {
    init[r] = g >= 2 ? 0u : neg; init[4 + r] = 4 * g + r <= n + 16 * qt ? 0u : neg; init[8 + r] = 4 * g + r + 16 <= n + 16 * qt ? 0u : neg;
  }
  asm volatile(ATTN_ASM : "+{v[100:115]}"(a) : "{v[116:127]}"(init) : "memory", ATTN_CLOBBERS);
}

template <bool MAIN>
__device__ __forceinline__ void head_role(const Params& p, int h, int first, int lane, int wave, int tid) {
  gu32* ctrl = (gu32*)(p.ws + CTRL_OFF);
  const int g = lane >> 4, q = (lane >> 2) & 3, pp = lane & 3;
  v32u wa[7]; v16u wa7;
  {
    if (MAIN) { __builtin_amdgcn_s_sleep(127); __builtin_amdgcn_s_sleep(127); }
    const rsrc_t ro = mkrsrc(p.wo, DM * HD * 2);
    stage4(
      [&](int m, int slot) {
#pragma unroll 1
        for (int i = 0; i < 15; ++i) {
          const int c = wave * 15 + i;
          dma16(ro, lane * 16, (h * DH + (c < 40 ? 32 * m : 96 + 16 * m)) * DM * 2 + c * 1024, slot * 61440 + c * 1024);
        }
      },
      [&](int m, int slot) {
#pragma unroll
        for (int c = 0; c < 10; ++c) {
          const unsigned col = slot * 61440 + (160 * wave + 16 * c + 4 * pp) * 2;
          const v2u lo = trread((8 * g + q) * 1280 + col), hi = trread((8 * g + 4 + q) * 1280 + col), tl = trread((32 + 4 * g + q) * 1280 + col);
          const int r = (c * 6 + m) * 4, r2 = (c * 6 + 4 + (m >> 1)) * 4 + 2 * (m & 1);
          W240_SET(wa, r, 0, lo[0]); W240_SET(wa, r, 1, lo[1]); W240_SET(wa, r, 2, hi[0]); W240_SET(wa, r, 3, hi[1]);
          W240_SET(wa, r2, 0, tl[0]); W240_SET(wa, r2, 1, tl[1]);
        }
      },
      [](int) {});
  }
  const unsigned long long parmask = load_parmask(p);
  const rsrc_t rH = mkrsrc(p.ws, (unsigned)HAND_BYTES);
  unsigned tw = 0, ta = 0, to = 0, td = 0, nbat = 0, tt0 = now_ticks(), tt1;
#define HT(x) tt1 = now_ticks(); x += tt1 - tt0; tt0 = tt1;
  int s = first;
  int pw = first;
  int sig = first;
  while (pw < (MAIN ? HS : NSEG)) {
    const int lane = fresh_lane(), tid = wave * 64 + lane, g = lane >> 4, q = (lane >> 2) & 3, pp = lane & 3;
    const long word = nword(parmask, N_CF + h * 128 + (lane & 3) * 4);
    const int ub = (h & 15) * 81920;
    const int vst = ub + (g >> 1) * 16384 + (g & 1) * 4096 + wave * 256 + (lane & 15) * 16, vst2 = ub + 65536 + g * 1024 + wave * 64 + (lane & 15) * 4;
    constexpr int LK = 73728, LH = 139264;
    const int vk = ub + wave * 16384 + lane * 16, vq = vk + 6144, vq2 = ub + 65536 + wave * 1024 + lane * 16;
    const int vv = ub + wave * 16384 + (lane ^ ((lane >> 1) & 8)) * 16;
    const int vm = lane < 48 ? h * 65536 + wave * 16384 + 3072 + lane * 16 : OOB;
    auto qo = [](int seg) { return ((seg >> 1) % 3) * 24576 + (seg & 1) * 12288; };
    auto sou = [&](int seg) { return so_far(parmask, 2 * (seg % RING) + (h >> 4)); };
    auto fetch_qkv = [&](int seg) {
      const int sq = sou(seg);
#pragma unroll
      for (int i = 0; i < 3; ++i) dma16_sc1(rH, i < 2 ? vq + i * 1024 : vq2, sq, qo(seg) + wave * 3072 + i * 1024);
#pragma unroll
      for (int i = 0; i < 6; ++i) dma16_sc1(rH, i < 3 ? vv : vk, sq + i * 1024, LK + (seg & 1) * 32768 + wave * 8192 + i * 1024);
    };
    auto fetch_mem = [&](int seg) {
      const int sk = so_near(parmask, 2 * (seg % RING) + 1);
#pragma unroll
      for (int i = 0; i < 2; ++i) dma16_sc1(rH, vm, sk + 3072 + i * 768, LK + (seg & 1) * 32768 + wave * 8192 + 6144 + i * 1024);
    };
    if (!MAIN) {
      if (s == first) { if (wave == 0) wait_ge<8>(ctrl, word, s - 1); BAR(); fetch_qkv(s); }
      if (wave == 0) wait_ge<2>(ctrl, word, s + 1);
      BAR();
      fetch_mem(s);
      VMCNT(0); BAR();
      if (wave < 2) attn_tile(qo(s), LK + (s & 1) * 32768, wave, lane);
      LGKM0(); BAR();
      if (s + 2 < NSEG) fetch_qkv(s + 2);
    }
    const bool fused = MAIN && s > 0 && s < HS;
    if (!MAIN || (!fused && pw < s)) {
      v16u sr; v8u vr;
      OPAQUE(vr);
      set_rsrc(sr, 0, p.ws, (unsigned)HAND_BYTES);
      sr[4] = rfl(sou(pw));
#pragma unroll
      for (int j = 5; j < 16; ++j) sr[j] = 0;
      vr[0] = qo(pw) + lane * 16; vr[1] = vst; vr[2] = vst2;
      asm volatile(WO1_ASM : "+{s[36:51]}"(sr), "+{v[100:107]}"(vr) : W240_IN(wa) : "memory", WO1_CLOBBERS);
      if (!MAIN) {
        VMCNT(0); BAR();
        if (wave == 0 && fresh_lane() == 0) __hip_atomic_fetch_add(ctrl + C_AF + pw * 32, 1u, RLX_AGENT);
        s += 2; pw = s;
        continue;
      }
      ++pw;
      HT(to)
      continue;
    }
    VMCNT(0); BAR();
    if (tid < pw - sig) __hip_atomic_fetch_add(ctrl + C_AF + (sig + tid) * 32, 1u, RLX_AGENT);
    sig = pw;
    const int slot = wave >> 1, qt = wave & 1, n = lane & 15;
    if (wave == 0) wait_ge<HPOLL>(ctrl, word, s + 2);
    BAR();
    if (s == 0) { fetch_qkv(0); fetch_qkv(1); }
    fetch_mem(s); fetch_mem(s + 1);
    HT(tw)
    if (fused) {
      v32u a; v12u init; v16u sr;
      OPAQUE(a);
      const unsigned LQ = qo(s + slot), LKs = LK + slot * 32768;
      a[0] = qo(pw) + lane * 16; a[1] = LQ + qt * 1024 + lane * 16; a[2] = LQ + lane * 16 + qt * 8; a[3] = LKs + lane * 16;
      a[4] = LKs + 6144 + (g * 8 + (n & 7)) * 16; a[5] = LKs + 6656 + (g * 8 + (n & 7)) * 8; a[6] = LKs + (pp * 16 + ((4 * g + q + 8 * (pp & 1)) & 15)) * 16;
#pragma unroll
      for (int c = 0; c < 3; ++c) a[7 + c] = LKs + 7168 + (c < 2 ? 8 * (n >> 2) + 4 * c + (n & 3) : 32 + n) * 16 + (g & 1) * 8;
      a[10] = vst; a[11] = vst2; a[12] = vq; a[13] = vq2 - 2048; a[14] = vv; a[15] = vk; a[16] = vm;
      a[17] = wave == 0 ? so_near(parmask, XB) + lmap(N_CF) + h * 128 + pp * 4 : OOB;
      a[18] = tid < 2 ? (int)CTRL_OFF + (C_AF - 4 * 32 + tid * 32) * 4 : OOB;
      a[19] = LH + 16 + wave * 16; a[20] = LH + 16;
      const unsigned neg = f2u(-1e30f);
#pragma unroll
      for (int r = 0; r < 4; ++r) { init[r] = g >= 2 ? 0u : neg; init[4 + r] = 4 * g + r <= n + 16 * qt ? 0u : neg; init[8 + r] = 4 * g + r + 16 <= n + 16 * qt ? 0u : neg; }
      set_rsrc(sr, 0, p.ws, (unsigned)HAND_BYTES + CTRL_BYTES);
      sr[4] = (unsigned)parmask; sr[5] = (unsigned)(parmask >> 32); sr[6] = s; sr[7] = h >> 4; sr[8] = qo(s + 2) + wave * 3072; sr[9] = LK + wave * 8192;
      sr[10] = sou(pw); sr[11] = sou(pw + 1); sr[12] = sou(s); sr[13] = sou(s + 1); sr[14] = 2 * ((s + 2) % RING); sr[15] = wave * 3072;
#pragma unroll
      for (int j = 4; j < 16; ++j) sr[j] = rfl(sr[j]);
      asm volatile(HEAD_ASM : "+{s[36:51]}"(sr), "+{v[0:31]}"(a) : "{v[32:43]}"(init), W240_IN(wa) : "memory", HEAD_CLOBBERS);
      nbat += (sr[6] - s) / 2 + 1000;
      s = sr[6]; pw = s - 2; sig = s - 4;
      LGKM0(); BAR();
      HT(td)
      continue;
    }
    VMCNT(0); BAR();
    attn_tile(qo(slot), LK + slot * 32768, qt, lane);
    LGKM0(); BAR();
    fetch_qkv(2); fetch_qkv(3);
    s = 2;
    HT(ta)
  }
  if (!MAIN) return;
  VMCNT(0); BAR();
  tid = wave * 64 + fresh_lane();
  if (tid < HS - sig) __hip_atomic_fetch_add(ctrl + C_AF + (sig + tid) * 32, 1u, RLX_AGENT);
  if (h == 0 && tid == 0) { ctrl[C_STAT + 11] = tw; ctrl[C_STAT + 12] = ta; ctrl[C_STAT + 13] = to; ctrl[C_STAT + 14] = td; ctrl[C_STAT + 15] = nbat; ctrl[C_STAT + 7] = now_ticks(); }
}

__device__ __forceinline__ void proj_role(const Params& p, int unit, int lane, int wave, int tid) {
  gu32* ctrl = (gu32*)(p.ws + CTRL_OFF);
  const int type = unit >> 5, head = unit & 31;
  const unsigned long long parmask = load_parmask(p);
  const int g = lane >> 4, q = (lane >> 2) & 3, pp = lane & 3;
  v32u wa[7]; v16u wa7;
  {
    const rsrc_t rw = mkrsrc(type == 0 ? p.wq : type == 1 ? p.wk : p.wv, DM * HD * 2);
    stage4(
      [&](int m, int slot) {
#pragma unroll 1
        for (int i = 0; i < 15; ++i) {
          const int c = (wave * 15 + i) * 64 + lane;
          dma16(rw, ((m * 160 + c / 24) * HD + head * DH) * 2 + (c % 24) * 16, 0, slot * 61440 + (wave * 15 + i) * 1024);
        }
      },
      [&](int m, int slot) {
#pragma unroll
        for (int ks = 0; ks < 5; ++ks)
#pragma unroll
          for (int t = 0; t < 3; ++t) {
            const unsigned a = slot * 61440 + (32 * ks + 8 * g + q) * 384 + (t < 2 ? 32 * wave + 8 * pp + 4 * t : 128 + 16 * wave + 4 * pp) * 2;
            const v2u lo = trread(a), hi = trread(a + 4 * 384);
            const int r = (t * 20 + m * 5 + ks) * 4;
            W240_SET(wa, r, 0, lo[0]); W240_SET(wa, r, 1, lo[1]); W240_SET(wa, r, 2, hi[0]); W240_SET(wa, r, 3, hi[1]);
          }
      },
      [&](int at) { TS(unit == 40 && tid == 0, 20 + at) if (at == 2 && wave == 0) { wait_ge<4>(ctrl, C_LN + (lane & 7) * 32, 32); __builtin_amdgcn_fence(__ATOMIC_ACQUIRE, "agent"); } });
  }
  TS(tid == 0, 25600 + 160 + unit)
  {
    const rsrc_t rh = mkrsrc(p.ws + HF_OFF, (unsigned)HF_BYTES);
#pragma unroll
    for (int k = 0; k < 20; ++k) dma16(rh, wave * 10240 + lane * 16, (k / 10) * 40960 + (k % 10) * 1024, (k / 10) * 40960 + wave * 10240 + (k % 10) * 1024);
  }
  if (unit == 0 && tid == 0) ctrl[C_STAT + 9] = now_ticks();
  v12u vc; v8u sr;
  vc[0] = lane * 16; vc[1] = wave * 10240 + lane * 16;
#pragma unroll
  for (int k = 0; k < 4; ++k) {
    const int pc = type == 0 ? (unit * 4 + wave) * 4 + k : 384 + unit * 4 + wave;
    vc[2 + k] = pc < BLK_END / 1024 ? lmap(pc * 1024 + lane * 16) : OOB;
  }
  const int ub = (head & 15) * 81920 + lane * 16, tof = type == 0 ? 6144 : type == 1 ? 3072 : 0;
  if (type) vc[3] = head * 65536 + wave * 16384 + tof + lane * 16;
  vc[6] = ub + wave * 16384 + tof;
  vc[7] = type == 0 ? ub + 65536 + wave * 1024 : vc[6] + 2048;
  vc[8] = tid == 0 ? so_near(parmask, XB) + lmap(N_PP) + (unit & 3) * 128 : OOB;
  vc[9] = 0; vc[10] = 0; vc[11] = 0;
  set_rsrc(sr, 0, p.ws + HF_OFF, (unsigned)HF_BYTES); set_rsrc(sr, 1, p.ws, (unsigned)HAND_BYTES);
  const rsrc_t rH = mkrsrc(p.ws, (unsigned)HAND_BYTES), rx = mkrsrc(p.x, S * DM * 2);
  const rsrc_t rT = mkrsrc(p.ws + X1_OFF, (unsigned)(WS_END - X1_OFF)), rO = mkrsrc(p.out, S * DM * 2);
  unsigned pr[5][5] = {};
  constexpr int NCH = (NSEG + CHUNK - 1) / CHUNK, LAG = 4;
  bool last = false;
  constexpr int ROW0 = (NCH - LAG - 1) * CHUNK * 32, NTAIL = (S - ROW0 + 127) / 128;
  const int nround = type == 0 ? NCH + NTAIL : NCH;
  auto row_of = [&](int k) {
    return k < LAG || k == NCH - 1 ? S : k < NCH ? (((unit ^ k) & 1) ? S : (k - LAG) * CHUNK * 32 + (unit >> 1) + 48 * wave) : ROW0 + (k - NCH) * 128 + 32 * wave + unit;
  };
#define RT(i) if (unit == 0 && tid == 0 && k == RTK) ctrl[C_STAT + 20 + i] = now_ticks();
  v4u xa = {}; unsigned xb = 0;
  for (int k = 0; k < nround; ++k) {
    if (k == LAG - 1) {
      const rsrc_t rm = mkrsrc(p.ws + MODB_OFF + 2560, 5120), rr = mkrsrc(p.wr, 1280);
#pragma unroll
      for (int a = 0; a < 5; ++a) {
        const v4u x = __builtin_amdgcn_raw_buffer_load_b128(a < 4 ? rm : rr, lane * 20, a < 4 ? a * 1280 : 0, 16);
        pr[a][0] = x[0]; pr[a][1] = x[1]; pr[a][2] = x[2]; pr[a][3] = x[3];
        pr[a][4] = __builtin_amdgcn_raw_buffer_load_b32(a < 4 ? rm : rr, lane * 20 + 16, a < 4 ? a * 1280 : 0, 16);
      }
    }
    const int row = row_of(k);
    if (row < S) {
      const int seg = row >> 5, r = row & 31, n = lane & 15;
      LAS float* tb = (LAS float*)(L_LN + wave * 3840);
      RT(0)
      if (seg < NSEG - 2) wait_ge<RPOLL>(ctrl, C_AF + seg * 32, NH); else wait_ge<6>(ctrl, C_AF + seg * 32, NH);
      RT(1)
      v4u lo, hi;
#pragma unroll
      for (int c = 0; c < 4; ++c) { const unsigned x = (n & 7) == 2 * c ? 0x3f80u : (n & 7) == 2 * c + 1 ? 0x3f800000u : 0u; lo[c] = n < 8 ? x : 0u; hi[c] = n < 8 ? 0u : x; }
      f4 acc[3] = {};
      v4u t[40];
#pragma unroll
      for (int i = 0; i < 40; ++i)
        t[i] = ld16s(rH, g * 81920 + n * 16 + (i % 5 < 4 ? (r >> 3) * 16384 + (r & 7) * 1024 + (i % 5) * 256 : 65536 + r * 256),
                     so_far(parmask, 2 * (seg % RING) + i / 20) + ((i / 5) & 3) * 4 * 81920);
      __builtin_amdgcn_sched_barrier(0);
#pragma unroll
      for (int i = 0; i < 40; ++i) acc[(i % 5) >> 1] = mfma32((i % 5) & 1 ? hi : lo, t[i], acc[(i % 5) >> 1]);
#pragma unroll
      for (int c = 0; c < 4; ++c) {
        tb[160 * (g >> 1) + 16 * (4 * (g & 1) + c) + n] = acc[0][c]; tb[320 + 160 * (g >> 1) + 16 * (4 * (g & 1) + c) + n] = acc[1][c];
        tb[g < 2 ? 160 * q + 128 + 16 * (c & 1) + 4 * pp + 2 * g + (c >> 1) : 640 + lane] = acc[2][c];
      }
      RT(2)
      VMCNT(0);
      if (lane == 0) __hip_atomic_fetch_add(ctrl + C_RD + seg * 32, 1u, RLX_AGENT);
      typedef float f2 __attribute__((ext_vector_type(2)));
      float x1[10], sum = 0.f;
#pragma unroll
      for (int j = 0; j < 5; ++j) {
        const f2 y = *(LAS f2*)(tb + lane * 10 + 2 * j);
        const unsigned xw = j < 4 ? xa[j] : xb, yb = pack2(y[0], y[1]), ga = pack2(lo2f(pr[0][j]) * lo2f(yb), hi2f(pr[0][j]) * hi2f(yb));
        const unsigned xs = pack2(lo2f(xw) + lo2f(ga), hi2f(xw) + hi2f(ga));
        x1[2 * j] = lo2f(xs); x1[2 * j + 1] = hi2f(xs);
        sum += x1[2 * j] + x1[2 * j + 1];
      }
      const float mean = wave_sum(sum) * (1.f / DM);
      float sq = 0.f;
#pragma unroll
      for (int j = 0; j < 10; ++j) sq = fmaf(x1[j] - mean, x1[j] - mean, sq);
      const float mb = rbf(mean), isd = __builtin_amdgcn_rcpf(rbf(__builtin_amdgcn_sqrtf(rbf(rbf(wave_sum(sq) * (1.f / DM)) + 1e-5f))));
      float rs = 0.f;
      LAS unsigned* tw = (LAS unsigned*)tb;
#pragma unroll
      for (int j = 0; j < 5; ++j) {
        const unsigned d = pack2(x1[2 * j] - mb, x1[2 * j + 1] - mb), nn = pack2(lo2f(d) * isd, hi2f(d) * isd);
        const unsigned m = pack2(lo2f(nn) * lo2f(pr[2][j]), hi2f(nn) * hi2f(pr[2][j])), oh = pack2(lo2f(m) + lo2f(pr[1][j]), hi2f(m) + hi2f(pr[1][j]));
        const float h0 = lo2f(oh), h1 = hi2f(oh);
        rs = fmaf(h0, lo2f(pr[4][j]), fmaf(h1, hi2f(pr[4][j]), rs));
        const unsigned gh = pack2(lo2f(pr[3][j]) * h0, hi2f(pr[3][j]) * h1);
        tw[lane * 5 + j] = pack2(x1[2 * j], x1[2 * j + 1]); tw[320 + lane * 5 + j] = oh; tw[640 + lane * 5 + j] = pack2(x1[2 * j] + lo2f(gh), x1[2 * j + 1] + hi2f(gh));
      }
      rs = wave_sum(rs);
      __builtin_amdgcn_raw_buffer_store_b32(f2u(rbf(rs)), rT, lane == 0 ? row * 4 : OOB, 2 * (int)HF_BYTES, 16);
#pragma unroll
      for (int j = 0; j < 3; ++j) __builtin_amdgcn_raw_buffer_store_b128(*(LAS v4u*)(tw + j * 256 + lane * 4), rT, j < 2 || lane < 32 ? row * 2560 + j * 1024 + lane * 16 : OOB, 0, 16);
#pragma unroll
      for (int j = 0; j < 2; ++j) __builtin_amdgcn_raw_buffer_store_b128(*(LAS v4u*)(tw + 640 + j * 256 + (j ? n : lane) * 4), rO, j < 1 || lane < 16 ? row * 1280 + j * 1024 + lane * 16 : OOB, 0, 16);
      RT(3)
      if (row + 128 >= S) {
        VMCNT(0);
        unsigned t = 0;
        if (lane == 0) t = __hip_atomic_fetch_add(ctrl + C_FIN, 1u, RLX_AGENT);
        last = rfl(t) == 64 + 127;
      }
      RT(4)
    }
    if (wave == 3 && k > LAG && k < NCH)
      wait_ge<RPOLL>(ctrl, lane % 7 < CHUNK ? C_RD + ((k - LAG - 1) * CHUNK + lane % 7) * 32 : C_AF + (k - LAG) * CHUNK * 32, 32u);
    {
      const int vo = row_of(k + 1) * 1280 + lane * 20;
      xa = __builtin_amdgcn_raw_buffer_load_b128(rx, vo, 0, 0); xb = __builtin_amdgcn_raw_buffer_load_b32(rx, vo + 16, 0, 0);
    }
    if (k < NCH) {
      v8u sc;
      sc[0] = (unsigned)parmask; sc[1] = (unsigned)(parmask >> 32); sc[2] = k * CHUNK; sc[3] = wave * 10240; sc[4] = 2 * CHUNK * (k % (RING / CHUNK)); sc[5] = head >> 4; sc[6] = 0; sc[7] = 0;
#pragma unroll
      for (int i = 0; i < 8; ++i) sc[i] = rfl(sc[i]);
      const unsigned pt0 = now_ticks(); const unsigned long long pc0 = __builtin_amdgcn_s_memtime();
      v12u vt = vc;
      if (type) asm volatile(PROJKV_ASM : "+{s[52:59]}"(sc), "+{v[0:11]}"(vt) : "{s[36:43]}"(sr), W240_IN(wa) : "memory", "vcc", "scc", PROJKV_CLOBBERS);
      else asm volatile(PROJQ_ASM : "+{s[52:59]}"(sc), "+{v[0:11]}"(vt) : "{s[36:43]}"(sr), W240_IN(wa) : "memory", "vcc", "scc", PROJQ_CLOBBERS);
      if (unit == 0 && tid == 0) ctrl[C_STAT + 6] = now_ticks();
      if (unit == 0 && tid == 0 && k == 2) ctrl[C_STAT + 16] = now_ticks();
      if (unit == 0 && tid == 0) { ctrl[C_STAT + 32 + 2 * k] = now_ticks() - pt0; ctrl[C_STAT + 33 + 2 * k] = (unsigned)(__builtin_amdgcn_s_memtime() - pc0); ctrl[C_STAT + 64 + k] = pt0; }
    }
  }
  if (last && lane == 0) ctrl[C_STAT + 17] = now_ticks();
  if (last) select_rows(ctrl, rT, lane);
  if (last && lane == 0) ctrl[C_STAT + 18] = now_ticks();
  if (type && tid == 0) __hip_atomic_fetch_add(ctrl + C_FIN, 1u, RLX_AGENT);
}

__global__ void __launch_bounds__(256, 1) k_main(Params p) {
  extern __shared__ __attribute__((aligned(16))) unsigned char smem[];
  const int tid = threadIdx.x, lane = tid & 63, wave = rfl(tid >> 6);
  gu32* ctrl = (gu32*)(p.ws + CTRL_OFF);
  if (tid == 0) {
    const unsigned x = xcc_id();
    int role = -1;
    if (x < 4) { unsigned t = __hip_atomic_fetch_add(ctrl + C_TICK + x * 64, 1u, RLX_AGENT); if (t < 32) role = x * 32 + t; }
    if (role < 0) { unsigned t = __hip_atomic_fetch_add(ctrl + C_TICK + 4 * 64, 1u, RLX_AGENT); if (t < 128) role = 128 + t; }
    for (int gg = 0; gg < 4 && role < 0; ++gg) { unsigned t = __hip_atomic_fetch_add(ctrl + C_TICK + gg * 64, 1u, RLX_AGENT); if (t < 32) role = gg * 32 + t; }
    *(int*)(smem + L_MISC) = role;
    if (role == 0) ctrl[C_STAT + 3] = now_ticks();
    ctrl[C_STAT + 100 + 26112 + role] = now_ticks();
  }
  __syncthreads();
  const int role = rfl(*(int*)(smem + L_MISC));
  __syncthreads();
  if (role < 0) return;
  if (role < 128) {
    { const int l = fresh_lane(); chain_role(p, role, l, wave, wave * 64 + l); }
    const __attribute__((address_space(4))) Params* pk = (const __attribute__((address_space(4))) Params*)__builtin_amdgcn_kernarg_segment_ptr();
    asm volatile("" : "+s"(pk) :: "memory");
    const int ln = fresh_lane();
    Params p2 = {};
    p2.wg = pk->wg; p2.wu = pk->wu; p2.wd = pk->wd; p2.out = pk->out; p2.ws = pk->ws;
    ffn_role(p2, role, ln, wave);
  }
  else if (role < 160) { const int l = fresh_lane(); head_role<true>(p, role - 128, 0, l, wave, wave * 64 + l); }
  else {
    { const int l = fresh_lane(); proj_role(p, role - 160, l, wave, wave * 64 + l); }
    if (role >= 192) {
      const int l = fresh_lane(); head_role<false>(p, role & 31, HS + (role >= 224), l, wave, wave * 64 + l);
      if (role == 224 && wave == 0 && fresh_lane() == 0) ctrl[C_STAT + 19] = now_ticks();
    }
  }
#define LD(i) __hip_atomic_load(ctrl + C_STAT + (i), RLX_AGENT)
#define LDA(i) __hip_atomic_load(ctrl + (i), RLX_AGENT)
  if (role == 0 && wave == 0) {
    asm volatile("s_waitcnt vmcnt(0)" ::: "memory");
    const unsigned t0 = rfl(LD(3));
    const unsigned tn = now_ticks();
    while ((int)(now_ticks() - tn) < 1000) __builtin_amdgcn_s_sleep(2);
    unsigned x = rfl(LDA(24576 + 2 * 4 + 1) * 500u);
    x = min(x, 30000u);
    while ((int)(now_ticks() - t0) < (int)(30000u + x)) __builtin_amdgcn_s_sleep(2);
  }
}

#define SCHED_DQ 73
#define SCHED_DP 77
#define SCHED_DM 44
#define SCHED_DS 80
#define SCHED_LEAD 220

extern "C" void kernel_launch(void* const* d_in, const int* in_sizes, int n_in, void* d_out, int out_size, void* d_ws, size_t ws_size,
                              hipStream_t stream) {
  Params p;
  p.x = (const u16*)d_in[0]; p.mem0 = (const u16*)d_in[1]; p.wq = (const u16*)d_in[2]; p.wk = (const u16*)d_in[3];
  p.wv = (const u16*)d_in[4]; p.wo = (const u16*)d_in[5]; p.wr = (const u16*)d_in[8]; p.wg = (const u16*)d_in[9];
  p.wu = (const u16*)d_in[10]; p.wd = (const u16*)d_in[11]; p.c = (const u16*)d_in[12]; p.wmod = (const u16*)d_in[13];
  p.out = (u16*)d_out;
  p.ws = (unsigned char*)(((uintptr_t)d_ws + 2 * MB - 1) & ~(uintptr_t)(2 * MB - 1));
  p.dQ = SCHED_DQ; p.dP = SCHED_DP; p.dM = SCHED_DM; p.dS = SCHED_DS; p.lead = SCHED_LEAD;
  p.pad = 0;
  (void)hipFuncSetAttribute((const void*)k_main, hipFuncAttributeMaxDynamicSharedMemorySize, MAIN_LDS);
  k_pre<<<200, 256, 0, stream>>>(p);
  k_main<<<256, 256, MAIN_LDS, stream>>>(p);
}
```

```cpp
#include <hip/hip_runtime.h>
#include <hip/hip_bf16.h>
#include <stdint.h>

#define GAS __attribute__((address_space(1)))
#define LAS __attribute__((address_space(3)))
#define RLX_AGENT __ATOMIC_RELAXED, __HIP_MEMORY_SCOPE_AGENT
typedef unsigned short u16;
typedef unsigned v4u __attribute__((ext_vector_type(4)));
typedef unsigned v2u __attribute__((ext_vector_type(2)));
typedef unsigned v8u __attribute__((ext_vector_type(8)));
typedef float f4 __attribute__((ext_vector_type(4)));
typedef __bf16 bf2 __attribute__((ext_vector_type(2)));
typedef __bf16 bf8 __attribute__((ext_vector_type(8)));
typedef __bf16 bf16v __attribute__((ext_vector_type(16)));
typedef short s4 __attribute__((ext_vector_type(4)));
typedef GAS unsigned gu32;
typedef __amdgpu_buffer_rsrc_t rsrc_t;

constexpr int S = 2048, DM = 640, NH = 32, DH = 192, HD = NH * DH, NSEG = 64, FF = 1536, TOPK = 8;

constexpr size_t MB = 1u << 20;
constexpr int RING = 18, CHUNK = 6, XB = 3 * RING;
constexpr size_t HAND_BYTES = 128 * MB;
constexpr size_t CTRL_OFF = HAND_BYTES;
constexpr unsigned CTRL_BYTES = 131072;
constexpr size_t HF_OFF = CTRL_OFF + CTRL_BYTES;
constexpr size_t HF_BYTES = (size_t)S * DM * 2;
constexpr size_t X1_OFF = HF_OFF + HF_BYTES;
constexpr size_t R_OFF = X1_OFF + 2 * HF_BYTES;
constexpr size_t ACT_OFF = R_OFF + S * 4;
constexpr size_t WS_END = ACT_OFF + TOPK * FF * 2;
static_assert(WS_END + 2 * MB <= 256 * MB, "workspace");

constexpr int C_EPOCH = 0;
constexpr int C_INIT = 64;
constexpr int C_LNF = 24576;
constexpr int C_MOD = 27648;
constexpr int C_Z0 = 2048;
constexpr int C_ANCH = 2048;
constexpr int C_ERR = 2304;
constexpr int C_FIN = 2336;
constexpr int C_IDX = 2560;
constexpr int C_AF = 4096;
constexpr int C_CL = 6144;
constexpr int C_VP = 8192;
constexpr int C_QP = 9216;
constexpr int C_OP = 10240;
constexpr int C_Z1 = 11264;
constexpr int C_STAT = 12288;

#define QSLEEP 60
#define STAG 2
#define VFAR 0
#define CALREP 10
#define FFN_WAIT 100
constexpr int M_OFF = 0, E_OFF = 16384, S_OFF = 49152, P_OFF = 57344, P2_OFF = P_OFF + 64 * 4096, BLK_END = P_OFF + 80 * 4096, V2_OFF = 393216;
constexpr int N_PP = 0x80000, N_CF = N_PP + 32768, N_REC = N_CF + 8192, N_END = N_REC + 16384;
static_assert(BLK_END <= V2_OFF && BLK_END == 8 * 47 * 1024 && P_OFF % 8192 == 0, "block");
__host__ __device__ constexpr int lmap(int off) { return ((off >> 13) << 14) | (off & 8191); }
constexpr int OOB = 0x40000000;
__host__ __device__ constexpr int tslot(int r) { return (r & ~15) | ((r & 3) << 2) | ((r >> 2) & 3); }

__device__ __forceinline__ float u2f(unsigned v) { return __builtin_bit_cast(float, v); }
__device__ __forceinline__ unsigned f2u(float v) { return __builtin_bit_cast(unsigned, v); }
__device__ __forceinline__ float bf2f(u16 v) { return __builtin_bit_cast(float, (unsigned)v << 16); }
__device__ __forceinline__ float lo2f(unsigned v) { return __builtin_bit_cast(float, v << 16); }
__device__ __forceinline__ float hi2f(unsigned v) { return __builtin_bit_cast(float, v & 0xffff0000u); }
__device__ __forceinline__ unsigned pack2(float a, float b) {
  bf2 v = {(__bf16)a, (__bf16)b};
  unsigned r = __builtin_bit_cast(unsigned, v);
  asm("" : "+v"(r));
  return r;
}
__device__ __forceinline__ float rbf(float a) { return (float)(__bf16)a; }
__device__ __forceinline__ unsigned long long uni64(unsigned long long a) {
  return (unsigned)__builtin_amdgcn_readfirstlane((unsigned)a) | (unsigned long long)(unsigned)__builtin_amdgcn_readfirstlane((unsigned)(a >> 32)) << 32;
}
__device__ __forceinline__ rsrc_t mkrsrc(const void* p, unsigned bytes) { return __builtin_amdgcn_make_buffer_rsrc((void*)uni64((unsigned long long)p), 0, bytes, 0x00020000); }
__device__ __forceinline__ unsigned xcc_id() { return (unsigned)__builtin_amdgcn_s_getreg(20 | (3 << 11)) & 0xfu; }
__device__ __forceinline__ int rfl(int v) { return __builtin_amdgcn_readfirstlane(v); }
__device__ __forceinline__ f4 mfma32(v4u a, v4u b, f4 c) { return __builtin_amdgcn_mfma_f32_16x16x32_bf16(__builtin_bit_cast(bf8, a), __builtin_bit_cast(bf8, b), c, 0, 0, 0); }
__device__ __forceinline__ f4 mfma16(v2u a, v2u b, f4 c) { return mfma32(v4u{a[0], a[1], 0u, 0u}, v4u{b[0], b[1], 0u, 0u}, c); }
__device__ __forceinline__ f4 smfma(v4u a, v8u b, f4 c, int idx) { return __builtin_amdgcn_smfmac_f32_16x16x64_bf16(__builtin_bit_cast(bf8, a), __builtin_bit_cast(bf16v, b), c, idx, 0, 0); }
__device__ __forceinline__ v2u trread(unsigned addr) { return __builtin_bit_cast(v2u, __builtin_amdgcn_ds_read_tr16_b64_v4i16((LAS s4*)addr)); }
__device__ __forceinline__ v4u lds128(unsigned addr) { return *(LAS v4u*)addr; }
__device__ __forceinline__ v2u lds64(unsigned addr) { return *(LAS v2u*)addr; }
__device__ __forceinline__ void dma16(rsrc_t r, int voff, int soff, unsigned ldsaddr) {
  asm volatile("s_mov_b32 m0, %0\n\ts_nop 3\n\tbuffer_load_dwordx4 %1, %2, %3 offen lds" ::"s"(ldsaddr), "v"(voff), "s"(r), "s"(soff) : "memory");
}
__device__ __forceinline__ void dma16_sc1(rsrc_t r, int voff, int soff, unsigned ldsaddr) {
  asm volatile("s_mov_b32 m0, %0\n\ts_nop 3\n\tbuffer_load_dwordx4 %1, %2, %3 offen sc1 lds" ::"s"(ldsaddr), "v"(voff), "s"(r), "s"(soff) : "memory");
}
#define VMCNT(n) asm volatile("s_waitcnt vmcnt(" #n ")" ::: "memory")
#define LGKM0() asm volatile("s_waitcnt lgkmcnt(0)" ::: "memory")
#define BAR() asm volatile("s_barrier" ::: "memory")
__device__ __forceinline__ float swapsum32(float a, float b) {
  auto r = __builtin_amdgcn_permlane32_swap(__builtin_bit_cast(unsigned, a), __builtin_bit_cast(unsigned, b), false, false);
  return u2f(r[0]) + u2f(r[1]);
}
__device__ __forceinline__ float swapsum16(float a, float b) {
  auto r = __builtin_amdgcn_permlane16_swap(__builtin_bit_cast(unsigned, a), __builtin_bit_cast(unsigned, b), false, false);
  return u2f(r[0]) + u2f(r[1]);
}
__device__ __forceinline__ float max_x32(float a) {
  auto r = __builtin_amdgcn_permlane32_swap(__builtin_bit_cast(unsigned, a), __builtin_bit_cast(unsigned, a), false, false);
  return fmaxf(u2f(r[0]), u2f(r[1]));
}
__device__ __forceinline__ float max_x16(float a) {
  auto r = __builtin_amdgcn_permlane16_swap(__builtin_bit_cast(unsigned, a), __builtin_bit_cast(unsigned, a), false, false);
  return fmaxf(u2f(r[0]), u2f(r[1]));
}
__device__ __forceinline__ unsigned ror8u(unsigned v) { return (unsigned)__builtin_amdgcn_update_dpp(0, (int)v, 0x128, 0xf, 0xf, true); }
__device__ __forceinline__ float ror8f(float v) { return __builtin_bit_cast(float, ror8u(__builtin_bit_cast(unsigned, v))); }
__device__ __forceinline__ float wave_sum(float v) {
  v += u2f(__builtin_amdgcn_update_dpp(0, (int)f2u(v), 0xb1, 0xf, 0xf, true)); v += u2f(__builtin_amdgcn_update_dpp(0, (int)f2u(v), 0x4e, 0xf, 0xf, true));
  v += u2f(__builtin_amdgcn_update_dpp(0, (int)f2u(v), 0x124, 0xf, 0xf, true)); v += ror8f(v);
  v = swapsum16(v, v);
  return swapsum32(v, v);
}
typedef unsigned short us2 __attribute__((ext_vector_type(2)));
__device__ __forceinline__ unsigned unsent(unsigned w) { return __builtin_bit_cast(unsigned, __builtin_elementwise_min(__builtin_bit_cast(us2, w), us2{0xfffe, 0xfffe})); }

constexpr unsigned CODE_BYTES = 118784;
struct Params {
  const u16 *x, *mem0, *wq, *wk, *wv, *wo, *wr, *wg, *wu, *wd, *c, *wmod;
  u16* out;
  unsigned char* ws;
  int dB, dM, dS, lead;
};

constexpr int L_MEM = 0, L_Q = 10240, L_RED = 16384, L_WO = 20480, L_VO = 20480, L_K = 40960, L_Q2 = L_Q + 2048;
constexpr int L_MISC = 122880;
constexpr int MAIN_LDS = 163840;
constexpr int SPIN_CAP = 1 << 18;
typedef unsigned v12u __attribute__((ext_vector_type(12)));
typedef unsigned v16u __attribute__((ext_vector_type(16)));
typedef unsigned v32u __attribute__((ext_vector_type(32)));

#define CHAIN_CLOBBERS \
  "v32", "v33", "v34", "v35", "v36", "v37", "v38", "v39", "v40", "v41", "v42", "v43", "v44", "v45", "v46", "v47", "v48", "v49", "v50", "v51", "v52", \
  "v53", "v54", "v55", "v56", "v57", "v58", "v59", "v60", "v61", "v62", "v63", "v64", "v65", "v66", "v67", "v68", "v69", "v70", "v71", "v72", "v73", \
  "v74", "v75", "v76", "v77", "v78", "v79", "v80", "v81", "v82", "v83", "v84", "v85", "v86", "v87", "v88", "v89", "v90", "v91", "v92", "v93", "v94", \
  "v95", "v100", "v101", "v102", "v103", "v104", "v105", "v106", "v107", "v108", "v109", "v110", "v111", "v112", "v113", "v114", "v115", "v116", "v117", \
  "v118", "v119", "v120", "v121", "v122", "v123", "v124", "v125", "v126", "v127", "v128", "v129", "v130", "v131", "v132", "v133", "v134", "v135", \
  "v136", "v137", "v138", "v139", "v140", "v141", "v142", "v143", "v144", "v145", "v146", "v147", "v148", "v149", "v150", "v151", "v152", "v153", \
  "v154", "v155", "v156", "v157", "v158", "v159", "v160", "v161", "v162", "v163", "v164", "v165", "v166", "v167", "v168", "v169", "v170", "v171", \
  "v172", "v173", "v174", "v175", "v176", "v177", "v178", "v179", "v180", "v181", "v182", "v183", "v184", "v185", "v186", "v187", "v188", "v189", \
  "v190", "v191", "v224", "v225", "v226", "v227", "v228", "v229", "v230", "v231", "v232", "v233", "v234", "v235", "v236", "v237", "v238", "v239", \
  "v240", "v241", "v242", "v243", "v244", "v245", "v246", "v247", "v248", "v249", "v250", "v251", "v252", "v253", "v254", "v255", "s18", "s19", "s20", \
  "s21", "s22", "s23", "s24", "s25", "s26", "s27", "s28", "s29", "s30", "s31", "s34", "s35", "s64", "s65", "s66", "s67", "s68", "s69", "s70", "s71", \
  "s72", "s73", "s74", "s75", "s76", "s77", "s78", "s79", "s80", "s81", "s82", "s83", "s84", "s85", "s86", "s87", "s88", "s89", "s90", "s91", "s92", \
  "s93", "s94", "s95", "s96", "s97", "s98", "s99", "a0", "a1", "a2", "a3", "a4", "a5", "a6", "a7", "a8", "a9", "a10", "a11", "a12", "a13", "a14", "a15", \
  "a16", "a17", "a18", "a19", "a20", "a21", "a22", "a23", "a24", "a25", "a26", "a27", "a28", "a29", "a30", "a31", "a32", "a33", "a34", "a35", "a36", \
  "a37", "a38", "a39", "a40", "a41", "a42", "a43", "a44", "a45", "a46", "a47", "a48", "a49", "a50", "a51", "a52", "a53", "a54", "a55", "a56", "a57", \
  "a58", "a59", "a60", "a61", "a62", "a63", "a64", "a65", "a66", "a67", "a68", "a69", "a70", "a71", "a72", "a73", "a74", "a75", "a76", "a77", "a78", \
  "a79", "a80", "a81", "a82", "a83", "a84", "a85", "a86", "a87", "a88", "a89", "a90", "a91", "a92", "a93", "a94", "a95", "a96", "a97", "a98", "a99", \
  "a100", "a101", "a102", "a103", "a104", "a105", "a106", "a107", "a108", "a109", "a110", "a111", "a112", "a113", "a114", "a115", "a116", "a117", \
  "a118", "a119", "a120", "a121", "a122", "a123", "a124", "a125", "a126", "a127", "a128", "a129", "a130", "a131", "a132", "a133", "a134", "a135", \
  "a136", "a137", "a138", "a139", "a140", "a141", "a142", "a143", "a144", "a145", "a146", "a147", "a148", "a149", "a150", "a151", "a152", "a153", \
  "a154", "a155", "a156", "a157", "a158", "a159", "a160", "a161", "a162", "a163", "a164", "a165", "a166", "a167", "a168", "a169", "a170", "a171", \
  "a172", "a173", "a174", "a175", "a176", "a177", "a178", "a179", "a180", "a181", "a182", "a183", "a184", "a185", "a186", "a187", "a188", "a189", \
  "a190", "a191", "a192", "a193", "a194", "a195", "a196", "a197", "a198", "a199", "a200", "a201", "a202", "a203", "a204", "a205", "a206", "a207", \
  "a208", "a209", "a210", "a211", "a212", "a213", "a214", "a215", "a216", "a217", "a218", "a219", "a220", "a221", "a222", "a223", "a224", "a225", \
  "a226", "a227", "a228", "a229", "a230", "a231", "a232", "a233", "a234", "a235", "a236", "a237", "a238", "a239", "a240", "a241", "a242", "a243", \
  "a244", "a245", "a246", "a247", "a248", "a249", "a250", "a251", "a252", "a253", "a254", "a255"
#define CHAIN_ASM \
  "s_and_b32 s71, s48, 3\n" \
  "s_cmp_eq_u32 s71, 2\n" \
  "s_cbranch_scc1 LF2_%=\n" \
  "s_cmp_ge_u32 s48, 8\n" \
  "s_cbranch_scc1 LF1_%=\n" \
  "s_mov_b32 s71, 0\n" \
  "s_add_u32 m0, s56, 57344\n" \
  "s_nop 0\n" \
  "buffer_load_dwordx4 v192, s[40:43], s71 offen lds\n" \
  "s_add_u32 m0, s56, 58368\n" \
  "s_nop 0\n" \
  "buffer_load_dwordx4 v193, s[40:43], s71 offen lds\n" \
  "s_add_u32 m0, s56, 59392\n" \
  "s_nop 0\n" \
  "buffer_load_dwordx4 v194, s[40:43], s71 offen lds\n" \
  "s_add_u32 m0, s56, 60416\n" \
  "s_nop 0\n" \
  "buffer_load_dwordx4 v195, s[40:43], s71 offen lds\n" \
  "s_add_u32 m0, s56, 61440\n" \
  "s_nop 0\n" \
  "buffer_load_dwordx4 v196, s[40:43], s71 offen lds\n" \
  "s_add_u32 m0, s56, 62464\n" \
  "s_nop 0\n" \
  "buffer_load_dwordx4 v197, s[40:43], s71 offen lds\n" \
  "s_add_u32 m0, s56, 63488\n" \
  "s_nop 0\n" \
  "buffer_load_dwordx4 v198, s[40:43], s71 offen lds\n" \
  "s_add_u32 m0, s56, 64512\n" \
  "s_nop 0\n" \
  "buffer_load_dwordx4 v199, s[40:43], s71 offen lds\n" \
  "s_mov_b32 s71, 786432\n" \
  "s_add_u32 m0, s56, 90112\n" \
  "s_nop 0\n" \
  "buffer_load_dwordx4 v192, s[40:43], s71 offen lds\n" \
  "s_add_u32 m0, s56, 91136\n" \
  "s_nop 0\n" \
  "buffer_load_dwordx4 v193, s[40:43], s71 offen lds\n" \
  "s_add_u32 m0, s56, 92160\n" \
  "s_nop 0\n" \
  "buffer_load_dwordx4 v194, s[40:43], s71 offen lds\n" \
  "s_add_u32 m0, s56, 93184\n" \
  "s_nop 0\n" \
  "buffer_load_dwordx4 v195, s[40:43], s71 offen lds\n" \
  "s_add_u32 m0, s56, 94208\n" \
  "s_nop 0\n" \
  "buffer_load_dwordx4 v196, s[40:43], s71 offen lds\n" \
  "s_add_u32 m0, s56, 95232\n" \
  "s_nop 0\n" \
  "buffer_load_dwordx4 v197, s[40:43], s71 offen lds\n" \
  "s_add_u32 m0, s56, 96256\n" \
  "s_nop 0\n" \
  "buffer_load_dwordx4 v198, s[40:43], s71 offen lds\n" \
  "s_add_u32 m0, s56, 97280\n" \
  "s_nop 0\n" \
  "buffer_load_dwordx4 v199, s[40:43], s71 offen lds\n" \
  "s_mov_b32 s71, 1572864\n" \
  "s_add_u32 m0, s56, 122880\n" \
  "s_nop 0\n" \
  "buffer_load_dwordx4 v192, s[40:43], s71 offen lds\n" \
  "s_add_u32 m0, s56, 123904\n" \
  "s_nop 0\n" \
  "buffer_load_dwordx4 v193, s[40:43], s71 offen lds\n" \
  "s_add_u32 m0, s56, 124928\n" \
  "s_nop 0\n" \
  "buffer_load_dwordx4 v194, s[40:43], s71 offen lds\n" \
  "s_add_u32 m0, s56, 125952\n" \
  "s_nop 0\n" \
  "buffer_load_dwordx4 v195, s[40:43], s71 offen lds\n" \
  "s_add_u32 m0, s56, 126976\n" \
  "s_nop 0\n" \
  "buffer_load_dwordx4 v196, s[40:43], s71 offen lds\n" \
  "s_add_u32 m0, s56, 128000\n" \
  "s_nop 0\n" \
  "buffer_load_dwordx4 v197, s[40:43], s71 offen lds\n" \
  "s_add_u32 m0, s56, 129024\n" \
  "s_nop 0\n" \
  "buffer_load_dwordx4 v198, s[40:43], s71 offen lds\n" \
  "s_add_u32 m0, s56, 130048\n" \
  "s_nop 0\n" \
  "buffer_load_dwordx4 v199, s[40:43], s71 offen lds\n" \
  "s_branch LF2_%=\n" \
  "LF1_%=:\n" \
  "s_mov_b32 s71, 0\n" \
  "s_add_u32 m0, s56, 16384\n" \
  "s_nop 0\n" \
  "buffer_load_dwordx4 v192, s[40:43], s71 offen lds\n" \
  "s_add_u32 m0, s56, 17408\n" \
  "s_nop 0\n" \
  "buffer_load_dwordx4 v193, s[40:43], s71 offen lds\n" \
  "s_add_u32 m0, s56, 18432\n" \
  "s_nop 0\n" \
  "buffer_load_dwordx4 v194, s[40:43], s71 offen lds\n" \
  "s_add_u32 m0, s56, 19456\n" \
  "s_nop 0\n" \
  "buffer_load_dwordx4 v195, s[40:43], s71 offen lds\n" \
  "s_add_u32 m0, s56, 20480\n" \
  "s_nop 0\n" \
  "buffer_load_dwordx4 v196, s[40:43], s71 offen lds\n" \
  "s_add_u32 m0, s56, 21504\n" \
  "s_nop 0\n" \
  "buffer_load_dwordx4 v197, s[40:43], s71 offen lds\n" \
  "s_mov_b32 s71, 786432\n" \
  "s_add_u32 m0, s56, 40960\n" \
  "s_nop 0\n" \
  "buffer_load_dwordx4 v192, s[40:43], s71 offen lds\n" \
  "s_add_u32 m0, s56, 41984\n" \
  "s_nop 0\n" \
  "buffer_load_dwordx4 v193, s[40:43], s71 offen lds\n" \
  "s_add_u32 m0, s56, 43008\n" \
  "s_nop 0\n" \
  "buffer_load_dwordx4 v194, s[40:43], s71 offen lds\n" \
  "s_add_u32 m0, s56, 44032\n" \
  "s_nop 0\n" \
  "buffer_load_dwordx4 v195, s[40:43], s71 offen lds\n" \
  "s_add_u32 m0, s56, 45056\n" \
  "s_nop 0\n" \
  "buffer_load_dwordx4 v196, s[40:43], s71 offen lds\n" \
  "s_add_u32 m0, s56, 46080\n" \
  "s_nop 0\n" \
  "buffer_load_dwordx4 v197, s[40:43], s71 offen lds\n" \
  "s_mov_b32 s71, 1572864\n" \
  "s_add_u32 m0, s56, 65536\n" \
  "s_nop 0\n" \
  "buffer_load_dwordx4 v192, s[40:43], s71 offen lds\n" \
  "s_add_u32 m0, s56, 66560\n" \
  "s_nop 0\n" \
  "buffer_load_dwordx4 v193, s[40:43], s71 offen lds\n" \
  "s_add_u32 m0, s56, 67584\n" \
  "s_nop 0\n" \
  "buffer_load_dwordx4 v194, s[40:43], s71 offen lds\n" \
  "s_add_u32 m0, s56, 68608\n" \
  "s_nop 0\n" \
  "buffer_load_dwordx4 v195, s[40:43], s71 offen lds\n" \
  "s_add_u32 m0, s56, 69632\n" \
  "s_nop 0\n" \
  "buffer_load_dwordx4 v196, s[40:43], s71 offen lds\n" \
  "s_add_u32 m0, s56, 70656\n" \
  "s_nop 0\n" \
  "buffer_load_dwordx4 v197, s[40:43], s71 offen lds\n" \
  "s_mov_b32 s71, 2359296\n" \
  "s_add_u32 m0, s56, 90112\n" \
  "s_nop 0\n" \
  "buffer_load_dwordx4 v192, s[40:43], s71 offen lds\n" \
  "s_add_u32 m0, s56, 91136\n" \
  "s_nop 0\n" \
  "buffer_load_dwordx4 v193, s[40:43], s71 offen lds\n" \
  "s_add_u32 m0, s56, 92160\n" \
  "s_nop 0\n" \
  "buffer_load_dwordx4 v194, s[40:43], s71 offen lds\n" \
  "s_add_u32 m0, s56, 93184\n" \
  "s_nop 0\n" \
  "buffer_load_dwordx4 v195, s[40:43], s71 offen lds\n" \
  "s_add_u32 m0, s56, 94208\n" \
  "s_nop 0\n" \
  "buffer_load_dwordx4 v196, s[40:43], s71 offen lds\n" \
  "s_add_u32 m0, s56, 95232\n" \
  "s_nop 0\n" \
  "buffer_load_dwordx4 v197, s[40:43], s71 offen lds\n" \
  "LF2_%=:\n" \
  "v_writelane_b32 v224, s50, 0\n" \
  "v_writelane_b32 v224, s51, 1\n" \
  "v_writelane_b32 v224, s52, 2\n" \
  "v_writelane_b32 v224, s58, 3\n" \
  "v_writelane_b32 v224, s40, 4\n" \
  "v_writelane_b32 v224, s41, 5\n" \
  "v_writelane_b32 v224, s42, 6\n" \
  "v_writelane_b32 v224, s43, 7\n" \
  "v_writelane_b32 v224, s44, 8\n" \
  "v_writelane_b32 v224, s45, 9\n" \
  "v_writelane_b32 v224, s46, 10\n" \
  "v_writelane_b32 v224, s47, 11\n" \
  "s_memrealtime s[74:75]\n" \
  "s_memtime s[76:77]\n" \
  "s_mov_b32 s73, 0\n" \
  "s_mov_b32 s38, 0\n" \
  "s_mov_b32 s64, 62\n" \
  "s_mov_b32 s60, 20001\n" \
  "s_mov_b32 s50, 0\n" \
  "s_mov_b32 s51, 0\n" \
  "s_mov_b32 s52, 4000\n" \
  "s_mov_b32 s90, 5900\n" \
  "s_waitcnt lgkmcnt(0)\n" \
  "s_mov_b32 s88, s74\n" \
  "v_mov_b32 v225, v25\n" \
  "v_mov_b32 v226, v26\n" \
  "s_cmp_ge_u32 s48, 8\n" \
  "s_cbranch_scc1 LDV_%=\n" \
  "LCN_%=:\n" \
  "s_mov_b32 s28, 0xffff0000\n" \
  "s_mov_b32 s29, 0x0\n" \
  "s_mov_b32 s26, 0x0\n" \
  "s_mov_b32 s27, 0xffff\n" \
  "s_mov_b32 s24, 0x0\n" \
  "s_mov_b32 s25, 0xffff0000\n" \
  "s_mov_b32 s46, 0x3030303\n" \
  "s_mov_b32 s47, 0x3030303\n" \
  "s_mov_b32 s30, 0xff00ff\n" \
  "s_mov_b32 s31, 0xff00ff00\n" \
  "s_mov_b32 s20, 0xff00ff\n" \
  "s_mov_b32 s21, 0xff00ff\n" \
  "s_mov_b32 s42, 0x3dd53b94\n" \
  "s_mov_b32 s43, 0x3dd53b94\n" \
  "s_mov_b32 s22, 0x3f80\n" \
  "s_mov_b32 s23, 0x3f800000\n" \
  "v_mov_b32 v56, 0x3f803f80\n" \
  "v_mov_b32 v72, 0\n" \
  "v_mov_b32 v57, 0x3f803f80\n" \
  "v_mov_b32 v73, 0\n" \
  "v_mov_b32 v58, 0x3f803f80\n" \
  "v_mov_b32 v74, 0\n" \
  "v_mov_b32 v59, 0x3f803f80\n" \
  "v_mov_b32 v75, 0\n" \
  "v_mov_b32 v60, 0x3f803f80\n" \
  "v_mov_b32 v76, 0\n" \
  "v_mov_b32 v61, 0x3f803f80\n" \
  "v_mov_b32 v77, 0\n" \
  "v_mov_b32 v62, 0x3f803f80\n" \
  "v_mov_b32 v78, 0\n" \
  "v_mov_b32 v63, 0x3f803f80\n" \
  "v_mov_b32 v79, 0\n" \
  "v_mov_b32 v64, 0x3f803f80\n" \
  "v_mov_b32 v80, 0\n" \
  "v_mov_b32 v65, 0x3f803f80\n" \
  "v_mov_b32 v81, 0\n" \
  "v_mov_b32 v66, 0x3f803f80\n" \
  "v_mov_b32 v82, 0\n" \
  "v_mov_b32 v67, 0x3f803f80\n" \
  "v_mov_b32 v83, 0\n" \
  "v_mov_b32 v68, 0x3f803f80\n" \
  "v_mov_b32 v84, 0\n" \
  "v_mov_b32 v69, 0x3f803f80\n" \
  "v_mov_b32 v85, 0\n" \
  "v_mov_b32 v70, 0x3f803f80\n" \
  "v_mov_b32 v86, 0\n" \
  "v_mov_b32 v71, 0x3f803f80\n" \
  "v_mov_b32 v87, 0\n" \
  "s_mov_b32 exec_lo, 0xfcfc\n" \
  "s_mov_b32 exec_hi, 0x0\n" \
  "v_mov_b32 v72, 0x3f803f80\n" \
  "v_mov_b32 v73, 0x3f803f80\n" \
  "v_mov_b32 v74, 0x3f803f80\n" \
  "v_mov_b32 v75, 0x3f803f80\n" \
  "s_mov_b32 exec_lo, 0xfcfc0000\n" \
  "s_mov_b32 exec_hi, 0x0\n" \
  "v_mov_b32 v76, 0x3f803f80\n" \
  "v_mov_b32 v77, 0x3f803f80\n" \
  "v_mov_b32 v78, 0x3f803f80\n" \
  "v_mov_b32 v79, 0x3f803f80\n" \
  "s_mov_b32 exec_lo, 0x0\n" \
  "s_mov_b32 exec_hi, 0xfcfc\n" \
  "v_mov_b32 v80, 0x3f803f80\n" \
  "v_mov_b32 v81, 0x3f803f80\n" \
  "v_mov_b32 v82, 0x3f803f80\n" \
  "v_mov_b32 v83, 0x3f803f80\n" \
  "s_mov_b32 exec_lo, 0x0\n" \
  "s_mov_b32 exec_hi, 0xfcfc0000\n" \
  "v_mov_b32 v84, 0x3f803f80\n" \
  "v_mov_b32 v85, 0x3f803f80\n" \
  "v_mov_b32 v86, 0x3f803f80\n" \
  "v_mov_b32 v87, 0x3f803f80\n" \
  "s_mov_b64 exec, -1\n" \
  "v_mov_b32 v33, 19456\n" \
  "v_mov_b32 v36, 0\n" \
  "ds_write_b32 v33, v36\n" \
  "s_cmp_eq_u32 s73, 1\n" \
  "s_cbranch_scc1 LSW_%=\n" \
  "s_branch LPASS_%=\n" \
  "LDV_%=:\n" \
  "s_mov_b32 s64, 63\n" \
  "LPASS_%=:\n" \
  "s_mul_i32 s93, s52, 24\n" \
  "s_cmp_eq_u32 s73, 0\n" \
  "s_cselect_b32 s94, -1073741824, -60\n" \
  "s_mov_b32 s97, 0\n" \
  "s_mov_b32 s98, 0\n" \
  "s_mov_b32 s19, 0\n" \
  "s_mov_b32 s34, 0\n" \
  "s_mov_b32 s35, 0\n" \
  "s_mul_i32 s58, s64, 100\n" \
  "s_mov_b32 s59, s58\n" \
  "s_mov_b32 s61, 64\n" \
  "s_mov_b32 s99, 3\n" \
  "s_mov_b32 s18, 16\n" \
  "s_mov_b32 s67, s54\n" \
  "s_add_u32 s50, s50, 16\n" \
  "s_mul_i32 s91, s50, 24\n" \
  "s_add_u32 s51, s51, 16\n" \
  "s_mul_i32 s92, s51, 24\n" \
  "s_memrealtime s[74:75]\n" \
  "s_memtime s[76:77]\n" \
  "s_waitcnt lgkmcnt(0)\n" \
  "s_add_u32 s89, s76, 0x1000000\n" \
  "s_mov_b32 s95, s74\n" \
  "s_mov_b32 s96, s76\n" \
  "v_readlane_b32 s66, v25, s64\n" \
  "v_readlane_b32 s68, v26, s64\n" \
  "s_nop 3\n" \
  "s_and_b32 s71, s48, 3\n" \
  "s_cmp_eq_u32 s71, 2\n" \
  "s_cbranch_scc0 LPA_%=\n" \
  "buffer_load_dword v34, v29, s[36:39], 0 offen sc1\n" \
  "s_waitcnt vmcnt(0)\n" \
  "LPA_%=:\n" \
  "s_cmp_eq_u32 s48, 0\n" \
  "s_cbranch_scc0 LPK0_%=\n" \
  "s_mov_b32 m0, 40960\n" \
  "s_add_u32 s72, s68, 0\n" \
  "buffer_load_dwordx4 v4, s[36:39], s72 offen sc1 lds\n" \
  "buffer_load_dwordx4 v4, s[36:39], s72 offen offset:1024 sc1 lds\n" \
  "buffer_load_dwordx4 v4, s[36:39], s72 offen offset:2048 sc1 lds\n" \
  "s_mov_b32 m0, 44032\n" \
  "s_add_u32 s72, s68, 16384\n" \
  "buffer_load_dwordx4 v4, s[36:39], s72 offen sc1 lds\n" \
  "buffer_load_dwordx4 v4, s[36:39], s72 offen offset:1024 sc1 lds\n" \
  "buffer_load_dwordx4 v4, s[36:39], s72 offen offset:2048 sc1 lds\n" \
  "LPK0_%=:\n" \
  "s_cmp_eq_u32 s48, 1\n" \
  "s_cbranch_scc0 LPK1_%=\n" \
  "s_mov_b32 m0, 47104\n" \
  "s_add_u32 s72, s68, 0\n" \
  "buffer_load_dwordx4 v4, s[36:39], s72 offen sc1 lds\n" \
  "buffer_load_dwordx4 v4, s[36:39], s72 offen offset:1024 sc1 lds\n" \
  "buffer_load_dwordx4 v4, s[36:39], s72 offen offset:2048 sc1 lds\n" \
  "s_mov_b32 m0, 50176\n" \
  "s_add_u32 s72, s68, 16384\n" \
  "buffer_load_dwordx4 v4, s[36:39], s72 offen sc1 lds\n" \
  "buffer_load_dwordx4 v4, s[36:39], s72 offen offset:1024 sc1 lds\n" \
  "buffer_load_dwordx4 v4, s[36:39], s72 offen offset:2048 sc1 lds\n" \
  "LPK1_%=:\n" \
  "LPB_%=:\n" \
  "s_cmp_eq_u32 s48, 1\n" \
  "s_cbranch_scc1 LSTART01_%=\n" \
  "s_cmp_eq_u32 s48, 2\n" \
  "s_cbranch_scc1 LSTART02_%=\n" \
  "s_cmp_eq_u32 s48, 3\n" \
  "s_cbranch_scc1 LSTART03_%=\n" \
  "s_cmp_eq_u32 s48, 4\n" \
  "s_cbranch_scc1 LSTART10_%=\n" \
  "s_cmp_eq_u32 s48, 5\n" \
  "s_cbranch_scc1 LSTART11_%=\n" \
  "s_cmp_eq_u32 s48, 6\n" \
  "s_cbranch_scc1 LSTART12_%=\n" \
  "s_cmp_eq_u32 s48, 7\n" \
  "s_cbranch_scc1 LSTART13_%=\n" \
  "s_cmp_eq_u32 s48, 8\n" \
  "s_cbranch_scc1 LSTART20_%=\n" \
  "s_cmp_eq_u32 s48, 9\n" \
  "s_cbranch_scc1 LSTART21_%=\n" \
  "s_cmp_eq_u32 s48, 10\n" \
  "s_cbranch_scc1 LSTART22_%=\n" \
  "s_cmp_eq_u32 s48, 11\n" \
  "s_cbranch_scc1 LSTART23_%=\n" \
  "LSTART00_%=:\n" \
  "LOOP00_%=:\n" \
  "v_readlane_b32 s70, v27, s64\n" \
  "s_mov_b32 s65, s66\n" \
  "s_mov_b32 s69, s68\n" \
  "s_add_u32 s64, s64, 1\n" \
  "s_barrier\n" \
  "s_mov_b32 m0, 20480\n" \
  "s_add_u32 s72, s70, 4096\n" \
  "buffer_load_dwordx4 v3, s[36:39], s70 offen sc1 lds\n" \
  "buffer_load_dwordx4 v3, s[36:39], s70 offen offset:1024 sc1 lds\n" \
  "buffer_load_dwordx4 v3, s[36:39], s70 offen offset:2048 sc1 lds\n" \
  "buffer_load_dwordx4 v3, s[36:39], s70 offen offset:3072 sc1 lds\n" \
  "s_mov_b32 m0, 24576\n" \
  "s_nop 0\n" \
  "buffer_load_dwordx4 v3, s[36:39], s72 offen sc1 lds\n" \
  "buffer_load_dwordx4 v3, s[36:39], s72 offen offset:1024 sc1 lds\n" \
  "buffer_load_dwordx4 v3, s[36:39], s72 offen offset:2048 sc1 lds\n" \
  "buffer_load_dwordx4 v3, s[36:39], s72 offen offset:3072 sc1 lds\n" \
  "v_mov_b32 v176, 0\n" \
  "v_mov_b32 v177, 0\n" \
  "v_mov_b32 v178, 0\n" \
  "v_mov_b32 v179, 0\n" \
  "v_mov_b32 v180, 0\n" \
  "v_mov_b32 v181, 0\n" \
  "v_mov_b32 v182, 0\n" \
  "v_mov_b32 v183, 0\n" \
  "v_readlane_b32 s66, v25, s64\n" \
  "v_readlane_b32 s68, v26, s64\n" \
  "s_waitcnt lgkmcnt(0)\n" \
  "LM00_%=:\n" \
  "s_barrier\n" \
  "ds_read_b128 v[112:115], v5\n" \
  "ds_read_b128 v[116:119], v5 offset:512\n" \
  "s_barrier\n" \
  "ds_read_b128 v[120:123], v5 offset:1024\n" \
  "ds_read_b128 v[124:127], v5 offset:1536\n" \
  "s_waitcnt lgkmcnt(3)\n" \
  "v_smfmac_f32_16x16x64_bf16 v[176:179], v[112:115], a[0:7], v0\n" \
  "v_smfmac_f32_16x16x64_bf16 v[180:183], v[112:115], a[160:167], v0\n" \
  "s_waitcnt lgkmcnt(2)\n" \
  "v_smfmac_f32_16x16x64_bf16 v[176:179], v[116:119], a[8:15], v0\n" \
  "s_barrier\n" \
  "ds_read_b128 v[128:131], v5 offset:2048\n" \
  "v_smfmac_f32_16x16x64_bf16 v[180:183], v[116:119], a[168:175], v0\n" \
  "s_waitcnt lgkmcnt(2)\n" \
  "ds_read_b128 v[132:135], v5 offset:2560\n" \
  "v_smfmac_f32_16x16x64_bf16 v[176:179], v[120:123], a[16:23], v0\n" \
  "ds_read_b128 v[136:139], v5 offset:3072\n" \
  "v_smfmac_f32_16x16x64_bf16 v[180:183], v[120:123], a[176:183], v0\n" \
  "s_waitcnt lgkmcnt(3)\n" \
  "ds_read_b128 v[140:143], v5 offset:3584\n" \
  "v_smfmac_f32_16x16x64_bf16 v[176:179], v[124:127], a[24:31], v0\n" \
  "s_barrier\n" \
  "ds_read_b128 v[144:147], v5 offset:4096\n" \
  "v_smfmac_f32_16x16x64_bf16 v[180:183], v[124:127], a[184:191], v0\n" \
  "s_waitcnt lgkmcnt(4)\n" \
  "ds_read_b128 v[148:151], v5 offset:4608\n" \
  "v_smfmac_f32_16x16x64_bf16 v[176:179], v[128:131], a[32:39], v0\n" \
  "ds_read_b128 v[152:155], v5 offset:5120\n" \
  "v_smfmac_f32_16x16x64_bf16 v[180:183], v[128:131], a[192:199], v0\n" \
  "s_waitcnt lgkmcnt(5)\n" \
  "ds_read_b128 v[156:159], v5 offset:5632\n" \
  "v_smfmac_f32_16x16x64_bf16 v[176:179], v[132:135], a[40:47], v0\n" \
  "s_barrier\n" \
  "ds_read_b128 v[160:163], v5 offset:6144\n" \
  "v_smfmac_f32_16x16x64_bf16 v[180:183], v[132:135], a[200:207], v0\n" \
  "s_waitcnt lgkmcnt(6)\n" \
  "ds_read_b128 v[164:167], v5 offset:6656\n" \
  "v_smfmac_f32_16x16x64_bf16 v[176:179], v[136:139], a[48:55], v0\n" \
  "ds_read_b128 v[168:171], v5 offset:7168\n" \
  "v_smfmac_f32_16x16x64_bf16 v[180:183], v[136:139], a[208:215], v0\n" \
  "s_waitcnt vmcnt(8)\n" \
  "s_waitcnt lgkmcnt(7)\n" \
  "ds_read_b128 v[172:175], v5 offset:7680\n" \
  "v_smfmac_f32_16x16x64_bf16 v[176:179], v[140:143], a[56:63], v0\n" \
  "s_barrier\n" \
  "ds_read2_b32 v[112:113], v6 offset0:0 offset1:32\n" \
  "v_smfmac_f32_16x16x64_bf16 v[180:183], v[140:143], a[216:223], v0\n" \
  "s_waitcnt lgkmcnt(8)\n" \
  "ds_read2_b32 v[114:115], v6 offset0:64 offset1:96\n" \
  "v_smfmac_f32_16x16x64_bf16 v[176:179], v[144:147], a[64:71], v0\n" \
  "s_waitcnt lgkmcnt(8)\n" \
  "ds_read2_b32 v[116:117], v6 offset0:128 offset1:160\n" \
  "v_smfmac_f32_16x16x64_bf16 v[176:179], v[148:151], a[72:79], v0\n" \
  "s_waitcnt lgkmcnt(8)\n" \
  "ds_read2_b32 v[118:119], v6 offset0:192 offset1:224\n" \
  "v_smfmac_f32_16x16x64_bf16 v[176:179], v[152:155], a[80:87], v0\n" \
  "s_waitcnt lgkmcnt(8)\n" \
  "ds_read2_b32 v[120:121], v7 offset0:0 offset1:32\n" \
  "v_smfmac_f32_16x16x64_bf16 v[176:179], v[156:159], a[88:95], v0\n" \
  "s_waitcnt lgkmcnt(8)\n" \
  "ds_read2_b32 v[122:123], v7 offset0:64 offset1:96\n" \
  "v_smfmac_f32_16x16x64_bf16 v[176:179], v[160:163], a[96:103], v0\n" \
  "s_waitcnt lgkmcnt(8)\n" \
  "ds_read2_b32 v[124:125], v7 offset0:128 offset1:160\n" \
  "v_smfmac_f32_16x16x64_bf16 v[176:179], v[164:167], a[104:111], v0\n" \
  "s_waitcnt lgkmcnt(8)\n" \
  "ds_read2_b32 v[126:127], v7 offset0:192 offset1:224\n" \
  "v_smfmac_f32_16x16x64_bf16 v[176:179], v[168:171], a[112:119], v0\n" \
  "s_waitcnt lgkmcnt(8)\n" \
  "ds_read_b128 v[88:91], v1\n" \
  "v_smfmac_f32_16x16x64_bf16 v[176:179], v[172:175], a[120:127], v0\n" \
  "s_waitcnt lgkmcnt(7)\n" \
  "ds_read_b128 v[92:95], v1 offset:3072\n" \
  "v_smfmac_f32_16x16x64_bf16 v[176:179], v[112:115], a[128:135], v0\n" \
  "s_waitcnt lgkmcnt(6)\n" \
  "ds_read_b128 v[36:39], v1 offset:6144\n" \
  "v_smfmac_f32_16x16x64_bf16 v[176:179], v[116:119], a[136:143], v0\n" \
  "s_waitcnt lgkmcnt(5)\n" \
  "ds_read_b128 v[100:103], v1 offset:9216\n" \
  "v_smfmac_f32_16x16x64_bf16 v[176:179], v[120:123], a[144:151], v0\n" \
  "s_waitcnt lgkmcnt(4)\n" \
  "ds_read_b64 v[104:105], v2\n" \
  "v_smfmac_f32_16x16x64_bf16 v[176:179], v[124:127], a[152:159], v0\n" \
  "ds_read_b64 v[106:107], v2 offset:3072\n" \
  "v_smfmac_f32_16x16x64_bf16 v[180:183], v[144:147], a[224:231], v0\n" \
  "ds_read_b64 v[108:109], v2 offset:6144\n" \
  "v_smfmac_f32_16x16x64_bf16 v[180:183], v[148:151], a[232:239], v0\n" \
  "ds_read_b64 v[110:111], v2 offset:9216\n" \
  "v_cvt_pk_bf16_f32 v184, v176, v177\n" \
  "v_smfmac_f32_16x16x64_bf16 v[180:183], v[152:155], a[240:247], v0\n" \
  "v_cvt_pk_bf16_f32 v185, v178, v179\n" \
  "v_smfmac_f32_16x16x64_bf16 v[180:183], v[156:159], a[248:255], v0\n" \
  "ds_write_b64 v8, v[184:185]\n" \
  "v_add_f32 v186, v176, v177\n" \
  "v_smfmac_f32_16x16x64_bf16 v[180:183], v[160:163], v[192:199], v0\n" \
  "v_add_f32 v187, v178, v179\n" \
  "v_smfmac_f32_16x16x64_bf16 v[180:183], v[164:167], v[200:207], v0\n" \
  "v_cmp_u_f32 vcc, v186, v187\n" \
  "v_smfmac_f32_16x16x64_bf16 v[180:183], v[168:171], v[208:215], v0\n" \
  "v_smfmac_f32_16x16x64_bf16 v[180:183], v[172:175], v[216:223], v0\n" \
  "s_cbranch_vccnz LMF00_%=\n" \
  "LMOK00_%=:\n" \
  "v_smfmac_f32_16x16x64_bf16 v[180:183], v[112:115], v[224:231], v0\n" \
  "s_waitcnt lgkmcnt(0)\n" \
  "s_barrier\n" \
  "ds_read_b64_tr_b16 v[128:129], v10\n" \
  "v_smfmac_f32_16x16x64_bf16 v[180:183], v[116:119], v[232:239], v0\n" \
  "ds_read_b64_tr_b16 v[130:131], v10 offset:32\n" \
  "ds_read_b64_tr_b16 v[132:133], v10 offset:512\n" \
  "v_smfmac_f32_16x16x64_bf16 v[180:183], v[120:123], v[240:247], v0\n" \
  "ds_read_b64_tr_b16 v[134:135], v10 offset:544\n" \
  "ds_read_b64_tr_b16 v[136:137], v10 offset:1024\n" \
  "v_smfmac_f32_16x16x64_bf16 v[180:183], v[124:127], v[248:255], v0\n" \
  "s_nop 0\n" \
  "ds_read_b64_tr_b16 v[138:139], v10 offset:1056\n" \
  "ds_read_b64_tr_b16 v[140:141], v10 offset:1536\n" \
  "ds_read_b64_tr_b16 v[142:143], v10 offset:1568\n" \
  "v_mov_b32 v31, v31\n" \
  "s_nop 2\n" \
  "v_cvt_pk_bf16_f32 v186, v180, v181\n" \
  "v_cvt_pk_bf16_f32 v187, v182, v183\n" \
  "ds_write_b64 v9, v[186:187]\n" \
  "s_waitcnt lgkmcnt(7)\n" \
  "v_mfma_f32_16x16x32_bf16 v[188:191], v[88:91], v[128:131], 0\n" \
  "s_waitcnt lgkmcnt(5)\n" \
  "v_mfma_f32_16x16x32_bf16 v[188:191], v[92:95], v[132:135], v[188:191]\n" \
  "s_waitcnt lgkmcnt(3)\n" \
  "v_mfma_f32_16x16x32_bf16 v[188:191], v[36:39], v[136:139], v[188:191]\n" \
  "s_waitcnt lgkmcnt(1)\n" \
  "v_mfma_f32_16x16x32_bf16 v[188:191], v[100:103], v[140:143], v[188:191]\n" \
  "s_waitcnt lgkmcnt(0)\n" \
  "s_barrier\n" \
  "ds_read_b64_tr_b16 v[144:145], v11\n" \
  "ds_read_b64_tr_b16 v[146:147], v11 offset:512\n" \
  "ds_read_b64_tr_b16 v[148:149], v11 offset:1024\n" \
  "ds_read_b64_tr_b16 v[150:151], v11 offset:1536\n" \
  "v_mov_b32 v31, v31\n" \
  "s_nop 2\n" \
  "v_mov_b32 v31, v31\n" \
  "s_nop 2\n" \
  "v_mov_b32 v31, v31\n" \
  "s_nop 0\n" \
  "s_waitcnt lgkmcnt(2)\n" \
  "v_mfma_f32_16x16x32_bf16 v[188:191], v[104:107], v[144:147], v[188:191]\n" \
  "s_waitcnt lgkmcnt(0)\n" \
  "v_mfma_f32_16x16x32_bf16 v[188:191], v[108:111], v[148:151], v[188:191]\n" \
  "s_nop 0\n" \
  "ds_read_b64_tr_b16 v[128:129], v12\n" \
  "s_nop 1\n" \
  "v_mov_b32 v31, v31\n" \
  "s_nop 2\n" \
  "v_cndmask_b32 v152, v190, v188, s[20:21]\n" \
  "v_cndmask_b32 v153, v191, v189, s[20:21]\n" \
  "v_mul_f32 v152, 0x3dd53b94, v152\n" \
  "v_mul_f32 v153, 0x3dd53b94, v153\n" \
  "v_max_f32 v156, |v152|, |v153|\n" \
  "v_cmp_lt_f32 vcc, 0x42800000, v156\n" \
  "v_exp_f32 v154, v152\n" \
  "v_exp_f32 v155, v153\n" \
  "s_cbranch_vccnz LMX00_%=\n" \
  "v_cvt_pk_bf16_f32 v154, v154, v155\n" \
  "LMY00_%=:\n" \
  "buffer_store_dword v154, v13, s[36:39], s65 offen sc1\n" \
  "s_waitcnt lgkmcnt(0)\n" \
  "buffer_store_dwordx2 v[128:129], v14, s[36:39], s69 offen sc1\n" \
  "s_mov_b32 s67, s66\n" \
  "s_cmp_eq_u32 s64, 64\n" \
  "s_cbranch_scc1 LDONE_%=\n" \
  "LB00_%=:\n" \
  "s_barrier\n" \
  "buffer_load_dwordx4 v[112:115], v15, s[36:39], s65 offen sc1\n" \
  "s_barrier\n" \
  "buffer_load_dwordx4 v[116:119], v15, s[36:39], s65 offen offset:128 sc1\n" \
  "s_barrier\n" \
  "buffer_load_dwordx4 v[120:123], v16, s[36:39], s65 offen sc1\n" \
  "s_barrier\n" \
  "buffer_load_dwordx4 v[124:127], v16, s[36:39], s65 offen offset:512 sc1\n" \
  "s_barrier\n" \
  "buffer_load_dwordx4 v[128:131], v16, s[36:39], s65 offen offset:1024 sc1\n" \
  "s_waitcnt vmcnt(7)\n" \
  "s_barrier\n" \
  "buffer_load_dwordx4 v[132:135], v16, s[36:39], s65 offen offset:1536 sc1\n" \
  "ds_read_b128 v[40:43], v19\n" \
  "ds_read_b128 v[44:47], v19 offset:512\n" \
  "ds_read_b128 v[48:51], v19 offset:1024\n" \
  "ds_read_b128 v[52:55], v19 offset:1536\n" \
  "s_mov_b64 exec, s[46:47]\n" \
  "ds_read_b128 v[56:59], v20\n" \
  "ds_read_b128 v[60:63], v20 offset:128\n" \
  "ds_read_b128 v[64:67], v20 offset:256\n" \
  "ds_read_b128 v[68:71], v20 offset:384\n" \
  "s_mov_b64 exec, -1\n" \
  "s_nop 15\n" \
  "s_nop 1\n" \
  "buffer_load_dwordx4 v[136:139], v17, s[36:39], s65 offen sc1\n" \
  "buffer_load_dwordx4 v[140:143], v18, s[36:39], s65 offen sc1\n" \
  "s_waitcnt lgkmcnt(0)\n" \
  "s_getreg_b32 s71, hwreg(HW_REG_IB_STS, 0, 4)\n" \
  "s_cmp_le_u32 s71, 6\n" \
  "v_mfma_f32_16x16x32_bf16 v[144:147], v[72:75], v[72:75], 0\n" \
  "s_cbranch_scc1 LHXS00_%=\n" \
  "s_getreg_b32 s71, hwreg(HW_REG_IB_STS, 0, 4)\n" \
  "s_cmp_le_u32 s71, 6\n" \
  "v_mfma_f32_16x16x32_bf16 v[144:147], v[72:75], v[72:75], 0\n" \
  "s_cbranch_scc1 LHXS00_%=\n" \
  "s_getreg_b32 s71, hwreg(HW_REG_IB_STS, 0, 4)\n" \
  "s_cmp_le_u32 s71, 6\n" \
  "v_mfma_f32_16x16x32_bf16 v[144:147], v[72:75], v[72:75], 0\n" \
  "s_cbranch_scc1 LHXS00_%=\n" \
  "s_getreg_b32 s71, hwreg(HW_REG_IB_STS, 0, 4)\n" \
  "s_cmp_le_u32 s71, 6\n" \
  "v_mfma_f32_16x16x32_bf16 v[144:147], v[72:75], v[72:75], 0\n" \
  "s_cbranch_scc1 LHXS00_%=\n" \
  "s_getreg_b32 s71, hwreg(HW_REG_IB_STS, 0, 4)\n" \
  "s_cmp_le_u32 s71, 6\n" \
  "v_mfma_f32_16x16x32_bf16 v[144:147], v[72:75], v[72:75], 0\n" \
  "s_cbranch_scc1 LHXS00_%=\n" \
  "s_getreg_b32 s71, hwreg(HW_REG_IB_STS, 0, 4)\n" \
  "s_cmp_le_u32 s71, 6\n" \
  "v_mfma_f32_16x16x32_bf16 v[144:147], v[72:75], v[72:75], 0\n" \
  "s_cbranch_scc1 LHXS00_%=\n" \
  "s_getreg_b32 s71, hwreg(HW_REG_IB_STS, 0, 4)\n" \
  "s_cmp_le_u32 s71, 6\n" \
  "v_mfma_f32_16x16x32_bf16 v[144:147], v[72:75], v[72:75], 0\n" \
  "s_cbranch_scc1 LHXS00_%=\n" \
  "s_getreg_b32 s71, hwreg(HW_REG_IB_STS, 0, 4)\n" \
  "s_cmp_le_u32 s71, 6\n" \
  "v_mfma_f32_16x16x32_bf16 v[144:147], v[72:75], v[72:75], 0\n" \
  "s_cbranch_scc1 LHXS00_%=\n" \
  "s_getreg_b32 s71, hwreg(HW_REG_IB_STS, 0, 4)\n" \
  "s_cmp_le_u32 s71, 6\n" \
  "v_mfma_f32_16x16x32_bf16 v[144:147], v[72:75], v[72:75], 0\n" \
  "s_cbranch_scc1 LHXS00_%=\n" \
  "s_getreg_b32 s71, hwreg(HW_REG_IB_STS, 0, 4)\n" \
  "s_cmp_le_u32 s71, 6\n" \
  "v_mfma_f32_16x16x32_bf16 v[144:147], v[72:75], v[72:75], 0\n" \
  "s_cbranch_scc1 LHXS00_%=\n" \
  "s_getreg_b32 s71, hwreg(HW_REG_IB_STS, 0, 4)\n" \
  "s_cmp_le_u32 s71, 6\n" \
  "v_mfma_f32_16x16x32_bf16 v[144:147], v[72:75], v[72:75], 0\n" \
  "s_cbranch_scc1 LHXS00_%=\n" \
  "s_getreg_b32 s71, hwreg(HW_REG_IB_STS, 0, 4)\n" \
  "s_cmp_le_u32 s71, 6\n" \
  "v_mfma_f32_16x16x32_bf16 v[144:147], v[72:75], v[72:75], 0\n" \
  "s_cbranch_scc1 LHXS00_%=\n" \
  "s_getreg_b32 s71, hwreg(HW_REG_IB_STS, 0, 4)\n" \
  "s_cmp_le_u32 s71, 6\n" \
  "v_mfma_f32_16x16x32_bf16 v[144:147], v[72:75], v[72:75], 0\n" \
  "s_cbranch_scc1 LHXS00_%=\n" \
  "s_getreg_b32 s71, hwreg(HW_REG_IB_STS, 0, 4)\n" \
  "s_cmp_le_u32 s71, 6\n" \
  "v_mfma_f32_16x16x32_bf16 v[144:147], v[72:75], v[72:75], 0\n" \
  "s_cbranch_scc1 LHXS00_%=\n" \
  "s_getreg_b32 s71, hwreg(HW_REG_IB_STS, 0, 4)\n" \
  "s_cmp_le_u32 s71, 6\n" \
  "v_mfma_f32_16x16x32_bf16 v[144:147], v[72:75], v[72:75], 0\n" \
  "s_cbranch_scc1 LHXS00_%=\n" \
  "s_getreg_b32 s71, hwreg(HW_REG_IB_STS, 0, 4)\n" \
  "s_cmp_le_u32 s71, 6\n" \
  "v_mfma_f32_16x16x32_bf16 v[144:147], v[72:75], v[72:75], 0\n" \
  "s_cbranch_scc1 LHXS00_%=\n" \
  "s_getreg_b32 s71, hwreg(HW_REG_IB_STS, 0, 4)\n" \
  "s_cmp_le_u32 s71, 6\n" \
  "v_mfma_f32_16x16x32_bf16 v[144:147], v[72:75], v[72:75], 0\n" \
  "s_cbranch_scc1 LHXS00_%=\n" \
  "s_getreg_b32 s71, hwreg(HW_REG_IB_STS, 0, 4)\n" \
  "s_cmp_le_u32 s71, 6\n" \
  "v_mfma_f32_16x16x32_bf16 v[144:147], v[72:75], v[72:75], 0\n" \
  "s_cbranch_scc1 LHXS00_%=\n" \
  "s_getreg_b32 s71, hwreg(HW_REG_IB_STS, 0, 4)\n" \
  "s_cmp_le_u32 s71, 6\n" \
  "v_mfma_f32_16x16x32_bf16 v[144:147], v[72:75], v[72:75], 0\n" \
  "s_cbranch_scc1 LHXS00_%=\n" \
  "s_getreg_b32 s71, hwreg(HW_REG_IB_STS, 0, 4)\n" \
  "s_cmp_le_u32 s71, 6\n" \
  "v_mfma_f32_16x16x32_bf16 v[144:147], v[72:75], v[72:75], 0\n" \
  "s_cbranch_scc1 LHXS00_%=\n" \
  "s_waitcnt vmcnt(6)\n" \
  "LHXS00_%=:\n" \
  "LS200_%=:\n" \
  "v_dot2_f32_bf16 v176, v112, s22, 0\n" \
  "v_dot2_f32_bf16 v177, v112, s23, 0\n" \
  "v_dot2_f32_bf16 v178, v113, s22, 0\n" \
  "v_dot2_f32_bf16 v179, v113, s23, 0\n" \
  "v_dot2_f32_bf16 v180, v114, s22, 0\n" \
  "v_dot2_f32_bf16 v181, v114, s23, 0\n" \
  "v_dot2_f32_bf16 v182, v115, s22, 0\n" \
  "v_dot2_f32_bf16 v183, v115, s23, 0\n" \
  "v_dot2_f32_bf16 v176, v116, s22, v176\n" \
  "v_dot2_f32_bf16 v177, v116, s23, v177\n" \
  "v_dot2_f32_bf16 v178, v117, s22, v178\n" \
  "v_dot2_f32_bf16 v179, v117, s23, v179\n" \
  "v_dot2_f32_bf16 v180, v118, s22, v180\n" \
  "v_dot2_f32_bf16 v181, v118, s23, v181\n" \
  "v_dot2_f32_bf16 v182, v119, s22, v182\n" \
  "v_dot2_f32_bf16 v183, v119, s23, v183\n" \
  "v_exp_f32 v176, v176\n" \
  "v_exp_f32 v177, v177\n" \
  "v_exp_f32 v178, v178\n" \
  "v_exp_f32 v179, v179\n" \
  "s_waitcnt vmcnt(5)\n" \
  "v_mfma_f32_16x16x32_bf16 v[160:163], v[56:59], v[120:123], 0\n" \
  "v_exp_f32 v180, v180\n" \
  "v_mfma_f32_16x16x32_bf16 v[144:147], v[40:43], v[120:123], 0\n" \
  "v_exp_f32 v181, v181\n" \
  "v_exp_f32 v182, v182\n" \
  "v_exp_f32 v183, v183\n" \
  "v_cvt_pk_bf16_f32 v184, v176, v177\n" \
  "v_cvt_pk_bf16_f32 v185, v178, v179\n" \
  "v_cvt_pk_bf16_f32 v186, v180, v181\n" \
  "v_cvt_pk_bf16_f32 v187, v182, v183\n" \
  "s_nop 1\n" \
  "v_mfma_f32_16x16x32_bf16 v[160:163], v[72:75], v[184:187], v[160:163]\n" \
  "v_mfma_f32_16x16x32_bf16 v[164:167], v[76:79], v[184:187], 0\n" \
  "s_waitcnt vmcnt(4)\n" \
  "v_mfma_f32_16x16x32_bf16 v[164:167], v[60:63], v[124:127], v[164:167]\n" \
  "v_rcp_f32 v188, v162\n" \
  "v_mfma_f32_16x16x32_bf16 v[148:151], v[44:47], v[124:127], 0\n" \
  "v_mul_f32 v144, v188, v144\n" \
  "v_mul_f32 v145, v188, v145\n" \
  "v_mfma_f32_16x16x32_bf16 v[168:171], v[80:83], v[184:187], 0\n" \
  "s_waitcnt vmcnt(3)\n" \
  "v_rcp_f32 v189, v166\n" \
  "v_mfma_f32_16x16x32_bf16 v[168:171], v[64:67], v[128:131], v[168:171]\n" \
  "v_mul_f32 v146, v188, v146\n" \
  "v_mul_f32 v147, v188, v147\n" \
  "v_mfma_f32_16x16x32_bf16 v[152:155], v[48:51], v[128:131], 0\n" \
  "v_mul_f32 v160, v188, v160\n" \
  "v_mul_f32 v161, v188, v161\n" \
  "v_mfma_f32_16x16x32_bf16 v[172:175], v[84:87], v[184:187], 0\n" \
  "s_waitcnt vmcnt(2)\n" \
  "v_rcp_f32 v190, v170\n" \
  "v_mfma_f32_16x16x32_bf16 v[172:175], v[68:71], v[132:135], v[172:175]\n" \
  "v_cndmask_b32 v167, v188, v189, s[28:29]\n" \
  "v_cndmask_b32 v167, v167, v190, s[26:27]\n" \
  "v_mfma_f32_16x16x32_bf16 v[156:159], v[52:55], v[132:135], 0\n" \
  "v_fmac_f32 v144, v189, v148\n" \
  "v_fmac_f32 v145, v189, v149\n" \
  "v_fmac_f32 v146, v189, v150\n" \
  "v_fmac_f32 v147, v189, v151\n" \
  "v_rcp_f32 v191, v174\n" \
  "v_fmac_f32 v160, v189, v164\n" \
  "v_cndmask_b32 v167, v167, v191, s[24:25]\n" \
  "v_pk_mul_f32 v[176:177], v[176:177], v[166:167] op_sel:[0,1] op_sel_hi:[1,1]\n" \
  "v_pk_mul_f32 v[178:179], v[178:179], v[166:167] op_sel:[0,1] op_sel_hi:[1,1]\n" \
  "v_pk_mul_f32 v[180:181], v[180:181], v[166:167] op_sel:[0,1] op_sel_hi:[1,1]\n" \
  "v_pk_mul_f32 v[182:183], v[182:183], v[166:167] op_sel:[0,1] op_sel_hi:[1,1]\n" \
  "v_cmp_nlt_f32 vcc, 0x0d800000, v167\n" \
  "v_cvt_pk_bf16_f32 v184, v176, v177\n" \
  "v_cvt_pk_bf16_f32 v185, v178, v179\n" \
  "v_cvt_pk_bf16_f32 v186, v180, v181\n" \
  "v_cvt_pk_bf16_f32 v187, v182, v183\n" \
  "v_fmac_f32 v161, v189, v165\n" \
  "v_pk_fma_f32 v[160:161], v[168:169], v[190:191], v[160:161] op_sel:[0,0,0] op_sel_hi:[1,0,1]\n" \
  "v_pk_fma_f32 v[160:161], v[172:173], v[190:191], v[160:161] op_sel:[0,1,0] op_sel_hi:[1,1,1]\n" \
  "v_pk_fma_f32 v[144:145], v[152:153], v[190:191], v[144:145] op_sel:[0,0,0] op_sel_hi:[1,0,1]\n" \
  "v_pk_fma_f32 v[146:147], v[154:155], v[190:191], v[146:147] op_sel:[0,0,0] op_sel_hi:[1,0,1]\n" \
  "v_pk_fma_f32 v[144:145], v[156:157], v[190:191], v[144:145] op_sel:[0,1,0] op_sel_hi:[1,1,1]\n" \
  "v_pk_fma_f32 v[146:147], v[158:159], v[190:191], v[146:147] op_sel:[0,1,0] op_sel_hi:[1,1,1]\n" \
  "s_waitcnt vmcnt(0)\n" \
  "v_mfma_f32_16x16x32_bf16 v[160:163], v[140:143], v[184:187], v[160:163]\n" \
  "s_mov_b64 s[80:81], vcc\n" \
  "v_mfma_f32_16x16x32_bf16 v[144:147], v[136:139], v[184:187], v[144:147]\n" \
  "s_mov_b64 exec, s[30:31]\n" \
  "s_nop 2\n" \
  "ds_write_b64 v22, v[160:161]\n" \
  "v_cmp_u_f32 s[78:79], v160, v161\n" \
  "s_nop 1\n" \
  "ds_write2_b64 v21, v[144:145], v[146:147] offset1:16\n" \
  "v_add_f32 v188, v144, v145\n" \
  "v_add_f32 v189, v146, v147\n" \
  "v_cmp_u_f32 vcc, v188, v189\n" \
  "s_or_b64 s[80:81], s[80:81], s[78:79]\n" \
  "s_or_b64 vcc, vcc, s[80:81]\n" \
  "s_cbranch_vccnz LSF00_%=\n" \
  "LSOK00_%=:\n" \
  "s_mov_b64 exec, -1\n" \
  "s_waitcnt lgkmcnt(0)\n" \
  "s_barrier\n" \
  "ds_read_b32 v188, v33\n" \
  "s_waitcnt lgkmcnt(0)\n" \
  "v_cmp_ne_u32 vcc, 0, v188\n" \
  "s_cbranch_vccnz LSR00_%=\n" \
  "s_nop 15\n" \
  "s_nop 3\n" \
  "s_mov_b32 m0, 40960\n" \
  "s_add_u32 s72, s68, 0\n" \
  "buffer_load_dwordx4 v4, s[36:39], s72 offen sc1 lds\n" \
  "buffer_load_dwordx4 v4, s[36:39], s72 offen offset:1024 sc1 lds\n" \
  "buffer_load_dwordx4 v4, s[36:39], s72 offen offset:2048 sc1 lds\n" \
  "s_mov_b32 m0, 44032\n" \
  "s_add_u32 s72, s68, 16384\n" \
  "buffer_load_dwordx4 v4, s[36:39], s72 offen sc1 lds\n" \
  "buffer_load_dwordx4 v4, s[36:39], s72 offen offset:1024 sc1 lds\n" \
  "buffer_load_dwordx4 v4, s[36:39], s72 offen offset:2048 sc1 lds\n" \
  "LNL00_%=:\n" \
  "s_waitcnt lgkmcnt(0)\n" \
  "s_cmp_eq_u32 s73, 1\n" \
  "s_cbranch_scc1 LOOP00_%=\n" \
  "s_branch LDONE_%=\n" \
  "LMF00_%=:\n" \
  "v_mbcnt_lo_u32_b32 v130, -1, 0\n" \
  "v_mbcnt_hi_u32_b32 v130, -1, v130\n" \
  "v_lshlrev_b32 v130, 4, v130\n" \
  "v_mov_b32 v128, 0\n" \
  "ds_read_b128 v[132:135], v130 offset:0\n" \
  "ds_read_b128 v[136:139], v130 offset:1024\n" \
  "ds_read_b128 v[140:143], v130 offset:2048\n" \
  "ds_read_b128 v[144:147], v130 offset:3072\n" \
  "ds_read_b128 v[148:151], v130 offset:4096\n" \
  "s_waitcnt lgkmcnt(0)\n" \
  "v_max3_u32 v128, v128, v132, v133\n" \
  "v_max3_u32 v128, v128, v134, v135\n" \
  "v_max3_u32 v128, v128, v136, v137\n" \
  "v_max3_u32 v128, v128, v138, v139\n" \
  "v_max3_u32 v128, v128, v140, v141\n" \
  "v_max3_u32 v128, v128, v142, v143\n" \
  "v_max3_u32 v128, v128, v144, v145\n" \
  "v_max3_u32 v128, v128, v146, v147\n" \
  "v_max3_u32 v128, v128, v148, v149\n" \
  "v_max3_u32 v128, v128, v150, v151\n" \
  "ds_read_b128 v[132:135], v130 offset:5120\n" \
  "ds_read_b128 v[136:139], v130 offset:6144\n" \
  "ds_read_b128 v[140:143], v130 offset:7168\n" \
  "ds_read_b128 v[144:147], v130 offset:8192\n" \
  "ds_read_b128 v[148:151], v130 offset:9216\n" \
  "s_waitcnt lgkmcnt(0)\n" \
  "v_max3_u32 v128, v128, v132, v133\n" \
  "v_max3_u32 v128, v128, v134, v135\n" \
  "v_max3_u32 v128, v128, v136, v137\n" \
  "v_max3_u32 v128, v128, v138, v139\n" \
  "v_max3_u32 v128, v128, v140, v141\n" \
  "v_max3_u32 v128, v128, v142, v143\n" \
  "v_max3_u32 v128, v128, v144, v145\n" \
  "v_max3_u32 v128, v128, v146, v147\n" \
  "v_max3_u32 v128, v128, v148, v149\n" \
  "v_max3_u32 v128, v128, v150, v151\n" \
  "v_cmp_eq_u32 vcc, -1, v128\n" \
  "s_cbranch_vccz LMOK00_%=\n" \
  "s_cmp_gt_u32 s58, 4000\n" \
  "s_cbranch_scc1 LMOK00_%=\n" \
  "s_add_u32 s58, s58, 1\n" \
  "s_sub_u32 s64, s64, 1\n" \
  "s_mov_b32 s66, s65\n" \
  "s_mov_b32 s68, s69\n" \
  "s_waitcnt vmcnt(0) lgkmcnt(0)\n" \
  "s_barrier\n" \
  "s_branch LOOP00_%=\n" \
  "LMX00_%=:\n" \
  "v_cvt_pk_bf16_f32 v155, v152, v153\n" \
  "v_pk_min_u16 v155, v155, -2 op_sel_hi:[1,0]\n" \
  "s_add_u32 s71, s65, 32768\n" \
  "buffer_store_dword v155, v13, s[36:39], s71 offen sc1\n" \
  "v_mov_b32 v154, 0x7fd57fd5\n" \
  "s_branch LMY00_%=\n" \
  "LSF00_%=:\n" \
  "s_getpc_b64 s[78:79]\n" \
  "s_branch LSF_%=\n" \
  "s_branch LSOK00_%=\n" \
  "LSR00_%=:\n" \
  "v_mov_b32 v188, 0\n" \
  "s_add_u32 s59, s59, 1\n" \
  "s_barrier\n" \
  "ds_write_b32 v33, v188\n" \
  "s_waitcnt vmcnt(0) lgkmcnt(0)\n" \
  "s_branch LB00_%=\n" \
  "LSTART01_%=:\n" \
  "LOOP01_%=:\n" \
  "v_readlane_b32 s70, v27, s64\n" \
  "s_mov_b32 s65, s66\n" \
  "s_mov_b32 s69, s68\n" \
  "s_add_u32 s64, s64, 1\n" \
  "s_barrier\n" \
  "s_mov_b32 m0, 28672\n" \
  "s_add_u32 s72, s70, 4096\n" \
  "buffer_load_dwordx4 v3, s[36:39], s70 offen sc1 lds\n" \
  "buffer_load_dwordx4 v3, s[36:39], s70 offen offset:1024 sc1 lds\n" \
  "buffer_load_dwordx4 v3, s[36:39], s70 offen offset:2048 sc1 lds\n" \
  "buffer_load_dwordx4 v3, s[36:39], s70 offen offset:3072 sc1 lds\n" \
  "s_mov_b32 m0, 32768\n" \
  "s_nop 0\n" \
  "buffer_load_dwordx4 v3, s[36:39], s72 offen sc1 lds\n" \
  "buffer_load_dwordx4 v3, s[36:39], s72 offen offset:1024 sc1 lds\n" \
  "buffer_load_dwordx4 v3, s[36:39], s72 offen offset:2048 sc1 lds\n" \
  "buffer_load_dwordx4 v3, s[36:39], s72 offen offset:3072 sc1 lds\n" \
  "v_mov_b32 v176, 0\n" \
  "v_mov_b32 v177, 0\n" \
  "v_mov_b32 v178, 0\n" \
  "v_mov_b32 v179, 0\n" \
  "v_mov_b32 v180, 0\n" \
  "v_mov_b32 v181, 0\n" \
  "v_mov_b32 v182, 0\n" \
  "v_mov_b32 v183, 0\n" \
  "v_readlane_b32 s66, v25, s64\n" \
  "v_readlane_b32 s68, v26, s64\n" \
  "s_waitcnt lgkmcnt(0)\n" \
  "LM01_%=:\n" \
  "s_barrier\n" \
  "ds_read_b128 v[112:115], v5\n" \
  "ds_read_b128 v[116:119], v5 offset:512\n" \
  "s_barrier\n" \
  "ds_read_b128 v[120:123], v5 offset:1024\n" \
  "ds_read_b128 v[124:127], v5 offset:1536\n" \
  "s_waitcnt lgkmcnt(3)\n" \
  "v_smfmac_f32_16x16x64_bf16 v[176:179], v[112:115], a[0:7], v0\n" \
  "v_smfmac_f32_16x16x64_bf16 v[180:183], v[112:115], a[160:167], v0\n" \
  "s_waitcnt lgkmcnt(2)\n" \
  "v_smfmac_f32_16x16x64_bf16 v[176:179], v[116:119], a[8:15], v0\n" \
  "s_barrier\n" \
  "ds_read_b128 v[128:131], v5 offset:2048\n" \
  "v_smfmac_f32_16x16x64_bf16 v[180:183], v[116:119], a[168:175], v0\n" \
  "s_waitcnt lgkmcnt(2)\n" \
  "ds_read_b128 v[132:135], v5 offset:2560\n" \
  "v_smfmac_f32_16x16x64_bf16 v[176:179], v[120:123], a[16:23], v0\n" \
  "ds_read_b128 v[136:139], v5 offset:3072\n" \
  "v_smfmac_f32_16x16x64_bf16 v[180:183], v[120:123], a[176:183], v0\n" \
  "s_waitcnt lgkmcnt(3)\n" \
  "ds_read_b128 v[140:143], v5 offset:3584\n" \
  "v_smfmac_f32_16x16x64_bf16 v[176:179], v[124:127], a[24:31], v0\n" \
  "s_barrier\n" \
  "ds_read_b128 v[144:147], v5 offset:4096\n" \
  "v_smfmac_f32_16x16x64_bf16 v[180:183], v[124:127], a[184:191], v0\n" \
  "s_waitcnt lgkmcnt(4)\n" \
  "ds_read_b128 v[148:151], v5 offset:4608\n" \
  "v_smfmac_f32_16x16x64_bf16 v[176:179], v[128:131], a[32:39], v0\n" \
  "ds_read_b128 v[152:155], v5 offset:5120\n" \
  "v_smfmac_f32_16x16x64_bf16 v[180:183], v[128:131], a[192:199], v0\n" \
  "s_waitcnt lgkmcnt(5)\n" \
  "ds_read_b128 v[156:159], v5 offset:5632\n" \
  "v_smfmac_f32_16x16x64_bf16 v[176:179], v[132:135], a[40:47], v0\n" \
  "s_barrier\n" \
  "ds_read_b128 v[160:163], v5 offset:6144\n" \
  "v_smfmac_f32_16x16x64_bf16 v[180:183], v[132:135], a[200:207], v0\n" \
  "s_waitcnt lgkmcnt(6)\n" \
  "ds_read_b128 v[164:167], v5 offset:6656\n" \
  "v_smfmac_f32_16x16x64_bf16 v[176:179], v[136:139], a[48:55], v0\n" \
  "ds_read_b128 v[168:171], v5 offset:7168\n" \
  "v_smfmac_f32_16x16x64_bf16 v[180:183], v[136:139], a[208:215], v0\n" \
  "s_waitcnt vmcnt(8)\n" \
  "s_waitcnt lgkmcnt(7)\n" \
  "ds_read_b128 v[172:175], v5 offset:7680\n" \
  "v_smfmac_f32_16x16x64_bf16 v[176:179], v[140:143], a[56:63], v0\n" \
  "s_barrier\n" \
  "ds_read2_b32 v[112:113], v6 offset0:0 offset1:32\n" \
  "v_smfmac_f32_16x16x64_bf16 v[180:183], v[140:143], a[216:223], v0\n" \
  "s_waitcnt lgkmcnt(8)\n" \
  "ds_read2_b32 v[114:115], v6 offset0:64 offset1:96\n" \
  "v_smfmac_f32_16x16x64_bf16 v[176:179], v[144:147], a[64:71], v0\n" \
  "s_waitcnt lgkmcnt(8)\n" \
  "ds_read2_b32 v[116:117], v6 offset0:128 offset1:160\n" \
  "v_smfmac_f32_16x16x64_bf16 v[176:179], v[148:151], a[72:79], v0\n" \
  "s_waitcnt lgkmcnt(8)\n" \
  "ds_read2_b32 v[118:119], v6 offset0:192 offset1:224\n" \
  "v_smfmac_f32_16x16x64_bf16 v[176:179], v[152:155], a[80:87], v0\n" \
  "s_waitcnt lgkmcnt(8)\n" \
  "ds_read2_b32 v[120:121], v7 offset0:0 offset1:32\n" \
  "v_smfmac_f32_16x16x64_bf16 v[176:179], v[156:159], a[88:95], v0\n" \
  "s_waitcnt lgkmcnt(8)\n" \
  "ds_read2_b32 v[122:123], v7 offset0:64 offset1:96\n" \
  "v_smfmac_f32_16x16x64_bf16 v[176:179], v[160:163], a[96:103], v0\n" \
  "s_waitcnt lgkmcnt(8)\n" \
  "ds_read2_b32 v[124:125], v7 offset0:128 offset1:160\n" \
  "v_smfmac_f32_16x16x64_bf16 v[176:179], v[164:167], a[104:111], v0\n" \
  "s_waitcnt lgkmcnt(8)\n" \
  "ds_read2_b32 v[126:127], v7 offset0:192 offset1:224\n" \
  "v_smfmac_f32_16x16x64_bf16 v[176:179], v[168:171], a[112:119], v0\n" \
  "s_waitcnt lgkmcnt(8)\n" \
  "ds_read_b128 v[88:91], v1\n" \
  "v_smfmac_f32_16x16x64_bf16 v[176:179], v[172:175], a[120:127], v0\n" \
  "s_waitcnt lgkmcnt(7)\n" \
  "ds_read_b128 v[92:95], v1 offset:3072\n" \
  "v_smfmac_f32_16x16x64_bf16 v[176:179], v[112:115], a[128:135], v0\n" \
  "s_waitcnt lgkmcnt(6)\n" \
  "ds_read_b128 v[36:39], v1 offset:6144\n" \
  "v_smfmac_f32_16x16x64_bf16 v[176:179], v[116:119], a[136:143], v0\n" \
  "s_waitcnt lgkmcnt(5)\n" \
  "ds_read_b128 v[100:103], v1 offset:9216\n" \
  "v_smfmac_f32_16x16x64_bf16 v[176:179], v[120:123], a[144:151], v0\n" \
  "s_waitcnt lgkmcnt(4)\n" \
  "ds_read_b64 v[104:105], v2\n" \
  "v_smfmac_f32_16x16x64_bf16 v[176:179], v[124:127], a[152:159], v0\n" \
  "ds_read_b64 v[106:107], v2 offset:3072\n" \
  "v_smfmac_f32_16x16x64_bf16 v[180:183], v[144:147], a[224:231], v0\n" \
  "ds_read_b64 v[108:109], v2 offset:6144\n" \
  "v_smfmac_f32_16x16x64_bf16 v[180:183], v[148:151], a[232:239], v0\n" \
  "ds_read_b64 v[110:111], v2 offset:9216\n" \
  "v_cvt_pk_bf16_f32 v184, v176, v177\n" \
  "v_smfmac_f32_16x16x64_bf16 v[180:183], v[152:155], a[240:247], v0\n" \
  "v_cvt_pk_bf16_f32 v185, v178, v179\n" \
  "v_smfmac_f32_16x16x64_bf16 v[180:183], v[156:159], a[248:255], v0\n" \
  "ds_write_b64 v8, v[184:185]\n" \
  "v_add_f32 v186, v176, v177\n" \
  "v_smfmac_f32_16x16x64_bf16 v[180:183], v[160:163], v[192:199], v0\n" \
  "v_add_f32 v187, v178, v179\n" \
  "v_smfmac_f32_16x16x64_bf16 v[180:183], v[164:167], v[200:207], v0\n" \
  "v_cmp_u_f32 vcc, v186, v187\n" \
  "v_smfmac_f32_16x16x64_bf16 v[180:183], v[168:171], v[208:215], v0\n" \
  "v_smfmac_f32_16x16x64_bf16 v[180:183], v[172:175], v[216:223], v0\n" \
  "s_cbranch_vccnz LMF01_%=\n" \
  "LMOK01_%=:\n" \
  "v_smfmac_f32_16x16x64_bf16 v[180:183], v[112:115], v[224:231], v0\n" \
  "s_waitcnt lgkmcnt(0)\n" \
  "s_barrier\n" \
  "ds_read_b64_tr_b16 v[128:129], v10\n" \
  "v_smfmac_f32_16x16x64_bf16 v[180:183], v[116:119], v[232:239], v0\n" \
  "ds_read_b64_tr_b16 v[130:131], v10 offset:32\n" \
  "ds_read_b64_tr_b16 v[132:133], v10 offset:512\n" \
  "v_smfmac_f32_16x16x64_bf16 v[180:183], v[120:123], v[240:247], v0\n" \
  "ds_read_b64_tr_b16 v[134:135], v10 offset:544\n" \
  "ds_read_b64_tr_b16 v[136:137], v10 offset:1024\n" \
  "v_smfmac_f32_16x16x64_bf16 v[180:183], v[124:127], v[248:255], v0\n" \
  "s_nop 0\n" \
  "ds_read_b64_tr_b16 v[138:139], v10 offset:1056\n" \
  "ds_read_b64_tr_b16 v[140:141], v10 offset:1536\n" \
  "ds_read_b64_tr_b16 v[142:143], v10 offset:1568\n" \
  "v_mov_b32 v31, v31\n" \
  "s_nop 2\n" \
  "v_cvt_pk_bf16_f32 v186, v180, v181\n" \
  "v_cvt_pk_bf16_f32 v187, v182, v183\n" \
  "ds_write_b64 v9, v[186:187]\n" \
  "s_waitcnt lgkmcnt(7)\n" \
  "v_mfma_f32_16x16x32_bf16 v[188:191], v[88:91], v[128:131], 0\n" \
  "s_waitcnt lgkmcnt(5)\n" \
  "v_mfma_f32_16x16x32_bf16 v[188:191], v[92:95], v[132:135], v[188:191]\n" \
  "s_waitcnt lgkmcnt(3)\n" \
  "v_mfma_f32_16x16x32_bf16 v[188:191], v[36:39], v[136:139], v[188:191]\n" \
  "s_waitcnt lgkmcnt(1)\n" \
  "v_mfma_f32_16x16x32_bf16 v[188:191], v[100:103], v[140:143], v[188:191]\n" \
  "s_waitcnt lgkmcnt(0)\n" \
  "s_barrier\n" \
  "ds_read_b64_tr_b16 v[144:145], v11\n" \
  "ds_read_b64_tr_b16 v[146:147], v11 offset:512\n" \
  "ds_read_b64_tr_b16 v[148:149], v11 offset:1024\n" \
  "ds_read_b64_tr_b16 v[150:151], v11 offset:1536\n" \
  "v_mov_b32 v31, v31\n" \
  "s_nop 2\n" \
  "v_mov_b32 v31, v31\n" \
  "s_nop 2\n" \
  "v_mov_b32 v31, v31\n" \
  "s_nop 0\n" \
  "s_waitcnt lgkmcnt(2)\n" \
  "v_mfma_f32_16x16x32_bf16 v[188:191], v[104:107], v[144:147], v[188:191]\n" \
  "s_waitcnt lgkmcnt(0)\n" \
  "v_mfma_f32_16x16x32_bf16 v[188:191], v[108:111], v[148:151], v[188:191]\n" \
  "s_nop 0\n" \
  "ds_read_b64_tr_b16 v[128:129], v12\n" \
  "s_nop 1\n" \
  "v_mov_b32 v31, v31\n" \
  "s_nop 2\n" \
  "v_cndmask_b32 v152, v190, v188, s[20:21]\n" \
  "v_cndmask_b32 v153, v191, v189, s[20:21]\n" \
  "v_mul_f32 v152, 0x3dd53b94, v152\n" \
  "v_mul_f32 v153, 0x3dd53b94, v153\n" \
  "v_max_f32 v156, |v152|, |v153|\n" \
  "v_cmp_lt_f32 vcc, 0x42800000, v156\n" \
  "v_exp_f32 v154, v152\n" \
  "v_exp_f32 v155, v153\n" \
  "s_cbranch_vccnz LMX01_%=\n" \
  "v_cvt_pk_bf16_f32 v154, v154, v155\n" \
  "LMY01_%=:\n" \
  "buffer_store_dword v154, v13, s[36:39], s65 offen sc1\n" \
  "s_waitcnt lgkmcnt(0)\n" \
  "buffer_store_dwordx2 v[128:129], v14, s[36:39], s69 offen sc1\n" \
  "s_mov_b32 s67, s66\n" \
  "s_cmp_eq_u32 s64, 64\n" \
  "s_cbranch_scc1 LDONE_%=\n" \
  "LB01_%=:\n" \
  "s_barrier\n" \
  "buffer_load_dwordx4 v[112:115], v15, s[36:39], s65 offen sc1\n" \
  "s_barrier\n" \
  "buffer_load_dwordx4 v[116:119], v15, s[36:39], s65 offen offset:128 sc1\n" \
  "s_barrier\n" \
  "buffer_load_dwordx4 v[120:123], v16, s[36:39], s65 offen sc1\n" \
  "s_barrier\n" \
  "buffer_load_dwordx4 v[124:127], v16, s[36:39], s65 offen offset:512 sc1\n" \
  "s_barrier\n" \
  "buffer_load_dwordx4 v[128:131], v16, s[36:39], s65 offen offset:1024 sc1\n" \
  "s_waitcnt vmcnt(7)\n" \
  "s_barrier\n" \
  "buffer_load_dwordx4 v[132:135], v16, s[36:39], s65 offen offset:1536 sc1\n" \
  "ds_read_b128 v[40:43], v19\n" \
  "ds_read_b128 v[44:47], v19 offset:512\n" \
  "ds_read_b128 v[48:51], v19 offset:1024\n" \
  "ds_read_b128 v[52:55], v19 offset:1536\n" \
  "s_mov_b64 exec, s[46:47]\n" \
  "ds_read_b128 v[56:59], v20\n" \
  "ds_read_b128 v[60:63], v20 offset:128\n" \
  "ds_read_b128 v[64:67], v20 offset:256\n" \
  "ds_read_b128 v[68:71], v20 offset:384\n" \
  "s_mov_b64 exec, -1\n" \
  "s_nop 15\n" \
  "s_nop 1\n" \
  "buffer_load_dwordx4 v[136:139], v17, s[36:39], s65 offen sc1\n" \
  "buffer_load_dwordx4 v[140:143], v18, s[36:39], s65 offen sc1\n" \
  "s_waitcnt lgkmcnt(0)\n" \
  "s_getreg_b32 s71, hwreg(HW_REG_IB_STS, 0, 4)\n" \
  "s_cmp_le_u32 s71, 6\n" \
  "v_mfma_f32_16x16x32_bf16 v[144:147], v[72:75], v[72:75], 0\n" \
  "s_cbranch_scc1 LHXS01_%=\n" \
  "s_getreg_b32 s71, hwreg(HW_REG_IB_STS, 0, 4)\n" \
  "s_cmp_le_u32 s71, 6\n" \
  "v_mfma_f32_16x16x32_bf16 v[144:147], v[72:75], v[72:75], 0\n" \
  "s_cbranch_scc1 LHXS01_%=\n" \
  "s_getreg_b32 s71, hwreg(HW_REG_IB_STS, 0, 4)\n" \
  "s_cmp_le_u32 s71, 6\n" \
  "v_mfma_f32_16x16x32_bf16 v[144:147], v[72:75], v[72:75], 0\n" \
  "s_cbranch_scc1 LHXS01_%=\n" \
  "s_getreg_b32 s71, hwreg(HW_REG_IB_STS, 0, 4)\n" \
  "s_cmp_le_u32 s71, 6\n" \
  "v_mfma_f32_16x16x32_bf16 v[144:147], v[72:75], v[72:75], 0\n" \
  "s_cbranch_scc1 LHXS01_%=\n" \
  "s_getreg_b32 s71, hwreg(HW_REG_IB_STS, 0, 4)\n" \
  "s_cmp_le_u32 s71, 6\n" \
  "v_mfma_f32_16x16x32_bf16 v[144:147], v[72:75], v[72:75], 0\n" \
  "s_cbranch_scc1 LHXS01_%=\n" \
  "s_getreg_b32 s71, hwreg(HW_REG_IB_STS, 0, 4)\n" \
  "s_cmp_le_u32 s71, 6\n" \
  "v_mfma_f32_16x16x32_bf16 v[144:147], v[72:75], v[72:75], 0\n" \
  "s_cbranch_scc1 LHXS01_%=\n" \
  "s_getreg_b32 s71, hwreg(HW_REG_IB_STS, 0, 4)\n" \
  "s_cmp_le_u32 s71, 6\n" \
  "v_mfma_f32_16x16x32_bf16 v[144:147], v[72:75], v[72:75], 0\n" \
  "s_cbranch_scc1 LHXS01_%=\n" \
  "s_getreg_b32 s71, hwreg(HW_REG_IB_STS, 0, 4)\n" \
  "s_cmp_le_u32 s71, 6\n" \
  "v_mfma_f32_16x16x32_bf16 v[144:147], v[72:75], v[72:75], 0\n" \
  "s_cbranch_scc1 LHXS01_%=\n" \
  "s_getreg_b32 s71, hwreg(HW_REG_IB_STS, 0, 4)\n" \
  "s_cmp_le_u32 s71, 6\n" \
  "v_mfma_f32_16x16x32_bf16 v[144:147], v[72:75], v[72:75], 0\n" \
  "s_cbranch_scc1 LHXS01_%=\n" \
  "s_getreg_b32 s71, hwreg(HW_REG_IB_STS, 0, 4)\n" \
  "s_cmp_le_u32 s71, 6\n" \
  "v_mfma_f32_16x16x32_bf16 v[144:147], v[72:75], v[72:75], 0\n" \
  "s_cbranch_scc1 LHXS01_%=\n" \
  "s_getreg_b32 s71, hwreg(HW_REG_IB_STS, 0, 4)\n" \
  "s_cmp_le_u32 s71, 6\n" \
  "v_mfma_f32_16x16x32_bf16 v[144:147], v[72:75], v[72:75], 0\n" \
  "s_cbranch_scc1 LHXS01_%=\n" \
  "s_getreg_b32 s71, hwreg(HW_REG_IB_STS, 0, 4)\n" \
  "s_cmp_le_u32 s71, 6\n" \
  "v_mfma_f32_16x16x32_bf16 v[144:147], v[72:75], v[72:75], 0\n" \
  "s_cbranch_scc1 LHXS01_%=\n" \
  "s_getreg_b32 s71, hwreg(HW_REG_IB_STS, 0, 4)\n" \
  "s_cmp_le_u32 s71, 6\n" \
  "v_mfma_f32_16x16x32_bf16 v[144:147], v[72:75], v[72:75], 0\n" \
  "s_cbranch_scc1 LHXS01_%=\n" \
  "s_getreg_b32 s71, hwreg(HW_REG_IB_STS, 0, 4)\n" \
  "s_cmp_le_u32 s71, 6\n" \
  "v_mfma_f32_16x16x32_bf16 v[144:147], v[72:75], v[72:75], 0\n" \
  "s_cbranch_scc1 LHXS01_%=\n" \
  "s_getreg_b32 s71, hwreg(HW_REG_IB_STS, 0, 4)\n" \
  "s_cmp_le_u32 s71, 6\n" \
  "v_mfma_f32_16x16x32_bf16 v[144:147], v[72:75], v[72:75], 0\n" \
  "s_cbranch_scc1 LHXS01_%=\n" \
  "s_getreg_b32 s71, hwreg(HW_REG_IB_STS, 0, 4)\n" \
  "s_cmp_le_u32 s71, 6\n" \
  "v_mfma_f32_16x16x32_bf16 v[144:147], v[72:75], v[72:75], 0\n" \
  "s_cbranch_scc1 LHXS01_%=\n" \
  "s_getreg_b32 s71, hwreg(HW_REG_IB_STS, 0, 4)\n" \
  "s_cmp_le_u32 s71, 6\n" \
  "v_mfma_f32_16x16x32_bf16 v[144:147], v[72:75], v[72:75], 0\n" \
  "s_cbranch_scc1 LHXS01_%=\n" \
  "s_getreg_b32 s71, hwreg(HW_REG_IB_STS, 0, 4)\n" \
  "s_cmp_le_u32 s71, 6\n" \
  "v_mfma_f32_16x16x32_bf16 v[144:147], v[72:75], v[72:75], 0\n" \
  "s_cbranch_scc1 LHXS01_%=\n" \
  "s_getreg_b32 s71, hwreg(HW_REG_IB_STS, 0, 4)\n" \
  "s_cmp_le_u32 s71, 6\n" \
  "v_mfma_f32_16x16x32_bf16 v[144:147], v[72:75], v[72:75], 0\n" \
  "s_cbranch_scc1 LHXS01_%=\n" \
  "s_getreg_b32 s71, hwreg(HW_REG_IB_STS, 0, 4)\n" \
  "s_cmp_le_u32 s71, 6\n" \
  "v_mfma_f32_16x16x32_bf16 v[144:147], v[72:75], v[72:75], 0\n" \
  "s_cbranch_scc1 LHXS01_%=\n" \
  "s_waitcnt vmcnt(6)\n" \
  "LHXS01_%=:\n" \
  "LS201_%=:\n" \
  "v_dot2_f32_bf16 v176, v112, s22, 0\n" \
  "v_dot2_f32_bf16 v177, v112, s23, 0\n" \
  "v_dot2_f32_bf16 v178, v113, s22, 0\n" \
  "v_dot2_f32_bf16 v179, v113, s23, 0\n" \
  "v_dot2_f32_bf16 v180, v114, s22, 0\n" \
  "v_dot2_f32_bf16 v181, v114, s23, 0\n" \
  "v_dot2_f32_bf16 v182, v115, s22, 0\n" \
  "v_dot2_f32_bf16 v183, v115, s23, 0\n" \
  "v_dot2_f32_bf16 v176, v116, s22, v176\n" \
  "v_dot2_f32_bf16 v177, v116, s23, v177\n" \
  "v_dot2_f32_bf16 v178, v117, s22, v178\n" \
  "v_dot2_f32_bf16 v179, v117, s23, v179\n" \
  "v_dot2_f32_bf16 v180, v118, s22, v180\n" \
  "v_dot2_f32_bf16 v181, v118, s23, v181\n" \
  "v_dot2_f32_bf16 v182, v119, s22, v182\n" \
  "v_dot2_f32_bf16 v183, v119, s23, v183\n" \
  "v_exp_f32 v176, v176\n" \
  "v_exp_f32 v177, v177\n" \
  "v_exp_f32 v178, v178\n" \
  "v_exp_f32 v179, v179\n" \
  "s_waitcnt vmcnt(5)\n" \
  "v_mfma_f32_16x16x32_bf16 v[160:163], v[56:59], v[120:123], 0\n" \
  "v_exp_f32 v180, v180\n" \
  "v_mfma_f32_16x16x32_bf16 v[144:147], v[40:43], v[120:123], 0\n" \
  "v_exp_f32 v181, v181\n" \
  "v_exp_f32 v182, v182\n" \
  "v_exp_f32 v183, v183\n" \
  "v_cvt_pk_bf16_f32 v184, v176, v177\n" \
  "v_cvt_pk_bf16_f32 v185, v178, v179\n" \
  "v_cvt_pk_bf16_f32 v186, v180, v181\n" \
  "v_cvt_pk_bf16_f32 v187, v182, v183\n" \
  "s_nop 1\n" \
  "v_mfma_f32_16x16x32_bf16 v[160:163], v[72:75], v[184:187], v[160:163]\n" \
  "v_mfma_f32_16x16x32_bf16 v[164:167], v[76:79], v[184:187], 0\n" \
  "s_waitcnt vmcnt(4)\n" \
  "v_mfma_f32_16x16x32_bf16 v[164:167], v[60:63], v[124:127], v[164:167]\n" \
  "v_rcp_f32 v188, v162\n" \
  "v_mfma_f32_16x16x32_bf16 v[148:151], v[44:47], v[124:127], 0\n" \
  "v_mul_f32 v144, v188, v144\n" \
  "v_mul_f32 v145, v188, v145\n" \
  "v_mfma_f32_16x16x32_bf16 v[168:171], v[80:83], v[184:187], 0\n" \
  "s_waitcnt vmcnt(3)\n" \
  "v_rcp_f32 v189, v166\n" \
  "v_mfma_f32_16x16x32_bf16 v[168:171], v[64:67], v[128:131], v[168:171]\n" \
  "v_mul_f32 v146, v188, v146\n" \
  "v_mul_f32 v147, v188, v147\n" \
  "v_mfma_f32_16x16x32_bf16 v[152:155], v[48:51], v[128:131], 0\n" \
  "v_mul_f32 v160, v188, v160\n" \
  "v_mul_f32 v161, v188, v161\n" \
  "v_mfma_f32_16x16x32_bf16 v[172:175], v[84:87], v[184:187], 0\n" \
  "s_waitcnt vmcnt(2)\n" \
  "v_rcp_f32 v190, v170\n" \
  "v_mfma_f32_16x16x32_bf16 v[172:175], v[68:71], v[132:135], v[172:175]\n" \
  "v_cndmask_b32 v167, v188, v189, s[28:29]\n" \
  "v_cndmask_b32 v167, v167, v190, s[26:27]\n" \
  "v_mfma_f32_16x16x32_bf16 v[156:159], v[52:55], v[132:135], 0\n" \
  "v_fmac_f32 v144, v189, v148\n" \
  "v_fmac_f32 v145, v189, v149\n" \
  "v_fmac_f32 v146, v189, v150\n" \
  "v_fmac_f32 v147, v189, v151\n" \
  "v_rcp_f32 v191, v174\n" \
  "v_fmac_f32 v160, v189, v164\n" \
  "v_cndmask_b32 v167, v167, v191, s[24:25]\n" \
  "v_pk_mul_f32 v[176:177], v[176:177], v[166:167] op_sel:[0,1] op_sel_hi:[1,1]\n" \
  "v_pk_mul_f32 v[178:179], v[178:179], v[166:167] op_sel:[0,1] op_sel_hi:[1,1]\n" \
  "v_pk_mul_f32 v[180:181], v[180:181], v[166:167] op_sel:[0,1] op_sel_hi:[1,1]\n" \
  "v_pk_mul_f32 v[182:183], v[182:183], v[166:167] op_sel:[0,1] op_sel_hi:[1,1]\n" \
  "v_cmp_nlt_f32 vcc, 0x0d800000, v167\n" \
  "v_cvt_pk_bf16_f32 v184, v176, v177\n" \
  "v_cvt_pk_bf16_f32 v185, v178, v179\n" \
  "v_cvt_pk_bf16_f32 v186, v180, v181\n" \
  "v_cvt_pk_bf16_f32 v187, v182, v183\n" \
  "v_fmac_f32 v161, v189, v165\n" \
  "v_pk_fma_f32 v[160:161], v[168:169], v[190:191], v[160:161] op_sel:[0,0,0] op_sel_hi:[1,0,1]\n" \
  "v_pk_fma_f32 v[160:161], v[172:173], v[190:191], v[160:161] op_sel:[0,1,0] op_sel_hi:[1,1,1]\n" \
  "v_pk_fma_f32 v[144:145], v[152:153], v[190:191], v[144:145] op_sel:[0,0,0] op_sel_hi:[1,0,1]\n" \
  "v_pk_fma_f32 v[146:147], v[154:155], v[190:191], v[146:147] op_sel:[0,0,0] op_sel_hi:[1,0,1]\n" \
  "v_pk_fma_f32 v[144:145], v[156:157], v[190:191], v[144:145] op_sel:[0,1,0] op_sel_hi:[1,1,1]\n" \
  "v_pk_fma_f32 v[146:147], v[158:159], v[190:191], v[146:147] op_sel:[0,1,0] op_sel_hi:[1,1,1]\n" \
  "s_waitcnt vmcnt(0)\n" \
  "v_mfma_f32_16x16x32_bf16 v[160:163], v[140:143], v[184:187], v[160:163]\n" \
  "s_mov_b64 s[80:81], vcc\n" \
  "v_mfma_f32_16x16x32_bf16 v[144:147], v[136:139], v[184:187], v[144:147]\n" \
  "s_mov_b64 exec, s[30:31]\n" \
  "s_nop 2\n" \
  "ds_write_b64 v22, v[160:161]\n" \
  "v_cmp_u_f32 s[78:79], v160, v161\n" \
  "s_nop 1\n" \
  "ds_write2_b64 v21, v[144:145], v[146:147] offset1:16\n" \
  "v_add_f32 v188, v144, v145\n" \
  "v_add_f32 v189, v146, v147\n" \
  "v_cmp_u_f32 vcc, v188, v189\n" \
  "s_or_b64 s[80:81], s[80:81], s[78:79]\n" \
  "s_or_b64 vcc, vcc, s[80:81]\n" \
  "s_cbranch_vccnz LSF01_%=\n" \
  "LSOK01_%=:\n" \
  "s_mov_b64 exec, -1\n" \
  "s_waitcnt lgkmcnt(0)\n" \
  "s_barrier\n" \
  "ds_read_b32 v188, v33\n" \
  "s_waitcnt lgkmcnt(0)\n" \
  "v_cmp_ne_u32 vcc, 0, v188\n" \
  "s_cbranch_vccnz LSR01_%=\n" \
  "s_nop 15\n" \
  "s_nop 3\n" \
  "s_mov_b32 m0, 47104\n" \
  "s_add_u32 s72, s68, 0\n" \
  "buffer_load_dwordx4 v4, s[36:39], s72 offen sc1 lds\n" \
  "buffer_load_dwordx4 v4, s[36:39], s72 offen offset:1024 sc1 lds\n" \
  "buffer_load_dwordx4 v4, s[36:39], s72 offen offset:2048 sc1 lds\n" \
  "s_mov_b32 m0, 50176\n" \
  "s_add_u32 s72, s68, 16384\n" \
  "buffer_load_dwordx4 v4, s[36:39], s72 offen sc1 lds\n" \
  "buffer_load_dwordx4 v4, s[36:39], s72 offen offset:1024 sc1 lds\n" \
  "buffer_load_dwordx4 v4, s[36:39], s72 offen offset:2048 sc1 lds\n" \
  "LNL01_%=:\n" \
  "s_waitcnt lgkmcnt(0)\n" \
  "s_cmp_eq_u32 s73, 1\n" \
  "s_cbranch_scc1 LOOP01_%=\n" \
  "s_branch LDONE_%=\n" \
  "LMF01_%=:\n" \
  "v_mbcnt_lo_u32_b32 v130, -1, 0\n" \
  "v_mbcnt_hi_u32_b32 v130, -1, v130\n" \
  "v_lshlrev_b32 v130, 4, v130\n" \
  "v_mov_b32 v128, 0\n" \
  "ds_read_b128 v[132:135], v130 offset:0\n" \
  "ds_read_b128 v[136:139], v130 offset:1024\n" \
  "ds_read_b128 v[140:143], v130 offset:2048\n" \
  "ds_read_b128 v[144:147], v130 offset:3072\n" \
  "ds_read_b128 v[148:151], v130 offset:4096\n" \
  "s_waitcnt lgkmcnt(0)\n" \
  "v_max3_u32 v128, v128, v132, v133\n" \
  "v_max3_u32 v128, v128, v134, v135\n" \
  "v_max3_u32 v128, v128, v136, v137\n" \
  "v_max3_u32 v128, v128, v138, v139\n" \
  "v_max3_u32 v128, v128, v140, v141\n" \
  "v_max3_u32 v128, v128, v142, v143\n" \
  "v_max3_u32 v128, v128, v144, v145\n" \
  "v_max3_u32 v128, v128, v146, v147\n" \
  "v_max3_u32 v128, v128, v148, v149\n" \
  "v_max3_u32 v128, v128, v150, v151\n" \
  "ds_read_b128 v[132:135], v130 offset:5120\n" \
  "ds_read_b128 v[136:139], v130 offset:6144\n" \
  "ds_read_b128 v[140:143], v130 offset:7168\n" \
  "ds_read_b128 v[144:147], v130 offset:8192\n" \
  "ds_read_b128 v[148:151], v130 offset:9216\n" \
  "s_waitcnt lgkmcnt(0)\n" \
  "v_max3_u32 v128, v128, v132, v133\n" \
  "v_max3_u32 v128, v128, v134, v135\n" \
  "v_max3_u32 v128, v128, v136, v137\n" \
  "v_max3_u32 v128, v128, v138, v139\n" \
  "v_max3_u32 v128, v128, v140, v141\n" \
  "v_max3_u32 v128, v128, v142, v143\n" \
  "v_max3_u32 v128, v128, v144, v145\n" \
  "v_max3_u32 v128, v128, v146, v147\n" \
  "v_max3_u32 v128, v128, v148, v149\n" \
  "v_max3_u32 v128, v128, v150, v151\n" \
  "v_cmp_eq_u32 vcc, -1, v128\n" \
  "s_cbranch_vccz LMOK01_%=\n" \
  "s_cmp_gt_u32 s58, 4000\n" \
  "s_cbranch_scc1 LMOK01_%=\n" \
  "s_add_u32 s58, s58, 1\n" \
  "s_sub_u32 s64, s64, 1\n" \
  "s_mov_b32 s66, s65\n" \
  "s_mov_b32 s68, s69\n" \
  "s_waitcnt vmcnt(0) lgkmcnt(0)\n" \
  "s_barrier\n" \
  "s_branch LOOP01_%=\n" \
  "LMX01_%=:\n" \
  "v_cvt_pk_bf16_f32 v155, v152, v153\n" \
  "v_pk_min_u16 v155, v155, -2 op_sel_hi:[1,0]\n" \
  "s_add_u32 s71, s65, 32768\n" \
  "buffer_store_dword v155, v13, s[36:39], s71 offen sc1\n" \
  "v_mov_b32 v154, 0x7fd57fd5\n" \
  "s_branch LMY01_%=\n" \
  "LSF01_%=:\n" \
  "s_getpc_b64 s[78:79]\n" \
  "s_branch LSF_%=\n" \
  "s_branch LSOK01_%=\n" \
  "LSR01_%=:\n" \
  "v_mov_b32 v188, 0\n" \
  "s_add_u32 s59, s59, 1\n" \
  "s_barrier\n" \
  "ds_write_b32 v33, v188\n" \
  "s_waitcnt vmcnt(0) lgkmcnt(0)\n" \
  "s_branch LB01_%=\n" \
  "LSTART02_%=:\n" \
  "s_getpc_b64 s[80:81]\n" \
  "LPC02_%=:\n" \
  "s_add_u32 s82, s80, LEA02_%=-LPC02_%=+24\n" \
  "s_addc_u32 s83, s81, 0\n" \
  "s_add_u32 s84, s80, LEB02_%=-LPC02_%=+24\n" \
  "s_addc_u32 s85, s81, 0\n" \
  "LOOP02_%=:\n" \
  "v_readfirstlane_b32 s86, v34\n" \
  "s_add_u32 s71, s64, 1\n" \
  "s_min_u32 s71, s71, 63\n" \
  "s_lshr_b32 s72, s71, 4\n" \
  "s_lshl_b32 s72, s72, 13\n" \
  "s_lshl_b32 s71, s71, 9\n" \
  "s_add_u32 s71, s71, s72\n" \
  "s_mov_b32 s87, s71\n" \
  "s_lshl_b32 s40, s64, 7\n" \
  "s_mov_b32 m0, 0\n" \
  "LWA02_%=:\n" \
  "s_memrealtime s[74:75]\n" \
  "s_memtime s[76:77]\n" \
  "s_waitcnt lgkmcnt(0)\n" \
  "s_sub_i32 s72, s88, s74\n" \
  "s_mul_i32 s72, s72, s90\n" \
  "s_ashr_i32 s72, s72, 8\n" \
  "s_sub_i32 s71, s89, s76\n" \
  "s_min_i32 s71, s71, s72\n" \
  "s_sub_i32 s72, s72, 92\n" \
  "s_max_i32 s71, s71, s72\n" \
  "s_add_u32 s89, s71, s76\n" \
  "s_cmp_gt_i32 s71, 168\n" \
  "s_cbranch_scc1 LWA02_%=\n" \
  "s_cmp_lt_i32 s71, s94\n" \
  "s_cbranch_scc1 LSA02_%=\n" \
  "s_max_i32 s71, s71, 48\n" \
  "s_lshr_b32 s71, s71, 3\n" \
  "s_lshl_b32 s71, s71, 2\n" \
  "s_sub_u32 s78, s82, s71\n" \
  "s_subb_u32 s79, s83, 0\n" \
  "s_setpc_b64 s[78:79]\n" \
  "s_nop 1\n" \
  "s_nop 1\n" \
  "s_nop 1\n" \
  "s_nop 1\n" \
  "s_nop 1\n" \
  "s_nop 1\n" \
  "s_nop 1\n" \
  "s_nop 1\n" \
  "s_nop 1\n" \
  "s_nop 1\n" \
  "s_nop 1\n" \
  "s_nop 1\n" \
  "s_nop 1\n" \
  "s_nop 1\n" \
  "s_nop 1\n" \
  "s_nop 1\n" \
  "LEA02_%=:\n" \
  "buffer_load_dwordx4 v1, s[36:39], s67 offen sc1 lds\n" \
  "buffer_load_dwordx4 v1, s[36:39], s67 offen offset:1024 sc1 lds\n" \
  "buffer_load_dwordx4 v1, s[36:39], s67 offen offset:2048 sc1 lds\n" \
  "buffer_load_dwordx4 v1, s[36:39], s67 offen offset:3072 sc1 lds\n" \
  "s_mov_b32 m0, 4096\n" \
  "s_nop 0\n" \
  "buffer_load_dwordx4 v2, s[36:39], s67 offen sc1 lds\n" \
  "buffer_load_dwordx4 v2, s[36:39], s67 offen offset:1024 sc1 lds\n" \
  "buffer_load_dwordx4 v2, s[36:39], s67 offen offset:2048 sc1 lds\n" \
  "buffer_load_dwordx4 v2, s[36:39], s67 offen offset:3072 sc1 lds\n" \
  "s_mov_b32 m0, 8192\n" \
  "s_nop 0\n" \
  "buffer_load_dwordx4 v3, s[36:39], s67 offen sc1 lds\n" \
  "buffer_load_dwordx4 v3, s[36:39], s67 offen offset:1024 sc1 lds\n" \
  "buffer_load_dword v35, v28, s[36:39], s87 offen sc1\n" \
  "buffer_load_dword v34, v29, s[36:39], s40 offen offset:128 sc1\n" \
  "v_readlane_b32 s70, v27, s64\n" \
  "s_mov_b32 s65, s66\n" \
  "s_mov_b32 s69, s68\n" \
  "s_add_u32 s64, s64, 1\n" \
  "s_barrier\n" \
  "v_mov_b32 v176, 0\n" \
  "v_mov_b32 v177, 0\n" \
  "v_mov_b32 v178, 0\n" \
  "v_mov_b32 v179, 0\n" \
  "v_mov_b32 v180, 0\n" \
  "v_mov_b32 v181, 0\n" \
  "v_mov_b32 v182, 0\n" \
  "v_mov_b32 v183, 0\n" \
  "v_readlane_b32 s66, v25, s64\n" \
  "v_readlane_b32 s68, v26, s64\n" \
  "s_cmp_eq_u32 s34, s64\n" \
  "s_cbranch_scc1 LRT02_%=\n" \
  "s_mov_b32 s34, s64\n" \
  "s_mov_b32 s71, s86\n" \
  "s_max_u32 s19, s19, s71\n" \
  "s_and_b32 s72, s71, 0xff\n" \
  "s_lshr_b32 s71, s71, 8\n" \
  "s_cselect_b32 s71, s71, s61\n" \
  "s_sub_i32 s72, s64, s72\n" \
  "s_sub_i32 s71, s71, s61\n" \
  "s_cmp_ge_i32 s72, 0\n" \
  "s_cselect_b32 s71, s71, 0\n" \
  "s_add_u32 s61, s61, s71\n" \
  "s_mul_i32 s72, s72, s71\n" \
  "s_lshl_b32 s72, s72, 1\n" \
  "s_add_u32 s88, s88, s72\n" \
  "s_mul_i32 s72, s72, s90\n" \
  "s_ashr_i32 s72, s72, 8\n" \
  "s_add_u32 s89, s89, s72\n" \
  "s_lshr_b32 s72, s18, 1\n" \
  "s_sub_u32 s80, s72, s18\n" \
  "s_mov_b32 s18, s72\n" \
  "s_add_u32 s71, s71, s80\n" \
  "s_add_u32 s50, s50, s71\n" \
  "s_mul_i32 s91, s50, s90\n" \
  "s_lshr_b32 s91, s91, 8\n" \
  "s_add_u32 s51, s51, s71\n" \
  "s_mul_i32 s92, s51, s90\n" \
  "s_lshr_b32 s92, s92, 8\n" \
  "LRT02_%=:\n" \
  "s_sub_u32 s72, s74, s95\n" \
  "s_cmp_lt_u32 s64, 3\n" \
  "s_cbranch_scc1 LRB02_%=\n" \
  "s_cmp_gt_u32 s72, 900\n" \
  "s_cbranch_scc0 LRC02_%=\n" \
  "s_sub_u32 s71, s76, s96\n" \
  "s_sub_u32 s72, s74, s95\n" \
  "v_cvt_f32_u32 v36, s71\n" \
  "v_cvt_f32_u32 v37, s72\n" \
  "v_rcp_f32 v37, v37\n" \
  "v_mul_f32 v36, 0x43820000, v36\n" \
  "v_mul_f32 v36, v36, v37\n" \
  "v_cvt_u32_f32 v36, v36\n" \
  "s_nop 0\n" \
  "v_readfirstlane_b32 s71, v36\n" \
  "s_min_u32 s90, s71, 6400\n" \
  "LRB02_%=:\n" \
  "s_mov_b32 s95, s74\n" \
  "s_mov_b32 s96, s76\n" \
  "LRC02_%=:\n" \
  "s_waitcnt lgkmcnt(0)\n" \
  "LM02_%=:\n" \
  "s_waitcnt vmcnt(11)\n" \
  "s_barrier\n" \
  "ds_read_b128 v[112:115], v5\n" \
  "ds_read_b128 v[116:119], v5 offset:512\n" \
  "s_waitcnt vmcnt(10)\n" \
  "s_barrier\n" \
  "ds_read_b128 v[120:123], v5 offset:1024\n" \
  "ds_read_b128 v[124:127], v5 offset:1536\n" \
  "s_waitcnt lgkmcnt(3)\n" \
  "v_smfmac_f32_16x16x64_bf16 v[176:179], v[112:115], a[0:7], v0\n" \
  "v_smfmac_f32_16x16x64_bf16 v[180:183], v[112:115], a[160:167], v0\n" \
  "s_waitcnt vmcnt(8)\n" \
  "s_waitcnt lgkmcnt(2)\n" \
  "v_smfmac_f32_16x16x64_bf16 v[176:179], v[116:119], a[8:15], v0\n" \
  "s_barrier\n" \
  "ds_read_b128 v[128:131], v5 offset:2048\n" \
  "v_smfmac_f32_16x16x64_bf16 v[180:183], v[116:119], a[168:175], v0\n" \
  "s_waitcnt lgkmcnt(2)\n" \
  "ds_read_b128 v[132:135], v5 offset:2560\n" \
  "v_smfmac_f32_16x16x64_bf16 v[176:179], v[120:123], a[16:23], v0\n" \
  "ds_read_b128 v[136:139], v5 offset:3072\n" \
  "v_smfmac_f32_16x16x64_bf16 v[180:183], v[120:123], a[176:183], v0\n" \
  "s_waitcnt vmcnt(6)\n" \
  "s_waitcnt lgkmcnt(3)\n" \
  "ds_read_b128 v[140:143], v5 offset:3584\n" \
  "v_smfmac_f32_16x16x64_bf16 v[176:179], v[124:127], a[24:31], v0\n" \
  "s_barrier\n" \
  "ds_read_b128 v[144:147], v5 offset:4096\n" \
  "v_smfmac_f32_16x16x64_bf16 v[180:183], v[124:127], a[184:191], v0\n" \
  "s_waitcnt lgkmcnt(4)\n" \
  "ds_read_b128 v[148:151], v5 offset:4608\n" \
  "v_smfmac_f32_16x16x64_bf16 v[176:179], v[128:131], a[32:39], v0\n" \
  "ds_read_b128 v[152:155], v5 offset:5120\n" \
  "v_smfmac_f32_16x16x64_bf16 v[180:183], v[128:131], a[192:199], v0\n" \
  "s_waitcnt vmcnt(4)\n" \
  "s_waitcnt lgkmcnt(5)\n" \
  "ds_read_b128 v[156:159], v5 offset:5632\n" \
  "v_smfmac_f32_16x16x64_bf16 v[176:179], v[132:135], a[40:47], v0\n" \
  "s_barrier\n" \
  "ds_read_b128 v[160:163], v5 offset:6144\n" \
  "v_smfmac_f32_16x16x64_bf16 v[180:183], v[132:135], a[200:207], v0\n" \
  "s_waitcnt lgkmcnt(6)\n" \
  "ds_read_b128 v[164:167], v5 offset:6656\n" \
  "v_smfmac_f32_16x16x64_bf16 v[176:179], v[136:139], a[48:55], v0\n" \
  "ds_read_b128 v[168:171], v5 offset:7168\n" \
  "v_smfmac_f32_16x16x64_bf16 v[180:183], v[136:139], a[208:215], v0\n" \
  "s_waitcnt vmcnt(2)\n" \
  "s_waitcnt lgkmcnt(7)\n" \
  "ds_read_b128 v[172:175], v5 offset:7680\n" \
  "v_smfmac_f32_16x16x64_bf16 v[176:179], v[140:143], a[56:63], v0\n" \
  "s_barrier\n" \
  "ds_read2_b32 v[112:113], v6 offset0:0 offset1:32\n" \
  "v_smfmac_f32_16x16x64_bf16 v[180:183], v[140:143], a[216:223], v0\n" \
  "s_waitcnt lgkmcnt(8)\n" \
  "ds_read2_b32 v[114:115], v6 offset0:64 offset1:96\n" \
  "v_smfmac_f32_16x16x64_bf16 v[176:179], v[144:147], a[64:71], v0\n" \
  "s_waitcnt lgkmcnt(8)\n" \
  "ds_read2_b32 v[116:117], v6 offset0:128 offset1:160\n" \
  "v_smfmac_f32_16x16x64_bf16 v[176:179], v[148:151], a[72:79], v0\n" \
  "s_waitcnt lgkmcnt(8)\n" \
  "ds_read2_b32 v[118:119], v6 offset0:192 offset1:224\n" \
  "v_smfmac_f32_16x16x64_bf16 v[176:179], v[152:155], a[80:87], v0\n" \
  "s_waitcnt lgkmcnt(8)\n" \
  "ds_read2_b32 v[120:121], v7 offset0:0 offset1:32\n" \
  "v_smfmac_f32_16x16x64_bf16 v[176:179], v[156:159], a[88:95], v0\n" \
  "s_waitcnt lgkmcnt(8)\n" \
  "ds_read2_b32 v[122:123], v7 offset0:64 offset1:96\n" \
  "v_smfmac_f32_16x16x64_bf16 v[176:179], v[160:163], a[96:103], v0\n" \
  "s_waitcnt lgkmcnt(8)\n" \
  "ds_read2_b32 v[124:125], v7 offset0:128 offset1:160\n" \
  "v_smfmac_f32_16x16x64_bf16 v[176:179], v[164:167], a[104:111], v0\n" \
  "s_waitcnt lgkmcnt(8)\n" \
  "ds_read2_b32 v[126:127], v7 offset0:192 offset1:224\n" \
  "v_smfmac_f32_16x16x64_bf16 v[176:179], v[168:171], a[112:119], v0\n" \
  "s_waitcnt lgkmcnt(8)\n" \
  "v_smfmac_f32_16x16x64_bf16 v[176:179], v[172:175], a[120:127], v0\n" \
  "s_waitcnt lgkmcnt(6)\n" \
  "v_smfmac_f32_16x16x64_bf16 v[176:179], v[112:115], a[128:135], v0\n" \
  "s_waitcnt lgkmcnt(4)\n" \
  "v_smfmac_f32_16x16x64_bf16 v[176:179], v[116:119], a[136:143], v0\n" \
  "s_waitcnt lgkmcnt(2)\n" \
  "v_smfmac_f32_16x16x64_bf16 v[176:179], v[120:123], a[144:151], v0\n" \
  "s_waitcnt lgkmcnt(0)\n" \
  "v_smfmac_f32_16x16x64_bf16 v[176:179], v[124:127], a[152:159], v0\n" \
  "v_smfmac_f32_16x16x64_bf16 v[180:183], v[144:147], a[224:231], v0\n" \
  "v_smfmac_f32_16x16x64_bf16 v[180:183], v[148:151], a[232:239], v0\n" \
  "v_cvt_pk_bf16_f32 v184, v176, v177\n" \
  "v_smfmac_f32_16x16x64_bf16 v[180:183], v[152:155], a[240:247], v0\n" \
  "v_cvt_pk_bf16_f32 v185, v178, v179\n" \
  "v_smfmac_f32_16x16x64_bf16 v[180:183], v[156:159], a[248:255], v0\n" \
  "ds_write_b64 v8, v[184:185]\n" \
  "v_add_f32 v186, v176, v177\n" \
  "v_smfmac_f32_16x16x64_bf16 v[180:183], v[160:163], v[192:199], v0\n" \
  "v_add_f32 v187, v178, v179\n" \
  "v_smfmac_f32_16x16x64_bf16 v[180:183], v[164:167], v[200:207], v0\n" \
  "v_cmp_u_f32 vcc, v186, v187\n" \
  "v_smfmac_f32_16x16x64_bf16 v[180:183], v[168:171], v[208:215], v0\n" \
  "v_smfmac_f32_16x16x64_bf16 v[180:183], v[172:175], v[216:223], v0\n" \
  "s_cbranch_vccnz LMF02_%=\n" \
  "LMOK02_%=:\n" \
  "v_smfmac_f32_16x16x64_bf16 v[180:183], v[112:115], v[224:231], v0\n" \
  "s_waitcnt lgkmcnt(0)\n" \
  "s_barrier\n" \
  "v_smfmac_f32_16x16x64_bf16 v[180:183], v[116:119], v[232:239], v0\n" \
  "v_smfmac_f32_16x16x64_bf16 v[180:183], v[120:123], v[240:247], v0\n" \
  "v_smfmac_f32_16x16x64_bf16 v[180:183], v[124:127], v[248:255], v0\n" \
  "s_nop 3\n" \
  "v_mov_b32 v31, v31\n" \
  "s_nop 2\n" \
  "v_cvt_pk_bf16_f32 v186, v180, v181\n" \
  "v_cvt_pk_bf16_f32 v187, v182, v183\n" \
  "ds_write_b64 v9, v[186:187]\n" \
  "s_waitcnt lgkmcnt(0)\n" \
  "s_barrier\n" \
  "s_nop 0\n" \
  "ds_read_b64_tr_b16 v[128:129], v12\n" \
  "s_waitcnt lgkmcnt(0)\n" \
  "buffer_store_dwordx2 v[128:129], v14, s[36:39], s69 offen sc1\n" \
  "s_mov_b32 s67, s66\n" \
  "s_cmp_eq_u32 s64, 64\n" \
  "s_cbranch_scc1 LDONE_%=\n" \
  "s_add_u32 s88, s88, s50\n" \
  "s_add_u32 s89, s89, s91\n" \
  "s_waitcnt vmcnt(1)\n" \
  "v_cmp_gt_u32 vcc, 16, v35\n" \
  "s_cbranch_vccnz LFF02_%=\n" \
  "LFOK02_%=:\n" \
  "LB02_%=:\n" \
  "LWB02_%=:\n" \
  "s_memrealtime s[74:75]\n" \
  "s_memtime s[76:77]\n" \
  "s_waitcnt lgkmcnt(0)\n" \
  "s_sub_i32 s72, s88, s74\n" \
  "s_mul_i32 s72, s72, s90\n" \
  "s_ashr_i32 s72, s72, 8\n" \
  "s_sub_i32 s71, s89, s76\n" \
  "s_min_i32 s71, s71, s72\n" \
  "s_sub_i32 s72, s72, 92\n" \
  "s_max_i32 s71, s71, s72\n" \
  "s_add_u32 s89, s71, s76\n" \
  "s_cmp_gt_i32 s71, 168\n" \
  "s_cbranch_scc1 LWB02_%=\n" \
  "s_cmp_lt_i32 s71, s94\n" \
  "s_cbranch_scc1 LSB02_%=\n" \
  "s_max_i32 s71, s71, 48\n" \
  "s_lshr_b32 s71, s71, 3\n" \
  "s_lshl_b32 s71, s71, 2\n" \
  "s_sub_u32 s78, s84, s71\n" \
  "s_subb_u32 s79, s85, 0\n" \
  "s_setpc_b64 s[78:79]\n" \
  "s_nop 1\n" \
  "s_nop 1\n" \
  "s_nop 1\n" \
  "s_nop 1\n" \
  "s_nop 1\n" \
  "s_nop 1\n" \
  "s_nop 1\n" \
  "s_nop 1\n" \
  "s_nop 1\n" \
  "s_nop 1\n" \
  "s_nop 1\n" \
  "s_nop 1\n" \
  "s_nop 1\n" \
  "s_nop 1\n" \
  "s_nop 1\n" \
  "s_nop 1\n" \
  "LEB02_%=:\n" \
  "s_barrier\n" \
  "buffer_load_dwordx4 v[112:115], v15, s[36:39], s65 offen sc1\n" \
  "s_barrier\n" \
  "buffer_load_dwordx4 v[116:119], v15, s[36:39], s65 offen offset:128 sc1\n" \
  "s_add_u32 s88, s88, s51\n" \
  "s_add_u32 s89, s89, s92\n" \
  "s_barrier\n" \
  "buffer_load_dwordx4 v[120:123], v16, s[36:39], s65 offen sc1\n" \
  "s_barrier\n" \
  "buffer_load_dwordx4 v[124:127], v16, s[36:39], s65 offen offset:512 sc1\n" \
  "s_barrier\n" \
  "buffer_load_dwordx4 v[128:131], v16, s[36:39], s65 offen offset:1024 sc1\n" \
  "s_barrier\n" \
  "buffer_load_dwordx4 v[132:135], v16, s[36:39], s65 offen offset:1536 sc1\n" \
  "ds_read_b128 v[40:43], v19\n" \
  "ds_read_b128 v[44:47], v19 offset:512\n" \
  "ds_read_b128 v[48:51], v19 offset:1024\n" \
  "ds_read_b128 v[52:55], v19 offset:1536\n" \
  "s_mov_b64 exec, s[46:47]\n" \
  "ds_read_b128 v[56:59], v20\n" \
  "ds_read_b128 v[60:63], v20 offset:128\n" \
  "ds_read_b128 v[64:67], v20 offset:256\n" \
  "ds_read_b128 v[68:71], v20 offset:384\n" \
  "s_mov_b64 exec, -1\n" \
  "s_nop 15\n" \
  "s_nop 1\n" \
  "buffer_load_dwordx4 v[136:139], v17, s[36:39], s65 offen sc1\n" \
  "buffer_load_dwordx4 v[140:143], v18, s[36:39], s65 offen sc1\n" \
  "s_waitcnt lgkmcnt(0)\n" \
  "s_getreg_b32 s71, hwreg(HW_REG_IB_STS, 0, 4)\n" \
  "s_cmp_le_u32 s71, 6\n" \
  "v_mfma_f32_16x16x32_bf16 v[144:147], v[72:75], v[72:75], 0\n" \
  "s_cbranch_scc1 LHXS02_%=\n" \
  "s_getreg_b32 s71, hwreg(HW_REG_IB_STS, 0, 4)\n" \
  "s_cmp_le_u32 s71, 6\n" \
  "v_mfma_f32_16x16x32_bf16 v[144:147], v[72:75], v[72:75], 0\n" \
  "s_cbranch_scc1 LHXS02_%=\n" \
  "s_getreg_b32 s71, hwreg(HW_REG_IB_STS, 0, 4)\n" \
  "s_cmp_le_u32 s71, 6\n" \
  "v_mfma_f32_16x16x32_bf16 v[144:147], v[72:75], v[72:75], 0\n" \
  "s_cbranch_scc1 LHXS02_%=\n" \
  "s_getreg_b32 s71, hwreg(HW_REG_IB_STS, 0, 4)\n" \
  "s_cmp_le_u32 s71, 6\n" \
  "v_mfma_f32_16x16x32_bf16 v[144:147], v[72:75], v[72:75], 0\n" \
  "s_cbranch_scc1 LHXS02_%=\n" \
  "s_getreg_b32 s71, hwreg(HW_REG_IB_STS, 0, 4)\n" \
  "s_cmp_le_u32 s71, 6\n" \
  "v_mfma_f32_16x16x32_bf16 v[144:147], v[72:75], v[72:75], 0\n" \
  "s_cbranch_scc1 LHXS02_%=\n" \
  "s_getreg_b32 s71, hwreg(HW_REG_IB_STS, 0, 4)\n" \
  "s_cmp_le_u32 s71, 6\n" \
  "v_mfma_f32_16x16x32_bf16 v[144:147], v[72:75], v[72:75], 0\n" \
  "s_cbranch_scc1 LHXS02_%=\n" \
  "s_getreg_b32 s71, hwreg(HW_REG_IB_STS, 0, 4)\n" \
  "s_cmp_le_u32 s71, 6\n" \
  "v_mfma_f32_16x16x32_bf16 v[144:147], v[72:75], v[72:75], 0\n" \
  "s_cbranch_scc1 LHXS02_%=\n" \
  "s_getreg_b32 s71, hwreg(HW_REG_IB_STS, 0, 4)\n" \
  "s_cmp_le_u32 s71, 6\n" \
  "v_mfma_f32_16x16x32_bf16 v[144:147], v[72:75], v[72:75], 0\n" \
  "s_cbranch_scc1 LHXS02_%=\n" \
  "s_getreg_b32 s71, hwreg(HW_REG_IB_STS, 0, 4)\n" \
  "s_cmp_le_u32 s71, 6\n" \
  "v_mfma_f32_16x16x32_bf16 v[144:147], v[72:75], v[72:75], 0\n" \
  "s_cbranch_scc1 LHXS02_%=\n" \
  "s_getreg_b32 s71, hwreg(HW_REG_IB_STS, 0, 4)\n" \
  "s_cmp_le_u32 s71, 6\n" \
  "v_mfma_f32_16x16x32_bf16 v[144:147], v[72:75], v[72:75], 0\n" \
  "s_cbranch_scc1 LHXS02_%=\n" \
  "s_getreg_b32 s71, hwreg(HW_REG_IB_STS, 0, 4)\n" \
  "s_cmp_le_u32 s71, 6\n" \
  "v_mfma_f32_16x16x32_bf16 v[144:147], v[72:75], v[72:75], 0\n" \
  "s_cbranch_scc1 LHXS02_%=\n" \
  "s_getreg_b32 s71, hwreg(HW_REG_IB_STS, 0, 4)\n" \
  "s_cmp_le_u32 s71, 6\n" \
  "v_mfma_f32_16x16x32_bf16 v[144:147], v[72:75], v[72:75], 0\n" \
  "s_cbranch_scc1 LHXS02_%=\n" \
  "s_getreg_b32 s71, hwreg(HW_REG_IB_STS, 0, 4)\n" \
  "s_cmp_le_u32 s71, 6\n" \
  "v_mfma_f32_16x16x32_bf16 v[144:147], v[72:75], v[72:75], 0\n" \
  "s_cbranch_scc1 LHXS02_%=\n" \
  "s_getreg_b32 s71, hwreg(HW_REG_IB_STS, 0, 4)\n" \
  "s_cmp_le_u32 s71, 6\n" \
  "v_mfma_f32_16x16x32_bf16 v[144:147], v[72:75], v[72:75], 0\n" \
  "s_cbranch_scc1 LHXS02_%=\n" \
  "s_getreg_b32 s71, hwreg(HW_REG_IB_STS, 0, 4)\n" \
  "s_cmp_le_u32 s71, 6\n" \
  "v_mfma_f32_16x16x32_bf16 v[144:147], v[72:75], v[72:75], 0\n" \
  "s_cbranch_scc1 LHXS02_%=\n" \
  "s_getreg_b32 s71, hwreg(HW_REG_IB_STS, 0, 4)\n" \
  "s_cmp_le_u32 s71, 6\n" \
  "v_mfma_f32_16x16x32_bf16 v[144:147], v[72:75], v[72:75], 0\n" \
  "s_cbranch_scc1 LHXS02_%=\n" \
  "s_getreg_b32 s71, hwreg(HW_REG_IB_STS, 0, 4)\n" \
  "s_cmp_le_u32 s71, 6\n" \
  "v_mfma_f32_16x16x32_bf16 v[144:147], v[72:75], v[72:75], 0\n" \
  "s_cbranch_scc1 LHXS02_%=\n" \
  "s_getreg_b32 s71, hwreg(HW_REG_IB_STS, 0, 4)\n" \
  "s_cmp_le_u32 s71, 6\n" \
  "v_mfma_f32_16x16x32_bf16 v[144:147], v[72:75], v[72:75], 0\n" \
  "s_cbranch_scc1 LHXS02_%=\n" \
  "s_getreg_b32 s71, hwreg(HW_REG_IB_STS, 0, 4)\n" \
  "s_cmp_le_u32 s71, 6\n" \
  "v_mfma_f32_16x16x32_bf16 v[144:147], v[72:75], v[72:75], 0\n" \
  "s_cbranch_scc1 LHXS02_%=\n" \
  "s_getreg_b32 s71, hwreg(HW_REG_IB_STS, 0, 4)\n" \
  "s_cmp_le_u32 s71, 6\n" \
  "v_mfma_f32_16x16x32_bf16 v[144:147], v[72:75], v[72:75], 0\n" \
  "s_cbranch_scc1 LHXS02_%=\n" \
  "s_waitcnt vmcnt(6)\n" \
  "LHXS02_%=:\n" \
  "LS202_%=:\n" \
  "v_dot2_f32_bf16 v176, v112, s22, 0\n" \
  "v_dot2_f32_bf16 v177, v112, s23, 0\n" \
  "v_dot2_f32_bf16 v178, v113, s22, 0\n" \
  "v_dot2_f32_bf16 v179, v113, s23, 0\n" \
  "v_dot2_f32_bf16 v180, v114, s22, 0\n" \
  "v_dot2_f32_bf16 v181, v114, s23, 0\n" \
  "v_dot2_f32_bf16 v182, v115, s22, 0\n" \
  "v_dot2_f32_bf16 v183, v115, s23, 0\n" \
  "v_dot2_f32_bf16 v176, v116, s22, v176\n" \
  "v_dot2_f32_bf16 v177, v116, s23, v177\n" \
  "v_dot2_f32_bf16 v178, v117, s22, v178\n" \
  "v_dot2_f32_bf16 v179, v117, s23, v179\n" \
  "v_dot2_f32_bf16 v180, v118, s22, v180\n" \
  "v_dot2_f32_bf16 v181, v118, s23, v181\n" \
  "v_dot2_f32_bf16 v182, v119, s22, v182\n" \
  "v_dot2_f32_bf16 v183, v119, s23, v183\n" \
  "v_exp_f32 v176, v176\n" \
  "v_exp_f32 v177, v177\n" \
  "v_exp_f32 v178, v178\n" \
  "v_exp_f32 v179, v179\n" \
  "s_waitcnt vmcnt(5)\n" \
  "v_mfma_f32_16x16x32_bf16 v[160:163], v[56:59], v[120:123], 0\n" \
  "v_exp_f32 v180, v180\n" \
  "v_mfma_f32_16x16x32_bf16 v[144:147], v[40:43], v[120:123], 0\n" \
  "v_exp_f32 v181, v181\n" \
  "v_exp_f32 v182, v182\n" \
  "v_exp_f32 v183, v183\n" \
  "v_cvt_pk_bf16_f32 v184, v176, v177\n" \
  "v_cvt_pk_bf16_f32 v185, v178, v179\n" \
  "v_cvt_pk_bf16_f32 v186, v180, v181\n" \
  "v_cvt_pk_bf16_f32 v187, v182, v183\n" \
  "s_nop 1\n" \
  "v_mfma_f32_16x16x32_bf16 v[160:163], v[72:75], v[184:187], v[160:163]\n" \
  "v_mfma_f32_16x16x32_bf16 v[164:167], v[76:79], v[184:187], 0\n" \
  "s_waitcnt vmcnt(4)\n" \
  "v_mfma_f32_16x16x32_bf16 v[164:167], v[60:63], v[124:127], v[164:167]\n" \
  "v_rcp_f32 v188, v162\n" \
  "v_mfma_f32_16x16x32_bf16 v[148:151], v[44:47], v[124:127], 0\n" \
  "v_mul_f32 v144, v188, v144\n" \
  "v_mul_f32 v145, v188, v145\n" \
  "v_mfma_f32_16x16x32_bf16 v[168:171], v[80:83], v[184:187], 0\n" \
  "s_waitcnt vmcnt(3)\n" \
  "v_rcp_f32 v189, v166\n" \
  "v_mfma_f32_16x16x32_bf16 v[168:171], v[64:67], v[128:131], v[168:171]\n" \
  "v_mul_f32 v146, v188, v146\n" \
  "v_mul_f32 v147, v188, v147\n" \
  "v_mfma_f32_16x16x32_bf16 v[152:155], v[48:51], v[128:131], 0\n" \
  "v_mul_f32 v160, v188, v160\n" \
  "v_mul_f32 v161, v188, v161\n" \
  "v_mfma_f32_16x16x32_bf16 v[172:175], v[84:87], v[184:187], 0\n" \
  "s_waitcnt vmcnt(2)\n" \
  "v_rcp_f32 v190, v170\n" \
  "v_mfma_f32_16x16x32_bf16 v[172:175], v[68:71], v[132:135], v[172:175]\n" \
  "v_cndmask_b32 v167, v188, v189, s[28:29]\n" \
  "v_cndmask_b32 v167, v167, v190, s[26:27]\n" \
  "v_mfma_f32_16x16x32_bf16 v[156:159], v[52:55], v[132:135], 0\n" \
  "v_fmac_f32 v144, v189, v148\n" \
  "v_fmac_f32 v145, v189, v149\n" \
  "v_fmac_f32 v146, v189, v150\n" \
  "v_fmac_f32 v147, v189, v151\n" \
  "v_rcp_f32 v191, v174\n" \
  "v_fmac_f32 v160, v189, v164\n" \
  "v_cndmask_b32 v167, v167, v191, s[24:25]\n" \
  "v_pk_mul_f32 v[176:177], v[176:177], v[166:167] op_sel:[0,1] op_sel_hi:[1,1]\n" \
  "v_pk_mul_f32 v[178:179], v[178:179], v[166:167] op_sel:[0,1] op_sel_hi:[1,1]\n" \
  "v_pk_mul_f32 v[180:181], v[180:181], v[166:167] op_sel:[0,1] op_sel_hi:[1,1]\n" \
  "v_pk_mul_f32 v[182:183], v[182:183], v[166:167] op_sel:[0,1] op_sel_hi:[1,1]\n" \
  "v_cmp_nlt_f32 vcc, 0x0d800000, v167\n" \
  "v_cvt_pk_bf16_f32 v184, v176, v177\n" \
  "v_cvt_pk_bf16_f32 v185, v178, v179\n" \
  "v_cvt_pk_bf16_f32 v186, v180, v181\n" \
  "v_cvt_pk_bf16_f32 v187, v182, v183\n" \
  "v_fmac_f32 v161, v189, v165\n" \
  "v_pk_fma_f32 v[160:161], v[168:169], v[190:191], v[160:161] op_sel:[0,0,0] op_sel_hi:[1,0,1]\n" \
  "v_pk_fma_f32 v[160:161], v[172:173], v[190:191], v[160:161] op_sel:[0,1,0] op_sel_hi:[1,1,1]\n" \
  "v_pk_fma_f32 v[144:145], v[152:153], v[190:191], v[144:145] op_sel:[0,0,0] op_sel_hi:[1,0,1]\n" \
  "v_pk_fma_f32 v[146:147], v[154:155], v[190:191], v[146:147] op_sel:[0,0,0] op_sel_hi:[1,0,1]\n" \
  "v_pk_fma_f32 v[144:145], v[156:157], v[190:191], v[144:145] op_sel:[0,1,0] op_sel_hi:[1,1,1]\n" \
  "v_pk_fma_f32 v[146:147], v[158:159], v[190:191], v[146:147] op_sel:[0,1,0] op_sel_hi:[1,1,1]\n" \
  "s_waitcnt vmcnt(0)\n" \
  "v_mfma_f32_16x16x32_bf16 v[160:163], v[140:143], v[184:187], v[160:163]\n" \
  "s_mov_b64 s[80:81], vcc\n" \
  "v_mfma_f32_16x16x32_bf16 v[144:147], v[136:139], v[184:187], v[144:147]\n" \
  "s_mov_b64 exec, s[30:31]\n" \
  "s_nop 2\n" \
  "ds_write_b64 v22, v[160:161]\n" \
  "v_cmp_u_f32 s[78:79], v160, v161\n" \
  "s_nop 1\n" \
  "ds_write2_b64 v21, v[144:145], v[146:147] offset1:16\n" \
  "v_add_f32 v188, v144, v145\n" \
  "v_add_f32 v189, v146, v147\n" \
  "v_cmp_u_f32 vcc, v188, v189\n" \
  "s_or_b64 s[80:81], s[80:81], s[78:79]\n" \
  "s_or_b64 vcc, vcc, s[80:81]\n" \
  "s_cbranch_vccnz LSF02_%=\n" \
  "LSOK02_%=:\n" \
  "s_mov_b64 exec, -1\n" \
  "s_waitcnt lgkmcnt(0)\n" \
  "s_barrier\n" \
  "ds_read_b32 v188, v33\n" \
  "s_waitcnt lgkmcnt(0)\n" \
  "v_cmp_ne_u32 vcc, 0, v188\n" \
  "s_cbranch_vccnz LSR02_%=\n" \
  "LNL02_%=:\n" \
  "s_waitcnt lgkmcnt(0)\n" \
  "s_cmp_eq_u32 s73, 1\n" \
  "s_cbranch_scc1 LOOP02_%=\n" \
  "s_branch LDONE_%=\n" \
  "LSA02_%=:\n" \
  "s_add_u32 s88, s88, s52\n" \
  "s_add_u32 s89, s89, s93\n" \
  "s_add_u32 s97, s97, 1\n" \
  "s_branch LWA02_%=\n" \
  "LSB02_%=:\n" \
  "s_add_u32 s88, s88, s52\n" \
  "s_add_u32 s89, s89, s93\n" \
  "s_add_u32 s97, s97, 1\n" \
  "s_branch LWB02_%=\n" \
  "LMF02_%=:\n" \
  "v_mbcnt_lo_u32_b32 v130, -1, 0\n" \
  "v_mbcnt_hi_u32_b32 v130, -1, v130\n" \
  "v_lshlrev_b32 v130, 4, v130\n" \
  "v_mov_b32 v128, 0\n" \
  "ds_read_b128 v[132:135], v130 offset:0\n" \
  "ds_read_b128 v[136:139], v130 offset:1024\n" \
  "ds_read_b128 v[140:143], v130 offset:2048\n" \
  "ds_read_b128 v[144:147], v130 offset:3072\n" \
  "ds_read_b128 v[148:151], v130 offset:4096\n" \
  "s_waitcnt lgkmcnt(0)\n" \
  "v_max3_u32 v128, v128, v132, v133\n" \
  "v_max3_u32 v128, v128, v134, v135\n" \
  "v_max3_u32 v128, v128, v136, v137\n" \
  "v_max3_u32 v128, v128, v138, v139\n" \
  "v_max3_u32 v128, v128, v140, v141\n" \
  "v_max3_u32 v128, v128, v142, v143\n" \
  "v_max3_u32 v128, v128, v144, v145\n" \
  "v_max3_u32 v128, v128, v146, v147\n" \
  "v_max3_u32 v128, v128, v148, v149\n" \
  "v_max3_u32 v128, v128, v150, v151\n" \
  "ds_read_b128 v[132:135], v130 offset:5120\n" \
  "ds_read_b128 v[136:139], v130 offset:6144\n" \
  "ds_read_b128 v[140:143], v130 offset:7168\n" \
  "ds_read_b128 v[144:147], v130 offset:8192\n" \
  "ds_read_b128 v[148:151], v130 offset:9216\n" \
  "s_waitcnt lgkmcnt(0)\n" \
  "v_max3_u32 v128, v128, v132, v133\n" \
  "v_max3_u32 v128, v128, v134, v135\n" \
  "v_max3_u32 v128, v128, v136, v137\n" \
  "v_max3_u32 v128, v128, v138, v139\n" \
  "v_max3_u32 v128, v128, v140, v141\n" \
  "v_max3_u32 v128, v128, v142, v143\n" \
  "v_max3_u32 v128, v128, v144, v145\n" \
  "v_max3_u32 v128, v128, v146, v147\n" \
  "v_max3_u32 v128, v128, v148, v149\n" \
  "v_max3_u32 v128, v128, v150, v151\n" \
  "v_cmp_eq_u32 vcc, -1, v128\n" \
  "s_cbranch_vccz LMOK02_%=\n" \
  "s_cmp_gt_u32 s58, 4000\n" \
  "s_cbranch_scc1 LMOK02_%=\n" \
  "s_add_u32 s58, s58, 1\n" \
  "s_sub_u32 s64, s64, 1\n" \
  "s_mov_b32 s66, s65\n" \
  "s_mov_b32 s68, s69\n" \
  "s_add_u32 s88, s88, s52\n" \
  "s_add_u32 s89, s89, s93\n" \
  "s_add_u32 s97, s97, 1\n" \
  "s_waitcnt vmcnt(0) lgkmcnt(0)\n" \
  "s_barrier\n" \
  "s_branch LOOP02_%=\n" \
  "LFF02_%=:\n" \
  "s_add_u32 s71, s64, 0\n" \
  "s_min_u32 s71, s71, 63\n" \
  "s_lshr_b32 s72, s71, 4\n" \
  "s_lshl_b32 s72, s72, 13\n" \
  "s_lshl_b32 s71, s71, 9\n" \
  "s_add_u32 s71, s71, s72\n" \
  "LOF02_%=:\n" \
  "buffer_load_dword v35, v28, s[36:39], s71 offen sc1\n" \
  "s_waitcnt vmcnt(0)\n" \
  "v_cmp_gt_u32 vcc, 16, v35\n" \
  "s_cbranch_vccz LOXF02_%=\n" \
  "s_add_u32 s60, s60, 1\n" \
  "s_cmp_gt_u32 s60, 20000\n" \
  "s_cbranch_scc1 LOXF02_%=\n" \
  "s_sleep 16\n" \
  "s_branch LOF02_%=\n" \
  "LOXF02_%=:\n" \
  "s_branch LFOK02_%=\n" \
  "LSF02_%=:\n" \
  "s_getpc_b64 s[78:79]\n" \
  "s_branch LSF_%=\n" \
  "s_branch LSOK02_%=\n" \
  "LSR02_%=:\n" \
  "v_mov_b32 v188, 0\n" \
  "s_add_u32 s59, s59, 1\n" \
  "s_barrier\n" \
  "ds_write_b32 v33, v188\n" \
  "s_sub_u32 s88, s88, s51\n" \
  "s_sub_u32 s89, s89, s92\n" \
  "s_add_u32 s88, s88, s52\n" \
  "s_add_u32 s89, s89, s93\n" \
  "s_add_u32 s97, s97, 1\n" \
  "s_waitcnt vmcnt(0) lgkmcnt(0)\n" \
  "s_branch LB02_%=\n" \
  "LSTART03_%=:\n" \
  "LOOP03_%=:\n" \
  "v_readlane_b32 s70, v27, s64\n" \
  "s_mov_b32 s65, s66\n" \
  "s_mov_b32 s69, s68\n" \
  "v_mov_b32 v34, s64\n" \
  "s_add_u32 s64, s64, 1\n" \
  "s_barrier\n" \
  "s_mov_b32 m0, 36864\n" \
  "s_xor_b32 s72, s65, 0\n" \
  "buffer_load_dwordx4 v3, s[36:39], s72 offen sc1 lds\n" \
  "buffer_load_dwordx4 v3, s[36:39], s72 offen offset:1024 sc1 lds\n" \
  "buffer_load_dwordx4 v3, s[36:39], s72 offen offset:2048 sc1 lds\n" \
  "buffer_load_dwordx4 v3, s[36:39], s72 offen offset:3072 sc1 lds\n" \
  "v_mov_b32 v176, 0\n" \
  "v_mov_b32 v177, 0\n" \
  "v_mov_b32 v178, 0\n" \
  "v_mov_b32 v179, 0\n" \
  "v_mov_b32 v180, 0\n" \
  "v_mov_b32 v181, 0\n" \
  "v_mov_b32 v182, 0\n" \
  "v_mov_b32 v183, 0\n" \
  "v_readlane_b32 s66, v25, s64\n" \
  "v_readlane_b32 s68, v26, s64\n" \
  "s_waitcnt lgkmcnt(0)\n" \
  "LM03_%=:\n" \
  "s_barrier\n" \
  "ds_read_b128 v[112:115], v5\n" \
  "ds_read_b128 v[116:119], v5 offset:512\n" \
  "s_barrier\n" \
  "ds_read_b128 v[120:123], v5 offset:1024\n" \
  "ds_read_b128 v[124:127], v5 offset:1536\n" \
  "s_waitcnt lgkmcnt(3)\n" \
  "v_smfmac_f32_16x16x64_bf16 v[176:179], v[112:115], a[0:7], v0\n" \
  "v_smfmac_f32_16x16x64_bf16 v[180:183], v[112:115], a[160:167], v0\n" \
  "s_waitcnt lgkmcnt(2)\n" \
  "v_smfmac_f32_16x16x64_bf16 v[176:179], v[116:119], a[8:15], v0\n" \
  "s_barrier\n" \
  "ds_read_b128 v[128:131], v5 offset:2048\n" \
  "v_smfmac_f32_16x16x64_bf16 v[180:183], v[116:119], a[168:175], v0\n" \
  "s_waitcnt lgkmcnt(2)\n" \
  "ds_read_b128 v[132:135], v5 offset:2560\n" \
  "v_smfmac_f32_16x16x64_bf16 v[176:179], v[120:123], a[16:23], v0\n" \
  "ds_read_b128 v[136:139], v5 offset:3072\n" \
  "v_smfmac_f32_16x16x64_bf16 v[180:183], v[120:123], a[176:183], v0\n" \
  "s_waitcnt lgkmcnt(3)\n" \
  "ds_read_b128 v[140:143], v5 offset:3584\n" \
  "v_smfmac_f32_16x16x64_bf16 v[176:179], v[124:127], a[24:31], v0\n" \
  "s_barrier\n" \
  "ds_read_b128 v[144:147], v5 offset:4096\n" \
  "v_smfmac_f32_16x16x64_bf16 v[180:183], v[124:127], a[184:191], v0\n" \
  "s_waitcnt lgkmcnt(4)\n" \
  "ds_read_b128 v[148:151], v5 offset:4608\n" \
  "v_smfmac_f32_16x16x64_bf16 v[176:179], v[128:131], a[32:39], v0\n" \
  "ds_read_b128 v[152:155], v5 offset:5120\n" \
  "v_smfmac_f32_16x16x64_bf16 v[180:183], v[128:131], a[192:199], v0\n" \
  "s_waitcnt lgkmcnt(5)\n" \
  "ds_read_b128 v[156:159], v5 offset:5632\n" \
  "v_smfmac_f32_16x16x64_bf16 v[176:179], v[132:135], a[40:47], v0\n" \
  "s_barrier\n" \
  "ds_read_b128 v[160:163], v5 offset:6144\n" \
  "v_smfmac_f32_16x16x64_bf16 v[180:183], v[132:135], a[200:207], v0\n" \
  "s_waitcnt lgkmcnt(6)\n" \
  "ds_read_b128 v[164:167], v5 offset:6656\n" \
  "v_smfmac_f32_16x16x64_bf16 v[176:179], v[136:139], a[48:55], v0\n" \
  "ds_read_b128 v[168:171], v5 offset:7168\n" \
  "v_smfmac_f32_16x16x64_bf16 v[180:183], v[136:139], a[208:215], v0\n" \
  "s_waitcnt lgkmcnt(7)\n" \
  "ds_read_b128 v[172:175], v5 offset:7680\n" \
  "v_smfmac_f32_16x16x64_bf16 v[176:179], v[140:143], a[56:63], v0\n" \
  "s_barrier\n" \
  "ds_read2_b32 v[112:113], v6 offset0:0 offset1:32\n" \
  "v_smfmac_f32_16x16x64_bf16 v[180:183], v[140:143], a[216:223], v0\n" \
  "s_waitcnt lgkmcnt(8)\n" \
  "ds_read2_b32 v[114:115], v6 offset0:64 offset1:96\n" \
  "v_smfmac_f32_16x16x64_bf16 v[176:179], v[144:147], a[64:71], v0\n" \
  "s_waitcnt lgkmcnt(8)\n" \
  "ds_read2_b32 v[116:117], v6 offset0:128 offset1:160\n" \
  "v_smfmac_f32_16x16x64_bf16 v[176:179], v[148:151], a[72:79], v0\n" \
  "s_waitcnt lgkmcnt(8)\n" \
  "ds_read2_b32 v[118:119], v6 offset0:192 offset1:224\n" \
  "v_smfmac_f32_16x16x64_bf16 v[176:179], v[152:155], a[80:87], v0\n" \
  "s_waitcnt lgkmcnt(8)\n" \
  "ds_read2_b32 v[120:121], v7 offset0:0 offset1:32\n" \
  "v_smfmac_f32_16x16x64_bf16 v[176:179], v[156:159], a[88:95], v0\n" \
  "s_waitcnt lgkmcnt(8)\n" \
  "ds_read2_b32 v[122:123], v7 offset0:64 offset1:96\n" \
  "v_smfmac_f32_16x16x64_bf16 v[176:179], v[160:163], a[96:103], v0\n" \
  "s_waitcnt lgkmcnt(8)\n" \
  "ds_read2_b32 v[124:125], v7 offset0:128 offset1:160\n" \
  "v_smfmac_f32_16x16x64_bf16 v[176:179], v[164:167], a[104:111], v0\n" \
  "s_waitcnt lgkmcnt(8)\n" \
  "ds_read2_b32 v[126:127], v7 offset0:192 offset1:224\n" \
  "v_smfmac_f32_16x16x64_bf16 v[176:179], v[168:171], a[112:119], v0\n" \
  "s_waitcnt lgkmcnt(8)\n" \
  "v_smfmac_f32_16x16x64_bf16 v[176:179], v[172:175], a[120:127], v0\n" \
  "s_waitcnt lgkmcnt(6)\n" \
  "v_smfmac_f32_16x16x64_bf16 v[176:179], v[112:115], a[128:135], v0\n" \
  "s_waitcnt lgkmcnt(4)\n" \
  "v_smfmac_f32_16x16x64_bf16 v[176:179], v[116:119], a[136:143], v0\n" \
  "s_waitcnt lgkmcnt(2)\n" \
  "v_smfmac_f32_16x16x64_bf16 v[176:179], v[120:123], a[144:151], v0\n" \
  "s_waitcnt lgkmcnt(0)\n" \
  "v_smfmac_f32_16x16x64_bf16 v[176:179], v[124:127], a[152:159], v0\n" \
  "buffer_store_dword v34, v1, s[36:39], 0 offen sc1\n" \
  "v_smfmac_f32_16x16x64_bf16 v[180:183], v[144:147], a[224:231], v0\n" \
  "v_smfmac_f32_16x16x64_bf16 v[180:183], v[148:151], a[232:239], v0\n" \
  "v_cvt_pk_bf16_f32 v184, v176, v177\n" \
  "v_smfmac_f32_16x16x64_bf16 v[180:183], v[152:155], a[240:247], v0\n" \
  "v_cvt_pk_bf16_f32 v185, v178, v179\n" \
  "v_smfmac_f32_16x16x64_bf16 v[180:183], v[156:159], a[248:255], v0\n" \
  "ds_write_b64 v8, v[184:185]\n" \
  "v_add_f32 v186, v176, v177\n" \
  "v_smfmac_f32_16x16x64_bf16 v[180:183], v[160:163], v[192:199], v0\n" \
  "v_add_f32 v187, v178, v179\n" \
  "v_smfmac_f32_16x16x64_bf16 v[180:183], v[164:167], v[200:207], v0\n" \
  "v_cmp_u_f32 vcc, v186, v187\n" \
  "v_smfmac_f32_16x16x64_bf16 v[180:183], v[168:171], v[208:215], v0\n" \
  "v_smfmac_f32_16x16x64_bf16 v[180:183], v[172:175], v[216:223], v0\n" \
  "s_cbranch_vccnz LMF03_%=\n" \
  "LMOK03_%=:\n" \
  "v_smfmac_f32_16x16x64_bf16 v[180:183], v[112:115], v[224:231], v0\n" \
  "s_waitcnt lgkmcnt(0)\n" \
  "s_barrier\n" \
  "v_smfmac_f32_16x16x64_bf16 v[180:183], v[116:119], v[232:239], v0\n" \
  "v_smfmac_f32_16x16x64_bf16 v[180:183], v[120:123], v[240:247], v0\n" \
  "v_smfmac_f32_16x16x64_bf16 v[180:183], v[124:127], v[248:255], v0\n" \
  "s_nop 3\n" \
  "v_mov_b32 v31, v31\n" \
  "s_nop 2\n" \
  "v_cvt_pk_bf16_f32 v186, v180, v181\n" \
  "v_cvt_pk_bf16_f32 v187, v182, v183\n" \
  "ds_write_b64 v9, v[186:187]\n" \
  "v_mov_b32 v31, v31\n" \
  "s_nop 2\n" \
  "v_mov_b32 v31, v31\n" \
  "s_nop 2\n" \
  "v_mov_b32 v31, v31\n" \
  "s_nop 2\n" \
  "s_waitcnt lgkmcnt(0)\n" \
  "s_barrier\n" \
  "ds_read_b64_tr_b16 v[144:145], v11\n" \
  "ds_read_b64_tr_b16 v[146:147], v11 offset:512\n" \
  "ds_read_b64_tr_b16 v[148:149], v11 offset:1024\n" \
  "ds_read_b64_tr_b16 v[150:151], v11 offset:1536\n" \
  "v_mov_b32 v31, v31\n" \
  "s_nop 2\n" \
  "v_mov_b32 v31, v31\n" \
  "s_nop 2\n" \
  "v_mov_b32 v31, v31\n" \
  "s_nop 0\n" \
  "s_waitcnt lgkmcnt(2)\n" \
  "v_mfma_f32_16x16x32_bf16 v[188:191], v[144:147], v[144:147], 0\n" \
  "s_waitcnt lgkmcnt(0)\n" \
  "v_mfma_f32_16x16x32_bf16 v[188:191], v[148:151], v[148:151], v[188:191]\n" \
  "s_nop 0\n" \
  "ds_read_b64_tr_b16 v[128:129], v12\n" \
  "s_nop 1\n" \
  "v_mov_b32 v31, v31\n" \
  "s_nop 2\n" \
  "v_pk_mul_f32 v[188:189], v[188:189], s[42:43]\n" \
  "v_pk_mul_f32 v[190:191], v[190:191], s[42:43]\n" \
  "v_cvt_pk_bf16_f32 v152, v188, v189\n" \
  "v_cvt_pk_bf16_f32 v153, v190, v191\n" \
  "buffer_store_dwordx2 v[152:153], v13, s[36:39], s65 offen sc1\n" \
  "s_waitcnt lgkmcnt(0)\n" \
  "buffer_store_dwordx2 v[128:129], v14, s[36:39], s69 offen sc1\n" \
  "s_mov_b32 s67, s66\n" \
  "s_cmp_eq_u32 s64, 64\n" \
  "s_cbranch_scc1 LDONE_%=\n" \
  "LB03_%=:\n" \
  "s_barrier\n" \
  "buffer_load_dwordx4 v[112:115], v15, s[36:39], s65 offen sc1\n" \
  "s_barrier\n" \
  "buffer_load_dwordx4 v[116:119], v15, s[36:39], s65 offen offset:128 sc1\n" \
  "s_barrier\n" \
  "buffer_load_dwordx4 v[120:123], v16, s[36:39], s65 offen sc1\n" \
  "s_barrier\n" \
  "buffer_load_dwordx4 v[124:127], v16, s[36:39], s65 offen offset:512 sc1\n" \
  "s_barrier\n" \
  "buffer_load_dwordx4 v[128:131], v16, s[36:39], s65 offen offset:1024 sc1\n" \
  "s_waitcnt vmcnt(8)\n" \
  "s_barrier\n" \
  "buffer_load_dwordx4 v[132:135], v16, s[36:39], s65 offen offset:1536 sc1\n" \
  "ds_read_b128 v[40:43], v19\n" \
  "ds_read_b128 v[44:47], v19 offset:512\n" \
  "ds_read_b128 v[48:51], v19 offset:1024\n" \
  "ds_read_b128 v[52:55], v19 offset:1536\n" \
  "s_mov_b64 exec, s[46:47]\n" \
  "ds_read_b128 v[56:59], v20\n" \
  "ds_read_b128 v[60:63], v20 offset:128\n" \
  "ds_read_b128 v[64:67], v20 offset:256\n" \
  "ds_read_b128 v[68:71], v20 offset:384\n" \
  "s_mov_b64 exec, -1\n" \
  "s_nop 15\n" \
  "s_nop 1\n" \
  "buffer_load_dwordx4 v[136:139], v17, s[36:39], s65 offen sc1\n" \
  "buffer_load_dwordx4 v[140:143], v18, s[36:39], s65 offen sc1\n" \
  "s_waitcnt lgkmcnt(0)\n" \
  "s_getreg_b32 s71, hwreg(HW_REG_IB_STS, 0, 4)\n" \
  "s_cmp_le_u32 s71, 6\n" \
  "v_mfma_f32_16x16x32_bf16 v[144:147], v[72:75], v[72:75], 0\n" \
  "s_cbranch_scc1 LHXS03_%=\n" \
  "s_getreg_b32 s71, hwreg(HW_REG_IB_STS, 0, 4)\n" \
  "s_cmp_le_u32 s71, 6\n" \
  "v_mfma_f32_16x16x32_bf16 v[144:147], v[72:75], v[72:75], 0\n" \
  "s_cbranch_scc1 LHXS03_%=\n" \
  "s_getreg_b32 s71, hwreg(HW_REG_IB_STS, 0, 4)\n" \
  "s_cmp_le_u32 s71, 6\n" \
  "v_mfma_f32_16x16x32_bf16 v[144:147], v[72:75], v[72:75], 0\n" \
  "s_cbranch_scc1 LHXS03_%=\n" \
  "s_getreg_b32 s71, hwreg(HW_REG_IB_STS, 0, 4)\n" \
  "s_cmp_le_u32 s71, 6\n" \
  "v_mfma_f32_16x16x32_bf16 v[144:147], v[72:75], v[72:75], 0\n" \
  "s_cbranch_scc1 LHXS03_%=\n" \
  "s_getreg_b32 s71, hwreg(HW_REG_IB_STS, 0, 4)\n" \
  "s_cmp_le_u32 s71, 6\n" \
  "v_mfma_f32_16x16x32_bf16 v[144:147], v[72:75], v[72:75], 0\n" \
  "s_cbranch_scc1 LHXS03_%=\n" \
  "s_getreg_b32 s71, hwreg(HW_REG_IB_STS, 0, 4)\n" \
  "s_cmp_le_u32 s71, 6\n" \
  "v_mfma_f32_16x16x32_bf16 v[144:147], v[72:75], v[72:75], 0\n" \
  "s_cbranch_scc1 LHXS03_%=\n" \
  "s_getreg_b32 s71, hwreg(HW_REG_IB_STS, 0, 4)\n" \
  "s_cmp_le_u32 s71, 6\n" \
  "v_mfma_f32_16x16x32_bf16 v[144:147], v[72:75], v[72:75], 0\n" \
  "s_cbranch_scc1 LHXS03_%=\n" \
  "s_getreg_b32 s71, hwreg(HW_REG_IB_STS, 0, 4)\n" \
  "s_cmp_le_u32 s71, 6\n" \
  "v_mfma_f32_16x16x32_bf16 v[144:147], v[72:75], v[72:75], 0\n" \
  "s_cbranch_scc1 LHXS03_%=\n" \
  "s_getreg_b32 s71, hwreg(HW_REG_IB_STS, 0, 4)\n" \
  "s_cmp_le_u32 s71, 6\n" \
  "v_mfma_f32_16x16x32_bf16 v[144:147], v[72:75], v[72:75], 0\n" \
  "s_cbranch_scc1 LHXS03_%=\n" \
  "s_getreg_b32 s71, hwreg(HW_REG_IB_STS, 0, 4)\n" \
  "s_cmp_le_u32 s71, 6\n" \
  "v_mfma_f32_16x16x32_bf16 v[144:147], v[72:75], v[72:75], 0\n" \
  "s_cbranch_scc1 LHXS03_%=\n" \
  "s_getreg_b32 s71, hwreg(HW_REG_IB_STS, 0, 4)\n" \
  "s_cmp_le_u32 s71, 6\n" \
  "v_mfma_f32_16x16x32_bf16 v[144:147], v[72:75], v[72:75], 0\n" \
  "s_cbranch_scc1 LHXS03_%=\n" \
  "s_getreg_b32 s71, hwreg(HW_REG_IB_STS, 0, 4)\n" \
  "s_cmp_le_u32 s71, 6\n" \
  "v_mfma_f32_16x16x32_bf16 v[144:147], v[72:75], v[72:75], 0\n" \
  "s_cbranch_scc1 LHXS03_%=\n" \
  "s_getreg_b32 s71, hwreg(HW_REG_IB_STS, 0, 4)\n" \
  "s_cmp_le_u32 s71, 6\n" \
  "v_mfma_f32_16x16x32_bf16 v[144:147], v[72:75], v[72:75], 0\n" \
  "s_cbranch_scc1 LHXS03_%=\n" \
  "s_getreg_b32 s71, hwreg(HW_REG_IB_STS, 0, 4)\n" \
  "s_cmp_le_u32 s71, 6\n" \
  "v_mfma_f32_16x16x32_bf16 v[144:147], v[72:75], v[72:75], 0\n" \
  "s_cbranch_scc1 LHXS03_%=\n" \
  "s_getreg_b32 s71, hwreg(HW_REG_IB_STS, 0, 4)\n" \
  "s_cmp_le_u32 s71, 6\n" \
  "v_mfma_f32_16x16x32_bf16 v[144:147], v[72:75], v[72:75], 0\n" \
  "s_cbranch_scc1 LHXS03_%=\n" \
  "s_getreg_b32 s71, hwreg(HW_REG_IB_STS, 0, 4)\n" \
  "s_cmp_le_u32 s71, 6\n" \
  "v_mfma_f32_16x16x32_bf16 v[144:147], v[72:75], v[72:75], 0\n" \
  "s_cbranch_scc1 LHXS03_%=\n" \
  "s_getreg_b32 s71, hwreg(HW_REG_IB_STS, 0, 4)\n" \
  "s_cmp_le_u32 s71, 6\n" \
  "v_mfma_f32_16x16x32_bf16 v[144:147], v[72:75], v[72:75], 0\n" \
  "s_cbranch_scc1 LHXS03_%=\n" \
  "s_getreg_b32 s71, hwreg(HW_REG_IB_STS, 0, 4)\n" \
  "s_cmp_le_u32 s71, 6\n" \
  "v_mfma_f32_16x16x32_bf16 v[144:147], v[72:75], v[72:75], 0\n" \
  "s_cbranch_scc1 LHXS03_%=\n" \
  "s_getreg_b32 s71, hwreg(HW_REG_IB_STS, 0, 4)\n" \
  "s_cmp_le_u32 s71, 6\n" \
  "v_mfma_f32_16x16x32_bf16 v[144:147], v[72:75], v[72:75], 0\n" \
  "s_cbranch_scc1 LHXS03_%=\n" \
  "s_getreg_b32 s71, hwreg(HW_REG_IB_STS, 0, 4)\n" \
  "s_cmp_le_u32 s71, 6\n" \
  "v_mfma_f32_16x16x32_bf16 v[144:147], v[72:75], v[72:75], 0\n" \
  "s_cbranch_scc1 LHXS03_%=\n" \
  "s_waitcnt vmcnt(6)\n" \
  "LHXS03_%=:\n" \
  "LS203_%=:\n" \
  "v_dot2_f32_bf16 v176, v112, s22, 0\n" \
  "v_dot2_f32_bf16 v177, v112, s23, 0\n" \
  "v_dot2_f32_bf16 v178, v113, s22, 0\n" \
  "v_dot2_f32_bf16 v179, v113, s23, 0\n" \
  "v_dot2_f32_bf16 v180, v114, s22, 0\n" \
  "v_dot2_f32_bf16 v181, v114, s23, 0\n" \
  "v_dot2_f32_bf16 v182, v115, s22, 0\n" \
  "v_dot2_f32_bf16 v183, v115, s23, 0\n" \
  "v_dot2_f32_bf16 v176, v116, s22, v176\n" \
  "v_dot2_f32_bf16 v177, v116, s23, v177\n" \
  "v_dot2_f32_bf16 v178, v117, s22, v178\n" \
  "v_dot2_f32_bf16 v179, v117, s23, v179\n" \
  "v_dot2_f32_bf16 v180, v118, s22, v180\n" \
  "v_dot2_f32_bf16 v181, v118, s23, v181\n" \
  "v_dot2_f32_bf16 v182, v119, s22, v182\n" \
  "v_dot2_f32_bf16 v183, v119, s23, v183\n" \
  "v_exp_f32 v176, v176\n" \
  "v_exp_f32 v177, v177\n" \
  "v_exp_f32 v178, v178\n" \
  "v_exp_f32 v179, v179\n" \
  "s_waitcnt vmcnt(5)\n" \
  "v_mfma_f32_16x16x32_bf16 v[160:163], v[56:59], v[120:123], 0\n" \
  "v_exp_f32 v180, v180\n" \
  "v_mfma_f32_16x16x32_bf16 v[144:147], v[40:43], v[120:123], 0\n" \
  "v_exp_f32 v181, v181\n" \
  "v_exp_f32 v182, v182\n" \
  "v_exp_f32 v183, v183\n" \
  "v_cvt_pk_bf16_f32 v184, v176, v177\n" \
  "v_cvt_pk_bf16_f32 v185, v178, v179\n" \
  "v_cvt_pk_bf16_f32 v186, v180, v181\n" \
  "v_cvt_pk_bf16_f32 v187, v182, v183\n" \
  "s_nop 1\n" \
  "v_mfma_f32_16x16x32_bf16 v[160:163], v[72:75], v[184:187], v[160:163]\n" \
  "v_mfma_f32_16x16x32_bf16 v[164:167], v[76:79], v[184:187], 0\n" \
  "s_waitcnt vmcnt(4)\n" \
  "v_mfma_f32_16x16x32_bf16 v[164:167], v[60:63], v[124:127], v[164:167]\n" \
  "v_rcp_f32 v188, v162\n" \
  "v_mfma_f32_16x16x32_bf16 v[148:151], v[44:47], v[124:127], 0\n" \
  "v_mul_f32 v144, v188, v144\n" \
  "v_mul_f32 v145, v188, v145\n" \
  "v_mfma_f32_16x16x32_bf16 v[168:171], v[80:83], v[184:187], 0\n" \
  "s_waitcnt vmcnt(3)\n" \
  "v_rcp_f32 v189, v166\n" \
  "v_mfma_f32_16x16x32_bf16 v[168:171], v[64:67], v[128:131], v[168:171]\n" \
  "v_mul_f32 v146, v188, v146\n" \
  "v_mul_f32 v147, v188, v147\n" \
  "v_mfma_f32_16x16x32_bf16 v[152:155], v[48:51], v[128:131], 0\n" \
  "v_mul_f32 v160, v188, v160\n" \
  "v_mul_f32 v161, v188, v161\n" \
  "v_mfma_f32_16x16x32_bf16 v[172:175], v[84:87], v[184:187], 0\n" \
  "s_waitcnt vmcnt(2)\n" \
  "v_rcp_f32 v190, v170\n" \
  "v_mfma_f32_16x16x32_bf16 v[172:175], v[68:71], v[132:135], v[172:175]\n" \
  "v_cndmask_b32 v167, v188, v189, s[28:29]\n" \
  "v_cndmask_b32 v167, v167, v190, s[26:27]\n" \
  "v_mfma_f32_16x16x32_bf16 v[156:159], v[52:55], v[132:135], 0\n" \
  "v_fmac_f32 v144, v189, v148\n" \
  "v_fmac_f32 v145, v189, v149\n" \
  "v_fmac_f32 v146, v189, v150\n" \
  "v_fmac_f32 v147, v189, v151\n" \
  "v_rcp_f32 v191, v174\n" \
  "v_fmac_f32 v160, v189, v164\n" \
  "v_cndmask_b32 v167, v167, v191, s[24:25]\n" \
  "v_pk_mul_f32 v[176:177], v[176:177], v[166:167] op_sel:[0,1] op_sel_hi:[1,1]\n" \
  "v_pk_mul_f32 v[178:179], v[178:179], v[166:167] op_sel:[0,1] op_sel_hi:[1,1]\n" \
  "v_pk_mul_f32 v[180:181], v[180:181], v[166:167] op_sel:[0,1] op_sel_hi:[1,1]\n" \
  "v_pk_mul_f32 v[182:183], v[182:183], v[166:167] op_sel:[0,1] op_sel_hi:[1,1]\n" \
  "v_cmp_nlt_f32 vcc, 0x0d800000, v167\n" \
  "v_cvt_pk_bf16_f32 v184, v176, v177\n" \
  "v_cvt_pk_bf16_f32 v185, v178, v179\n" \
  "v_cvt_pk_bf16_f32 v186, v180, v181\n" \
  "v_cvt_pk_bf16_f32 v187, v182, v183\n" \
  "v_fmac_f32 v161, v189, v165\n" \
  "v_pk_fma_f32 v[160:161], v[168:169], v[190:191], v[160:161] op_sel:[0,0,0] op_sel_hi:[1,0,1]\n" \
  "v_pk_fma_f32 v[160:161], v[172:173], v[190:191], v[160:161] op_sel:[0,1,0] op_sel_hi:[1,1,1]\n" \
  "v_pk_fma_f32 v[144:145], v[152:153], v[190:191], v[144:145] op_sel:[0,0,0] op_sel_hi:[1,0,1]\n" \
  "v_pk_fma_f32 v[146:147], v[154:155], v[190:191], v[146:147] op_sel:[0,0,0] op_sel_hi:[1,0,1]\n" \
  "v_pk_fma_f32 v[144:145], v[156:157], v[190:191], v[144:145] op_sel:[0,1,0] op_sel_hi:[1,1,1]\n" \
  "v_pk_fma_f32 v[146:147], v[158:159], v[190:191], v[146:147] op_sel:[0,1,0] op_sel_hi:[1,1,1]\n" \
  "s_waitcnt vmcnt(0)\n" \
  "v_mfma_f32_16x16x32_bf16 v[160:163], v[140:143], v[184:187], v[160:163]\n" \
  "s_mov_b64 s[80:81], vcc\n" \
  "v_mfma_f32_16x16x32_bf16 v[144:147], v[136:139], v[184:187], v[144:147]\n" \
  "s_mov_b64 exec, s[30:31]\n" \
  "s_nop 2\n" \
  "ds_write_b64 v22, v[160:161]\n" \
  "v_cmp_u_f32 s[78:79], v160, v161\n" \
  "s_nop 1\n" \
  "ds_write2_b64 v21, v[144:145], v[146:147] offset1:16\n" \
  "v_add_f32 v188, v144, v145\n" \
  "v_add_f32 v189, v146, v147\n" \
  "v_cmp_u_f32 vcc, v188, v189\n" \
  "s_or_b64 s[80:81], s[80:81], s[78:79]\n" \
  "s_or_b64 vcc, vcc, s[80:81]\n" \
  "s_cbranch_vccnz LSF03_%=\n" \
  "LSOK03_%=:\n" \
  "s_mov_b64 exec, -1\n" \
  "s_waitcnt lgkmcnt(0)\n" \
  "s_barrier\n" \
  "ds_read_b32 v188, v33\n" \
  "ds_read_b128 v[120:123], v23\n" \
  "ds_read_b128 v[124:127], v23 offset:768\n" \
  "ds_read_b128 v[128:131], v23 offset:1536\n" \
  "ds_read_b128 v[132:135], v23 offset:2304\n" \
  "s_waitcnt lgkmcnt(4)\n" \
  "v_cmp_ne_u32 vcc, 0, v188\n" \
  "s_waitcnt lgkmcnt(2)\n" \
  "v_pk_add_f32 v[120:121], v[120:121], v[122:123]\n" \
  "v_pk_add_f32 v[124:125], v[124:125], v[126:127]\n" \
  "v_pk_add_f32 v[120:121], v[120:121], v[124:125]\n" \
  "s_waitcnt lgkmcnt(1)\n" \
  "v_pk_add_f32 v[128:129], v[128:129], v[130:131]\n" \
  "v_pk_add_f32 v[120:121], v[120:121], v[128:129]\n" \
  "s_waitcnt lgkmcnt(0)\n" \
  "v_pk_add_f32 v[132:133], v[132:133], v[134:135]\n" \
  "v_pk_add_f32 v[120:121], v[120:121], v[132:133]\n" \
  "v_cvt_pk_bf16_f32 v120, v120, v121\n" \
  "s_cbranch_vccnz LSR03_%=\n" \
  "buffer_store_dword v120, v24, s[36:39], s66 offen sc1\n" \
  "LNL03_%=:\n" \
  "s_waitcnt lgkmcnt(0)\n" \
  "s_cmp_eq_u32 s73, 1\n" \
  "s_cbranch_scc1 LOOP03_%=\n" \
  "s_branch LDONE_%=\n" \
  "LMF03_%=:\n" \
  "v_mbcnt_lo_u32_b32 v130, -1, 0\n" \
  "v_mbcnt_hi_u32_b32 v130, -1, v130\n" \
  "v_lshlrev_b32 v130, 4, v130\n" \
  "v_mov_b32 v128, 0\n" \
  "ds_read_b128 v[132:135], v130 offset:0\n" \
  "ds_read_b128 v[136:139], v130 offset:1024\n" \
  "ds_read_b128 v[140:143], v130 offset:2048\n" \
  "ds_read_b128 v[144:147], v130 offset:3072\n" \
  "ds_read_b128 v[148:151], v130 offset:4096\n" \
  "s_waitcnt lgkmcnt(0)\n" \
  "v_max3_u32 v128, v128, v132, v133\n" \
  "v_max3_u32 v128, v128, v134, v135\n" \
  "v_max3_u32 v128, v128, v136, v137\n" \
  "v_max3_u32 v128, v128, v138, v139\n" \
  "v_max3_u32 v128, v128, v140, v141\n" \
  "v_max3_u32 v128, v128, v142, v143\n" \
  "v_max3_u32 v128, v128, v144, v145\n" \
  "v_max3_u32 v128, v128, v146, v147\n" \
  "v_max3_u32 v128, v128, v148, v149\n" \
  "v_max3_u32 v128, v128, v150, v151\n" \
  "ds_read_b128 v[132:135], v130 offset:5120\n" \
  "ds_read_b128 v[136:139], v130 offset:6144\n" \
  "ds_read_b128 v[140:143], v130 offset:7168\n" \
  "ds_read_b128 v[144:147], v130 offset:8192\n" \
  "ds_read_b128 v[148:151], v130 offset:9216\n" \
  "s_waitcnt lgkmcnt(0)\n" \
  "v_max3_u32 v128, v128, v132, v133\n" \
  "v_max3_u32 v128, v128, v134, v135\n" \
  "v_max3_u32 v128, v128, v136, v137\n" \
  "v_max3_u32 v128, v128, v138, v139\n" \
  "v_max3_u32 v128, v128, v140, v141\n" \
  "v_max3_u32 v128, v128, v142, v143\n" \
  "v_max3_u32 v128, v128, v144, v145\n" \
  "v_max3_u32 v128, v128, v146, v147\n" \
  "v_max3_u32 v128, v128, v148, v149\n" \
  "v_max3_u32 v128, v128, v150, v151\n" \
  "v_cmp_eq_u32 vcc, -1, v128\n" \
  "s_cbranch_vccz LMOK03_%=\n" \
  "s_cmp_gt_u32 s58, 4000\n" \
  "s_cbranch_scc1 LMOK03_%=\n" \
  "s_add_u32 s58, s58, 1\n" \
  "s_sub_u32 s64, s64, 1\n" \
  "s_mov_b32 s66, s65\n" \
  "s_mov_b32 s68, s69\n" \
  "s_waitcnt vmcnt(0) lgkmcnt(0)\n" \
  "s_barrier\n" \
  "s_branch LOOP03_%=\n" \
  "LSF03_%=:\n" \
  "s_getpc_b64 s[78:79]\n" \
  "s_branch LSF_%=\n" \
  "s_branch LSOK03_%=\n" \
  "LSR03_%=:\n" \
  "v_mov_b32 v188, 0\n" \
  "s_add_u32 s59, s59, 1\n" \
  "s_barrier\n" \
  "ds_write_b32 v33, v188\n" \
  "s_waitcnt vmcnt(0) lgkmcnt(0)\n" \
  "s_branch LB03_%=\n" \
  "LSTART10_%=:\n" \
  "LOOP10_%=:\n" \
  "v_readlane_b32 s70, v27, s64\n" \
  "s_mov_b32 s65, s66\n" \
  "s_mov_b32 s69, s68\n" \
  "s_add_u32 s64, s64, 1\n" \
  "s_barrier\n" \
  "s_mov_b32 m0, 20480\n" \
  "s_add_u32 s72, s70, 4096\n" \
  "buffer_load_dwordx4 v3, s[36:39], s70 offen sc1 lds\n" \
  "buffer_load_dwordx4 v3, s[36:39], s70 offen offset:1024 sc1 lds\n" \
  "buffer_load_dwordx4 v3, s[36:39], s70 offen offset:2048 sc1 lds\n" \
  "buffer_load_dwordx4 v3, s[36:39], s70 offen offset:3072 sc1 lds\n" \
  "s_mov_b32 m0, 24576\n" \
  "s_nop 0\n" \
  "buffer_load_dwordx4 v3, s[36:39], s72 offen sc1 lds\n" \
  "buffer_load_dwordx4 v3, s[36:39], s72 offen offset:1024 sc1 lds\n" \
  "buffer_load_dwordx4 v3, s[36:39], s72 offen offset:2048 sc1 lds\n" \
  "buffer_load_dwordx4 v3, s[36:39], s72 offen offset:3072 sc1 lds\n" \
  "v_mov_b32 v176, 0\n" \
  "v_mov_b32 v177, 0\n" \
  "v_mov_b32 v178, 0\n" \
  "v_mov_b32 v179, 0\n" \
  "v_mov_b32 v180, 0\n" \
  "v_mov_b32 v181, 0\n" \
  "v_mov_b32 v182, 0\n" \
  "v_mov_b32 v183, 0\n" \
  "v_readlane_b32 s66, v25, s64\n" \
  "v_readlane_b32 s68, v26, s64\n" \
  "s_waitcnt lgkmcnt(0)\n" \
  "LM10_%=:\n" \
  "s_barrier\n" \
  "ds_read_b128 v[112:115], v5\n" \
  "ds_read_b128 v[116:119], v5 offset:512\n" \
  "s_barrier\n" \
  "ds_read_b128 v[120:123], v5 offset:1024\n" \
  "ds_read_b128 v[124:127], v5 offset:1536\n" \
  "s_waitcnt lgkmcnt(3)\n" \
  "v_smfmac_f32_16x16x64_bf16 v[176:179], v[112:115], a[0:7], v0\n" \
  "v_smfmac_f32_16x16x64_bf16 v[180:183], v[112:115], a[160:167], v0\n" \
  "s_waitcnt lgkmcnt(2)\n" \
  "v_smfmac_f32_16x16x64_bf16 v[176:179], v[116:119], a[8:15], v0\n" \
  "s_barrier\n" \
  "ds_read_b128 v[128:131], v5 offset:2048\n" \
  "v_smfmac_f32_16x16x64_bf16 v[180:183], v[116:119], a[168:175], v0\n" \
  "s_waitcnt lgkmcnt(2)\n" \
  "ds_read_b128 v[132:135], v5 offset:2560\n" \
  "v_smfmac_f32_16x16x64_bf16 v[176:179], v[120:123], a[16:23], v0\n" \
  "ds_read_b128 v[136:139], v5 offset:3072\n" \
  "v_smfmac_f32_16x16x64_bf16 v[180:183], v[120:123], a[176:183], v0\n" \
  "s_waitcnt lgkmcnt(3)\n" \
  "ds_read_b128 v[140:143], v5 offset:3584\n" \
  "v_smfmac_f32_16x16x64_bf16 v[176:179], v[124:127], a[24:31], v0\n" \
  "s_barrier\n" \
  "ds_read_b128 v[144:147], v5 offset:4096\n" \
  "v_smfmac_f32_16x16x64_bf16 v[180:183], v[124:127], a[184:191], v0\n" \
  "s_waitcnt lgkmcnt(4)\n" \
  "ds_read_b128 v[148:151], v5 offset:4608\n" \
  "v_smfmac_f32_16x16x64_bf16 v[176:179], v[128:131], a[32:39], v0\n" \
  "ds_read_b128 v[152:155], v5 offset:5120\n" \
  "v_smfmac_f32_16x16x64_bf16 v[180:183], v[128:131], a[192:199], v0\n" \
  "s_waitcnt lgkmcnt(5)\n" \
  "ds_read_b128 v[156:159], v5 offset:5632\n" \
  "v_smfmac_f32_16x16x64_bf16 v[176:179], v[132:135], a[40:47], v0\n" \
  "s_barrier\n" \
  "ds_read_b128 v[160:163], v5 offset:6144\n" \
  "v_smfmac_f32_16x16x64_bf16 v[180:183], v[132:135], a[200:207], v0\n" \
  "s_waitcnt lgkmcnt(6)\n" \
  "ds_read_b128 v[164:167], v5 offset:6656\n" \
  "v_smfmac_f32_16x16x64_bf16 v[176:179], v[136:139], a[48:55], v0\n" \
  "ds_read_b128 v[168:171], v5 offset:7168\n" \
  "v_smfmac_f32_16x16x64_bf16 v[180:183], v[136:139], a[208:215], v0\n" \
  "s_waitcnt lgkmcnt(7)\n" \
  "ds_read_b128 v[172:175], v5 offset:7680\n" \
  "v_smfmac_f32_16x16x64_bf16 v[176:179], v[140:143], a[56:63], v0\n" \
  "s_barrier\n" \
  "ds_read2_b32 v[112:113], v6 offset0:0 offset1:32\n" \
  "v_smfmac_f32_16x16x64_bf16 v[180:183], v[140:143], a[216:223], v0\n" \
  "s_waitcnt lgkmcnt(8)\n" \
  "ds_read2_b32 v[114:115], v6 offset0:64 offset1:96\n" \
  "v_smfmac_f32_16x16x64_bf16 v[176:179], v[144:147], a[64:71], v0\n" \
  "s_waitcnt lgkmcnt(8)\n" \
  "ds_read2_b32 v[116:117], v6 offset0:128 offset1:160\n" \
  "v_smfmac_f32_16x16x64_bf16 v[176:179], v[148:151], a[72:79], v0\n" \
  "s_waitcnt lgkmcnt(8)\n" \
  "ds_read2_b32 v[118:119], v6 offset0:192 offset1:224\n" \
  "v_smfmac_f32_16x16x64_bf16 v[176:179], v[152:155], a[80:87], v0\n" \
  "s_waitcnt lgkmcnt(8)\n" \
  "ds_read2_b32 v[120:121], v7 offset0:0 offset1:32\n" \
  "v_smfmac_f32_16x16x64_bf16 v[176:179], v[156:159], a[88:95], v0\n" \
  "s_waitcnt lgkmcnt(8)\n" \
  "ds_read2_b32 v[122:123], v7 offset0:64 offset1:96\n" \
  "v_smfmac_f32_16x16x64_bf16 v[176:179], v[160:163], a[96:103], v0\n" \
  "s_waitcnt lgkmcnt(8)\n" \
  "ds_read2_b32 v[124:125], v7 offset0:128 offset1:160\n" \
  "v_smfmac_f32_16x16x64_bf16 v[176:179], v[164:167], a[104:111], v0\n" \
  "s_waitcnt lgkmcnt(8)\n" \
  "ds_read2_b32 v[126:127], v7 offset0:192 offset1:224\n" \
  "v_smfmac_f32_16x16x64_bf16 v[176:179], v[168:171], a[112:119], v0\n" \
  "s_waitcnt lgkmcnt(8)\n" \
  "v_smfmac_f32_16x16x64_bf16 v[176:179], v[172:175], a[120:127], v0\n" \
  "s_waitcnt lgkmcnt(6)\n" \
  "v_smfmac_f32_16x16x64_bf16 v[176:179], v[112:115], a[128:135], v0\n" \
  "s_waitcnt lgkmcnt(4)\n" \
  "v_smfmac_f32_16x16x64_bf16 v[176:179], v[116:119], a[136:143], v0\n" \
  "s_waitcnt lgkmcnt(2)\n" \
  "v_smfmac_f32_16x16x64_bf16 v[176:179], v[120:123], a[144:151], v0\n" \
  "s_waitcnt lgkmcnt(0)\n" \
  "v_smfmac_f32_16x16x64_bf16 v[176:179], v[124:127], a[152:159], v0\n" \
  "v_smfmac_f32_16x16x64_bf16 v[180:183], v[144:147], a[224:231], v0\n" \
  "v_smfmac_f32_16x16x64_bf16 v[180:183], v[148:151], a[232:239], v0\n" \
  "v_cvt_pk_bf16_f32 v184, v176, v177\n" \
  "v_smfmac_f32_16x16x64_bf16 v[180:183], v[152:155], a[240:247], v0\n" \
  "v_cvt_pk_bf16_f32 v185, v178, v179\n" \
  "v_smfmac_f32_16x16x64_bf16 v[180:183], v[156:159], a[248:255], v0\n" \
  "ds_write_b64 v8, v[184:185]\n" \
  "v_add_f32 v186, v176, v177\n" \
  "v_smfmac_f32_16x16x64_bf16 v[180:183], v[160:163], v[192:199], v0\n" \
  "v_add_f32 v187, v178, v179\n" \
  "v_smfmac_f32_16x16x64_bf16 v[180:183], v[164:167], v[200:207], v0\n" \
  "v_cmp_u_f32 vcc, v186, v187\n" \
  "v_smfmac_f32_16x16x64_bf16 v[180:183], v[168:171], v[208:215], v0\n" \
  "v_smfmac_f32_16x16x64_bf16 v[180:183], v[172:175], v[216:223], v0\n" \
  "s_cbranch_vccnz LMF10_%=\n" \
  "LMOK10_%=:\n" \
  "v_smfmac_f32_16x16x64_bf16 v[180:183], v[112:115], v[224:231], v0\n" \
  "s_waitcnt lgkmcnt(0)\n" \
  "s_barrier\n" \
  "ds_read_b64_tr_b16 v[128:129], v10\n" \
  "v_smfmac_f32_16x16x64_bf16 v[180:183], v[116:119], v[232:239], v0\n" \
  "ds_read_b64_tr_b16 v[130:131], v10 offset:1024\n" \
  "ds_read_b64_tr_b16 v[132:133], v10 offset:2048\n" \
  "v_smfmac_f32_16x16x64_bf16 v[180:183], v[120:123], v[240:247], v0\n" \
  "ds_read_b64_tr_b16 v[134:135], v10 offset:3072\n" \
  "v_smfmac_f32_16x16x64_bf16 v[180:183], v[124:127], v[248:255], v0\n" \
  "s_nop 3\n" \
  "v_mov_b32 v31, v31\n" \
  "s_nop 2\n" \
  "v_cvt_pk_bf16_f32 v186, v180, v181\n" \
  "v_cvt_pk_bf16_f32 v187, v182, v183\n" \
  "ds_write_b64 v9, v[186:187]\n" \
  "s_waitcnt lgkmcnt(3)\n" \
  "v_mfma_f32_16x16x32_bf16 v[188:191], v[128:131], v[128:131], 0\n" \
  "s_waitcnt lgkmcnt(1)\n" \
  "v_mfma_f32_16x16x32_bf16 v[188:191], v[132:135], v[132:135], v[188:191]\n" \
  "s_waitcnt lgkmcnt(0)\n" \
  "s_barrier\n" \
  "ds_read_b64_tr_b16 v[144:145], v11\n" \
  "ds_read_b64_tr_b16 v[146:147], v11 offset:1024\n" \
  "ds_read_b64_tr_b16 v[148:149], v11 offset:2048\n" \
  "ds_read_b64_tr_b16 v[150:151], v11 offset:3072\n" \
  "s_waitcnt lgkmcnt(2)\n" \
  "v_mfma_f32_16x16x32_bf16 v[188:191], v[144:147], v[144:147], v[188:191]\n" \
  "s_waitcnt lgkmcnt(0)\n" \
  "v_mfma_f32_16x16x32_bf16 v[188:191], v[148:151], v[148:151], v[188:191]\n" \
  "s_nop 0\n" \
  "ds_read_b64_tr_b16 v[128:129], v12\n" \
  "ds_read_b64_tr_b16 v[130:131], v12 offset:32\n" \
  "s_nop 1\n" \
  "v_mov_b32 v31, v31\n" \
  "s_nop 1\n" \
  "v_pk_mul_f32 v[188:189], v[188:189], s[42:43]\n" \
  "v_pk_mul_f32 v[190:191], v[190:191], s[42:43]\n" \
  "v_cvt_pk_bf16_f32 v152, v188, v189\n" \
  "v_cvt_pk_bf16_f32 v153, v190, v191\n" \
  "buffer_store_dwordx2 v[152:153], v13, s[36:39], s65 offen sc1\n" \
  "s_waitcnt lgkmcnt(0)\n" \
  "buffer_store_dwordx4 v[128:131], v14, s[36:39], s69 offen sc1\n" \
  "s_mov_b32 s67, s66\n" \
  "s_cmp_eq_u32 s64, 64\n" \
  "s_cbranch_scc1 LDONE_%=\n" \
  "LB10_%=:\n" \
  "s_barrier\n" \
  "buffer_load_dwordx4 v[112:115], v15, s[36:39], s65 offen sc1\n" \
  "s_barrier\n" \
  "buffer_load_dwordx4 v[116:119], v15, s[36:39], s65 offen offset:128 sc1\n" \
  "s_barrier\n" \
  "buffer_load_dwordx4 v[120:123], v16, s[36:39], s65 offen sc1\n" \
  "s_barrier\n" \
  "buffer_load_dwordx4 v[124:127], v16, s[36:39], s65 offen offset:512 sc1\n" \
  "s_barrier\n" \
  "buffer_load_dwordx4 v[128:131], v16, s[36:39], s65 offen offset:1024 sc1\n" \
  "s_waitcnt vmcnt(7)\n" \
  "s_barrier\n" \
  "buffer_load_dwordx4 v[132:135], v16, s[36:39], s65 offen offset:1536 sc1\n" \
  "ds_read_b128 v[40:43], v19\n" \
  "ds_read_b128 v[44:47], v19 offset:512\n" \
  "ds_read_b128 v[48:51], v19 offset:1024\n" \
  "ds_read_b128 v[52:55], v19 offset:1536\n" \
  "s_mov_b64 exec, s[46:47]\n" \
  "ds_read_b128 v[56:59], v20\n" \
  "ds_read_b128 v[60:63], v20 offset:128\n" \
  "ds_read_b128 v[64:67], v20 offset:256\n" \
  "ds_read_b128 v[68:71], v20 offset:384\n" \
  "s_mov_b64 exec, -1\n" \
  "s_nop 15\n" \
  "s_nop 1\n" \
  "buffer_load_dwordx4 v[136:139], v17, s[36:39], s65 offen sc1\n" \
  "buffer_load_dwordx4 v[140:143], v18, s[36:39], s65 offen sc1\n" \
  "s_waitcnt lgkmcnt(0)\n" \
  "s_getreg_b32 s71, hwreg(HW_REG_IB_STS, 0, 4)\n" \
  "s_cmp_le_u32 s71, 6\n" \
  "v_mfma_f32_16x16x32_bf16 v[144:147], v[72:75], v[72:75], 0\n" \
  "s_cbranch_scc1 LHXS10_%=\n" \
  "s_getreg_b32 s71, hwreg(HW_REG_IB_STS, 0, 4)\n" \
  "s_cmp_le_u32 s71, 6\n" \
  "v_mfma_f32_16x16x32_bf16 v[144:147], v[72:75], v[72:75], 0\n" \
  "s_cbranch_scc1 LHXS10_%=\n" \
  "s_getreg_b32 s71, hwreg(HW_REG_IB_STS, 0, 4)\n" \
  "s_cmp_le_u32 s71, 6\n" \
  "v_mfma_f32_16x16x32_bf16 v[144:147], v[72:75], v[72:75], 0\n" \
  "s_cbranch_scc1 LHXS10_%=\n" \
  "s_getreg_b32 s71, hwreg(HW_REG_IB_STS, 0, 4)\n" \
  "s_cmp_le_u32 s71, 6\n" \
  "v_mfma_f32_16x16x32_bf16 v[144:147], v[72:75], v[72:75], 0\n" \
  "s_cbranch_scc1 LHXS10_%=\n" \
  "s_getreg_b32 s71, hwreg(HW_REG_IB_STS, 0, 4)\n" \
  "s_cmp_le_u32 s71, 6\n" \
  "v_mfma_f32_16x16x32_bf16 v[144:147], v[72:75], v[72:75], 0\n" \
  "s_cbranch_scc1 LHXS10_%=\n" \
  "s_getreg_b32 s71, hwreg(HW_REG_IB_STS, 0, 4)\n" \
  "s_cmp_le_u32 s71, 6\n" \
  "v_mfma_f32_16x16x32_bf16 v[144:147], v[72:75], v[72:75], 0\n" \
  "s_cbranch_scc1 LHXS10_%=\n" \
  "s_getreg_b32 s71, hwreg(HW_REG_IB_STS, 0, 4)\n" \
  "s_cmp_le_u32 s71, 6\n" \
  "v_mfma_f32_16x16x32_bf16 v[144:147], v[72:75], v[72:75], 0\n" \
  "s_cbranch_scc1 LHXS10_%=\n" \
  "s_getreg_b32 s71, hwreg(HW_REG_IB_STS, 0, 4)\n" \
  "s_cmp_le_u32 s71, 6\n" \
  "v_mfma_f32_16x16x32_bf16 v[144:147], v[72:75], v[72:75], 0\n" \
  "s_cbranch_scc1 LHXS10_%=\n" \
  "s_getreg_b32 s71, hwreg(HW_REG_IB_STS, 0, 4)\n" \
  "s_cmp_le_u32 s71, 6\n" \
  "v_mfma_f32_16x16x32_bf16 v[144:147], v[72:75], v[72:75], 0\n" \
  "s_cbranch_scc1 LHXS10_%=\n" \
  "s_getreg_b32 s71, hwreg(HW_REG_IB_STS, 0, 4)\n" \
  "s_cmp_le_u32 s71, 6\n" \
  "v_mfma_f32_16x16x32_bf16 v[144:147], v[72:75], v[72:75], 0\n" \
  "s_cbranch_scc1 LHXS10_%=\n" \
  "s_getreg_b32 s71, hwreg(HW_REG_IB_STS, 0, 4)\n" \
  "s_cmp_le_u32 s71, 6\n" \
  "v_mfma_f32_16x16x32_bf16 v[144:147], v[72:75], v[72:75], 0\n" \
  "s_cbranch_scc1 LHXS10_%=\n" \
  "s_getreg_b32 s71, hwreg(HW_REG_IB_STS, 0, 4)\n" \
  "s_cmp_le_u32 s71, 6\n" \
  "v_mfma_f32_16x16x32_bf16 v[144:147], v[72:75], v[72:75], 0\n" \
  "s_cbranch_scc1 LHXS10_%=\n" \
  "s_getreg_b32 s71, hwreg(HW_REG_IB_STS, 0, 4)\n" \
  "s_cmp_le_u32 s71, 6\n" \
  "v_mfma_f32_16x16x32_bf16 v[144:147], v[72:75], v[72:75], 0\n" \
  "s_cbranch_scc1 LHXS10_%=\n" \
  "s_getreg_b32 s71, hwreg(HW_REG_IB_STS, 0, 4)\n" \
  "s_cmp_le_u32 s71, 6\n" \
  "v_mfma_f32_16x16x32_bf16 v[144:147], v[72:75], v[72:75], 0\n" \
  "s_cbranch_scc1 LHXS10_%=\n" \
  "s_getreg_b32 s71, hwreg(HW_REG_IB_STS, 0, 4)\n" \
  "s_cmp_le_u32 s71, 6\n" \
  "v_mfma_f32_16x16x32_bf16 v[144:147], v[72:75], v[72:75], 0\n" \
  "s_cbranch_scc1 LHXS10_%=\n" \
  "s_getreg_b32 s71, hwreg(HW_REG_IB_STS, 0, 4)\n" \
  "s_cmp_le_u32 s71, 6\n" \
  "v_mfma_f32_16x16x32_bf16 v[144:147], v[72:75], v[72:75], 0\n" \
  "s_cbranch_scc1 LHXS10_%=\n" \
  "s_getreg_b32 s71, hwreg(HW_REG_IB_STS, 0, 4)\n" \
  "s_cmp_le_u32 s71, 6\n" \
  "v_mfma_f32_16x16x32_bf16 v[144:147], v[72:75], v[72:75], 0\n" \
  "s_cbranch_scc1 LHXS10_%=\n" \
  "s_getreg_b32 s71, hwreg(HW_REG_IB_STS, 0, 4)\n" \
  "s_cmp_le_u32 s71, 6\n" \
  "v_mfma_f32_16x16x32_bf16 v[144:147], v[72:75], v[72:75], 0\n" \
  "s_cbranch_scc1 LHXS10_%=\n" \
  "s_getreg_b32 s71, hwreg(HW_REG_IB_STS, 0, 4)\n" \
  "s_cmp_le_u32 s71, 6\n" \
  "v_mfma_f32_16x16x32_bf16 v[144:147], v[72:75], v[72:75], 0\n" \
  "s_cbranch_scc1 LHXS10_%=\n" \
  "s_getreg_b32 s71, hwreg(HW_REG_IB_STS, 0, 4)\n" \
  "s_cmp_le_u32 s71, 6\n" \
  "v_mfma_f32_16x16x32_bf16 v[144:147], v[72:75], v[72:75], 0\n" \
  "s_cbranch_scc1 LHXS10_%=\n" \
  "s_waitcnt vmcnt(6)\n" \
  "LHXS10_%=:\n" \
  "LS210_%=:\n" \
  "v_dot2_f32_bf16 v176, v112, s22, 0\n" \
  "v_dot2_f32_bf16 v177, v112, s23, 0\n" \
  "v_dot2_f32_bf16 v178, v113, s22, 0\n" \
  "v_dot2_f32_bf16 v179, v113, s23, 0\n" \
  "v_dot2_f32_bf16 v180, v114, s22, 0\n" \
  "v_dot2_f32_bf16 v181, v114, s23, 0\n" \
  "v_dot2_f32_bf16 v182, v115, s22, 0\n" \
  "v_dot2_f32_bf16 v183, v115, s23, 0\n" \
  "v_dot2_f32_bf16 v176, v116, s22, v176\n" \
  "v_dot2_f32_bf16 v177, v116, s23, v177\n" \
  "v_dot2_f32_bf16 v178, v117, s22, v178\n" \
  "v_dot2_f32_bf16 v179, v117, s23, v179\n" \
  "v_dot2_f32_bf16 v180, v118, s22, v180\n" \
  "v_dot2_f32_bf16 v181, v118, s23, v181\n" \
  "v_dot2_f32_bf16 v182, v119, s22, v182\n" \
  "v_dot2_f32_bf16 v183, v119, s23, v183\n" \
  "v_exp_f32 v176, v176\n" \
  "v_exp_f32 v177, v177\n" \
  "v_exp_f32 v178, v178\n" \
  "v_exp_f32 v179, v179\n" \
  "s_waitcnt vmcnt(5)\n" \
  "v_mfma_f32_16x16x32_bf16 v[160:163], v[56:59], v[120:123], 0\n" \
  "v_exp_f32 v180, v180\n" \
  "v_mfma_f32_16x16x32_bf16 v[144:147], v[40:43], v[120:123], 0\n" \
  "v_exp_f32 v181, v181\n" \
  "v_exp_f32 v182, v182\n" \
  "v_exp_f32 v183, v183\n" \
  "v_cvt_pk_bf16_f32 v184, v176, v177\n" \
  "v_cvt_pk_bf16_f32 v185, v178, v179\n" \
  "v_cvt_pk_bf16_f32 v186, v180, v181\n" \
  "v_cvt_pk_bf16_f32 v187, v182, v183\n" \
  "s_nop 1\n" \
  "v_mfma_f32_16x16x32_bf16 v[160:163], v[72:75], v[184:187], v[160:163]\n" \
  "v_mfma_f32_16x16x32_bf16 v[164:167], v[76:79], v[184:187], 0\n" \
  "s_waitcnt vmcnt(4)\n" \
  "v_mfma_f32_16x16x32_bf16 v[164:167], v[60:63], v[124:127], v[164:167]\n" \
  "v_rcp_f32 v188, v162\n" \
  "v_mfma_f32_16x16x32_bf16 v[148:151], v[44:47], v[124:127], 0\n" \
  "v_mul_f32 v144, v188, v144\n" \
  "v_mul_f32 v145, v188, v145\n" \
  "v_mfma_f32_16x16x32_bf16 v[168:171], v[80:83], v[184:187], 0\n" \
  "s_waitcnt vmcnt(3)\n" \
  "v_rcp_f32 v189, v166\n" \
  "v_mfma_f32_16x16x32_bf16 v[168:171], v[64:67], v[128:131], v[168:171]\n" \
  "v_mul_f32 v146, v188, v146\n" \
  "v_mul_f32 v147, v188, v147\n" \
  "v_mfma_f32_16x16x32_bf16 v[152:155], v[48:51], v[128:131], 0\n" \
  "v_mul_f32 v160, v188, v160\n" \
  "v_mul_f32 v161, v188, v161\n" \
  "v_mfma_f32_16x16x32_bf16 v[172:175], v[84:87], v[184:187], 0\n" \
  "s_waitcnt vmcnt(2)\n" \
  "v_rcp_f32 v190, v170\n" \
  "v_mfma_f32_16x16x32_bf16 v[172:175], v[68:71], v[132:135], v[172:175]\n" \
  "v_cndmask_b32 v167, v188, v189, s[28:29]\n" \
  "v_cndmask_b32 v167, v167, v190, s[26:27]\n" \
  "v_mfma_f32_16x16x32_bf16 v[156:159], v[52:55], v[132:135], 0\n" \
  "v_fmac_f32 v144, v189, v148\n" \
  "v_fmac_f32 v145, v189, v149\n" \
  "v_fmac_f32 v146, v189, v150\n" \
  "v_fmac_f32 v147, v189, v151\n" \
  "v_rcp_f32 v191, v174\n" \
  "v_fmac_f32 v160, v189, v164\n" \
  "v_cndmask_b32 v167, v167, v191, s[24:25]\n" \
  "v_pk_mul_f32 v[176:177], v[176:177], v[166:167] op_sel:[0,1] op_sel_hi:[1,1]\n" \
  "v_pk_mul_f32 v[178:179], v[178:179], v[166:167] op_sel:[0,1] op_sel_hi:[1,1]\n" \
  "v_pk_mul_f32 v[180:181], v[180:181], v[166:167] op_sel:[0,1] op_sel_hi:[1,1]\n" \
  "v_pk_mul_f32 v[182:183], v[182:183], v[166:167] op_sel:[0,1] op_sel_hi:[1,1]\n" \
  "v_cmp_nlt_f32 vcc, 0x0d800000, v167\n" \
  "v_cvt_pk_bf16_f32 v184, v176, v177\n" \
  "v_cvt_pk_bf16_f32 v185, v178, v179\n" \
  "v_cvt_pk_bf16_f32 v186, v180, v181\n" \
  "v_cvt_pk_bf16_f32 v187, v182, v183\n" \
  "v_fmac_f32 v161, v189, v165\n" \
  "v_pk_fma_f32 v[160:161], v[168:169], v[190:191], v[160:161] op_sel:[0,0,0] op_sel_hi:[1,0,1]\n" \
  "v_pk_fma_f32 v[160:161], v[172:173], v[190:191], v[160:161] op_sel:[0,1,0] op_sel_hi:[1,1,1]\n" \
  "v_pk_fma_f32 v[144:145], v[152:153], v[190:191], v[144:145] op_sel:[0,0,0] op_sel_hi:[1,0,1]\n" \
  "v_pk_fma_f32 v[146:147], v[154:155], v[190:191], v[146:147] op_sel:[0,0,0] op_sel_hi:[1,0,1]\n" \
  "v_pk_fma_f32 v[144:145], v[156:157], v[190:191], v[144:145] op_sel:[0,1,0] op_sel_hi:[1,1,1]\n" \
  "v_pk_fma_f32 v[146:147], v[158:159], v[190:191], v[146:147] op_sel:[0,1,0] op_sel_hi:[1,1,1]\n" \
  "s_waitcnt vmcnt(0)\n" \
  "v_mfma_f32_16x16x32_bf16 v[160:163], v[140:143], v[184:187], v[160:163]\n" \
  "s_mov_b64 s[80:81], vcc\n" \
  "v_mfma_f32_16x16x32_bf16 v[144:147], v[136:139], v[184:187], v[144:147]\n" \
  "s_mov_b64 exec, s[30:31]\n" \
  "s_nop 2\n" \
  "ds_write_b64 v22, v[160:161]\n" \
  "v_cmp_u_f32 s[78:79], v160, v161\n" \
  "s_nop 1\n" \
  "ds_write2_b64 v21, v[144:145], v[146:147] offset1:16\n" \
  "v_add_f32 v188, v144, v145\n" \
  "v_add_f32 v189, v146, v147\n" \
  "v_cmp_u_f32 vcc, v188, v189\n" \
  "s_or_b64 s[80:81], s[80:81], s[78:79]\n" \
  "s_or_b64 vcc, vcc, s[80:81]\n" \
  "s_cbranch_vccnz LSF10_%=\n" \
  "LSOK10_%=:\n" \
  "s_mov_b64 exec, -1\n" \
  "s_waitcnt lgkmcnt(0)\n" \
  "s_barrier\n" \
  "ds_read_b32 v188, v33\n" \
  "s_waitcnt lgkmcnt(0)\n" \
  "v_cmp_ne_u32 vcc, 0, v188\n" \
  "s_cbranch_vccnz LSR10_%=\n" \
  "LNL10_%=:\n" \
  "s_waitcnt lgkmcnt(0)\n" \
  "s_cmp_eq_u32 s73, 1\n" \
  "s_cbranch_scc1 LOOP10_%=\n" \
  "s_branch LDONE_%=\n" \
  "LMF10_%=:\n" \
  "v_mbcnt_lo_u32_b32 v130, -1, 0\n" \
  "v_mbcnt_hi_u32_b32 v130, -1, v130\n" \
  "v_lshlrev_b32 v130, 4, v130\n" \
  "v_mov_b32 v128, 0\n" \
  "ds_read_b128 v[132:135], v130 offset:0\n" \
  "ds_read_b128 v[136:139], v130 offset:1024\n" \
  "ds_read_b128 v[140:143], v130 offset:2048\n" \
  "ds_read_b128 v[144:147], v130 offset:3072\n" \
  "ds_read_b128 v[148:151], v130 offset:4096\n" \
  "s_waitcnt lgkmcnt(0)\n" \
  "v_max3_u32 v128, v128, v132, v133\n" \
  "v_max3_u32 v128, v128, v134, v135\n" \
  "v_max3_u32 v128, v128, v136, v137\n" \
  "v_max3_u32 v128, v128, v138, v139\n" \
  "v_max3_u32 v128, v128, v140, v141\n" \
  "v_max3_u32 v128, v128, v142, v143\n" \
  "v_max3_u32 v128, v128, v144, v145\n" \
  "v_max3_u32 v128, v128, v146, v147\n" \
  "v_max3_u32 v128, v128, v148, v149\n" \
  "v_max3_u32 v128, v128, v150, v151\n" \
  "ds_read_b128 v[132:135], v130 offset:5120\n" \
  "ds_read_b128 v[136:139], v130 offset:6144\n" \
  "ds_read_b128 v[140:143], v130 offset:7168\n" \
  "ds_read_b128 v[144:147], v130 offset:8192\n" \
  "ds_read_b128 v[148:151], v130 offset:9216\n" \
  "s_waitcnt lgkmcnt(0)\n" \
  "v_max3_u32 v128, v128, v132, v133\n" \
  "v_max3_u32 v128, v128, v134, v135\n" \
  "v_max3_u32 v128, v128, v136, v137\n" \
  "v_max3_u32 v128, v128, v138, v139\n" \
  "v_max3_u32 v128, v128, v140, v141\n" \
  "v_max3_u32 v128, v128, v142, v143\n" \
  "v_max3_u32 v128, v128, v144, v145\n" \
  "v_max3_u32 v128, v128, v146, v147\n" \
  "v_max3_u32 v128, v128, v148, v149\n" \
  "v_max3_u32 v128, v128, v150, v151\n" \
  "v_cmp_eq_u32 vcc, -1, v128\n" \
  "s_cbranch_vccz LMOK10_%=\n" \
  "s_cmp_gt_u32 s58, 4000\n" \
  "s_cbranch_scc1 LMOK10_%=\n" \
  "s_add_u32 s58, s58, 1\n" \
  "s_sub_u32 s64, s64, 1\n" \
  "s_mov_b32 s66, s65\n" \
  "s_mov_b32 s68, s69\n" \
  "s_waitcnt vmcnt(0) lgkmcnt(0)\n" \
  "s_barrier\n" \
  "s_branch LOOP10_%=\n" \
  "LSF10_%=:\n" \
  "s_getpc_b64 s[78:79]\n" \
  "s_branch LSF_%=\n" \
  "s_branch LSOK10_%=\n" \
  "LSR10_%=:\n" \
  "v_mov_b32 v188, 0\n" \
  "s_add_u32 s59, s59, 1\n" \
  "s_barrier\n" \
  "ds_write_b32 v33, v188\n" \
  "s_waitcnt vmcnt(0) lgkmcnt(0)\n" \
  "s_branch LB10_%=\n" \
  "LSTART11_%=:\n" \
  "LOOP11_%=:\n" \
  "v_readlane_b32 s70, v27, s64\n" \
  "s_mov_b32 s65, s66\n" \
  "s_mov_b32 s69, s68\n" \
  "s_add_u32 s64, s64, 1\n" \
  "s_barrier\n" \
  "s_mov_b32 m0, 28672\n" \
  "s_add_u32 s72, s70, 4096\n" \
  "buffer_load_dwordx4 v3, s[36:39], s70 offen sc1 lds\n" \
  "buffer_load_dwordx4 v3, s[36:39], s70 offen offset:1024 sc1 lds\n" \
  "buffer_load_dwordx4 v3, s[36:39], s70 offen offset:2048 sc1 lds\n" \
  "buffer_load_dwordx4 v3, s[36:39], s70 offen offset:3072 sc1 lds\n" \
  "s_mov_b32 m0, 32768\n" \
  "s_nop 0\n" \
  "buffer_load_dwordx4 v3, s[36:39], s72 offen sc1 lds\n" \
  "buffer_load_dwordx4 v3, s[36:39], s72 offen offset:1024 sc1 lds\n" \
  "buffer_load_dwordx4 v3, s[36:39], s72 offen offset:2048 sc1 lds\n" \
  "buffer_load_dwordx4 v3, s[36:39], s72 offen offset:3072 sc1 lds\n" \
  "v_mov_b32 v176, 0\n" \
  "v_mov_b32 v177, 0\n" \
  "v_mov_b32 v178, 0\n" \
  "v_mov_b32 v179, 0\n" \
  "v_mov_b32 v180, 0\n" \
  "v_mov_b32 v181, 0\n" \
  "v_mov_b32 v182, 0\n" \
  "v_mov_b32 v183, 0\n" \
  "v_readlane_b32 s66, v25, s64\n" \
  "v_readlane_b32 s68, v26, s64\n" \
  "s_waitcnt lgkmcnt(0)\n" \
  "LM11_%=:\n" \
  "s_barrier\n" \
  "ds_read_b128 v[112:115], v5\n" \
  "ds_read_b128 v[116:119], v5 offset:512\n" \
  "s_barrier\n" \
  "ds_read_b128 v[120:123], v5 offset:1024\n" \
  "ds_read_b128 v[124:127], v5 offset:1536\n" \
  "s_waitcnt lgkmcnt(3)\n" \
  "v_smfmac_f32_16x16x64_bf16 v[176:179], v[112:115], a[0:7], v0\n" \
  "v_smfmac_f32_16x16x64_bf16 v[180:183], v[112:115], a[160:167], v0\n" \
  "s_waitcnt lgkmcnt(2)\n" \
  "v_smfmac_f32_16x16x64_bf16 v[176:179], v[116:119], a[8:15], v0\n" \
  "s_barrier\n" \
  "ds_read_b128 v[128:131], v5 offset:2048\n" \
  "v_smfmac_f32_16x16x64_bf16 v[180:183], v[116:119], a[168:175], v0\n" \
  "s_waitcnt lgkmcnt(2)\n" \
  "ds_read_b128 v[132:135], v5 offset:2560\n" \
  "v_smfmac_f32_16x16x64_bf16 v[176:179], v[120:123], a[16:23], v0\n" \
  "ds_read_b128 v[136:139], v5 offset:3072\n" \
  "v_smfmac_f32_16x16x64_bf16 v[180:183], v[120:123], a[176:183], v0\n" \
  "s_waitcnt lgkmcnt(3)\n" \
  "ds_read_b128 v[140:143], v5 offset:3584\n" \
  "v_smfmac_f32_16x16x64_bf16 v[176:179], v[124:127], a[24:31], v0\n" \
  "s_barrier\n" \
  "ds_read_b128 v[144:147], v5 offset:4096\n" \
  "v_smfmac_f32_16x16x64_bf16 v[180:183], v[124:127], a[184:191], v0\n" \
  "s_waitcnt lgkmcnt(4)\n" \
  "ds_read_b128 v[148:151], v5 offset:4608\n" \
  "v_smfmac_f32_16x16x64_bf16 v[176:179], v[128:131], a[32:39], v0\n" \
  "ds_read_b128 v[152:155], v5 offset:5120\n" \
  "v_smfmac_f32_16x16x64_bf16 v[180:183], v[128:131], a[192:199], v0\n" \
  "s_waitcnt lgkmcnt(5)\n" \
  "ds_read_b128 v[156:159], v5 offset:5632\n" \
  "v_smfmac_f32_16x16x64_bf16 v[176:179], v[132:135], a[40:47], v0\n" \
  "s_barrier\n" \
  "ds_read_b128 v[160:163], v5 offset:6144\n" \
  "v_smfmac_f32_16x16x64_bf16 v[180:183], v[132:135], a[200:207], v0\n" \
  "s_waitcnt lgkmcnt(6)\n" \
  "ds_read_b128 v[164:167], v5 offset:6656\n" \
  "v_smfmac_f32_16x16x64_bf16 v[176:179], v[136:139], a[48:55], v0\n" \
  "ds_read_b128 v[168:171], v5 offset:7168\n" \
  "v_smfmac_f32_16x16x64_bf16 v[180:183], v[136:139], a[208:215], v0\n" \
  "s_waitcnt lgkmcnt(7)\n" \
  "ds_read_b128 v[172:175], v5 offset:7680\n" \
  "v_smfmac_f32_16x16x64_bf16 v[176:179], v[140:143], a[56:63], v0\n" \
  "s_barrier\n" \
  "ds_read2_b32 v[112:113], v6 offset0:0 offset1:32\n" \
  "v_smfmac_f32_16x16x64_bf16 v[180:183], v[140:143], a[216:223], v0\n" \
  "s_waitcnt lgkmcnt(8)\n" \
  "ds_read2_b32 v[114:115], v6 offset0:64 offset1:96\n" \
  "v_smfmac_f32_16x16x64_bf16 v[176:179], v[144:147], a[64:71], v0\n" \
  "s_waitcnt lgkmcnt(8)\n" \
  "ds_read2_b32 v[116:117], v6 offset0:128 offset1:160\n" \
  "v_smfmac_f32_16x16x64_bf16 v[176:179], v[148:151], a[72:79], v0\n" \
  "s_waitcnt lgkmcnt(8)\n" \
  "ds_read2_b32 v[118:119], v6 offset0:192 offset1:224\n" \
  "v_smfmac_f32_16x16x64_bf16 v[176:179], v[152:155], a[80:87], v0\n" \
  "s_waitcnt lgkmcnt(8)\n" \
  "ds_read2_b32 v[120:121], v7 offset0:0 offset1:32\n" \
  "v_smfmac_f32_16x16x64_bf16 v[176:179], v[156:159], a[88:95], v0\n" \
  "s_waitcnt lgkmcnt(8)\n" \
  "ds_read2_b32 v[122:123], v7 offset0:64 offset1:96\n" \
  "v_smfmac_f32_16x16x64_bf16 v[176:179], v[160:163], a[96:103], v0\n" \
  "s_waitcnt lgkmcnt(8)\n" \
  "ds_read2_b32 v[124:125], v7 offset0:128 offset1:160\n" \
  "v_smfmac_f32_16x16x64_bf16 v[176:179], v[164:167], a[104:111], v0\n" \
  "s_waitcnt lgkmcnt(8)\n" \
  "ds_read2_b32 v[126:127], v7 offset0:192 offset1:224\n" \
  "v_smfmac_f32_16x16x64_bf16 v[176:179], v[168:171], a[112:119], v0\n" \
  "s_waitcnt lgkmcnt(8)\n" \
  "v_smfmac_f32_16x16x64_bf16 v[176:179], v[172:175], a[120:127], v0\n" \
  "s_waitcnt lgkmcnt(6)\n" \
  "v_smfmac_f32_16x16x64_bf16 v[176:179], v[112:115], a[128:135], v0\n" \
  "s_waitcnt lgkmcnt(4)\n" \
  "v_smfmac_f32_16x16x64_bf16 v[176:179], v[116:119], a[136:143], v0\n" \
  "s_waitcnt lgkmcnt(2)\n" \
  "v_smfmac_f32_16x16x64_bf16 v[176:179], v[120:123], a[144:151], v0\n" \
  "s_waitcnt lgkmcnt(0)\n" \
  "v_smfmac_f32_16x16x64_bf16 v[176:179], v[124:127], a[152:159], v0\n" \
  "v_smfmac_f32_16x16x64_bf16 v[180:183], v[144:147], a[224:231], v0\n" \
  "v_smfmac_f32_16x16x64_bf16 v[180:183], v[148:151], a[232:239], v0\n" \
  "v_cvt_pk_bf16_f32 v184, v176, v177\n" \
  "v_smfmac_f32_16x16x64_bf16 v[180:183], v[152:155], a[240:247], v0\n" \
  "v_cvt_pk_bf16_f32 v185, v178, v179\n" \
  "v_smfmac_f32_16x16x64_bf16 v[180:183], v[156:159], a[248:255], v0\n" \
  "ds_write_b64 v8, v[184:185]\n" \
  "v_add_f32 v186, v176, v177\n" \
  "v_smfmac_f32_16x16x64_bf16 v[180:183], v[160:163], v[192:199], v0\n" \
  "v_add_f32 v187, v178, v179\n" \
  "v_smfmac_f32_16x16x64_bf16 v[180:183], v[164:167], v[200:207], v0\n" \
  "v_cmp_u_f32 vcc, v186, v187\n" \
  "v_smfmac_f32_16x16x64_bf16 v[180:183], v[168:171], v[208:215], v0\n" \
  "v_smfmac_f32_16x16x64_bf16 v[180:183], v[172:175], v[216:223], v0\n" \
  "s_cbranch_vccnz LMF11_%=\n" \
  "LMOK11_%=:\n" \
  "v_smfmac_f32_16x16x64_bf16 v[180:183], v[112:115], v[224:231], v0\n" \
  "s_waitcnt lgkmcnt(0)\n" \
  "s_barrier\n" \
  "v_smfmac_f32_16x16x64_bf16 v[180:183], v[116:119], v[232:239], v0\n" \
  "v_smfmac_f32_16x16x64_bf16 v[180:183], v[120:123], v[240:247], v0\n" \
  "v_smfmac_f32_16x16x64_bf16 v[180:183], v[124:127], v[248:255], v0\n" \
  "s_nop 3\n" \
  "v_mov_b32 v31, v31\n" \
  "s_nop 2\n" \
  "v_cvt_pk_bf16_f32 v186, v180, v181\n" \
  "v_cvt_pk_bf16_f32 v187, v182, v183\n" \
  "ds_write_b64 v9, v[186:187]\n" \
  "s_waitcnt lgkmcnt(0)\n" \
  "s_barrier\n" \
  "s_nop 15\n" \
  "s_nop 0\n" \
  "ds_read_b64_tr_b16 v[128:129], v12\n" \
  "ds_read_b64_tr_b16 v[130:131], v12 offset:32\n" \
  "s_waitcnt lgkmcnt(0)\n" \
  "buffer_store_dwordx4 v[128:131], v14, s[36:39], s69 offen sc1\n" \
  "s_mov_b32 s67, s66\n" \
  "s_cmp_eq_u32 s64, 64\n" \
  "s_cbranch_scc1 LDONE_%=\n" \
  "LB11_%=:\n" \
  "s_barrier\n" \
  "buffer_load_dwordx4 v[112:115], v15, s[36:39], s65 offen sc1\n" \
  "s_barrier\n" \
  "buffer_load_dwordx4 v[116:119], v15, s[36:39], s65 offen offset:128 sc1\n" \
  "s_barrier\n" \
  "buffer_load_dwordx4 v[120:123], v16, s[36:39], s65 offen sc1\n" \
  "s_barrier\n" \
  "buffer_load_dwordx4 v[124:127], v16, s[36:39], s65 offen offset:512 sc1\n" \
  "s_barrier\n" \
  "buffer_load_dwordx4 v[128:131], v16, s[36:39], s65 offen offset:1024 sc1\n" \
  "s_waitcnt vmcnt(6)\n" \
  "s_barrier\n" \
  "buffer_load_dwordx4 v[132:135], v16, s[36:39], s65 offen offset:1536 sc1\n" \
  "ds_read_b128 v[40:43], v19\n" \
  "ds_read_b128 v[44:47], v19 offset:512\n" \
  "ds_read_b128 v[48:51], v19 offset:1024\n" \
  "ds_read_b128 v[52:55], v19 offset:1536\n" \
  "s_mov_b64 exec, s[46:47]\n" \
  "ds_read_b128 v[56:59], v20\n" \
  "ds_read_b128 v[60:63], v20 offset:128\n" \
  "ds_read_b128 v[64:67], v20 offset:256\n" \
  "ds_read_b128 v[68:71], v20 offset:384\n" \
  "s_mov_b64 exec, -1\n" \
  "s_nop 15\n" \
  "s_nop 1\n" \
  "buffer_load_dwordx4 v[136:139], v17, s[36:39], s65 offen sc1\n" \
  "buffer_load_dwordx4 v[140:143], v18, s[36:39], s65 offen sc1\n" \
  "s_waitcnt lgkmcnt(0)\n" \
  "s_getreg_b32 s71, hwreg(HW_REG_IB_STS, 0, 4)\n" \
  "s_cmp_le_u32 s71, 6\n" \
  "v_mfma_f32_16x16x32_bf16 v[144:147], v[72:75], v[72:75], 0\n" \
  "s_cbranch_scc1 LHXS11_%=\n" \
  "s_getreg_b32 s71, hwreg(HW_REG_IB_STS, 0, 4)\n" \
  "s_cmp_le_u32 s71, 6\n" \
  "v_mfma_f32_16x16x32_bf16 v[144:147], v[72:75], v[72:75], 0\n" \
  "s_cbranch_scc1 LHXS11_%=\n" \
  "s_getreg_b32 s71, hwreg(HW_REG_IB_STS, 0, 4)\n" \
  "s_cmp_le_u32 s71, 6\n" \
  "v_mfma_f32_16x16x32_bf16 v[144:147], v[72:75], v[72:75], 0\n" \
  "s_cbranch_scc1 LHXS11_%=\n" \
  "s_getreg_b32 s71, hwreg(HW_REG_IB_STS, 0, 4)\n" \
  "s_cmp_le_u32 s71, 6\n" \
  "v_mfma_f32_16x16x32_bf16 v[144:147], v[72:75], v[72:75], 0\n" \
  "s_cbranch_scc1 LHXS11_%=\n" \
  "s_getreg_b32 s71, hwreg(HW_REG_IB_STS, 0, 4)\n" \
  "s_cmp_le_u32 s71, 6\n" \
  "v_mfma_f32_16x16x32_bf16 v[144:147], v[72:75], v[72:75], 0\n" \
  "s_cbranch_scc1 LHXS11_%=\n" \
  "s_getreg_b32 s71, hwreg(HW_REG_IB_STS, 0, 4)\n" \
  "s_cmp_le_u32 s71, 6\n" \
  "v_mfma_f32_16x16x32_bf16 v[144:147], v[72:75], v[72:75], 0\n" \
  "s_cbranch_scc1 LHXS11_%=\n" \
  "s_getreg_b32 s71, hwreg(HW_REG_IB_STS, 0, 4)\n" \
  "s_cmp_le_u32 s71, 6\n" \
  "v_mfma_f32_16x16x32_bf16 v[144:147], v[72:75], v[72:75], 0\n" \
  "s_cbranch_scc1 LHXS11_%=\n" \
  "s_getreg_b32 s71, hwreg(HW_REG_IB_STS, 0, 4)\n" \
  "s_cmp_le_u32 s71, 6\n" \
  "v_mfma_f32_16x16x32_bf16 v[144:147], v[72:75], v[72:75], 0\n" \
  "s_cbranch_scc1 LHXS11_%=\n" \
  "s_getreg_b32 s71, hwreg(HW_REG_IB_STS, 0, 4)\n" \
  "s_cmp_le_u32 s71, 6\n" \
  "v_mfma_f32_16x16x32_bf16 v[144:147], v[72:75], v[72:75], 0\n" \
  "s_cbranch_scc1 LHXS11_%=\n" \
  "s_getreg_b32 s71, hwreg(HW_REG_IB_STS, 0, 4)\n" \
  "s_cmp_le_u32 s71, 6\n" \
  "v_mfma_f32_16x16x32_bf16 v[144:147], v[72:75], v[72:75], 0\n" \
  "s_cbranch_scc1 LHXS11_%=\n" \
  "s_getreg_b32 s71, hwreg(HW_REG_IB_STS, 0, 4)\n" \
  "s_cmp_le_u32 s71, 6\n" \
  "v_mfma_f32_16x16x32_bf16 v[144:147], v[72:75], v[72:75], 0\n" \
  "s_cbranch_scc1 LHXS11_%=\n" \
  "s_getreg_b32 s71, hwreg(HW_REG_IB_STS, 0, 4)\n" \
  "s_cmp_le_u32 s71, 6\n" \
  "v_mfma_f32_16x16x32_bf16 v[144:147], v[72:75], v[72:75], 0\n" \
  "s_cbranch_scc1 LHXS11_%=\n" \
  "s_getreg_b32 s71, hwreg(HW_REG_IB_STS, 0, 4)\n" \
  "s_cmp_le_u32 s71, 6\n" \
  "v_mfma_f32_16x16x32_bf16 v[144:147], v[72:75], v[72:75], 0\n" \
  "s_cbranch_scc1 LHXS11_%=\n" \
  "s_getreg_b32 s71, hwreg(HW_REG_IB_STS, 0, 4)\n" \
  "s_cmp_le_u32 s71, 6\n" \
  "v_mfma_f32_16x16x32_bf16 v[144:147], v[72:75], v[72:75], 0\n" \
  "s_cbranch_scc1 LHXS11_%=\n" \
  "s_getreg_b32 s71, hwreg(HW_REG_IB_STS, 0, 4)\n" \
  "s_cmp_le_u32 s71, 6\n" \
  "v_mfma_f32_16x16x32_bf16 v[144:147], v[72:75], v[72:75], 0\n" \
  "s_cbranch_scc1 LHXS11_%=\n" \
  "s_getreg_b32 s71, hwreg(HW_REG_IB_STS, 0, 4)\n" \
  "s_cmp_le_u32 s71, 6\n" \
  "v_mfma_f32_16x16x32_bf16 v[144:147], v[72:75], v[72:75], 0\n" \
  "s_cbranch_scc1 LHXS11_%=\n" \
  "s_getreg_b32 s71, hwreg(HW_REG_IB_STS, 0, 4)\n" \
  "s_cmp_le_u32 s71, 6\n" \
  "v_mfma_f32_16x16x32_bf16 v[144:147], v[72:75], v[72:75], 0\n" \
  "s_cbranch_scc1 LHXS11_%=\n" \
  "s_getreg_b32 s71, hwreg(HW_REG_IB_STS, 0, 4)\n" \
  "s_cmp_le_u32 s71, 6\n" \
  "v_mfma_f32_16x16x32_bf16 v[144:147], v[72:75], v[72:75], 0\n" \
  "s_cbranch_scc1 LHXS11_%=\n" \
  "s_getreg_b32 s71, hwreg(HW_REG_IB_STS, 0, 4)\n" \
  "s_cmp_le_u32 s71, 6\n" \
  "v_mfma_f32_16x16x32_bf16 v[144:147], v[72:75], v[72:75], 0\n" \
  "s_cbranch_scc1 LHXS11_%=\n" \
  "s_getreg_b32 s71, hwreg(HW_REG_IB_STS, 0, 4)\n" \
  "s_cmp_le_u32 s71, 6\n" \
  "v_mfma_f32_16x16x32_bf16 v[144:147], v[72:75], v[72:75], 0\n" \
  "s_cbranch_scc1 LHXS11_%=\n" \
  "s_waitcnt vmcnt(6)\n" \
  "LHXS11_%=:\n" \
  "LS211_%=:\n" \
  "v_dot2_f32_bf16 v176, v112, s22, 0\n" \
  "v_dot2_f32_bf16 v177, v112, s23, 0\n" \
  "v_dot2_f32_bf16 v178, v113, s22, 0\n" \
  "v_dot2_f32_bf16 v179, v113, s23, 0\n" \
  "v_dot2_f32_bf16 v180, v114, s22, 0\n" \
  "v_dot2_f32_bf16 v181, v114, s23, 0\n" \
  "v_dot2_f32_bf16 v182, v115, s22, 0\n" \
  "v_dot2_f32_bf16 v183, v115, s23, 0\n" \
  "v_dot2_f32_bf16 v176, v116, s22, v176\n" \
  "v_dot2_f32_bf16 v177, v116, s23, v177\n" \
  "v_dot2_f32_bf16 v178, v117, s22, v178\n" \
  "v_dot2_f32_bf16 v179, v117, s23, v179\n" \
  "v_dot2_f32_bf16 v180, v118, s22, v180\n" \
  "v_dot2_f32_bf16 v181, v118, s23, v181\n" \
  "v_dot2_f32_bf16 v182, v119, s22, v182\n" \
  "v_dot2_f32_bf16 v183, v119, s23, v183\n" \
  "v_exp_f32 v176, v176\n" \
  "v_exp_f32 v177, v177\n" \
  "v_exp_f32 v178, v178\n" \
  "v_exp_f32 v179, v179\n" \
  "s_waitcnt vmcnt(5)\n" \
  "v_mfma_f32_16x16x32_bf16 v[160:163], v[56:59], v[120:123], 0\n" \
  "v_exp_f32 v180, v180\n" \
  "v_mfma_f32_16x16x32_bf16 v[144:147], v[40:43], v[120:123], 0\n" \
  "v_exp_f32 v181, v181\n" \
  "v_exp_f32 v182, v182\n" \
  "v_exp_f32 v183, v183\n" \
  "v_cvt_pk_bf16_f32 v184, v176, v177\n" \
  "v_cvt_pk_bf16_f32 v185, v178, v179\n" \
  "v_cvt_pk_bf16_f32 v186, v180, v181\n" \
  "v_cvt_pk_bf16_f32 v187, v182, v183\n" \
  "s_nop 1\n" \
  "v_mfma_f32_16x16x32_bf16 v[160:163], v[72:75], v[184:187], v[160:163]\n" \
  "v_mfma_f32_16x16x32_bf16 v[164:167], v[76:79], v[184:187], 0\n" \
  "s_waitcnt vmcnt(4)\n" \
  "v_mfma_f32_16x16x32_bf16 v[164:167], v[60:63], v[124:127], v[164:167]\n" \
  "v_rcp_f32 v188, v162\n" \
  "v_mfma_f32_16x16x32_bf16 v[148:151], v[44:47], v[124:127], 0\n" \
  "v_mul_f32 v144, v188, v144\n" \
  "v_mul_f32 v145, v188, v145\n" \
  "v_mfma_f32_16x16x32_bf16 v[168:171], v[80:83], v[184:187], 0\n" \
  "s_waitcnt vmcnt(3)\n" \
  "v_rcp_f32 v189, v166\n" \
  "v_mfma_f32_16x16x32_bf16 v[168:171], v[64:67], v[128:131], v[168:171]\n" \
  "v_mul_f32 v146, v188, v146\n" \
  "v_mul_f32 v147, v188, v147\n" \
  "v_mfma_f32_16x16x32_bf16 v[152:155], v[48:51], v[128:131], 0\n" \
  "v_mul_f32 v160, v188, v160\n" \
  "v_mul_f32 v161, v188, v161\n" \
  "v_mfma_f32_16x16x32_bf16 v[172:175], v[84:87], v[184:187], 0\n" \
  "s_waitcnt vmcnt(2)\n" \
  "v_rcp_f32 v190, v170\n" \
  "v_mfma_f32_16x16x32_bf16 v[172:175], v[68:71], v[132:135], v[172:175]\n" \
  "v_cndmask_b32 v167, v188, v189, s[28:29]\n" \
  "v_cndmask_b32 v167, v167, v190, s[26:27]\n" \
  "v_mfma_f32_16x16x32_bf16 v[156:159], v[52:55], v[132:135], 0\n" \
  "v_fmac_f32 v144, v189, v148\n" \
  "v_fmac_f32 v145, v189, v149\n" \
  "v_fmac_f32 v146, v189, v150\n" \
  "v_fmac_f32 v147, v189, v151\n" \
  "v_rcp_f32 v191, v174\n" \
  "v_fmac_f32 v160, v189, v164\n" \
  "v_cndmask_b32 v167, v167, v191, s[24:25]\n" \
  "v_pk_mul_f32 v[176:177], v[176:177], v[166:167] op_sel:[0,1] op_sel_hi:[1,1]\n" \
  "v_pk_mul_f32 v[178:179], v[178:179], v[166:167] op_sel:[0,1] op_sel_hi:[1,1]\n" \
  "v_pk_mul_f32 v[180:181], v[180:181], v[166:167] op_sel:[0,1] op_sel_hi:[1,1]\n" \
  "v_pk_mul_f32 v[182:183], v[182:183], v[166:167] op_sel:[0,1] op_sel_hi:[1,1]\n" \
  "v_cmp_nlt_f32 vcc, 0x0d800000, v167\n" \
  "v_cvt_pk_bf16_f32 v184, v176, v177\n" \
  "v_cvt_pk_bf16_f32 v185, v178, v179\n" \
  "v_cvt_pk_bf16_f32 v186, v180, v181\n" \
  "v_cvt_pk_bf16_f32 v187, v182, v183\n" \
  "v_fmac_f32 v161, v189, v165\n" \
  "v_pk_fma_f32 v[160:161], v[168:169], v[190:191], v[160:161] op_sel:[0,0,0] op_sel_hi:[1,0,1]\n" \
  "v_pk_fma_f32 v[160:161], v[172:173], v[190:191], v[160:161] op_sel:[0,1,0] op_sel_hi:[1,1,1]\n" \
  "v_pk_fma_f32 v[144:145], v[152:153], v[190:191], v[144:145] op_sel:[0,0,0] op_sel_hi:[1,0,1]\n" \
  "v_pk_fma_f32 v[146:147], v[154:155], v[190:191], v[146:147] op_sel:[0,0,0] op_sel_hi:[1,0,1]\n" \
  "v_pk_fma_f32 v[144:145], v[156:157], v[190:191], v[144:145] op_sel:[0,1,0] op_sel_hi:[1,1,1]\n" \
  "v_pk_fma_f32 v[146:147], v[158:159], v[190:191], v[146:147] op_sel:[0,1,0] op_sel_hi:[1,1,1]\n" \
  "s_waitcnt vmcnt(0)\n" \
  "v_mfma_f32_16x16x32_bf16 v[160:163], v[140:143], v[184:187], v[160:163]\n" \
  "s_mov_b64 s[80:81], vcc\n" \
  "v_mfma_f32_16x16x32_bf16 v[144:147], v[136:139], v[184:187], v[144:147]\n" \
  "s_mov_b64 exec, s[30:31]\n" \
  "s_nop 2\n" \
  "ds_write_b64 v22, v[160:161]\n" \
  "v_cmp_u_f32 s[78:79], v160, v161\n" \
  "s_nop 1\n" \
  "ds_write2_b64 v21, v[144:145], v[146:147] offset1:16\n" \
  "v_add_f32 v188, v144, v145\n" \
  "v_add_f32 v189, v146, v147\n" \
  "v_cmp_u_f32 vcc, v188, v189\n" \
  "s_or_b64 s[80:81], s[80:81], s[78:79]\n" \
  "s_or_b64 vcc, vcc, s[80:81]\n" \
  "s_cbranch_vccnz LSF11_%=\n" \
  "LSOK11_%=:\n" \
  "s_mov_b64 exec, -1\n" \
  "s_waitcnt lgkmcnt(0)\n" \
  "s_barrier\n" \
  "ds_read_b32 v188, v33\n" \
  "s_waitcnt lgkmcnt(0)\n" \
  "v_cmp_ne_u32 vcc, 0, v188\n" \
  "s_cbranch_vccnz LSR11_%=\n" \
  "LNL11_%=:\n" \
  "s_waitcnt lgkmcnt(0)\n" \
  "s_cmp_eq_u32 s73, 1\n" \
  "s_cbranch_scc1 LOOP11_%=\n" \
  "s_branch LDONE_%=\n" \
  "LMF11_%=:\n" \
  "v_mbcnt_lo_u32_b32 v130, -1, 0\n" \
  "v_mbcnt_hi_u32_b32 v130, -1, v130\n" \
  "v_lshlrev_b32 v130, 4, v130\n" \
  "v_mov_b32 v128, 0\n" \
  "ds_read_b128 v[132:135], v130 offset:0\n" \
  "ds_read_b128 v[136:139], v130 offset:1024\n" \
  "ds_read_b128 v[140:143], v130 offset:2048\n" \
  "ds_read_b128 v[144:147], v130 offset:3072\n" \
  "ds_read_b128 v[148:151], v130 offset:4096\n" \
  "s_waitcnt lgkmcnt(0)\n" \
  "v_max3_u32 v128, v128, v132, v133\n" \
  "v_max3_u32 v128, v128, v134, v135\n" \
  "v_max3_u32 v128, v128, v136, v137\n" \
  "v_max3_u32 v128, v128, v138, v139\n" \
  "v_max3_u32 v128, v128, v140, v141\n" \
  "v_max3_u32 v128, v128, v142, v143\n" \
  "v_max3_u32 v128, v128, v144, v145\n" \
  "v_max3_u32 v128, v128, v146, v147\n" \
  "v_max3_u32 v128, v128, v148, v149\n" \
  "v_max3_u32 v128, v128, v150, v151\n" \
  "ds_read_b128 v[132:135], v130 offset:5120\n" \
  "ds_read_b128 v[136:139], v130 offset:6144\n" \
  "ds_read_b128 v[140:143], v130 offset:7168\n" \
  "ds_read_b128 v[144:147], v130 offset:8192\n" \
  "ds_read_b128 v[148:151], v130 offset:9216\n" \
  "s_waitcnt lgkmcnt(0)\n" \
  "v_max3_u32 v128, v128, v132, v133\n" \
  "v_max3_u32 v128, v128, v134, v135\n" \
  "v_max3_u32 v128, v128, v136, v137\n" \
  "v_max3_u32 v128, v128, v138, v139\n" \
  "v_max3_u32 v128, v128, v140, v141\n" \
  "v_max3_u32 v128, v128, v142, v143\n" \
  "v_max3_u32 v128, v128, v144, v145\n" \
  "v_max3_u32 v128, v128, v146, v147\n" \
  "v_max3_u32 v128, v128, v148, v149\n" \
  "v_max3_u32 v128, v128, v150, v151\n" \
  "v_cmp_eq_u32 vcc, -1, v128\n" \
  "s_cbranch_vccz LMOK11_%=\n" \
  "s_cmp_gt_u32 s58, 4000\n" \
  "s_cbranch_scc1 LMOK11_%=\n" \
  "s_add_u32 s58, s58, 1\n" \
  "s_sub_u32 s64, s64, 1\n" \
  "s_mov_b32 s66, s65\n" \
  "s_mov_b32 s68, s69\n" \
  "s_waitcnt vmcnt(0) lgkmcnt(0)\n" \
  "s_barrier\n" \
  "s_branch LOOP11_%=\n" \
  "LSF11_%=:\n" \
  "s_getpc_b64 s[78:79]\n" \
  "s_branch LSF_%=\n" \
  "s_branch LSOK11_%=\n" \
  "LSR11_%=:\n" \
  "v_mov_b32 v188, 0\n" \
  "s_add_u32 s59, s59, 1\n" \
  "s_barrier\n" \
  "ds_write_b32 v33, v188\n" \
  "s_waitcnt vmcnt(0) lgkmcnt(0)\n" \
  "s_branch LB11_%=\n" \
  "LSTART12_%=:\n" \
  "s_getpc_b64 s[80:81]\n" \
  "LPC12_%=:\n" \
  "s_add_u32 s82, s80, LEA12_%=-LPC12_%=+24\n" \
  "s_addc_u32 s83, s81, 0\n" \
  "s_add_u32 s84, s80, LEB12_%=-LPC12_%=+24\n" \
  "s_addc_u32 s85, s81, 0\n" \
  "LOOP12_%=:\n" \
  "v_readfirstlane_b32 s86, v34\n" \
  "s_add_u32 s71, s64, 1\n" \
  "s_min_u32 s71, s71, 63\n" \
  "s_lshr_b32 s72, s71, 4\n" \
  "s_lshl_b32 s72, s72, 13\n" \
  "s_lshl_b32 s71, s71, 9\n" \
  "s_add_u32 s71, s71, s72\n" \
  "s_mov_b32 s87, s71\n" \
  "s_lshl_b32 s40, s64, 7\n" \
  "s_mov_b32 m0, 0\n" \
  "LWA12_%=:\n" \
  "s_memrealtime s[74:75]\n" \
  "s_memtime s[76:77]\n" \
  "s_waitcnt lgkmcnt(0)\n" \
  "s_sub_i32 s72, s88, s74\n" \
  "s_mul_i32 s72, s72, s90\n" \
  "s_ashr_i32 s72, s72, 8\n" \
  "s_sub_i32 s71, s89, s76\n" \
  "s_min_i32 s71, s71, s72\n" \
  "s_sub_i32 s72, s72, 92\n" \
  "s_max_i32 s71, s71, s72\n" \
  "s_add_u32 s89, s71, s76\n" \
  "s_cmp_gt_i32 s71, 168\n" \
  "s_cbranch_scc1 LWA12_%=\n" \
  "s_cmp_lt_i32 s71, s94\n" \
  "s_cbranch_scc1 LSA12_%=\n" \
  "s_max_i32 s71, s71, 48\n" \
  "s_lshr_b32 s71, s71, 3\n" \
  "s_lshl_b32 s71, s71, 2\n" \
  "s_sub_u32 s78, s82, s71\n" \
  "s_subb_u32 s79, s83, 0\n" \
  "s_setpc_b64 s[78:79]\n" \
  "s_nop 1\n" \
  "s_nop 1\n" \
  "s_nop 1\n" \
  "s_nop 1\n" \
  "s_nop 1\n" \
  "s_nop 1\n" \
  "s_nop 1\n" \
  "s_nop 1\n" \
  "s_nop 1\n" \
  "s_nop 1\n" \
  "s_nop 1\n" \
  "s_nop 1\n" \
  "s_nop 1\n" \
  "s_nop 1\n" \
  "s_nop 1\n" \
  "s_nop 1\n" \
  "LEA12_%=:\n" \
  "buffer_load_dwordx4 v1, s[36:39], s67 offen sc1 lds\n" \
  "buffer_load_dwordx4 v1, s[36:39], s67 offen offset:1024 sc1 lds\n" \
  "buffer_load_dwordx4 v1, s[36:39], s67 offen offset:2048 sc1 lds\n" \
  "buffer_load_dwordx4 v1, s[36:39], s67 offen offset:3072 sc1 lds\n" \
  "s_mov_b32 m0, 4096\n" \
  "s_nop 0\n" \
  "buffer_load_dwordx4 v2, s[36:39], s67 offen sc1 lds\n" \
  "buffer_load_dwordx4 v2, s[36:39], s67 offen offset:1024 sc1 lds\n" \
  "buffer_load_dwordx4 v2, s[36:39], s67 offen offset:2048 sc1 lds\n" \
  "buffer_load_dwordx4 v2, s[36:39], s67 offen offset:3072 sc1 lds\n" \
  "s_mov_b32 m0, 8192\n" \
  "s_nop 0\n" \
  "buffer_load_dwordx4 v3, s[36:39], s67 offen sc1 lds\n" \
  "buffer_load_dwordx4 v3, s[36:39], s67 offen offset:1024 sc1 lds\n" \
  "buffer_load_dword v35, v28, s[36:39], s87 offen sc1\n" \
  "buffer_load_dword v34, v29, s[36:39], s40 offen offset:128 sc1\n" \
  "v_readlane_b32 s70, v27, s64\n" \
  "s_mov_b32 s65, s66\n" \
  "s_mov_b32 s69, s68\n" \
  "s_add_u32 s64, s64, 1\n" \
  "s_barrier\n" \
  "v_mov_b32 v176, 0\n" \
  "v_mov_b32 v177, 0\n" \
  "v_mov_b32 v178, 0\n" \
  "v_mov_b32 v179, 0\n" \
  "v_mov_b32 v180, 0\n" \
  "v_mov_b32 v181, 0\n" \
  "v_mov_b32 v182, 0\n" \
  "v_mov_b32 v183, 0\n" \
  "v_readlane_b32 s66, v25, s64\n" \
  "v_readlane_b32 s68, v26, s64\n" \
  "s_cmp_eq_u32 s34, s64\n" \
  "s_cbranch_scc1 LRT12_%=\n" \
  "s_mov_b32 s34, s64\n" \
  "s_mov_b32 s71, s86\n" \
  "s_max_u32 s19, s19, s71\n" \
  "s_and_b32 s72, s71, 0xff\n" \
  "s_lshr_b32 s71, s71, 8\n" \
  "s_cselect_b32 s71, s71, s61\n" \
  "s_sub_i32 s72, s64, s72\n" \
  "s_sub_i32 s71, s71, s61\n" \
  "s_cmp_ge_i32 s72, 0\n" \
  "s_cselect_b32 s71, s71, 0\n" \
  "s_add_u32 s61, s61, s71\n" \
  "s_mul_i32 s72, s72, s71\n" \
  "s_lshl_b32 s72, s72, 1\n" \
  "s_add_u32 s88, s88, s72\n" \
  "s_mul_i32 s72, s72, s90\n" \
  "s_ashr_i32 s72, s72, 8\n" \
  "s_add_u32 s89, s89, s72\n" \
  "s_lshr_b32 s72, s18, 1\n" \
  "s_sub_u32 s80, s72, s18\n" \
  "s_mov_b32 s18, s72\n" \
  "s_add_u32 s71, s71, s80\n" \
  "s_add_u32 s50, s50, s71\n" \
  "s_mul_i32 s91, s50, s90\n" \
  "s_lshr_b32 s91, s91, 8\n" \
  "s_add_u32 s51, s51, s71\n" \
  "s_mul_i32 s92, s51, s90\n" \
  "s_lshr_b32 s92, s92, 8\n" \
  "LRT12_%=:\n" \
  "s_sub_u32 s72, s74, s95\n" \
  "s_cmp_lt_u32 s64, 3\n" \
  "s_cbranch_scc1 LRB12_%=\n" \
  "s_cmp_gt_u32 s72, 900\n" \
  "s_cbranch_scc0 LRC12_%=\n" \
  "s_sub_u32 s71, s76, s96\n" \
  "s_sub_u32 s72, s74, s95\n" \
  "v_cvt_f32_u32 v36, s71\n" \
  "v_cvt_f32_u32 v37, s72\n" \
  "v_rcp_f32 v37, v37\n" \
  "v_mul_f32 v36, 0x43820000, v36\n" \
  "v_mul_f32 v36, v36, v37\n" \
  "v_cvt_u32_f32 v36, v36\n" \
  "s_nop 0\n" \
  "v_readfirstlane_b32 s71, v36\n" \
  "s_min_u32 s90, s71, 6400\n" \
  "LRB12_%=:\n" \
  "s_mov_b32 s95, s74\n" \
  "s_mov_b32 s96, s76\n" \
  "LRC12_%=:\n" \
  "s_waitcnt lgkmcnt(0)\n" \
  "LM12_%=:\n" \
  "s_waitcnt vmcnt(11)\n" \
  "s_barrier\n" \
  "ds_read_b128 v[112:115], v5\n" \
  "ds_read_b128 v[116:119], v5 offset:512\n" \
  "s_waitcnt vmcnt(10)\n" \
  "s_barrier\n" \
  "ds_read_b128 v[120:123], v5 offset:1024\n" \
  "ds_read_b128 v[124:127], v5 offset:1536\n" \
  "s_waitcnt lgkmcnt(3)\n" \
  "v_smfmac_f32_16x16x64_bf16 v[176:179], v[112:115], a[0:7], v0\n" \
  "v_smfmac_f32_16x16x64_bf16 v[180:183], v[112:115], a[160:167], v0\n" \
  "s_waitcnt vmcnt(8)\n" \
  "s_waitcnt lgkmcnt(2)\n" \
  "v_smfmac_f32_16x16x64_bf16 v[176:179], v[116:119], a[8:15], v0\n" \
  "s_barrier\n" \
  "ds_read_b128 v[128:131], v5 offset:2048\n" \
  "v_smfmac_f32_16x16x64_bf16 v[180:183], v[116:119], a[168:175], v0\n" \
  "s_waitcnt lgkmcnt(2)\n" \
  "ds_read_b128 v[132:135], v5 offset:2560\n" \
  "v_smfmac_f32_16x16x64_bf16 v[176:179], v[120:123], a[16:23], v0\n" \
  "ds_read_b128 v[136:139], v5 offset:3072\n" \
  "v_smfmac_f32_16x16x64_bf16 v[180:183], v[120:123], a[176:183], v0\n" \
  "s_waitcnt vmcnt(6)\n" \
  "s_waitcnt lgkmcnt(3)\n" \
  "ds_read_b128 v[140:143], v5 offset:3584\n" \
  "v_smfmac_f32_16x16x64_bf16 v[176:179], v[124:127], a[24:31], v0\n" \
  "s_barrier\n" \
  "ds_read_b128 v[144:147], v5 offset:4096\n" \
  "v_smfmac_f32_16x16x64_bf16 v[180:183], v[124:127], a[184:191], v0\n" \
  "s_waitcnt lgkmcnt(4)\n" \
  "ds_read_b128 v[148:151], v5 offset:4608\n" \
  "v_smfmac_f32_16x16x64_bf16 v[176:179], v[128:131], a[32:39], v0\n" \
  "ds_read_b128 v[152:155], v5 offset:5120\n" \
  "v_smfmac_f32_16x16x64_bf16 v[180:183], v[128:131], a[192:199], v0\n" \
  "s_waitcnt vmcnt(4)\n" \
  "s_waitcnt lgkmcnt(5)\n" \
  "ds_read_b128 v[156:159], v5 offset:5632\n" \
  "v_smfmac_f32_16x16x64_bf16 v[176:179], v[132:135], a[40:47], v0\n" \
  "s_barrier\n" \
  "ds_read_b128 v[160:163], v5 offset:6144\n" \
  "v_smfmac_f32_16x16x64_bf16 v[180:183], v[132:135], a[200:207], v0\n" \
  "s_waitcnt lgkmcnt(6)\n" \
  "ds_read_b128 v[164:167], v5 offset:6656\n" \
  "v_smfmac_f32_16x16x64_bf16 v[176:179], v[136:139], a[48:55], v0\n" \
  "ds_read_b128 v[168:171], v5 offset:7168\n" \
  "v_smfmac_f32_16x16x64_bf16 v[180:183], v[136:139], a[208:215], v0\n" \
  "s_waitcnt vmcnt(2)\n" \
  "s_waitcnt lgkmcnt(7)\n" \
  "ds_read_b128 v[172:175], v5 offset:7680\n" \
  "v_smfmac_f32_16x16x64_bf16 v[176:179], v[140:143], a[56:63], v0\n" \
  "s_barrier\n" \
  "ds_read2_b32 v[112:113], v6 offset0:0 offset1:32\n" \
  "v_smfmac_f32_16x16x64_bf16 v[180:183], v[140:143], a[216:223], v0\n" \
  "s_waitcnt lgkmcnt(8)\n" \
  "ds_read2_b32 v[114:115], v6 offset0:64 offset1:96\n" \
  "v_smfmac_f32_16x16x64_bf16 v[176:179], v[144:147], a[64:71], v0\n" \
  "s_waitcnt lgkmcnt(8)\n" \
  "ds_read2_b32 v[116:117], v6 offset0:128 offset1:160\n" \
  "v_smfmac_f32_16x16x64_bf16 v[176:179], v[148:151], a[72:79], v0\n" \
  "s_waitcnt lgkmcnt(8)\n" \
  "ds_read2_b32 v[118:119], v6 offset0:192 offset1:224\n" \
  "v_smfmac_f32_16x16x64_bf16 v[176:179], v[152:155], a[80:87], v0\n" \
  "s_waitcnt lgkmcnt(8)\n" \
  "ds_read2_b32 v[120:121], v7 offset0:0 offset1:32\n" \
  "v_smfmac_f32_16x16x64_bf16 v[176:179], v[156:159], a[88:95], v0\n" \
  "s_waitcnt lgkmcnt(8)\n" \
  "ds_read2_b32 v[122:123], v7 offset0:64 offset1:96\n" \
  "v_smfmac_f32_16x16x64_bf16 v[176:179], v[160:163], a[96:103], v0\n" \
  "s_waitcnt lgkmcnt(8)\n" \
  "ds_read2_b32 v[124:125], v7 offset0:128 offset1:160\n" \
  "v_smfmac_f32_16x16x64_bf16 v[176:179], v[164:167], a[104:111], v0\n" \
  "s_waitcnt lgkmcnt(8)\n" \
  "ds_read2_b32 v[126:127], v7 offset0:192 offset1:224\n" \
  "v_smfmac_f32_16x16x64_bf16 v[176:179], v[168:171], a[112:119], v0\n" \
  "s_waitcnt lgkmcnt(8)\n" \
  "v_smfmac_f32_16x16x64_bf16 v[176:179], v[172:175], a[120:127], v0\n" \
  "s_waitcnt lgkmcnt(6)\n" \
  "v_smfmac_f32_16x16x64_bf16 v[176:179], v[112:115], a[128:135], v0\n" \
  "s_waitcnt lgkmcnt(4)\n" \
  "v_smfmac_f32_16x16x64_bf16 v[176:179], v[116:119], a[136:143], v0\n" \
  "s_waitcnt lgkmcnt(2)\n" \
  "v_smfmac_f32_16x16x64_bf16 v[176:179], v[120:123], a[144:151], v0\n" \
  "s_waitcnt lgkmcnt(0)\n" \
  "v_smfmac_f32_16x16x64_bf16 v[176:179], v[124:127], a[152:159], v0\n" \
  "v_smfmac_f32_16x16x64_bf16 v[180:183], v[144:147], a[224:231], v0\n" \
  "v_smfmac_f32_16x16x64_bf16 v[180:183], v[148:151], a[232:239], v0\n" \
  "v_cvt_pk_bf16_f32 v184, v176, v177\n" \
  "v_smfmac_f32_16x16x64_bf16 v[180:183], v[152:155], a[240:247], v0\n" \
  "v_cvt_pk_bf16_f32 v185, v178, v179\n" \
  "v_smfmac_f32_16x16x64_bf16 v[180:183], v[156:159], a[248:255], v0\n" \
  "ds_write_b64 v8, v[184:185]\n" \
  "v_add_f32 v186, v176, v177\n" \
  "v_smfmac_f32_16x16x64_bf16 v[180:183], v[160:163], v[192:199], v0\n" \
  "v_add_f32 v187, v178, v179\n" \
  "v_smfmac_f32_16x16x64_bf16 v[180:183], v[164:167], v[200:207], v0\n" \
  "v_cmp_u_f32 vcc, v186, v187\n" \
  "v_smfmac_f32_16x16x64_bf16 v[180:183], v[168:171], v[208:215], v0\n" \
  "v_smfmac_f32_16x16x64_bf16 v[180:183], v[172:175], v[216:223], v0\n" \
  "s_cbranch_vccnz LMF12_%=\n" \
  "LMOK12_%=:\n" \
  "v_smfmac_f32_16x16x64_bf16 v[180:183], v[112:115], v[224:231], v0\n" \
  "s_waitcnt lgkmcnt(0)\n" \
  "s_barrier\n" \
  "v_smfmac_f32_16x16x64_bf16 v[180:183], v[116:119], v[232:239], v0\n" \
  "v_smfmac_f32_16x16x64_bf16 v[180:183], v[120:123], v[240:247], v0\n" \
  "v_smfmac_f32_16x16x64_bf16 v[180:183], v[124:127], v[248:255], v0\n" \
  "s_nop 3\n" \
  "v_mov_b32 v31, v31\n" \
  "s_nop 2\n" \
  "v_cvt_pk_bf16_f32 v186, v180, v181\n" \
  "v_cvt_pk_bf16_f32 v187, v182, v183\n" \
  "ds_write_b64 v9, v[186:187]\n" \
  "s_waitcnt lgkmcnt(0)\n" \
  "s_barrier\n" \
  "s_nop 0\n" \
  "ds_read_b64_tr_b16 v[128:129], v12\n" \
  "ds_read_b64_tr_b16 v[130:131], v12 offset:32\n" \
  "s_waitcnt lgkmcnt(0)\n" \
  "buffer_store_dwordx4 v[128:131], v14, s[36:39], s69 offen sc1\n" \
  "s_mov_b32 s67, s66\n" \
  "s_cmp_eq_u32 s64, 64\n" \
  "s_cbranch_scc1 LDONE_%=\n" \
  "s_add_u32 s88, s88, s50\n" \
  "s_add_u32 s89, s89, s91\n" \
  "s_waitcnt vmcnt(1)\n" \
  "v_cmp_gt_u32 vcc, 16, v35\n" \
  "s_cbranch_vccnz LFF12_%=\n" \
  "LFOK12_%=:\n" \
  "LB12_%=:\n" \
  "LWB12_%=:\n" \
  "s_memrealtime s[74:75]\n" \
  "s_memtime s[76:77]\n" \
  "s_waitcnt lgkmcnt(0)\n" \
  "s_sub_i32 s72, s88, s74\n" \
  "s_mul_i32 s72, s72, s90\n" \
  "s_ashr_i32 s72, s72, 8\n" \
  "s_sub_i32 s71, s89, s76\n" \
  "s_min_i32 s71, s71, s72\n" \
  "s_sub_i32 s72, s72, 92\n" \
  "s_max_i32 s71, s71, s72\n" \
  "s_add_u32 s89, s71, s76\n" \
  "s_cmp_gt_i32 s71, 168\n" \
  "s_cbranch_scc1 LWB12_%=\n" \
  "s_cmp_lt_i32 s71, s94\n" \
  "s_cbranch_scc1 LSB12_%=\n" \
  "s_max_i32 s71, s71, 48\n" \
  "s_lshr_b32 s71, s71, 3\n" \
  "s_lshl_b32 s71, s71, 2\n" \
  "s_sub_u32 s78, s84, s71\n" \
  "s_subb_u32 s79, s85, 0\n" \
  "s_setpc_b64 s[78:79]\n" \
  "s_nop 1\n" \
  "s_nop 1\n" \
  "s_nop 1\n" \
  "s_nop 1\n" \
  "s_nop 1\n" \
  "s_nop 1\n" \
  "s_nop 1\n" \
  "s_nop 1\n" \
  "s_nop 1\n" \
  "s_nop 1\n" \
  "s_nop 1\n" \
  "s_nop 1\n" \
  "s_nop 1\n" \
  "s_nop 1\n" \
  "s_nop 1\n" \
  "s_nop 1\n" \
  "LEB12_%=:\n" \
  "s_barrier\n" \
  "buffer_load_dwordx4 v[112:115], v15, s[36:39], s65 offen sc1\n" \
  "s_barrier\n" \
  "buffer_load_dwordx4 v[116:119], v15, s[36:39], s65 offen offset:128 sc1\n" \
  "s_add_u32 s88, s88, s51\n" \
  "s_add_u32 s89, s89, s92\n" \
  "s_barrier\n" \
  "buffer_load_dwordx4 v[120:123], v16, s[36:39], s65 offen sc1\n" \
  "s_barrier\n" \
  "buffer_load_dwordx4 v[124:127], v16, s[36:39], s65 offen offset:512 sc1\n" \
  "s_barrier\n" \
  "buffer_load_dwordx4 v[128:131], v16, s[36:39], s65 offen offset:1024 sc1\n" \
  "s_barrier\n" \
  "buffer_load_dwordx4 v[132:135], v16, s[36:39], s65 offen offset:1536 sc1\n" \
  "ds_read_b128 v[40:43], v19\n" \
  "ds_read_b128 v[44:47], v19 offset:512\n" \
  "ds_read_b128 v[48:51], v19 offset:1024\n" \
  "ds_read_b128 v[52:55], v19 offset:1536\n" \
  "s_mov_b64 exec, s[46:47]\n" \
  "ds_read_b128 v[56:59], v20\n" \
  "ds_read_b128 v[60:63], v20 offset:128\n" \
  "ds_read_b128 v[64:67], v20 offset:256\n" \
  "ds_read_b128 v[68:71], v20 offset:384\n" \
  "s_mov_b64 exec, -1\n" \
  "s_nop 15\n" \
  "s_nop 1\n" \
  "buffer_load_dwordx4 v[136:139], v17, s[36:39], s65 offen sc1\n" \
  "buffer_load_dwordx4 v[140:143], v18, s[36:39], s65 offen sc1\n" \
  "s_waitcnt lgkmcnt(0)\n" \
  "s_getreg_b32 s71, hwreg(HW_REG_IB_STS, 0, 4)\n" \
  "s_cmp_le_u32 s71, 6\n" \
  "v_mfma_f32_16x16x32_bf16 v[144:147], v[72:75], v[72:75], 0\n" \
  "s_cbranch_scc1 LHXS12_%=\n" \
  "s_getreg_b32 s71, hwreg(HW_REG_IB_STS, 0, 4)\n" \
  "s_cmp_le_u32 s71, 6\n" \
  "v_mfma_f32_16x16x32_bf16 v[144:147], v[72:75], v[72:75], 0\n" \
  "s_cbranch_scc1 LHXS12_%=\n" \
  "s_getreg_b32 s71, hwreg(HW_REG_IB_STS, 0, 4)\n" \
  "s_cmp_le_u32 s71, 6\n" \
  "v_mfma_f32_16x16x32_bf16 v[144:147], v[72:75], v[72:75], 0\n" \
  "s_cbranch_scc1 LHXS12_%=\n" \
  "s_getreg_b32 s71, hwreg(HW_REG_IB_STS, 0, 4)\n" \
  "s_cmp_le_u32 s71, 6\n" \
  "v_mfma_f32_16x16x32_bf16 v[144:147], v[72:75], v[72:75], 0\n" \
  "s_cbranch_scc1 LHXS12_%=\n" \
  "s_getreg_b32 s71, hwreg(HW_REG_IB_STS, 0, 4)\n" \
  "s_cmp_le_u32 s71, 6\n" \
  "v_mfma_f32_16x16x32_bf16 v[144:147], v[72:75], v[72:75], 0\n" \
  "s_cbranch_scc1 LHXS12_%=\n" \
  "s_getreg_b32 s71, hwreg(HW_REG_IB_STS, 0, 4)\n" \
  "s_cmp_le_u32 s71, 6\n" \
  "v_mfma_f32_16x16x32_bf16 v[144:147], v[72:75], v[72:75], 0\n" \
  "s_cbranch_scc1 LHXS12_%=\n" \
  "s_getreg_b32 s71, hwreg(HW_REG_IB_STS, 0, 4)\n" \
  "s_cmp_le_u32 s71, 6\n" \
  "v_mfma_f32_16x16x32_bf16 v[144:147], v[72:75], v[72:75], 0\n" \
  "s_cbranch_scc1 LHXS12_%=\n" \
  "s_getreg_b32 s71, hwreg(HW_REG_IB_STS, 0, 4)\n" \
  "s_cmp_le_u32 s71, 6\n" \
  "v_mfma_f32_16x16x32_bf16 v[144:147], v[72:75], v[72:75], 0\n" \
  "s_cbranch_scc1 LHXS12_%=\n" \
  "s_getreg_b32 s71, hwreg(HW_REG_IB_STS, 0, 4)\n" \
  "s_cmp_le_u32 s71, 6\n" \
  "v_mfma_f32_16x16x32_bf16 v[144:147], v[72:75], v[72:75], 0\n" \
  "s_cbranch_scc1 LHXS12_%=\n" \
  "s_getreg_b32 s71, hwreg(HW_REG_IB_STS, 0, 4)\n" \
  "s_cmp_le_u32 s71, 6\n" \
  "v_mfma_f32_16x16x32_bf16 v[144:147], v[72:75], v[72:75], 0\n" \
  "s_cbranch_scc1 LHXS12_%=\n" \
  "s_getreg_b32 s71, hwreg(HW_REG_IB_STS, 0, 4)\n" \
  "s_cmp_le_u32 s71, 6\n" \
  "v_mfma_f32_16x16x32_bf16 v[144:147], v[72:75], v[72:75], 0\n" \
  "s_cbranch_scc1 LHXS12_%=\n" \
  "s_getreg_b32 s71, hwreg(HW_REG_IB_STS, 0, 4)\n" \
  "s_cmp_le_u32 s71, 6\n" \
  "v_mfma_f32_16x16x32_bf16 v[144:147], v[72:75], v[72:75], 0\n" \
  "s_cbranch_scc1 LHXS12_%=\n" \
  "s_getreg_b32 s71, hwreg(HW_REG_IB_STS, 0, 4)\n" \
  "s_cmp_le_u32 s71, 6\n" \
  "v_mfma_f32_16x16x32_bf16 v[144:147], v[72:75], v[72:75], 0\n" \
  "s_cbranch_scc1 LHXS12_%=\n" \
  "s_getreg_b32 s71, hwreg(HW_REG_IB_STS, 0, 4)\n" \
  "s_cmp_le_u32 s71, 6\n" \
  "v_mfma_f32_16x16x32_bf16 v[144:147], v[72:75], v[72:75], 0\n" \
  "s_cbranch_scc1 LHXS12_%=\n" \
  "s_getreg_b32 s71, hwreg(HW_REG_IB_STS, 0, 4)\n" \
  "s_cmp_le_u32 s71, 6\n" \
  "v_mfma_f32_16x16x32_bf16 v[144:147], v[72:75], v[72:75], 0\n" \
  "s_cbranch_scc1 LHXS12_%=\n" \
  "s_getreg_b32 s71, hwreg(HW_REG_IB_STS, 0, 4)\n" \
  "s_cmp_le_u32 s71, 6\n" \
  "v_mfma_f32_16x16x32_bf16 v[144:147], v[72:75], v[72:75], 0\n" \
  "s_cbranch_scc1 LHXS12_%=\n" \
  "s_getreg_b32 s71, hwreg(HW_REG_IB_STS, 0, 4)\n" \
  "s_cmp_le_u32 s71, 6\n" \
  "v_mfma_f32_16x16x32_bf16 v[144:147], v[72:75], v[72:75], 0\n" \
  "s_cbranch_scc1 LHXS12_%=\n" \
  "s_getreg_b32 s71, hwreg(HW_REG_IB_STS, 0, 4)\n" \
  "s_cmp_le_u32 s71, 6\n" \
  "v_mfma_f32_16x16x32_bf16 v[144:147], v[72:75], v[72:75], 0\n" \
  "s_cbranch_scc1 LHXS12_%=\n" \
  "s_getreg_b32 s71, hwreg(HW_REG_IB_STS, 0, 4)\n" \
  "s_cmp_le_u32 s71, 6\n" \
  "v_mfma_f32_16x16x32_bf16 v[144:147], v[72:75], v[72:75], 0\n" \
  "s_cbranch_scc1 LHXS12_%=\n" \
  "s_getreg_b32 s71, hwreg(HW_REG_IB_STS, 0, 4)\n" \
  "s_cmp_le_u32 s71, 6\n" \
  "v_mfma_f32_16x16x32_bf16 v[144:147], v[72:75], v[72:75], 0\n" \
  "s_cbranch_scc1 LHXS12_%=\n" \
  "s_waitcnt vmcnt(6)\n" \
  "LHXS12_%=:\n" \
  "LS212_%=:\n" \
  "v_dot2_f32_bf16 v176, v112, s22, 0\n" \
  "v_dot2_f32_bf16 v177, v112, s23, 0\n" \
  "v_dot2_f32_bf16 v178, v113, s22, 0\n" \
  "v_dot2_f32_bf16 v179, v113, s23, 0\n" \
  "v_dot2_f32_bf16 v180, v114, s22, 0\n" \
  "v_dot2_f32_bf16 v181, v114, s23, 0\n" \
  "v_dot2_f32_bf16 v182, v115, s22, 0\n" \
  "v_dot2_f32_bf16 v183, v115, s23, 0\n" \
  "v_dot2_f32_bf16 v176, v116, s22, v176\n" \
  "v_dot2_f32_bf16 v177, v116, s23, v177\n" \
  "v_dot2_f32_bf16 v178, v117, s22, v178\n" \
  "v_dot2_f32_bf16 v179, v117, s23, v179\n" \
  "v_dot2_f32_bf16 v180, v118, s22, v180\n" \
  "v_dot2_f32_bf16 v181, v118, s23, v181\n" \
  "v_dot2_f32_bf16 v182, v119, s22, v182\n" \
  "v_dot2_f32_bf16 v183, v119, s23, v183\n" \
  "v_exp_f32 v176, v176\n" \
  "v_exp_f32 v177, v177\n" \
  "v_exp_f32 v178, v178\n" \
  "v_exp_f32 v179, v179\n" \
  "s_waitcnt vmcnt(5)\n" \
  "v_mfma_f32_16x16x32_bf16 v[160:163], v[56:59], v[120:123], 0\n" \
  "v_exp_f32 v180, v180\n" \
  "v_mfma_f32_16x16x32_bf16 v[144:147], v[40:43], v[120:123], 0\n" \
  "v_exp_f32 v181, v181\n" \
  "v_exp_f32 v182, v182\n" \
  "v_exp_f32 v183, v183\n" \
  "v_cvt_pk_bf16_f32 v184, v176, v177\n" \
  "v_cvt_pk_bf16_f32 v185, v178, v179\n" \
  "v_cvt_pk_bf16_f32 v186, v180, v181\n" \
  "v_cvt_pk_bf16_f32 v187, v182, v183\n" \
  "s_nop 1\n" \
  "v_mfma_f32_16x16x32_bf16 v[160:163], v[72:75], v[184:187], v[160:163]\n" \
  "v_mfma_f32_16x16x32_bf16 v[164:167], v[76:79], v[184:187], 0\n" \
  "s_waitcnt vmcnt(4)\n" \
  "v_mfma_f32_16x16x32_bf16 v[164:167], v[60:63], v[124:127], v[164:167]\n" \
  "v_rcp_f32 v188, v162\n" \
  "v_mfma_f32_16x16x32_bf16 v[148:151], v[44:47], v[124:127], 0\n" \
  "v_mul_f32 v144, v188, v144\n" \
  "v_mul_f32 v145, v188, v145\n" \
  "v_mfma_f32_16x16x32_bf16 v[168:171], v[80:83], v[184:187], 0\n" \
  "s_waitcnt vmcnt(3)\n" \
  "v_rcp_f32 v189, v166\n" \
  "v_mfma_f32_16x16x32_bf16 v[168:171], v[64:67], v[128:131], v[168:171]\n" \
  "v_mul_f32 v146, v188, v146\n" \
  "v_mul_f32 v147, v188, v147\n" \
  "v_mfma_f32_16x16x32_bf16 v[152:155], v[48:51], v[128:131], 0\n" \
  "v_mul_f32 v160, v188, v160\n" \
  "v_mul_f32 v161, v188, v161\n" \
  "v_mfma_f32_16x16x32_bf16 v[172:175], v[84:87], v[184:187], 0\n" \
  "s_waitcnt vmcnt(2)\n" \
  "v_rcp_f32 v190, v170\n" \
  "v_mfma_f32_16x16x32_bf16 v[172:175], v[68:71], v[132:135], v[172:175]\n" \
  "v_cndmask_b32 v167, v188, v189, s[28:29]\n" \
  "v_cndmask_b32 v167, v167, v190, s[26:27]\n" \
  "v_mfma_f32_16x16x32_bf16 v[156:159], v[52:55], v[132:135], 0\n" \
  "v_fmac_f32 v144, v189, v148\n" \
  "v_fmac_f32 v145, v189, v149\n" \
  "v_fmac_f32 v146, v189, v150\n" \
  "v_fmac_f32 v147, v189, v151\n" \
  "v_rcp_f32 v191, v174\n" \
  "v_fmac_f32 v160, v189, v164\n" \
  "v_cndmask_b32 v167, v167, v191, s[24:25]\n" \
  "v_pk_mul_f32 v[176:177], v[176:177], v[166:167] op_sel:[0,1] op_sel_hi:[1,1]\n" \
  "v_pk_mul_f32 v[178:179], v[178:179], v[166:167] op_sel:[0,1] op_sel_hi:[1,1]\n" \
  "v_pk_mul_f32 v[180:181], v[180:181], v[166:167] op_sel:[0,1] op_sel_hi:[1,1]\n" \
  "v_pk_mul_f32 v[182:183], v[182:183], v[166:167] op_sel:[0,1] op_sel_hi:[1,1]\n" \
  "v_cmp_nlt_f32 vcc, 0x0d800000, v167\n" \
  "v_cvt_pk_bf16_f32 v184, v176, v177\n" \
  "v_cvt_pk_bf16_f32 v185, v178, v179\n" \
  "v_cvt_pk_bf16_f32 v186, v180, v181\n" \
  "v_cvt_pk_bf16_f32 v187, v182, v183\n" \
  "v_fmac_f32 v161, v189, v165\n" \
  "v_pk_fma_f32 v[160:161], v[168:169], v[190:191], v[160:161] op_sel:[0,0,0] op_sel_hi:[1,0,1]\n" \
  "v_pk_fma_f32 v[160:161], v[172:173], v[190:191], v[160:161] op_sel:[0,1,0] op_sel_hi:[1,1,1]\n" \
  "v_pk_fma_f32 v[144:145], v[152:153], v[190:191], v[144:145] op_sel:[0,0,0] op_sel_hi:[1,0,1]\n" \
  "v_pk_fma_f32 v[146:147], v[154:155], v[190:191], v[146:147] op_sel:[0,0,0] op_sel_hi:[1,0,1]\n" \
  "v_pk_fma_f32 v[144:145], v[156:157], v[190:191], v[144:145] op_sel:[0,1,0] op_sel_hi:[1,1,1]\n" \
  "v_pk_fma_f32 v[146:147], v[158:159], v[190:191], v[146:147] op_sel:[0,1,0] op_sel_hi:[1,1,1]\n" \
  "s_waitcnt vmcnt(0)\n" \
  "v_mfma_f32_16x16x32_bf16 v[160:163], v[140:143], v[184:187], v[160:163]\n" \
  "s_mov_b64 s[80:81], vcc\n" \
  "v_mfma_f32_16x16x32_bf16 v[144:147], v[136:139], v[184:187], v[144:147]\n" \
  "s_mov_b64 exec, s[30:31]\n" \
  "s_nop 2\n" \
  "ds_write_b64 v22, v[160:161]\n" \
  "v_cmp_u_f32 s[78:79], v160, v161\n" \
  "s_nop 1\n" \
  "ds_write2_b64 v21, v[144:145], v[146:147] offset1:16\n" \
  "v_add_f32 v188, v144, v145\n" \
  "v_add_f32 v189, v146, v147\n" \
  "v_cmp_u_f32 vcc, v188, v189\n" \
  "s_or_b64 s[80:81], s[80:81], s[78:79]\n" \
  "s_or_b64 vcc, vcc, s[80:81]\n" \
  "s_cbranch_vccnz LSF12_%=\n" \
  "LSOK12_%=:\n" \
  "s_mov_b64 exec, -1\n" \
  "s_waitcnt lgkmcnt(0)\n" \
  "s_barrier\n" \
  "ds_read_b32 v188, v33\n" \
  "s_waitcnt lgkmcnt(0)\n" \
  "v_cmp_ne_u32 vcc, 0, v188\n" \
  "s_cbranch_vccnz LSR12_%=\n" \
  "LNL12_%=:\n" \
  "s_waitcnt lgkmcnt(0)\n" \
  "s_cmp_eq_u32 s73, 1\n" \
  "s_cbranch_scc1 LOOP12_%=\n" \
  "s_branch LDONE_%=\n" \
  "LSA12_%=:\n" \
  "s_add_u32 s88, s88, s52\n" \
  "s_add_u32 s89, s89, s93\n" \
  "s_add_u32 s97, s97, 1\n" \
  "s_branch LWA12_%=\n" \
  "LSB12_%=:\n" \
  "s_add_u32 s88, s88, s52\n" \
  "s_add_u32 s89, s89, s93\n" \
  "s_add_u32 s97, s97, 1\n" \
  "s_branch LWB12_%=\n" \
  "LMF12_%=:\n" \
  "v_mbcnt_lo_u32_b32 v130, -1, 0\n" \
  "v_mbcnt_hi_u32_b32 v130, -1, v130\n" \
  "v_lshlrev_b32 v130, 4, v130\n" \
  "v_mov_b32 v128, 0\n" \
  "ds_read_b128 v[132:135], v130 offset:0\n" \
  "ds_read_b128 v[136:139], v130 offset:1024\n" \
  "ds_read_b128 v[140:143], v130 offset:2048\n" \
  "ds_read_b128 v[144:147], v130 offset:3072\n" \
  "ds_read_b128 v[148:151], v130 offset:4096\n" \
  "s_waitcnt lgkmcnt(0)\n" \
  "v_max3_u32 v128, v128, v132, v133\n" \
  "v_max3_u32 v128, v128, v134, v135\n" \
  "v_max3_u32 v128, v128, v136, v137\n" \
  "v_max3_u32 v128, v128, v138, v139\n" \
  "v_max3_u32 v128, v128, v140, v141\n" \
  "v_max3_u32 v128, v128, v142, v143\n" \
  "v_max3_u32 v128, v128, v144, v145\n" \
  "v_max3_u32 v128, v128, v146, v147\n" \
  "v_max3_u32 v128, v128, v148, v149\n" \
  "v_max3_u32 v128, v128, v150, v151\n" \
  "ds_read_b128 v[132:135], v130 offset:5120\n" \
  "ds_read_b128 v[136:139], v130 offset:6144\n" \
  "ds_read_b128 v[140:143], v130 offset:7168\n" \
  "ds_read_b128 v[144:147], v130 offset:8192\n" \
  "ds_read_b128 v[148:151], v130 offset:9216\n" \
  "s_waitcnt lgkmcnt(0)\n" \
  "v_max3_u32 v128, v128, v132, v133\n" \
  "v_max3_u32 v128, v128, v134, v135\n" \
  "v_max3_u32 v128, v128, v136, v137\n" \
  "v_max3_u32 v128, v128, v138, v139\n" \
  "v_max3_u32 v128, v128, v140, v141\n" \
  "v_max3_u32 v128, v128, v142, v143\n" \
  "v_max3_u32 v128, v128, v144, v145\n" \
  "v_max3_u32 v128, v128, v146, v147\n" \
  "v_max3_u32 v128, v128, v148, v149\n" \
  "v_max3_u32 v128, v128, v150, v151\n" \
  "v_cmp_eq_u32 vcc, -1, v128\n" \
  "s_cbranch_vccz LMOK12_%=\n" \
  "s_cmp_gt_u32 s58, 4000\n" \
  "s_cbranch_scc1 LMOK12_%=\n" \
  "s_add_u32 s58, s58, 1\n" \
  "s_sub_u32 s64, s64, 1\n" \
  "s_mov_b32 s66, s65\n" \
  "s_mov_b32 s68, s69\n" \
  "s_add_u32 s88, s88, s52\n" \
  "s_add_u32 s89, s89, s93\n" \
  "s_add_u32 s97, s97, 1\n" \
  "s_waitcnt vmcnt(0) lgkmcnt(0)\n" \
  "s_barrier\n" \
  "s_branch LOOP12_%=\n" \
  "LFF12_%=:\n" \
  "s_add_u32 s71, s64, 0\n" \
  "s_min_u32 s71, s71, 63\n" \
  "s_lshr_b32 s72, s71, 4\n" \
  "s_lshl_b32 s72, s72, 13\n" \
  "s_lshl_b32 s71, s71, 9\n" \
  "s_add_u32 s71, s71, s72\n" \
  "LOF12_%=:\n" \
  "buffer_load_dword v35, v28, s[36:39], s71 offen sc1\n" \
  "s_waitcnt vmcnt(0)\n" \
  "v_cmp_gt_u32 vcc, 16, v35\n" \
  "s_cbranch_vccz LOXF12_%=\n" \
  "s_add_u32 s60, s60, 1\n" \
  "s_cmp_gt_u32 s60, 20000\n" \
  "s_cbranch_scc1 LOXF12_%=\n" \
  "s_sleep 16\n" \
  "s_branch LOF12_%=\n" \
  "LOXF12_%=:\n" \
  "s_branch LFOK12_%=\n" \
  "LSF12_%=:\n" \
  "s_getpc_b64 s[78:79]\n" \
  "s_branch LSF_%=\n" \
  "s_branch LSOK12_%=\n" \
  "LSR12_%=:\n" \
  "v_mov_b32 v188, 0\n" \
  "s_add_u32 s59, s59, 1\n" \
  "s_barrier\n" \
  "ds_write_b32 v33, v188\n" \
  "s_sub_u32 s88, s88, s51\n" \
  "s_sub_u32 s89, s89, s92\n" \
  "s_add_u32 s88, s88, s52\n" \
  "s_add_u32 s89, s89, s93\n" \
  "s_add_u32 s97, s97, 1\n" \
  "s_waitcnt vmcnt(0) lgkmcnt(0)\n" \
  "s_branch LB12_%=\n" \
  "LSTART13_%=:\n" \
  "LOOP13_%=:\n" \
  "v_readlane_b32 s70, v27, s64\n" \
  "s_mov_b32 s65, s66\n" \
  "s_mov_b32 s69, s68\n" \
  "v_mov_b32 v34, s64\n" \
  "s_add_u32 s64, s64, 1\n" \
  "s_barrier\n" \
  "s_mov_b32 m0, 36864\n" \
  "s_xor_b32 s72, s65, 0\n" \
  "buffer_load_dwordx4 v3, s[36:39], s72 offen sc1 lds\n" \
  "buffer_load_dwordx4 v3, s[36:39], s72 offen offset:1024 sc1 lds\n" \
  "buffer_load_dwordx4 v3, s[36:39], s72 offen offset:2048 sc1 lds\n" \
  "buffer_load_dwordx4 v3, s[36:39], s72 offen offset:3072 sc1 lds\n" \
  "v_mov_b32 v176, 0\n" \
  "v_mov_b32 v177, 0\n" \
  "v_mov_b32 v178, 0\n" \
  "v_mov_b32 v179, 0\n" \
  "v_mov_b32 v180, 0\n" \
  "v_mov_b32 v181, 0\n" \
  "v_mov_b32 v182, 0\n" \
  "v_mov_b32 v183, 0\n" \
  "v_readlane_b32 s66, v25, s64\n" \
  "v_readlane_b32 s68, v26, s64\n" \
  "s_waitcnt lgkmcnt(0)\n" \
  "LM13_%=:\n" \
  "s_barrier\n" \
  "ds_read_b128 v[112:115], v5\n" \
  "ds_read_b128 v[116:119], v5 offset:512\n" \
  "s_barrier\n" \
  "ds_read_b128 v[120:123], v5 offset:1024\n" \
  "ds_read_b128 v[124:127], v5 offset:1536\n" \
  "s_waitcnt lgkmcnt(3)\n" \
  "v_smfmac_f32_16x16x64_bf16 v[176:179], v[112:115], a[0:7], v0\n" \
  "v_smfmac_f32_16x16x64_bf16 v[180:183], v[112:115], a[160:167], v0\n" \
  "s_waitcnt lgkmcnt(2)\n" \
  "v_smfmac_f32_16x16x64_bf16 v[176:179], v[116:119], a[8:15], v0\n" \
  "s_barrier\n" \
  "ds_read_b128 v[128:131], v5 offset:2048\n" \
  "v_smfmac_f32_16x16x64_bf16 v[180:183], v[116:119], a[168:175], v0\n" \
  "s_waitcnt lgkmcnt(2)\n" \
  "ds_read_b128 v[132:135], v5 offset:2560\n" \
  "v_smfmac_f32_16x16x64_bf16 v[176:179], v[120:123], a[16:23], v0\n" \
  "ds_read_b128 v[136:139], v5 offset:3072\n" \
  "v_smfmac_f32_16x16x64_bf16 v[180:183], v[120:123], a[176:183], v0\n" \
  "s_waitcnt lgkmcnt(3)\n" \
  "ds_read_b128 v[140:143], v5 offset:3584\n" \
  "v_smfmac_f32_16x16x64_bf16 v[176:179], v[124:127], a[24:31], v0\n" \
  "s_barrier\n" \
  "ds_read_b128 v[144:147], v5 offset:4096\n" \
  "v_smfmac_f32_16x16x64_bf16 v[180:183], v[124:127], a[184:191], v0\n" \
  "s_waitcnt lgkmcnt(4)\n" \
  "ds_read_b128 v[148:151], v5 offset:4608\n" \
  "v_smfmac_f32_16x16x64_bf16 v[176:179], v[128:131], a[32:39], v0\n" \
  "ds_read_b128 v[152:155], v5 offset:5120\n" \
  "v_smfmac_f32_16x16x64_bf16 v[180:183], v[128:131], a[192:199], v0\n" \
  "s_waitcnt lgkmcnt(5)\n" \
  "ds_read_b128 v[156:159], v5 offset:5632\n" \
  "v_smfmac_f32_16x16x64_bf16 v[176:179], v[132:135], a[40:47], v0\n" \
  "s_barrier\n" \
  "ds_read_b128 v[160:163], v5 offset:6144\n" \
  "v_smfmac_f32_16x16x64_bf16 v[180:183], v[132:135], a[200:207], v0\n" \
  "s_waitcnt lgkmcnt(6)\n" \
  "ds_read_b128 v[164:167], v5 offset:6656\n" \
  "v_smfmac_f32_16x16x64_bf16 v[176:179], v[136:139], a[48:55], v0\n" \
  "ds_read_b128 v[168:171], v5 offset:7168\n" \
  "v_smfmac_f32_16x16x64_bf16 v[180:183], v[136:139], a[208:215], v0\n" \
  "s_waitcnt lgkmcnt(7)\n" \
  "ds_read_b128 v[172:175], v5 offset:7680\n" \
  "v_smfmac_f32_16x16x64_bf16 v[176:179], v[140:143], a[56:63], v0\n" \
  "s_barrier\n" \
  "ds_read2_b32 v[112:113], v6 offset0:0 offset1:32\n" \
  "v_smfmac_f32_16x16x64_bf16 v[180:183], v[140:143], a[216:223], v0\n" \
  "s_waitcnt lgkmcnt(8)\n" \
  "ds_read2_b32 v[114:115], v6 offset0:64 offset1:96\n" \
  "v_smfmac_f32_16x16x64_bf16 v[176:179], v[144:147], a[64:71], v0\n" \
  "s_waitcnt lgkmcnt(8)\n" \
  "ds_read2_b32 v[116:117], v6 offset0:128 offset1:160\n" \
  "v_smfmac_f32_16x16x64_bf16 v[176:179], v[148:151], a[72:79], v0\n" \
  "s_waitcnt lgkmcnt(8)\n" \
  "ds_read2_b32 v[118:119], v6 offset0:192 offset1:224\n" \
  "v_smfmac_f32_16x16x64_bf16 v[176:179], v[152:155], a[80:87], v0\n" \
  "s_waitcnt lgkmcnt(8)\n" \
  "ds_read2_b32 v[120:121], v7 offset0:0 offset1:32\n" \
  "v_smfmac_f32_16x16x64_bf16 v[176:179], v[156:159], a[88:95], v0\n" \
  "s_waitcnt lgkmcnt(8)\n" \
  "ds_read2_b32 v[122:123], v7 offset0:64 offset1:96\n" \
  "v_smfmac_f32_16x16x64_bf16 v[176:179], v[160:163], a[96:103], v0\n" \
  "s_waitcnt lgkmcnt(8)\n" \
  "ds_read2_b32 v[124:125], v7 offset0:128 offset1:160\n" \
  "v_smfmac_f32_16x16x64_bf16 v[176:179], v[164:167], a[104:111], v0\n" \
  "s_waitcnt lgkmcnt(8)\n" \
  "ds_read2_b32 v[126:127], v7 offset0:192 offset1:224\n" \
  "v_smfmac_f32_16x16x64_bf16 v[176:179], v[168:171], a[112:119], v0\n" \
  "s_waitcnt lgkmcnt(8)\n" \
  "v_smfmac_f32_16x16x64_bf16 v[176:179], v[172:175], a[120:127], v0\n" \
  "s_waitcnt lgkmcnt(6)\n" \
  "v_smfmac_f32_16x16x64_bf16 v[176:179], v[112:115], a[128:135], v0\n" \
  "s_waitcnt lgkmcnt(4)\n" \
  "v_smfmac_f32_16x16x64_bf16 v[176:179], v[116:119], a[136:143], v0\n" \
  "s_waitcnt lgkmcnt(2)\n" \
  "v_smfmac_f32_16x16x64_bf16 v[176:179], v[120:123], a[144:151], v0\n" \
  "s_waitcnt lgkmcnt(0)\n" \
  "v_smfmac_f32_16x16x64_bf16 v[176:179], v[124:127], a[152:159], v0\n" \
  "buffer_store_dword v34, v1, s[36:39], 0 offen sc1\n" \
  "v_smfmac_f32_16x16x64_bf16 v[180:183], v[144:147], a[224:231], v0\n" \
  "v_smfmac_f32_16x16x64_bf16 v[180:183], v[148:151], a[232:239], v0\n" \
  "v_cvt_pk_bf16_f32 v184, v176, v177\n" \
  "v_smfmac_f32_16x16x64_bf16 v[180:183], v[152:155], a[240:247], v0\n" \
  "v_cvt_pk_bf16_f32 v185, v178, v179\n" \
  "v_smfmac_f32_16x16x64_bf16 v[180:183], v[156:159], a[248:255], v0\n" \
  "ds_write_b64 v8, v[184:185]\n" \
  "v_add_f32 v186, v176, v177\n" \
  "v_smfmac_f32_16x16x64_bf16 v[180:183], v[160:163], v[192:199], v0\n" \
  "v_add_f32 v187, v178, v179\n" \
  "v_smfmac_f32_16x16x64_bf16 v[180:183], v[164:167], v[200:207], v0\n" \
  "v_cmp_u_f32 vcc, v186, v187\n" \
  "v_smfmac_f32_16x16x64_bf16 v[180:183], v[168:171], v[208:215], v0\n" \
  "v_smfmac_f32_16x16x64_bf16 v[180:183], v[172:175], v[216:223], v0\n" \
  "s_cbranch_vccnz LMF13_%=\n" \
  "LMOK13_%=:\n" \
  "v_smfmac_f32_16x16x64_bf16 v[180:183], v[112:115], v[224:231], v0\n" \
  "s_waitcnt lgkmcnt(0)\n" \
  "s_barrier\n" \
  "v_smfmac_f32_16x16x64_bf16 v[180:183], v[116:119], v[232:239], v0\n" \
  "v_smfmac_f32_16x16x64_bf16 v[180:183], v[120:123], v[240:247], v0\n" \
  "v_smfmac_f32_16x16x64_bf16 v[180:183], v[124:127], v[248:255], v0\n" \
  "s_nop 3\n" \
  "v_mov_b32 v31, v31\n" \
  "s_nop 2\n" \
  "v_cvt_pk_bf16_f32 v186, v180, v181\n" \
  "v_cvt_pk_bf16_f32 v187, v182, v183\n" \
  "ds_write_b64 v9, v[186:187]\n" \
  "s_waitcnt lgkmcnt(0)\n" \
  "s_barrier\n" \
  "s_nop 15\n" \
  "s_nop 0\n" \
  "ds_read_b64_tr_b16 v[128:129], v12\n" \
  "ds_read_b64_tr_b16 v[130:131], v12 offset:32\n" \
  "s_waitcnt lgkmcnt(0)\n" \
  "buffer_store_dwordx4 v[128:131], v14, s[36:39], s69 offen sc1\n" \
  "s_mov_b32 s67, s66\n" \
  "s_cmp_eq_u32 s64, 64\n" \
  "s_cbranch_scc1 LDONE_%=\n" \
  "LB13_%=:\n" \
  "s_barrier\n" \
  "buffer_load_dwordx4 v[112:115], v15, s[36:39], s65 offen sc1\n" \
  "s_barrier\n" \
  "buffer_load_dwordx4 v[116:119], v15, s[36:39], s65 offen offset:128 sc1\n" \
  "s_barrier\n" \
  "buffer_load_dwordx4 v[120:123], v16, s[36:39], s65 offen sc1\n" \
  "s_barrier\n" \
  "buffer_load_dwordx4 v[124:127], v16, s[36:39], s65 offen offset:512 sc1\n" \
  "s_barrier\n" \
  "buffer_load_dwordx4 v[128:131], v16, s[36:39], s65 offen offset:1024 sc1\n" \
  "s_waitcnt vmcnt(7)\n" \
  "s_barrier\n" \
  "buffer_load_dwordx4 v[132:135], v16, s[36:39], s65 offen offset:1536 sc1\n" \
  "ds_read_b128 v[40:43], v19\n" \
  "ds_read_b128 v[44:47], v19 offset:512\n" \
  "ds_read_b128 v[48:51], v19 offset:1024\n" \
  "ds_read_b128 v[52:55], v19 offset:1536\n" \
  "s_mov_b64 exec, s[46:47]\n" \
  "ds_read_b128 v[56:59], v20\n" \
  "ds_read_b128 v[60:63], v20 offset:128\n" \
  "ds_read_b128 v[64:67], v20 offset:256\n" \
  "ds_read_b128 v[68:71], v20 offset:384\n" \
  "s_mov_b64 exec, -1\n" \
  "s_nop 15\n" \
  "s_nop 1\n" \
  "buffer_load_dwordx4 v[136:139], v17, s[36:39], s65 offen sc1\n" \
  "buffer_load_dwordx4 v[140:143], v18, s[36:39], s65 offen sc1\n" \
  "s_waitcnt lgkmcnt(0)\n" \
  "s_getreg_b32 s71, hwreg(HW_REG_IB_STS, 0, 4)\n" \
  "s_cmp_le_u32 s71, 6\n" \
  "v_mfma_f32_16x16x32_bf16 v[144:147], v[72:75], v[72:75], 0\n" \
  "s_cbranch_scc1 LHXS13_%=\n" \
  "s_getreg_b32 s71, hwreg(HW_REG_IB_STS, 0, 4)\n" \
  "s_cmp_le_u32 s71, 6\n" \
  "v_mfma_f32_16x16x32_bf16 v[144:147], v[72:75], v[72:75], 0\n" \
  "s_cbranch_scc1 LHXS13_%=\n" \
  "s_getreg_b32 s71, hwreg(HW_REG_IB_STS, 0, 4)\n" \
  "s_cmp_le_u32 s71, 6\n" \
  "v_mfma_f32_16x16x32_bf16 v[144:147], v[72:75], v[72:75], 0\n" \
  "s_cbranch_scc1 LHXS13_%=\n" \
  "s_getreg_b32 s71, hwreg(HW_REG_IB_STS, 0, 4)\n" \
  "s_cmp_le_u32 s71, 6\n" \
  "v_mfma_f32_16x16x32_bf16 v[144:147], v[72:75], v[72:75], 0\n" \
  "s_cbranch_scc1 LHXS13_%=\n" \
  "s_getreg_b32 s71, hwreg(HW_REG_IB_STS, 0, 4)\n" \
  "s_cmp_le_u32 s71, 6\n" \
  "v_mfma_f32_16x16x32_bf16 v[144:147], v[72:75], v[72:75], 0\n" \
  "s_cbranch_scc1 LHXS13_%=\n" \
  "s_getreg_b32 s71, hwreg(HW_REG_IB_STS, 0, 4)\n" \
  "s_cmp_le_u32 s71, 6\n" \
  "v_mfma_f32_16x16x32_bf16 v[144:147], v[72:75], v[72:75], 0\n" \
  "s_cbranch_scc1 LHXS13_%=\n" \
  "s_getreg_b32 s71, hwreg(HW_REG_IB_STS, 0, 4)\n" \
  "s_cmp_le_u32 s71, 6\n" \
  "v_mfma_f32_16x16x32_bf16 v[144:147], v[72:75], v[72:75], 0\n" \
  "s_cbranch_scc1 LHXS13_%=\n" \
  "s_getreg_b32 s71, hwreg(HW_REG_IB_STS, 0, 4)\n" \
  "s_cmp_le_u32 s71, 6\n" \
  "v_mfma_f32_16x16x32_bf16 v[144:147], v[72:75], v[72:75], 0\n" \
  "s_cbranch_scc1 LHXS13_%=\n" \
  "s_getreg_b32 s71, hwreg(HW_REG_IB_STS, 0, 4)\n" \
  "s_cmp_le_u32 s71, 6\n" \
  "v_mfma_f32_16x16x32_bf16 v[144:147], v[72:75], v[72:75], 0\n" \
  "s_cbranch_scc1 LHXS13_%=\n" \
  "s_getreg_b32 s71, hwreg(HW_REG_IB_STS, 0, 4)\n" \
  "s_cmp_le_u32 s71, 6\n" \
  "v_mfma_f32_16x16x32_bf16 v[144:147], v[72:75], v[72:75], 0\n" \
  "s_cbranch_scc1 LHXS13_%=\n" \
  "s_getreg_b32 s71, hwreg(HW_REG_IB_STS, 0, 4)\n" \
  "s_cmp_le_u32 s71, 6\n" \
  "v_mfma_f32_16x16x32_bf16 v[144:147], v[72:75], v[72:75], 0\n" \
  "s_cbranch_scc1 LHXS13_%=\n" \
  "s_getreg_b32 s71, hwreg(HW_REG_IB_STS, 0, 4)\n" \
  "s_cmp_le_u32 s71, 6\n" \
  "v_mfma_f32_16x16x32_bf16 v[144:147], v[72:75], v[72:75], 0\n" \
  "s_cbranch_scc1 LHXS13_%=\n" \
  "s_getreg_b32 s71, hwreg(HW_REG_IB_STS, 0, 4)\n" \
  "s_cmp_le_u32 s71, 6\n" \
  "v_mfma_f32_16x16x32_bf16 v[144:147], v[72:75], v[72:75], 0\n" \
  "s_cbranch_scc1 LHXS13_%=\n" \
  "s_getreg_b32 s71, hwreg(HW_REG_IB_STS, 0, 4)\n" \
  "s_cmp_le_u32 s71, 6\n" \
  "v_mfma_f32_16x16x32_bf16 v[144:147], v[72:75], v[72:75], 0\n" \
  "s_cbranch_scc1 LHXS13_%=\n" \
  "s_getreg_b32 s71, hwreg(HW_REG_IB_STS, 0, 4)\n" \
  "s_cmp_le_u32 s71, 6\n" \
  "v_mfma_f32_16x16x32_bf16 v[144:147], v[72:75], v[72:75], 0\n" \
  "s_cbranch_scc1 LHXS13_%=\n" \
  "s_getreg_b32 s71, hwreg(HW_REG_IB_STS, 0, 4)\n" \
  "s_cmp_le_u32 s71, 6\n" \
  "v_mfma_f32_16x16x32_bf16 v[144:147], v[72:75], v[72:75], 0\n" \
  "s_cbranch_scc1 LHXS13_%=\n" \
  "s_getreg_b32 s71, hwreg(HW_REG_IB_STS, 0, 4)\n" \
  "s_cmp_le_u32 s71, 6\n" \
  "v_mfma_f32_16x16x32_bf16 v[144:147], v[72:75], v[72:75], 0\n" \
  "s_cbranch_scc1 LHXS13_%=\n" \
  "s_getreg_b32 s71, hwreg(HW_REG_IB_STS, 0, 4)\n" \
  "s_cmp_le_u32 s71, 6\n" \
  "v_mfma_f32_16x16x32_bf16 v[144:147], v[72:75], v[72:75], 0\n" \
  "s_cbranch_scc1 LHXS13_%=\n" \
  "s_getreg_b32 s71, hwreg(HW_REG_IB_STS, 0, 4)\n" \
  "s_cmp_le_u32 s71, 6\n" \
  "v_mfma_f32_16x16x32_bf16 v[144:147], v[72:75], v[72:75], 0\n" \
  "s_cbranch_scc1 LHXS13_%=\n" \
  "s_getreg_b32 s71, hwreg(HW_REG_IB_STS, 0, 4)\n" \
  "s_cmp_le_u32 s71, 6\n" \
  "v_mfma_f32_16x16x32_bf16 v[144:147], v[72:75], v[72:75], 0\n" \
  "s_cbranch_scc1 LHXS13_%=\n" \
  "s_waitcnt vmcnt(6)\n" \
  "LHXS13_%=:\n" \
  "LS213_%=:\n" \
  "v_dot2_f32_bf16 v176, v112, s22, 0\n" \
  "v_dot2_f32_bf16 v177, v112, s23, 0\n" \
  "v_dot2_f32_bf16 v178, v113, s22, 0\n" \
  "v_dot2_f32_bf16 v179, v113, s23, 0\n" \
  "v_dot2_f32_bf16 v180, v114, s22, 0\n" \
  "v_dot2_f32_bf16 v181, v114, s23, 0\n" \
  "v_dot2_f32_bf16 v182, v115, s22, 0\n" \
  "v_dot2_f32_bf16 v183, v115, s23, 0\n" \
  "v_dot2_f32_bf16 v176, v116, s22, v176\n" \
  "v_dot2_f32_bf16 v177, v116, s23, v177\n" \
  "v_dot2_f32_bf16 v178, v117, s22, v178\n" \
  "v_dot2_f32_bf16 v179, v117, s23, v179\n" \
  "v_dot2_f32_bf16 v180, v118, s22, v180\n" \
  "v_dot2_f32_bf16 v181, v118, s23, v181\n" \
  "v_dot2_f32_bf16 v182, v119, s22, v182\n" \
  "v_dot2_f32_bf16 v183, v119, s23, v183\n" \
  "v_exp_f32 v176, v176\n" \
  "v_exp_f32 v177, v177\n" \
  "v_exp_f32 v178, v178\n" \
  "v_exp_f32 v179, v179\n" \
  "s_waitcnt vmcnt(5)\n" \
  "v_mfma_f32_16x16x32_bf16 v[160:163], v[56:59], v[120:123], 0\n" \
  "v_exp_f32 v180, v180\n" \
  "v_mfma_f32_16x16x32_bf16 v[144:147], v[40:43], v[120:123], 0\n" \
  "v_exp_f32 v181, v181\n" \
  "v_exp_f32 v182, v182\n" \
  "v_exp_f32 v183, v183\n" \
  "v_cvt_pk_bf16_f32 v184, v176, v177\n" \
  "v_cvt_pk_bf16_f32 v185, v178, v179\n" \
  "v_cvt_pk_bf16_f32 v186, v180, v181\n" \
  "v_cvt_pk_bf16_f32 v187, v182, v183\n" \
  "s_nop 1\n" \
  "v_mfma_f32_16x16x32_bf16 v[160:163], v[72:75], v[184:187], v[160:163]\n" \
  "v_mfma_f32_16x16x32_bf16 v[164:167], v[76:79], v[184:187], 0\n" \
  "s_waitcnt vmcnt(4)\n" \
  "v_mfma_f32_16x16x32_bf16 v[164:167], v[60:63], v[124:127], v[164:167]\n" \
  "v_rcp_f32 v188, v162\n" \
  "v_mfma_f32_16x16x32_bf16 v[148:151], v[44:47], v[124:127], 0\n" \
  "v_mul_f32 v144, v188, v144\n" \
  "v_mul_f32 v145, v188, v145\n" \
  "v_mfma_f32_16x16x32_bf16 v[168:171], v[80:83], v[184:187], 0\n" \
  "s_waitcnt vmcnt(3)\n" \
  "v_rcp_f32 v189, v166\n" \
  "v_mfma_f32_16x16x32_bf16 v[168:171], v[64:67], v[128:131], v[168:171]\n" \
  "v_mul_f32 v146, v188, v146\n" \
  "v_mul_f32 v147, v188, v147\n" \
  "v_mfma_f32_16x16x32_bf16 v[152:155], v[48:51], v[128:131], 0\n" \
  "v_mul_f32 v160, v188, v160\n" \
  "v_mul_f32 v161, v188, v161\n" \
  "v_mfma_f32_16x16x32_bf16 v[172:175], v[84:87], v[184:187], 0\n" \
  "s_waitcnt vmcnt(2)\n" \
  "v_rcp_f32 v190, v170\n" \
  "v_mfma_f32_16x16x32_bf16 v[172:175], v[68:71], v[132:135], v[172:175]\n" \
  "v_cndmask_b32 v167, v188, v189, s[28:29]\n" \
  "v_cndmask_b32 v167, v167, v190, s[26:27]\n" \
  "v_mfma_f32_16x16x32_bf16 v[156:159], v[52:55], v[132:135], 0\n" \
  "v_fmac_f32 v144, v189, v148\n" \
  "v_fmac_f32 v145, v189, v149\n" \
  "v_fmac_f32 v146, v189, v150\n" \
  "v_fmac_f32 v147, v189, v151\n" \
  "v_rcp_f32 v191, v174\n" \
  "v_fmac_f32 v160, v189, v164\n" \
  "v_cndmask_b32 v167, v167, v191, s[24:25]\n" \
  "v_pk_mul_f32 v[176:177], v[176:177], v[166:167] op_sel:[0,1] op_sel_hi:[1,1]\n" \
  "v_pk_mul_f32 v[178:179], v[178:179], v[166:167] op_sel:[0,1] op_sel_hi:[1,1]\n" \
  "v_pk_mul_f32 v[180:181], v[180:181], v[166:167] op_sel:[0,1] op_sel_hi:[1,1]\n" \
  "v_pk_mul_f32 v[182:183], v[182:183], v[166:167] op_sel:[0,1] op_sel_hi:[1,1]\n" \
  "v_cmp_nlt_f32 vcc, 0x0d800000, v167\n" \
  "v_cvt_pk_bf16_f32 v184, v176, v177\n" \
  "v_cvt_pk_bf16_f32 v185, v178, v179\n" \
  "v_cvt_pk_bf16_f32 v186, v180, v181\n" \
  "v_cvt_pk_bf16_f32 v187, v182, v183\n" \
  "v_fmac_f32 v161, v189, v165\n" \
  "v_pk_fma_f32 v[160:161], v[168:169], v[190:191], v[160:161] op_sel:[0,0,0] op_sel_hi:[1,0,1]\n" \
  "v_pk_fma_f32 v[160:161], v[172:173], v[190:191], v[160:161] op_sel:[0,1,0] op_sel_hi:[1,1,1]\n" \
  "v_pk_fma_f32 v[144:145], v[152:153], v[190:191], v[144:145] op_sel:[0,0,0] op_sel_hi:[1,0,1]\n" \
  "v_pk_fma_f32 v[146:147], v[154:155], v[190:191], v[146:147] op_sel:[0,0,0] op_sel_hi:[1,0,1]\n" \
  "v_pk_fma_f32 v[144:145], v[156:157], v[190:191], v[144:145] op_sel:[0,1,0] op_sel_hi:[1,1,1]\n" \
  "v_pk_fma_f32 v[146:147], v[158:159], v[190:191], v[146:147] op_sel:[0,1,0] op_sel_hi:[1,1,1]\n" \
  "s_waitcnt vmcnt(0)\n" \
  "v_mfma_f32_16x16x32_bf16 v[160:163], v[140:143], v[184:187], v[160:163]\n" \
  "s_mov_b64 s[80:81], vcc\n" \
  "v_mfma_f32_16x16x32_bf16 v[144:147], v[136:139], v[184:187], v[144:147]\n" \
  "s_mov_b64 exec, s[30:31]\n" \
  "s_nop 2\n" \
  "ds_write_b64 v22, v[160:161]\n" \
  "v_cmp_u_f32 s[78:79], v160, v161\n" \
  "s_nop 1\n" \
  "ds_write2_b64 v21, v[144:145], v[146:147] offset1:16\n" \
  "v_add_f32 v188, v144, v145\n" \
  "v_add_f32 v189, v146, v147\n" \
  "v_cmp_u_f32 vcc, v188, v189\n" \
  "s_or_b64 s[80:81], s[80:81], s[78:79]\n" \
  "s_or_b64 vcc, vcc, s[80:81]\n" \
  "s_cbranch_vccnz LSF13_%=\n" \
  "LSOK13_%=:\n" \
  "s_mov_b64 exec, -1\n" \
  "s_waitcnt lgkmcnt(0)\n" \
  "s_barrier\n" \
  "ds_read_b32 v188, v33\n" \
  "ds_read_b128 v[120:123], v23\n" \
  "ds_read_b128 v[124:127], v23 offset:768\n" \
  "ds_read_b128 v[128:131], v23 offset:1536\n" \
  "ds_read_b128 v[132:135], v23 offset:2304\n" \
  "s_waitcnt lgkmcnt(4)\n" \
  "v_cmp_ne_u32 vcc, 0, v188\n" \
  "s_waitcnt lgkmcnt(2)\n" \
  "v_pk_add_f32 v[120:121], v[120:121], v[122:123]\n" \
  "v_pk_add_f32 v[124:125], v[124:125], v[126:127]\n" \
  "v_pk_add_f32 v[120:121], v[120:121], v[124:125]\n" \
  "s_waitcnt lgkmcnt(1)\n" \
  "v_pk_add_f32 v[128:129], v[128:129], v[130:131]\n" \
  "v_pk_add_f32 v[120:121], v[120:121], v[128:129]\n" \
  "s_waitcnt lgkmcnt(0)\n" \
  "v_pk_add_f32 v[132:133], v[132:133], v[134:135]\n" \
  "v_pk_add_f32 v[120:121], v[120:121], v[132:133]\n" \
  "v_cvt_pk_bf16_f32 v120, v120, v121\n" \
  "s_cbranch_vccnz LSR13_%=\n" \
  "buffer_store_dword v120, v24, s[36:39], s66 offen sc1\n" \
  "LNL13_%=:\n" \
  "s_waitcnt lgkmcnt(0)\n" \
  "s_cmp_eq_u32 s73, 1\n" \
  "s_cbranch_scc1 LOOP13_%=\n" \
  "s_branch LDONE_%=\n" \
  "LMF13_%=:\n" \
  "v_mbcnt_lo_u32_b32 v130, -1, 0\n" \
  "v_mbcnt_hi_u32_b32 v130, -1, v130\n" \
  "v_lshlrev_b32 v130, 4, v130\n" \
  "v_mov_b32 v128, 0\n" \
  "ds_read_b128 v[132:135], v130 offset:0\n" \
  "ds_read_b128 v[136:139], v130 offset:1024\n" \
  "ds_read_b128 v[140:143], v130 offset:2048\n" \
  "ds_read_b128 v[144:147], v130 offset:3072\n" \
  "ds_read_b128 v[148:151], v130 offset:4096\n" \
  "s_waitcnt lgkmcnt(0)\n" \
  "v_max3_u32 v128, v128, v132, v133\n" \
  "v_max3_u32 v128, v128, v134, v135\n" \
  "v_max3_u32 v128, v128, v136, v137\n" \
  "v_max3_u32 v128, v128, v138, v139\n" \
  "v_max3_u32 v128, v128, v140, v141\n" \
  "v_max3_u32 v128, v128, v142, v143\n" \
  "v_max3_u32 v128, v128, v144, v145\n" \
  "v_max3_u32 v128, v128, v146, v147\n" \
  "v_max3_u32 v128, v128, v148, v149\n" \
  "v_max3_u32 v128, v128, v150, v151\n" \
  "ds_read_b128 v[132:135], v130 offset:5120\n" \
  "ds_read_b128 v[136:139], v130 offset:6144\n" \
  "ds_read_b128 v[140:143], v130 offset:7168\n" \
  "ds_read_b128 v[144:147], v130 offset:8192\n" \
  "ds_read_b128 v[148:151], v130 offset:9216\n" \
  "s_waitcnt lgkmcnt(0)\n" \
  "v_max3_u32 v128, v128, v132, v133\n" \
  "v_max3_u32 v128, v128, v134, v135\n" \
  "v_max3_u32 v128, v128, v136, v137\n" \
  "v_max3_u32 v128, v128, v138, v139\n" \
  "v_max3_u32 v128, v128, v140, v141\n" \
  "v_max3_u32 v128, v128, v142, v143\n" \
  "v_max3_u32 v128, v128, v144, v145\n" \
  "v_max3_u32 v128, v128, v146, v147\n" \
  "v_max3_u32 v128, v128, v148, v149\n" \
  "v_max3_u32 v128, v128, v150, v151\n" \
  "v_cmp_eq_u32 vcc, -1, v128\n" \
  "s_cbranch_vccz LMOK13_%=\n" \
  "s_cmp_gt_u32 s58, 4000\n" \
  "s_cbranch_scc1 LMOK13_%=\n" \
  "s_add_u32 s58, s58, 1\n" \
  "s_sub_u32 s64, s64, 1\n" \
  "s_mov_b32 s66, s65\n" \
  "s_mov_b32 s68, s69\n" \
  "s_waitcnt vmcnt(0) lgkmcnt(0)\n" \
  "s_barrier\n" \
  "s_branch LOOP13_%=\n" \
  "LSF13_%=:\n" \
  "s_getpc_b64 s[78:79]\n" \
  "s_branch LSF_%=\n" \
  "s_branch LSOK13_%=\n" \
  "LSR13_%=:\n" \
  "v_mov_b32 v188, 0\n" \
  "s_add_u32 s59, s59, 1\n" \
  "s_barrier\n" \
  "ds_write_b32 v33, v188\n" \
  "s_waitcnt vmcnt(0) lgkmcnt(0)\n" \
  "s_branch LB13_%=\n" \
  "LSF_%=:\n" \
  "s_mov_b64 exec, -1\n" \
  "v_mov_b32 v188, 0\n" \
  "v_max3_u32 v188, v188, v112, v113\n" \
  "v_max3_u32 v188, v188, v114, v115\n" \
  "v_max3_u32 v188, v188, v116, v117\n" \
  "v_max3_u32 v188, v188, v118, v119\n" \
  "v_max3_u32 v188, v188, v120, v121\n" \
  "v_max3_u32 v188, v188, v122, v123\n" \
  "v_max3_u32 v188, v188, v124, v125\n" \
  "v_max3_u32 v188, v188, v126, v127\n" \
  "v_max3_u32 v188, v188, v128, v129\n" \
  "v_max3_u32 v188, v188, v130, v131\n" \
  "v_max3_u32 v188, v188, v132, v133\n" \
  "v_max3_u32 v188, v188, v134, v135\n" \
  "v_max3_u32 v188, v188, v136, v137\n" \
  "v_max3_u32 v188, v188, v138, v139\n" \
  "v_max3_u32 v188, v188, v140, v141\n" \
  "v_max3_u32 v188, v188, v142, v143\n" \
  "v_cmp_eq_u32 vcc, -1, v188\n" \
  "s_cbranch_vccz LSG_%=\n" \
  "s_cmp_gt_u32 s59, 4000\n" \
  "s_cbranch_scc1 LSG_%=\n" \
  "ds_write_b32 v33, v32\n" \
  "s_add_u32 s78, s78, 4\n" \
  "s_addc_u32 s79, s79, 0\n" \
  "s_setpc_b64 s[78:79]\n" \
  "LSG_%=:\n" \
  "v_dot2_f32_bf16 v176, v112, s22, 0\n" \
  "v_dot2_f32_bf16 v176, v116, s22, v176\n" \
  "v_dot2_f32_bf16 v177, v112, s23, 0\n" \
  "v_dot2_f32_bf16 v177, v116, s23, v177\n" \
  "v_dot2_f32_bf16 v178, v113, s22, 0\n" \
  "v_dot2_f32_bf16 v178, v117, s22, v178\n" \
  "v_dot2_f32_bf16 v179, v113, s23, 0\n" \
  "v_dot2_f32_bf16 v179, v117, s23, v179\n" \
  "v_dot2_f32_bf16 v180, v114, s22, 0\n" \
  "v_dot2_f32_bf16 v180, v118, s22, v180\n" \
  "v_dot2_f32_bf16 v181, v114, s23, 0\n" \
  "v_dot2_f32_bf16 v181, v118, s23, v181\n" \
  "v_dot2_f32_bf16 v182, v115, s22, 0\n" \
  "v_dot2_f32_bf16 v182, v119, s22, v182\n" \
  "v_dot2_f32_bf16 v183, v115, s23, 0\n" \
  "v_dot2_f32_bf16 v183, v119, s23, v183\n" \
  "s_nop 2\n" \
  "v_max3_f32 v158, v176, v177, v178\n" \
  "v_max3_f32 v158, v158, v179, v180\n" \
  "v_max3_f32 v158, v158, v181, v182\n" \
  "v_max_f32 v158, v158, v183\n" \
  "s_add_u32 s72, s65, 32768\n" \
  "v_mov_b32 v157, 0x7fd57fd5\n" \
  "v_cmp_eq_u32 s[80:81], v120, v157\n" \
  "LSP0_%=:\n" \
  "buffer_load_dwordx4 v[152:155], v16, s[36:39], s72 offen sc1\n" \
  "s_waitcnt vmcnt(0)\n" \
  "v_max3_u32 v156, v152, v153, v154\n" \
  "v_max_u32 v156, v156, v155\n" \
  "v_cmp_eq_u32 vcc, -1, v156\n" \
  "s_and_b64 vcc, vcc, s[80:81]\n" \
  "s_cbranch_vccz LSQ0_%=\n" \
  "s_add_u32 s60, s60, 1\n" \
  "s_cmp_gt_u32 s60, 20000\n" \
  "s_cbranch_scc1 LSQ0_%=\n" \
  "s_sleep 8\n" \
  "s_branch LSP0_%=\n" \
  "LSQ0_%=:\n" \
  "v_lshlrev_b32 v144, 16, v120\n" \
  "v_log_f32 v144, v144\n" \
  "v_lshlrev_b32 v157, 16, v152\n" \
  "v_cndmask_b32 v144, v144, v157, s[80:81]\n" \
  "v_and_b32 v145, 0xffff0000, v120\n" \
  "v_log_f32 v145, v145\n" \
  "v_and_b32 v157, 0xffff0000, v152\n" \
  "v_cndmask_b32 v145, v145, v157, s[80:81]\n" \
  "v_lshlrev_b32 v146, 16, v121\n" \
  "v_log_f32 v146, v146\n" \
  "v_lshlrev_b32 v157, 16, v153\n" \
  "v_cndmask_b32 v146, v146, v157, s[80:81]\n" \
  "v_and_b32 v147, 0xffff0000, v121\n" \
  "v_log_f32 v147, v147\n" \
  "v_and_b32 v157, 0xffff0000, v153\n" \
  "v_cndmask_b32 v147, v147, v157, s[80:81]\n" \
  "v_lshlrev_b32 v148, 16, v122\n" \
  "v_log_f32 v148, v148\n" \
  "v_lshlrev_b32 v157, 16, v154\n" \
  "v_cndmask_b32 v148, v148, v157, s[80:81]\n" \
  "v_and_b32 v149, 0xffff0000, v122\n" \
  "v_log_f32 v149, v149\n" \
  "v_and_b32 v157, 0xffff0000, v154\n" \
  "v_cndmask_b32 v149, v149, v157, s[80:81]\n" \
  "v_lshlrev_b32 v150, 16, v123\n" \
  "v_log_f32 v150, v150\n" \
  "v_lshlrev_b32 v157, 16, v155\n" \
  "v_cndmask_b32 v150, v150, v157, s[80:81]\n" \
  "v_and_b32 v151, 0xffff0000, v123\n" \
  "v_log_f32 v151, v151\n" \
  "v_and_b32 v157, 0xffff0000, v155\n" \
  "v_cndmask_b32 v151, v151, v157, s[80:81]\n" \
  "v_max3_f32 v156, v144, v145, v146\n" \
  "v_max3_f32 v156, v156, v147, v148\n" \
  "v_max3_f32 v156, v156, v149, v150\n" \
  "v_max_f32 v156, v156, v151\n" \
  "s_mov_b64 exec, 0xffff\n" \
  "v_max_f32 v156, v156, v158\n" \
  "s_mov_b64 exec, -1\n" \
  "v_mov_b32 v157, v156\n" \
  "s_nop 1\n" \
  "v_permlane16_swap_b32 v156, v157\n" \
  "v_max_f32 v156, v156, v157\n" \
  "v_mov_b32 v157, v156\n" \
  "s_nop 1\n" \
  "v_permlane32_swap_b32 v156, v157\n" \
  "v_max_f32 v156, v156, v157\n" \
  "s_mov_b64 exec, 0xffff\n" \
  "v_mov_b32 v188, v156\n" \
  "s_mov_b64 exec, -1\n" \
  "v_sub_f32 v144, v144, v156\n" \
  "v_exp_f32 v144, v144\n" \
  "v_sub_f32 v145, v145, v156\n" \
  "v_exp_f32 v145, v145\n" \
  "v_sub_f32 v146, v146, v156\n" \
  "v_exp_f32 v146, v146\n" \
  "v_sub_f32 v147, v147, v156\n" \
  "v_exp_f32 v147, v147\n" \
  "v_sub_f32 v148, v148, v156\n" \
  "v_exp_f32 v148, v148\n" \
  "v_sub_f32 v149, v149, v156\n" \
  "v_exp_f32 v149, v149\n" \
  "v_sub_f32 v150, v150, v156\n" \
  "v_exp_f32 v150, v150\n" \
  "v_sub_f32 v151, v151, v156\n" \
  "v_exp_f32 v151, v151\n" \
  "v_cvt_pk_bf16_f32 v120, v144, v145\n" \
  "v_cvt_pk_bf16_f32 v121, v146, v147\n" \
  "v_cvt_pk_bf16_f32 v122, v148, v149\n" \
  "v_cvt_pk_bf16_f32 v123, v150, v151\n" \
  "v_mov_b32 v157, 0x7fd57fd5\n" \
  "v_cmp_eq_u32 s[80:81], v124, v157\n" \
  "LSP1_%=:\n" \
  "buffer_load_dwordx4 v[152:155], v16, s[36:39], s72 offen offset:512 sc1\n" \
  "s_waitcnt vmcnt(0)\n" \
  "v_max3_u32 v156, v152, v153, v154\n" \
  "v_max_u32 v156, v156, v155\n" \
  "v_cmp_eq_u32 vcc, -1, v156\n" \
  "s_and_b64 vcc, vcc, s[80:81]\n" \
  "s_cbranch_vccz LSQ1_%=\n" \
  "s_add_u32 s60, s60, 1\n" \
  "s_cmp_gt_u32 s60, 20000\n" \
  "s_cbranch_scc1 LSQ1_%=\n" \
  "s_sleep 8\n" \
  "s_branch LSP1_%=\n" \
  "LSQ1_%=:\n" \
  "v_lshlrev_b32 v144, 16, v124\n" \
  "v_log_f32 v144, v144\n" \
  "v_lshlrev_b32 v157, 16, v152\n" \
  "v_cndmask_b32 v144, v144, v157, s[80:81]\n" \
  "v_and_b32 v145, 0xffff0000, v124\n" \
  "v_log_f32 v145, v145\n" \
  "v_and_b32 v157, 0xffff0000, v152\n" \
  "v_cndmask_b32 v145, v145, v157, s[80:81]\n" \
  "v_lshlrev_b32 v146, 16, v125\n" \
  "v_log_f32 v146, v146\n" \
  "v_lshlrev_b32 v157, 16, v153\n" \
  "v_cndmask_b32 v146, v146, v157, s[80:81]\n" \
  "v_and_b32 v147, 0xffff0000, v125\n" \
  "v_log_f32 v147, v147\n" \
  "v_and_b32 v157, 0xffff0000, v153\n" \
  "v_cndmask_b32 v147, v147, v157, s[80:81]\n" \
  "v_lshlrev_b32 v148, 16, v126\n" \
  "v_log_f32 v148, v148\n" \
  "v_lshlrev_b32 v157, 16, v154\n" \
  "v_cndmask_b32 v148, v148, v157, s[80:81]\n" \
  "v_and_b32 v149, 0xffff0000, v126\n" \
  "v_log_f32 v149, v149\n" \
  "v_and_b32 v157, 0xffff0000, v154\n" \
  "v_cndmask_b32 v149, v149, v157, s[80:81]\n" \
  "v_lshlrev_b32 v150, 16, v127\n" \
  "v_log_f32 v150, v150\n" \
  "v_lshlrev_b32 v157, 16, v155\n" \
  "v_cndmask_b32 v150, v150, v157, s[80:81]\n" \
  "v_and_b32 v151, 0xffff0000, v127\n" \
  "v_log_f32 v151, v151\n" \
  "v_and_b32 v157, 0xffff0000, v155\n" \
  "v_cndmask_b32 v151, v151, v157, s[80:81]\n" \
  "v_max3_f32 v156, v144, v145, v146\n" \
  "v_max3_f32 v156, v156, v147, v148\n" \
  "v_max3_f32 v156, v156, v149, v150\n" \
  "v_max_f32 v156, v156, v151\n" \
  "s_mov_b64 exec, s[28:29]\n" \
  "v_max_f32 v156, v156, v158\n" \
  "s_mov_b64 exec, -1\n" \
  "v_mov_b32 v157, v156\n" \
  "s_nop 1\n" \
  "v_permlane16_swap_b32 v156, v157\n" \
  "v_max_f32 v156, v156, v157\n" \
  "v_mov_b32 v157, v156\n" \
  "s_nop 1\n" \
  "v_permlane32_swap_b32 v156, v157\n" \
  "v_max_f32 v156, v156, v157\n" \
  "s_mov_b64 exec, s[28:29]\n" \
  "v_mov_b32 v188, v156\n" \
  "s_mov_b64 exec, -1\n" \
  "v_sub_f32 v144, v144, v156\n" \
  "v_exp_f32 v144, v144\n" \
  "v_sub_f32 v145, v145, v156\n" \
  "v_exp_f32 v145, v145\n" \
  "v_sub_f32 v146, v146, v156\n" \
  "v_exp_f32 v146, v146\n" \
  "v_sub_f32 v147, v147, v156\n" \
  "v_exp_f32 v147, v147\n" \
  "v_sub_f32 v148, v148, v156\n" \
  "v_exp_f32 v148, v148\n" \
  "v_sub_f32 v149, v149, v156\n" \
  "v_exp_f32 v149, v149\n" \
  "v_sub_f32 v150, v150, v156\n" \
  "v_exp_f32 v150, v150\n" \
  "v_sub_f32 v151, v151, v156\n" \
  "v_exp_f32 v151, v151\n" \
  "v_cvt_pk_bf16_f32 v124, v144, v145\n" \
  "v_cvt_pk_bf16_f32 v125, v146, v147\n" \
  "v_cvt_pk_bf16_f32 v126, v148, v149\n" \
  "v_cvt_pk_bf16_f32 v127, v150, v151\n" \
  "v_mov_b32 v157, 0x7fd57fd5\n" \
  "v_cmp_eq_u32 s[80:81], v128, v157\n" \
  "LSP2_%=:\n" \
  "buffer_load_dwordx4 v[152:155], v16, s[36:39], s72 offen offset:1024 sc1\n" \
  "s_waitcnt vmcnt(0)\n" \
  "v_max3_u32 v156, v152, v153, v154\n" \
  "v_max_u32 v156, v156, v155\n" \
  "v_cmp_eq_u32 vcc, -1, v156\n" \
  "s_and_b64 vcc, vcc, s[80:81]\n" \
  "s_cbranch_vccz LSQ2_%=\n" \
  "s_add_u32 s60, s60, 1\n" \
  "s_cmp_gt_u32 s60, 20000\n" \
  "s_cbranch_scc1 LSQ2_%=\n" \
  "s_sleep 8\n" \
  "s_branch LSP2_%=\n" \
  "LSQ2_%=:\n" \
  "v_lshlrev_b32 v144, 16, v128\n" \
  "v_log_f32 v144, v144\n" \
  "v_lshlrev_b32 v157, 16, v152\n" \
  "v_cndmask_b32 v144, v144, v157, s[80:81]\n" \
  "v_and_b32 v145, 0xffff0000, v128\n" \
  "v_log_f32 v145, v145\n" \
  "v_and_b32 v157, 0xffff0000, v152\n" \
  "v_cndmask_b32 v145, v145, v157, s[80:81]\n" \
  "v_lshlrev_b32 v146, 16, v129\n" \
  "v_log_f32 v146, v146\n" \
  "v_lshlrev_b32 v157, 16, v153\n" \
  "v_cndmask_b32 v146, v146, v157, s[80:81]\n" \
  "v_and_b32 v147, 0xffff0000, v129\n" \
  "v_log_f32 v147, v147\n" \
  "v_and_b32 v157, 0xffff0000, v153\n" \
  "v_cndmask_b32 v147, v147, v157, s[80:81]\n" \
  "v_lshlrev_b32 v148, 16, v130\n" \
  "v_log_f32 v148, v148\n" \
  "v_lshlrev_b32 v157, 16, v154\n" \
  "v_cndmask_b32 v148, v148, v157, s[80:81]\n" \
  "v_and_b32 v149, 0xffff0000, v130\n" \
  "v_log_f32 v149, v149\n" \
  "v_and_b32 v157, 0xffff0000, v154\n" \
  "v_cndmask_b32 v149, v149, v157, s[80:81]\n" \
  "v_lshlrev_b32 v150, 16, v131\n" \
  "v_log_f32 v150, v150\n" \
  "v_lshlrev_b32 v157, 16, v155\n" \
  "v_cndmask_b32 v150, v150, v157, s[80:81]\n" \
  "v_and_b32 v151, 0xffff0000, v131\n" \
  "v_log_f32 v151, v151\n" \
  "v_and_b32 v157, 0xffff0000, v155\n" \
  "v_cndmask_b32 v151, v151, v157, s[80:81]\n" \
  "v_max3_f32 v156, v144, v145, v146\n" \
  "v_max3_f32 v156, v156, v147, v148\n" \
  "v_max3_f32 v156, v156, v149, v150\n" \
  "v_max_f32 v156, v156, v151\n" \
  "s_mov_b64 exec, s[26:27]\n" \
  "v_max_f32 v156, v156, v158\n" \
  "s_mov_b64 exec, -1\n" \
  "v_mov_b32 v157, v156\n" \
  "s_nop 1\n" \
  "v_permlane16_swap_b32 v156, v157\n" \
  "v_max_f32 v156, v156, v157\n" \
  "v_mov_b32 v157, v156\n" \
  "s_nop 1\n" \
  "v_permlane32_swap_b32 v156, v157\n" \
  "v_max_f32 v156, v156, v157\n" \
  "s_mov_b64 exec, s[26:27]\n" \
  "v_mov_b32 v188, v156\n" \
  "s_mov_b64 exec, -1\n" \
  "v_sub_f32 v144, v144, v156\n" \
  "v_exp_f32 v144, v144\n" \
  "v_sub_f32 v145, v145, v156\n" \
  "v_exp_f32 v145, v145\n" \
  "v_sub_f32 v146, v146, v156\n" \
  "v_exp_f32 v146, v146\n" \
  "v_sub_f32 v147, v147, v156\n" \
  "v_exp_f32 v147, v147\n" \
  "v_sub_f32 v148, v148, v156\n" \
  "v_exp_f32 v148, v148\n" \
  "v_sub_f32 v149, v149, v156\n" \
  "v_exp_f32 v149, v149\n" \
  "v_sub_f32 v150, v150, v156\n" \
  "v_exp_f32 v150, v150\n" \
  "v_sub_f32 v151, v151, v156\n" \
  "v_exp_f32 v151, v151\n" \
  "v_cvt_pk_bf16_f32 v128, v144, v145\n" \
  "v_cvt_pk_bf16_f32 v129, v146, v147\n" \
  "v_cvt_pk_bf16_f32 v130, v148, v149\n" \
  "v_cvt_pk_bf16_f32 v131, v150, v151\n" \
  "v_mov_b32 v157, 0x7fd57fd5\n" \
  "v_cmp_eq_u32 s[80:81], v132, v157\n" \
  "LSP3_%=:\n" \
  "buffer_load_dwordx4 v[152:155], v16, s[36:39], s72 offen offset:1536 sc1\n" \
  "s_waitcnt vmcnt(0)\n" \
  "v_max3_u32 v156, v152, v153, v154\n" \
  "v_max_u32 v156, v156, v155\n" \
  "v_cmp_eq_u32 vcc, -1, v156\n" \
  "s_and_b64 vcc, vcc, s[80:81]\n" \
  "s_cbranch_vccz LSQ3_%=\n" \
  "s_add_u32 s60, s60, 1\n" \
  "s_cmp_gt_u32 s60, 20000\n" \
  "s_cbranch_scc1 LSQ3_%=\n" \
  "s_sleep 8\n" \
  "s_branch LSP3_%=\n" \
  "LSQ3_%=:\n" \
  "v_lshlrev_b32 v144, 16, v132\n" \
  "v_log_f32 v144, v144\n" \
  "v_lshlrev_b32 v157, 16, v152\n" \
  "v_cndmask_b32 v144, v144, v157, s[80:81]\n" \
  "v_and_b32 v145, 0xffff0000, v132\n" \
  "v_log_f32 v145, v145\n" \
  "v_and_b32 v157, 0xffff0000, v152\n" \
  "v_cndmask_b32 v145, v145, v157, s[80:81]\n" \
  "v_lshlrev_b32 v146, 16, v133\n" \
  "v_log_f32 v146, v146\n" \
  "v_lshlrev_b32 v157, 16, v153\n" \
  "v_cndmask_b32 v146, v146, v157, s[80:81]\n" \
  "v_and_b32 v147, 0xffff0000, v133\n" \
  "v_log_f32 v147, v147\n" \
  "v_and_b32 v157, 0xffff0000, v153\n" \
  "v_cndmask_b32 v147, v147, v157, s[80:81]\n" \
  "v_lshlrev_b32 v148, 16, v134\n" \
  "v_log_f32 v148, v148\n" \
  "v_lshlrev_b32 v157, 16, v154\n" \
  "v_cndmask_b32 v148, v148, v157, s[80:81]\n" \
  "v_and_b32 v149, 0xffff0000, v134\n" \
  "v_log_f32 v149, v149\n" \
  "v_and_b32 v157, 0xffff0000, v154\n" \
  "v_cndmask_b32 v149, v149, v157, s[80:81]\n" \
  "v_lshlrev_b32 v150, 16, v135\n" \
  "v_log_f32 v150, v150\n" \
  "v_lshlrev_b32 v157, 16, v155\n" \
  "v_cndmask_b32 v150, v150, v157, s[80:81]\n" \
  "v_and_b32 v151, 0xffff0000, v135\n" \
  "v_log_f32 v151, v151\n" \
  "v_and_b32 v157, 0xffff0000, v155\n" \
  "v_cndmask_b32 v151, v151, v157, s[80:81]\n" \
  "v_max3_f32 v156, v144, v145, v146\n" \
  "v_max3_f32 v156, v156, v147, v148\n" \
  "v_max3_f32 v156, v156, v149, v150\n" \
  "v_max_f32 v156, v156, v151\n" \
  "s_mov_b64 exec, s[24:25]\n" \
  "v_max_f32 v156, v156, v158\n" \
  "s_mov_b64 exec, -1\n" \
  "v_mov_b32 v157, v156\n" \
  "s_nop 1\n" \
  "v_permlane16_swap_b32 v156, v157\n" \
  "v_max_f32 v156, v156, v157\n" \
  "v_mov_b32 v157, v156\n" \
  "s_nop 1\n" \
  "v_permlane32_swap_b32 v156, v157\n" \
  "v_max_f32 v156, v156, v157\n" \
  "s_mov_b64 exec, s[24:25]\n" \
  "v_mov_b32 v188, v156\n" \
  "s_mov_b64 exec, -1\n" \
  "v_sub_f32 v144, v144, v156\n" \
  "v_exp_f32 v144, v144\n" \
  "v_sub_f32 v145, v145, v156\n" \
  "v_exp_f32 v145, v145\n" \
  "v_sub_f32 v146, v146, v156\n" \
  "v_exp_f32 v146, v146\n" \
  "v_sub_f32 v147, v147, v156\n" \
  "v_exp_f32 v147, v147\n" \
  "v_sub_f32 v148, v148, v156\n" \
  "v_exp_f32 v148, v148\n" \
  "v_sub_f32 v149, v149, v156\n" \
  "v_exp_f32 v149, v149\n" \
  "v_sub_f32 v150, v150, v156\n" \
  "v_exp_f32 v150, v150\n" \
  "v_sub_f32 v151, v151, v156\n" \
  "v_exp_f32 v151, v151\n" \
  "v_cvt_pk_bf16_f32 v132, v144, v145\n" \
  "v_cvt_pk_bf16_f32 v133, v146, v147\n" \
  "v_cvt_pk_bf16_f32 v134, v148, v149\n" \
  "v_cvt_pk_bf16_f32 v135, v150, v151\n" \
  "v_sub_f32 v176, v176, v188\n" \
  "v_exp_f32 v176, v176\n" \
  "v_sub_f32 v177, v177, v188\n" \
  "v_exp_f32 v177, v177\n" \
  "v_sub_f32 v178, v178, v188\n" \
  "v_exp_f32 v178, v178\n" \
  "v_sub_f32 v179, v179, v188\n" \
  "v_exp_f32 v179, v179\n" \
  "v_sub_f32 v180, v180, v188\n" \
  "v_exp_f32 v180, v180\n" \
  "v_sub_f32 v181, v181, v188\n" \
  "v_exp_f32 v181, v181\n" \
  "v_sub_f32 v182, v182, v188\n" \
  "v_exp_f32 v182, v182\n" \
  "v_sub_f32 v183, v183, v188\n" \
  "v_exp_f32 v183, v183\n" \
  "v_cvt_pk_bf16_f32 v184, v176, v177\n" \
  "v_cvt_pk_bf16_f32 v185, v178, v179\n" \
  "v_cvt_pk_bf16_f32 v186, v180, v181\n" \
  "v_cvt_pk_bf16_f32 v187, v182, v183\n" \
  "v_mfma_f32_16x16x32_bf16 v[144:147], v[40:43], v[120:123], 0\n" \
  "v_mfma_f32_16x16x32_bf16 v[160:163], v[56:59], v[120:123], 0\n" \
  "v_mfma_f32_16x16x32_bf16 v[160:163], v[72:75], v[184:187], v[160:163]\n" \
  "v_mfma_f32_16x16x32_bf16 v[148:151], v[44:47], v[124:127], 0\n" \
  "v_mfma_f32_16x16x32_bf16 v[164:167], v[60:63], v[124:127], 0\n" \
  "v_mfma_f32_16x16x32_bf16 v[164:167], v[76:79], v[184:187], v[164:167]\n" \
  "v_mfma_f32_16x16x32_bf16 v[152:155], v[48:51], v[128:131], 0\n" \
  "v_mfma_f32_16x16x32_bf16 v[168:171], v[64:67], v[128:131], 0\n" \
  "v_mfma_f32_16x16x32_bf16 v[168:171], v[80:83], v[184:187], v[168:171]\n" \
  "v_mfma_f32_16x16x32_bf16 v[156:159], v[52:55], v[132:135], 0\n" \
  "v_mfma_f32_16x16x32_bf16 v[172:175], v[68:71], v[132:135], 0\n" \
  "v_mfma_f32_16x16x32_bf16 v[172:175], v[84:87], v[184:187], v[172:175]\n" \
  "v_rcp_f32 v188, v162\n" \
  "s_nop 0\n" \
  "v_pk_mul_f32 v[144:145], v[144:145], v[188:189] op_sel_hi:[1,0]\n" \
  "v_pk_mul_f32 v[146:147], v[146:147], v[188:189] op_sel_hi:[1,0]\n" \
  "v_pk_mul_f32 v[160:161], v[160:161], v[188:189] op_sel_hi:[1,0]\n" \
  "v_rcp_f32 v189, v166\n" \
  "s_nop 0\n" \
  "v_cndmask_b32 v167, v188, v189, s[28:29]\n" \
  "v_pk_fma_f32 v[144:145], v[148:149], v[188:189], v[144:145] op_sel:[0,1,0] op_sel_hi:[1,1,1]\n" \
  "v_pk_fma_f32 v[146:147], v[150:151], v[188:189], v[146:147] op_sel:[0,1,0] op_sel_hi:[1,1,1]\n" \
  "v_pk_fma_f32 v[160:161], v[164:165], v[188:189], v[160:161] op_sel:[0,1,0] op_sel_hi:[1,1,1]\n" \
  "v_rcp_f32 v190, v170\n" \
  "s_nop 0\n" \
  "v_cndmask_b32 v167, v167, v190, s[26:27]\n" \
  "v_pk_fma_f32 v[144:145], v[152:153], v[190:191], v[144:145] op_sel:[0,0,0] op_sel_hi:[1,0,1]\n" \
  "v_pk_fma_f32 v[146:147], v[154:155], v[190:191], v[146:147] op_sel:[0,0,0] op_sel_hi:[1,0,1]\n" \
  "v_pk_fma_f32 v[160:161], v[168:169], v[190:191], v[160:161] op_sel:[0,0,0] op_sel_hi:[1,0,1]\n" \
  "v_rcp_f32 v191, v174\n" \
  "s_nop 0\n" \
  "v_cndmask_b32 v167, v167, v191, s[24:25]\n" \
  "v_pk_fma_f32 v[144:145], v[156:157], v[190:191], v[144:145] op_sel:[0,1,0] op_sel_hi:[1,1,1]\n" \
  "v_pk_fma_f32 v[146:147], v[158:159], v[190:191], v[146:147] op_sel:[0,1,0] op_sel_hi:[1,1,1]\n" \
  "v_pk_fma_f32 v[160:161], v[172:173], v[190:191], v[160:161] op_sel:[0,1,0] op_sel_hi:[1,1,1]\n" \
  "v_pk_mul_f32 v[176:177], v[176:177], v[166:167] op_sel:[0,1] op_sel_hi:[1,1]\n" \
  "v_pk_mul_f32 v[178:179], v[178:179], v[166:167] op_sel:[0,1] op_sel_hi:[1,1]\n" \
  "v_pk_mul_f32 v[180:181], v[180:181], v[166:167] op_sel:[0,1] op_sel_hi:[1,1]\n" \
  "v_pk_mul_f32 v[182:183], v[182:183], v[166:167] op_sel:[0,1] op_sel_hi:[1,1]\n" \
  "v_cvt_pk_bf16_f32 v184, v176, v177\n" \
  "v_cvt_pk_bf16_f32 v185, v178, v179\n" \
  "v_cvt_pk_bf16_f32 v186, v180, v181\n" \
  "v_cvt_pk_bf16_f32 v187, v182, v183\n" \
  "s_nop 1\n" \
  "v_mfma_f32_16x16x32_bf16 v[144:147], v[136:139], v[184:187], v[144:147]\n" \
  "v_mfma_f32_16x16x32_bf16 v[160:163], v[140:143], v[184:187], v[160:163]\n" \
  "s_mov_b64 exec, s[30:31]\n" \
  "s_nop 6\n" \
  "ds_write_b64 v22, v[160:161]\n" \
  "ds_write2_b64 v21, v[144:145], v[146:147] offset1:16\n" \
  "s_add_u32 s78, s78, 4\n" \
  "s_addc_u32 s79, s79, 0\n" \
  "s_setpc_b64 s[78:79]\n" \
  "LSTART20_%=:\n" \
  "LOOP20_%=:\n" \
  "v_mov_b32 v80, 0\n" \
  "v_mov_b32 v81, 0\n" \
  "v_mov_b32 v82, 0\n" \
  "v_mov_b32 v83, 0\n" \
  "v_mov_b32 v84, 0\n" \
  "v_mov_b32 v85, 0\n" \
  "v_mov_b32 v86, 0\n" \
  "v_mov_b32 v87, 0\n" \
  "v_mov_b32 v88, 0\n" \
  "v_mov_b32 v89, 0\n" \
  "v_mov_b32 v90, 0\n" \
  "v_mov_b32 v91, 0\n" \
  "v_mov_b32 v16, 0\n" \
  "v_mov_b32 v17, 0\n" \
  "v_mov_b32 v18, 0\n" \
  "v_mov_b32 v19, 0\n" \
  "v_mov_b32 v20, 0\n" \
  "v_mov_b32 v21, 0\n" \
  "v_mov_b32 v22, 0\n" \
  "v_mov_b32 v23, 0\n" \
  "v_mov_b32 v24, 0\n" \
  "v_mov_b32 v25, 0\n" \
  "v_mov_b32 v26, 0\n" \
  "v_mov_b32 v27, 0\n" \
  "v_mov_b32 v104, 0\n" \
  "v_mov_b32 v105, 0\n" \
  "v_mov_b32 v106, 0\n" \
  "v_mov_b32 v107, 0\n" \
  "s_mov_b32 s65, s66\n" \
  "s_mov_b32 s69, s68\n" \
  "s_add_u32 s64, s64, 1\n" \
  "s_barrier\n" \
  "v_readlane_b32 s66, v28, s64\n" \
  "v_readlane_b32 s68, v35, s64\n" \
  "s_add_u32 s40, s65, 131072\n" \
  "s_add_u32 s41, s65, 262144\n" \
  "s_add_u32 s42, s65, 393216\n" \
  "s_waitcnt lgkmcnt(0)\n" \
  "LM20_%=:\n" \
  "s_barrier\n" \
  "ds_read_b128 v[40:43], v5\n" \
  "s_barrier\n" \
  "ds_read_b128 v[44:47], v5 offset:1024\n" \
  "s_waitcnt lgkmcnt(1)\n" \
  "v_smfmac_f32_16x16x64_bf16 v[80:83], v[40:43], a[0:7], v0\n" \
  "v_smfmac_f32_16x16x64_bf16 v[84:87], v[40:43], a[80:87], v0\n" \
  "s_barrier\n" \
  "ds_read_b128 v[48:51], v5 offset:2048\n" \
  "v_smfmac_f32_16x16x64_bf16 v[88:91], v[40:43], a[160:167], v0\n" \
  "s_waitcnt lgkmcnt(1)\n" \
  "ds_read_b128 v[52:55], v5 offset:3072\n" \
  "v_smfmac_f32_16x16x64_bf16 v[80:83], v[44:47], a[8:15], v0\n" \
  "v_smfmac_f32_16x16x64_bf16 v[84:87], v[44:47], a[88:95], v0\n" \
  "v_smfmac_f32_16x16x64_bf16 v[88:91], v[44:47], a[168:175], v0\n" \
  "s_barrier\n" \
  "s_waitcnt lgkmcnt(1)\n" \
  "ds_read_b128 v[56:59], v5 offset:4096\n" \
  "v_smfmac_f32_16x16x64_bf16 v[80:83], v[48:51], a[16:23], v0\n" \
  "ds_read_b128 v[60:63], v5 offset:5120\n" \
  "v_smfmac_f32_16x16x64_bf16 v[84:87], v[48:51], a[96:103], v0\n" \
  "v_smfmac_f32_16x16x64_bf16 v[88:91], v[48:51], a[176:183], v0\n" \
  "s_waitcnt lgkmcnt(2)\n" \
  "v_smfmac_f32_16x16x64_bf16 v[80:83], v[52:55], a[24:31], v0\n" \
  "s_barrier\n" \
  "ds_read_b128 v[64:67], v5 offset:6144\n" \
  "v_smfmac_f32_16x16x64_bf16 v[84:87], v[52:55], a[104:111], v0\n" \
  "s_waitcnt lgkmcnt(2)\n" \
  "ds_read_b128 v[68:71], v5 offset:7168\n" \
  "v_smfmac_f32_16x16x64_bf16 v[80:83], v[56:59], a[32:39], v0\n" \
  "v_smfmac_f32_16x16x64_bf16 v[84:87], v[56:59], a[112:119], v0\n" \
  "s_waitcnt lgkmcnt(2)\n" \
  "v_smfmac_f32_16x16x64_bf16 v[80:83], v[60:63], a[40:47], v0\n" \
  "s_barrier\n" \
  "ds_read2_b32 v[72:73], v6 offset0:0 offset1:32\n" \
  "ds_read2_b32 v[74:75], v6 offset0:64 offset1:96\n" \
  "v_smfmac_f32_16x16x64_bf16 v[84:87], v[60:63], a[120:127], v0\n" \
  "s_waitcnt lgkmcnt(3)\n" \
  "ds_read2_b32 v[76:77], v7 offset0:0 offset1:32\n" \
  "ds_read2_b32 v[78:79], v7 offset0:64 offset1:96\n" \
  "v_smfmac_f32_16x16x64_bf16 v[80:83], v[64:67], a[48:55], v0\n" \
  "v_smfmac_f32_16x16x64_bf16 v[84:87], v[64:67], a[128:135], v0\n" \
  "s_waitcnt lgkmcnt(4)\n" \
  "v_smfmac_f32_16x16x64_bf16 v[80:83], v[68:71], a[56:63], v0\n" \
  "v_smfmac_f32_16x16x64_bf16 v[84:87], v[68:71], a[136:143], v0\n" \
  "s_waitcnt lgkmcnt(2)\n" \
  "v_smfmac_f32_16x16x64_bf16 v[80:83], v[72:75], a[64:71], v0\n" \
  "v_smfmac_f32_16x16x64_bf16 v[84:87], v[72:75], a[144:151], v0\n" \
  "s_waitcnt lgkmcnt(0)\n" \
  "ds_read_b128 v[40:43], v11 offset:8192\n" \
  "v_smfmac_f32_16x16x64_bf16 v[80:83], v[76:79], a[72:79], v0\n" \
  "ds_read_b128 v[44:47], v11 offset:9216\n" \
  "v_smfmac_f32_16x16x64_bf16 v[84:87], v[76:79], a[152:159], v0\n" \
  "v_smfmac_f32_16x16x64_bf16 v[88:91], v[52:55], a[184:191], v0\n" \
  "ds_read_b128 v[48:51], v11 offset:49152\n" \
  "v_smfmac_f32_16x16x64_bf16 v[88:91], v[56:59], a[192:199], v0\n" \
  "ds_read_b128 v[52:55], v11 offset:50176\n" \
  "v_permlane32_swap_b32 v80, v84\n" \
  "v_smfmac_f32_16x16x64_bf16 v[88:91], v[60:63], a[200:207], v0\n" \
  "ds_read_b128 v[56:59], v11 offset:0\n" \
  "v_permlane32_swap_b32 v81, v85\n" \
  "v_smfmac_f32_16x16x64_bf16 v[88:91], v[64:67], a[208:215], v0\n" \
  "ds_read_b128 v[60:63], v11 offset:1024\n" \
  "v_permlane32_swap_b32 v82, v86\n" \
  "v_smfmac_f32_16x16x64_bf16 v[88:91], v[68:71], a[216:223], v0\n" \
  "ds_read_b128 v[64:67], v11 offset:40960\n" \
  "v_permlane32_swap_b32 v83, v87\n" \
  "v_smfmac_f32_16x16x64_bf16 v[88:91], v[72:75], a[224:231], v0\n" \
  "ds_read_b128 v[68:71], v11 offset:41984\n" \
  "v_add_f32 v108, v80, v84\n" \
  "v_smfmac_f32_16x16x64_bf16 v[88:91], v[76:79], a[232:239], v0\n" \
  "s_nop 1\n" \
  "v_add_f32 v109, v81, v85\n" \
  "v_add_f32 v110, v82, v86\n" \
  "v_add_f32 v111, v83, v87\n" \
  "v_cvt_pk_bf16_f32 v32, v108, v109\n" \
  "v_cvt_pk_bf16_f32 v33, v110, v111\n" \
  "s_nop 0\n" \
  "v_permlane32_swap_b32 v88, v90\n" \
  "v_permlane32_swap_b32 v89, v91\n" \
  "v_pk_add_f32 v[80:81], v[88:89], v[90:91]\n" \
  "v_cvt_pk_bf16_f32 v12, v80, v81\n" \
  "v_cmp_u_f32 vcc, v80, v81\n" \
  "ds_write_b64 v8, v[32:33]\n" \
  "ds_write_b32 v9, v12\n" \
  "s_cbranch_vccnz LMF20_%=\n" \
  "LMOK20_%=:\n" \
  "s_waitcnt lgkmcnt(0)\n" \
  "s_barrier\n" \
  "ds_read_b64_tr_b16 v[80:81], v10\n" \
  "ds_read_b64_tr_b16 v[82:83], v10 offset:32\n" \
  "ds_read_b64_tr_b16 v[84:85], v10 offset:512\n" \
  "ds_read_b64_tr_b16 v[86:87], v10 offset:544\n" \
  "ds_read_b64_tr_b16 v[88:89], v10 offset:1024\n" \
  "ds_read_b64_tr_b16 v[90:91], v10 offset:1056\n" \
  "ds_read_b64_tr_b16 v[92:93], v10 offset:1536\n" \
  "ds_read_b64_tr_b16 v[94:95], v10 offset:1568\n" \
  "ds_read_b64_tr_b16 v[36:37], v10 offset:2048\n" \
  "ds_read_b64_tr_b16 v[38:39], v10 offset:2080\n" \
  "ds_read_b64_tr_b16 v[100:101], v10 offset:2560\n" \
  "ds_read_b64_tr_b16 v[102:103], v10 offset:2592\n" \
  "s_waitcnt lgkmcnt(10)\n" \
  "v_smfmac_f32_16x16x64_bf16 v[16:19], v[80:83], v[240:247], v0\n" \
  "s_waitcnt lgkmcnt(8)\n" \
  "v_smfmac_f32_16x16x64_bf16 v[16:19], v[84:87], v[248:255], v0\n" \
  "s_waitcnt lgkmcnt(6)\n" \
  "v_smfmac_f32_16x16x64_bf16 v[16:19], v[88:91], a[240:247], v0\n" \
  "s_waitcnt lgkmcnt(4)\n" \
  "ds_read_b128 v[72:75], v11 offset:2048\n" \
  "v_smfmac_f32_16x16x64_bf16 v[16:19], v[92:95], a[248:255], v0\n" \
  "s_waitcnt lgkmcnt(3)\n" \
  "ds_read_b128 v[76:79], v11 offset:3072\n" \
  "v_smfmac_f32_16x16x64_bf16 v[16:19], v[36:39], v[40:47], v0\n" \
  "s_waitcnt lgkmcnt(2)\n" \
  "ds_read_b128 v[40:43], v11 offset:43008\n" \
  "v_smfmac_f32_16x16x64_bf16 v[16:19], v[100:103], v[48:55], v0\n" \
  "ds_read_b128 v[44:47], v11 offset:44032\n" \
  "v_smfmac_f32_16x16x64_bf16 v[20:23], v[80:83], v[112:119], v0\n" \
  "ds_read_b128 v[48:51], v11 offset:4096\n" \
  "v_smfmac_f32_16x16x64_bf16 v[20:23], v[84:87], v[120:127], v0\n" \
  "ds_read_b128 v[52:55], v11 offset:5120\n" \
  "v_cvt_pk_bf16_f32 v108, v16, v17\n" \
  "v_smfmac_f32_16x16x64_bf16 v[20:23], v[88:91], v[128:135], v0\n" \
  "v_cvt_pk_bf16_f32 v109, v18, v19\n" \
  "v_smfmac_f32_16x16x64_bf16 v[20:23], v[92:95], v[136:143], v0\n" \
  "buffer_store_dwordx2 v[108:109], v4, s[36:39], s65 offen sc1\n" \
  "v_smfmac_f32_16x16x64_bf16 v[20:23], v[36:39], v[56:63], v0\n" \
  "ds_read_b128 v[56:59], v11 offset:45056\n" \
  "v_smfmac_f32_16x16x64_bf16 v[20:23], v[100:103], v[64:71], v0\n" \
  "ds_read_b128 v[60:63], v11 offset:46080\n" \
  "v_smfmac_f32_16x16x64_bf16 v[104:107], v[80:83], v[144:151], v0\n" \
  "ds_read_b128 v[64:67], v11 offset:6144\n" \
  "v_smfmac_f32_16x16x64_bf16 v[104:107], v[84:87], v[152:159], v0\n" \
  "ds_read_b128 v[68:71], v11 offset:7168\n" \
  "v_cvt_pk_bf16_f32 v110, v20, v21\n" \
  "v_smfmac_f32_16x16x64_bf16 v[104:107], v[88:91], v[160:167], v0\n" \
  "v_cvt_pk_bf16_f32 v111, v22, v23\n" \
  "v_smfmac_f32_16x16x64_bf16 v[104:107], v[92:95], v[168:175], v0\n" \
  "buffer_store_dwordx2 v[110:111], v13, s[36:39], s65 offen sc1\n" \
  "v_mov_b32 v16, 0\n" \
  "s_waitcnt lgkmcnt(8)\n" \
  "v_smfmac_f32_16x16x64_bf16 v[104:107], v[36:39], v[72:79], v0\n" \
  "s_waitcnt lgkmcnt(6)\n" \
  "ds_read_b128 v[72:75], v11 offset:47104\n" \
  "v_mov_b32 v17, 0\n" \
  "v_smfmac_f32_16x16x64_bf16 v[104:107], v[100:103], v[40:47], v0\n" \
  "ds_read_b128 v[76:79], v11 offset:48128\n" \
  "v_mov_b32 v18, 0\n" \
  "v_smfmac_f32_16x16x64_bf16 v[24:27], v[80:83], v[176:183], v0\n" \
  "v_mov_b32 v19, 0\n" \
  "v_smfmac_f32_16x16x64_bf16 v[24:27], v[84:87], v[184:191], v0\n" \
  "v_cvt_pk_bf16_f32 v108, v104, v105\n" \
  "v_smfmac_f32_16x16x64_bf16 v[24:27], v[88:91], v[192:199], v0\n" \
  "v_cvt_pk_bf16_f32 v109, v106, v107\n" \
  "v_smfmac_f32_16x16x64_bf16 v[24:27], v[92:95], v[200:207], v0\n" \
  "buffer_store_dwordx2 v[108:109], v13, s[36:39], s40 offen sc1\n" \
  "s_waitcnt lgkmcnt(6)\n" \
  "v_smfmac_f32_16x16x64_bf16 v[24:27], v[36:39], v[48:55], v0\n" \
  "s_waitcnt lgkmcnt(4)\n" \
  "v_smfmac_f32_16x16x64_bf16 v[24:27], v[100:103], v[56:63], v0\n" \
  "v_smfmac_f32_16x16x64_bf16 v[16:19], v[80:83], v[208:215], v0\n" \
  "v_smfmac_f32_16x16x64_bf16 v[16:19], v[84:87], v[216:223], v0\n" \
  "v_cvt_pk_bf16_f32 v110, v24, v25\n" \
  "v_smfmac_f32_16x16x64_bf16 v[16:19], v[88:91], v[224:231], v0\n" \
  "v_cvt_pk_bf16_f32 v111, v26, v27\n" \
  "v_smfmac_f32_16x16x64_bf16 v[16:19], v[92:95], v[232:239], v0\n" \
  "buffer_store_dwordx2 v[110:111], v13, s[36:39], s41 offen sc1\n" \
  "s_waitcnt lgkmcnt(2)\n" \
  "v_smfmac_f32_16x16x64_bf16 v[16:19], v[36:39], v[64:71], v0\n" \
  "s_waitcnt lgkmcnt(0)\n" \
  "v_smfmac_f32_16x16x64_bf16 v[16:19], v[100:103], v[72:79], v0\n" \
  "s_nop 3\n" \
  "v_mov_b32 v31, v31\n" \
  "s_nop 2\n" \
  "v_cvt_pk_bf16_f32 v108, v16, v17\n" \
  "v_cvt_pk_bf16_f32 v109, v18, v19\n" \
  "buffer_store_dwordx2 v[108:109], v13, s[36:39], s42 offen sc1\n" \
  "s_mov_b32 s67, s66\n" \
  "s_cmp_eq_u32 s64, 64\n" \
  "s_cbranch_scc1 LDONE_%=\n" \
  "s_waitcnt vmcnt(0)\n" \
  "s_branch LOOP20_%=\n" \
  "LMF20_%=:\n" \
  "v_mbcnt_lo_u32_b32 v94, -1, 0\n" \
  "v_mbcnt_hi_u32_b32 v94, -1, v94\n" \
  "v_lshlrev_b32 v94, 4, v94\n" \
  "v_mov_b32 v92, 0\n" \
  "ds_read_b128 v[72:75], v94 offset:0\n" \
  "ds_read_b128 v[76:79], v94 offset:1024\n" \
  "ds_read_b128 v[80:83], v94 offset:2048\n" \
  "ds_read_b128 v[84:87], v94 offset:3072\n" \
  "ds_read_b128 v[88:91], v94 offset:4096\n" \
  "s_waitcnt lgkmcnt(0)\n" \
  "v_max3_u32 v92, v92, v72, v73\n" \
  "v_max3_u32 v92, v92, v74, v75\n" \
  "v_max3_u32 v92, v92, v76, v77\n" \
  "v_max3_u32 v92, v92, v78, v79\n" \
  "v_max3_u32 v92, v92, v80, v81\n" \
  "v_max3_u32 v92, v92, v82, v83\n" \
  "v_max3_u32 v92, v92, v84, v85\n" \
  "v_max3_u32 v92, v92, v86, v87\n" \
  "v_max3_u32 v92, v92, v88, v89\n" \
  "v_max3_u32 v92, v92, v90, v91\n" \
  "ds_read_b128 v[72:75], v94 offset:5120\n" \
  "ds_read_b128 v[76:79], v94 offset:6144\n" \
  "ds_read_b128 v[80:83], v94 offset:7168\n" \
  "ds_read_b128 v[84:87], v94 offset:8192\n" \
  "ds_read_b128 v[88:91], v94 offset:9216\n" \
  "s_waitcnt lgkmcnt(0)\n" \
  "v_max3_u32 v92, v92, v72, v73\n" \
  "v_max3_u32 v92, v92, v74, v75\n" \
  "v_max3_u32 v92, v92, v76, v77\n" \
  "v_max3_u32 v92, v92, v78, v79\n" \
  "v_max3_u32 v92, v92, v80, v81\n" \
  "v_max3_u32 v92, v92, v82, v83\n" \
  "v_max3_u32 v92, v92, v84, v85\n" \
  "v_max3_u32 v92, v92, v86, v87\n" \
  "v_max3_u32 v92, v92, v88, v89\n" \
  "v_max3_u32 v92, v92, v90, v91\n" \
  "v_cmp_eq_u32 vcc, -1, v92\n" \
  "s_cbranch_vccz LMOK20_%=\n" \
  "s_cmp_gt_u32 s58, 4000\n" \
  "s_cbranch_scc1 LMOK20_%=\n" \
  "s_add_u32 s58, s58, 1\n" \
  "s_sub_u32 s64, s64, 1\n" \
  "s_mov_b32 s66, s65\n" \
  "s_mov_b32 s68, s69\n" \
  "s_waitcnt vmcnt(0) lgkmcnt(0)\n" \
  "s_barrier\n" \
  "s_branch LOOP20_%=\n" \
  "LSTART21_%=:\n" \
  "LOOP21_%=:\n" \
  "v_mov_b32 v80, 0\n" \
  "v_mov_b32 v81, 0\n" \
  "v_mov_b32 v82, 0\n" \
  "v_mov_b32 v83, 0\n" \
  "v_mov_b32 v84, 0\n" \
  "v_mov_b32 v85, 0\n" \
  "v_mov_b32 v86, 0\n" \
  "v_mov_b32 v87, 0\n" \
  "v_mov_b32 v88, 0\n" \
  "v_mov_b32 v89, 0\n" \
  "v_mov_b32 v90, 0\n" \
  "v_mov_b32 v91, 0\n" \
  "v_mov_b32 v16, 0\n" \
  "v_mov_b32 v17, 0\n" \
  "v_mov_b32 v18, 0\n" \
  "v_mov_b32 v19, 0\n" \
  "v_mov_b32 v20, 0\n" \
  "v_mov_b32 v21, 0\n" \
  "v_mov_b32 v22, 0\n" \
  "v_mov_b32 v23, 0\n" \
  "v_mov_b32 v24, 0\n" \
  "v_mov_b32 v25, 0\n" \
  "v_mov_b32 v26, 0\n" \
  "v_mov_b32 v27, 0\n" \
  "v_mov_b32 v104, 0\n" \
  "v_mov_b32 v105, 0\n" \
  "v_mov_b32 v106, 0\n" \
  "v_mov_b32 v107, 0\n" \
  "s_mov_b32 s65, s66\n" \
  "s_mov_b32 s69, s68\n" \
  "s_add_u32 s64, s64, 1\n" \
  "s_barrier\n" \
  "v_readlane_b32 s66, v28, s64\n" \
  "v_readlane_b32 s68, v35, s64\n" \
  "s_add_u32 s40, s65, 131072\n" \
  "s_add_u32 s41, s65, 262144\n" \
  "s_add_u32 s42, s65, 393216\n" \
  "s_waitcnt lgkmcnt(0)\n" \
  "LM21_%=:\n" \
  "s_barrier\n" \
  "ds_read_b128 v[40:43], v5\n" \
  "s_barrier\n" \
  "ds_read_b128 v[44:47], v5 offset:1024\n" \
  "s_waitcnt lgkmcnt(1)\n" \
  "v_smfmac_f32_16x16x64_bf16 v[80:83], v[40:43], a[0:7], v0\n" \
  "v_smfmac_f32_16x16x64_bf16 v[84:87], v[40:43], a[80:87], v0\n" \
  "s_barrier\n" \
  "ds_read_b128 v[48:51], v5 offset:2048\n" \
  "v_smfmac_f32_16x16x64_bf16 v[88:91], v[40:43], a[160:167], v0\n" \
  "s_waitcnt lgkmcnt(1)\n" \
  "ds_read_b128 v[52:55], v5 offset:3072\n" \
  "v_smfmac_f32_16x16x64_bf16 v[80:83], v[44:47], a[8:15], v0\n" \
  "v_smfmac_f32_16x16x64_bf16 v[84:87], v[44:47], a[88:95], v0\n" \
  "v_smfmac_f32_16x16x64_bf16 v[88:91], v[44:47], a[168:175], v0\n" \
  "s_barrier\n" \
  "s_waitcnt lgkmcnt(1)\n" \
  "ds_read_b128 v[56:59], v5 offset:4096\n" \
  "v_smfmac_f32_16x16x64_bf16 v[80:83], v[48:51], a[16:23], v0\n" \
  "ds_read_b128 v[60:63], v5 offset:5120\n" \
  "v_smfmac_f32_16x16x64_bf16 v[84:87], v[48:51], a[96:103], v0\n" \
  "v_smfmac_f32_16x16x64_bf16 v[88:91], v[48:51], a[176:183], v0\n" \
  "s_waitcnt lgkmcnt(2)\n" \
  "v_smfmac_f32_16x16x64_bf16 v[80:83], v[52:55], a[24:31], v0\n" \
  "s_barrier\n" \
  "ds_read_b128 v[64:67], v5 offset:6144\n" \
  "v_smfmac_f32_16x16x64_bf16 v[84:87], v[52:55], a[104:111], v0\n" \
  "s_waitcnt lgkmcnt(2)\n" \
  "ds_read_b128 v[68:71], v5 offset:7168\n" \
  "v_smfmac_f32_16x16x64_bf16 v[80:83], v[56:59], a[32:39], v0\n" \
  "v_smfmac_f32_16x16x64_bf16 v[84:87], v[56:59], a[112:119], v0\n" \
  "s_waitcnt lgkmcnt(2)\n" \
  "v_smfmac_f32_16x16x64_bf16 v[80:83], v[60:63], a[40:47], v0\n" \
  "s_barrier\n" \
  "ds_read2_b32 v[72:73], v6 offset0:0 offset1:32\n" \
  "ds_read2_b32 v[74:75], v6 offset0:64 offset1:96\n" \
  "v_smfmac_f32_16x16x64_bf16 v[84:87], v[60:63], a[120:127], v0\n" \
  "s_waitcnt lgkmcnt(3)\n" \
  "ds_read2_b32 v[76:77], v7 offset0:0 offset1:32\n" \
  "ds_read2_b32 v[78:79], v7 offset0:64 offset1:96\n" \
  "v_smfmac_f32_16x16x64_bf16 v[80:83], v[64:67], a[48:55], v0\n" \
  "v_smfmac_f32_16x16x64_bf16 v[84:87], v[64:67], a[128:135], v0\n" \
  "s_waitcnt lgkmcnt(4)\n" \
  "v_smfmac_f32_16x16x64_bf16 v[80:83], v[68:71], a[56:63], v0\n" \
  "v_smfmac_f32_16x16x64_bf16 v[84:87], v[68:71], a[136:143], v0\n" \
  "s_waitcnt lgkmcnt(2)\n" \
  "v_smfmac_f32_16x16x64_bf16 v[80:83], v[72:75], a[64:71], v0\n" \
  "v_smfmac_f32_16x16x64_bf16 v[84:87], v[72:75], a[144:151], v0\n" \
  "s_waitcnt lgkmcnt(0)\n" \
  "ds_read_b128 v[40:43], v11 offset:8192\n" \
  "v_smfmac_f32_16x16x64_bf16 v[80:83], v[76:79], a[72:79], v0\n" \
  "ds_read_b128 v[44:47], v11 offset:9216\n" \
  "v_smfmac_f32_16x16x64_bf16 v[84:87], v[76:79], a[152:159], v0\n" \
  "v_smfmac_f32_16x16x64_bf16 v[88:91], v[52:55], a[184:191], v0\n" \
  "ds_read_b128 v[48:51], v11 offset:49152\n" \
  "v_smfmac_f32_16x16x64_bf16 v[88:91], v[56:59], a[192:199], v0\n" \
  "ds_read_b128 v[52:55], v11 offset:50176\n" \
  "v_permlane32_swap_b32 v80, v84\n" \
  "v_smfmac_f32_16x16x64_bf16 v[88:91], v[60:63], a[200:207], v0\n" \
  "ds_read_b128 v[56:59], v11 offset:0\n" \
  "v_permlane32_swap_b32 v81, v85\n" \
  "v_smfmac_f32_16x16x64_bf16 v[88:91], v[64:67], a[208:215], v0\n" \
  "ds_read_b128 v[60:63], v11 offset:1024\n" \
  "v_permlane32_swap_b32 v82, v86\n" \
  "v_smfmac_f32_16x16x64_bf16 v[88:91], v[68:71], a[216:223], v0\n" \
  "ds_read_b128 v[64:67], v11 offset:40960\n" \
  "v_permlane32_swap_b32 v83, v87\n" \
  "v_smfmac_f32_16x16x64_bf16 v[88:91], v[72:75], a[224:231], v0\n" \
  "ds_read_b128 v[68:71], v11 offset:41984\n" \
  "v_add_f32 v108, v80, v84\n" \
  "v_smfmac_f32_16x16x64_bf16 v[88:91], v[76:79], a[232:239], v0\n" \
  "s_nop 1\n" \
  "v_add_f32 v109, v81, v85\n" \
  "v_add_f32 v110, v82, v86\n" \
  "v_add_f32 v111, v83, v87\n" \
  "v_cvt_pk_bf16_f32 v32, v108, v109\n" \
  "v_cvt_pk_bf16_f32 v33, v110, v111\n" \
  "s_nop 0\n" \
  "v_permlane32_swap_b32 v88, v90\n" \
  "v_permlane32_swap_b32 v89, v91\n" \
  "v_pk_add_f32 v[80:81], v[88:89], v[90:91]\n" \
  "v_cvt_pk_bf16_f32 v12, v80, v81\n" \
  "v_cmp_u_f32 vcc, v80, v81\n" \
  "ds_write_b64 v8, v[32:33]\n" \
  "ds_write_b32 v9, v12\n" \
  "s_cbranch_vccnz LMF21_%=\n" \
  "LMOK21_%=:\n" \
  "s_waitcnt lgkmcnt(0)\n" \
  "s_barrier\n" \
  "ds_read_b64_tr_b16 v[80:81], v10\n" \
  "ds_read_b64_tr_b16 v[82:83], v10 offset:32\n" \
  "ds_read_b64_tr_b16 v[84:85], v10 offset:512\n" \
  "ds_read_b64_tr_b16 v[86:87], v10 offset:544\n" \
  "ds_read_b64_tr_b16 v[88:89], v10 offset:1024\n" \
  "ds_read_b64_tr_b16 v[90:91], v10 offset:1056\n" \
  "ds_read_b64_tr_b16 v[92:93], v10 offset:1536\n" \
  "ds_read_b64_tr_b16 v[94:95], v10 offset:1568\n" \
  "ds_read_b64_tr_b16 v[36:37], v10 offset:2048\n" \
  "ds_read_b64_tr_b16 v[38:39], v10 offset:2080\n" \
  "ds_read_b64_tr_b16 v[100:101], v10 offset:2560\n" \
  "ds_read_b64_tr_b16 v[102:103], v10 offset:2592\n" \
  "s_waitcnt lgkmcnt(10)\n" \
  "v_smfmac_f32_16x16x64_bf16 v[16:19], v[80:83], v[240:247], v0\n" \
  "s_waitcnt lgkmcnt(8)\n" \
  "v_smfmac_f32_16x16x64_bf16 v[16:19], v[84:87], v[248:255], v0\n" \
  "s_waitcnt lgkmcnt(6)\n" \
  "v_smfmac_f32_16x16x64_bf16 v[16:19], v[88:91], a[240:247], v0\n" \
  "s_waitcnt lgkmcnt(4)\n" \
  "ds_read_b128 v[72:75], v11 offset:2048\n" \
  "v_smfmac_f32_16x16x64_bf16 v[16:19], v[92:95], a[248:255], v0\n" \
  "s_waitcnt lgkmcnt(3)\n" \
  "ds_read_b128 v[76:79], v11 offset:3072\n" \
  "v_smfmac_f32_16x16x64_bf16 v[16:19], v[36:39], v[40:47], v0\n" \
  "s_waitcnt lgkmcnt(2)\n" \
  "ds_read_b128 v[40:43], v11 offset:43008\n" \
  "v_smfmac_f32_16x16x64_bf16 v[16:19], v[100:103], v[48:55], v0\n" \
  "ds_read_b128 v[44:47], v11 offset:44032\n" \
  "v_smfmac_f32_16x16x64_bf16 v[20:23], v[80:83], v[112:119], v0\n" \
  "ds_read_b128 v[48:51], v11 offset:4096\n" \
  "v_smfmac_f32_16x16x64_bf16 v[20:23], v[84:87], v[120:127], v0\n" \
  "ds_read_b128 v[52:55], v11 offset:5120\n" \
  "v_cvt_pk_bf16_f32 v108, v16, v17\n" \
  "v_smfmac_f32_16x16x64_bf16 v[20:23], v[88:91], v[128:135], v0\n" \
  "v_cvt_pk_bf16_f32 v109, v18, v19\n" \
  "v_smfmac_f32_16x16x64_bf16 v[20:23], v[92:95], v[136:143], v0\n" \
  "buffer_store_dwordx2 v[108:109], v4, s[36:39], s65 offen sc1\n" \
  "v_smfmac_f32_16x16x64_bf16 v[20:23], v[36:39], v[56:63], v0\n" \
  "ds_read_b128 v[56:59], v11 offset:45056\n" \
  "v_smfmac_f32_16x16x64_bf16 v[20:23], v[100:103], v[64:71], v0\n" \
  "ds_read_b128 v[60:63], v11 offset:46080\n" \
  "v_smfmac_f32_16x16x64_bf16 v[104:107], v[80:83], v[144:151], v0\n" \
  "ds_read_b128 v[64:67], v11 offset:6144\n" \
  "v_smfmac_f32_16x16x64_bf16 v[104:107], v[84:87], v[152:159], v0\n" \
  "ds_read_b128 v[68:71], v11 offset:7168\n" \
  "v_cvt_pk_bf16_f32 v110, v20, v21\n" \
  "v_smfmac_f32_16x16x64_bf16 v[104:107], v[88:91], v[160:167], v0\n" \
  "v_cvt_pk_bf16_f32 v111, v22, v23\n" \
  "v_smfmac_f32_16x16x64_bf16 v[104:107], v[92:95], v[168:175], v0\n" \
  "buffer_store_dwordx2 v[110:111], v13, s[36:39], s65 offen sc1\n" \
  "v_mov_b32 v16, 0\n" \
  "s_waitcnt lgkmcnt(8)\n" \
  "v_smfmac_f32_16x16x64_bf16 v[104:107], v[36:39], v[72:79], v0\n" \
  "s_waitcnt lgkmcnt(6)\n" \
  "ds_read_b128 v[72:75], v11 offset:47104\n" \
  "v_mov_b32 v17, 0\n" \
  "v_smfmac_f32_16x16x64_bf16 v[104:107], v[100:103], v[40:47], v0\n" \
  "ds_read_b128 v[76:79], v11 offset:48128\n" \
  "v_mov_b32 v18, 0\n" \
  "v_smfmac_f32_16x16x64_bf16 v[24:27], v[80:83], v[176:183], v0\n" \
  "v_mov_b32 v19, 0\n" \
  "v_smfmac_f32_16x16x64_bf16 v[24:27], v[84:87], v[184:191], v0\n" \
  "v_cvt_pk_bf16_f32 v108, v104, v105\n" \
  "v_smfmac_f32_16x16x64_bf16 v[24:27], v[88:91], v[192:199], v0\n" \
  "v_cvt_pk_bf16_f32 v109, v106, v107\n" \
  "v_smfmac_f32_16x16x64_bf16 v[24:27], v[92:95], v[200:207], v0\n" \
  "buffer_store_dwordx2 v[108:109], v13, s[36:39], s40 offen sc1\n" \
  "s_waitcnt lgkmcnt(6)\n" \
  "v_smfmac_f32_16x16x64_bf16 v[24:27], v[36:39], v[48:55], v0\n" \
  "s_waitcnt lgkmcnt(4)\n" \
  "v_smfmac_f32_16x16x64_bf16 v[24:27], v[100:103], v[56:63], v0\n" \
  "v_smfmac_f32_16x16x64_bf16 v[16:19], v[80:83], v[208:215], v0\n" \
  "v_smfmac_f32_16x16x64_bf16 v[16:19], v[84:87], v[216:223], v0\n" \
  "v_cvt_pk_bf16_f32 v110, v24, v25\n" \
  "v_smfmac_f32_16x16x64_bf16 v[16:19], v[88:91], v[224:231], v0\n" \
  "v_cvt_pk_bf16_f32 v111, v26, v27\n" \
  "v_smfmac_f32_16x16x64_bf16 v[16:19], v[92:95], v[232:239], v0\n" \
  "buffer_store_dwordx2 v[110:111], v13, s[36:39], s41 offen sc1\n" \
  "s_waitcnt lgkmcnt(2)\n" \
  "v_smfmac_f32_16x16x64_bf16 v[16:19], v[36:39], v[64:71], v0\n" \
  "s_waitcnt lgkmcnt(0)\n" \
  "v_smfmac_f32_16x16x64_bf16 v[16:19], v[100:103], v[72:79], v0\n" \
  "s_nop 3\n" \
  "v_mov_b32 v31, v31\n" \
  "s_nop 2\n" \
  "v_cvt_pk_bf16_f32 v108, v16, v17\n" \
  "v_cvt_pk_bf16_f32 v109, v18, v19\n" \
  "buffer_store_dwordx2 v[108:109], v13, s[36:39], s42 offen sc1\n" \
  "s_mov_b32 s67, s66\n" \
  "s_cmp_eq_u32 s64, 64\n" \
  "s_cbranch_scc1 LDONE_%=\n" \
  "s_waitcnt vmcnt(0)\n" \
  "s_branch LOOP21_%=\n" \
  "LMF21_%=:\n" \
  "v_mbcnt_lo_u32_b32 v94, -1, 0\n" \
  "v_mbcnt_hi_u32_b32 v94, -1, v94\n" \
  "v_lshlrev_b32 v94, 4, v94\n" \
  "v_mov_b32 v92, 0\n" \
  "ds_read_b128 v[72:75], v94 offset:0\n" \
  "ds_read_b128 v[76:79], v94 offset:1024\n" \
  "ds_read_b128 v[80:83], v94 offset:2048\n" \
  "ds_read_b128 v[84:87], v94 offset:3072\n" \
  "ds_read_b128 v[88:91], v94 offset:4096\n" \
  "s_waitcnt lgkmcnt(0)\n" \
  "v_max3_u32 v92, v92, v72, v73\n" \
  "v_max3_u32 v92, v92, v74, v75\n" \
  "v_max3_u32 v92, v92, v76, v77\n" \
  "v_max3_u32 v92, v92, v78, v79\n" \
  "v_max3_u32 v92, v92, v80, v81\n" \
  "v_max3_u32 v92, v92, v82, v83\n" \
  "v_max3_u32 v92, v92, v84, v85\n" \
  "v_max3_u32 v92, v92, v86, v87\n" \
  "v_max3_u32 v92, v92, v88, v89\n" \
  "v_max3_u32 v92, v92, v90, v91\n" \
  "ds_read_b128 v[72:75], v94 offset:5120\n" \
  "ds_read_b128 v[76:79], v94 offset:6144\n" \
  "ds_read_b128 v[80:83], v94 offset:7168\n" \
  "ds_read_b128 v[84:87], v94 offset:8192\n" \
  "ds_read_b128 v[88:91], v94 offset:9216\n" \
  "s_waitcnt lgkmcnt(0)\n" \
  "v_max3_u32 v92, v92, v72, v73\n" \
  "v_max3_u32 v92, v92, v74, v75\n" \
  "v_max3_u32 v92, v92, v76, v77\n" \
  "v_max3_u32 v92, v92, v78, v79\n" \
  "v_max3_u32 v92, v92, v80, v81\n" \
  "v_max3_u32 v92, v92, v82, v83\n" \
  "v_max3_u32 v92, v92, v84, v85\n" \
  "v_max3_u32 v92, v92, v86, v87\n" \
  "v_max3_u32 v92, v92, v88, v89\n" \
  "v_max3_u32 v92, v92, v90, v91\n" \
  "v_cmp_eq_u32 vcc, -1, v92\n" \
  "s_cbranch_vccz LMOK21_%=\n" \
  "s_cmp_gt_u32 s58, 4000\n" \
  "s_cbranch_scc1 LMOK21_%=\n" \
  "s_add_u32 s58, s58, 1\n" \
  "s_sub_u32 s64, s64, 1\n" \
  "s_mov_b32 s66, s65\n" \
  "s_mov_b32 s68, s69\n" \
  "s_waitcnt vmcnt(0) lgkmcnt(0)\n" \
  "s_barrier\n" \
  "s_branch LOOP21_%=\n" \
  "LSTART22_%=:\n" \
  "s_getpc_b64 s[80:81]\n" \
  "LPC22_%=:\n" \
  "s_add_u32 s82, s80, LEA22_%=-LPC22_%=+24\n" \
  "s_addc_u32 s83, s81, 0\n" \
  "LOOP22_%=:\n" \
  "v_mov_b32 v80, 0\n" \
  "v_mov_b32 v81, 0\n" \
  "v_mov_b32 v82, 0\n" \
  "v_mov_b32 v83, 0\n" \
  "v_mov_b32 v84, 0\n" \
  "v_mov_b32 v85, 0\n" \
  "v_mov_b32 v86, 0\n" \
  "v_mov_b32 v87, 0\n" \
  "v_mov_b32 v88, 0\n" \
  "v_mov_b32 v89, 0\n" \
  "v_mov_b32 v90, 0\n" \
  "v_mov_b32 v91, 0\n" \
  "v_mov_b32 v16, 0\n" \
  "v_mov_b32 v17, 0\n" \
  "v_mov_b32 v18, 0\n" \
  "v_mov_b32 v19, 0\n" \
  "v_mov_b32 v20, 0\n" \
  "v_mov_b32 v21, 0\n" \
  "v_mov_b32 v22, 0\n" \
  "v_mov_b32 v23, 0\n" \
  "v_mov_b32 v24, 0\n" \
  "v_mov_b32 v25, 0\n" \
  "v_mov_b32 v26, 0\n" \
  "v_mov_b32 v27, 0\n" \
  "v_mov_b32 v104, 0\n" \
  "v_mov_b32 v105, 0\n" \
  "v_mov_b32 v106, 0\n" \
  "v_mov_b32 v107, 0\n" \
  "v_readfirstlane_b32 s86, v34\n" \
  "s_mov_b32 m0, 0\n" \
  "LWA22_%=:\n" \
  "s_memrealtime s[74:75]\n" \
  "s_memtime s[76:77]\n" \
  "s_waitcnt lgkmcnt(0)\n" \
  "s_sub_i32 s72, s88, s74\n" \
  "s_mul_i32 s72, s72, s90\n" \
  "s_ashr_i32 s72, s72, 8\n" \
  "s_sub_i32 s71, s89, s76\n" \
  "s_min_i32 s71, s71, s72\n" \
  "s_sub_i32 s72, s72, 92\n" \
  "s_max_i32 s71, s71, s72\n" \
  "s_add_u32 s89, s71, s76\n" \
  "s_cmp_gt_i32 s71, 168\n" \
  "s_cbranch_scc1 LWA22_%=\n" \
  "s_cmp_lt_i32 s71, s94\n" \
  "s_cbranch_scc1 LSA22_%=\n" \
  "s_max_i32 s71, s71, 48\n" \
  "s_lshr_b32 s71, s71, 3\n" \
  "s_lshl_b32 s71, s71, 2\n" \
  "s_sub_u32 s78, s82, s71\n" \
  "s_subb_u32 s79, s83, 0\n" \
  "s_setpc_b64 s[78:79]\n" \
  "s_nop 1\n" \
  "s_nop 1\n" \
  "s_nop 1\n" \
  "s_nop 1\n" \
  "s_nop 1\n" \
  "s_nop 1\n" \
  "s_nop 1\n" \
  "s_nop 1\n" \
  "s_nop 1\n" \
  "s_nop 1\n" \
  "s_nop 1\n" \
  "s_nop 1\n" \
  "s_nop 1\n" \
  "s_nop 1\n" \
  "s_nop 1\n" \
  "s_nop 1\n" \
  "LEA22_%=:\n" \
  "buffer_load_dwordx4 v1, s[36:39], s67 offen sc1 lds\n" \
  "buffer_load_dwordx4 v1, s[36:39], s67 offen offset:1024 sc1 lds\n" \
  "buffer_load_dwordx4 v1, s[36:39], s67 offen offset:2048 sc1 lds\n" \
  "buffer_load_dwordx4 v1, s[36:39], s67 offen offset:3072 sc1 lds\n" \
  "s_mov_b32 m0, 4096\n" \
  "s_nop 0\n" \
  "buffer_load_dwordx4 v2, s[36:39], s67 offen sc1 lds\n" \
  "buffer_load_dwordx4 v2, s[36:39], s67 offen offset:1024 sc1 lds\n" \
  "buffer_load_dwordx4 v2, s[36:39], s67 offen offset:2048 sc1 lds\n" \
  "buffer_load_dwordx4 v2, s[36:39], s67 offen offset:3072 sc1 lds\n" \
  "s_mov_b32 m0, 8192\n" \
  "s_nop 0\n" \
  "buffer_load_dwordx4 v3, s[36:39], s67 offen sc1 lds\n" \
  "buffer_load_dwordx4 v3, s[36:39], s67 offen offset:1024 sc1 lds\n" \
  "s_mov_b32 s65, s66\n" \
  "s_mov_b32 s69, s68\n" \
  "s_add_u32 s64, s64, 1\n" \
  "s_barrier\n" \
  "v_readlane_b32 s66, v28, s64\n" \
  "v_readlane_b32 s68, v35, s64\n" \
  "s_add_u32 s40, s65, 131072\n" \
  "s_add_u32 s41, s65, 262144\n" \
  "s_add_u32 s42, s65, 393216\n" \
  "s_cmp_eq_u32 s34, s64\n" \
  "s_cbranch_scc1 LRT22_%=\n" \
  "s_mov_b32 s34, s64\n" \
  "s_mov_b32 s71, s86\n" \
  "s_max_u32 s19, s19, s71\n" \
  "s_and_b32 s72, s71, 0xff\n" \
  "s_lshr_b32 s71, s71, 8\n" \
  "s_cselect_b32 s71, s71, s61\n" \
  "s_sub_i32 s72, s64, s72\n" \
  "s_sub_i32 s71, s71, s61\n" \
  "s_cmp_ge_i32 s72, 0\n" \
  "s_cselect_b32 s71, s71, 0\n" \
  "s_add_u32 s61, s61, s71\n" \
  "s_mul_i32 s72, s72, s71\n" \
  "s_lshl_b32 s72, s72, 1\n" \
  "s_add_u32 s88, s88, s72\n" \
  "s_mul_i32 s72, s72, s90\n" \
  "s_ashr_i32 s72, s72, 8\n" \
  "s_add_u32 s89, s89, s72\n" \
  "s_lshr_b32 s72, s18, 1\n" \
  "s_sub_u32 s80, s72, s18\n" \
  "s_mov_b32 s18, s72\n" \
  "s_add_u32 s71, s71, s80\n" \
  "s_add_u32 s50, s50, s71\n" \
  "s_mul_i32 s91, s50, s90\n" \
  "s_lshr_b32 s91, s91, 8\n" \
  "s_add_u32 s51, s51, s71\n" \
  "s_mul_i32 s92, s51, s90\n" \
  "s_lshr_b32 s92, s92, 8\n" \
  "LRT22_%=:\n" \
  "s_memrealtime s[74:75]\n" \
  "s_memtime s[76:77]\n" \
  "s_waitcnt lgkmcnt(0)\n" \
  "s_sub_i32 s72, s88, s74\n" \
  "s_mul_i32 s72, s72, s90\n" \
  "s_ashr_i32 s72, s72, 8\n" \
  "s_sub_i32 s71, s89, s76\n" \
  "s_min_i32 s71, s71, s72\n" \
  "s_sub_i32 s72, s72, 92\n" \
  "s_max_i32 s71, s71, s72\n" \
  "s_add_u32 s89, s71, s76\n" \
  "s_waitcnt lgkmcnt(0)\n" \
  "LM22_%=:\n" \
  "s_waitcnt vmcnt(9)\n" \
  "s_barrier\n" \
  "ds_read_b128 v[40:43], v5\n" \
  "s_waitcnt vmcnt(8)\n" \
  "s_barrier\n" \
  "ds_read_b128 v[44:47], v5 offset:1024\n" \
  "s_waitcnt lgkmcnt(1)\n" \
  "v_smfmac_f32_16x16x64_bf16 v[80:83], v[40:43], a[0:7], v0\n" \
  "s_waitcnt vmcnt(6)\n" \
  "v_smfmac_f32_16x16x64_bf16 v[84:87], v[40:43], a[80:87], v0\n" \
  "s_barrier\n" \
  "ds_read_b128 v[48:51], v5 offset:2048\n" \
  "v_smfmac_f32_16x16x64_bf16 v[88:91], v[40:43], a[160:167], v0\n" \
  "s_waitcnt lgkmcnt(1)\n" \
  "ds_read_b128 v[52:55], v5 offset:3072\n" \
  "v_smfmac_f32_16x16x64_bf16 v[80:83], v[44:47], a[8:15], v0\n" \
  "v_smfmac_f32_16x16x64_bf16 v[84:87], v[44:47], a[88:95], v0\n" \
  "s_waitcnt vmcnt(4)\n" \
  "v_smfmac_f32_16x16x64_bf16 v[88:91], v[44:47], a[168:175], v0\n" \
  "s_barrier\n" \
  "s_waitcnt lgkmcnt(1)\n" \
  "ds_read_b128 v[56:59], v5 offset:4096\n" \
  "v_smfmac_f32_16x16x64_bf16 v[80:83], v[48:51], a[16:23], v0\n" \
  "ds_read_b128 v[60:63], v5 offset:5120\n" \
  "v_smfmac_f32_16x16x64_bf16 v[84:87], v[48:51], a[96:103], v0\n" \
  "v_smfmac_f32_16x16x64_bf16 v[88:91], v[48:51], a[176:183], v0\n" \
  "s_waitcnt vmcnt(2)\n" \
  "s_waitcnt lgkmcnt(2)\n" \
  "v_smfmac_f32_16x16x64_bf16 v[80:83], v[52:55], a[24:31], v0\n" \
  "s_barrier\n" \
  "ds_read_b128 v[64:67], v5 offset:6144\n" \
  "v_smfmac_f32_16x16x64_bf16 v[84:87], v[52:55], a[104:111], v0\n" \
  "s_waitcnt lgkmcnt(2)\n" \
  "ds_read_b128 v[68:71], v5 offset:7168\n" \
  "v_smfmac_f32_16x16x64_bf16 v[80:83], v[56:59], a[32:39], v0\n" \
  "v_smfmac_f32_16x16x64_bf16 v[84:87], v[56:59], a[112:119], v0\n" \
  "s_waitcnt vmcnt(0)\n" \
  "s_waitcnt lgkmcnt(2)\n" \
  "v_smfmac_f32_16x16x64_bf16 v[80:83], v[60:63], a[40:47], v0\n" \
  "s_barrier\n" \
  "ds_read2_b32 v[72:73], v6 offset0:0 offset1:32\n" \
  "ds_read2_b32 v[74:75], v6 offset0:64 offset1:96\n" \
  "v_smfmac_f32_16x16x64_bf16 v[84:87], v[60:63], a[120:127], v0\n" \
  "s_waitcnt lgkmcnt(3)\n" \
  "ds_read2_b32 v[76:77], v7 offset0:0 offset1:32\n" \
  "ds_read2_b32 v[78:79], v7 offset0:64 offset1:96\n" \
  "v_smfmac_f32_16x16x64_bf16 v[80:83], v[64:67], a[48:55], v0\n" \
  "v_smfmac_f32_16x16x64_bf16 v[84:87], v[64:67], a[128:135], v0\n" \
  "s_waitcnt lgkmcnt(4)\n" \
  "v_smfmac_f32_16x16x64_bf16 v[80:83], v[68:71], a[56:63], v0\n" \
  "v_smfmac_f32_16x16x64_bf16 v[84:87], v[68:71], a[136:143], v0\n" \
  "s_waitcnt lgkmcnt(2)\n" \
  "v_smfmac_f32_16x16x64_bf16 v[80:83], v[72:75], a[64:71], v0\n" \
  "v_smfmac_f32_16x16x64_bf16 v[84:87], v[72:75], a[144:151], v0\n" \
  "s_waitcnt lgkmcnt(0)\n" \
  "ds_read_b128 v[40:43], v11 offset:8192\n" \
  "v_smfmac_f32_16x16x64_bf16 v[80:83], v[76:79], a[72:79], v0\n" \
  "ds_read_b128 v[44:47], v11 offset:9216\n" \
  "v_smfmac_f32_16x16x64_bf16 v[84:87], v[76:79], a[152:159], v0\n" \
  "v_smfmac_f32_16x16x64_bf16 v[88:91], v[52:55], a[184:191], v0\n" \
  "ds_read_b128 v[48:51], v11 offset:49152\n" \
  "v_smfmac_f32_16x16x64_bf16 v[88:91], v[56:59], a[192:199], v0\n" \
  "ds_read_b128 v[52:55], v11 offset:50176\n" \
  "v_permlane32_swap_b32 v80, v84\n" \
  "v_smfmac_f32_16x16x64_bf16 v[88:91], v[60:63], a[200:207], v0\n" \
  "ds_read_b128 v[56:59], v11 offset:0\n" \
  "v_permlane32_swap_b32 v81, v85\n" \
  "v_smfmac_f32_16x16x64_bf16 v[88:91], v[64:67], a[208:215], v0\n" \
  "ds_read_b128 v[60:63], v11 offset:1024\n" \
  "v_permlane32_swap_b32 v82, v86\n" \
  "v_smfmac_f32_16x16x64_bf16 v[88:91], v[68:71], a[216:223], v0\n" \
  "ds_read_b128 v[64:67], v11 offset:40960\n" \
  "v_permlane32_swap_b32 v83, v87\n" \
  "v_smfmac_f32_16x16x64_bf16 v[88:91], v[72:75], a[224:231], v0\n" \
  "ds_read_b128 v[68:71], v11 offset:41984\n" \
  "v_add_f32 v108, v80, v84\n" \
  "v_smfmac_f32_16x16x64_bf16 v[88:91], v[76:79], a[232:239], v0\n" \
  "s_nop 1\n" \
  "v_add_f32 v109, v81, v85\n" \
  "v_add_f32 v110, v82, v86\n" \
  "v_add_f32 v111, v83, v87\n" \
  "v_cvt_pk_bf16_f32 v32, v108, v109\n" \
  "v_cvt_pk_bf16_f32 v33, v110, v111\n" \
  "s_nop 0\n" \
  "v_permlane32_swap_b32 v88, v90\n" \
  "v_permlane32_swap_b32 v89, v91\n" \
  "v_pk_add_f32 v[80:81], v[88:89], v[90:91]\n" \
  "v_cvt_pk_bf16_f32 v12, v80, v81\n" \
  "v_cmp_u_f32 vcc, v80, v81\n" \
  "ds_write_b64 v8, v[32:33]\n" \
  "ds_write_b32 v9, v12\n" \
  "s_cbranch_vccnz LMF22_%=\n" \
  "LMOK22_%=:\n" \
  "s_waitcnt lgkmcnt(0)\n" \
  "s_barrier\n" \
  "ds_read_b64_tr_b16 v[80:81], v10\n" \
  "ds_read_b64_tr_b16 v[82:83], v10 offset:32\n" \
  "ds_read_b64_tr_b16 v[84:85], v10 offset:512\n" \
  "ds_read_b64_tr_b16 v[86:87], v10 offset:544\n" \
  "ds_read_b64_tr_b16 v[88:89], v10 offset:1024\n" \
  "ds_read_b64_tr_b16 v[90:91], v10 offset:1056\n" \
  "ds_read_b64_tr_b16 v[92:93], v10 offset:1536\n" \
  "ds_read_b64_tr_b16 v[94:95], v10 offset:1568\n" \
  "ds_read_b64_tr_b16 v[36:37], v10 offset:2048\n" \
  "ds_read_b64_tr_b16 v[38:39], v10 offset:2080\n" \
  "ds_read_b64_tr_b16 v[100:101], v10 offset:2560\n" \
  "ds_read_b64_tr_b16 v[102:103], v10 offset:2592\n" \
  "s_waitcnt lgkmcnt(10)\n" \
  "v_smfmac_f32_16x16x64_bf16 v[16:19], v[80:83], v[240:247], v0\n" \
  "s_waitcnt lgkmcnt(8)\n" \
  "v_smfmac_f32_16x16x64_bf16 v[16:19], v[84:87], v[248:255], v0\n" \
  "s_waitcnt lgkmcnt(6)\n" \
  "v_smfmac_f32_16x16x64_bf16 v[16:19], v[88:91], a[240:247], v0\n" \
  "s_waitcnt lgkmcnt(4)\n" \
  "ds_read_b128 v[72:75], v11 offset:2048\n" \
  "v_smfmac_f32_16x16x64_bf16 v[16:19], v[92:95], a[248:255], v0\n" \
  "s_waitcnt lgkmcnt(3)\n" \
  "ds_read_b128 v[76:79], v11 offset:3072\n" \
  "v_smfmac_f32_16x16x64_bf16 v[16:19], v[36:39], v[40:47], v0\n" \
  "s_waitcnt lgkmcnt(2)\n" \
  "ds_read_b128 v[40:43], v11 offset:43008\n" \
  "v_smfmac_f32_16x16x64_bf16 v[16:19], v[100:103], v[48:55], v0\n" \
  "ds_read_b128 v[44:47], v11 offset:44032\n" \
  "v_smfmac_f32_16x16x64_bf16 v[20:23], v[80:83], v[112:119], v0\n" \
  "ds_read_b128 v[48:51], v11 offset:4096\n" \
  "v_smfmac_f32_16x16x64_bf16 v[20:23], v[84:87], v[120:127], v0\n" \
  "ds_read_b128 v[52:55], v11 offset:5120\n" \
  "v_cvt_pk_bf16_f32 v108, v16, v17\n" \
  "v_smfmac_f32_16x16x64_bf16 v[20:23], v[88:91], v[128:135], v0\n" \
  "v_cvt_pk_bf16_f32 v109, v18, v19\n" \
  "v_smfmac_f32_16x16x64_bf16 v[20:23], v[92:95], v[136:143], v0\n" \
  "buffer_store_dwordx2 v[108:109], v4, s[36:39], s65 offen sc1\n" \
  "v_smfmac_f32_16x16x64_bf16 v[20:23], v[36:39], v[56:63], v0\n" \
  "ds_read_b128 v[56:59], v11 offset:45056\n" \
  "v_smfmac_f32_16x16x64_bf16 v[20:23], v[100:103], v[64:71], v0\n" \
  "ds_read_b128 v[60:63], v11 offset:46080\n" \
  "v_smfmac_f32_16x16x64_bf16 v[104:107], v[80:83], v[144:151], v0\n" \
  "ds_read_b128 v[64:67], v11 offset:6144\n" \
  "v_smfmac_f32_16x16x64_bf16 v[104:107], v[84:87], v[152:159], v0\n" \
  "ds_read_b128 v[68:71], v11 offset:7168\n" \
  "v_cvt_pk_bf16_f32 v110, v20, v21\n" \
  "v_smfmac_f32_16x16x64_bf16 v[104:107], v[88:91], v[160:167], v0\n" \
  "v_cvt_pk_bf16_f32 v111, v22, v23\n" \
  "v_smfmac_f32_16x16x64_bf16 v[104:107], v[92:95], v[168:175], v0\n" \
  "buffer_store_dwordx2 v[110:111], v13, s[36:39], s65 offen sc1\n" \
  "v_mov_b32 v16, 0\n" \
  "s_waitcnt lgkmcnt(8)\n" \
  "v_smfmac_f32_16x16x64_bf16 v[104:107], v[36:39], v[72:79], v0\n" \
  "s_waitcnt lgkmcnt(6)\n" \
  "ds_read_b128 v[72:75], v11 offset:47104\n" \
  "v_mov_b32 v17, 0\n" \
  "v_smfmac_f32_16x16x64_bf16 v[104:107], v[100:103], v[40:47], v0\n" \
  "ds_read_b128 v[76:79], v11 offset:48128\n" \
  "v_mov_b32 v18, 0\n" \
  "v_smfmac_f32_16x16x64_bf16 v[24:27], v[80:83], v[176:183], v0\n" \
  "v_mov_b32 v19, 0\n" \
  "v_smfmac_f32_16x16x64_bf16 v[24:27], v[84:87], v[184:191], v0\n" \
  "v_cvt_pk_bf16_f32 v108, v104, v105\n" \
  "v_smfmac_f32_16x16x64_bf16 v[24:27], v[88:91], v[192:199], v0\n" \
  "v_cvt_pk_bf16_f32 v109, v106, v107\n" \
  "v_smfmac_f32_16x16x64_bf16 v[24:27], v[92:95], v[200:207], v0\n" \
  "buffer_store_dwordx2 v[108:109], v13, s[36:39], s40 offen sc1\n" \
  "s_waitcnt lgkmcnt(6)\n" \
  "v_smfmac_f32_16x16x64_bf16 v[24:27], v[36:39], v[48:55], v0\n" \
  "s_waitcnt lgkmcnt(4)\n" \
  "v_smfmac_f32_16x16x64_bf16 v[24:27], v[100:103], v[56:63], v0\n" \
  "v_smfmac_f32_16x16x64_bf16 v[16:19], v[80:83], v[208:215], v0\n" \
  "v_smfmac_f32_16x16x64_bf16 v[16:19], v[84:87], v[216:223], v0\n" \
  "v_cvt_pk_bf16_f32 v110, v24, v25\n" \
  "v_smfmac_f32_16x16x64_bf16 v[16:19], v[88:91], v[224:231], v0\n" \
  "v_cvt_pk_bf16_f32 v111, v26, v27\n" \
  "v_smfmac_f32_16x16x64_bf16 v[16:19], v[92:95], v[232:239], v0\n" \
  "buffer_store_dwordx2 v[110:111], v13, s[36:39], s41 offen sc1\n" \
  "s_waitcnt lgkmcnt(2)\n" \
  "v_smfmac_f32_16x16x64_bf16 v[16:19], v[36:39], v[64:71], v0\n" \
  "s_waitcnt lgkmcnt(0)\n" \
  "v_smfmac_f32_16x16x64_bf16 v[16:19], v[100:103], v[72:79], v0\n" \
  "s_nop 3\n" \
  "v_mov_b32 v31, v31\n" \
  "s_nop 2\n" \
  "v_cvt_pk_bf16_f32 v108, v16, v17\n" \
  "v_cvt_pk_bf16_f32 v109, v18, v19\n" \
  "buffer_store_dwordx2 v[108:109], v13, s[36:39], s42 offen sc1\n" \
  "s_mov_b32 s67, s66\n" \
  "s_cmp_eq_u32 s64, 64\n" \
  "s_cbranch_scc1 LDONE_%=\n" \
  "s_add_u32 s88, s88, s50\n" \
  "s_add_u32 s89, s89, s91\n" \
  "s_add_u32 s88, s88, s51\n" \
  "s_add_u32 s89, s89, s92\n" \
  "s_sub_u32 s72, s74, s95\n" \
  "s_cmp_lt_u32 s64, 3\n" \
  "s_cbranch_scc1 LRB22_%=\n" \
  "s_cmp_gt_u32 s72, 900\n" \
  "s_cbranch_scc0 LRC22_%=\n" \
  "s_sub_u32 s71, s76, s96\n" \
  "s_sub_u32 s72, s74, s95\n" \
  "v_cvt_f32_u32 v110, s71\n" \
  "v_cvt_f32_u32 v111, s72\n" \
  "v_rcp_f32 v111, v111\n" \
  "v_mul_f32 v110, 0x43820000, v110\n" \
  "v_mul_f32 v110, v110, v111\n" \
  "v_cvt_u32_f32 v110, v110\n" \
  "s_nop 0\n" \
  "v_readfirstlane_b32 s71, v110\n" \
  "s_min_u32 s90, s71, 6400\n" \
  "LRB22_%=:\n" \
  "s_mov_b32 s95, s74\n" \
  "s_mov_b32 s96, s76\n" \
  "LRC22_%=:\n" \
  "v_cvt_f32_u32 v110, s90\n" \
  "v_rcp_f32 v110, v110\n" \
  "v_mov_b32 v111, 0xc0b00000\n" \
  "v_fmac_f32 v111, 0x48cc6500, v110\n" \
  "v_cvt_u32_f32 v111, v111\n" \
  "s_cmp_lt_u32 s64, s99\n" \
  "s_cselect_b32 s98, s97, s98\n" \
  "s_cselect_b32 s80, 0, -1\n" \
  "s_cmp_lg_u32 s98, s97\n" \
  "s_addc_u32 s72, s61, 0\n" \
  "s_mov_b32 s98, s97\n" \
  "v_readfirstlane_b32 s71, v111\n" \
  "s_max_u32 s71, s71, s72\n" \
  "s_and_b32 s71, s71, s80\n" \
  "s_add_u32 s72, s64, 2\n" \
  "s_lshl_b32 s80, s71, 8\n" \
  "s_or_b32 s80, s80, s72\n" \
  "s_add_u32 s72, s64, 3\n" \
  "s_cmp_gt_u32 s71, s61\n" \
  "s_cselect_b32 s19, s80, s19\n" \
  "s_cselect_b32 s99, s72, s99\n" \
  "v_mov_b32 v110, s19\n" \
  "s_lshl_b32 s72, s64, 7\n" \
  "buffer_store_dword v110, v30, s[36:39], s72 offen offset:128 sc1\n" \
  "buffer_load_dword v34, v29, s[36:39], s72 offen sc1\n" \
  "s_waitcnt vmcnt(0)\n" \
  "s_branch LOOP22_%=\n" \
  "LSA22_%=:\n" \
  "s_add_u32 s88, s88, s52\n" \
  "s_add_u32 s89, s89, s93\n" \
  "s_add_u32 s97, s97, 1\n" \
  "s_branch LWA22_%=\n" \
  "LMF22_%=:\n" \
  "v_mbcnt_lo_u32_b32 v94, -1, 0\n" \
  "v_mbcnt_hi_u32_b32 v94, -1, v94\n" \
  "v_lshlrev_b32 v94, 4, v94\n" \
  "v_mov_b32 v92, 0\n" \
  "ds_read_b128 v[72:75], v94 offset:0\n" \
  "ds_read_b128 v[76:79], v94 offset:1024\n" \
  "ds_read_b128 v[80:83], v94 offset:2048\n" \
  "ds_read_b128 v[84:87], v94 offset:3072\n" \
  "ds_read_b128 v[88:91], v94 offset:4096\n" \
  "s_waitcnt lgkmcnt(0)\n" \
  "v_max3_u32 v92, v92, v72, v73\n" \
  "v_max3_u32 v92, v92, v74, v75\n" \
  "v_max3_u32 v92, v92, v76, v77\n" \
  "v_max3_u32 v92, v92, v78, v79\n" \
  "v_max3_u32 v92, v92, v80, v81\n" \
  "v_max3_u32 v92, v92, v82, v83\n" \
  "v_max3_u32 v92, v92, v84, v85\n" \
  "v_max3_u32 v92, v92, v86, v87\n" \
  "v_max3_u32 v92, v92, v88, v89\n" \
  "v_max3_u32 v92, v92, v90, v91\n" \
  "ds_read_b128 v[72:75], v94 offset:5120\n" \
  "ds_read_b128 v[76:79], v94 offset:6144\n" \
  "ds_read_b128 v[80:83], v94 offset:7168\n" \
  "ds_read_b128 v[84:87], v94 offset:8192\n" \
  "ds_read_b128 v[88:91], v94 offset:9216\n" \
  "s_waitcnt lgkmcnt(0)\n" \
  "v_max3_u32 v92, v92, v72, v73\n" \
  "v_max3_u32 v92, v92, v74, v75\n" \
  "v_max3_u32 v92, v92, v76, v77\n" \
  "v_max3_u32 v92, v92, v78, v79\n" \
  "v_max3_u32 v92, v92, v80, v81\n" \
  "v_max3_u32 v92, v92, v82, v83\n" \
  "v_max3_u32 v92, v92, v84, v85\n" \
  "v_max3_u32 v92, v92, v86, v87\n" \
  "v_max3_u32 v92, v92, v88, v89\n" \
  "v_max3_u32 v92, v92, v90, v91\n" \
  "v_cmp_eq_u32 vcc, -1, v92\n" \
  "s_cbranch_vccz LMOK22_%=\n" \
  "s_cmp_gt_u32 s58, 4000\n" \
  "s_cbranch_scc1 LMOK22_%=\n" \
  "s_add_u32 s58, s58, 1\n" \
  "s_sub_u32 s64, s64, 1\n" \
  "s_mov_b32 s66, s65\n" \
  "s_mov_b32 s68, s69\n" \
  "s_add_u32 s88, s88, s52\n" \
  "s_add_u32 s89, s89, s93\n" \
  "s_add_u32 s97, s97, 1\n" \
  "s_waitcnt vmcnt(0) lgkmcnt(0)\n" \
  "s_barrier\n" \
  "s_branch LOOP22_%=\n" \
  "LSTART23_%=:\n" \
  "LOOP23_%=:\n" \
  "v_mov_b32 v80, 0\n" \
  "v_mov_b32 v81, 0\n" \
  "v_mov_b32 v82, 0\n" \
  "v_mov_b32 v83, 0\n" \
  "v_mov_b32 v84, 0\n" \
  "v_mov_b32 v85, 0\n" \
  "v_mov_b32 v86, 0\n" \
  "v_mov_b32 v87, 0\n" \
  "v_mov_b32 v88, 0\n" \
  "v_mov_b32 v89, 0\n" \
  "v_mov_b32 v90, 0\n" \
  "v_mov_b32 v91, 0\n" \
  "v_mov_b32 v16, 0\n" \
  "v_mov_b32 v17, 0\n" \
  "v_mov_b32 v18, 0\n" \
  "v_mov_b32 v19, 0\n" \
  "v_mov_b32 v20, 0\n" \
  "v_mov_b32 v21, 0\n" \
  "v_mov_b32 v22, 0\n" \
  "v_mov_b32 v23, 0\n" \
  "v_mov_b32 v24, 0\n" \
  "v_mov_b32 v25, 0\n" \
  "v_mov_b32 v26, 0\n" \
  "v_mov_b32 v27, 0\n" \
  "v_mov_b32 v104, 0\n" \
  "v_mov_b32 v105, 0\n" \
  "v_mov_b32 v106, 0\n" \
  "v_mov_b32 v107, 0\n" \
  "s_mov_b32 s65, s66\n" \
  "s_mov_b32 s69, s68\n" \
  "v_mov_b32 v34, s64\n" \
  "s_add_u32 s64, s64, 1\n" \
  "s_barrier\n" \
  "v_readlane_b32 s66, v28, s64\n" \
  "v_readlane_b32 s68, v35, s64\n" \
  "s_add_u32 s40, s65, 131072\n" \
  "s_add_u32 s41, s65, 262144\n" \
  "s_add_u32 s42, s65, 393216\n" \
  "s_waitcnt lgkmcnt(0)\n" \
  "LM23_%=:\n" \
  "s_barrier\n" \
  "ds_read_b128 v[40:43], v5\n" \
  "s_barrier\n" \
  "ds_read_b128 v[44:47], v5 offset:1024\n" \
  "s_waitcnt lgkmcnt(1)\n" \
  "v_smfmac_f32_16x16x64_bf16 v[80:83], v[40:43], a[0:7], v0\n" \
  "v_smfmac_f32_16x16x64_bf16 v[84:87], v[40:43], a[80:87], v0\n" \
  "s_barrier\n" \
  "ds_read_b128 v[48:51], v5 offset:2048\n" \
  "v_smfmac_f32_16x16x64_bf16 v[88:91], v[40:43], a[160:167], v0\n" \
  "s_waitcnt lgkmcnt(1)\n" \
  "ds_read_b128 v[52:55], v5 offset:3072\n" \
  "v_smfmac_f32_16x16x64_bf16 v[80:83], v[44:47], a[8:15], v0\n" \
  "v_smfmac_f32_16x16x64_bf16 v[84:87], v[44:47], a[88:95], v0\n" \
  "buffer_store_dword v34, v1, s[36:39], 0 offen sc1\n" \
  "v_smfmac_f32_16x16x64_bf16 v[88:91], v[44:47], a[168:175], v0\n" \
  "s_barrier\n" \
  "s_waitcnt lgkmcnt(1)\n" \
  "ds_read_b128 v[56:59], v5 offset:4096\n" \
  "v_smfmac_f32_16x16x64_bf16 v[80:83], v[48:51], a[16:23], v0\n" \
  "ds_read_b128 v[60:63], v5 offset:5120\n" \
  "v_smfmac_f32_16x16x64_bf16 v[84:87], v[48:51], a[96:103], v0\n" \
  "v_smfmac_f32_16x16x64_bf16 v[88:91], v[48:51], a[176:183], v0\n" \
  "s_waitcnt lgkmcnt(2)\n" \
  "v_smfmac_f32_16x16x64_bf16 v[80:83], v[52:55], a[24:31], v0\n" \
  "s_barrier\n" \
  "ds_read_b128 v[64:67], v5 offset:6144\n" \
  "v_smfmac_f32_16x16x64_bf16 v[84:87], v[52:55], a[104:111], v0\n" \
  "s_waitcnt lgkmcnt(2)\n" \
  "ds_read_b128 v[68:71], v5 offset:7168\n" \
  "v_smfmac_f32_16x16x64_bf16 v[80:83], v[56:59], a[32:39], v0\n" \
  "v_smfmac_f32_16x16x64_bf16 v[84:87], v[56:59], a[112:119], v0\n" \
  "s_waitcnt lgkmcnt(2)\n" \
  "v_smfmac_f32_16x16x64_bf16 v[80:83], v[60:63], a[40:47], v0\n" \
  "s_barrier\n" \
  "ds_read2_b32 v[72:73], v6 offset0:0 offset1:32\n" \
  "ds_read2_b32 v[74:75], v6 offset0:64 offset1:96\n" \
  "v_smfmac_f32_16x16x64_bf16 v[84:87], v[60:63], a[120:127], v0\n" \
  "s_waitcnt lgkmcnt(3)\n" \
  "ds_read2_b32 v[76:77], v7 offset0:0 offset1:32\n" \
  "ds_read2_b32 v[78:79], v7 offset0:64 offset1:96\n" \
  "v_smfmac_f32_16x16x64_bf16 v[80:83], v[64:67], a[48:55], v0\n" \
  "v_smfmac_f32_16x16x64_bf16 v[84:87], v[64:67], a[128:135], v0\n" \
  "s_waitcnt lgkmcnt(4)\n" \
  "v_smfmac_f32_16x16x64_bf16 v[80:83], v[68:71], a[56:63], v0\n" \
  "v_smfmac_f32_16x16x64_bf16 v[84:87], v[68:71], a[136:143], v0\n" \
  "s_waitcnt lgkmcnt(2)\n" \
  "v_smfmac_f32_16x16x64_bf16 v[80:83], v[72:75], a[64:71], v0\n" \
  "v_smfmac_f32_16x16x64_bf16 v[84:87], v[72:75], a[144:151], v0\n" \
  "s_waitcnt lgkmcnt(0)\n" \
  "ds_read_b128 v[40:43], v11 offset:8192\n" \
  "v_smfmac_f32_16x16x64_bf16 v[80:83], v[76:79], a[72:79], v0\n" \
  "ds_read_b128 v[44:47], v11 offset:9216\n" \
  "v_smfmac_f32_16x16x64_bf16 v[84:87], v[76:79], a[152:159], v0\n" \
  "v_smfmac_f32_16x16x64_bf16 v[88:91], v[52:55], a[184:191], v0\n" \
  "ds_read_b128 v[48:51], v11 offset:49152\n" \
  "v_smfmac_f32_16x16x64_bf16 v[88:91], v[56:59], a[192:199], v0\n" \
  "ds_read_b128 v[52:55], v11 offset:50176\n" \
  "v_permlane32_swap_b32 v80, v84\n" \
  "v_smfmac_f32_16x16x64_bf16 v[88:91], v[60:63], a[200:207], v0\n" \
  "ds_read_b128 v[56:59], v11 offset:0\n" \
  "v_permlane32_swap_b32 v81, v85\n" \
  "v_smfmac_f32_16x16x64_bf16 v[88:91], v[64:67], a[208:215], v0\n" \
  "ds_read_b128 v[60:63], v11 offset:1024\n" \
  "v_permlane32_swap_b32 v82, v86\n" \
  "v_smfmac_f32_16x16x64_bf16 v[88:91], v[68:71], a[216:223], v0\n" \
  "ds_read_b128 v[64:67], v11 offset:40960\n" \
  "v_permlane32_swap_b32 v83, v87\n" \
  "v_smfmac_f32_16x16x64_bf16 v[88:91], v[72:75], a[224:231], v0\n" \
  "ds_read_b128 v[68:71], v11 offset:41984\n" \
  "v_add_f32 v108, v80, v84\n" \
  "v_smfmac_f32_16x16x64_bf16 v[88:91], v[76:79], a[232:239], v0\n" \
  "s_nop 1\n" \
  "v_add_f32 v109, v81, v85\n" \
  "v_add_f32 v110, v82, v86\n" \
  "v_add_f32 v111, v83, v87\n" \
  "v_cvt_pk_bf16_f32 v32, v108, v109\n" \
  "v_cvt_pk_bf16_f32 v33, v110, v111\n" \
  "s_nop 0\n" \
  "v_permlane32_swap_b32 v88, v90\n" \
  "v_permlane32_swap_b32 v89, v91\n" \
  "v_pk_add_f32 v[80:81], v[88:89], v[90:91]\n" \
  "v_cvt_pk_bf16_f32 v12, v80, v81\n" \
  "v_cmp_u_f32 vcc, v80, v81\n" \
  "ds_write_b64 v8, v[32:33]\n" \
  "ds_write_b32 v9, v12\n" \
  "s_cbranch_vccnz LMF23_%=\n" \
  "LMOK23_%=:\n" \
  "s_waitcnt lgkmcnt(0)\n" \
  "s_barrier\n" \
  "ds_read_b64_tr_b16 v[80:81], v10\n" \
  "ds_read_b64_tr_b16 v[82:83], v10 offset:32\n" \
  "ds_read_b64_tr_b16 v[84:85], v10 offset:512\n" \
  "ds_read_b64_tr_b16 v[86:87], v10 offset:544\n" \
  "ds_read_b64_tr_b16 v[88:89], v10 offset:1024\n" \
  "ds_read_b64_tr_b16 v[90:91], v10 offset:1056\n" \
  "ds_read_b64_tr_b16 v[92:93], v10 offset:1536\n" \
  "ds_read_b64_tr_b16 v[94:95], v10 offset:1568\n" \
  "ds_read_b64_tr_b16 v[36:37], v10 offset:2048\n" \
  "ds_read_b64_tr_b16 v[38:39], v10 offset:2080\n" \
  "ds_read_b64_tr_b16 v[100:101], v10 offset:2560\n" \
  "ds_read_b64_tr_b16 v[102:103], v10 offset:2592\n" \
  "s_waitcnt lgkmcnt(10)\n" \
  "v_smfmac_f32_16x16x64_bf16 v[16:19], v[80:83], v[240:247], v0\n" \
  "s_waitcnt lgkmcnt(8)\n" \
  "v_smfmac_f32_16x16x64_bf16 v[16:19], v[84:87], v[248:255], v0\n" \
  "s_waitcnt lgkmcnt(6)\n" \
  "v_smfmac_f32_16x16x64_bf16 v[16:19], v[88:91], a[240:247], v0\n" \
  "s_waitcnt lgkmcnt(4)\n" \
  "ds_read_b128 v[72:75], v11 offset:2048\n" \
  "v_smfmac_f32_16x16x64_bf16 v[16:19], v[92:95], a[248:255], v0\n" \
  "s_waitcnt lgkmcnt(3)\n" \
  "ds_read_b128 v[76:79], v11 offset:3072\n" \
  "v_smfmac_f32_16x16x64_bf16 v[16:19], v[36:39], v[40:47], v0\n" \
  "s_waitcnt lgkmcnt(2)\n" \
  "ds_read_b128 v[40:43], v11 offset:43008\n" \
  "v_smfmac_f32_16x16x64_bf16 v[16:19], v[100:103], v[48:55], v0\n" \
  "ds_read_b128 v[44:47], v11 offset:44032\n" \
  "v_smfmac_f32_16x16x64_bf16 v[20:23], v[80:83], v[112:119], v0\n" \
  "ds_read_b128 v[48:51], v11 offset:4096\n" \
  "v_smfmac_f32_16x16x64_bf16 v[20:23], v[84:87], v[120:127], v0\n" \
  "ds_read_b128 v[52:55], v11 offset:5120\n" \
  "v_cvt_pk_bf16_f32 v108, v16, v17\n" \
  "v_smfmac_f32_16x16x64_bf16 v[20:23], v[88:91], v[128:135], v0\n" \
  "v_cvt_pk_bf16_f32 v109, v18, v19\n" \
  "v_smfmac_f32_16x16x64_bf16 v[20:23], v[92:95], v[136:143], v0\n" \
  "buffer_store_dwordx2 v[108:109], v4, s[36:39], s65 offen sc1\n" \
  "v_smfmac_f32_16x16x64_bf16 v[20:23], v[36:39], v[56:63], v0\n" \
  "ds_read_b128 v[56:59], v11 offset:45056\n" \
  "v_smfmac_f32_16x16x64_bf16 v[20:23], v[100:103], v[64:71], v0\n" \
  "ds_read_b128 v[60:63], v11 offset:46080\n" \
  "v_smfmac_f32_16x16x64_bf16 v[104:107], v[80:83], v[144:151], v0\n" \
  "ds_read_b128 v[64:67], v11 offset:6144\n" \
  "v_smfmac_f32_16x16x64_bf16 v[104:107], v[84:87], v[152:159], v0\n" \
  "ds_read_b128 v[68:71], v11 offset:7168\n" \
  "v_cvt_pk_bf16_f32 v110, v20, v21\n" \
  "v_smfmac_f32_16x16x64_bf16 v[104:107], v[88:91], v[160:167], v0\n" \
  "v_cvt_pk_bf16_f32 v111, v22, v23\n" \
  "v_smfmac_f32_16x16x64_bf16 v[104:107], v[92:95], v[168:175], v0\n" \
  "buffer_store_dwordx2 v[110:111], v13, s[36:39], s65 offen sc1\n" \
  "v_mov_b32 v16, 0\n" \
  "s_waitcnt lgkmcnt(8)\n" \
  "v_smfmac_f32_16x16x64_bf16 v[104:107], v[36:39], v[72:79], v0\n" \
  "s_waitcnt lgkmcnt(6)\n" \
  "ds_read_b128 v[72:75], v11 offset:47104\n" \
  "v_mov_b32 v17, 0\n" \
  "v_smfmac_f32_16x16x64_bf16 v[104:107], v[100:103], v[40:47], v0\n" \
  "ds_read_b128 v[76:79], v11 offset:48128\n" \
  "v_mov_b32 v18, 0\n" \
  "v_smfmac_f32_16x16x64_bf16 v[24:27], v[80:83], v[176:183], v0\n" \
  "v_mov_b32 v19, 0\n" \
  "v_smfmac_f32_16x16x64_bf16 v[24:27], v[84:87], v[184:191], v0\n" \
  "v_cvt_pk_bf16_f32 v108, v104, v105\n" \
  "v_smfmac_f32_16x16x64_bf16 v[24:27], v[88:91], v[192:199], v0\n" \
  "v_cvt_pk_bf16_f32 v109, v106, v107\n" \
  "v_smfmac_f32_16x16x64_bf16 v[24:27], v[92:95], v[200:207], v0\n" \
  "buffer_store_dwordx2 v[108:109], v13, s[36:39], s40 offen sc1\n" \
  "s_waitcnt lgkmcnt(6)\n" \
  "v_smfmac_f32_16x16x64_bf16 v[24:27], v[36:39], v[48:55], v0\n" \
  "s_waitcnt lgkmcnt(4)\n" \
  "v_smfmac_f32_16x16x64_bf16 v[24:27], v[100:103], v[56:63], v0\n" \
  "v_smfmac_f32_16x16x64_bf16 v[16:19], v[80:83], v[208:215], v0\n" \
  "v_smfmac_f32_16x16x64_bf16 v[16:19], v[84:87], v[216:223], v0\n" \
  "v_cvt_pk_bf16_f32 v110, v24, v25\n" \
  "v_smfmac_f32_16x16x64_bf16 v[16:19], v[88:91], v[224:231], v0\n" \
  "v_cvt_pk_bf16_f32 v111, v26, v27\n" \
  "v_smfmac_f32_16x16x64_bf16 v[16:19], v[92:95], v[232:239], v0\n" \
  "buffer_store_dwordx2 v[110:111], v13, s[36:39], s41 offen sc1\n" \
  "s_waitcnt lgkmcnt(2)\n" \
  "v_smfmac_f32_16x16x64_bf16 v[16:19], v[36:39], v[64:71], v0\n" \
  "s_waitcnt lgkmcnt(0)\n" \
  "v_smfmac_f32_16x16x64_bf16 v[16:19], v[100:103], v[72:79], v0\n" \
  "s_nop 3\n" \
  "v_mov_b32 v31, v31\n" \
  "s_nop 2\n" \
  "v_cvt_pk_bf16_f32 v108, v16, v17\n" \
  "v_cvt_pk_bf16_f32 v109, v18, v19\n" \
  "buffer_store_dwordx2 v[108:109], v13, s[36:39], s42 offen sc1\n" \
  "s_mov_b32 s67, s66\n" \
  "s_cmp_eq_u32 s64, 64\n" \
  "s_cbranch_scc1 LDONE_%=\n" \
  "s_waitcnt vmcnt(0)\n" \
  "s_branch LOOP23_%=\n" \
  "LMF23_%=:\n" \
  "v_mbcnt_lo_u32_b32 v94, -1, 0\n" \
  "v_mbcnt_hi_u32_b32 v94, -1, v94\n" \
  "v_lshlrev_b32 v94, 4, v94\n" \
  "v_mov_b32 v92, 0\n" \
  "ds_read_b128 v[72:75], v94 offset:0\n" \
  "ds_read_b128 v[76:79], v94 offset:1024\n" \
  "ds_read_b128 v[80:83], v94 offset:2048\n" \
  "ds_read_b128 v[84:87], v94 offset:3072\n" \
  "ds_read_b128 v[88:91], v94 offset:4096\n" \
  "s_waitcnt lgkmcnt(0)\n" \
  "v_max3_u32 v92, v92, v72, v73\n" \
  "v_max3_u32 v92, v92, v74, v75\n" \
  "v_max3_u32 v92, v92, v76, v77\n" \
  "v_max3_u32 v92, v92, v78, v79\n" \
  "v_max3_u32 v92, v92, v80, v81\n" \
  "v_max3_u32 v92, v92, v82, v83\n" \
  "v_max3_u32 v92, v92, v84, v85\n" \
  "v_max3_u32 v92, v92, v86, v87\n" \
  "v_max3_u32 v92, v92, v88, v89\n" \
  "v_max3_u32 v92, v92, v90, v91\n" \
  "ds_read_b128 v[72:75], v94 offset:5120\n" \
  "ds_read_b128 v[76:79], v94 offset:6144\n" \
  "ds_read_b128 v[80:83], v94 offset:7168\n" \
  "ds_read_b128 v[84:87], v94 offset:8192\n" \
  "ds_read_b128 v[88:91], v94 offset:9216\n" \
  "s_waitcnt lgkmcnt(0)\n" \
  "v_max3_u32 v92, v92, v72, v73\n" \
  "v_max3_u32 v92, v92, v74, v75\n" \
  "v_max3_u32 v92, v92, v76, v77\n" \
  "v_max3_u32 v92, v92, v78, v79\n" \
  "v_max3_u32 v92, v92, v80, v81\n" \
  "v_max3_u32 v92, v92, v82, v83\n" \
  "v_max3_u32 v92, v92, v84, v85\n" \
  "v_max3_u32 v92, v92, v86, v87\n" \
  "v_max3_u32 v92, v92, v88, v89\n" \
  "v_max3_u32 v92, v92, v90, v91\n" \
  "v_cmp_eq_u32 vcc, -1, v92\n" \
  "s_cbranch_vccz LMOK23_%=\n" \
  "s_cmp_gt_u32 s58, 4000\n" \
  "s_cbranch_scc1 LMOK23_%=\n" \
  "s_add_u32 s58, s58, 1\n" \
  "s_sub_u32 s64, s64, 1\n" \
  "s_mov_b32 s66, s65\n" \
  "s_mov_b32 s68, s69\n" \
  "s_waitcnt vmcnt(0) lgkmcnt(0)\n" \
  "s_barrier\n" \
  "s_branch LOOP23_%=\n" \
  "LDONE_%=:\n" \
  "s_cmp_eq_u32 s73, 1\n" \
  "s_cbranch_scc1 LFIN_%=\n" \
  "s_mov_b32 s73, 1\n" \
  "s_mov_b32 s38, s55\n" \
  "s_mov_b32 s64, 0\n" \
  "s_mov_b32 s60, 0\n" \
  "s_memrealtime s[74:75]\n" \
  "s_memtime s[76:77]\n" \
  "v_readlane_b32 s50, v224, 0\n" \
  "v_readlane_b32 s51, v224, 1\n" \
  "v_readlane_b32 s52, v224, 2\n" \
  "v_readlane_b32 s58, v224, 3\n" \
  "v_readlane_b32 s40, v224, 4\n" \
  "v_readlane_b32 s41, v224, 5\n" \
  "v_readlane_b32 s42, v224, 6\n" \
  "v_readlane_b32 s43, v224, 7\n" \
  "v_readlane_b32 s44, v224, 8\n" \
  "v_readlane_b32 s45, v224, 9\n" \
  "v_readlane_b32 s46, v224, 10\n" \
  "v_readlane_b32 s47, v224, 11\n" \
  "v_mov_b32 v40, v192\n" \
  "v_mov_b32 v41, v193\n" \
  "v_mov_b32 v42, v194\n" \
  "v_mov_b32 v43, v195\n" \
  "v_mov_b32 v44, v196\n" \
  "v_mov_b32 v45, v197\n" \
  "v_mov_b32 v46, v198\n" \
  "v_mov_b32 v47, v199\n" \
  "v_mov_b32 v48, v200\n" \
  "v_mov_b32 v49, v201\n" \
  "v_mov_b32 v50, v202\n" \
  "v_mov_b32 v51, v203\n" \
  "v_mov_b32 v52, v204\n" \
  "v_mov_b32 v53, v205\n" \
  "v_mov_b32 v54, v206\n" \
  "v_mov_b32 v55, v207\n" \
  "v_mov_b32 v56, v208\n" \
  "v_mov_b32 v57, v209\n" \
  "v_mov_b32 v58, v210\n" \
  "v_mov_b32 v59, v211\n" \
  "v_mov_b32 v60, v212\n" \
  "v_mov_b32 v61, v213\n" \
  "v_mov_b32 v62, v214\n" \
  "v_mov_b32 v63, v215\n" \
  "v_mov_b32 v64, v216\n" \
  "v_mov_b32 v65, v217\n" \
  "v_mov_b32 v66, v218\n" \
  "v_mov_b32 v67, v219\n" \
  "v_mov_b32 v68, v220\n" \
  "v_mov_b32 v69, v221\n" \
  "v_mov_b32 v70, v222\n" \
  "v_mov_b32 v71, v223\n" \
  "v_mov_b32 v25, v225\n" \
  "v_mov_b32 v26, v226\n" \
  "v_mov_b32 v32, 1\n" \
  "s_waitcnt lgkmcnt(0)\n" \
  "s_mov_b32 s55, s74\n" \
  "s_mov_b32 s62, s74\n" \
  "s_mov_b32 s86, s76\n" \
  "s_cmp_ge_u32 s48, 8\n" \
  "s_cbranch_scc1 LSV_%=\n" \
  "v_mad_u32_u24 v112, v62, 0, v60\n" \
  "v_add_u32 v128, 90112, v112\n" \
  "v_mad_u32_u24 v113, v62, 2, v60\n" \
  "v_add_u32 v129, 90112, v113\n" \
  "v_mad_u32_u24 v114, v62, 16, v60\n" \
  "v_add_u32 v130, 90112, v114\n" \
  "v_mad_u32_u24 v115, v62, 18, v60\n" \
  "v_add_u32 v131, 90112, v115\n" \
  "v_mad_u32_u24 v116, v62, 32, v60\n" \
  "v_add_u32 v132, 90112, v116\n" \
  "v_mad_u32_u24 v117, v62, 34, v60\n" \
  "v_add_u32 v133, 90112, v117\n" \
  "v_mad_u32_u24 v118, v62, 48, v60\n" \
  "v_add_u32 v134, 90112, v118\n" \
  "v_mad_u32_u24 v119, v62, 50, v60\n" \
  "v_add_u32 v135, 90112, v119\n" \
  "v_mad_u32_u24 v120, v63, 0, v61\n" \
  "v_add_u32 v136, 90112, v120\n" \
  "v_mad_u32_u24 v121, v63, 2, v61\n" \
  "v_add_u32 v137, 90112, v121\n" \
  "v_mad_u32_u24 v122, v63, 16, v61\n" \
  "v_add_u32 v138, 90112, v122\n" \
  "v_mad_u32_u24 v123, v63, 18, v61\n" \
  "v_add_u32 v139, 90112, v123\n" \
  "v_mad_u32_u24 v124, v63, 32, v61\n" \
  "v_add_u32 v140, 90112, v124\n" \
  "v_mad_u32_u24 v125, v63, 34, v61\n" \
  "v_add_u32 v141, 90112, v125\n" \
  "v_mad_u32_u24 v126, v63, 48, v61\n" \
  "v_add_u32 v142, 90112, v126\n" \
  "v_mad_u32_u24 v127, v63, 50, v61\n" \
  "v_add_u32 v143, 90112, v127\n" \
  "s_and_b32 s71, s48, 3\n" \
  "s_cmp_eq_u32 s71, 2\n" \
  "s_cbranch_scc0 LGQ_%=\n" \
  "s_mov_b32 s71, 0\n" \
  "s_add_u32 m0, s56, 57344\n" \
  "s_nop 0\n" \
  "buffer_load_dwordx4 v40, s[40:43], s71 offen lds\n" \
  "s_add_u32 m0, s56, 58368\n" \
  "s_nop 0\n" \
  "buffer_load_dwordx4 v41, s[40:43], s71 offen lds\n" \
  "s_add_u32 m0, s56, 59392\n" \
  "s_nop 0\n" \
  "buffer_load_dwordx4 v42, s[40:43], s71 offen lds\n" \
  "s_add_u32 m0, s56, 60416\n" \
  "s_nop 0\n" \
  "buffer_load_dwordx4 v43, s[40:43], s71 offen lds\n" \
  "s_add_u32 m0, s56, 61440\n" \
  "s_nop 0\n" \
  "buffer_load_dwordx4 v44, s[40:43], s71 offen lds\n" \
  "s_add_u32 m0, s56, 62464\n" \
  "s_nop 0\n" \
  "buffer_load_dwordx4 v45, s[40:43], s71 offen lds\n" \
  "s_add_u32 m0, s56, 63488\n" \
  "s_nop 0\n" \
  "buffer_load_dwordx4 v46, s[40:43], s71 offen lds\n" \
  "s_add_u32 m0, s56, 64512\n" \
  "s_nop 0\n" \
  "buffer_load_dwordx4 v47, s[40:43], s71 offen lds\n" \
  "s_mov_b32 s71, 786432\n" \
  "s_add_u32 m0, s56, 90112\n" \
  "s_nop 0\n" \
  "buffer_load_dwordx4 v40, s[40:43], s71 offen lds\n" \
  "s_add_u32 m0, s56, 91136\n" \
  "s_nop 0\n" \
  "buffer_load_dwordx4 v41, s[40:43], s71 offen lds\n" \
  "s_add_u32 m0, s56, 92160\n" \
  "s_nop 0\n" \
  "buffer_load_dwordx4 v42, s[40:43], s71 offen lds\n" \
  "s_add_u32 m0, s56, 93184\n" \
  "s_nop 0\n" \
  "buffer_load_dwordx4 v43, s[40:43], s71 offen lds\n" \
  "s_add_u32 m0, s56, 94208\n" \
  "s_nop 0\n" \
  "buffer_load_dwordx4 v44, s[40:43], s71 offen lds\n" \
  "s_add_u32 m0, s56, 95232\n" \
  "s_nop 0\n" \
  "buffer_load_dwordx4 v45, s[40:43], s71 offen lds\n" \
  "s_add_u32 m0, s56, 96256\n" \
  "s_nop 0\n" \
  "buffer_load_dwordx4 v46, s[40:43], s71 offen lds\n" \
  "s_add_u32 m0, s56, 97280\n" \
  "s_nop 0\n" \
  "buffer_load_dwordx4 v47, s[40:43], s71 offen lds\n" \
  "s_mov_b32 s71, 1572864\n" \
  "s_add_u32 m0, s56, 122880\n" \
  "s_nop 0\n" \
  "buffer_load_dwordx4 v40, s[40:43], s71 offen lds\n" \
  "s_add_u32 m0, s56, 123904\n" \
  "s_nop 0\n" \
  "buffer_load_dwordx4 v41, s[40:43], s71 offen lds\n" \
  "s_add_u32 m0, s56, 124928\n" \
  "s_nop 0\n" \
  "buffer_load_dwordx4 v42, s[40:43], s71 offen lds\n" \
  "s_add_u32 m0, s56, 125952\n" \
  "s_nop 0\n" \
  "buffer_load_dwordx4 v43, s[40:43], s71 offen lds\n" \
  "s_add_u32 m0, s56, 126976\n" \
  "s_nop 0\n" \
  "buffer_load_dwordx4 v44, s[40:43], s71 offen lds\n" \
  "s_add_u32 m0, s56, 128000\n" \
  "s_nop 0\n" \
  "buffer_load_dwordx4 v45, s[40:43], s71 offen lds\n" \
  "s_add_u32 m0, s56, 129024\n" \
  "s_nop 0\n" \
  "buffer_load_dwordx4 v46, s[40:43], s71 offen lds\n" \
  "s_add_u32 m0, s56, 130048\n" \
  "s_nop 0\n" \
  "buffer_load_dwordx4 v47, s[40:43], s71 offen lds\n" \
  "LGQ_%=:\n" \
  "s_waitcnt vmcnt(16) lgkmcnt(0)\n" \
  "s_barrier\n" \
  "s_mov_b32 s71, 2359296\n" \
  "s_add_u32 m0, s56, 0\n" \
  "s_nop 0\n" \
  "buffer_load_dwordx4 v40, s[40:43], s71 offen lds\n" \
  "s_add_u32 m0, s56, 1024\n" \
  "s_nop 0\n" \
  "buffer_load_dwordx4 v41, s[40:43], s71 offen lds\n" \
  "s_add_u32 m0, s56, 2048\n" \
  "s_nop 0\n" \
  "buffer_load_dwordx4 v42, s[40:43], s71 offen lds\n" \
  "s_add_u32 m0, s56, 3072\n" \
  "s_nop 0\n" \
  "buffer_load_dwordx4 v43, s[40:43], s71 offen lds\n" \
  "s_add_u32 m0, s56, 4096\n" \
  "s_nop 0\n" \
  "buffer_load_dwordx4 v44, s[40:43], s71 offen lds\n" \
  "s_add_u32 m0, s56, 5120\n" \
  "s_nop 0\n" \
  "buffer_load_dwordx4 v45, s[40:43], s71 offen lds\n" \
  "s_add_u32 m0, s56, 6144\n" \
  "s_nop 0\n" \
  "buffer_load_dwordx4 v46, s[40:43], s71 offen lds\n" \
  "s_add_u32 m0, s56, 7168\n" \
  "s_nop 0\n" \
  "buffer_load_dwordx4 v47, s[40:43], s71 offen lds\n" \
  "ds_read_b64_tr_b16 a[0:1], v112 offset:57344\n" \
  "ds_read_b64_tr_b16 a[2:3], v113 offset:57344\n" \
  "ds_read_b64_tr_b16 a[4:5], v114 offset:57344\n" \
  "ds_read_b64_tr_b16 a[6:7], v115 offset:57344\n" \
  "ds_read_b64_tr_b16 a[8:9], v116 offset:57344\n" \
  "ds_read_b64_tr_b16 a[10:11], v117 offset:57344\n" \
  "ds_read_b64_tr_b16 a[12:13], v118 offset:57344\n" \
  "ds_read_b64_tr_b16 a[14:15], v119 offset:57344\n" \
  "s_waitcnt lgkmcnt(0)\n" \
  "ds_read_b64_tr_b16 a[160:161], v120 offset:57344\n" \
  "ds_read_b64_tr_b16 a[162:163], v121 offset:57344\n" \
  "ds_read_b64_tr_b16 a[164:165], v122 offset:57344\n" \
  "ds_read_b64_tr_b16 a[166:167], v123 offset:57344\n" \
  "ds_read_b64_tr_b16 a[168:169], v124 offset:57344\n" \
  "ds_read_b64_tr_b16 a[170:171], v125 offset:57344\n" \
  "ds_read_b64_tr_b16 a[172:173], v126 offset:57344\n" \
  "ds_read_b64_tr_b16 a[174:175], v127 offset:57344\n" \
  "s_waitcnt lgkmcnt(0)\n" \
  "s_waitcnt vmcnt(16) lgkmcnt(0)\n" \
  "s_barrier\n" \
  "s_mov_b32 s71, 3145728\n" \
  "s_add_u32 m0, s56, 57344\n" \
  "s_nop 0\n" \
  "buffer_load_dwordx4 v40, s[40:43], s71 offen lds\n" \
  "s_add_u32 m0, s56, 58368\n" \
  "s_nop 0\n" \
  "buffer_load_dwordx4 v41, s[40:43], s71 offen lds\n" \
  "s_add_u32 m0, s56, 59392\n" \
  "s_nop 0\n" \
  "buffer_load_dwordx4 v42, s[40:43], s71 offen lds\n" \
  "s_add_u32 m0, s56, 60416\n" \
  "s_nop 0\n" \
  "buffer_load_dwordx4 v43, s[40:43], s71 offen lds\n" \
  "s_add_u32 m0, s56, 61440\n" \
  "s_nop 0\n" \
  "buffer_load_dwordx4 v44, s[40:43], s71 offen lds\n" \
  "s_add_u32 m0, s56, 62464\n" \
  "s_nop 0\n" \
  "buffer_load_dwordx4 v45, s[40:43], s71 offen lds\n" \
  "s_add_u32 m0, s56, 63488\n" \
  "s_nop 0\n" \
  "buffer_load_dwordx4 v46, s[40:43], s71 offen lds\n" \
  "s_add_u32 m0, s56, 64512\n" \
  "s_nop 0\n" \
  "buffer_load_dwordx4 v47, s[40:43], s71 offen lds\n" \
  "ds_read_b64_tr_b16 a[16:17], v128\n" \
  "ds_read_b64_tr_b16 a[18:19], v129\n" \
  "ds_read_b64_tr_b16 a[20:21], v130\n" \
  "ds_read_b64_tr_b16 a[22:23], v131\n" \
  "ds_read_b64_tr_b16 a[24:25], v132\n" \
  "ds_read_b64_tr_b16 a[26:27], v133\n" \
  "ds_read_b64_tr_b16 a[28:29], v134\n" \
  "ds_read_b64_tr_b16 a[30:31], v135\n" \
  "s_waitcnt lgkmcnt(0)\n" \
  "ds_read_b64_tr_b16 a[176:177], v136\n" \
  "ds_read_b64_tr_b16 a[178:179], v137\n" \
  "ds_read_b64_tr_b16 a[180:181], v138\n" \
  "ds_read_b64_tr_b16 a[182:183], v139\n" \
  "ds_read_b64_tr_b16 a[184:185], v140\n" \
  "ds_read_b64_tr_b16 a[186:187], v141\n" \
  "ds_read_b64_tr_b16 a[188:189], v142\n" \
  "ds_read_b64_tr_b16 a[190:191], v143\n" \
  "s_waitcnt lgkmcnt(0)\n" \
  "s_waitcnt vmcnt(16) lgkmcnt(0)\n" \
  "s_barrier\n" \
  "s_mov_b32 s71, 3932160\n" \
  "s_add_u32 m0, s56, 90112\n" \
  "s_nop 0\n" \
  "buffer_load_dwordx4 v40, s[40:43], s71 offen lds\n" \
  "s_add_u32 m0, s56, 91136\n" \
  "s_nop 0\n" \
  "buffer_load_dwordx4 v41, s[40:43], s71 offen lds\n" \
  "s_add_u32 m0, s56, 92160\n" \
  "s_nop 0\n" \
  "buffer_load_dwordx4 v42, s[40:43], s71 offen lds\n" \
  "s_add_u32 m0, s56, 93184\n" \
  "s_nop 0\n" \
  "buffer_load_dwordx4 v43, s[40:43], s71 offen lds\n" \
  "s_add_u32 m0, s56, 94208\n" \
  "s_nop 0\n" \
  "buffer_load_dwordx4 v44, s[40:43], s71 offen lds\n" \
  "s_add_u32 m0, s56, 95232\n" \
  "s_nop 0\n" \
  "buffer_load_dwordx4 v45, s[40:43], s71 offen lds\n" \
  "s_add_u32 m0, s56, 96256\n" \
  "s_nop 0\n" \
  "buffer_load_dwordx4 v46, s[40:43], s71 offen lds\n" \
  "s_add_u32 m0, s56, 97280\n" \
  "s_nop 0\n" \
  "buffer_load_dwordx4 v47, s[40:43], s71 offen lds\n" \
  "ds_read_b64_tr_b16 a[32:33], v128 offset:32768\n" \
  "ds_read_b64_tr_b16 a[34:35], v129 offset:32768\n" \
  "ds_read_b64_tr_b16 a[36:37], v130 offset:32768\n" \
  "ds_read_b64_tr_b16 a[38:39], v131 offset:32768\n" \
  "ds_read_b64_tr_b16 a[40:41], v132 offset:32768\n" \
  "ds_read_b64_tr_b16 a[42:43], v133 offset:32768\n" \
  "ds_read_b64_tr_b16 a[44:45], v134 offset:32768\n" \
  "ds_read_b64_tr_b16 a[46:47], v135 offset:32768\n" \
  "s_waitcnt lgkmcnt(0)\n" \
  "ds_read_b64_tr_b16 a[192:193], v136 offset:32768\n" \
  "ds_read_b64_tr_b16 a[194:195], v137 offset:32768\n" \
  "ds_read_b64_tr_b16 a[196:197], v138 offset:32768\n" \
  "ds_read_b64_tr_b16 a[198:199], v139 offset:32768\n" \
  "ds_read_b64_tr_b16 a[200:201], v140 offset:32768\n" \
  "ds_read_b64_tr_b16 a[202:203], v141 offset:32768\n" \
  "ds_read_b64_tr_b16 a[204:205], v142 offset:32768\n" \
  "ds_read_b64_tr_b16 a[206:207], v143 offset:32768\n" \
  "s_waitcnt lgkmcnt(0)\n" \
  "s_waitcnt vmcnt(16) lgkmcnt(0)\n" \
  "s_barrier\n" \
  "s_mov_b32 s71, 4718592\n" \
  "s_add_u32 m0, s56, 122880\n" \
  "s_nop 0\n" \
  "buffer_load_dwordx4 v40, s[40:43], s71 offen lds\n" \
  "s_add_u32 m0, s56, 123904\n" \
  "s_nop 0\n" \
  "buffer_load_dwordx4 v41, s[40:43], s71 offen lds\n" \
  "s_add_u32 m0, s56, 124928\n" \
  "s_nop 0\n" \
  "buffer_load_dwordx4 v42, s[40:43], s71 offen lds\n" \
  "s_add_u32 m0, s56, 125952\n" \
  "s_nop 0\n" \
  "buffer_load_dwordx4 v43, s[40:43], s71 offen lds\n" \
  "s_add_u32 m0, s56, 126976\n" \
  "s_nop 0\n" \
  "buffer_load_dwordx4 v44, s[40:43], s71 offen lds\n" \
  "s_add_u32 m0, s56, 128000\n" \
  "s_nop 0\n" \
  "buffer_load_dwordx4 v45, s[40:43], s71 offen lds\n" \
  "s_add_u32 m0, s56, 129024\n" \
  "s_nop 0\n" \
  "buffer_load_dwordx4 v46, s[40:43], s71 offen lds\n" \
  "s_add_u32 m0, s56, 130048\n" \
  "s_nop 0\n" \
  "buffer_load_dwordx4 v47, s[40:43], s71 offen lds\n" \
  "ds_read_b64_tr_b16 a[48:49], v112\n" \
  "ds_read_b64_tr_b16 a[50:51], v113\n" \
  "ds_read_b64_tr_b16 a[52:53], v114\n" \
  "ds_read_b64_tr_b16 a[54:55], v115\n" \
  "ds_read_b64_tr_b16 a[56:57], v116\n" \
  "ds_read_b64_tr_b16 a[58:59], v117\n" \
  "ds_read_b64_tr_b16 a[60:61], v118\n" \
  "ds_read_b64_tr_b16 a[62:63], v119\n" \
  "s_waitcnt lgkmcnt(0)\n" \
  "ds_read_b64_tr_b16 a[208:209], v120\n" \
  "ds_read_b64_tr_b16 a[210:211], v121\n" \
  "ds_read_b64_tr_b16 a[212:213], v122\n" \
  "ds_read_b64_tr_b16 a[214:215], v123\n" \
  "ds_read_b64_tr_b16 a[216:217], v124\n" \
  "ds_read_b64_tr_b16 a[218:219], v125\n" \
  "ds_read_b64_tr_b16 a[220:221], v126\n" \
  "ds_read_b64_tr_b16 a[222:223], v127\n" \
  "s_waitcnt lgkmcnt(0)\n" \
  "s_waitcnt vmcnt(16) lgkmcnt(0)\n" \
  "s_barrier\n" \
  "s_mov_b32 s71, 5505024\n" \
  "s_add_u32 m0, s56, 0\n" \
  "s_nop 0\n" \
  "buffer_load_dwordx4 v40, s[40:43], s71 offen lds\n" \
  "s_add_u32 m0, s56, 1024\n" \
  "s_nop 0\n" \
  "buffer_load_dwordx4 v41, s[40:43], s71 offen lds\n" \
  "s_add_u32 m0, s56, 2048\n" \
  "s_nop 0\n" \
  "buffer_load_dwordx4 v42, s[40:43], s71 offen lds\n" \
  "s_add_u32 m0, s56, 3072\n" \
  "s_nop 0\n" \
  "buffer_load_dwordx4 v43, s[40:43], s71 offen lds\n" \
  "s_add_u32 m0, s56, 4096\n" \
  "s_nop 0\n" \
  "buffer_load_dwordx4 v44, s[40:43], s71 offen lds\n" \
  "s_add_u32 m0, s56, 5120\n" \
  "s_nop 0\n" \
  "buffer_load_dwordx4 v45, s[40:43], s71 offen lds\n" \
  "s_add_u32 m0, s56, 6144\n" \
  "s_nop 0\n" \
  "buffer_load_dwordx4 v46, s[40:43], s71 offen lds\n" \
  "s_add_u32 m0, s56, 7168\n" \
  "s_nop 0\n" \
  "buffer_load_dwordx4 v47, s[40:43], s71 offen lds\n" \
  "ds_read_b64_tr_b16 a[64:65], v112 offset:57344\n" \
  "ds_read_b64_tr_b16 a[66:67], v113 offset:57344\n" \
  "ds_read_b64_tr_b16 a[68:69], v114 offset:57344\n" \
  "ds_read_b64_tr_b16 a[70:71], v115 offset:57344\n" \
  "ds_read_b64_tr_b16 a[72:73], v116 offset:57344\n" \
  "ds_read_b64_tr_b16 a[74:75], v117 offset:57344\n" \
  "ds_read_b64_tr_b16 a[76:77], v118 offset:57344\n" \
  "ds_read_b64_tr_b16 a[78:79], v119 offset:57344\n" \
  "s_waitcnt lgkmcnt(0)\n" \
  "ds_read_b64_tr_b16 a[224:225], v120 offset:57344\n" \
  "ds_read_b64_tr_b16 a[226:227], v121 offset:57344\n" \
  "ds_read_b64_tr_b16 a[228:229], v122 offset:57344\n" \
  "ds_read_b64_tr_b16 a[230:231], v123 offset:57344\n" \
  "ds_read_b64_tr_b16 a[232:233], v124 offset:57344\n" \
  "ds_read_b64_tr_b16 a[234:235], v125 offset:57344\n" \
  "ds_read_b64_tr_b16 a[236:237], v126 offset:57344\n" \
  "ds_read_b64_tr_b16 a[238:239], v127 offset:57344\n" \
  "s_waitcnt lgkmcnt(0)\n" \
  "s_waitcnt vmcnt(16) lgkmcnt(0)\n" \
  "s_barrier\n" \
  "s_mov_b32 s71, 6291456\n" \
  "s_add_u32 m0, s56, 57344\n" \
  "s_nop 0\n" \
  "buffer_load_dwordx4 v40, s[40:43], s71 offen lds\n" \
  "s_add_u32 m0, s56, 58368\n" \
  "s_nop 0\n" \
  "buffer_load_dwordx4 v41, s[40:43], s71 offen lds\n" \
  "s_add_u32 m0, s56, 59392\n" \
  "s_nop 0\n" \
  "buffer_load_dwordx4 v42, s[40:43], s71 offen lds\n" \
  "s_add_u32 m0, s56, 60416\n" \
  "s_nop 0\n" \
  "buffer_load_dwordx4 v43, s[40:43], s71 offen lds\n" \
  "s_add_u32 m0, s56, 61440\n" \
  "s_nop 0\n" \
  "buffer_load_dwordx4 v44, s[40:43], s71 offen lds\n" \
  "s_add_u32 m0, s56, 62464\n" \
  "s_nop 0\n" \
  "buffer_load_dwordx4 v45, s[40:43], s71 offen lds\n" \
  "s_add_u32 m0, s56, 63488\n" \
  "s_nop 0\n" \
  "buffer_load_dwordx4 v46, s[40:43], s71 offen lds\n" \
  "s_add_u32 m0, s56, 64512\n" \
  "s_nop 0\n" \
  "buffer_load_dwordx4 v47, s[40:43], s71 offen lds\n" \
  "ds_read_b64_tr_b16 a[80:81], v128\n" \
  "ds_read_b64_tr_b16 a[82:83], v129\n" \
  "ds_read_b64_tr_b16 a[84:85], v130\n" \
  "ds_read_b64_tr_b16 a[86:87], v131\n" \
  "ds_read_b64_tr_b16 a[88:89], v132\n" \
  "ds_read_b64_tr_b16 a[90:91], v133\n" \
  "ds_read_b64_tr_b16 a[92:93], v134\n" \
  "ds_read_b64_tr_b16 a[94:95], v135\n" \
  "s_waitcnt lgkmcnt(0)\n" \
  "ds_read_b64_tr_b16 a[240:241], v136\n" \
  "ds_read_b64_tr_b16 a[242:243], v137\n" \
  "ds_read_b64_tr_b16 a[244:245], v138\n" \
  "ds_read_b64_tr_b16 a[246:247], v139\n" \
  "ds_read_b64_tr_b16 a[248:249], v140\n" \
  "ds_read_b64_tr_b16 a[250:251], v141\n" \
  "ds_read_b64_tr_b16 a[252:253], v142\n" \
  "ds_read_b64_tr_b16 a[254:255], v143\n" \
  "s_waitcnt lgkmcnt(0)\n" \
  "s_waitcnt vmcnt(16) lgkmcnt(0)\n" \
  "s_barrier\n" \
  "s_mov_b32 s71, 7077888\n" \
  "s_add_u32 m0, s56, 90112\n" \
  "s_nop 0\n" \
  "buffer_load_dwordx4 v40, s[40:43], s71 offen lds\n" \
  "s_add_u32 m0, s56, 91136\n" \
  "s_nop 0\n" \
  "buffer_load_dwordx4 v41, s[40:43], s71 offen lds\n" \
  "s_add_u32 m0, s56, 92160\n" \
  "s_nop 0\n" \
  "buffer_load_dwordx4 v42, s[40:43], s71 offen lds\n" \
  "s_add_u32 m0, s56, 93184\n" \
  "s_nop 0\n" \
  "buffer_load_dwordx4 v43, s[40:43], s71 offen lds\n" \
  "s_add_u32 m0, s56, 94208\n" \
  "s_nop 0\n" \
  "buffer_load_dwordx4 v44, s[40:43], s71 offen lds\n" \
  "s_add_u32 m0, s56, 95232\n" \
  "s_nop 0\n" \
  "buffer_load_dwordx4 v45, s[40:43], s71 offen lds\n" \
  "s_add_u32 m0, s56, 96256\n" \
  "s_nop 0\n" \
  "buffer_load_dwordx4 v46, s[40:43], s71 offen lds\n" \
  "s_add_u32 m0, s56, 97280\n" \
  "s_nop 0\n" \
  "buffer_load_dwordx4 v47, s[40:43], s71 offen lds\n" \
  "ds_read_b64_tr_b16 a[96:97], v128 offset:32768\n" \
  "ds_read_b64_tr_b16 a[98:99], v129 offset:32768\n" \
  "ds_read_b64_tr_b16 a[100:101], v130 offset:32768\n" \
  "ds_read_b64_tr_b16 a[102:103], v131 offset:32768\n" \
  "ds_read_b64_tr_b16 a[104:105], v132 offset:32768\n" \
  "ds_read_b64_tr_b16 a[106:107], v133 offset:32768\n" \
  "ds_read_b64_tr_b16 a[108:109], v134 offset:32768\n" \
  "ds_read_b64_tr_b16 a[110:111], v135 offset:32768\n" \
  "s_waitcnt lgkmcnt(0)\n" \
  "ds_read_b64_tr_b16 v[192:193], v136 offset:32768\n" \
  "ds_read_b64_tr_b16 v[194:195], v137 offset:32768\n" \
  "ds_read_b64_tr_b16 v[196:197], v138 offset:32768\n" \
  "ds_read_b64_tr_b16 v[198:199], v139 offset:32768\n" \
  "ds_read_b64_tr_b16 v[200:201], v140 offset:32768\n" \
  "ds_read_b64_tr_b16 v[202:203], v141 offset:32768\n" \
  "ds_read_b64_tr_b16 v[204:205], v142 offset:32768\n" \
  "ds_read_b64_tr_b16 v[206:207], v143 offset:32768\n" \
  "s_waitcnt lgkmcnt(0)\n" \
  "s_waitcnt vmcnt(16) lgkmcnt(0)\n" \
  "s_barrier\n" \
  "ds_read_b64_tr_b16 a[112:113], v112\n" \
  "ds_read_b64_tr_b16 a[114:115], v113\n" \
  "ds_read_b64_tr_b16 a[116:117], v114\n" \
  "ds_read_b64_tr_b16 a[118:119], v115\n" \
  "ds_read_b64_tr_b16 a[120:121], v116\n" \
  "ds_read_b64_tr_b16 a[122:123], v117\n" \
  "ds_read_b64_tr_b16 a[124:125], v118\n" \
  "ds_read_b64_tr_b16 a[126:127], v119\n" \
  "s_waitcnt lgkmcnt(0)\n" \
  "ds_read_b64_tr_b16 v[208:209], v120\n" \
  "ds_read_b64_tr_b16 v[210:211], v121\n" \
  "ds_read_b64_tr_b16 v[212:213], v122\n" \
  "ds_read_b64_tr_b16 v[214:215], v123\n" \
  "ds_read_b64_tr_b16 v[216:217], v124\n" \
  "ds_read_b64_tr_b16 v[218:219], v125\n" \
  "ds_read_b64_tr_b16 v[220:221], v126\n" \
  "ds_read_b64_tr_b16 v[222:223], v127\n" \
  "s_waitcnt lgkmcnt(0)\n" \
  "s_waitcnt vmcnt(8) lgkmcnt(0)\n" \
  "s_barrier\n" \
  "ds_read_b64_tr_b16 a[128:129], v112 offset:57344\n" \
  "ds_read_b64_tr_b16 a[130:131], v113 offset:57344\n" \
  "ds_read_b64_tr_b16 a[132:133], v114 offset:57344\n" \
  "ds_read_b64_tr_b16 a[134:135], v115 offset:57344\n" \
  "ds_read_b64_tr_b16 a[136:137], v116 offset:57344\n" \
  "ds_read_b64_tr_b16 a[138:139], v117 offset:57344\n" \
  "ds_read_b64_tr_b16 a[140:141], v118 offset:57344\n" \
  "ds_read_b64_tr_b16 a[142:143], v119 offset:57344\n" \
  "s_waitcnt lgkmcnt(0)\n" \
  "ds_read_b64_tr_b16 v[224:225], v120 offset:57344\n" \
  "ds_read_b64_tr_b16 v[226:227], v121 offset:57344\n" \
  "ds_read_b64_tr_b16 v[228:229], v122 offset:57344\n" \
  "ds_read_b64_tr_b16 v[230:231], v123 offset:57344\n" \
  "ds_read_b64_tr_b16 v[232:233], v124 offset:57344\n" \
  "ds_read_b64_tr_b16 v[234:235], v125 offset:57344\n" \
  "ds_read_b64_tr_b16 v[236:237], v126 offset:57344\n" \
  "ds_read_b64_tr_b16 v[238:239], v127 offset:57344\n" \
  "s_waitcnt lgkmcnt(0)\n" \
  "s_waitcnt vmcnt(0) lgkmcnt(0)\n" \
  "s_barrier\n" \
  "ds_read_b64_tr_b16 a[144:145], v128\n" \
  "ds_read_b64_tr_b16 a[146:147], v129\n" \
  "ds_read_b64_tr_b16 a[148:149], v130\n" \
  "ds_read_b64_tr_b16 a[150:151], v131\n" \
  "ds_read_b64_tr_b16 a[152:153], v132\n" \
  "ds_read_b64_tr_b16 a[154:155], v133\n" \
  "ds_read_b64_tr_b16 a[156:157], v134\n" \
  "ds_read_b64_tr_b16 a[158:159], v135\n" \
  "s_waitcnt lgkmcnt(0)\n" \
  "ds_read_b64_tr_b16 v[240:241], v136\n" \
  "ds_read_b64_tr_b16 v[242:243], v137\n" \
  "ds_read_b64_tr_b16 v[244:245], v138\n" \
  "ds_read_b64_tr_b16 v[246:247], v139\n" \
  "ds_read_b64_tr_b16 v[248:249], v140\n" \
  "ds_read_b64_tr_b16 v[250:251], v141\n" \
  "ds_read_b64_tr_b16 v[252:253], v142\n" \
  "ds_read_b64_tr_b16 v[254:255], v143\n" \
  "s_waitcnt lgkmcnt(0)\n" \
  "s_waitcnt lgkmcnt(0)\n" \
  "s_barrier\n" \
  "LIQ_%=:\n" \
  "buffer_load_dword v92, v69, s[36:39], 0 offen sc1\n" \
  "s_waitcnt vmcnt(0)\n" \
  "v_and_b32 v93, -4, v92\n" \
  "v_cmp_ne_u32 vcc, s58, v93\n" \
  "s_cbranch_vccz LIXQ_%=\n" \
  "s_add_u32 s60, s60, 1\n" \
  "s_cmp_gt_u32 s60, 20000\n" \
  "s_cbranch_scc1 LIXQ_%=\n" \
  "s_sleep 8\n" \
  "s_branch LIQ_%=\n" \
  "LIXQ_%=:\n" \
  "buffer_atomic_add v32, v68, s[36:39], 0 offen\n" \
  "v_and_b32 v92, v70, v92\n" \
  "v_cmp_ne_u32 s[80:81], 0, v92\n" \
  "s_mov_b32 s78, 0x55555555\n" \
  "s_mov_b32 s79, 0x55555555\n" \
  "s_xor_b64 s[82:83], s[80:81], s[78:79]\n" \
  "s_bcnt1_i32_b64 s71, s[82:83]\n" \
  "s_cmp_le_u32 s71, 26\n" \
  "s_cselect_b64 s[80:81], s[78:79], s[80:81]\n" \
  "s_not_b64 s[78:79], s[78:79]\n" \
  "s_cmp_ge_u32 s71, 38\n" \
  "s_cselect_b64 s[80:81], s[78:79], s[80:81]\n" \
  "v_lshrrev_b32 v92, 21, v25\n" \
  "v_lshrrev_b64 v[92:93], v92, s[80:81]\n" \
  "v_and_b32 v92, 1, v92\n" \
  "v_lshlrev_b32 v92, 13, v92\n" \
  "v_xor_b32 v25, v92, v25\n" \
  "v_lshrrev_b32 v92, 21, v26\n" \
  "v_lshrrev_b64 v[92:93], v92, s[80:81]\n" \
  "v_and_b32 v92, 1, v92\n" \
  "v_lshlrev_b32 v92, 13, v92\n" \
  "v_xor_b32 v26, v92, v26\n" \
  "v_lshrrev_b32 v92, 21, v27\n" \
  "v_lshrrev_b64 v[92:93], v92, s[80:81]\n" \
  "v_and_b32 v92, 1, v92\n" \
  "v_lshlrev_b32 v92, 13, v92\n" \
  "v_xor_b32 v27, v92, v27\n" \
  "v_lshrrev_b32 v92, 21, v28\n" \
  "v_lshrrev_b64 v[92:93], v92, s[80:81]\n" \
  "v_and_b32 v92, 1, v92\n" \
  "v_lshlrev_b32 v92, 13, v92\n" \
  "v_xor_b32 v28, v92, v28\n" \
  "v_lshrrev_b32 v92, 21, v29\n" \
  "v_lshrrev_b64 v[92:93], v92, s[80:81]\n" \
  "v_and_b32 v92, 1, v92\n" \
  "v_lshlrev_b32 v92, 13, v92\n" \
  "v_xor_b32 v29, v92, v29\n" \
  "v_lshrrev_b32 v92, 21, v30\n" \
  "v_lshrrev_b64 v[92:93], v92, s[80:81]\n" \
  "v_and_b32 v92, 1, v92\n" \
  "v_lshlrev_b32 v92, 13, v92\n" \
  "v_xor_b32 v30, v92, v30\n" \
  "v_lshrrev_b32 v92, 21, v67\n" \
  "v_lshrrev_b64 v[92:93], v92, s[80:81]\n" \
  "v_and_b32 v92, 1, v92\n" \
  "v_lshlrev_b32 v92, 13, v92\n" \
  "v_xor_b32 v67, v92, v67\n" \
  "v_lshrrev_b32 v92, 21, v71\n" \
  "v_lshrrev_b64 v[92:93], v92, s[80:81]\n" \
  "v_and_b32 v92, 1, v92\n" \
  "v_lshlrev_b32 v92, 13, v92\n" \
  "v_xor_b32 v71, v92, v71\n" \
  "s_lshr_b32 s71, s54, 21\n" \
  "s_lshr_b64 s[80:81], s[80:81], s71\n" \
  "s_and_b32 s71, s80, 1\n" \
  "s_lshl_b32 s71, s71, 13\n" \
  "s_or_b32 s54, s54, s71\n" \
  "s_and_b32 s71, s48, 3\n" \
  "s_cmp_eq_u32 s71, 3\n" \
  "s_cbranch_scc0 LIYQ_%=\n" \
  "v_mov_b32 v1, v71\n" \
  "LIYQ_%=:\n" \
  "v_mov_b32 v104, v64\n" \
  "v_mov_b32 v105, v65\n" \
  "v_mov_b32 v106, v66\n" \
  "v_mov_b32 v107, v67\n" \
  "s_branch LCN_%=\n" \
  "LSV_%=:\n" \
  "v_add_u32 v72, 16384, v60\n" \
  "v_add_u32 v73, 16384, v61\n" \
  "v_add_u32 v74, 16384, v62\n" \
  "v_add_u32 v76, 65536, v60\n" \
  "v_add_u32 v77, 65536, v61\n" \
  "v_add_u32 v78, 65536, v62\n" \
  "v_add_u32 v80, 114688, v60\n" \
  "v_add_u32 v81, 114688, v61\n" \
  "v_add_u32 v82, 114688, v62\n" \
  "s_and_b32 s71, s48, 3\n" \
  "s_cmp_eq_u32 s71, 2\n" \
  "s_cbranch_scc0 LGV_%=\n" \
  "s_mov_b32 s71, 0\n" \
  "s_add_u32 m0, s56, 16384\n" \
  "s_nop 0\n" \
  "buffer_load_dwordx4 v40, s[40:43], s71 offen lds\n" \
  "s_add_u32 m0, s56, 17408\n" \
  "s_nop 0\n" \
  "buffer_load_dwordx4 v41, s[40:43], s71 offen lds\n" \
  "s_add_u32 m0, s56, 18432\n" \
  "s_nop 0\n" \
  "buffer_load_dwordx4 v42, s[40:43], s71 offen lds\n" \
  "s_add_u32 m0, s56, 19456\n" \
  "s_nop 0\n" \
  "buffer_load_dwordx4 v43, s[40:43], s71 offen lds\n" \
  "s_add_u32 m0, s56, 20480\n" \
  "s_nop 0\n" \
  "buffer_load_dwordx4 v44, s[40:43], s71 offen lds\n" \
  "s_add_u32 m0, s56, 21504\n" \
  "s_nop 0\n" \
  "buffer_load_dwordx4 v45, s[40:43], s71 offen lds\n" \
  "s_mov_b32 s71, 786432\n" \
  "s_add_u32 m0, s56, 40960\n" \
  "s_nop 0\n" \
  "buffer_load_dwordx4 v40, s[40:43], s71 offen lds\n" \
  "s_add_u32 m0, s56, 41984\n" \
  "s_nop 0\n" \
  "buffer_load_dwordx4 v41, s[40:43], s71 offen lds\n" \
  "s_add_u32 m0, s56, 43008\n" \
  "s_nop 0\n" \
  "buffer_load_dwordx4 v42, s[40:43], s71 offen lds\n" \
  "s_add_u32 m0, s56, 44032\n" \
  "s_nop 0\n" \
  "buffer_load_dwordx4 v43, s[40:43], s71 offen lds\n" \
  "s_add_u32 m0, s56, 45056\n" \
  "s_nop 0\n" \
  "buffer_load_dwordx4 v44, s[40:43], s71 offen lds\n" \
  "s_add_u32 m0, s56, 46080\n" \
  "s_nop 0\n" \
  "buffer_load_dwordx4 v45, s[40:43], s71 offen lds\n" \
  "s_mov_b32 s71, 1572864\n" \
  "s_add_u32 m0, s56, 65536\n" \
  "s_nop 0\n" \
  "buffer_load_dwordx4 v40, s[40:43], s71 offen lds\n" \
  "s_add_u32 m0, s56, 66560\n" \
  "s_nop 0\n" \
  "buffer_load_dwordx4 v41, s[40:43], s71 offen lds\n" \
  "s_add_u32 m0, s56, 67584\n" \
  "s_nop 0\n" \
  "buffer_load_dwordx4 v42, s[40:43], s71 offen lds\n" \
  "s_add_u32 m0, s56, 68608\n" \
  "s_nop 0\n" \
  "buffer_load_dwordx4 v43, s[40:43], s71 offen lds\n" \
  "s_add_u32 m0, s56, 69632\n" \
  "s_nop 0\n" \
  "buffer_load_dwordx4 v44, s[40:43], s71 offen lds\n" \
  "s_add_u32 m0, s56, 70656\n" \
  "s_nop 0\n" \
  "buffer_load_dwordx4 v45, s[40:43], s71 offen lds\n" \
  "s_mov_b32 s71, 2359296\n" \
  "s_add_u32 m0, s56, 90112\n" \
  "s_nop 0\n" \
  "buffer_load_dwordx4 v40, s[40:43], s71 offen lds\n" \
  "s_add_u32 m0, s56, 91136\n" \
  "s_nop 0\n" \
  "buffer_load_dwordx4 v41, s[40:43], s71 offen lds\n" \
  "s_add_u32 m0, s56, 92160\n" \
  "s_nop 0\n" \
  "buffer_load_dwordx4 v42, s[40:43], s71 offen lds\n" \
  "s_add_u32 m0, s56, 93184\n" \
  "s_nop 0\n" \
  "buffer_load_dwordx4 v43, s[40:43], s71 offen lds\n" \
  "s_add_u32 m0, s56, 94208\n" \
  "s_nop 0\n" \
  "buffer_load_dwordx4 v44, s[40:43], s71 offen lds\n" \
  "s_add_u32 m0, s56, 95232\n" \
  "s_nop 0\n" \
  "buffer_load_dwordx4 v45, s[40:43], s71 offen lds\n" \
  "LGV_%=:\n" \
  "s_waitcnt vmcnt(12) lgkmcnt(0)\n" \
  "s_barrier\n" \
  "s_mov_b32 s71, 3145728\n" \
  "s_add_u32 m0, s56, 114688\n" \
  "s_nop 0\n" \
  "buffer_load_dwordx4 v40, s[40:43], s71 offen lds\n" \
  "s_add_u32 m0, s56, 115712\n" \
  "s_nop 0\n" \
  "buffer_load_dwordx4 v41, s[40:43], s71 offen lds\n" \
  "s_add_u32 m0, s56, 116736\n" \
  "s_nop 0\n" \
  "buffer_load_dwordx4 v42, s[40:43], s71 offen lds\n" \
  "s_add_u32 m0, s56, 117760\n" \
  "s_nop 0\n" \
  "buffer_load_dwordx4 v43, s[40:43], s71 offen lds\n" \
  "s_add_u32 m0, s56, 118784\n" \
  "s_nop 0\n" \
  "buffer_load_dwordx4 v44, s[40:43], s71 offen lds\n" \
  "s_add_u32 m0, s56, 119808\n" \
  "s_nop 0\n" \
  "buffer_load_dwordx4 v45, s[40:43], s71 offen lds\n" \
  "s_mov_b32 s71, 3932160\n" \
  "s_add_u32 m0, s56, 139264\n" \
  "s_nop 0\n" \
  "buffer_load_dwordx4 v40, s[40:43], s71 offen lds\n" \
  "s_add_u32 m0, s56, 140288\n" \
  "s_nop 0\n" \
  "buffer_load_dwordx4 v41, s[40:43], s71 offen lds\n" \
  "s_add_u32 m0, s56, 141312\n" \
  "s_nop 0\n" \
  "buffer_load_dwordx4 v42, s[40:43], s71 offen lds\n" \
  "s_add_u32 m0, s56, 142336\n" \
  "s_nop 0\n" \
  "buffer_load_dwordx4 v43, s[40:43], s71 offen lds\n" \
  "s_add_u32 m0, s56, 143360\n" \
  "s_nop 0\n" \
  "buffer_load_dwordx4 v44, s[40:43], s71 offen lds\n" \
  "s_add_u32 m0, s56, 144384\n" \
  "s_nop 0\n" \
  "buffer_load_dwordx4 v45, s[40:43], s71 offen lds\n" \
  "ds_read_b64_tr_b16 a[0:1], v72\n" \
  "ds_read_b64_tr_b16 a[2:3], v72 offset:768\n" \
  "ds_read_b64_tr_b16 a[4:5], v72 offset:12288\n" \
  "ds_read_b64_tr_b16 a[6:7], v72 offset:13056\n" \
  "ds_read_b64_tr_b16 a[80:81], v72 offset:32\n" \
  "ds_read_b64_tr_b16 a[82:83], v72 offset:800\n" \
  "ds_read_b64_tr_b16 a[84:85], v72 offset:12320\n" \
  "ds_read_b64_tr_b16 a[86:87], v72 offset:13088\n" \
  "ds_read_b64_tr_b16 a[160:161], v73\n" \
  "ds_read_b64_tr_b16 a[162:163], v73 offset:768\n" \
  "ds_read_b64_tr_b16 a[164:165], v73 offset:12288\n" \
  "ds_read_b64_tr_b16 a[166:167], v73 offset:13056\n" \
  "s_waitcnt lgkmcnt(0)\n" \
  "ds_read_b64_tr_b16 a[8:9], v72 offset:24576\n" \
  "ds_read_b64_tr_b16 a[10:11], v72 offset:25344\n" \
  "ds_read_b64_tr_b16 a[12:13], v72 offset:36864\n" \
  "ds_read_b64_tr_b16 a[14:15], v72 offset:37632\n" \
  "ds_read_b64_tr_b16 a[88:89], v72 offset:24608\n" \
  "ds_read_b64_tr_b16 a[90:91], v72 offset:25376\n" \
  "ds_read_b64_tr_b16 a[92:93], v72 offset:36896\n" \
  "ds_read_b64_tr_b16 a[94:95], v72 offset:37664\n" \
  "ds_read_b64_tr_b16 a[168:169], v73 offset:24576\n" \
  "ds_read_b64_tr_b16 a[170:171], v73 offset:25344\n" \
  "ds_read_b64_tr_b16 a[172:173], v73 offset:36864\n" \
  "ds_read_b64_tr_b16 a[174:175], v73 offset:37632\n" \
  "s_waitcnt lgkmcnt(0)\n" \
  "s_waitcnt vmcnt(12) lgkmcnt(0)\n" \
  "s_barrier\n" \
  "s_mov_b32 s71, 4718592\n" \
  "s_add_u32 m0, s56, 16384\n" \
  "s_nop 0\n" \
  "buffer_load_dwordx4 v40, s[40:43], s71 offen lds\n" \
  "s_add_u32 m0, s56, 17408\n" \
  "s_nop 0\n" \
  "buffer_load_dwordx4 v41, s[40:43], s71 offen lds\n" \
  "s_add_u32 m0, s56, 18432\n" \
  "s_nop 0\n" \
  "buffer_load_dwordx4 v42, s[40:43], s71 offen lds\n" \
  "s_add_u32 m0, s56, 19456\n" \
  "s_nop 0\n" \
  "buffer_load_dwordx4 v43, s[40:43], s71 offen lds\n" \
  "s_add_u32 m0, s56, 20480\n" \
  "s_nop 0\n" \
  "buffer_load_dwordx4 v44, s[40:43], s71 offen lds\n" \
  "s_add_u32 m0, s56, 21504\n" \
  "s_nop 0\n" \
  "buffer_load_dwordx4 v45, s[40:43], s71 offen lds\n" \
  "s_mov_b32 s71, 5505024\n" \
  "s_add_u32 m0, s56, 40960\n" \
  "s_nop 0\n" \
  "buffer_load_dwordx4 v40, s[40:43], s71 offen lds\n" \
  "s_add_u32 m0, s56, 41984\n" \
  "s_nop 0\n" \
  "buffer_load_dwordx4 v41, s[40:43], s71 offen lds\n" \
  "s_add_u32 m0, s56, 43008\n" \
  "s_nop 0\n" \
  "buffer_load_dwordx4 v42, s[40:43], s71 offen lds\n" \
  "s_add_u32 m0, s56, 44032\n" \
  "s_nop 0\n" \
  "buffer_load_dwordx4 v43, s[40:43], s71 offen lds\n" \
  "s_add_u32 m0, s56, 45056\n" \
  "s_nop 0\n" \
  "buffer_load_dwordx4 v44, s[40:43], s71 offen lds\n" \
  "s_add_u32 m0, s56, 46080\n" \
  "s_nop 0\n" \
  "buffer_load_dwordx4 v45, s[40:43], s71 offen lds\n" \
  "ds_read_b64_tr_b16 a[16:17], v76\n" \
  "ds_read_b64_tr_b16 a[18:19], v76 offset:768\n" \
  "ds_read_b64_tr_b16 a[20:21], v76 offset:12288\n" \
  "ds_read_b64_tr_b16 a[22:23], v76 offset:13056\n" \
  "ds_read_b64_tr_b16 a[96:97], v76 offset:32\n" \
  "ds_read_b64_tr_b16 a[98:99], v76 offset:800\n" \
  "ds_read_b64_tr_b16 a[100:101], v76 offset:12320\n" \
  "ds_read_b64_tr_b16 a[102:103], v76 offset:13088\n" \
  "ds_read_b64_tr_b16 a[176:177], v77\n" \
  "ds_read_b64_tr_b16 a[178:179], v77 offset:768\n" \
  "ds_read_b64_tr_b16 a[180:181], v77 offset:12288\n" \
  "ds_read_b64_tr_b16 a[182:183], v77 offset:13056\n" \
  "s_waitcnt lgkmcnt(0)\n" \
  "ds_read_b64_tr_b16 a[24:25], v76 offset:24576\n" \
  "ds_read_b64_tr_b16 a[26:27], v76 offset:25344\n" \
  "ds_read_b64_tr_b16 a[28:29], v76 offset:36864\n" \
  "ds_read_b64_tr_b16 a[30:31], v76 offset:37632\n" \
  "ds_read_b64_tr_b16 a[104:105], v76 offset:24608\n" \
  "ds_read_b64_tr_b16 a[106:107], v76 offset:25376\n" \
  "ds_read_b64_tr_b16 a[108:109], v76 offset:36896\n" \
  "ds_read_b64_tr_b16 a[110:111], v76 offset:37664\n" \
  "ds_read_b64_tr_b16 a[184:185], v77 offset:24576\n" \
  "ds_read_b64_tr_b16 a[186:187], v77 offset:25344\n" \
  "ds_read_b64_tr_b16 a[188:189], v77 offset:36864\n" \
  "ds_read_b64_tr_b16 a[190:191], v77 offset:37632\n" \
  "s_waitcnt lgkmcnt(0)\n" \
  "s_waitcnt vmcnt(12) lgkmcnt(0)\n" \
  "s_barrier\n" \
  "s_mov_b32 s71, 6291456\n" \
  "s_add_u32 m0, s56, 65536\n" \
  "s_nop 0\n" \
  "buffer_load_dwordx4 v40, s[40:43], s71 offen lds\n" \
  "s_add_u32 m0, s56, 66560\n" \
  "s_nop 0\n" \
  "buffer_load_dwordx4 v41, s[40:43], s71 offen lds\n" \
  "s_add_u32 m0, s56, 67584\n" \
  "s_nop 0\n" \
  "buffer_load_dwordx4 v42, s[40:43], s71 offen lds\n" \
  "s_add_u32 m0, s56, 68608\n" \
  "s_nop 0\n" \
  "buffer_load_dwordx4 v43, s[40:43], s71 offen lds\n" \
  "s_add_u32 m0, s56, 69632\n" \
  "s_nop 0\n" \
  "buffer_load_dwordx4 v44, s[40:43], s71 offen lds\n" \
  "s_add_u32 m0, s56, 70656\n" \
  "s_nop 0\n" \
  "buffer_load_dwordx4 v45, s[40:43], s71 offen lds\n" \
  "s_mov_b32 s71, 7077888\n" \
  "s_add_u32 m0, s56, 90112\n" \
  "s_nop 0\n" \
  "buffer_load_dwordx4 v40, s[40:43], s71 offen lds\n" \
  "s_add_u32 m0, s56, 91136\n" \
  "s_nop 0\n" \
  "buffer_load_dwordx4 v41, s[40:43], s71 offen lds\n" \
  "s_add_u32 m0, s56, 92160\n" \
  "s_nop 0\n" \
  "buffer_load_dwordx4 v42, s[40:43], s71 offen lds\n" \
  "s_add_u32 m0, s56, 93184\n" \
  "s_nop 0\n" \
  "buffer_load_dwordx4 v43, s[40:43], s71 offen lds\n" \
  "s_add_u32 m0, s56, 94208\n" \
  "s_nop 0\n" \
  "buffer_load_dwordx4 v44, s[40:43], s71 offen lds\n" \
  "s_add_u32 m0, s56, 95232\n" \
  "s_nop 0\n" \
  "buffer_load_dwordx4 v45, s[40:43], s71 offen lds\n" \
  "ds_read_b64_tr_b16 a[32:33], v80\n" \
  "ds_read_b64_tr_b16 a[34:35], v80 offset:768\n" \
  "ds_read_b64_tr_b16 a[36:37], v80 offset:12288\n" \
  "ds_read_b64_tr_b16 a[38:39], v80 offset:13056\n" \
  "ds_read_b64_tr_b16 a[112:113], v80 offset:32\n" \
  "ds_read_b64_tr_b16 a[114:115], v80 offset:800\n" \
  "ds_read_b64_tr_b16 a[116:117], v80 offset:12320\n" \
  "ds_read_b64_tr_b16 a[118:119], v80 offset:13088\n" \
  "ds_read_b64_tr_b16 a[192:193], v81\n" \
  "ds_read_b64_tr_b16 a[194:195], v81 offset:768\n" \
  "ds_read_b64_tr_b16 a[196:197], v81 offset:12288\n" \
  "ds_read_b64_tr_b16 a[198:199], v81 offset:13056\n" \
  "s_waitcnt lgkmcnt(0)\n" \
  "ds_read_b64_tr_b16 a[40:41], v80 offset:24576\n" \
  "ds_read_b64_tr_b16 a[42:43], v80 offset:25344\n" \
  "ds_read_b64_tr_b16 a[44:45], v80 offset:36864\n" \
  "ds_read_b64_tr_b16 a[46:47], v80 offset:37632\n" \
  "ds_read_b64_tr_b16 a[120:121], v80 offset:24608\n" \
  "ds_read_b64_tr_b16 a[122:123], v80 offset:25376\n" \
  "ds_read_b64_tr_b16 a[124:125], v80 offset:36896\n" \
  "ds_read_b64_tr_b16 a[126:127], v80 offset:37664\n" \
  "ds_read_b64_tr_b16 a[200:201], v81 offset:24576\n" \
  "ds_read_b64_tr_b16 a[202:203], v81 offset:25344\n" \
  "ds_read_b64_tr_b16 a[204:205], v81 offset:36864\n" \
  "ds_read_b64_tr_b16 a[206:207], v81 offset:37632\n" \
  "s_waitcnt lgkmcnt(0)\n" \
  "s_waitcnt vmcnt(12) lgkmcnt(0)\n" \
  "s_barrier\n" \
  "s_mov_b32 s71, 0\n" \
  "s_add_u32 m0, s57, 114688\n" \
  "s_nop 0\n" \
  "buffer_load_dwordx4 v50, s[44:47], s71 offen lds\n" \
  "s_add_u32 m0, s57, 115712\n" \
  "s_nop 0\n" \
  "buffer_load_dwordx4 v51, s[44:47], s71 offen lds\n" \
  "s_add_u32 m0, s57, 116736\n" \
  "s_nop 0\n" \
  "buffer_load_dwordx4 v52, s[44:47], s71 offen lds\n" \
  "s_add_u32 m0, s57, 117760\n" \
  "s_nop 0\n" \
  "buffer_load_dwordx4 v53, s[44:47], s71 offen lds\n" \
  "s_add_u32 m0, s57, 118784\n" \
  "s_nop 0\n" \
  "buffer_load_dwordx4 v54, s[44:47], s71 offen lds\n" \
  "s_add_u32 m0, s57, 119808\n" \
  "s_nop 0\n" \
  "buffer_load_dwordx4 v55, s[44:47], s71 offen lds\n" \
  "s_add_u32 m0, s57, 120832\n" \
  "s_nop 0\n" \
  "buffer_load_dwordx4 v56, s[44:47], s71 offen lds\n" \
  "s_add_u32 m0, s57, 121856\n" \
  "s_nop 0\n" \
  "buffer_load_dwordx4 v57, s[44:47], s71 offen lds\n" \
  "s_add_u32 m0, s57, 122880\n" \
  "s_nop 0\n" \
  "buffer_load_dwordx4 v58, s[44:47], s71 offen lds\n" \
  "s_add_u32 m0, s57, 123904\n" \
  "s_nop 0\n" \
  "buffer_load_dwordx4 v59, s[44:47], s71 offen lds\n" \
  "ds_read_b64_tr_b16 a[48:49], v72\n" \
  "ds_read_b64_tr_b16 a[50:51], v72 offset:768\n" \
  "ds_read_b64_tr_b16 a[52:53], v72 offset:12288\n" \
  "ds_read_b64_tr_b16 a[54:55], v72 offset:13056\n" \
  "ds_read_b64_tr_b16 a[128:129], v72 offset:32\n" \
  "ds_read_b64_tr_b16 a[130:131], v72 offset:800\n" \
  "ds_read_b64_tr_b16 a[132:133], v72 offset:12320\n" \
  "ds_read_b64_tr_b16 a[134:135], v72 offset:13088\n" \
  "ds_read_b64_tr_b16 a[208:209], v73\n" \
  "ds_read_b64_tr_b16 a[210:211], v73 offset:768\n" \
  "ds_read_b64_tr_b16 a[212:213], v73 offset:12288\n" \
  "ds_read_b64_tr_b16 a[214:215], v73 offset:13056\n" \
  "s_waitcnt lgkmcnt(0)\n" \
  "ds_read_b64_tr_b16 a[56:57], v72 offset:24576\n" \
  "ds_read_b64_tr_b16 a[58:59], v72 offset:25344\n" \
  "ds_read_b64_tr_b16 a[60:61], v72 offset:36864\n" \
  "ds_read_b64_tr_b16 a[62:63], v72 offset:37632\n" \
  "ds_read_b64_tr_b16 a[136:137], v72 offset:24608\n" \
  "ds_read_b64_tr_b16 a[138:139], v72 offset:25376\n" \
  "ds_read_b64_tr_b16 a[140:141], v72 offset:36896\n" \
  "ds_read_b64_tr_b16 a[142:143], v72 offset:37664\n" \
  "ds_read_b64_tr_b16 a[216:217], v73 offset:24576\n" \
  "ds_read_b64_tr_b16 a[218:219], v73 offset:25344\n" \
  "ds_read_b64_tr_b16 a[220:221], v73 offset:36864\n" \
  "ds_read_b64_tr_b16 a[222:223], v73 offset:37632\n" \
  "s_waitcnt lgkmcnt(0)\n" \
  "s_waitcnt vmcnt(10) lgkmcnt(0)\n" \
  "s_barrier\n" \
  "s_mov_b32 s71, 40960\n" \
  "s_add_u32 m0, s57, 16384\n" \
  "s_nop 0\n" \
  "buffer_load_dwordx4 v50, s[44:47], s71 offen lds\n" \
  "s_add_u32 m0, s57, 17408\n" \
  "s_nop 0\n" \
  "buffer_load_dwordx4 v51, s[44:47], s71 offen lds\n" \
  "s_add_u32 m0, s57, 18432\n" \
  "s_nop 0\n" \
  "buffer_load_dwordx4 v52, s[44:47], s71 offen lds\n" \
  "s_add_u32 m0, s57, 19456\n" \
  "s_nop 0\n" \
  "buffer_load_dwordx4 v53, s[44:47], s71 offen lds\n" \
  "s_add_u32 m0, s57, 20480\n" \
  "s_nop 0\n" \
  "buffer_load_dwordx4 v54, s[44:47], s71 offen lds\n" \
  "s_add_u32 m0, s57, 21504\n" \
  "s_nop 0\n" \
  "buffer_load_dwordx4 v55, s[44:47], s71 offen lds\n" \
  "s_add_u32 m0, s57, 22528\n" \
  "s_nop 0\n" \
  "buffer_load_dwordx4 v56, s[44:47], s71 offen lds\n" \
  "s_add_u32 m0, s57, 23552\n" \
  "s_nop 0\n" \
  "buffer_load_dwordx4 v57, s[44:47], s71 offen lds\n" \
  "s_add_u32 m0, s57, 24576\n" \
  "s_nop 0\n" \
  "buffer_load_dwordx4 v58, s[44:47], s71 offen lds\n" \
  "s_add_u32 m0, s57, 25600\n" \
  "s_nop 0\n" \
  "buffer_load_dwordx4 v59, s[44:47], s71 offen lds\n" \
  "ds_read_b64_tr_b16 a[64:65], v76\n" \
  "ds_read_b64_tr_b16 a[66:67], v76 offset:768\n" \
  "ds_read_b64_tr_b16 a[68:69], v76 offset:12288\n" \
  "ds_read_b64_tr_b16 a[70:71], v76 offset:13056\n" \
  "ds_read_b64_tr_b16 a[144:145], v76 offset:32\n" \
  "ds_read_b64_tr_b16 a[146:147], v76 offset:800\n" \
  "ds_read_b64_tr_b16 a[148:149], v76 offset:12320\n" \
  "ds_read_b64_tr_b16 a[150:151], v76 offset:13088\n" \
  "ds_read_b64_tr_b16 a[224:225], v77\n" \
  "ds_read_b64_tr_b16 a[226:227], v77 offset:768\n" \
  "ds_read_b64_tr_b16 a[228:229], v77 offset:12288\n" \
  "ds_read_b64_tr_b16 a[230:231], v77 offset:13056\n" \
  "s_waitcnt lgkmcnt(0)\n" \
  "ds_read_b64_tr_b16 a[72:73], v76 offset:24576\n" \
  "ds_read_b64_tr_b16 a[74:75], v76 offset:25344\n" \
  "ds_read_b64_tr_b16 a[76:77], v76 offset:36864\n" \
  "ds_read_b64_tr_b16 a[78:79], v76 offset:37632\n" \
  "ds_read_b64_tr_b16 a[152:153], v76 offset:24608\n" \
  "ds_read_b64_tr_b16 a[154:155], v76 offset:25376\n" \
  "ds_read_b64_tr_b16 a[156:157], v76 offset:36896\n" \
  "ds_read_b64_tr_b16 a[158:159], v76 offset:37664\n" \
  "ds_read_b64_tr_b16 a[232:233], v77 offset:24576\n" \
  "ds_read_b64_tr_b16 a[234:235], v77 offset:25344\n" \
  "ds_read_b64_tr_b16 a[236:237], v77 offset:36864\n" \
  "ds_read_b64_tr_b16 a[238:239], v77 offset:37632\n" \
  "s_waitcnt lgkmcnt(0)\n" \
  "s_waitcnt vmcnt(10) lgkmcnt(0)\n" \
  "s_barrier\n" \
  "s_mov_b32 s71, 81920\n" \
  "s_add_u32 m0, s57, 65536\n" \
  "s_nop 0\n" \
  "buffer_load_dwordx4 v50, s[44:47], s71 offen lds\n" \
  "s_add_u32 m0, s57, 66560\n" \
  "s_nop 0\n" \
  "buffer_load_dwordx4 v51, s[44:47], s71 offen lds\n" \
  "s_add_u32 m0, s57, 67584\n" \
  "s_nop 0\n" \
  "buffer_load_dwordx4 v52, s[44:47], s71 offen lds\n" \
  "s_add_u32 m0, s57, 68608\n" \
  "s_nop 0\n" \
  "buffer_load_dwordx4 v53, s[44:47], s71 offen lds\n" \
  "s_add_u32 m0, s57, 69632\n" \
  "s_nop 0\n" \
  "buffer_load_dwordx4 v54, s[44:47], s71 offen lds\n" \
  "s_add_u32 m0, s57, 70656\n" \
  "s_nop 0\n" \
  "buffer_load_dwordx4 v55, s[44:47], s71 offen lds\n" \
  "s_add_u32 m0, s57, 71680\n" \
  "s_nop 0\n" \
  "buffer_load_dwordx4 v56, s[44:47], s71 offen lds\n" \
  "s_add_u32 m0, s57, 72704\n" \
  "s_nop 0\n" \
  "buffer_load_dwordx4 v57, s[44:47], s71 offen lds\n" \
  "s_add_u32 m0, s57, 73728\n" \
  "s_nop 0\n" \
  "buffer_load_dwordx4 v58, s[44:47], s71 offen lds\n" \
  "s_add_u32 m0, s57, 74752\n" \
  "s_nop 0\n" \
  "buffer_load_dwordx4 v59, s[44:47], s71 offen lds\n" \
  "ds_read_b64_tr_b16 v[112:113], v82\n" \
  "ds_read_b64_tr_b16 v[114:115], v82 offset:2560\n" \
  "ds_read_b64_tr_b16 v[116:117], v82 offset:20480\n" \
  "ds_read_b64_tr_b16 v[118:119], v82 offset:23040\n" \
  "ds_read_b64_tr_b16 v[144:145], v82 offset:256\n" \
  "ds_read_b64_tr_b16 v[146:147], v82 offset:2816\n" \
  "ds_read_b64_tr_b16 v[148:149], v82 offset:20736\n" \
  "ds_read_b64_tr_b16 v[150:151], v82 offset:23296\n" \
  "s_waitcnt lgkmcnt(0)\n" \
  "ds_read_b64_tr_b16 v[176:177], v82 offset:512\n" \
  "ds_read_b64_tr_b16 v[178:179], v82 offset:3072\n" \
  "ds_read_b64_tr_b16 v[180:181], v82 offset:20992\n" \
  "ds_read_b64_tr_b16 v[182:183], v82 offset:23552\n" \
  "ds_read_b64_tr_b16 v[208:209], v82 offset:768\n" \
  "ds_read_b64_tr_b16 v[210:211], v82 offset:3328\n" \
  "ds_read_b64_tr_b16 v[212:213], v82 offset:21248\n" \
  "ds_read_b64_tr_b16 v[214:215], v82 offset:23808\n" \
  "s_waitcnt lgkmcnt(0)\n" \
  "ds_read_b64_tr_b16 v[240:241], v82 offset:1024\n" \
  "ds_read_b64_tr_b16 v[242:243], v82 offset:3584\n" \
  "ds_read_b64_tr_b16 v[244:245], v82 offset:21504\n" \
  "ds_read_b64_tr_b16 v[246:247], v82 offset:24064\n" \
  "s_waitcnt vmcnt(10) lgkmcnt(0)\n" \
  "s_barrier\n" \
  "s_mov_b32 s71, 122880\n" \
  "s_add_u32 m0, s57, 114688\n" \
  "s_nop 0\n" \
  "buffer_load_dwordx4 v50, s[44:47], s71 offen lds\n" \
  "s_add_u32 m0, s57, 115712\n" \
  "s_nop 0\n" \
  "buffer_load_dwordx4 v51, s[44:47], s71 offen lds\n" \
  "s_add_u32 m0, s57, 116736\n" \
  "s_nop 0\n" \
  "buffer_load_dwordx4 v52, s[44:47], s71 offen lds\n" \
  "s_add_u32 m0, s57, 117760\n" \
  "s_nop 0\n" \
  "buffer_load_dwordx4 v53, s[44:47], s71 offen lds\n" \
  "s_add_u32 m0, s57, 118784\n" \
  "s_nop 0\n" \
  "buffer_load_dwordx4 v54, s[44:47], s71 offen lds\n" \
  "s_add_u32 m0, s57, 119808\n" \
  "s_nop 0\n" \
  "buffer_load_dwordx4 v55, s[44:47], s71 offen lds\n" \
  "s_add_u32 m0, s57, 120832\n" \
  "s_nop 0\n" \
  "buffer_load_dwordx4 v56, s[44:47], s71 offen lds\n" \
  "s_add_u32 m0, s57, 121856\n" \
  "s_nop 0\n" \
  "buffer_load_dwordx4 v57, s[44:47], s71 offen lds\n" \
  "s_add_u32 m0, s57, 122880\n" \
  "s_nop 0\n" \
  "buffer_load_dwordx4 v58, s[44:47], s71 offen lds\n" \
  "s_add_u32 m0, s57, 123904\n" \
  "s_nop 0\n" \
  "buffer_load_dwordx4 v59, s[44:47], s71 offen lds\n" \
  "ds_read_b64_tr_b16 v[120:121], v74\n" \
  "ds_read_b64_tr_b16 v[122:123], v74 offset:2560\n" \
  "ds_read_b64_tr_b16 v[124:125], v74 offset:20480\n" \
  "ds_read_b64_tr_b16 v[126:127], v74 offset:23040\n" \
  "ds_read_b64_tr_b16 v[152:153], v74 offset:256\n" \
  "ds_read_b64_tr_b16 v[154:155], v74 offset:2816\n" \
  "ds_read_b64_tr_b16 v[156:157], v74 offset:20736\n" \
  "ds_read_b64_tr_b16 v[158:159], v74 offset:23296\n" \
  "s_waitcnt lgkmcnt(0)\n" \
  "ds_read_b64_tr_b16 v[184:185], v74 offset:512\n" \
  "ds_read_b64_tr_b16 v[186:187], v74 offset:3072\n" \
  "ds_read_b64_tr_b16 v[188:189], v74 offset:20992\n" \
  "ds_read_b64_tr_b16 v[190:191], v74 offset:23552\n" \
  "ds_read_b64_tr_b16 v[216:217], v74 offset:768\n" \
  "ds_read_b64_tr_b16 v[218:219], v74 offset:3328\n" \
  "ds_read_b64_tr_b16 v[220:221], v74 offset:21248\n" \
  "ds_read_b64_tr_b16 v[222:223], v74 offset:23808\n" \
  "s_waitcnt lgkmcnt(0)\n" \
  "ds_read_b64_tr_b16 v[248:249], v74 offset:1024\n" \
  "ds_read_b64_tr_b16 v[250:251], v74 offset:3584\n" \
  "ds_read_b64_tr_b16 v[252:253], v74 offset:21504\n" \
  "ds_read_b64_tr_b16 v[254:255], v74 offset:24064\n" \
  "s_waitcnt vmcnt(10) lgkmcnt(0)\n" \
  "s_barrier\n" \
  "s_mov_b32 s71, 163840\n" \
  "s_add_u32 m0, s57, 16384\n" \
  "s_nop 0\n" \
  "buffer_load_dwordx4 v50, s[44:47], s71 offen lds\n" \
  "s_add_u32 m0, s57, 17408\n" \
  "s_nop 0\n" \
  "buffer_load_dwordx4 v51, s[44:47], s71 offen lds\n" \
  "s_add_u32 m0, s57, 18432\n" \
  "s_nop 0\n" \
  "buffer_load_dwordx4 v52, s[44:47], s71 offen lds\n" \
  "s_add_u32 m0, s57, 19456\n" \
  "s_nop 0\n" \
  "buffer_load_dwordx4 v53, s[44:47], s71 offen lds\n" \
  "s_add_u32 m0, s57, 20480\n" \
  "s_nop 0\n" \
  "buffer_load_dwordx4 v54, s[44:47], s71 offen lds\n" \
  "s_add_u32 m0, s57, 21504\n" \
  "s_nop 0\n" \
  "buffer_load_dwordx4 v55, s[44:47], s71 offen lds\n" \
  "s_add_u32 m0, s57, 22528\n" \
  "s_nop 0\n" \
  "buffer_load_dwordx4 v56, s[44:47], s71 offen lds\n" \
  "s_add_u32 m0, s57, 23552\n" \
  "s_nop 0\n" \
  "buffer_load_dwordx4 v57, s[44:47], s71 offen lds\n" \
  "s_add_u32 m0, s57, 24576\n" \
  "s_nop 0\n" \
  "buffer_load_dwordx4 v58, s[44:47], s71 offen lds\n" \
  "s_add_u32 m0, s57, 25600\n" \
  "s_nop 0\n" \
  "buffer_load_dwordx4 v59, s[44:47], s71 offen lds\n" \
  "ds_read_b64_tr_b16 v[128:129], v78\n" \
  "ds_read_b64_tr_b16 v[130:131], v78 offset:2560\n" \
  "ds_read_b64_tr_b16 v[132:133], v78 offset:20480\n" \
  "ds_read_b64_tr_b16 v[134:135], v78 offset:23040\n" \
  "ds_read_b64_tr_b16 v[160:161], v78 offset:256\n" \
  "ds_read_b64_tr_b16 v[162:163], v78 offset:2816\n" \
  "ds_read_b64_tr_b16 v[164:165], v78 offset:20736\n" \
  "ds_read_b64_tr_b16 v[166:167], v78 offset:23296\n" \
  "s_waitcnt lgkmcnt(0)\n" \
  "ds_read_b64_tr_b16 v[192:193], v78 offset:512\n" \
  "ds_read_b64_tr_b16 v[194:195], v78 offset:3072\n" \
  "ds_read_b64_tr_b16 v[196:197], v78 offset:20992\n" \
  "ds_read_b64_tr_b16 v[198:199], v78 offset:23552\n" \
  "ds_read_b64_tr_b16 v[224:225], v78 offset:768\n" \
  "ds_read_b64_tr_b16 v[226:227], v78 offset:3328\n" \
  "ds_read_b64_tr_b16 v[228:229], v78 offset:21248\n" \
  "ds_read_b64_tr_b16 v[230:231], v78 offset:23808\n" \
  "s_waitcnt lgkmcnt(0)\n" \
  "ds_read_b64_tr_b16 a[240:241], v78 offset:1024\n" \
  "ds_read_b64_tr_b16 a[242:243], v78 offset:3584\n" \
  "ds_read_b64_tr_b16 a[244:245], v78 offset:21504\n" \
  "ds_read_b64_tr_b16 a[246:247], v78 offset:24064\n" \
  "s_waitcnt vmcnt(10) lgkmcnt(0)\n" \
  "s_barrier\n" \
  "s_mov_b32 s71, 204800\n" \
  "s_add_u32 m0, s57, 65536\n" \
  "s_nop 0\n" \
  "buffer_load_dwordx4 v50, s[44:47], s71 offen lds\n" \
  "s_add_u32 m0, s57, 66560\n" \
  "s_nop 0\n" \
  "buffer_load_dwordx4 v51, s[44:47], s71 offen lds\n" \
  "s_add_u32 m0, s57, 67584\n" \
  "s_nop 0\n" \
  "buffer_load_dwordx4 v52, s[44:47], s71 offen lds\n" \
  "s_add_u32 m0, s57, 68608\n" \
  "s_nop 0\n" \
  "buffer_load_dwordx4 v53, s[44:47], s71 offen lds\n" \
  "s_add_u32 m0, s57, 69632\n" \
  "s_nop 0\n" \
  "buffer_load_dwordx4 v54, s[44:47], s71 offen lds\n" \
  "s_add_u32 m0, s57, 70656\n" \
  "s_nop 0\n" \
  "buffer_load_dwordx4 v55, s[44:47], s71 offen lds\n" \
  "s_add_u32 m0, s57, 71680\n" \
  "s_nop 0\n" \
  "buffer_load_dwordx4 v56, s[44:47], s71 offen lds\n" \
  "s_add_u32 m0, s57, 72704\n" \
  "s_nop 0\n" \
  "buffer_load_dwordx4 v57, s[44:47], s71 offen lds\n" \
  "s_add_u32 m0, s57, 73728\n" \
  "s_nop 0\n" \
  "buffer_load_dwordx4 v58, s[44:47], s71 offen lds\n" \
  "s_add_u32 m0, s57, 74752\n" \
  "s_nop 0\n" \
  "buffer_load_dwordx4 v59, s[44:47], s71 offen lds\n" \
  "ds_read_b64_tr_b16 v[136:137], v82\n" \
  "ds_read_b64_tr_b16 v[138:139], v82 offset:2560\n" \
  "ds_read_b64_tr_b16 v[140:141], v82 offset:20480\n" \
  "ds_read_b64_tr_b16 v[142:143], v82 offset:23040\n" \
  "ds_read_b64_tr_b16 v[168:169], v82 offset:256\n" \
  "ds_read_b64_tr_b16 v[170:171], v82 offset:2816\n" \
  "ds_read_b64_tr_b16 v[172:173], v82 offset:20736\n" \
  "ds_read_b64_tr_b16 v[174:175], v82 offset:23296\n" \
  "s_waitcnt lgkmcnt(0)\n" \
  "ds_read_b64_tr_b16 v[200:201], v82 offset:512\n" \
  "ds_read_b64_tr_b16 v[202:203], v82 offset:3072\n" \
  "ds_read_b64_tr_b16 v[204:205], v82 offset:20992\n" \
  "ds_read_b64_tr_b16 v[206:207], v82 offset:23552\n" \
  "ds_read_b64_tr_b16 v[232:233], v82 offset:768\n" \
  "ds_read_b64_tr_b16 v[234:235], v82 offset:3328\n" \
  "ds_read_b64_tr_b16 v[236:237], v82 offset:21248\n" \
  "ds_read_b64_tr_b16 v[238:239], v82 offset:23808\n" \
  "s_waitcnt lgkmcnt(0)\n" \
  "ds_read_b64_tr_b16 a[248:249], v82 offset:1024\n" \
  "ds_read_b64_tr_b16 a[250:251], v82 offset:3584\n" \
  "ds_read_b64_tr_b16 a[252:253], v82 offset:21504\n" \
  "ds_read_b64_tr_b16 a[254:255], v82 offset:24064\n" \
  "s_waitcnt vmcnt(10) lgkmcnt(0)\n" \
  "s_barrier\n" \
  "s_waitcnt vmcnt(0) lgkmcnt(0)\n" \
  "s_barrier\n" \
  "s_waitcnt lgkmcnt(0)\n" \
  "s_barrier\n" \
  "LIV_%=:\n" \
  "buffer_load_dword v92, v69, s[36:39], 0 offen sc1\n" \
  "s_waitcnt vmcnt(0)\n" \
  "v_and_b32 v93, -4, v92\n" \
  "v_cmp_ne_u32 vcc, s58, v93\n" \
  "s_cbranch_vccz LIXV_%=\n" \
  "s_add_u32 s60, s60, 1\n" \
  "s_cmp_gt_u32 s60, 20000\n" \
  "s_cbranch_scc1 LIXV_%=\n" \
  "s_sleep 8\n" \
  "s_branch LIV_%=\n" \
  "LIXV_%=:\n" \
  "buffer_atomic_add v32, v68, s[36:39], 0 offen\n" \
  "v_and_b32 v92, v70, v92\n" \
  "v_cmp_ne_u32 s[80:81], 0, v92\n" \
  "s_mov_b32 s78, 0x55555555\n" \
  "s_mov_b32 s79, 0x55555555\n" \
  "s_xor_b64 s[82:83], s[80:81], s[78:79]\n" \
  "s_bcnt1_i32_b64 s71, s[82:83]\n" \
  "s_cmp_le_u32 s71, 26\n" \
  "s_cselect_b64 s[80:81], s[78:79], s[80:81]\n" \
  "s_not_b64 s[78:79], s[78:79]\n" \
  "s_cmp_ge_u32 s71, 38\n" \
  "s_cselect_b64 s[80:81], s[78:79], s[80:81]\n" \
  "v_lshrrev_b32 v92, 21, v25\n" \
  "v_lshrrev_b64 v[92:93], v92, s[80:81]\n" \
  "v_and_b32 v92, 1, v92\n" \
  "v_lshlrev_b32 v92, 13, v92\n" \
  "v_xor_b32 v25, v92, v25\n" \
  "v_lshrrev_b32 v92, 21, v26\n" \
  "v_lshrrev_b64 v[92:93], v92, s[80:81]\n" \
  "v_and_b32 v92, 1, v92\n" \
  "v_lshlrev_b32 v92, 13, v92\n" \
  "v_xor_b32 v26, v92, v26\n" \
  "v_lshrrev_b32 v92, 21, v27\n" \
  "v_lshrrev_b64 v[92:93], v92, s[80:81]\n" \
  "v_and_b32 v92, 1, v92\n" \
  "v_lshlrev_b32 v92, 13, v92\n" \
  "v_xor_b32 v27, v92, v27\n" \
  "v_lshrrev_b32 v92, 21, v28\n" \
  "v_lshrrev_b64 v[92:93], v92, s[80:81]\n" \
  "v_and_b32 v92, 1, v92\n" \
  "v_lshlrev_b32 v92, 13, v92\n" \
  "v_xor_b32 v28, v92, v28\n" \
  "v_lshrrev_b32 v92, 21, v29\n" \
  "v_lshrrev_b64 v[92:93], v92, s[80:81]\n" \
  "v_and_b32 v92, 1, v92\n" \
  "v_lshlrev_b32 v92, 13, v92\n" \
  "v_xor_b32 v29, v92, v29\n" \
  "v_lshrrev_b32 v92, 21, v30\n" \
  "v_lshrrev_b64 v[92:93], v92, s[80:81]\n" \
  "v_and_b32 v92, 1, v92\n" \
  "v_lshlrev_b32 v92, 13, v92\n" \
  "v_xor_b32 v30, v92, v30\n" \
  "v_lshrrev_b32 v92, 21, v67\n" \
  "v_lshrrev_b64 v[92:93], v92, s[80:81]\n" \
  "v_and_b32 v92, 1, v92\n" \
  "v_lshlrev_b32 v92, 13, v92\n" \
  "v_xor_b32 v67, v92, v67\n" \
  "v_lshrrev_b32 v92, 21, v71\n" \
  "v_lshrrev_b64 v[92:93], v92, s[80:81]\n" \
  "v_and_b32 v92, 1, v92\n" \
  "v_lshlrev_b32 v92, 13, v92\n" \
  "v_xor_b32 v71, v92, v71\n" \
  "s_lshr_b32 s71, s54, 21\n" \
  "s_lshr_b64 s[80:81], s[80:81], s71\n" \
  "s_and_b32 s71, s80, 1\n" \
  "s_lshl_b32 s71, s71, 13\n" \
  "s_or_b32 s54, s54, s71\n" \
  "s_and_b32 s71, s48, 3\n" \
  "s_cmp_eq_u32 s71, 3\n" \
  "s_cbranch_scc0 LIYV_%=\n" \
  "v_mov_b32 v1, v71\n" \
  "LIYV_%=:\n" \
  "v_mov_b32 v104, v64\n" \
  "v_mov_b32 v105, v65\n" \
  "v_mov_b32 v106, v66\n" \
  "v_mov_b32 v107, v67\n" \
  "v_mov_b32 v28, v25\n" \
  "v_mov_b32 v35, v26\n" \
  "v_mov_b32 v88, v74\n" \
  "v_mov_b32 v89, v78\n" \
  "ds_read_b64_tr_b16 v[40:41], v88\n" \
  "ds_read_b64_tr_b16 v[42:43], v88 offset:2560\n" \
  "ds_read_b64_tr_b16 v[44:45], v88 offset:20480\n" \
  "ds_read_b64_tr_b16 v[46:47], v88 offset:23040\n" \
  "ds_read_b64_tr_b16 v[48:49], v88 offset:256\n" \
  "ds_read_b64_tr_b16 v[50:51], v88 offset:2816\n" \
  "ds_read_b64_tr_b16 v[52:53], v88 offset:20736\n" \
  "ds_read_b64_tr_b16 v[54:55], v88 offset:23296\n" \
  "s_waitcnt lgkmcnt(0)\n" \
  "ds_read_b64_tr_b16 v[56:57], v88 offset:512\n" \
  "ds_read_b64_tr_b16 v[58:59], v88 offset:3072\n" \
  "ds_read_b64_tr_b16 v[60:61], v88 offset:20992\n" \
  "ds_read_b64_tr_b16 v[62:63], v88 offset:23552\n" \
  "ds_read_b64_tr_b16 v[64:65], v88 offset:768\n" \
  "ds_read_b64_tr_b16 v[66:67], v88 offset:3328\n" \
  "ds_read_b64_tr_b16 v[68:69], v88 offset:21248\n" \
  "ds_read_b64_tr_b16 v[70:71], v88 offset:23808\n" \
  "s_waitcnt lgkmcnt(0)\n" \
  "ds_read_b64_tr_b16 v[72:73], v88 offset:1024\n" \
  "ds_read_b64_tr_b16 v[74:75], v88 offset:3584\n" \
  "ds_read_b64_tr_b16 v[76:77], v88 offset:21504\n" \
  "ds_read_b64_tr_b16 v[78:79], v88 offset:24064\n" \
  "s_waitcnt lgkmcnt(0)\n" \
  "s_barrier\n" \
  "ds_write_b128 v11, v[40:43] offset:0\n" \
  "ds_write_b128 v11, v[44:47] offset:1024\n" \
  "ds_write_b128 v11, v[48:51] offset:2048\n" \
  "ds_write_b128 v11, v[52:55] offset:3072\n" \
  "ds_write_b128 v11, v[56:59] offset:4096\n" \
  "ds_write_b128 v11, v[60:63] offset:5120\n" \
  "ds_write_b128 v11, v[64:67] offset:6144\n" \
  "ds_write_b128 v11, v[68:71] offset:7168\n" \
  "ds_write_b128 v11, v[72:75] offset:8192\n" \
  "ds_write_b128 v11, v[76:79] offset:9216\n" \
  "s_waitcnt lgkmcnt(0)\n" \
  "ds_read_b64_tr_b16 v[40:41], v89\n" \
  "ds_read_b64_tr_b16 v[42:43], v89 offset:2560\n" \
  "ds_read_b64_tr_b16 v[44:45], v89 offset:20480\n" \
  "ds_read_b64_tr_b16 v[46:47], v89 offset:23040\n" \
  "ds_read_b64_tr_b16 v[48:49], v89 offset:256\n" \
  "ds_read_b64_tr_b16 v[50:51], v89 offset:2816\n" \
  "ds_read_b64_tr_b16 v[52:53], v89 offset:20736\n" \
  "ds_read_b64_tr_b16 v[54:55], v89 offset:23296\n" \
  "s_waitcnt lgkmcnt(0)\n" \
  "ds_read_b64_tr_b16 v[56:57], v89 offset:512\n" \
  "ds_read_b64_tr_b16 v[58:59], v89 offset:3072\n" \
  "ds_read_b64_tr_b16 v[60:61], v89 offset:20992\n" \
  "ds_read_b64_tr_b16 v[62:63], v89 offset:23552\n" \
  "ds_read_b64_tr_b16 v[64:65], v89 offset:768\n" \
  "ds_read_b64_tr_b16 v[66:67], v89 offset:3328\n" \
  "ds_read_b64_tr_b16 v[68:69], v89 offset:21248\n" \
  "ds_read_b64_tr_b16 v[70:71], v89 offset:23808\n" \
  "s_waitcnt lgkmcnt(0)\n" \
  "ds_read_b64_tr_b16 v[72:73], v89 offset:1024\n" \
  "ds_read_b64_tr_b16 v[74:75], v89 offset:3584\n" \
  "ds_read_b64_tr_b16 v[76:77], v89 offset:21504\n" \
  "ds_read_b64_tr_b16 v[78:79], v89 offset:24064\n" \
  "s_waitcnt lgkmcnt(0)\n" \
  "s_barrier\n" \
  "ds_write_b128 v11, v[40:43] offset:40960\n" \
  "ds_write_b128 v11, v[44:47] offset:41984\n" \
  "ds_write_b128 v11, v[48:51] offset:43008\n" \
  "ds_write_b128 v11, v[52:55] offset:44032\n" \
  "ds_write_b128 v11, v[56:59] offset:45056\n" \
  "ds_write_b128 v11, v[60:63] offset:46080\n" \
  "ds_write_b128 v11, v[64:67] offset:47104\n" \
  "ds_write_b128 v11, v[68:71] offset:48128\n" \
  "ds_write_b128 v11, v[72:75] offset:49152\n" \
  "ds_write_b128 v11, v[76:79] offset:50176\n" \
  "s_waitcnt lgkmcnt(0)\n" \
  "s_barrier\n" \
  "LSW_%=:\n" \
  "s_memrealtime s[74:75]\n" \
  "s_waitcnt vmcnt(0) lgkmcnt(0)\n" \
  "s_mov_b32 s63, s74\n" \
  "s_and_b32 s71, s48, 3\n" \
  "s_cmp_eq_u32 s71, 2\n" \
  "s_cbranch_scc0 LHB_%=\n" \
  "s_cmp_eq_u32 s49, 0\n" \
  "s_cbranch_scc0 LHA_%=\n" \
  "LOA_%=:\n" \
  "buffer_load_dword v36, v104, s[36:39], 0 offen sc1\n" \
  "s_waitcnt vmcnt(0)\n" \
  "v_cmp_gt_u32 vcc, 96, v36\n" \
  "s_cbranch_vccz LOXA_%=\n" \
  "s_add_u32 s60, s60, 1\n" \
  "s_cmp_gt_u32 s60, 20000\n" \
  "s_cbranch_scc1 LOXA_%=\n" \
  "s_sleep 4\n" \
  "s_branch LOA_%=\n" \
  "LOXA_%=:\n" \
  "LOB_%=:\n" \
  "buffer_load_dword v36, v107, s[36:39], 0 offen sc1\n" \
  "s_waitcnt vmcnt(0)\n" \
  "v_cmp_gt_u32 vcc, 8, v36\n" \
  "s_cbranch_vccz LOXB_%=\n" \
  "s_add_u32 s60, s60, 1\n" \
  "s_cmp_gt_u32 s60, 20000\n" \
  "s_cbranch_scc1 LOXB_%=\n" \
  "s_sleep 2\n" \
  "s_branch LOB_%=\n" \
  "LOXB_%=:\n" \
  "v_cmp_gt_u32 vcc, 16, v36\n" \
  "s_cmp_eq_u64 vcc, 0\n" \
  "s_cselect_b32 s53, 135, s53\n" \
  "s_mov_b32 s95, s62\n" \
  "s_mov_b32 s96, s86\n" \
  "s_memrealtime s[74:75]\n" \
  "s_memtime s[76:77]\n" \
  "s_waitcnt lgkmcnt(0)\n" \
  "s_sub_u32 s71, s76, s96\n" \
  "s_sub_u32 s72, s74, s95\n" \
  "v_cvt_f32_u32 v36, s71\n" \
  "v_cvt_f32_u32 v37, s72\n" \
  "v_rcp_f32 v37, v37\n" \
  "v_mul_f32 v36, 0x43800000, v36\n" \
  "v_mul_f32 v36, v36, v37\n" \
  "v_cvt_u32_f32 v36, v36\n" \
  "s_nop 0\n" \
  "v_readfirstlane_b32 s71, v36\n" \
  "s_min_u32 s90, s71, 5950\n" \
  "v_cvt_f32_u32 v36, s90\n" \
  "v_rcp_f32 v36, v36\n" \
  "v_mov_b32 v37, 0xc0b00000\n" \
  "v_fmac_f32 v37, 0x48cc6500, v36\n" \
  "v_cvt_u32_f32 v37, v37\n" \
  "v_lshl_or_b32 v37, v37, 8, 1\n" \
  "buffer_store_dword v37, v30, s[36:39], 0 offen sc1\n" \
  "s_add_u32 s71, s74, s53\n" \
  "s_or_b32 s71, s71, 1\n" \
  "v_mov_b32 v36, s71\n" \
  "buffer_store_dword v36, v106, s[36:39], 0 offen sc1\n" \
  "s_nop 1\n" \
  "LHA_%=:\n" \
  "LOC_%=:\n" \
  "buffer_load_dword v104, v105, s[36:39], 0 offen sc1\n" \
  "s_waitcnt vmcnt(0)\n" \
  "v_cmp_gt_u32 vcc, 1, v104\n" \
  "s_cbranch_vccz LOXC_%=\n" \
  "s_add_u32 s60, s60, 1\n" \
  "s_cmp_gt_u32 s60, 20000\n" \
  "s_cbranch_scc1 LOXC_%=\n" \
  "s_sleep 4\n" \
  "s_branch LOC_%=\n" \
  "LOXC_%=:\n" \
  "s_mov_b32 s95, s62\n" \
  "s_mov_b32 s96, s86\n" \
  "s_memrealtime s[74:75]\n" \
  "s_memtime s[76:77]\n" \
  "s_waitcnt lgkmcnt(0)\n" \
  "s_sub_u32 s71, s76, s96\n" \
  "s_sub_u32 s72, s74, s95\n" \
  "v_cvt_f32_u32 v36, s71\n" \
  "v_cvt_f32_u32 v37, s72\n" \
  "v_rcp_f32 v37, v37\n" \
  "v_mul_f32 v36, 0x43800000, v36\n" \
  "v_mul_f32 v36, v36, v37\n" \
  "v_cvt_u32_f32 v36, v36\n" \
  "s_nop 0\n" \
  "v_readfirstlane_b32 s71, v36\n" \
  "s_min_u32 s90, s71, 5950\n" \
  "v_readfirstlane_b32 s88, v104\n" \
  "s_sub_u32 s71, s88, s74\n" \
  "s_add_u32 s71, s71, 100000\n" \
  "s_cmp_gt_u32 s71, 200000\n" \
  "s_cselect_b32 s88, s74, s88\n" \
  "s_mov_b32 s62, s88\n" \
  "LHD_%=:\n" \
  "s_memrealtime s[74:75]\n" \
  "s_waitcnt lgkmcnt(0)\n" \
  "s_sub_i32 s71, s88, s74\n" \
  "s_cmp_gt_i32 s71, 75\n" \
  "s_cbranch_scc1 LHD_%=\n" \
  "LOD_%=:\n" \
  "buffer_load_dword v36, v107, s[36:39], 0 offen sc1\n" \
  "s_waitcnt vmcnt(0)\n" \
  "v_cmp_gt_u32 vcc, 16, v36\n" \
  "s_cbranch_vccz LOXD_%=\n" \
  "s_add_u32 s60, s60, 1\n" \
  "s_cmp_gt_u32 s60, 20000\n" \
  "s_cbranch_scc1 LOXD_%=\n" \
  "s_sleep 2\n" \
  "s_branch LOD_%=\n" \
  "LOXD_%=:\n" \
  "LHB_%=:\n" \
  "s_barrier\n" \
  "s_branch LPASS_%=\n" \
  "LFIN_%=:\n" \
  "s_waitcnt vmcnt(0) lgkmcnt(0)\n" \
  "s_barrier\n" \
  "s_and_b32 s71, s48, 3\n" \
  "s_cmp_eq_u32 s71, 3\n" \
  "s_cbranch_scc0 LEND_%=\n" \
  "v_mov_b32 v34, 64\n" \
  "buffer_store_dword v34, v1, s[36:39], 0 offen sc1\n" \
  "LEND_%=:\n" \
  "s_mov_b32 s57, s35\n"
#define PROJ_CLOBBERS \
  "v9", "v10", "v11", "v12", "v13", "v14", "v15", "v16", "v17", "v18", "v19", "v20", "v21", "v22", "v23", "v24", "v25", "v26", "v27", "v28", "v29", \
  "v30", "v31", "v32", "v33", "v34", "v35", "v36", "v37", "v38", "v39", "v40", "v41", "v42", "v43", "v44", "v45", "v46", "v47", "v48", "v49", "v50", \
  "v51", "v52", "v53", "v54", "v55", "v56", "v57", "v58", "v59", "v60", "v61", "v62", "v63", "v64", "v65", "v66", "v67", "v68", "v69", "v70", "v71", \
  "v72", "v73", "v74", "v75", "v76", "v77", "v78", "v79", "v100", "v101", "v102", "v103", "v104", "v105", "v106", "v107", "v108", "v109", "v110", \
  "v111", "v112", "v113", "v114", "v115", "v116", "v117", "v118", "v119", "v120", "v121", "v122", "v123", "s68", "s69", "s70", "s71", "s72"
#define PROJ_ASM \
  "s_waitcnt vmcnt(10)\n" \
  "s_barrier\n" \
  "ds_read_b128 v[32:35], v0 offset:0\n" \
  "ds_read_b128 v[36:39], v0 offset:20480\n" \
  "ds_read_b128 v[40:43], v0 offset:1024\n" \
  "ds_read_b128 v[44:47], v0 offset:21504\n" \
  "v_mov_b32 v11, 1\n" \
  "v_add_u32 v9, 40960, v0\n" \
  "v_add_u32 v10, 81920, v0\n" \
  "s_mul_i32 s68, s52, 40960\n" \
  "s_add_u32 s68, s68, 81920\n" \
  "s_mov_b32 s72, s52\n" \
  "s_waitcnt lgkmcnt(2)\n" \
  "v_mfma_f32_16x16x32_bf16 v[56:59], a[0:3], v[32:35], 0\n" \
  "ds_read_b128 v[48:51], v0 offset:2048\n" \
  "v_mfma_f32_16x16x32_bf16 v[68:71], a[0:3], v[36:39], 0\n" \
  "ds_read_b128 v[52:55], v0 offset:22528\n" \
  "v_mfma_f32_16x16x32_bf16 v[60:63], a[80:83], v[32:35], 0\n" \
  "v_mfma_f32_16x16x32_bf16 v[72:75], a[80:83], v[36:39], 0\n" \
  "v_mfma_f32_16x16x32_bf16 v[64:67], a[160:163], v[32:35], 0\n" \
  "v_mfma_f32_16x16x32_bf16 v[76:79], a[160:163], v[36:39], 0\n" \
  "s_waitcnt lgkmcnt(2)\n" \
  "v_mfma_f32_16x16x32_bf16 v[56:59], a[4:7], v[40:43], v[56:59]\n" \
  "ds_read_b128 v[32:35], v0 offset:3072\n" \
  "v_mfma_f32_16x16x32_bf16 v[68:71], a[4:7], v[44:47], v[68:71]\n" \
  "ds_read_b128 v[36:39], v0 offset:23552\n" \
  "v_mfma_f32_16x16x32_bf16 v[60:63], a[84:87], v[40:43], v[60:63]\n" \
  "v_mfma_f32_16x16x32_bf16 v[72:75], a[84:87], v[44:47], v[72:75]\n" \
  "v_mfma_f32_16x16x32_bf16 v[64:67], a[164:167], v[40:43], v[64:67]\n" \
  "v_mfma_f32_16x16x32_bf16 v[76:79], a[164:167], v[44:47], v[76:79]\n" \
  "s_waitcnt lgkmcnt(2)\n" \
  "v_mfma_f32_16x16x32_bf16 v[56:59], a[8:11], v[48:51], v[56:59]\n" \
  "ds_read_b128 v[40:43], v0 offset:4096\n" \
  "v_mfma_f32_16x16x32_bf16 v[68:71], a[8:11], v[52:55], v[68:71]\n" \
  "ds_read_b128 v[44:47], v0 offset:24576\n" \
  "v_mfma_f32_16x16x32_bf16 v[60:63], a[88:91], v[48:51], v[60:63]\n" \
  "v_mfma_f32_16x16x32_bf16 v[72:75], a[88:91], v[52:55], v[72:75]\n" \
  "v_mfma_f32_16x16x32_bf16 v[64:67], a[168:171], v[48:51], v[64:67]\n" \
  "v_mfma_f32_16x16x32_bf16 v[76:79], a[168:171], v[52:55], v[76:79]\n" \
  "s_waitcnt lgkmcnt(2)\n" \
  "v_mfma_f32_16x16x32_bf16 v[56:59], a[12:15], v[32:35], v[56:59]\n" \
  "ds_read_b128 v[48:51], v0 offset:5120\n" \
  "v_mfma_f32_16x16x32_bf16 v[68:71], a[12:15], v[36:39], v[68:71]\n" \
  "ds_read_b128 v[52:55], v0 offset:25600\n" \
  "v_mfma_f32_16x16x32_bf16 v[60:63], a[92:95], v[32:35], v[60:63]\n" \
  "v_mfma_f32_16x16x32_bf16 v[72:75], a[92:95], v[36:39], v[72:75]\n" \
  "v_mfma_f32_16x16x32_bf16 v[64:67], a[172:175], v[32:35], v[64:67]\n" \
  "v_mfma_f32_16x16x32_bf16 v[76:79], a[172:175], v[36:39], v[76:79]\n" \
  "s_waitcnt lgkmcnt(2)\n" \
  "v_mfma_f32_16x16x32_bf16 v[56:59], a[16:19], v[40:43], v[56:59]\n" \
  "ds_read_b128 v[32:35], v0 offset:6144\n" \
  "v_mfma_f32_16x16x32_bf16 v[68:71], a[16:19], v[44:47], v[68:71]\n" \
  "ds_read_b128 v[36:39], v0 offset:26624\n" \
  "v_mfma_f32_16x16x32_bf16 v[60:63], a[96:99], v[40:43], v[60:63]\n" \
  "s_add_u32 s70, s68, 4096\n" \
  "v_mfma_f32_16x16x32_bf16 v[72:75], a[96:99], v[44:47], v[72:75]\n" \
  "s_add_u32 s71, s68, 8192\n" \
  "v_mfma_f32_16x16x32_bf16 v[64:67], a[176:179], v[40:43], v[64:67]\n" \
  "s_add_u32 m0, s53, 81920\n" \
  "v_mfma_f32_16x16x32_bf16 v[76:79], a[176:179], v[44:47], v[76:79]\n" \
  "buffer_load_dwordx4 v1, s[36:39], s68 offen sc1 lds\n" \
  "s_waitcnt lgkmcnt(2)\n" \
  "v_mfma_f32_16x16x32_bf16 v[56:59], a[20:23], v[48:51], v[56:59]\n" \
  "ds_read_b128 v[40:43], v0 offset:7168\n" \
  "v_mfma_f32_16x16x32_bf16 v[68:71], a[20:23], v[52:55], v[68:71]\n" \
  "ds_read_b128 v[44:47], v0 offset:27648\n" \
  "v_mfma_f32_16x16x32_bf16 v[60:63], a[100:103], v[48:51], v[60:63]\n" \
  "buffer_load_dwordx4 v1, s[36:39], s68 offen offset:1024 sc1 lds\n" \
  "v_mfma_f32_16x16x32_bf16 v[72:75], a[100:103], v[52:55], v[72:75]\n" \
  "v_mfma_f32_16x16x32_bf16 v[64:67], a[180:183], v[48:51], v[64:67]\n" \
  "v_mfma_f32_16x16x32_bf16 v[76:79], a[180:183], v[52:55], v[76:79]\n" \
  "s_waitcnt lgkmcnt(2)\n" \
  "v_mfma_f32_16x16x32_bf16 v[56:59], a[24:27], v[32:35], v[56:59]\n" \
  "ds_read_b128 v[48:51], v0 offset:8192\n" \
  "v_mfma_f32_16x16x32_bf16 v[68:71], a[24:27], v[36:39], v[68:71]\n" \
  "ds_read_b128 v[52:55], v0 offset:28672\n" \
  "v_mfma_f32_16x16x32_bf16 v[60:63], a[104:107], v[32:35], v[60:63]\n" \
  "buffer_load_dwordx4 v1, s[36:39], s68 offen offset:2048 sc1 lds\n" \
  "v_mfma_f32_16x16x32_bf16 v[72:75], a[104:107], v[36:39], v[72:75]\n" \
  "v_mfma_f32_16x16x32_bf16 v[64:67], a[184:187], v[32:35], v[64:67]\n" \
  "v_mfma_f32_16x16x32_bf16 v[76:79], a[184:187], v[36:39], v[76:79]\n" \
  "s_waitcnt lgkmcnt(2)\n" \
  "v_mfma_f32_16x16x32_bf16 v[56:59], a[28:31], v[40:43], v[56:59]\n" \
  "ds_read_b128 v[32:35], v0 offset:9216\n" \
  "v_mfma_f32_16x16x32_bf16 v[68:71], a[28:31], v[44:47], v[68:71]\n" \
  "ds_read_b128 v[36:39], v0 offset:29696\n" \
  "v_mfma_f32_16x16x32_bf16 v[60:63], a[108:111], v[40:43], v[60:63]\n" \
  "buffer_load_dwordx4 v1, s[36:39], s68 offen offset:3072 sc1 lds\n" \
  "v_mfma_f32_16x16x32_bf16 v[72:75], a[108:111], v[44:47], v[72:75]\n" \
  "v_mfma_f32_16x16x32_bf16 v[64:67], a[188:191], v[40:43], v[64:67]\n" \
  "v_mfma_f32_16x16x32_bf16 v[76:79], a[188:191], v[44:47], v[76:79]\n" \
  "s_waitcnt lgkmcnt(2)\n" \
  "v_mfma_f32_16x16x32_bf16 v[56:59], a[32:35], v[48:51], v[56:59]\n" \
  "ds_read_b128 v[40:43], v0 offset:10240\n" \
  "v_mfma_f32_16x16x32_bf16 v[68:71], a[32:35], v[52:55], v[68:71]\n" \
  "ds_read_b128 v[44:47], v0 offset:30720\n" \
  "v_mfma_f32_16x16x32_bf16 v[60:63], a[112:115], v[48:51], v[60:63]\n" \
  "s_add_u32 m0, s53, 86016\n" \
  "v_mfma_f32_16x16x32_bf16 v[72:75], a[112:115], v[52:55], v[72:75]\n" \
  "buffer_load_dwordx4 v1, s[36:39], s70 offen sc1 lds\n" \
  "v_mfma_f32_16x16x32_bf16 v[64:67], a[192:195], v[48:51], v[64:67]\n" \
  "v_mfma_f32_16x16x32_bf16 v[76:79], a[192:195], v[52:55], v[76:79]\n" \
  "s_waitcnt lgkmcnt(2)\n" \
  "v_mfma_f32_16x16x32_bf16 v[56:59], a[36:39], v[32:35], v[56:59]\n" \
  "ds_read_b128 v[48:51], v0 offset:11264\n" \
  "v_mfma_f32_16x16x32_bf16 v[68:71], a[36:39], v[36:39], v[68:71]\n" \
  "ds_read_b128 v[52:55], v0 offset:31744\n" \
  "v_mfma_f32_16x16x32_bf16 v[60:63], a[116:119], v[32:35], v[60:63]\n" \
  "buffer_load_dwordx4 v1, s[36:39], s70 offen offset:1024 sc1 lds\n" \
  "v_mfma_f32_16x16x32_bf16 v[72:75], a[116:119], v[36:39], v[72:75]\n" \
  "v_mfma_f32_16x16x32_bf16 v[64:67], a[196:199], v[32:35], v[64:67]\n" \
  "v_mfma_f32_16x16x32_bf16 v[76:79], a[196:199], v[36:39], v[76:79]\n" \
  "s_waitcnt lgkmcnt(2)\n" \
  "v_mfma_f32_16x16x32_bf16 v[56:59], a[40:43], v[40:43], v[56:59]\n" \
  "ds_read_b128 v[32:35], v0 offset:12288\n" \
  "v_mfma_f32_16x16x32_bf16 v[68:71], a[40:43], v[44:47], v[68:71]\n" \
  "ds_read_b128 v[36:39], v0 offset:32768\n" \
  "v_mfma_f32_16x16x32_bf16 v[60:63], a[120:123], v[40:43], v[60:63]\n" \
  "buffer_load_dwordx4 v1, s[36:39], s70 offen offset:2048 sc1 lds\n" \
  "v_mfma_f32_16x16x32_bf16 v[72:75], a[120:123], v[44:47], v[72:75]\n" \
  "v_mfma_f32_16x16x32_bf16 v[64:67], a[200:203], v[40:43], v[64:67]\n" \
  "v_mfma_f32_16x16x32_bf16 v[76:79], a[200:203], v[44:47], v[76:79]\n" \
  "s_waitcnt lgkmcnt(2)\n" \
  "v_mfma_f32_16x16x32_bf16 v[56:59], a[44:47], v[48:51], v[56:59]\n" \
  "ds_read_b128 v[40:43], v0 offset:13312\n" \
  "v_mfma_f32_16x16x32_bf16 v[68:71], a[44:47], v[52:55], v[68:71]\n" \
  "ds_read_b128 v[44:47], v0 offset:33792\n" \
  "v_mfma_f32_16x16x32_bf16 v[60:63], a[124:127], v[48:51], v[60:63]\n" \
  "v_mfma_f32_16x16x32_bf16 v[72:75], a[124:127], v[52:55], v[72:75]\n" \
  "v_mfma_f32_16x16x32_bf16 v[64:67], a[204:207], v[48:51], v[64:67]\n" \
  "v_mfma_f32_16x16x32_bf16 v[76:79], a[204:207], v[52:55], v[76:79]\n" \
  "s_waitcnt lgkmcnt(2)\n" \
  "v_mfma_f32_16x16x32_bf16 v[56:59], a[48:51], v[32:35], v[56:59]\n" \
  "ds_read_b128 v[48:51], v0 offset:14336\n" \
  "v_mfma_f32_16x16x32_bf16 v[68:71], a[48:51], v[36:39], v[68:71]\n" \
  "ds_read_b128 v[52:55], v0 offset:34816\n" \
  "v_mfma_f32_16x16x32_bf16 v[60:63], a[128:131], v[32:35], v[60:63]\n" \
  "buffer_load_dwordx4 v1, s[36:39], s70 offen offset:3072 sc1 lds\n" \
  "v_mfma_f32_16x16x32_bf16 v[72:75], a[128:131], v[36:39], v[72:75]\n" \
  "v_mfma_f32_16x16x32_bf16 v[64:67], a[208:211], v[32:35], v[64:67]\n" \
  "v_mfma_f32_16x16x32_bf16 v[76:79], a[208:211], v[36:39], v[76:79]\n" \
  "s_waitcnt lgkmcnt(2)\n" \
  "v_mfma_f32_16x16x32_bf16 v[56:59], a[52:55], v[40:43], v[56:59]\n" \
  "ds_read_b128 v[32:35], v0 offset:15360\n" \
  "v_mfma_f32_16x16x32_bf16 v[68:71], a[52:55], v[44:47], v[68:71]\n" \
  "ds_read_b128 v[36:39], v0 offset:35840\n" \
  "v_mfma_f32_16x16x32_bf16 v[60:63], a[132:135], v[40:43], v[60:63]\n" \
  "s_add_u32 m0, s53, 90112\n" \
  "v_mfma_f32_16x16x32_bf16 v[72:75], a[132:135], v[44:47], v[72:75]\n" \
  "buffer_load_dwordx4 v1, s[36:39], s71 offen sc1 lds\n" \
  "v_mfma_f32_16x16x32_bf16 v[64:67], a[212:215], v[40:43], v[64:67]\n" \
  "v_mfma_f32_16x16x32_bf16 v[76:79], a[212:215], v[44:47], v[76:79]\n" \
  "s_waitcnt lgkmcnt(2)\n" \
  "v_mfma_f32_16x16x32_bf16 v[56:59], a[56:59], v[48:51], v[56:59]\n" \
  "ds_read_b128 v[40:43], v0 offset:16384\n" \
  "v_mfma_f32_16x16x32_bf16 v[68:71], a[56:59], v[52:55], v[68:71]\n" \
  "ds_read_b128 v[44:47], v0 offset:36864\n" \
  "v_mfma_f32_16x16x32_bf16 v[60:63], a[136:139], v[48:51], v[60:63]\n" \
  "buffer_load_dwordx4 v1, s[36:39], s71 offen offset:1024 sc1 lds\n" \
  "v_mfma_f32_16x16x32_bf16 v[72:75], a[136:139], v[52:55], v[72:75]\n" \
  "v_mfma_f32_16x16x32_bf16 v[64:67], a[216:219], v[48:51], v[64:67]\n" \
  "v_mfma_f32_16x16x32_bf16 v[76:79], a[216:219], v[52:55], v[76:79]\n" \
  "s_waitcnt lgkmcnt(2)\n" \
  "v_mfma_f32_16x16x32_bf16 v[56:59], a[60:63], v[32:35], v[56:59]\n" \
  "ds_read_b128 v[48:51], v0 offset:17408\n" \
  "v_mfma_f32_16x16x32_bf16 v[68:71], a[60:63], v[36:39], v[68:71]\n" \
  "ds_read_b128 v[52:55], v0 offset:37888\n" \
  "v_mfma_f32_16x16x32_bf16 v[60:63], a[140:143], v[32:35], v[60:63]\n" \
  "s_add_u32 s68, s68, 40960\n" \
  "v_mfma_f32_16x16x32_bf16 v[72:75], a[140:143], v[36:39], v[72:75]\n" \
  "v_mfma_f32_16x16x32_bf16 v[64:67], a[220:223], v[32:35], v[64:67]\n" \
  "v_mfma_f32_16x16x32_bf16 v[76:79], a[220:223], v[36:39], v[76:79]\n" \
  "s_waitcnt lgkmcnt(2)\n" \
  "v_mfma_f32_16x16x32_bf16 v[56:59], a[64:67], v[40:43], v[56:59]\n" \
  "ds_read_b128 v[32:35], v0 offset:18432\n" \
  "v_mfma_f32_16x16x32_bf16 v[68:71], a[64:67], v[44:47], v[68:71]\n" \
  "ds_read_b128 v[36:39], v0 offset:38912\n" \
  "v_mfma_f32_16x16x32_bf16 v[60:63], a[144:147], v[40:43], v[60:63]\n" \
  "v_readlane_b32 s69, v3, s72\n" \
  "v_mfma_f32_16x16x32_bf16 v[72:75], a[144:147], v[44:47], v[72:75]\n" \
  "s_add_u32 s72, s72, 1\n" \
  "v_mfma_f32_16x16x32_bf16 v[64:67], a[224:227], v[40:43], v[64:67]\n" \
  "v_mfma_f32_16x16x32_bf16 v[76:79], a[224:227], v[44:47], v[76:79]\n" \
  "s_waitcnt lgkmcnt(2)\n" \
  "v_mfma_f32_16x16x32_bf16 v[56:59], a[68:71], v[48:51], v[56:59]\n" \
  "ds_read_b128 v[40:43], v0 offset:19456\n" \
  "v_mfma_f32_16x16x32_bf16 v[68:71], a[68:71], v[52:55], v[68:71]\n" \
  "ds_read_b128 v[44:47], v0 offset:39936\n" \
  "v_mfma_f32_16x16x32_bf16 v[60:63], a[148:151], v[48:51], v[60:63]\n" \
  "v_mfma_f32_16x16x32_bf16 v[72:75], a[148:151], v[52:55], v[72:75]\n" \
  "v_mfma_f32_16x16x32_bf16 v[64:67], a[228:231], v[48:51], v[64:67]\n" \
  "v_mfma_f32_16x16x32_bf16 v[76:79], a[228:231], v[52:55], v[76:79]\n" \
  "s_waitcnt vmcnt(10) lgkmcnt(0)\n" \
  "s_barrier\n" \
  "v_mfma_f32_16x16x32_bf16 v[56:59], a[72:75], v[32:35], v[56:59]\n" \
  "ds_read_b128 v[48:51], v9 offset:0\n" \
  "v_mfma_f32_16x16x32_bf16 v[68:71], a[72:75], v[36:39], v[68:71]\n" \
  "ds_read_b128 v[52:55], v9 offset:20480\n" \
  "v_mfma_f32_16x16x32_bf16 v[60:63], a[152:155], v[32:35], v[60:63]\n" \
  "v_mfma_f32_16x16x32_bf16 v[72:75], a[152:155], v[36:39], v[72:75]\n" \
  "v_mfma_f32_16x16x32_bf16 v[64:67], a[232:235], v[32:35], v[64:67]\n" \
  "v_mfma_f32_16x16x32_bf16 v[76:79], a[232:235], v[36:39], v[76:79]\n" \
  "v_mfma_f32_16x16x32_bf16 v[56:59], a[76:79], v[40:43], v[56:59]\n" \
  "ds_read_b128 v[32:35], v9 offset:1024\n" \
  "v_mfma_f32_16x16x32_bf16 v[68:71], a[76:79], v[44:47], v[68:71]\n" \
  "ds_read_b128 v[36:39], v9 offset:21504\n" \
  "v_mfma_f32_16x16x32_bf16 v[60:63], a[156:159], v[40:43], v[60:63]\n" \
  "s_cmp_eq_u32 s52, 60\n" \
  "v_mfma_f32_16x16x32_bf16 v[72:75], a[156:159], v[44:47], v[72:75]\n" \
  "v_mfma_f32_16x16x32_bf16 v[64:67], a[236:239], v[40:43], v[64:67]\n" \
  "v_mfma_f32_16x16x32_bf16 v[76:79], a[236:239], v[44:47], v[76:79]\n" \
  "s_waitcnt lgkmcnt(2)\n" \
  "v_mfma_f32_16x16x32_bf16 v[100:103], a[0:3], v[48:51], 0\n" \
  "ds_read_b128 v[40:43], v9 offset:2048\n" \
  "v_mfma_f32_16x16x32_bf16 v[112:115], a[0:3], v[52:55], 0\n" \
  "ds_read_b128 v[44:47], v9 offset:22528\n" \
  "v_mfma_f32_16x16x32_bf16 v[104:107], a[80:83], v[48:51], 0\n" \
  "v_cvt_pk_bf16_f32 v16, v56, v57\n" \
  "v_mfma_f32_16x16x32_bf16 v[116:119], a[80:83], v[52:55], 0\n" \
  "v_cvt_pk_bf16_f32 v17, v58, v59\n" \
  "v_mfma_f32_16x16x32_bf16 v[108:111], a[160:163], v[48:51], 0\n" \
  "v_cvt_pk_bf16_f32 v18, v60, v61\n" \
  "v_mfma_f32_16x16x32_bf16 v[120:123], a[160:163], v[52:55], 0\n" \
  "v_cvt_pk_bf16_f32 v19, v62, v63\n" \
  "s_waitcnt lgkmcnt(2)\n" \
  "v_mfma_f32_16x16x32_bf16 v[100:103], a[4:7], v[32:35], v[100:103]\n" \
  "ds_read_b128 v[48:51], v9 offset:3072\n" \
  "v_mfma_f32_16x16x32_bf16 v[112:115], a[4:7], v[36:39], v[112:115]\n" \
  "ds_read_b128 v[52:55], v9 offset:23552\n" \
  "v_mfma_f32_16x16x32_bf16 v[104:107], a[84:87], v[32:35], v[104:107]\n" \
  "buffer_store_dwordx4 v[16:19], v2, s[40:43], s69 offen offset:0 sc1\n" \
  "v_mfma_f32_16x16x32_bf16 v[116:119], a[84:87], v[36:39], v[116:119]\n" \
  "v_cvt_pk_bf16_f32 v20, v68, v69\n" \
  "v_mfma_f32_16x16x32_bf16 v[108:111], a[164:167], v[32:35], v[108:111]\n" \
  "v_cvt_pk_bf16_f32 v21, v70, v71\n" \
  "v_mfma_f32_16x16x32_bf16 v[120:123], a[164:167], v[36:39], v[120:123]\n" \
  "v_cvt_pk_bf16_f32 v22, v72, v73\n" \
  "s_waitcnt lgkmcnt(2)\n" \
  "v_mfma_f32_16x16x32_bf16 v[100:103], a[8:11], v[40:43], v[100:103]\n" \
  "ds_read_b128 v[32:35], v9 offset:4096\n" \
  "v_mfma_f32_16x16x32_bf16 v[112:115], a[8:11], v[44:47], v[112:115]\n" \
  "ds_read_b128 v[36:39], v9 offset:24576\n" \
  "v_mfma_f32_16x16x32_bf16 v[104:107], a[88:91], v[40:43], v[104:107]\n" \
  "v_cvt_pk_bf16_f32 v23, v74, v75\n" \
  "v_mfma_f32_16x16x32_bf16 v[116:119], a[88:91], v[44:47], v[116:119]\n" \
  "buffer_store_dwordx4 v[20:23], v2, s[40:43], s69 offen offset:1024 sc1\n" \
  "v_mfma_f32_16x16x32_bf16 v[108:111], a[168:171], v[40:43], v[108:111]\n" \
  "v_cvt_pk_bf16_f32 v24, v64, v65\n" \
  "v_mfma_f32_16x16x32_bf16 v[120:123], a[168:171], v[44:47], v[120:123]\n" \
  "v_cvt_pk_bf16_f32 v25, v66, v67\n" \
  "s_waitcnt lgkmcnt(2)\n" \
  "v_mfma_f32_16x16x32_bf16 v[100:103], a[12:15], v[48:51], v[100:103]\n" \
  "ds_read_b128 v[40:43], v9 offset:5120\n" \
  "v_mfma_f32_16x16x32_bf16 v[112:115], a[12:15], v[52:55], v[112:115]\n" \
  "ds_read_b128 v[44:47], v9 offset:25600\n" \
  "v_mfma_f32_16x16x32_bf16 v[104:107], a[92:95], v[48:51], v[104:107]\n" \
  "v_cvt_pk_bf16_f32 v26, v76, v77\n" \
  "v_mfma_f32_16x16x32_bf16 v[116:119], a[92:95], v[52:55], v[116:119]\n" \
  "v_cvt_pk_bf16_f32 v27, v78, v79\n" \
  "v_mfma_f32_16x16x32_bf16 v[108:111], a[172:175], v[48:51], v[108:111]\n" \
  "buffer_store_dwordx4 v[24:27], v2, s[40:43], s69 offen offset:2048 sc1\n" \
  "v_mfma_f32_16x16x32_bf16 v[120:123], a[172:175], v[52:55], v[120:123]\n" \
  "s_waitcnt lgkmcnt(2)\n" \
  "v_mfma_f32_16x16x32_bf16 v[100:103], a[16:19], v[32:35], v[100:103]\n" \
  "ds_read_b128 v[48:51], v9 offset:6144\n" \
  "v_mfma_f32_16x16x32_bf16 v[112:115], a[16:19], v[36:39], v[112:115]\n" \
  "ds_read_b128 v[52:55], v9 offset:26624\n" \
  "v_mfma_f32_16x16x32_bf16 v[104:107], a[96:99], v[32:35], v[104:107]\n" \
  "s_add_u32 s70, s68, 4096\n" \
  "v_mfma_f32_16x16x32_bf16 v[116:119], a[96:99], v[36:39], v[116:119]\n" \
  "s_add_u32 s71, s68, 8192\n" \
  "v_mfma_f32_16x16x32_bf16 v[108:111], a[176:179], v[32:35], v[108:111]\n" \
  "s_add_u32 m0, s53, 0\n" \
  "v_mfma_f32_16x16x32_bf16 v[120:123], a[176:179], v[36:39], v[120:123]\n" \
  "buffer_load_dwordx4 v1, s[36:39], s68 offen sc1 lds\n" \
  "s_waitcnt lgkmcnt(2)\n" \
  "v_mfma_f32_16x16x32_bf16 v[100:103], a[20:23], v[40:43], v[100:103]\n" \
  "ds_read_b128 v[32:35], v9 offset:7168\n" \
  "v_mfma_f32_16x16x32_bf16 v[112:115], a[20:23], v[44:47], v[112:115]\n" \
  "ds_read_b128 v[36:39], v9 offset:27648\n" \
  "v_mfma_f32_16x16x32_bf16 v[104:107], a[100:103], v[40:43], v[104:107]\n" \
  "buffer_load_dwordx4 v1, s[36:39], s68 offen offset:1024 sc1 lds\n" \
  "v_mfma_f32_16x16x32_bf16 v[116:119], a[100:103], v[44:47], v[116:119]\n" \
  "v_mfma_f32_16x16x32_bf16 v[108:111], a[180:183], v[40:43], v[108:111]\n" \
  "v_mfma_f32_16x16x32_bf16 v[120:123], a[180:183], v[44:47], v[120:123]\n" \
  "s_waitcnt lgkmcnt(2)\n" \
  "v_mfma_f32_16x16x32_bf16 v[100:103], a[24:27], v[48:51], v[100:103]\n" \
  "ds_read_b128 v[40:43], v9 offset:8192\n" \
  "v_mfma_f32_16x16x32_bf16 v[112:115], a[24:27], v[52:55], v[112:115]\n" \
  "ds_read_b128 v[44:47], v9 offset:28672\n" \
  "v_mfma_f32_16x16x32_bf16 v[104:107], a[104:107], v[48:51], v[104:107]\n" \
  "buffer_load_dwordx4 v1, s[36:39], s68 offen offset:2048 sc1 lds\n" \
  "v_mfma_f32_16x16x32_bf16 v[116:119], a[104:107], v[52:55], v[116:119]\n" \
  "v_mfma_f32_16x16x32_bf16 v[108:111], a[184:187], v[48:51], v[108:111]\n" \
  "v_mfma_f32_16x16x32_bf16 v[120:123], a[184:187], v[52:55], v[120:123]\n" \
  "s_waitcnt lgkmcnt(2)\n" \
  "v_mfma_f32_16x16x32_bf16 v[100:103], a[28:31], v[32:35], v[100:103]\n" \
  "ds_read_b128 v[48:51], v9 offset:9216\n" \
  "v_mfma_f32_16x16x32_bf16 v[112:115], a[28:31], v[36:39], v[112:115]\n" \
  "ds_read_b128 v[52:55], v9 offset:29696\n" \
  "v_mfma_f32_16x16x32_bf16 v[104:107], a[108:111], v[32:35], v[104:107]\n" \
  "buffer_load_dwordx4 v1, s[36:39], s68 offen offset:3072 sc1 lds\n" \
  "v_mfma_f32_16x16x32_bf16 v[116:119], a[108:111], v[36:39], v[116:119]\n" \
  "v_mfma_f32_16x16x32_bf16 v[108:111], a[188:191], v[32:35], v[108:111]\n" \
  "v_mfma_f32_16x16x32_bf16 v[120:123], a[188:191], v[36:39], v[120:123]\n" \
  "s_waitcnt lgkmcnt(2)\n" \
  "v_mfma_f32_16x16x32_bf16 v[100:103], a[32:35], v[40:43], v[100:103]\n" \
  "ds_read_b128 v[32:35], v9 offset:10240\n" \
  "v_mfma_f32_16x16x32_bf16 v[112:115], a[32:35], v[44:47], v[112:115]\n" \
  "ds_read_b128 v[36:39], v9 offset:30720\n" \
  "v_mfma_f32_16x16x32_bf16 v[104:107], a[112:115], v[40:43], v[104:107]\n" \
  "s_add_u32 m0, s53, 4096\n" \
  "v_mfma_f32_16x16x32_bf16 v[116:119], a[112:115], v[44:47], v[116:119]\n" \
  "buffer_load_dwordx4 v1, s[36:39], s70 offen sc1 lds\n" \
  "v_mfma_f32_16x16x32_bf16 v[108:111], a[192:195], v[40:43], v[108:111]\n" \
  "v_mfma_f32_16x16x32_bf16 v[120:123], a[192:195], v[44:47], v[120:123]\n" \
  "s_waitcnt lgkmcnt(2)\n" \
  "v_mfma_f32_16x16x32_bf16 v[100:103], a[36:39], v[48:51], v[100:103]\n" \
  "ds_read_b128 v[40:43], v9 offset:11264\n" \
  "v_mfma_f32_16x16x32_bf16 v[112:115], a[36:39], v[52:55], v[112:115]\n" \
  "ds_read_b128 v[44:47], v9 offset:31744\n" \
  "v_mfma_f32_16x16x32_bf16 v[104:107], a[116:119], v[48:51], v[104:107]\n" \
  "buffer_load_dwordx4 v1, s[36:39], s70 offen offset:1024 sc1 lds\n" \
  "v_mfma_f32_16x16x32_bf16 v[116:119], a[116:119], v[52:55], v[116:119]\n" \
  "v_mfma_f32_16x16x32_bf16 v[108:111], a[196:199], v[48:51], v[108:111]\n" \
  "v_mfma_f32_16x16x32_bf16 v[120:123], a[196:199], v[52:55], v[120:123]\n" \
  "s_waitcnt lgkmcnt(2)\n" \
  "v_mfma_f32_16x16x32_bf16 v[100:103], a[40:43], v[32:35], v[100:103]\n" \
  "ds_read_b128 v[48:51], v9 offset:12288\n" \
  "v_mfma_f32_16x16x32_bf16 v[112:115], a[40:43], v[36:39], v[112:115]\n" \
  "ds_read_b128 v[52:55], v9 offset:32768\n" \
  "v_mfma_f32_16x16x32_bf16 v[104:107], a[120:123], v[32:35], v[104:107]\n" \
  "buffer_load_dwordx4 v1, s[36:39], s70 offen offset:2048 sc1 lds\n" \
  "v_mfma_f32_16x16x32_bf16 v[116:119], a[120:123], v[36:39], v[116:119]\n" \
  "v_mfma_f32_16x16x32_bf16 v[108:111], a[200:203], v[32:35], v[108:111]\n" \
  "v_mfma_f32_16x16x32_bf16 v[120:123], a[200:203], v[36:39], v[120:123]\n" \
  "s_waitcnt vmcnt(7) lgkmcnt(2)\n" \
  "s_barrier\n" \
  "v_mfma_f32_16x16x32_bf16 v[100:103], a[44:47], v[40:43], v[100:103]\n" \
  "ds_read_b128 v[32:35], v9 offset:13312\n" \
  "v_mfma_f32_16x16x32_bf16 v[112:115], a[44:47], v[44:47], v[112:115]\n" \
  "ds_read_b128 v[36:39], v9 offset:33792\n" \
  "v_mfma_f32_16x16x32_bf16 v[104:107], a[124:127], v[40:43], v[104:107]\n" \
  "buffer_atomic_add v11, v4, s[40:43], s54 offen\n" \
  "v_mfma_f32_16x16x32_bf16 v[116:119], a[124:127], v[44:47], v[116:119]\n" \
  "s_add_u32 s54, s54, s55\n" \
  "v_mfma_f32_16x16x32_bf16 v[108:111], a[204:207], v[40:43], v[108:111]\n" \
  "s_and_b32 s54, s54, 0xffffdfff\n" \
  "v_mfma_f32_16x16x32_bf16 v[120:123], a[204:207], v[44:47], v[120:123]\n" \
  "s_waitcnt lgkmcnt(2)\n" \
  "v_mfma_f32_16x16x32_bf16 v[100:103], a[48:51], v[48:51], v[100:103]\n" \
  "ds_read_b128 v[40:43], v9 offset:14336\n" \
  "v_mfma_f32_16x16x32_bf16 v[112:115], a[48:51], v[52:55], v[112:115]\n" \
  "ds_read_b128 v[44:47], v9 offset:34816\n" \
  "v_mfma_f32_16x16x32_bf16 v[104:107], a[128:131], v[48:51], v[104:107]\n" \
  "buffer_load_dwordx4 v1, s[36:39], s70 offen offset:3072 sc1 lds\n" \
  "v_mfma_f32_16x16x32_bf16 v[116:119], a[128:131], v[52:55], v[116:119]\n" \
  "v_mfma_f32_16x16x32_bf16 v[108:111], a[208:211], v[48:51], v[108:111]\n" \
  "v_mfma_f32_16x16x32_bf16 v[120:123], a[208:211], v[52:55], v[120:123]\n" \
  "s_waitcnt lgkmcnt(2)\n" \
  "v_mfma_f32_16x16x32_bf16 v[100:103], a[52:55], v[32:35], v[100:103]\n" \
  "ds_read_b128 v[48:51], v9 offset:15360\n" \
  "v_mfma_f32_16x16x32_bf16 v[112:115], a[52:55], v[36:39], v[112:115]\n" \
  "ds_read_b128 v[52:55], v9 offset:35840\n" \
  "v_mfma_f32_16x16x32_bf16 v[104:107], a[132:135], v[32:35], v[104:107]\n" \
  "s_add_u32 m0, s53, 8192\n" \
  "v_mfma_f32_16x16x32_bf16 v[116:119], a[132:135], v[36:39], v[116:119]\n" \
  "buffer_load_dwordx4 v1, s[36:39], s71 offen sc1 lds\n" \
  "v_mfma_f32_16x16x32_bf16 v[108:111], a[212:215], v[32:35], v[108:111]\n" \
  "v_mfma_f32_16x16x32_bf16 v[120:123], a[212:215], v[36:39], v[120:123]\n" \
  "s_waitcnt lgkmcnt(2)\n" \
  "v_mfma_f32_16x16x32_bf16 v[100:103], a[56:59], v[40:43], v[100:103]\n" \
  "ds_read_b128 v[32:35], v9 offset:16384\n" \
  "v_mfma_f32_16x16x32_bf16 v[112:115], a[56:59], v[44:47], v[112:115]\n" \
  "ds_read_b128 v[36:39], v9 offset:36864\n" \
  "v_mfma_f32_16x16x32_bf16 v[104:107], a[136:139], v[40:43], v[104:107]\n" \
  "buffer_load_dwordx4 v1, s[36:39], s71 offen offset:1024 sc1 lds\n" \
  "v_mfma_f32_16x16x32_bf16 v[116:119], a[136:139], v[44:47], v[116:119]\n" \
  "v_mfma_f32_16x16x32_bf16 v[108:111], a[216:219], v[40:43], v[108:111]\n" \
  "v_mfma_f32_16x16x32_bf16 v[120:123], a[216:219], v[44:47], v[120:123]\n" \
  "s_waitcnt lgkmcnt(2)\n" \
  "v_mfma_f32_16x16x32_bf16 v[100:103], a[60:63], v[48:51], v[100:103]\n" \
  "ds_read_b128 v[40:43], v9 offset:17408\n" \
  "v_mfma_f32_16x16x32_bf16 v[112:115], a[60:63], v[52:55], v[112:115]\n" \
  "ds_read_b128 v[44:47], v9 offset:37888\n" \
  "v_mfma_f32_16x16x32_bf16 v[104:107], a[140:143], v[48:51], v[104:107]\n" \
  "s_add_u32 s68, s68, 40960\n" \
  "v_mfma_f32_16x16x32_bf16 v[116:119], a[140:143], v[52:55], v[116:119]\n" \
  "v_mfma_f32_16x16x32_bf16 v[108:111], a[220:223], v[48:51], v[108:111]\n" \
  "v_mfma_f32_16x16x32_bf16 v[120:123], a[220:223], v[52:55], v[120:123]\n" \
  "s_waitcnt lgkmcnt(2)\n" \
  "v_mfma_f32_16x16x32_bf16 v[100:103], a[64:67], v[32:35], v[100:103]\n" \
  "ds_read_b128 v[48:51], v9 offset:18432\n" \
  "v_mfma_f32_16x16x32_bf16 v[112:115], a[64:67], v[36:39], v[112:115]\n" \
  "ds_read_b128 v[52:55], v9 offset:38912\n" \
  "v_mfma_f32_16x16x32_bf16 v[104:107], a[144:147], v[32:35], v[104:107]\n" \
  "v_readlane_b32 s69, v3, s72\n" \
  "v_mfma_f32_16x16x32_bf16 v[116:119], a[144:147], v[36:39], v[116:119]\n" \
  "s_add_u32 s72, s72, 1\n" \
  "v_mfma_f32_16x16x32_bf16 v[108:111], a[224:227], v[32:35], v[108:111]\n" \
  "v_mfma_f32_16x16x32_bf16 v[120:123], a[224:227], v[36:39], v[120:123]\n" \
  "s_waitcnt lgkmcnt(2)\n" \
  "v_mfma_f32_16x16x32_bf16 v[100:103], a[68:71], v[40:43], v[100:103]\n" \
  "ds_read_b128 v[32:35], v9 offset:19456\n" \
  "v_mfma_f32_16x16x32_bf16 v[112:115], a[68:71], v[44:47], v[112:115]\n" \
  "ds_read_b128 v[36:39], v9 offset:39936\n" \
  "v_mfma_f32_16x16x32_bf16 v[104:107], a[148:151], v[40:43], v[104:107]\n" \
  "v_mfma_f32_16x16x32_bf16 v[116:119], a[148:151], v[44:47], v[116:119]\n" \
  "v_mfma_f32_16x16x32_bf16 v[108:111], a[228:231], v[40:43], v[108:111]\n" \
  "v_mfma_f32_16x16x32_bf16 v[120:123], a[228:231], v[44:47], v[120:123]\n" \
  "s_waitcnt vmcnt(11) lgkmcnt(0)\n" \
  "s_barrier\n" \
  "v_mfma_f32_16x16x32_bf16 v[100:103], a[72:75], v[48:51], v[100:103]\n" \
  "ds_read_b128 v[40:43], v10 offset:0\n" \
  "v_mfma_f32_16x16x32_bf16 v[112:115], a[72:75], v[52:55], v[112:115]\n" \
  "ds_read_b128 v[44:47], v10 offset:20480\n" \
  "v_mfma_f32_16x16x32_bf16 v[104:107], a[152:155], v[48:51], v[104:107]\n" \
  "v_mfma_f32_16x16x32_bf16 v[116:119], a[152:155], v[52:55], v[116:119]\n" \
  "v_mfma_f32_16x16x32_bf16 v[108:111], a[232:235], v[48:51], v[108:111]\n" \
  "v_mfma_f32_16x16x32_bf16 v[120:123], a[232:235], v[52:55], v[120:123]\n" \
  "v_mfma_f32_16x16x32_bf16 v[100:103], a[76:79], v[32:35], v[100:103]\n" \
  "ds_read_b128 v[48:51], v10 offset:1024\n" \
  "v_mfma_f32_16x16x32_bf16 v[112:115], a[76:79], v[36:39], v[112:115]\n" \
  "ds_read_b128 v[52:55], v10 offset:21504\n" \
  "v_mfma_f32_16x16x32_bf16 v[104:107], a[156:159], v[32:35], v[104:107]\n" \
  "s_cmp_eq_u32 s52, 60\n" \
  "v_mfma_f32_16x16x32_bf16 v[116:119], a[156:159], v[36:39], v[116:119]\n" \
  "v_mfma_f32_16x16x32_bf16 v[108:111], a[236:239], v[32:35], v[108:111]\n" \
  "v_mfma_f32_16x16x32_bf16 v[120:123], a[236:239], v[36:39], v[120:123]\n" \
  "s_waitcnt lgkmcnt(2)\n" \
  "v_mfma_f32_16x16x32_bf16 v[56:59], a[0:3], v[40:43], 0\n" \
  "ds_read_b128 v[32:35], v10 offset:2048\n" \
  "v_mfma_f32_16x16x32_bf16 v[68:71], a[0:3], v[44:47], 0\n" \
  "ds_read_b128 v[36:39], v10 offset:22528\n" \
  "v_mfma_f32_16x16x32_bf16 v[60:63], a[80:83], v[40:43], 0\n" \
  "v_cvt_pk_bf16_f32 v16, v100, v101\n" \
  "v_mfma_f32_16x16x32_bf16 v[72:75], a[80:83], v[44:47], 0\n" \
  "v_cvt_pk_bf16_f32 v17, v102, v103\n" \
  "v_mfma_f32_16x16x32_bf16 v[64:67], a[160:163], v[40:43], 0\n" \
  "v_cvt_pk_bf16_f32 v18, v104, v105\n" \
  "v_mfma_f32_16x16x32_bf16 v[76:79], a[160:163], v[44:47], 0\n" \
  "v_cvt_pk_bf16_f32 v19, v106, v107\n" \
  "s_waitcnt lgkmcnt(2)\n" \
  "v_mfma_f32_16x16x32_bf16 v[56:59], a[4:7], v[48:51], v[56:59]\n" \
  "ds_read_b128 v[40:43], v10 offset:3072\n" \
  "v_mfma_f32_16x16x32_bf16 v[68:71], a[4:7], v[52:55], v[68:71]\n" \
  "ds_read_b128 v[44:47], v10 offset:23552\n" \
  "v_mfma_f32_16x16x32_bf16 v[60:63], a[84:87], v[48:51], v[60:63]\n" \
  "buffer_store_dwordx4 v[16:19], v2, s[40:43], s69 offen offset:0 sc1\n" \
  "v_mfma_f32_16x16x32_bf16 v[72:75], a[84:87], v[52:55], v[72:75]\n" \
  "v_cvt_pk_bf16_f32 v20, v112, v113\n" \
  "v_mfma_f32_16x16x32_bf16 v[64:67], a[164:167], v[48:51], v[64:67]\n" \
  "v_cvt_pk_bf16_f32 v21, v114, v115\n" \
  "v_mfma_f32_16x16x32_bf16 v[76:79], a[164:167], v[52:55], v[76:79]\n" \
  "v_cvt_pk_bf16_f32 v22, v116, v117\n" \
  "s_waitcnt lgkmcnt(2)\n" \
  "v_mfma_f32_16x16x32_bf16 v[56:59], a[8:11], v[32:35], v[56:59]\n" \
  "ds_read_b128 v[48:51], v10 offset:4096\n" \
  "v_mfma_f32_16x16x32_bf16 v[68:71], a[8:11], v[36:39], v[68:71]\n" \
  "ds_read_b128 v[52:55], v10 offset:24576\n" \
  "v_mfma_f32_16x16x32_bf16 v[60:63], a[88:91], v[32:35], v[60:63]\n" \
  "v_cvt_pk_bf16_f32 v23, v118, v119\n" \
  "v_mfma_f32_16x16x32_bf16 v[72:75], a[88:91], v[36:39], v[72:75]\n" \
  "buffer_store_dwordx4 v[20:23], v2, s[40:43], s69 offen offset:1024 sc1\n" \
  "v_mfma_f32_16x16x32_bf16 v[64:67], a[168:171], v[32:35], v[64:67]\n" \
  "v_cvt_pk_bf16_f32 v24, v108, v109\n" \
  "v_mfma_f32_16x16x32_bf16 v[76:79], a[168:171], v[36:39], v[76:79]\n" \
  "v_cvt_pk_bf16_f32 v25, v110, v111\n" \
  "s_waitcnt lgkmcnt(2)\n" \
  "v_mfma_f32_16x16x32_bf16 v[56:59], a[12:15], v[40:43], v[56:59]\n" \
  "ds_read_b128 v[32:35], v10 offset:5120\n" \
  "v_mfma_f32_16x16x32_bf16 v[68:71], a[12:15], v[44:47], v[68:71]\n" \
  "ds_read_b128 v[36:39], v10 offset:25600\n" \
  "v_mfma_f32_16x16x32_bf16 v[60:63], a[92:95], v[40:43], v[60:63]\n" \
  "v_cvt_pk_bf16_f32 v26, v120, v121\n" \
  "v_mfma_f32_16x16x32_bf16 v[72:75], a[92:95], v[44:47], v[72:75]\n" \
  "v_cvt_pk_bf16_f32 v27, v122, v123\n" \
  "v_mfma_f32_16x16x32_bf16 v[64:67], a[172:175], v[40:43], v[64:67]\n" \
  "buffer_store_dwordx4 v[24:27], v2, s[40:43], s69 offen offset:2048 sc1\n" \
  "v_mfma_f32_16x16x32_bf16 v[76:79], a[172:175], v[44:47], v[76:79]\n" \
  "s_waitcnt lgkmcnt(2)\n" \
  "v_mfma_f32_16x16x32_bf16 v[56:59], a[16:19], v[48:51], v[56:59]\n" \
  "ds_read_b128 v[40:43], v10 offset:6144\n" \
  "v_mfma_f32_16x16x32_bf16 v[68:71], a[16:19], v[52:55], v[68:71]\n" \
  "ds_read_b128 v[44:47], v10 offset:26624\n" \
  "v_mfma_f32_16x16x32_bf16 v[60:63], a[96:99], v[48:51], v[60:63]\n" \
  "s_add_u32 s70, s68, 4096\n" \
  "v_mfma_f32_16x16x32_bf16 v[72:75], a[96:99], v[52:55], v[72:75]\n" \
  "s_add_u32 s71, s68, 8192\n" \
  "v_mfma_f32_16x16x32_bf16 v[64:67], a[176:179], v[48:51], v[64:67]\n" \
  "s_add_u32 m0, s53, 40960\n" \
  "v_mfma_f32_16x16x32_bf16 v[76:79], a[176:179], v[52:55], v[76:79]\n" \
  "buffer_load_dwordx4 v1, s[36:39], s68 offen sc1 lds\n" \
  "s_waitcnt lgkmcnt(2)\n" \
  "v_mfma_f32_16x16x32_bf16 v[56:59], a[20:23], v[32:35], v[56:59]\n" \
  "ds_read_b128 v[48:51], v10 offset:7168\n" \
  "v_mfma_f32_16x16x32_bf16 v[68:71], a[20:23], v[36:39], v[68:71]\n" \
  "ds_read_b128 v[52:55], v10 offset:27648\n" \
  "v_mfma_f32_16x16x32_bf16 v[60:63], a[100:103], v[32:35], v[60:63]\n" \
  "buffer_load_dwordx4 v1, s[36:39], s68 offen offset:1024 sc1 lds\n" \
  "v_mfma_f32_16x16x32_bf16 v[72:75], a[100:103], v[36:39], v[72:75]\n" \
  "v_mfma_f32_16x16x32_bf16 v[64:67], a[180:183], v[32:35], v[64:67]\n" \
  "v_mfma_f32_16x16x32_bf16 v[76:79], a[180:183], v[36:39], v[76:79]\n" \
  "s_waitcnt lgkmcnt(2)\n" \
  "v_mfma_f32_16x16x32_bf16 v[56:59], a[24:27], v[40:43], v[56:59]\n" \
  "ds_read_b128 v[32:35], v10 offset:8192\n" \
  "v_mfma_f32_16x16x32_bf16 v[68:71], a[24:27], v[44:47], v[68:71]\n" \
  "ds_read_b128 v[36:39], v10 offset:28672\n" \
  "v_mfma_f32_16x16x32_bf16 v[60:63], a[104:107], v[40:43], v[60:63]\n" \
  "buffer_load_dwordx4 v1, s[36:39], s68 offen offset:2048 sc1 lds\n" \
  "v_mfma_f32_16x16x32_bf16 v[72:75], a[104:107], v[44:47], v[72:75]\n" \
  "v_mfma_f32_16x16x32_bf16 v[64:67], a[184:187], v[40:43], v[64:67]\n" \
  "v_mfma_f32_16x16x32_bf16 v[76:79], a[184:187], v[44:47], v[76:79]\n" \
  "s_waitcnt lgkmcnt(2)\n" \
  "v_mfma_f32_16x16x32_bf16 v[56:59], a[28:31], v[48:51], v[56:59]\n" \
  "ds_read_b128 v[40:43], v10 offset:9216\n" \
  "v_mfma_f32_16x16x32_bf16 v[68:71], a[28:31], v[52:55], v[68:71]\n" \
  "ds_read_b128 v[44:47], v10 offset:29696\n" \
  "v_mfma_f32_16x16x32_bf16 v[60:63], a[108:111], v[48:51], v[60:63]\n" \
  "buffer_load_dwordx4 v1, s[36:39], s68 offen offset:3072 sc1 lds\n" \
  "v_mfma_f32_16x16x32_bf16 v[72:75], a[108:111], v[52:55], v[72:75]\n" \
  "v_mfma_f32_16x16x32_bf16 v[64:67], a[188:191], v[48:51], v[64:67]\n" \
  "v_mfma_f32_16x16x32_bf16 v[76:79], a[188:191], v[52:55], v[76:79]\n" \
  "s_waitcnt lgkmcnt(2)\n" \
  "v_mfma_f32_16x16x32_bf16 v[56:59], a[32:35], v[32:35], v[56:59]\n" \
  "ds_read_b128 v[48:51], v10 offset:10240\n" \
  "v_mfma_f32_16x16x32_bf16 v[68:71], a[32:35], v[36:39], v[68:71]\n" \
  "ds_read_b128 v[52:55], v10 offset:30720\n" \
  "v_mfma_f32_16x16x32_bf16 v[60:63], a[112:115], v[32:35], v[60:63]\n" \
  "s_add_u32 m0, s53, 45056\n" \
  "v_mfma_f32_16x16x32_bf16 v[72:75], a[112:115], v[36:39], v[72:75]\n" \
  "buffer_load_dwordx4 v1, s[36:39], s70 offen sc1 lds\n" \
  "v_mfma_f32_16x16x32_bf16 v[64:67], a[192:195], v[32:35], v[64:67]\n" \
  "v_mfma_f32_16x16x32_bf16 v[76:79], a[192:195], v[36:39], v[76:79]\n" \
  "s_waitcnt lgkmcnt(2)\n" \
  "v_mfma_f32_16x16x32_bf16 v[56:59], a[36:39], v[40:43], v[56:59]\n" \
  "ds_read_b128 v[32:35], v10 offset:11264\n" \
  "v_mfma_f32_16x16x32_bf16 v[68:71], a[36:39], v[44:47], v[68:71]\n" \
  "ds_read_b128 v[36:39], v10 offset:31744\n" \
  "v_mfma_f32_16x16x32_bf16 v[60:63], a[116:119], v[40:43], v[60:63]\n" \
  "buffer_load_dwordx4 v1, s[36:39], s70 offen offset:1024 sc1 lds\n" \
  "v_mfma_f32_16x16x32_bf16 v[72:75], a[116:119], v[44:47], v[72:75]\n" \
  "v_mfma_f32_16x16x32_bf16 v[64:67], a[196:199], v[40:43], v[64:67]\n" \
  "v_mfma_f32_16x16x32_bf16 v[76:79], a[196:199], v[44:47], v[76:79]\n" \
  "s_waitcnt lgkmcnt(2)\n" \
  "v_mfma_f32_16x16x32_bf16 v[56:59], a[40:43], v[48:51], v[56:59]\n" \
  "ds_read_b128 v[40:43], v10 offset:12288\n" \
  "v_mfma_f32_16x16x32_bf16 v[68:71], a[40:43], v[52:55], v[68:71]\n" \
  "ds_read_b128 v[44:47], v10 offset:32768\n" \
  "v_mfma_f32_16x16x32_bf16 v[60:63], a[120:123], v[48:51], v[60:63]\n" \
  "buffer_load_dwordx4 v1, s[36:39], s70 offen offset:2048 sc1 lds\n" \
  "v_mfma_f32_16x16x32_bf16 v[72:75], a[120:123], v[52:55], v[72:75]\n" \
  "v_mfma_f32_16x16x32_bf16 v[64:67], a[200:203], v[48:51], v[64:67]\n" \
  "v_mfma_f32_16x16x32_bf16 v[76:79], a[200:203], v[52:55], v[76:79]\n" \
  "s_waitcnt vmcnt(7) lgkmcnt(2)\n" \
  "s_barrier\n" \
  "v_mfma_f32_16x16x32_bf16 v[56:59], a[44:47], v[32:35], v[56:59]\n" \
  "ds_read_b128 v[48:51], v10 offset:13312\n" \
  "v_mfma_f32_16x16x32_bf16 v[68:71], a[44:47], v[36:39], v[68:71]\n" \
  "ds_read_b128 v[52:55], v10 offset:33792\n" \
  "v_mfma_f32_16x16x32_bf16 v[60:63], a[124:127], v[32:35], v[60:63]\n" \
  "buffer_atomic_add v11, v4, s[40:43], s54 offen\n" \
  "v_mfma_f32_16x16x32_bf16 v[72:75], a[124:127], v[36:39], v[72:75]\n" \
  "s_add_u32 s54, s54, s55\n" \
  "v_mfma_f32_16x16x32_bf16 v[64:67], a[204:207], v[32:35], v[64:67]\n" \
  "s_and_b32 s54, s54, 0xffffdfff\n" \
  "v_mfma_f32_16x16x32_bf16 v[76:79], a[204:207], v[36:39], v[76:79]\n" \
  "s_waitcnt lgkmcnt(2)\n" \
  "v_mfma_f32_16x16x32_bf16 v[56:59], a[48:51], v[40:43], v[56:59]\n" \
  "ds_read_b128 v[32:35], v10 offset:14336\n" \
  "v_mfma_f32_16x16x32_bf16 v[68:71], a[48:51], v[44:47], v[68:71]\n" \
  "ds_read_b128 v[36:39], v10 offset:34816\n" \
  "v_mfma_f32_16x16x32_bf16 v[60:63], a[128:131], v[40:43], v[60:63]\n" \
  "buffer_load_dwordx4 v1, s[36:39], s70 offen offset:3072 sc1 lds\n" \
  "v_mfma_f32_16x16x32_bf16 v[72:75], a[128:131], v[44:47], v[72:75]\n" \
  "v_mfma_f32_16x16x32_bf16 v[64:67], a[208:211], v[40:43], v[64:67]\n" \
  "v_mfma_f32_16x16x32_bf16 v[76:79], a[208:211], v[44:47], v[76:79]\n" \
  "s_waitcnt lgkmcnt(2)\n" \
  "v_mfma_f32_16x16x32_bf16 v[56:59], a[52:55], v[48:51], v[56:59]\n" \
  "ds_read_b128 v[40:43], v10 offset:15360\n" \
  "v_mfma_f32_16x16x32_bf16 v[68:71], a[52:55], v[52:55], v[68:71]\n" \
  "ds_read_b128 v[44:47], v10 offset:35840\n" \
  "v_mfma_f32_16x16x32_bf16 v[60:63], a[132:135], v[48:51], v[60:63]\n" \
  "s_add_u32 m0, s53, 49152\n" \
  "v_mfma_f32_16x16x32_bf16 v[72:75], a[132:135], v[52:55], v[72:75]\n" \
  "buffer_load_dwordx4 v1, s[36:39], s71 offen sc1 lds\n" \
  "v_mfma_f32_16x16x32_bf16 v[64:67], a[212:215], v[48:51], v[64:67]\n" \
  "v_mfma_f32_16x16x32_bf16 v[76:79], a[212:215], v[52:55], v[76:79]\n" \
  "s_waitcnt lgkmcnt(2)\n" \
  "v_mfma_f32_16x16x32_bf16 v[56:59], a[56:59], v[32:35], v[56:59]\n" \
  "ds_read_b128 v[48:51], v10 offset:16384\n" \
  "v_mfma_f32_16x16x32_bf16 v[68:71], a[56:59], v[36:39], v[68:71]\n" \
  "ds_read_b128 v[52:55], v10 offset:36864\n" \
  "v_mfma_f32_16x16x32_bf16 v[60:63], a[136:139], v[32:35], v[60:63]\n" \
  "buffer_load_dwordx4 v1, s[36:39], s71 offen offset:1024 sc1 lds\n" \
  "v_mfma_f32_16x16x32_bf16 v[72:75], a[136:139], v[36:39], v[72:75]\n" \
  "v_mfma_f32_16x16x32_bf16 v[64:67], a[216:219], v[32:35], v[64:67]\n" \
  "v_mfma_f32_16x16x32_bf16 v[76:79], a[216:219], v[36:39], v[76:79]\n" \
  "s_waitcnt lgkmcnt(2)\n" \
  "v_mfma_f32_16x16x32_bf16 v[56:59], a[60:63], v[40:43], v[56:59]\n" \
  "ds_read_b128 v[32:35], v10 offset:17408\n" \
  "v_mfma_f32_16x16x32_bf16 v[68:71], a[60:63], v[44:47], v[68:71]\n" \
  "ds_read_b128 v[36:39], v10 offset:37888\n" \
  "v_mfma_f32_16x16x32_bf16 v[60:63], a[140:143], v[40:43], v[60:63]\n" \
  "s_add_u32 s68, s68, 40960\n" \
  "v_mfma_f32_16x16x32_bf16 v[72:75], a[140:143], v[44:47], v[72:75]\n" \
  "v_mfma_f32_16x16x32_bf16 v[64:67], a[220:223], v[40:43], v[64:67]\n" \
  "v_mfma_f32_16x16x32_bf16 v[76:79], a[220:223], v[44:47], v[76:79]\n" \
  "s_waitcnt lgkmcnt(2)\n" \
  "v_mfma_f32_16x16x32_bf16 v[56:59], a[64:67], v[48:51], v[56:59]\n" \
  "ds_read_b128 v[40:43], v10 offset:18432\n" \
  "v_mfma_f32_16x16x32_bf16 v[68:71], a[64:67], v[52:55], v[68:71]\n" \
  "ds_read_b128 v[44:47], v10 offset:38912\n" \
  "v_mfma_f32_16x16x32_bf16 v[60:63], a[144:147], v[48:51], v[60:63]\n" \
  "v_readlane_b32 s69, v3, s72\n" \
  "v_mfma_f32_16x16x32_bf16 v[72:75], a[144:147], v[52:55], v[72:75]\n" \
  "s_add_u32 s72, s72, 1\n" \
  "v_mfma_f32_16x16x32_bf16 v[64:67], a[224:227], v[48:51], v[64:67]\n" \
  "v_mfma_f32_16x16x32_bf16 v[76:79], a[224:227], v[52:55], v[76:79]\n" \
  "s_waitcnt lgkmcnt(2)\n" \
  "v_mfma_f32_16x16x32_bf16 v[56:59], a[68:71], v[32:35], v[56:59]\n" \
  "ds_read_b128 v[48:51], v10 offset:19456\n" \
  "v_mfma_f32_16x16x32_bf16 v[68:71], a[68:71], v[36:39], v[68:71]\n" \
  "ds_read_b128 v[52:55], v10 offset:39936\n" \
  "v_mfma_f32_16x16x32_bf16 v[60:63], a[148:151], v[32:35], v[60:63]\n" \
  "v_mfma_f32_16x16x32_bf16 v[72:75], a[148:151], v[36:39], v[72:75]\n" \
  "v_mfma_f32_16x16x32_bf16 v[64:67], a[228:231], v[32:35], v[64:67]\n" \
  "v_mfma_f32_16x16x32_bf16 v[76:79], a[228:231], v[36:39], v[76:79]\n" \
  "s_waitcnt vmcnt(11) lgkmcnt(0)\n" \
  "s_barrier\n" \
  "v_mfma_f32_16x16x32_bf16 v[56:59], a[72:75], v[40:43], v[56:59]\n" \
  "ds_read_b128 v[32:35], v0 offset:0\n" \
  "v_mfma_f32_16x16x32_bf16 v[68:71], a[72:75], v[44:47], v[68:71]\n" \
  "ds_read_b128 v[36:39], v0 offset:20480\n" \
  "v_mfma_f32_16x16x32_bf16 v[60:63], a[152:155], v[40:43], v[60:63]\n" \
  "v_mfma_f32_16x16x32_bf16 v[72:75], a[152:155], v[44:47], v[72:75]\n" \
  "v_mfma_f32_16x16x32_bf16 v[64:67], a[232:235], v[40:43], v[64:67]\n" \
  "v_mfma_f32_16x16x32_bf16 v[76:79], a[232:235], v[44:47], v[76:79]\n" \
  "v_mfma_f32_16x16x32_bf16 v[56:59], a[76:79], v[48:51], v[56:59]\n" \
  "ds_read_b128 v[40:43], v0 offset:1024\n" \
  "v_mfma_f32_16x16x32_bf16 v[68:71], a[76:79], v[52:55], v[68:71]\n" \
  "ds_read_b128 v[44:47], v0 offset:21504\n" \
  "v_mfma_f32_16x16x32_bf16 v[60:63], a[156:159], v[48:51], v[60:63]\n" \
  "s_cmp_eq_u32 s52, 60\n" \
  "v_mfma_f32_16x16x32_bf16 v[72:75], a[156:159], v[52:55], v[72:75]\n" \
  "v_mfma_f32_16x16x32_bf16 v[64:67], a[236:239], v[48:51], v[64:67]\n" \
  "v_mfma_f32_16x16x32_bf16 v[76:79], a[236:239], v[52:55], v[76:79]\n" \
  "s_waitcnt lgkmcnt(2)\n" \
  "v_mfma_f32_16x16x32_bf16 v[100:103], a[0:3], v[32:35], 0\n" \
  "ds_read_b128 v[48:51], v0 offset:2048\n" \
  "v_mfma_f32_16x16x32_bf16 v[112:115], a[0:3], v[36:39], 0\n" \
  "ds_read_b128 v[52:55], v0 offset:22528\n" \
  "v_mfma_f32_16x16x32_bf16 v[104:107], a[80:83], v[32:35], 0\n" \
  "v_cvt_pk_bf16_f32 v16, v56, v57\n" \
  "v_mfma_f32_16x16x32_bf16 v[116:119], a[80:83], v[36:39], 0\n" \
  "v_cvt_pk_bf16_f32 v17, v58, v59\n" \
  "v_mfma_f32_16x16x32_bf16 v[108:111], a[160:163], v[32:35], 0\n" \
  "v_cvt_pk_bf16_f32 v18, v60, v61\n" \
  "v_mfma_f32_16x16x32_bf16 v[120:123], a[160:163], v[36:39], 0\n" \
  "v_cvt_pk_bf16_f32 v19, v62, v63\n" \
  "s_waitcnt lgkmcnt(2)\n" \
  "v_mfma_f32_16x16x32_bf16 v[100:103], a[4:7], v[40:43], v[100:103]\n" \
  "ds_read_b128 v[32:35], v0 offset:3072\n" \
  "v_mfma_f32_16x16x32_bf16 v[112:115], a[4:7], v[44:47], v[112:115]\n" \
  "ds_read_b128 v[36:39], v0 offset:23552\n" \
  "v_mfma_f32_16x16x32_bf16 v[104:107], a[84:87], v[40:43], v[104:107]\n" \
  "buffer_store_dwordx4 v[16:19], v2, s[40:43], s69 offen offset:0 sc1\n" \
  "v_mfma_f32_16x16x32_bf16 v[116:119], a[84:87], v[44:47], v[116:119]\n" \
  "v_cvt_pk_bf16_f32 v20, v68, v69\n" \
  "v_mfma_f32_16x16x32_bf16 v[108:111], a[164:167], v[40:43], v[108:111]\n" \
  "v_cvt_pk_bf16_f32 v21, v70, v71\n" \
  "v_mfma_f32_16x16x32_bf16 v[120:123], a[164:167], v[44:47], v[120:123]\n" \
  "v_cvt_pk_bf16_f32 v22, v72, v73\n" \
  "s_waitcnt lgkmcnt(2)\n" \
  "v_mfma_f32_16x16x32_bf16 v[100:103], a[8:11], v[48:51], v[100:103]\n" \
  "ds_read_b128 v[40:43], v0 offset:4096\n" \
  "v_mfma_f32_16x16x32_bf16 v[112:115], a[8:11], v[52:55], v[112:115]\n" \
  "ds_read_b128 v[44:47], v0 offset:24576\n" \
  "v_mfma_f32_16x16x32_bf16 v[104:107], a[88:91], v[48:51], v[104:107]\n" \
  "v_cvt_pk_bf16_f32 v23, v74, v75\n" \
  "v_mfma_f32_16x16x32_bf16 v[116:119], a[88:91], v[52:55], v[116:119]\n" \
  "buffer_store_dwordx4 v[20:23], v2, s[40:43], s69 offen offset:1024 sc1\n" \
  "v_mfma_f32_16x16x32_bf16 v[108:111], a[168:171], v[48:51], v[108:111]\n" \
  "v_cvt_pk_bf16_f32 v24, v64, v65\n" \
  "v_mfma_f32_16x16x32_bf16 v[120:123], a[168:171], v[52:55], v[120:123]\n" \
  "v_cvt_pk_bf16_f32 v25, v66, v67\n" \
  "s_waitcnt lgkmcnt(2)\n" \
  "v_mfma_f32_16x16x32_bf16 v[100:103], a[12:15], v[32:35], v[100:103]\n" \
  "ds_read_b128 v[48:51], v0 offset:5120\n" \
  "v_mfma_f32_16x16x32_bf16 v[112:115], a[12:15], v[36:39], v[112:115]\n" \
  "ds_read_b128 v[52:55], v0 offset:25600\n" \
  "v_mfma_f32_16x16x32_bf16 v[104:107], a[92:95], v[32:35], v[104:107]\n" \
  "v_cvt_pk_bf16_f32 v26, v76, v77\n" \
  "v_mfma_f32_16x16x32_bf16 v[116:119], a[92:95], v[36:39], v[116:119]\n" \
  "v_cvt_pk_bf16_f32 v27, v78, v79\n" \
  "v_mfma_f32_16x16x32_bf16 v[108:111], a[172:175], v[32:35], v[108:111]\n" \
  "buffer_store_dwordx4 v[24:27], v2, s[40:43], s69 offen offset:2048 sc1\n" \
  "v_mfma_f32_16x16x32_bf16 v[120:123], a[172:175], v[36:39], v[120:123]\n" \
  "s_waitcnt lgkmcnt(2)\n" \
  "v_mfma_f32_16x16x32_bf16 v[100:103], a[16:19], v[40:43], v[100:103]\n" \
  "ds_read_b128 v[32:35], v0 offset:6144\n" \
  "v_mfma_f32_16x16x32_bf16 v[112:115], a[16:19], v[44:47], v[112:115]\n" \
  "ds_read_b128 v[36:39], v0 offset:26624\n" \
  "v_mfma_f32_16x16x32_bf16 v[104:107], a[96:99], v[40:43], v[104:107]\n" \
  "s_add_u32 s70, s68, 4096\n" \
  "v_mfma_f32_16x16x32_bf16 v[116:119], a[96:99], v[44:47], v[116:119]\n" \
  "s_add_u32 s71, s68, 8192\n" \
  "v_mfma_f32_16x16x32_bf16 v[108:111], a[176:179], v[40:43], v[108:111]\n" \
  "s_add_u32 m0, s53, 81920\n" \
  "v_mfma_f32_16x16x32_bf16 v[120:123], a[176:179], v[44:47], v[120:123]\n" \
  "buffer_load_dwordx4 v1, s[36:39], s68 offen sc1 lds\n" \
  "s_waitcnt lgkmcnt(2)\n" \
  "v_mfma_f32_16x16x32_bf16 v[100:103], a[20:23], v[48:51], v[100:103]\n" \
  "ds_read_b128 v[40:43], v0 offset:7168\n" \
  "v_mfma_f32_16x16x32_bf16 v[112:115], a[20:23], v[52:55], v[112:115]\n" \
  "ds_read_b128 v[44:47], v0 offset:27648\n" \
  "v_mfma_f32_16x16x32_bf16 v[104:107], a[100:103], v[48:51], v[104:107]\n" \
  "buffer_load_dwordx4 v1, s[36:39], s68 offen offset:1024 sc1 lds\n" \
  "v_mfma_f32_16x16x32_bf16 v[116:119], a[100:103], v[52:55], v[116:119]\n" \
  "v_mfma_f32_16x16x32_bf16 v[108:111], a[180:183], v[48:51], v[108:111]\n" \
  "v_mfma_f32_16x16x32_bf16 v[120:123], a[180:183], v[52:55], v[120:123]\n" \
  "s_waitcnt lgkmcnt(2)\n" \
  "v_mfma_f32_16x16x32_bf16 v[100:103], a[24:27], v[32:35], v[100:103]\n" \
  "ds_read_b128 v[48:51], v0 offset:8192\n" \
  "v_mfma_f32_16x16x32_bf16 v[112:115], a[24:27], v[36:39], v[112:115]\n" \
  "ds_read_b128 v[52:55], v0 offset:28672\n" \
  "v_mfma_f32_16x16x32_bf16 v[104:107], a[104:107], v[32:35], v[104:107]\n" \
  "buffer_load_dwordx4 v1, s[36:39], s68 offen offset:2048 sc1 lds\n" \
  "v_mfma_f32_16x16x32_bf16 v[116:119], a[104:107], v[36:39], v[116:119]\n" \
  "v_mfma_f32_16x16x32_bf16 v[108:111], a[184:187], v[32:35], v[108:111]\n" \
  "v_mfma_f32_16x16x32_bf16 v[120:123], a[184:187], v[36:39], v[120:123]\n" \
  "s_waitcnt lgkmcnt(2)\n" \
  "v_mfma_f32_16x16x32_bf16 v[100:103], a[28:31], v[40:43], v[100:103]\n" \
  "ds_read_b128 v[32:35], v0 offset:9216\n" \
  "v_mfma_f32_16x16x32_bf16 v[112:115], a[28:31], v[44:47], v[112:115]\n" \
  "ds_read_b128 v[36:39], v0 offset:29696\n" \
  "v_mfma_f32_16x16x32_bf16 v[104:107], a[108:111], v[40:43], v[104:107]\n" \
  "buffer_load_dwordx4 v1, s[36:39], s68 offen offset:3072 sc1 lds\n" \
  "v_mfma_f32_16x16x32_bf16 v[116:119], a[108:111], v[44:47], v[116:119]\n" \
  "v_mfma_f32_16x16x32_bf16 v[108:111], a[188:191], v[40:43], v[108:111]\n" \
  "v_mfma_f32_16x16x32_bf16 v[120:123], a[188:191], v[44:47], v[120:123]\n" \
  "s_waitcnt lgkmcnt(2)\n" \
  "v_mfma_f32_16x16x32_bf16 v[100:103], a[32:35], v[48:51], v[100:103]\n" \
  "ds_read_b128 v[40:43], v0 offset:10240\n" \
  "v_mfma_f32_16x16x32_bf16 v[112:115], a[32:35], v[52:55], v[112:115]\n" \
  "ds_read_b128 v[44:47], v0 offset:30720\n" \
  "v_mfma_f32_16x16x32_bf16 v[104:107], a[112:115], v[48:51], v[104:107]\n" \
  "s_add_u32 m0, s53, 86016\n" \
  "v_mfma_f32_16x16x32_bf16 v[116:119], a[112:115], v[52:55], v[116:119]\n" \
  "buffer_load_dwordx4 v1, s[36:39], s70 offen sc1 lds\n" \
  "v_mfma_f32_16x16x32_bf16 v[108:111], a[192:195], v[48:51], v[108:111]\n" \
  "v_mfma_f32_16x16x32_bf16 v[120:123], a[192:195], v[52:55], v[120:123]\n" \
  "s_waitcnt lgkmcnt(2)\n" \
  "v_mfma_f32_16x16x32_bf16 v[100:103], a[36:39], v[32:35], v[100:103]\n" \
  "ds_read_b128 v[48:51], v0 offset:11264\n" \
  "v_mfma_f32_16x16x32_bf16 v[112:115], a[36:39], v[36:39], v[112:115]\n" \
  "ds_read_b128 v[52:55], v0 offset:31744\n" \
  "v_mfma_f32_16x16x32_bf16 v[104:107], a[116:119], v[32:35], v[104:107]\n" \
  "buffer_load_dwordx4 v1, s[36:39], s70 offen offset:1024 sc1 lds\n" \
  "v_mfma_f32_16x16x32_bf16 v[116:119], a[116:119], v[36:39], v[116:119]\n" \
  "v_mfma_f32_16x16x32_bf16 v[108:111], a[196:199], v[32:35], v[108:111]\n" \
  "v_mfma_f32_16x16x32_bf16 v[120:123], a[196:199], v[36:39], v[120:123]\n" \
  "s_waitcnt lgkmcnt(2)\n" \
  "v_mfma_f32_16x16x32_bf16 v[100:103], a[40:43], v[40:43], v[100:103]\n" \
  "ds_read_b128 v[32:35], v0 offset:12288\n" \
  "v_mfma_f32_16x16x32_bf16 v[112:115], a[40:43], v[44:47], v[112:115]\n" \
  "ds_read_b128 v[36:39], v0 offset:32768\n" \
  "v_mfma_f32_16x16x32_bf16 v[104:107], a[120:123], v[40:43], v[104:107]\n" \
  "buffer_load_dwordx4 v1, s[36:39], s70 offen offset:2048 sc1 lds\n" \
  "v_mfma_f32_16x16x32_bf16 v[116:119], a[120:123], v[44:47], v[116:119]\n" \
  "v_mfma_f32_16x16x32_bf16 v[108:111], a[200:203], v[40:43], v[108:111]\n" \
  "v_mfma_f32_16x16x32_bf16 v[120:123], a[200:203], v[44:47], v[120:123]\n" \
  "s_waitcnt vmcnt(7) lgkmcnt(2)\n" \
  "s_barrier\n" \
  "v_mfma_f32_16x16x32_bf16 v[100:103], a[44:47], v[48:51], v[100:103]\n" \
  "ds_read_b128 v[40:43], v0 offset:13312\n" \
  "v_mfma_f32_16x16x32_bf16 v[112:115], a[44:47], v[52:55], v[112:115]\n" \
  "ds_read_b128 v[44:47], v0 offset:33792\n" \
  "v_mfma_f32_16x16x32_bf16 v[104:107], a[124:127], v[48:51], v[104:107]\n" \
  "buffer_atomic_add v11, v4, s[40:43], s54 offen\n" \
  "v_mfma_f32_16x16x32_bf16 v[116:119], a[124:127], v[52:55], v[116:119]\n" \
  "s_add_u32 s54, s54, s55\n" \
  "v_mfma_f32_16x16x32_bf16 v[108:111], a[204:207], v[48:51], v[108:111]\n" \
  "s_and_b32 s54, s54, 0xffffdfff\n" \
  "v_mfma_f32_16x16x32_bf16 v[120:123], a[204:207], v[52:55], v[120:123]\n" \
  "s_waitcnt lgkmcnt(2)\n" \
  "v_mfma_f32_16x16x32_bf16 v[100:103], a[48:51], v[32:35], v[100:103]\n" \
  "ds_read_b128 v[48:51], v0 offset:14336\n" \
  "v_mfma_f32_16x16x32_bf16 v[112:115], a[48:51], v[36:39], v[112:115]\n" \
  "ds_read_b128 v[52:55], v0 offset:34816\n" \
  "v_mfma_f32_16x16x32_bf16 v[104:107], a[128:131], v[32:35], v[104:107]\n" \
  "buffer_load_dwordx4 v1, s[36:39], s70 offen offset:3072 sc1 lds\n" \
  "v_mfma_f32_16x16x32_bf16 v[116:119], a[128:131], v[36:39], v[116:119]\n" \
  "v_mfma_f32_16x16x32_bf16 v[108:111], a[208:211], v[32:35], v[108:111]\n" \
  "v_mfma_f32_16x16x32_bf16 v[120:123], a[208:211], v[36:39], v[120:123]\n" \
  "s_waitcnt lgkmcnt(2)\n" \
  "v_mfma_f32_16x16x32_bf16 v[100:103], a[52:55], v[40:43], v[100:103]\n" \
  "ds_read_b128 v[32:35], v0 offset:15360\n" \
  "v_mfma_f32_16x16x32_bf16 v[112:115], a[52:55], v[44:47], v[112:115]\n" \
  "ds_read_b128 v[36:39], v0 offset:35840\n" \
  "v_mfma_f32_16x16x32_bf16 v[104:107], a[132:135], v[40:43], v[104:107]\n" \
  "s_add_u32 m0, s53, 90112\n" \
  "v_mfma_f32_16x16x32_bf16 v[116:119], a[132:135], v[44:47], v[116:119]\n" \
  "buffer_load_dwordx4 v1, s[36:39], s71 offen sc1 lds\n" \
  "v_mfma_f32_16x16x32_bf16 v[108:111], a[212:215], v[40:43], v[108:111]\n" \
  "v_mfma_f32_16x16x32_bf16 v[120:123], a[212:215], v[44:47], v[120:123]\n" \
  "s_waitcnt lgkmcnt(2)\n" \
  "v_mfma_f32_16x16x32_bf16 v[100:103], a[56:59], v[48:51], v[100:103]\n" \
  "ds_read_b128 v[40:43], v0 offset:16384\n" \
  "v_mfma_f32_16x16x32_bf16 v[112:115], a[56:59], v[52:55], v[112:115]\n" \
  "ds_read_b128 v[44:47], v0 offset:36864\n" \
  "v_mfma_f32_16x16x32_bf16 v[104:107], a[136:139], v[48:51], v[104:107]\n" \
  "buffer_load_dwordx4 v1, s[36:39], s71 offen offset:1024 sc1 lds\n" \
  "v_mfma_f32_16x16x32_bf16 v[116:119], a[136:139], v[52:55], v[116:119]\n" \
  "v_mfma_f32_16x16x32_bf16 v[108:111], a[216:219], v[48:51], v[108:111]\n" \
  "v_mfma_f32_16x16x32_bf16 v[120:123], a[216:219], v[52:55], v[120:123]\n" \
  "s_waitcnt lgkmcnt(2)\n" \
  "v_mfma_f32_16x16x32_bf16 v[100:103], a[60:63], v[32:35], v[100:103]\n" \
  "ds_read_b128 v[48:51], v0 offset:17408\n" \
  "v_mfma_f32_16x16x32_bf16 v[112:115], a[60:63], v[36:39], v[112:115]\n" \
  "ds_read_b128 v[52:55], v0 offset:37888\n" \
  "v_mfma_f32_16x16x32_bf16 v[104:107], a[140:143], v[32:35], v[104:107]\n" \
  "s_add_u32 s68, s68, 40960\n" \
  "v_mfma_f32_16x16x32_bf16 v[116:119], a[140:143], v[36:39], v[116:119]\n" \
  "v_mfma_f32_16x16x32_bf16 v[108:111], a[220:223], v[32:35], v[108:111]\n" \
  "v_mfma_f32_16x16x32_bf16 v[120:123], a[220:223], v[36:39], v[120:123]\n" \
  "s_waitcnt lgkmcnt(2)\n" \
  "v_mfma_f32_16x16x32_bf16 v[100:103], a[64:67], v[40:43], v[100:103]\n" \
  "ds_read_b128 v[32:35], v0 offset:18432\n" \
  "v_mfma_f32_16x16x32_bf16 v[112:115], a[64:67], v[44:47], v[112:115]\n" \
  "ds_read_b128 v[36:39], v0 offset:38912\n" \
  "v_mfma_f32_16x16x32_bf16 v[104:107], a[144:147], v[40:43], v[104:107]\n" \
  "v_readlane_b32 s69, v3, s72\n" \
  "v_mfma_f32_16x16x32_bf16 v[116:119], a[144:147], v[44:47], v[116:119]\n" \
  "s_add_u32 s72, s72, 1\n" \
  "v_mfma_f32_16x16x32_bf16 v[108:111], a[224:227], v[40:43], v[108:111]\n" \
  "v_mfma_f32_16x16x32_bf16 v[120:123], a[224:227], v[44:47], v[120:123]\n" \
  "s_waitcnt lgkmcnt(2)\n" \
  "v_mfma_f32_16x16x32_bf16 v[100:103], a[68:71], v[48:51], v[100:103]\n" \
  "ds_read_b128 v[40:43], v0 offset:19456\n" \
  "v_mfma_f32_16x16x32_bf16 v[112:115], a[68:71], v[52:55], v[112:115]\n" \
  "ds_read_b128 v[44:47], v0 offset:39936\n" \
  "v_mfma_f32_16x16x32_bf16 v[104:107], a[148:151], v[48:51], v[104:107]\n" \
  "v_mfma_f32_16x16x32_bf16 v[116:119], a[148:151], v[52:55], v[116:119]\n" \
  "v_mfma_f32_16x16x32_bf16 v[108:111], a[228:231], v[48:51], v[108:111]\n" \
  "v_mfma_f32_16x16x32_bf16 v[120:123], a[228:231], v[52:55], v[120:123]\n" \
  "s_waitcnt vmcnt(11) lgkmcnt(0)\n" \
  "s_barrier\n" \
  "v_mfma_f32_16x16x32_bf16 v[100:103], a[72:75], v[32:35], v[100:103]\n" \
  "ds_read_b128 v[48:51], v9 offset:0\n" \
  "v_mfma_f32_16x16x32_bf16 v[112:115], a[72:75], v[36:39], v[112:115]\n" \
  "ds_read_b128 v[52:55], v9 offset:20480\n" \
  "v_mfma_f32_16x16x32_bf16 v[104:107], a[152:155], v[32:35], v[104:107]\n" \
  "v_mfma_f32_16x16x32_bf16 v[116:119], a[152:155], v[36:39], v[116:119]\n" \
  "v_mfma_f32_16x16x32_bf16 v[108:111], a[232:235], v[32:35], v[108:111]\n" \
  "v_mfma_f32_16x16x32_bf16 v[120:123], a[232:235], v[36:39], v[120:123]\n" \
  "v_mfma_f32_16x16x32_bf16 v[100:103], a[76:79], v[40:43], v[100:103]\n" \
  "ds_read_b128 v[32:35], v9 offset:1024\n" \
  "v_mfma_f32_16x16x32_bf16 v[112:115], a[76:79], v[44:47], v[112:115]\n" \
  "ds_read_b128 v[36:39], v9 offset:21504\n" \
  "v_mfma_f32_16x16x32_bf16 v[104:107], a[156:159], v[40:43], v[104:107]\n" \
  "s_cmp_eq_u32 s52, 60\n" \
  "v_mfma_f32_16x16x32_bf16 v[116:119], a[156:159], v[44:47], v[116:119]\n" \
  "v_mfma_f32_16x16x32_bf16 v[108:111], a[236:239], v[40:43], v[108:111]\n" \
  "v_mfma_f32_16x16x32_bf16 v[120:123], a[236:239], v[44:47], v[120:123]\n" \
  "s_cbranch_scc1 LPE_%=\n" \
  "s_waitcnt lgkmcnt(2)\n" \
  "v_mfma_f32_16x16x32_bf16 v[56:59], a[0:3], v[48:51], 0\n" \
  "ds_read_b128 v[40:43], v9 offset:2048\n" \
  "v_mfma_f32_16x16x32_bf16 v[68:71], a[0:3], v[52:55], 0\n" \
  "ds_read_b128 v[44:47], v9 offset:22528\n" \
  "v_mfma_f32_16x16x32_bf16 v[60:63], a[80:83], v[48:51], 0\n" \
  "v_cvt_pk_bf16_f32 v16, v100, v101\n" \
  "v_mfma_f32_16x16x32_bf16 v[72:75], a[80:83], v[52:55], 0\n" \
  "v_cvt_pk_bf16_f32 v17, v102, v103\n" \
  "v_mfma_f32_16x16x32_bf16 v[64:67], a[160:163], v[48:51], 0\n" \
  "v_cvt_pk_bf16_f32 v18, v104, v105\n" \
  "v_mfma_f32_16x16x32_bf16 v[76:79], a[160:163], v[52:55], 0\n" \
  "v_cvt_pk_bf16_f32 v19, v106, v107\n" \
  "s_waitcnt lgkmcnt(2)\n" \
  "v_mfma_f32_16x16x32_bf16 v[56:59], a[4:7], v[32:35], v[56:59]\n" \
  "ds_read_b128 v[48:51], v9 offset:3072\n" \
  "v_mfma_f32_16x16x32_bf16 v[68:71], a[4:7], v[36:39], v[68:71]\n" \
  "ds_read_b128 v[52:55], v9 offset:23552\n" \
  "v_mfma_f32_16x16x32_bf16 v[60:63], a[84:87], v[32:35], v[60:63]\n" \
  "buffer_store_dwordx4 v[16:19], v2, s[40:43], s69 offen offset:0 sc1\n" \
  "v_mfma_f32_16x16x32_bf16 v[72:75], a[84:87], v[36:39], v[72:75]\n" \
  "v_cvt_pk_bf16_f32 v20, v112, v113\n" \
  "v_mfma_f32_16x16x32_bf16 v[64:67], a[164:167], v[32:35], v[64:67]\n" \
  "v_cvt_pk_bf16_f32 v21, v114, v115\n" \
  "v_mfma_f32_16x16x32_bf16 v[76:79], a[164:167], v[36:39], v[76:79]\n" \
  "v_cvt_pk_bf16_f32 v22, v116, v117\n" \
  "s_waitcnt lgkmcnt(2)\n" \
  "v_mfma_f32_16x16x32_bf16 v[56:59], a[8:11], v[40:43], v[56:59]\n" \
  "ds_read_b128 v[32:35], v9 offset:4096\n" \
  "v_mfma_f32_16x16x32_bf16 v[68:71], a[8:11], v[44:47], v[68:71]\n" \
  "ds_read_b128 v[36:39], v9 offset:24576\n" \
  "v_mfma_f32_16x16x32_bf16 v[60:63], a[88:91], v[40:43], v[60:63]\n" \
  "v_cvt_pk_bf16_f32 v23, v118, v119\n" \
  "v_mfma_f32_16x16x32_bf16 v[72:75], a[88:91], v[44:47], v[72:75]\n" \
  "buffer_store_dwordx4 v[20:23], v2, s[40:43], s69 offen offset:1024 sc1\n" \
  "v_mfma_f32_16x16x32_bf16 v[64:67], a[168:171], v[40:43], v[64:67]\n" \
  "v_cvt_pk_bf16_f32 v24, v108, v109\n" \
  "v_mfma_f32_16x16x32_bf16 v[76:79], a[168:171], v[44:47], v[76:79]\n" \
  "v_cvt_pk_bf16_f32 v25, v110, v111\n" \
  "s_waitcnt lgkmcnt(2)\n" \
  "v_mfma_f32_16x16x32_bf16 v[56:59], a[12:15], v[48:51], v[56:59]\n" \
  "ds_read_b128 v[40:43], v9 offset:5120\n" \
  "v_mfma_f32_16x16x32_bf16 v[68:71], a[12:15], v[52:55], v[68:71]\n" \
  "ds_read_b128 v[44:47], v9 offset:25600\n" \
  "v_mfma_f32_16x16x32_bf16 v[60:63], a[92:95], v[48:51], v[60:63]\n" \
  "v_cvt_pk_bf16_f32 v26, v120, v121\n" \
  "v_mfma_f32_16x16x32_bf16 v[72:75], a[92:95], v[52:55], v[72:75]\n" \
  "v_cvt_pk_bf16_f32 v27, v122, v123\n" \
  "v_mfma_f32_16x16x32_bf16 v[64:67], a[172:175], v[48:51], v[64:67]\n" \
  "buffer_store_dwordx4 v[24:27], v2, s[40:43], s69 offen offset:2048 sc1\n" \
  "v_mfma_f32_16x16x32_bf16 v[76:79], a[172:175], v[52:55], v[76:79]\n" \
  "s_waitcnt lgkmcnt(2)\n" \
  "v_mfma_f32_16x16x32_bf16 v[56:59], a[16:19], v[32:35], v[56:59]\n" \
  "ds_read_b128 v[48:51], v9 offset:6144\n" \
  "v_mfma_f32_16x16x32_bf16 v[68:71], a[16:19], v[36:39], v[68:71]\n" \
  "ds_read_b128 v[52:55], v9 offset:26624\n" \
  "v_mfma_f32_16x16x32_bf16 v[60:63], a[96:99], v[32:35], v[60:63]\n" \
  "s_add_u32 s70, s68, 4096\n" \
  "v_mfma_f32_16x16x32_bf16 v[72:75], a[96:99], v[36:39], v[72:75]\n" \
  "s_add_u32 s71, s68, 8192\n" \
  "v_mfma_f32_16x16x32_bf16 v[64:67], a[176:179], v[32:35], v[64:67]\n" \
  "s_add_u32 m0, s53, 0\n" \
  "v_mfma_f32_16x16x32_bf16 v[76:79], a[176:179], v[36:39], v[76:79]\n" \
  "buffer_load_dwordx4 v1, s[36:39], s68 offen sc1 lds\n" \
  "s_waitcnt lgkmcnt(2)\n" \
  "v_mfma_f32_16x16x32_bf16 v[56:59], a[20:23], v[40:43], v[56:59]\n" \
  "ds_read_b128 v[32:35], v9 offset:7168\n" \
  "v_mfma_f32_16x16x32_bf16 v[68:71], a[20:23], v[44:47], v[68:71]\n" \
  "ds_read_b128 v[36:39], v9 offset:27648\n" \
  "v_mfma_f32_16x16x32_bf16 v[60:63], a[100:103], v[40:43], v[60:63]\n" \
  "buffer_load_dwordx4 v1, s[36:39], s68 offen offset:1024 sc1 lds\n" \
  "v_mfma_f32_16x16x32_bf16 v[72:75], a[100:103], v[44:47], v[72:75]\n" \
  "v_mfma_f32_16x16x32_bf16 v[64:67], a[180:183], v[40:43], v[64:67]\n" \
  "v_mfma_f32_16x16x32_bf16 v[76:79], a[180:183], v[44:47], v[76:79]\n" \
  "s_waitcnt lgkmcnt(2)\n" \
  "v_mfma_f32_16x16x32_bf16 v[56:59], a[24:27], v[48:51], v[56:59]\n" \
  "ds_read_b128 v[40:43], v9 offset:8192\n" \
  "v_mfma_f32_16x16x32_bf16 v[68:71], a[24:27], v[52:55], v[68:71]\n" \
  "ds_read_b128 v[44:47], v9 offset:28672\n" \
  "v_mfma_f32_16x16x32_bf16 v[60:63], a[104:107], v[48:51], v[60:63]\n" \
  "buffer_load_dwordx4 v1, s[36:39], s68 offen offset:2048 sc1 lds\n" \
  "v_mfma_f32_16x16x32_bf16 v[72:75], a[104:107], v[52:55], v[72:75]\n" \
  "v_mfma_f32_16x16x32_bf16 v[64:67], a[184:187], v[48:51], v[64:67]\n" \
  "v_mfma_f32_16x16x32_bf16 v[76:79], a[184:187], v[52:55], v[76:79]\n" \
  "s_waitcnt lgkmcnt(2)\n" \
  "v_mfma_f32_16x16x32_bf16 v[56:59], a[28:31], v[32:35], v[56:59]\n" \
  "ds_read_b128 v[48:51], v9 offset:9216\n" \
  "v_mfma_f32_16x16x32_bf16 v[68:71], a[28:31], v[36:39], v[68:71]\n" \
  "ds_read_b128 v[52:55], v9 offset:29696\n" \
  "v_mfma_f32_16x16x32_bf16 v[60:63], a[108:111], v[32:35], v[60:63]\n" \
  "buffer_load_dwordx4 v1, s[36:39], s68 offen offset:3072 sc1 lds\n" \
  "v_mfma_f32_16x16x32_bf16 v[72:75], a[108:111], v[36:39], v[72:75]\n" \
  "v_mfma_f32_16x16x32_bf16 v[64:67], a[188:191], v[32:35], v[64:67]\n" \
  "v_mfma_f32_16x16x32_bf16 v[76:79], a[188:191], v[36:39], v[76:79]\n" \
  "s_waitcnt lgkmcnt(2)\n" \
  "v_mfma_f32_16x16x32_bf16 v[56:59], a[32:35], v[40:43], v[56:59]\n" \
  "ds_read_b128 v[32:35], v9 offset:10240\n" \
  "v_mfma_f32_16x16x32_bf16 v[68:71], a[32:35], v[44:47], v[68:71]\n" \
  "ds_read_b128 v[36:39], v9 offset:30720\n" \
  "v_mfma_f32_16x16x32_bf16 v[60:63], a[112:115], v[40:43], v[60:63]\n" \
  "s_add_u32 m0, s53, 4096\n" \
  "v_mfma_f32_16x16x32_bf16 v[72:75], a[112:115], v[44:47], v[72:75]\n" \
  "buffer_load_dwordx4 v1, s[36:39], s70 offen sc1 lds\n" \
  "v_mfma_f32_16x16x32_bf16 v[64:67], a[192:195], v[40:43], v[64:67]\n" \
  "v_mfma_f32_16x16x32_bf16 v[76:79], a[192:195], v[44:47], v[76:79]\n" \
  "s_waitcnt lgkmcnt(2)\n" \
  "v_mfma_f32_16x16x32_bf16 v[56:59], a[36:39], v[48:51], v[56:59]\n" \
  "ds_read_b128 v[40:43], v9 offset:11264\n" \
  "v_mfma_f32_16x16x32_bf16 v[68:71], a[36:39], v[52:55], v[68:71]\n" \
  "ds_read_b128 v[44:47], v9 offset:31744\n" \
  "v_mfma_f32_16x16x32_bf16 v[60:63], a[116:119], v[48:51], v[60:63]\n" \
  "buffer_load_dwordx4 v1, s[36:39], s70 offen offset:1024 sc1 lds\n" \
  "v_mfma_f32_16x16x32_bf16 v[72:75], a[116:119], v[52:55], v[72:75]\n" \
  "v_mfma_f32_16x16x32_bf16 v[64:67], a[196:199], v[48:51], v[64:67]\n" \
  "v_mfma_f32_16x16x32_bf16 v[76:79], a[196:199], v[52:55], v[76:79]\n" \
  "s_waitcnt lgkmcnt(2)\n" \
  "v_mfma_f32_16x16x32_bf16 v[56:59], a[40:43], v[32:35], v[56:59]\n" \
  "ds_read_b128 v[48:51], v9 offset:12288\n" \
  "v_mfma_f32_16x16x32_bf16 v[68:71], a[40:43], v[36:39], v[68:71]\n" \
  "ds_read_b128 v[52:55], v9 offset:32768\n" \
  "v_mfma_f32_16x16x32_bf16 v[60:63], a[120:123], v[32:35], v[60:63]\n" \
  "buffer_load_dwordx4 v1, s[36:39], s70 offen offset:2048 sc1 lds\n" \
  "v_mfma_f32_16x16x32_bf16 v[72:75], a[120:123], v[36:39], v[72:75]\n" \
  "v_mfma_f32_16x16x32_bf16 v[64:67], a[200:203], v[32:35], v[64:67]\n" \
  "v_mfma_f32_16x16x32_bf16 v[76:79], a[200:203], v[36:39], v[76:79]\n" \
  "s_waitcnt vmcnt(7) lgkmcnt(2)\n" \
  "s_barrier\n" \
  "v_mfma_f32_16x16x32_bf16 v[56:59], a[44:47], v[40:43], v[56:59]\n" \
  "ds_read_b128 v[32:35], v9 offset:13312\n" \
  "v_mfma_f32_16x16x32_bf16 v[68:71], a[44:47], v[44:47], v[68:71]\n" \
  "ds_read_b128 v[36:39], v9 offset:33792\n" \
  "v_mfma_f32_16x16x32_bf16 v[60:63], a[124:127], v[40:43], v[60:63]\n" \
  "buffer_atomic_add v11, v4, s[40:43], s54 offen\n" \
  "v_mfma_f32_16x16x32_bf16 v[72:75], a[124:127], v[44:47], v[72:75]\n" \
  "s_add_u32 s54, s54, s55\n" \
  "v_mfma_f32_16x16x32_bf16 v[64:67], a[204:207], v[40:43], v[64:67]\n" \
  "s_and_b32 s54, s54, 0xffffdfff\n" \
  "v_mfma_f32_16x16x32_bf16 v[76:79], a[204:207], v[44:47], v[76:79]\n" \
  "s_waitcnt lgkmcnt(2)\n" \
  "v_mfma_f32_16x16x32_bf16 v[56:59], a[48:51], v[48:51], v[56:59]\n" \
  "ds_read_b128 v[40:43], v9 offset:14336\n" \
  "v_mfma_f32_16x16x32_bf16 v[68:71], a[48:51], v[52:55], v[68:71]\n" \
  "ds_read_b128 v[44:47], v9 offset:34816\n" \
  "v_mfma_f32_16x16x32_bf16 v[60:63], a[128:131], v[48:51], v[60:63]\n" \
  "buffer_load_dwordx4 v1, s[36:39], s70 offen offset:3072 sc1 lds\n" \
  "v_mfma_f32_16x16x32_bf16 v[72:75], a[128:131], v[52:55], v[72:75]\n" \
  "v_mfma_f32_16x16x32_bf16 v[64:67], a[208:211], v[48:51], v[64:67]\n" \
  "v_mfma_f32_16x16x32_bf16 v[76:79], a[208:211], v[52:55], v[76:79]\n" \
  "s_waitcnt lgkmcnt(2)\n" \
  "v_mfma_f32_16x16x32_bf16 v[56:59], a[52:55], v[32:35], v[56:59]\n" \
  "ds_read_b128 v[48:51], v9 offset:15360\n" \
  "v_mfma_f32_16x16x32_bf16 v[68:71], a[52:55], v[36:39], v[68:71]\n" \
  "ds_read_b128 v[52:55], v9 offset:35840\n" \
  "v_mfma_f32_16x16x32_bf16 v[60:63], a[132:135], v[32:35], v[60:63]\n" \
  "s_add_u32 m0, s53, 8192\n" \
  "v_mfma_f32_16x16x32_bf16 v[72:75], a[132:135], v[36:39], v[72:75]\n" \
  "buffer_load_dwordx4 v1, s[36:39], s71 offen sc1 lds\n" \
  "v_mfma_f32_16x16x32_bf16 v[64:67], a[212:215], v[32:35], v[64:67]\n" \
  "v_mfma_f32_16x16x32_bf16 v[76:79], a[212:215], v[36:39], v[76:79]\n" \
  "s_waitcnt lgkmcnt(2)\n" \
  "v_mfma_f32_16x16x32_bf16 v[56:59], a[56:59], v[40:43], v[56:59]\n" \
  "ds_read_b128 v[32:35], v9 offset:16384\n" \
  "v_mfma_f32_16x16x32_bf16 v[68:71], a[56:59], v[44:47], v[68:71]\n" \
  "ds_read_b128 v[36:39], v9 offset:36864\n" \
  "v_mfma_f32_16x16x32_bf16 v[60:63], a[136:139], v[40:43], v[60:63]\n" \
  "buffer_load_dwordx4 v1, s[36:39], s71 offen offset:1024 sc1 lds\n" \
  "v_mfma_f32_16x16x32_bf16 v[72:75], a[136:139], v[44:47], v[72:75]\n" \
  "v_mfma_f32_16x16x32_bf16 v[64:67], a[216:219], v[40:43], v[64:67]\n" \
  "v_mfma_f32_16x16x32_bf16 v[76:79], a[216:219], v[44:47], v[76:79]\n" \
  "s_waitcnt lgkmcnt(2)\n" \
  "v_mfma_f32_16x16x32_bf16 v[56:59], a[60:63], v[48:51], v[56:59]\n" \
  "ds_read_b128 v[40:43], v9 offset:17408\n" \
  "v_mfma_f32_16x16x32_bf16 v[68:71], a[60:63], v[52:55], v[68:71]\n" \
  "ds_read_b128 v[44:47], v9 offset:37888\n" \
  "v_mfma_f32_16x16x32_bf16 v[60:63], a[140:143], v[48:51], v[60:63]\n" \
  "s_add_u32 s68, s68, 40960\n" \
  "v_mfma_f32_16x16x32_bf16 v[72:75], a[140:143], v[52:55], v[72:75]\n" \
  "v_mfma_f32_16x16x32_bf16 v[64:67], a[220:223], v[48:51], v[64:67]\n" \
  "v_mfma_f32_16x16x32_bf16 v[76:79], a[220:223], v[52:55], v[76:79]\n" \
  "s_waitcnt lgkmcnt(2)\n" \
  "v_mfma_f32_16x16x32_bf16 v[56:59], a[64:67], v[32:35], v[56:59]\n" \
  "ds_read_b128 v[48:51], v9 offset:18432\n" \
  "v_mfma_f32_16x16x32_bf16 v[68:71], a[64:67], v[36:39], v[68:71]\n" \
  "ds_read_b128 v[52:55], v9 offset:38912\n" \
  "v_mfma_f32_16x16x32_bf16 v[60:63], a[144:147], v[32:35], v[60:63]\n" \
  "v_readlane_b32 s69, v3, s72\n" \
  "v_mfma_f32_16x16x32_bf16 v[72:75], a[144:147], v[36:39], v[72:75]\n" \
  "s_add_u32 s72, s72, 1\n" \
  "v_mfma_f32_16x16x32_bf16 v[64:67], a[224:227], v[32:35], v[64:67]\n" \
  "v_mfma_f32_16x16x32_bf16 v[76:79], a[224:227], v[36:39], v[76:79]\n" \
  "s_waitcnt lgkmcnt(2)\n" \
  "v_mfma_f32_16x16x32_bf16 v[56:59], a[68:71], v[40:43], v[56:59]\n" \
  "ds_read_b128 v[32:35], v9 offset:19456\n" \
  "v_mfma_f32_16x16x32_bf16 v[68:71], a[68:71], v[44:47], v[68:71]\n" \
  "ds_read_b128 v[36:39], v9 offset:39936\n" \
  "v_mfma_f32_16x16x32_bf16 v[60:63], a[148:151], v[40:43], v[60:63]\n" \
  "v_mfma_f32_16x16x32_bf16 v[72:75], a[148:151], v[44:47], v[72:75]\n" \
  "v_mfma_f32_16x16x32_bf16 v[64:67], a[228:231], v[40:43], v[64:67]\n" \
  "v_mfma_f32_16x16x32_bf16 v[76:79], a[228:231], v[44:47], v[76:79]\n" \
  "s_waitcnt vmcnt(11) lgkmcnt(0)\n" \
  "s_barrier\n" \
  "v_mfma_f32_16x16x32_bf16 v[56:59], a[72:75], v[48:51], v[56:59]\n" \
  "ds_read_b128 v[40:43], v10 offset:0\n" \
  "v_mfma_f32_16x16x32_bf16 v[68:71], a[72:75], v[52:55], v[68:71]\n" \
  "ds_read_b128 v[44:47], v10 offset:20480\n" \
  "v_mfma_f32_16x16x32_bf16 v[60:63], a[152:155], v[48:51], v[60:63]\n" \
  "v_mfma_f32_16x16x32_bf16 v[72:75], a[152:155], v[52:55], v[72:75]\n" \
  "v_mfma_f32_16x16x32_bf16 v[64:67], a[232:235], v[48:51], v[64:67]\n" \
  "v_mfma_f32_16x16x32_bf16 v[76:79], a[232:235], v[52:55], v[76:79]\n" \
  "v_mfma_f32_16x16x32_bf16 v[56:59], a[76:79], v[32:35], v[56:59]\n" \
  "ds_read_b128 v[48:51], v10 offset:1024\n" \
  "v_mfma_f32_16x16x32_bf16 v[68:71], a[76:79], v[36:39], v[68:71]\n" \
  "ds_read_b128 v[52:55], v10 offset:21504\n" \
  "v_mfma_f32_16x16x32_bf16 v[60:63], a[156:159], v[32:35], v[60:63]\n" \
  "s_cmp_eq_u32 s52, 60\n" \
  "v_mfma_f32_16x16x32_bf16 v[72:75], a[156:159], v[36:39], v[72:75]\n" \
  "v_mfma_f32_16x16x32_bf16 v[64:67], a[236:239], v[32:35], v[64:67]\n" \
  "v_mfma_f32_16x16x32_bf16 v[76:79], a[236:239], v[36:39], v[76:79]\n" \
  "s_waitcnt lgkmcnt(2)\n" \
  "v_mfma_f32_16x16x32_bf16 v[100:103], a[0:3], v[40:43], 0\n" \
  "ds_read_b128 v[32:35], v10 offset:2048\n" \
  "v_mfma_f32_16x16x32_bf16 v[112:115], a[0:3], v[44:47], 0\n" \
  "ds_read_b128 v[36:39], v10 offset:22528\n" \
  "v_mfma_f32_16x16x32_bf16 v[104:107], a[80:83], v[40:43], 0\n" \
  "v_cvt_pk_bf16_f32 v16, v56, v57\n" \
  "v_mfma_f32_16x16x32_bf16 v[116:119], a[80:83], v[44:47], 0\n" \
  "v_cvt_pk_bf16_f32 v17, v58, v59\n" \
  "v_mfma_f32_16x16x32_bf16 v[108:111], a[160:163], v[40:43], 0\n" \
  "v_cvt_pk_bf16_f32 v18, v60, v61\n" \
  "v_mfma_f32_16x16x32_bf16 v[120:123], a[160:163], v[44:47], 0\n" \
  "v_cvt_pk_bf16_f32 v19, v62, v63\n" \
  "s_waitcnt lgkmcnt(2)\n" \
  "v_mfma_f32_16x16x32_bf16 v[100:103], a[4:7], v[48:51], v[100:103]\n" \
  "ds_read_b128 v[40:43], v10 offset:3072\n" \
  "v_mfma_f32_16x16x32_bf16 v[112:115], a[4:7], v[52:55], v[112:115]\n" \
  "ds_read_b128 v[44:47], v10 offset:23552\n" \
  "v_mfma_f32_16x16x32_bf16 v[104:107], a[84:87], v[48:51], v[104:107]\n" \
  "buffer_store_dwordx4 v[16:19], v2, s[40:43], s69 offen offset:0 sc1\n" \
  "v_mfma_f32_16x16x32_bf16 v[116:119], a[84:87], v[52:55], v[116:119]\n" \
  "v_cvt_pk_bf16_f32 v20, v68, v69\n" \
  "v_mfma_f32_16x16x32_bf16 v[108:111], a[164:167], v[48:51], v[108:111]\n" \
  "v_cvt_pk_bf16_f32 v21, v70, v71\n" \
  "v_mfma_f32_16x16x32_bf16 v[120:123], a[164:167], v[52:55], v[120:123]\n" \
  "v_cvt_pk_bf16_f32 v22, v72, v73\n" \
  "s_waitcnt lgkmcnt(2)\n" \
  "v_mfma_f32_16x16x32_bf16 v[100:103], a[8:11], v[32:35], v[100:103]\n" \
  "ds_read_b128 v[48:51], v10 offset:4096\n" \
  "v_mfma_f32_16x16x32_bf16 v[112:115], a[8:11], v[36:39], v[112:115]\n" \
  "ds_read_b128 v[52:55], v10 offset:24576\n" \
  "v_mfma_f32_16x16x32_bf16 v[104:107], a[88:91], v[32:35], v[104:107]\n" \
  "v_cvt_pk_bf16_f32 v23, v74, v75\n" \
  "v_mfma_f32_16x16x32_bf16 v[116:119], a[88:91], v[36:39], v[116:119]\n" \
  "buffer_store_dwordx4 v[20:23], v2, s[40:43], s69 offen offset:1024 sc1\n" \
  "v_mfma_f32_16x16x32_bf16 v[108:111], a[168:171], v[32:35], v[108:111]\n" \
  "v_cvt_pk_bf16_f32 v24, v64, v65\n" \
  "v_mfma_f32_16x16x32_bf16 v[120:123], a[168:171], v[36:39], v[120:123]\n" \
  "v_cvt_pk_bf16_f32 v25, v66, v67\n" \
  "s_waitcnt lgkmcnt(2)\n" \
  "v_mfma_f32_16x16x32_bf16 v[100:103], a[12:15], v[40:43], v[100:103]\n" \
  "ds_read_b128 v[32:35], v10 offset:5120\n" \
  "v_mfma_f32_16x16x32_bf16 v[112:115], a[12:15], v[44:47], v[112:115]\n" \
  "ds_read_b128 v[36:39], v10 offset:25600\n" \
  "v_mfma_f32_16x16x32_bf16 v[104:107], a[92:95], v[40:43], v[104:107]\n" \
  "v_cvt_pk_bf16_f32 v26, v76, v77\n" \
  "v_mfma_f32_16x16x32_bf16 v[116:119], a[92:95], v[44:47], v[116:119]\n" \
  "v_cvt_pk_bf16_f32 v27, v78, v79\n" \
  "v_mfma_f32_16x16x32_bf16 v[108:111], a[172:175], v[40:43], v[108:111]\n" \
  "buffer_store_dwordx4 v[24:27], v2, s[40:43], s69 offen offset:2048 sc1\n" \
  "v_mfma_f32_16x16x32_bf16 v[120:123], a[172:175], v[44:47], v[120:123]\n" \
  "s_waitcnt lgkmcnt(2)\n" \
  "v_mfma_f32_16x16x32_bf16 v[100:103], a[16:19], v[48:51], v[100:103]\n" \
  "ds_read_b128 v[40:43], v10 offset:6144\n" \
  "v_mfma_f32_16x16x32_bf16 v[112:115], a[16:19], v[52:55], v[112:115]\n" \
  "ds_read_b128 v[44:47], v10 offset:26624\n" \
  "v_mfma_f32_16x16x32_bf16 v[104:107], a[96:99], v[48:51], v[104:107]\n" \
  "s_add_u32 s70, s68, 4096\n" \
  "v_mfma_f32_16x16x32_bf16 v[116:119], a[96:99], v[52:55], v[116:119]\n" \
  "s_add_u32 s71, s68, 8192\n" \
  "v_mfma_f32_16x16x32_bf16 v[108:111], a[176:179], v[48:51], v[108:111]\n" \
  "s_add_u32 m0, s53, 40960\n" \
  "v_mfma_f32_16x16x32_bf16 v[120:123], a[176:179], v[52:55], v[120:123]\n" \
  "buffer_load_dwordx4 v1, s[36:39], s68 offen sc1 lds\n" \
  "s_waitcnt lgkmcnt(2)\n" \
  "v_mfma_f32_16x16x32_bf16 v[100:103], a[20:23], v[32:35], v[100:103]\n" \
  "ds_read_b128 v[48:51], v10 offset:7168\n" \
  "v_mfma_f32_16x16x32_bf16 v[112:115], a[20:23], v[36:39], v[112:115]\n" \
  "ds_read_b128 v[52:55], v10 offset:27648\n" \
  "v_mfma_f32_16x16x32_bf16 v[104:107], a[100:103], v[32:35], v[104:107]\n" \
  "buffer_load_dwordx4 v1, s[36:39], s68 offen offset:1024 sc1 lds\n" \
  "v_mfma_f32_16x16x32_bf16 v[116:119], a[100:103], v[36:39], v[116:119]\n" \
  "v_mfma_f32_16x16x32_bf16 v[108:111], a[180:183], v[32:35], v[108:111]\n" \
  "v_mfma_f32_16x16x32_bf16 v[120:123], a[180:183], v[36:39], v[120:123]\n" \
  "s_waitcnt lgkmcnt(2)\n" \
  "v_mfma_f32_16x16x32_bf16 v[100:103], a[24:27], v[40:43], v[100:103]\n" \
  "ds_read_b128 v[32:35], v10 offset:8192\n" \
  "v_mfma_f32_16x16x32_bf16 v[112:115], a[24:27], v[44:47], v[112:115]\n" \
  "ds_read_b128 v[36:39], v10 offset:28672\n" \
  "v_mfma_f32_16x16x32_bf16 v[104:107], a[104:107], v[40:43], v[104:107]\n" \
  "buffer_load_dwordx4 v1, s[36:39], s68 offen offset:2048 sc1 lds\n" \
  "v_mfma_f32_16x16x32_bf16 v[116:119], a[104:107], v[44:47], v[116:119]\n" \
  "v_mfma_f32_16x16x32_bf16 v[108:111], a[184:187], v[40:43], v[108:111]\n" \
  "v_mfma_f32_16x16x32_bf16 v[120:123], a[184:187], v[44:47], v[120:123]\n" \
  "s_waitcnt lgkmcnt(2)\n" \
  "v_mfma_f32_16x16x32_bf16 v[100:103], a[28:31], v[48:51], v[100:103]\n" \
  "ds_read_b128 v[40:43], v10 offset:9216\n" \
  "v_mfma_f32_16x16x32_bf16 v[112:115], a[28:31], v[52:55], v[112:115]\n" \
  "ds_read_b128 v[44:47], v10 offset:29696\n" \
  "v_mfma_f32_16x16x32_bf16 v[104:107], a[108:111], v[48:51], v[104:107]\n" \
  "buffer_load_dwordx4 v1, s[36:39], s68 offen offset:3072 sc1 lds\n" \
  "v_mfma_f32_16x16x32_bf16 v[116:119], a[108:111], v[52:55], v[116:119]\n" \
  "v_mfma_f32_16x16x32_bf16 v[108:111], a[188:191], v[48:51], v[108:111]\n" \
  "v_mfma_f32_16x16x32_bf16 v[120:123], a[188:191], v[52:55], v[120:123]\n" \
  "s_waitcnt lgkmcnt(2)\n" \
  "v_mfma_f32_16x16x32_bf16 v[100:103], a[32:35], v[32:35], v[100:103]\n" \
  "ds_read_b128 v[48:51], v10 offset:10240\n" \
  "v_mfma_f32_16x16x32_bf16 v[112:115], a[32:35], v[36:39], v[112:115]\n" \
  "ds_read_b128 v[52:55], v10 offset:30720\n" \
  "v_mfma_f32_16x16x32_bf16 v[104:107], a[112:115], v[32:35], v[104:107]\n" \
  "s_add_u32 m0, s53, 45056\n" \
  "v_mfma_f32_16x16x32_bf16 v[116:119], a[112:115], v[36:39], v[116:119]\n" \
  "buffer_load_dwordx4 v1, s[36:39], s70 offen sc1 lds\n" \
  "v_mfma_f32_16x16x32_bf16 v[108:111], a[192:195], v[32:35], v[108:111]\n" \
  "v_mfma_f32_16x16x32_bf16 v[120:123], a[192:195], v[36:39], v[120:123]\n" \
  "s_waitcnt lgkmcnt(2)\n" \
  "v_mfma_f32_16x16x32_bf16 v[100:103], a[36:39], v[40:43], v[100:103]\n" \
  "ds_read_b128 v[32:35], v10 offset:11264\n" \
  "v_mfma_f32_16x16x32_bf16 v[112:115], a[36:39], v[44:47], v[112:115]\n" \
  "ds_read_b128 v[36:39], v10 offset:31744\n" \
  "v_mfma_f32_16x16x32_bf16 v[104:107], a[116:119], v[40:43], v[104:107]\n" \
  "buffer_load_dwordx4 v1, s[36:39], s70 offen offset:1024 sc1 lds\n" \
  "v_mfma_f32_16x16x32_bf16 v[116:119], a[116:119], v[44:47], v[116:119]\n" \
  "v_mfma_f32_16x16x32_bf16 v[108:111], a[196:199], v[40:43], v[108:111]\n" \
  "v_mfma_f32_16x16x32_bf16 v[120:123], a[196:199], v[44:47], v[120:123]\n" \
  "s_waitcnt lgkmcnt(2)\n" \
  "v_mfma_f32_16x16x32_bf16 v[100:103], a[40:43], v[48:51], v[100:103]\n" \
  "ds_read_b128 v[40:43], v10 offset:12288\n" \
  "v_mfma_f32_16x16x32_bf16 v[112:115], a[40:43], v[52:55], v[112:115]\n" \
  "ds_read_b128 v[44:47], v10 offset:32768\n" \
  "v_mfma_f32_16x16x32_bf16 v[104:107], a[120:123], v[48:51], v[104:107]\n" \
  "buffer_load_dwordx4 v1, s[36:39], s70 offen offset:2048 sc1 lds\n" \
  "v_mfma_f32_16x16x32_bf16 v[116:119], a[120:123], v[52:55], v[116:119]\n" \
  "v_mfma_f32_16x16x32_bf16 v[108:111], a[200:203], v[48:51], v[108:111]\n" \
  "v_mfma_f32_16x16x32_bf16 v[120:123], a[200:203], v[52:55], v[120:123]\n" \
  "s_waitcnt vmcnt(7) lgkmcnt(2)\n" \
  "s_barrier\n" \
  "v_mfma_f32_16x16x32_bf16 v[100:103], a[44:47], v[32:35], v[100:103]\n" \
  "ds_read_b128 v[48:51], v10 offset:13312\n" \
  "v_mfma_f32_16x16x32_bf16 v[112:115], a[44:47], v[36:39], v[112:115]\n" \
  "ds_read_b128 v[52:55], v10 offset:33792\n" \
  "v_mfma_f32_16x16x32_bf16 v[104:107], a[124:127], v[32:35], v[104:107]\n" \
  "buffer_atomic_add v11, v4, s[40:43], s54 offen\n" \
  "v_mfma_f32_16x16x32_bf16 v[116:119], a[124:127], v[36:39], v[116:119]\n" \
  "s_add_u32 s54, s54, s55\n" \
  "v_mfma_f32_16x16x32_bf16 v[108:111], a[204:207], v[32:35], v[108:111]\n" \
  "s_and_b32 s54, s54, 0xffffdfff\n" \
  "v_mfma_f32_16x16x32_bf16 v[120:123], a[204:207], v[36:39], v[120:123]\n" \
  "s_waitcnt lgkmcnt(2)\n" \
  "v_mfma_f32_16x16x32_bf16 v[100:103], a[48:51], v[40:43], v[100:103]\n" \
  "ds_read_b128 v[32:35], v10 offset:14336\n" \
  "v_mfma_f32_16x16x32_bf16 v[112:115], a[48:51], v[44:47], v[112:115]\n" \
  "ds_read_b128 v[36:39], v10 offset:34816\n" \
  "v_mfma_f32_16x16x32_bf16 v[104:107], a[128:131], v[40:43], v[104:107]\n" \
  "buffer_load_dwordx4 v1, s[36:39], s70 offen offset:3072 sc1 lds\n" \
  "v_mfma_f32_16x16x32_bf16 v[116:119], a[128:131], v[44:47], v[116:119]\n" \
  "v_mfma_f32_16x16x32_bf16 v[108:111], a[208:211], v[40:43], v[108:111]\n" \
  "v_mfma_f32_16x16x32_bf16 v[120:123], a[208:211], v[44:47], v[120:123]\n" \
  "s_waitcnt lgkmcnt(2)\n" \
  "v_mfma_f32_16x16x32_bf16 v[100:103], a[52:55], v[48:51], v[100:103]\n" \
  "ds_read_b128 v[40:43], v10 offset:15360\n" \
  "v_mfma_f32_16x16x32_bf16 v[112:115], a[52:55], v[52:55], v[112:115]\n" \
  "ds_read_b128 v[44:47], v10 offset:35840\n" \
  "v_mfma_f32_16x16x32_bf16 v[104:107], a[132:135], v[48:51], v[104:107]\n" \
  "s_add_u32 m0, s53, 49152\n" \
  "v_mfma_f32_16x16x32_bf16 v[116:119], a[132:135], v[52:55], v[116:119]\n" \
  "buffer_load_dwordx4 v1, s[36:39], s71 offen sc1 lds\n" \
  "v_mfma_f32_16x16x32_bf16 v[108:111], a[212:215], v[48:51], v[108:111]\n" \
  "v_mfma_f32_16x16x32_bf16 v[120:123], a[212:215], v[52:55], v[120:123]\n" \
  "s_waitcnt lgkmcnt(2)\n" \
  "v_mfma_f32_16x16x32_bf16 v[100:103], a[56:59], v[32:35], v[100:103]\n" \
  "ds_read_b128 v[48:51], v10 offset:16384\n" \
  "v_mfma_f32_16x16x32_bf16 v[112:115], a[56:59], v[36:39], v[112:115]\n" \
  "ds_read_b128 v[52:55], v10 offset:36864\n" \
  "v_mfma_f32_16x16x32_bf16 v[104:107], a[136:139], v[32:35], v[104:107]\n" \
  "buffer_load_dwordx4 v1, s[36:39], s71 offen offset:1024 sc1 lds\n" \
  "v_mfma_f32_16x16x32_bf16 v[116:119], a[136:139], v[36:39], v[116:119]\n" \
  "v_mfma_f32_16x16x32_bf16 v[108:111], a[216:219], v[32:35], v[108:111]\n" \
  "v_mfma_f32_16x16x32_bf16 v[120:123], a[216:219], v[36:39], v[120:123]\n" \
  "s_waitcnt lgkmcnt(2)\n" \
  "v_mfma_f32_16x16x32_bf16 v[100:103], a[60:63], v[40:43], v[100:103]\n" \
  "ds_read_b128 v[32:35], v10 offset:17408\n" \
  "v_mfma_f32_16x16x32_bf16 v[112:115], a[60:63], v[44:47], v[112:115]\n" \
  "ds_read_b128 v[36:39], v10 offset:37888\n" \
  "v_mfma_f32_16x16x32_bf16 v[104:107], a[140:143], v[40:43], v[104:107]\n" \
  "s_add_u32 s68, s68, 40960\n" \
  "v_mfma_f32_16x16x32_bf16 v[116:119], a[140:143], v[44:47], v[116:119]\n" \
  "v_mfma_f32_16x16x32_bf16 v[108:111], a[220:223], v[40:43], v[108:111]\n" \
  "v_mfma_f32_16x16x32_bf16 v[120:123], a[220:223], v[44:47], v[120:123]\n" \
  "s_waitcnt lgkmcnt(2)\n" \
  "v_mfma_f32_16x16x32_bf16 v[100:103], a[64:67], v[48:51], v[100:103]\n" \
  "ds_read_b128 v[40:43], v10 offset:18432\n" \
  "v_mfma_f32_16x16x32_bf16 v[112:115], a[64:67], v[52:55], v[112:115]\n" \
  "ds_read_b128 v[44:47], v10 offset:38912\n" \
  "v_mfma_f32_16x16x32_bf16 v[104:107], a[144:147], v[48:51], v[104:107]\n" \
  "v_readlane_b32 s69, v3, s72\n" \
  "v_mfma_f32_16x16x32_bf16 v[116:119], a[144:147], v[52:55], v[116:119]\n" \
  "s_add_u32 s72, s72, 1\n" \
  "v_mfma_f32_16x16x32_bf16 v[108:111], a[224:227], v[48:51], v[108:111]\n" \
  "v_mfma_f32_16x16x32_bf16 v[120:123], a[224:227], v[52:55], v[120:123]\n" \
  "s_waitcnt lgkmcnt(2)\n" \
  "v_mfma_f32_16x16x32_bf16 v[100:103], a[68:71], v[32:35], v[100:103]\n" \
  "ds_read_b128 v[48:51], v10 offset:19456\n" \
  "v_mfma_f32_16x16x32_bf16 v[112:115], a[68:71], v[36:39], v[112:115]\n" \
  "ds_read_b128 v[52:55], v10 offset:39936\n" \
  "v_mfma_f32_16x16x32_bf16 v[104:107], a[148:151], v[32:35], v[104:107]\n" \
  "v_mfma_f32_16x16x32_bf16 v[116:119], a[148:151], v[36:39], v[116:119]\n" \
  "v_mfma_f32_16x16x32_bf16 v[108:111], a[228:231], v[32:35], v[108:111]\n" \
  "v_mfma_f32_16x16x32_bf16 v[120:123], a[228:231], v[36:39], v[120:123]\n" \
  "s_waitcnt vmcnt(11) lgkmcnt(0)\n" \
  "s_barrier\n" \
  "v_mfma_f32_16x16x32_bf16 v[100:103], a[72:75], v[40:43], v[100:103]\n" \
  "v_mfma_f32_16x16x32_bf16 v[112:115], a[72:75], v[44:47], v[112:115]\n" \
  "v_mfma_f32_16x16x32_bf16 v[104:107], a[152:155], v[40:43], v[104:107]\n" \
  "v_mfma_f32_16x16x32_bf16 v[116:119], a[152:155], v[44:47], v[116:119]\n" \
  "v_mfma_f32_16x16x32_bf16 v[108:111], a[232:235], v[40:43], v[108:111]\n" \
  "v_mfma_f32_16x16x32_bf16 v[120:123], a[232:235], v[44:47], v[120:123]\n" \
  "v_mfma_f32_16x16x32_bf16 v[100:103], a[76:79], v[48:51], v[100:103]\n" \
  "s_cmp_eq_u32 s52, 60\n" \
  "v_mfma_f32_16x16x32_bf16 v[112:115], a[76:79], v[52:55], v[112:115]\n" \
  "v_mfma_f32_16x16x32_bf16 v[104:107], a[156:159], v[48:51], v[104:107]\n" \
  "v_mfma_f32_16x16x32_bf16 v[116:119], a[156:159], v[52:55], v[116:119]\n" \
  "v_mfma_f32_16x16x32_bf16 v[108:111], a[236:239], v[48:51], v[108:111]\n" \
  "v_mfma_f32_16x16x32_bf16 v[120:123], a[236:239], v[52:55], v[120:123]\n" \
  "LPE_%=:\n" \
  "s_nop 6\n" \
  "v_cvt_pk_bf16_f32 v16, v100, v101\n" \
  "v_cvt_pk_bf16_f32 v17, v102, v103\n" \
  "v_cvt_pk_bf16_f32 v18, v104, v105\n" \
  "v_cvt_pk_bf16_f32 v19, v106, v107\n" \
  "buffer_store_dwordx4 v[16:19], v2, s[40:43], s69 offen offset:0 sc1\n" \
  "v_cvt_pk_bf16_f32 v20, v112, v113\n" \
  "v_cvt_pk_bf16_f32 v21, v114, v115\n" \
  "v_cvt_pk_bf16_f32 v22, v116, v117\n" \
  "v_cvt_pk_bf16_f32 v23, v118, v119\n" \
  "buffer_store_dwordx4 v[20:23], v2, s[40:43], s69 offen offset:1024 sc1\n" \
  "v_cvt_pk_bf16_f32 v24, v108, v109\n" \
  "v_cvt_pk_bf16_f32 v25, v110, v111\n" \
  "v_cvt_pk_bf16_f32 v26, v120, v121\n" \
  "v_cvt_pk_bf16_f32 v27, v122, v123\n" \
  "buffer_store_dwordx4 v[24:27], v2, s[40:43], s69 offen offset:2048 sc1\n" \
  "s_waitcnt vmcnt(0) lgkmcnt(0)\n" \
  "s_barrier\n" \
  "buffer_atomic_add v11, v4, s[40:43], s54 offen\n" \
  "s_add_u32 s54, s54, s55\n" \
  "s_and_b32 s54, s54, 0xffffdfff\n"
#define VO_CLOBBERS \
  "v24", "v25", "v26", "v27", "v28", "v29", "v30", "v31", "v32", "v33", "v34", "v35", "v36", "v37", "v38", "v39", "v40", "v41", "v42", "v43", "v44", \
  "v45", "v46", "v47", "v48", "v49", "v50", "v51", "v52", "v53", "v54", "v55", "v56", "v57", "v58", "v59", "v60", "v61", "v62", "v63", "v64", "v65", \
  "v66", "v67", "v68", "v69", "v70", "v71", "v72", "v73", "v74", "v75", "v76", "v77", "v78", "v79", "v80", "v81", "v82", "v83", "v84", "v85", "v86", \
  "v87", "v88", "v89", "v90", "v91", "v92", "v93", "v94", "v95", "v100", "v101", "v102", "v103", "v104", "v105", "v106", "v107", "v108", "v109", "v110", \
  "v111", "v112", "v113", "v114", "v115", "v116", "v117", "v118", "v119", "v120", "v121", "v122", "v123", "v124", "v125", "v126", "v127", "v128", \
  "v129", "v130", "v131", "v132", "v133", "v134", "v135", "v136", "v137", "v138", "v139", "v140", "v141", "v142", "v143", "v144", "v145", "v146", \
  "v147", "v148", "v149", "v150", "v151", "v152", "v153", "v154", "v155", "v156", "v157", "v158", "v159", "v160", "v161", "v162", "v163", "v164", \
  "v165", "v166", "v167", "v168", "v169", "v170", "v171", "v172", "v173", "v174", "v175", "v176", "v177", "v178", "v179", "v180", "v181", "v182", \
  "v183", "v184", "v185", "v186", "v187", "v188", "v189", "v190", "v191", "v192", "v193", "v194", "v195", "v196", "v197", "v198", "v199", "v200", \
  "v201", "v202", "v203", "v204", "v205", "v206", "v207", "v220", "v221", "v222", "v223", "v224", "v225", "v226", "v227", "v228", "v229", "v230", \
  "v231", "v232", "v233", "v234", "v235", "v236", "v237", "v238", "v239", "v240", "v241", "v242", "v243", "s41", "s42", "s43", "s44", "s45", "s46", \
  "s47", "s48", "s49", "s50", "s51", "s52", "s53", "s54", "s55"
#define VO_ASM \
  "v_mov_b32 v91, 1\n" \
  "v_mov_b32 v92, 2\n" \
  "s_mov_b32 s41, 0\n" \
  "s_mov_b32 s48, 0\n" \
  "s_mov_b32 s47, 1073741824\n" \
  "s_mov_b32 s44, 1073741824\n" \
  "s_mov_b32 s53, 1073741824\n" \
  "v_readlane_b32 s42, v4, 0\n" \
  "s_mul_i32 s50, s40, 3072\n" \
  "s_add_u32 s51, s50, 24576\n" \
  "s_lshl_b32 s52, s40, 12\n" \
  "s_add_u32 s52, s52, 49152\n" \
  "s_add_u32 s49, s41, 0\n" \
  "s_min_u32 s46, s49, 63\n" \
  "v_mad_u32_u24 v88, v8, s46, v7\n" \
  "v_mad_i32_i24 v89, v9, s46, v11\n" \
  "v_mad_i32_i24 v89, v10, s49, v89\n" \
  "buffer_load_dword v90, v88, s[36:39], 0 offen sc1\n" \
  "s_waitcnt vmcnt(0)\n" \
  "v_cmp_gt_i32 vcc, v89, v90\n" \
  "s_cbranch_vccnz LPS_%=\n" \
  "LKS_%=:\n" \
  "s_add_u32 m0, s50, 0\n" \
  "s_nop 0\n" \
  "buffer_load_dwordx4 v1, s[36:39], s42 offen offset:0 sc1 lds\n" \
  "buffer_load_dwordx4 v1, s[36:39], s42 offen offset:1024 sc1 lds\n" \
  "buffer_load_dwordx4 v1, s[36:39], s42 offen offset:2048 sc1 lds\n" \
  "s_waitcnt vmcnt(0)\n" \
  "s_barrier\n" \
  "ds_read_b128 v[40:43], v0 offset:0\n" \
  "ds_read_b128 v[44:47], v0 offset:3072\n" \
  "ds_read_b128 v[48:51], v0 offset:6144\n" \
  "ds_read_b128 v[52:55], v0 offset:9216\n" \
  "ds_read_b64 v[56:57], v0 offset:2048\n" \
  "ds_read_b64 v[58:59], v0 offset:5120\n" \
  "ds_read_b64 v[60:61], v0 offset:8192\n" \
  "ds_read_b64 v[62:63], v0 offset:11264\n" \
  "s_waitcnt lgkmcnt(0)\n" \
  "ds_read_b128 v[64:67], v0 offset:1024\n" \
  "ds_read_b128 v[68:71], v0 offset:4096\n" \
  "ds_read_b128 v[72:75], v0 offset:7168\n" \
  "ds_read_b128 v[76:79], v0 offset:10240\n" \
  "ds_read_b64 v[80:81], v0 offset:2056\n" \
  "ds_read_b64 v[82:83], v0 offset:5128\n" \
  "ds_read_b64 v[84:85], v0 offset:8200\n" \
  "ds_read_b64 v[86:87], v0 offset:11272\n" \
  "s_waitcnt lgkmcnt(0)\n" \
  "LL_%=:\n" \
  "v_mfma_f32_16x16x32_bf16 v[24:27], v[40:43], a[0:3], 0\n" \
  "s_add_u32 s49, s41, 1\n" \
  "s_min_u32 s46, s49, 63\n" \
  "v_mad_u32_u24 v88, v8, s46, v7\n" \
  "v_mfma_f32_16x16x32_bf16 v[28:31], v[64:67], a[0:3], 0\n" \
  "v_mad_i32_i24 v89, v9, s46, v11\n" \
  "v_mad_i32_i24 v89, v10, s49, v89\n" \
  "v_mfma_f32_16x16x32_bf16 v[24:27], v[44:47], a[4:7], v[24:27]\n" \
  "v_cvt_pk_bf16_f32 v240, v32, v33\n" \
  "v_mfma_f32_16x16x32_bf16 v[28:31], v[68:71], a[4:7], v[28:31]\n" \
  "v_cvt_pk_bf16_f32 v241, v34, v35\n" \
  "buffer_load_dword v90, v88, s[36:39], 0 offen sc1\n" \
  "v_mfma_f32_16x16x32_bf16 v[24:27], v[48:51], a[8:11], v[24:27]\n" \
  "v_cvt_pk_bf16_f32 v242, v36, v37\n" \
  "v_mfma_f32_16x16x32_bf16 v[28:31], v[72:75], a[8:11], v[28:31]\n" \
  "v_cvt_pk_bf16_f32 v243, v38, v39\n" \
  "v_mfma_f32_16x16x32_bf16 v[24:27], v[52:55], a[12:15], v[24:27]\n" \
  "buffer_store_dwordx4 v[240:243], v3, s[36:39], s44 offen sc1\n" \
  "v_mfma_f32_16x16x32_bf16 v[28:31], v[76:79], a[12:15], v[28:31]\n" \
  "s_add_u32 s44, s44, 0x40000\n" \
  "v_mfma_f32_16x16x32_bf16 v[24:27], v[56:59], a[16:19], v[24:27]\n" \
  "v_readlane_b32 s43, v5, s41\n" \
  "v_readlane_b32 s44, v6, s41\n" \
  "v_mfma_f32_16x16x32_bf16 v[28:31], v[80:83], a[16:19], v[28:31]\n" \
  "v_readlane_b32 s42, v4, s46\n" \
  "v_mfma_f32_16x16x32_bf16 v[24:27], v[60:63], a[20:23], v[24:27]\n" \
  "s_cmp_lt_u32 s41, 64\n" \
  "s_cselect_b32 s43, s43, 1073741824\n" \
  "s_cselect_b32 s44, s44, 1073741824\n" \
  "v_mfma_f32_16x16x32_bf16 v[28:31], v[84:87], a[20:23], v[28:31]\n" \
  "v_mfma_f32_16x16x32_bf16 v[32:35], v[40:43], a[24:27], 0\n" \
  "v_mfma_f32_16x16x32_bf16 v[36:39], v[64:67], a[24:27], 0\n" \
  "v_mfma_f32_16x16x32_bf16 v[32:35], v[44:47], a[28:31], v[32:35]\n" \
  "v_cvt_pk_bf16_f32 v236, v24, v25\n" \
  "v_mfma_f32_16x16x32_bf16 v[36:39], v[68:71], a[28:31], v[36:39]\n" \
  "v_cvt_pk_bf16_f32 v237, v26, v27\n" \
  "v_mfma_f32_16x16x32_bf16 v[32:35], v[48:51], a[32:35], v[32:35]\n" \
  "v_cvt_pk_bf16_f32 v238, v28, v29\n" \
  "v_mfma_f32_16x16x32_bf16 v[36:39], v[72:75], a[32:35], v[36:39]\n" \
  "v_cvt_pk_bf16_f32 v239, v30, v31\n" \
  "v_mfma_f32_16x16x32_bf16 v[32:35], v[52:55], a[36:39], v[32:35]\n" \
  "buffer_store_dwordx4 v[236:239], v2, s[36:39], s43 offen sc1\n" \
  "v_mfma_f32_16x16x32_bf16 v[36:39], v[76:79], a[36:39], v[36:39]\n" \
  "s_add_u32 s43, s43, 0x40000\n" \
  "v_mfma_f32_16x16x32_bf16 v[32:35], v[56:59], a[40:43], v[32:35]\n" \
  "s_sub_u32 s45, s41, 6\n" \
  "v_readlane_b32 s54, v4, s45\n" \
  "v_mfma_f32_16x16x32_bf16 v[36:39], v[80:83], a[40:43], v[36:39]\n" \
  "s_cmp_lt_u32 s41, 6\n" \
  "s_cselect_b32 s54, 1073741824, s54\n" \
  "v_mfma_f32_16x16x32_bf16 v[32:35], v[60:63], a[44:47], v[32:35]\n" \
  "s_add_u32 s54, s54, 3072\n" \
  "s_xor_b32 s55, s54, 0x2000\n" \
  "v_mfma_f32_16x16x32_bf16 v[36:39], v[84:87], a[44:47], v[36:39]\n" \
  "v_mfma_f32_16x16x32_bf16 v[24:27], v[40:43], a[48:51], 0\n" \
  "v_mfma_f32_16x16x32_bf16 v[28:31], v[64:67], a[48:51], 0\n" \
  "v_mfma_f32_16x16x32_bf16 v[24:27], v[44:47], a[52:55], v[24:27]\n" \
  "v_cvt_pk_bf16_f32 v240, v32, v33\n" \
  "v_mfma_f32_16x16x32_bf16 v[28:31], v[68:71], a[52:55], v[28:31]\n" \
  "v_cvt_pk_bf16_f32 v241, v34, v35\n" \
  "v_mfma_f32_16x16x32_bf16 v[24:27], v[48:51], a[56:59], v[24:27]\n" \
  "v_cvt_pk_bf16_f32 v242, v36, v37\n" \
  "v_mfma_f32_16x16x32_bf16 v[28:31], v[72:75], a[56:59], v[28:31]\n" \
  "v_cvt_pk_bf16_f32 v243, v38, v39\n" \
  "v_mfma_f32_16x16x32_bf16 v[24:27], v[52:55], a[60:63], v[24:27]\n" \
  "buffer_store_dwordx4 v[240:243], v2, s[36:39], s43 offen sc1\n" \
  "v_mfma_f32_16x16x32_bf16 v[28:31], v[76:79], a[60:63], v[28:31]\n" \
  "s_add_u32 s43, s43, 0x40000\n" \
  "v_mfma_f32_16x16x32_bf16 v[24:27], v[56:59], a[64:67], v[24:27]\n" \
  "v_mfma_f32_16x16x32_bf16 v[28:31], v[80:83], a[64:67], v[28:31]\n" \
  "v_mfma_f32_16x16x32_bf16 v[24:27], v[60:63], a[68:71], v[24:27]\n" \
  "v_mfma_f32_16x16x32_bf16 v[28:31], v[84:87], a[68:71], v[28:31]\n" \
  "v_mfma_f32_16x16x32_bf16 v[32:35], v[40:43], a[72:75], 0\n" \
  "s_waitcnt vmcnt(2)\n" \
  "s_barrier\n" \
  "v_mfma_f32_16x16x32_bf16 v[36:39], v[64:67], a[72:75], 0\n" \
  "buffer_atomic_add v91, v12, s[36:39], s47 offen\n" \
  "v_mfma_f32_16x16x32_bf16 v[32:35], v[44:47], a[76:79], v[32:35]\n" \
  "v_cvt_pk_bf16_f32 v236, v24, v25\n" \
  "v_mfma_f32_16x16x32_bf16 v[36:39], v[68:71], a[76:79], v[36:39]\n" \
  "v_cvt_pk_bf16_f32 v237, v26, v27\n" \
  "v_mfma_f32_16x16x32_bf16 v[32:35], v[48:51], a[80:83], v[32:35]\n" \
  "v_cvt_pk_bf16_f32 v238, v28, v29\n" \
  "buffer_atomic_add v92, v13, s[36:39], s53 offen\n" \
  "v_mfma_f32_16x16x32_bf16 v[36:39], v[72:75], a[80:83], v[36:39]\n" \
  "v_cvt_pk_bf16_f32 v239, v30, v31\n" \
  "v_mfma_f32_16x16x32_bf16 v[32:35], v[52:55], a[84:87], v[32:35]\n" \
  "buffer_store_dwordx4 v[236:239], v2, s[36:39], s43 offen sc1\n" \
  "v_mfma_f32_16x16x32_bf16 v[36:39], v[76:79], a[84:87], v[36:39]\n" \
  "s_add_u32 s43, s43, 0x40000\n" \
  "v_mfma_f32_16x16x32_bf16 v[32:35], v[56:59], a[88:91], v[32:35]\n" \
  "v_cmp_gt_i32 vcc, v89, v90\n" \
  "s_cbranch_vccnz LP0_%=\n" \
  "LK0_%=:\n" \
  "v_mfma_f32_16x16x32_bf16 v[36:39], v[80:83], a[88:91], v[36:39]\n" \
  "s_add_u32 m0, s50, 12288\n" \
  "s_nop 0\n" \
  "buffer_load_dwordx4 v1, s[36:39], s42 offen offset:0 sc1 lds\n" \
  "v_mfma_f32_16x16x32_bf16 v[32:35], v[60:63], a[92:95], v[32:35]\n" \
  "v_mfma_f32_16x16x32_bf16 v[36:39], v[84:87], a[92:95], v[36:39]\n" \
  "buffer_load_dwordx4 v1, s[36:39], s42 offen offset:1024 sc1 lds\n" \
  "v_mfma_f32_16x16x32_bf16 v[24:27], v[40:43], a[96:99], 0\n" \
  "v_mfma_f32_16x16x32_bf16 v[28:31], v[64:67], a[96:99], 0\n" \
  "buffer_load_dwordx4 v1, s[36:39], s42 offen offset:2048 sc1 lds\n" \
  "v_mfma_f32_16x16x32_bf16 v[24:27], v[44:47], a[100:103], v[24:27]\n" \
  "v_cvt_pk_bf16_f32 v240, v32, v33\n" \
  "v_mfma_f32_16x16x32_bf16 v[28:31], v[68:71], a[100:103], v[28:31]\n" \
  "v_cvt_pk_bf16_f32 v241, v34, v35\n" \
  "v_mfma_f32_16x16x32_bf16 v[24:27], v[48:51], a[104:107], v[24:27]\n" \
  "v_cvt_pk_bf16_f32 v242, v36, v37\n" \
  "v_mfma_f32_16x16x32_bf16 v[28:31], v[72:75], a[104:107], v[28:31]\n" \
  "v_cvt_pk_bf16_f32 v243, v38, v39\n" \
  "v_mfma_f32_16x16x32_bf16 v[24:27], v[52:55], a[108:111], v[24:27]\n" \
  "buffer_store_dwordx4 v[240:243], v2, s[36:39], s43 offen sc1\n" \
  "v_mfma_f32_16x16x32_bf16 v[28:31], v[76:79], a[108:111], v[28:31]\n" \
  "s_add_u32 s43, s43, 0x40000\n" \
  "v_mfma_f32_16x16x32_bf16 v[24:27], v[56:59], a[112:115], v[24:27]\n" \
  "s_lshr_b32 s45, s41, 4\n" \
  "s_lshl_b32 s45, s45, 13\n" \
  "v_mfma_f32_16x16x32_bf16 v[28:31], v[80:83], a[112:115], v[28:31]\n" \
  "s_lshl_b32 s47, s41, 9\n" \
  "s_add_u32 s47, s47, s45\n" \
  "v_mfma_f32_16x16x32_bf16 v[24:27], v[60:63], a[116:119], v[24:27]\n" \
  "s_cmp_lt_u32 s41, 64\n" \
  "s_cselect_b32 s47, s47, 1073741824\n" \
  "v_mfma_f32_16x16x32_bf16 v[28:31], v[84:87], a[116:119], v[28:31]\n" \
  "v_mfma_f32_16x16x32_bf16 v[32:35], v[40:43], a[120:123], 0\n" \
  "s_add_u32 m0, s51, 0\n" \
  "s_nop 0\n" \
  "buffer_load_dwordx4 v1, s[36:39], s54 offen offset:0 sc1 lds\n" \
  "v_mfma_f32_16x16x32_bf16 v[36:39], v[64:67], a[120:123], 0\n" \
  "v_mfma_f32_16x16x32_bf16 v[32:35], v[44:47], a[124:127], v[32:35]\n" \
  "v_cvt_pk_bf16_f32 v236, v24, v25\n" \
  "v_mfma_f32_16x16x32_bf16 v[36:39], v[68:71], a[124:127], v[36:39]\n" \
  "v_cvt_pk_bf16_f32 v237, v26, v27\n" \
  "buffer_load_dwordx4 v1, s[36:39], s54 offen offset:1024 sc1 lds\n" \
  "v_mfma_f32_16x16x32_bf16 v[32:35], v[48:51], a[128:131], v[32:35]\n" \
  "v_cvt_pk_bf16_f32 v238, v28, v29\n" \
  "v_mfma_f32_16x16x32_bf16 v[36:39], v[72:75], a[128:131], v[36:39]\n" \
  "v_cvt_pk_bf16_f32 v239, v30, v31\n" \
  "v_mfma_f32_16x16x32_bf16 v[32:35], v[52:55], a[132:135], v[32:35]\n" \
  "buffer_store_dwordx4 v[236:239], v2, s[36:39], s43 offen sc1\n" \
  "v_mfma_f32_16x16x32_bf16 v[36:39], v[76:79], a[132:135], v[36:39]\n" \
  "s_add_u32 s43, s43, 0x40000\n" \
  "v_mfma_f32_16x16x32_bf16 v[32:35], v[56:59], a[136:139], v[32:35]\n" \
  "v_mfma_f32_16x16x32_bf16 v[36:39], v[80:83], a[136:139], v[36:39]\n" \
  "buffer_load_dwordx4 v1, s[36:39], s54 offen offset:2048 sc1 lds\n" \
  "v_mfma_f32_16x16x32_bf16 v[32:35], v[60:63], a[140:143], v[32:35]\n" \
  "v_mfma_f32_16x16x32_bf16 v[36:39], v[84:87], a[140:143], v[36:39]\n" \
  "v_mfma_f32_16x16x32_bf16 v[24:27], v[40:43], a[144:147], 0\n" \
  "s_add_u32 m0, s52, 0\n" \
  "s_nop 0\n" \
  "buffer_load_dwordx4 v1, s[36:39], s55 offen offset:0 sc1 lds\n" \
  "v_mfma_f32_16x16x32_bf16 v[28:31], v[64:67], a[144:147], 0\n" \
  "v_mfma_f32_16x16x32_bf16 v[24:27], v[44:47], a[148:151], v[24:27]\n" \
  "v_cvt_pk_bf16_f32 v240, v32, v33\n" \
  "v_mfma_f32_16x16x32_bf16 v[28:31], v[68:71], a[148:151], v[28:31]\n" \
  "v_cvt_pk_bf16_f32 v241, v34, v35\n" \
  "buffer_load_dwordx4 v1, s[36:39], s55 offen offset:1024 sc1 lds\n" \
  "v_mfma_f32_16x16x32_bf16 v[24:27], v[48:51], a[152:155], v[24:27]\n" \
  "v_cvt_pk_bf16_f32 v242, v36, v37\n" \
  "v_mfma_f32_16x16x32_bf16 v[28:31], v[72:75], a[152:155], v[28:31]\n" \
  "v_cvt_pk_bf16_f32 v243, v38, v39\n" \
  "v_mfma_f32_16x16x32_bf16 v[24:27], v[52:55], a[156:159], v[24:27]\n" \
  "buffer_store_dwordx4 v[240:243], v2, s[36:39], s43 offen sc1\n" \
  "v_mfma_f32_16x16x32_bf16 v[28:31], v[76:79], a[156:159], v[28:31]\n" \
  "s_add_u32 s43, s43, 0x40000\n" \
  "v_mfma_f32_16x16x32_bf16 v[24:27], v[56:59], a[160:163], v[24:27]\n" \
  "v_mfma_f32_16x16x32_bf16 v[28:31], v[80:83], a[160:163], v[28:31]\n" \
  "buffer_load_dwordx4 v1, s[36:39], s55 offen offset:2048 sc1 lds\n" \
  "v_mfma_f32_16x16x32_bf16 v[24:27], v[60:63], a[164:167], v[24:27]\n" \
  "v_mfma_f32_16x16x32_bf16 v[28:31], v[84:87], a[164:167], v[28:31]\n" \
  "v_mfma_f32_16x16x32_bf16 v[32:35], v[40:43], a[168:171], 0\n" \
  "buffer_load_dwordx4 v14, s[36:39], s55 offen offset:3072 sc1 lds\n" \
  "v_mfma_f32_16x16x32_bf16 v[36:39], v[64:67], a[168:171], 0\n" \
  "v_mfma_f32_16x16x32_bf16 v[32:35], v[44:47], a[172:175], v[32:35]\n" \
  "v_cvt_pk_bf16_f32 v236, v24, v25\n" \
  "v_mfma_f32_16x16x32_bf16 v[36:39], v[68:71], a[172:175], v[36:39]\n" \
  "v_cvt_pk_bf16_f32 v237, v26, v27\n" \
  "v_mfma_f32_16x16x32_bf16 v[32:35], v[48:51], a[176:179], v[32:35]\n" \
  "v_cvt_pk_bf16_f32 v238, v28, v29\n" \
  "v_mfma_f32_16x16x32_bf16 v[36:39], v[72:75], a[176:179], v[36:39]\n" \
  "v_cvt_pk_bf16_f32 v239, v30, v31\n" \
  "v_mfma_f32_16x16x32_bf16 v[32:35], v[52:55], a[180:183], v[32:35]\n" \
  "buffer_store_dwordx4 v[236:239], v2, s[36:39], s43 offen sc1\n" \
  "v_mfma_f32_16x16x32_bf16 v[36:39], v[76:79], a[180:183], v[36:39]\n" \
  "s_add_u32 s43, s43, 0x40000\n" \
  "v_mfma_f32_16x16x32_bf16 v[32:35], v[56:59], a[184:187], v[32:35]\n" \
  "v_mfma_f32_16x16x32_bf16 v[36:39], v[80:83], a[184:187], v[36:39]\n" \
  "v_mfma_f32_16x16x32_bf16 v[32:35], v[60:63], a[188:191], v[32:35]\n" \
  "v_mfma_f32_16x16x32_bf16 v[36:39], v[84:87], a[188:191], v[36:39]\n" \
  "v_mfma_f32_16x16x32_bf16 v[24:27], v[40:43], a[192:195], 0\n" \
  "v_mfma_f32_16x16x32_bf16 v[28:31], v[64:67], a[192:195], 0\n" \
  "s_waitcnt vmcnt(11)\n" \
  "s_barrier\n" \
  "v_mfma_f32_16x16x32_bf16 v[24:27], v[44:47], a[196:199], v[24:27]\n" \
  "v_cvt_pk_bf16_f32 v240, v32, v33\n" \
  "ds_read_b128 v[100:103], v0 offset:12288\n" \
  "v_mfma_f32_16x16x32_bf16 v[28:31], v[68:71], a[196:199], v[28:31]\n" \
  "v_cvt_pk_bf16_f32 v241, v34, v35\n" \
  "ds_read_b128 v[104:107], v0 offset:15360\n" \
  "v_mfma_f32_16x16x32_bf16 v[24:27], v[48:51], a[200:203], v[24:27]\n" \
  "v_cvt_pk_bf16_f32 v242, v36, v37\n" \
  "ds_read_b128 v[108:111], v0 offset:18432\n" \
  "v_mfma_f32_16x16x32_bf16 v[28:31], v[72:75], a[200:203], v[28:31]\n" \
  "v_cvt_pk_bf16_f32 v243, v38, v39\n" \
  "ds_read_b128 v[112:115], v0 offset:21504\n" \
  "v_mfma_f32_16x16x32_bf16 v[24:27], v[52:55], a[204:207], v[24:27]\n" \
  "buffer_store_dwordx4 v[240:243], v2, s[36:39], s43 offen sc1\n" \
  "v_mfma_f32_16x16x32_bf16 v[28:31], v[76:79], a[204:207], v[28:31]\n" \
  "s_add_u32 s43, s43, 0x40000\n" \
  "ds_read_b64 v[116:117], v0 offset:14336\n" \
  "v_mfma_f32_16x16x32_bf16 v[24:27], v[56:59], a[208:211], v[24:27]\n" \
  "ds_read_b64 v[118:119], v0 offset:17408\n" \
  "v_mfma_f32_16x16x32_bf16 v[28:31], v[80:83], a[208:211], v[28:31]\n" \
  "ds_read_b64 v[120:121], v0 offset:20480\n" \
  "v_mfma_f32_16x16x32_bf16 v[24:27], v[60:63], a[212:215], v[24:27]\n" \
  "ds_read_b64 v[122:123], v0 offset:23552\n" \
  "v_mfma_f32_16x16x32_bf16 v[28:31], v[84:87], a[212:215], v[28:31]\n" \
  "ds_read_b128 v[124:127], v0 offset:13312\n" \
  "v_mfma_f32_16x16x32_bf16 v[32:35], v[40:43], a[216:219], 0\n" \
  "ds_read_b128 v[128:131], v0 offset:16384\n" \
  "v_mfma_f32_16x16x32_bf16 v[36:39], v[64:67], a[216:219], 0\n" \
  "ds_read_b128 v[132:135], v0 offset:19456\n" \
  "v_mfma_f32_16x16x32_bf16 v[32:35], v[44:47], a[220:223], v[32:35]\n" \
  "v_cvt_pk_bf16_f32 v236, v24, v25\n" \
  "ds_read_b128 v[136:139], v0 offset:22528\n" \
  "v_mfma_f32_16x16x32_bf16 v[36:39], v[68:71], a[220:223], v[36:39]\n" \
  "v_cvt_pk_bf16_f32 v237, v26, v27\n" \
  "ds_read_b64 v[140:141], v0 offset:14344\n" \
  "v_mfma_f32_16x16x32_bf16 v[32:35], v[48:51], a[224:227], v[32:35]\n" \
  "v_cvt_pk_bf16_f32 v238, v28, v29\n" \
  "ds_read_b64 v[142:143], v0 offset:17416\n" \
  "v_mfma_f32_16x16x32_bf16 v[36:39], v[72:75], a[224:227], v[36:39]\n" \
  "v_cvt_pk_bf16_f32 v239, v30, v31\n" \
  "ds_read_b64 v[144:145], v0 offset:20488\n" \
  "v_mfma_f32_16x16x32_bf16 v[32:35], v[52:55], a[228:231], v[32:35]\n" \
  "buffer_store_dwordx4 v[236:239], v3, s[36:39], s44 offen sc1\n" \
  "v_mfma_f32_16x16x32_bf16 v[36:39], v[76:79], a[228:231], v[36:39]\n" \
  "s_add_u32 s44, s44, 0x40000\n" \
  "ds_read_b64 v[146:147], v0 offset:23560\n" \
  "v_mfma_f32_16x16x32_bf16 v[32:35], v[56:59], a[232:235], v[32:35]\n" \
  "v_mfma_f32_16x16x32_bf16 v[36:39], v[80:83], a[232:235], v[36:39]\n" \
  "v_mfma_f32_16x16x32_bf16 v[32:35], v[60:63], a[236:239], v[32:35]\n" \
  "v_mfma_f32_16x16x32_bf16 v[36:39], v[84:87], a[236:239], v[36:39]\n" \
  "s_add_u32 s41, s41, 1\n" \
  "s_waitcnt lgkmcnt(0)\n" \
  "v_mfma_f32_16x16x32_bf16 v[24:27], v[100:103], a[0:3], 0\n" \
  "s_add_u32 s49, s41, 1\n" \
  "s_min_u32 s46, s49, 63\n" \
  "v_mad_u32_u24 v88, v8, s46, v7\n" \
  "v_mfma_f32_16x16x32_bf16 v[28:31], v[124:127], a[0:3], 0\n" \
  "v_mad_i32_i24 v89, v9, s46, v11\n" \
  "v_mad_i32_i24 v89, v10, s49, v89\n" \
  "v_mfma_f32_16x16x32_bf16 v[24:27], v[104:107], a[4:7], v[24:27]\n" \
  "v_cvt_pk_bf16_f32 v240, v32, v33\n" \
  "v_mfma_f32_16x16x32_bf16 v[28:31], v[128:131], a[4:7], v[28:31]\n" \
  "v_cvt_pk_bf16_f32 v241, v34, v35\n" \
  "buffer_load_dword v90, v88, s[36:39], 0 offen sc1\n" \
  "v_mfma_f32_16x16x32_bf16 v[24:27], v[108:111], a[8:11], v[24:27]\n" \
  "v_cvt_pk_bf16_f32 v242, v36, v37\n" \
  "v_mfma_f32_16x16x32_bf16 v[28:31], v[132:135], a[8:11], v[28:31]\n" \
  "v_cvt_pk_bf16_f32 v243, v38, v39\n" \
  "v_mfma_f32_16x16x32_bf16 v[24:27], v[112:115], a[12:15], v[24:27]\n" \
  "buffer_store_dwordx4 v[240:243], v3, s[36:39], s44 offen sc1\n" \
  "v_mfma_f32_16x16x32_bf16 v[28:31], v[136:139], a[12:15], v[28:31]\n" \
  "s_add_u32 s44, s44, 0x40000\n" \
  "v_mfma_f32_16x16x32_bf16 v[24:27], v[116:119], a[16:19], v[24:27]\n" \
  "v_readlane_b32 s43, v5, s41\n" \
  "v_readlane_b32 s44, v6, s41\n" \
  "v_mfma_f32_16x16x32_bf16 v[28:31], v[140:143], a[16:19], v[28:31]\n" \
  "v_readlane_b32 s42, v4, s46\n" \
  "v_mfma_f32_16x16x32_bf16 v[24:27], v[120:123], a[20:23], v[24:27]\n" \
  "s_cmp_lt_u32 s41, 64\n" \
  "s_cselect_b32 s43, s43, 1073741824\n" \
  "s_cselect_b32 s44, s44, 1073741824\n" \
  "v_mfma_f32_16x16x32_bf16 v[28:31], v[144:147], a[20:23], v[28:31]\n" \
  "v_mfma_f32_16x16x32_bf16 v[32:35], v[100:103], a[24:27], 0\n" \
  "v_mfma_f32_16x16x32_bf16 v[36:39], v[124:127], a[24:27], 0\n" \
  "v_mfma_f32_16x16x32_bf16 v[32:35], v[104:107], a[28:31], v[32:35]\n" \
  "v_cvt_pk_bf16_f32 v236, v24, v25\n" \
  "v_mfma_f32_16x16x32_bf16 v[36:39], v[128:131], a[28:31], v[36:39]\n" \
  "v_cvt_pk_bf16_f32 v237, v26, v27\n" \
  "v_mfma_f32_16x16x32_bf16 v[32:35], v[108:111], a[32:35], v[32:35]\n" \
  "v_cvt_pk_bf16_f32 v238, v28, v29\n" \
  "v_mfma_f32_16x16x32_bf16 v[36:39], v[132:135], a[32:35], v[36:39]\n" \
  "v_cvt_pk_bf16_f32 v239, v30, v31\n" \
  "v_mfma_f32_16x16x32_bf16 v[32:35], v[112:115], a[36:39], v[32:35]\n" \
  "buffer_store_dwordx4 v[236:239], v2, s[36:39], s43 offen sc1\n" \
  "v_mfma_f32_16x16x32_bf16 v[36:39], v[136:139], a[36:39], v[36:39]\n" \
  "s_add_u32 s43, s43, 0x40000\n" \
  "v_mfma_f32_16x16x32_bf16 v[32:35], v[116:119], a[40:43], v[32:35]\n" \
  "s_sub_u32 s45, s41, 6\n" \
  "v_readlane_b32 s54, v4, s45\n" \
  "v_mfma_f32_16x16x32_bf16 v[36:39], v[140:143], a[40:43], v[36:39]\n" \
  "s_cmp_lt_u32 s41, 6\n" \
  "s_cselect_b32 s54, 1073741824, s54\n" \
  "v_mfma_f32_16x16x32_bf16 v[32:35], v[120:123], a[44:47], v[32:35]\n" \
  "s_add_u32 s54, s54, 3072\n" \
  "s_xor_b32 s55, s54, 0x2000\n" \
  "v_mfma_f32_16x16x32_bf16 v[36:39], v[144:147], a[44:47], v[36:39]\n" \
  "v_mfma_f32_16x16x32_bf16 v[24:27], v[100:103], a[48:51], 0\n" \
  "v_mfma_f32_16x16x32_bf16 v[28:31], v[124:127], a[48:51], 0\n" \
  "v_mfma_f32_16x16x32_bf16 v[24:27], v[104:107], a[52:55], v[24:27]\n" \
  "v_cvt_pk_bf16_f32 v240, v32, v33\n" \
  "v_mfma_f32_16x16x32_bf16 v[28:31], v[128:131], a[52:55], v[28:31]\n" \
  "v_cvt_pk_bf16_f32 v241, v34, v35\n" \
  "v_mfma_f32_16x16x32_bf16 v[24:27], v[108:111], a[56:59], v[24:27]\n" \
  "v_cvt_pk_bf16_f32 v242, v36, v37\n" \
  "v_mfma_f32_16x16x32_bf16 v[28:31], v[132:135], a[56:59], v[28:31]\n" \
  "v_cvt_pk_bf16_f32 v243, v38, v39\n" \
  "v_mfma_f32_16x16x32_bf16 v[24:27], v[112:115], a[60:63], v[24:27]\n" \
  "buffer_store_dwordx4 v[240:243], v2, s[36:39], s43 offen sc1\n" \
  "v_mfma_f32_16x16x32_bf16 v[28:31], v[136:139], a[60:63], v[28:31]\n" \
  "s_add_u32 s43, s43, 0x40000\n" \
  "v_mfma_f32_16x16x32_bf16 v[24:27], v[116:119], a[64:67], v[24:27]\n" \
  "v_mfma_f32_16x16x32_bf16 v[28:31], v[140:143], a[64:67], v[28:31]\n" \
  "v_mfma_f32_16x16x32_bf16 v[24:27], v[120:123], a[68:71], v[24:27]\n" \
  "v_mfma_f32_16x16x32_bf16 v[28:31], v[144:147], a[68:71], v[28:31]\n" \
  "v_mfma_f32_16x16x32_bf16 v[32:35], v[100:103], a[72:75], 0\n" \
  "s_waitcnt vmcnt(2)\n" \
  "s_barrier\n" \
  "v_mfma_f32_16x16x32_bf16 v[36:39], v[124:127], a[72:75], 0\n" \
  "buffer_atomic_add v91, v12, s[36:39], s47 offen\n" \
  "v_mfma_f32_16x16x32_bf16 v[32:35], v[104:107], a[76:79], v[32:35]\n" \
  "v_cvt_pk_bf16_f32 v236, v24, v25\n" \
  "v_mfma_f32_16x16x32_bf16 v[36:39], v[128:131], a[76:79], v[36:39]\n" \
  "v_cvt_pk_bf16_f32 v237, v26, v27\n" \
  "v_mfma_f32_16x16x32_bf16 v[32:35], v[108:111], a[80:83], v[32:35]\n" \
  "v_cvt_pk_bf16_f32 v238, v28, v29\n" \
  "v_mfma_f32_16x16x32_bf16 v[36:39], v[132:135], a[80:83], v[36:39]\n" \
  "v_cvt_pk_bf16_f32 v239, v30, v31\n" \
  "v_mfma_f32_16x16x32_bf16 v[32:35], v[112:115], a[84:87], v[32:35]\n" \
  "buffer_store_dwordx4 v[236:239], v2, s[36:39], s43 offen sc1\n" \
  "v_mfma_f32_16x16x32_bf16 v[36:39], v[136:139], a[84:87], v[36:39]\n" \
  "s_add_u32 s43, s43, 0x40000\n" \
  "v_mfma_f32_16x16x32_bf16 v[32:35], v[116:119], a[88:91], v[32:35]\n" \
  "v_cmp_gt_i32 vcc, v89, v90\n" \
  "s_cbranch_vccnz LP1_%=\n" \
  "LK1_%=:\n" \
  "v_mfma_f32_16x16x32_bf16 v[36:39], v[140:143], a[88:91], v[36:39]\n" \
  "s_add_u32 m0, s50, 0\n" \
  "s_nop 0\n" \
  "buffer_load_dwordx4 v1, s[36:39], s42 offen offset:0 sc1 lds\n" \
  "v_mfma_f32_16x16x32_bf16 v[32:35], v[120:123], a[92:95], v[32:35]\n" \
  "v_mfma_f32_16x16x32_bf16 v[36:39], v[144:147], a[92:95], v[36:39]\n" \
  "buffer_load_dwordx4 v1, s[36:39], s42 offen offset:1024 sc1 lds\n" \
  "v_mfma_f32_16x16x32_bf16 v[24:27], v[100:103], a[96:99], 0\n" \
  "v_mfma_f32_16x16x32_bf16 v[28:31], v[124:127], a[96:99], 0\n" \
  "buffer_load_dwordx4 v1, s[36:39], s42 offen offset:2048 sc1 lds\n" \
  "v_mfma_f32_16x16x32_bf16 v[24:27], v[104:107], a[100:103], v[24:27]\n" \
  "v_cvt_pk_bf16_f32 v240, v32, v33\n" \
  "v_mfma_f32_16x16x32_bf16 v[28:31], v[128:131], a[100:103], v[28:31]\n" \
  "v_cvt_pk_bf16_f32 v241, v34, v35\n" \
  "v_mfma_f32_16x16x32_bf16 v[24:27], v[108:111], a[104:107], v[24:27]\n" \
  "v_cvt_pk_bf16_f32 v242, v36, v37\n" \
  "v_mfma_f32_16x16x32_bf16 v[28:31], v[132:135], a[104:107], v[28:31]\n" \
  "v_cvt_pk_bf16_f32 v243, v38, v39\n" \
  "v_mfma_f32_16x16x32_bf16 v[24:27], v[112:115], a[108:111], v[24:27]\n" \
  "buffer_store_dwordx4 v[240:243], v2, s[36:39], s43 offen sc1\n" \
  "v_mfma_f32_16x16x32_bf16 v[28:31], v[136:139], a[108:111], v[28:31]\n" \
  "s_add_u32 s43, s43, 0x40000\n" \
  "v_mfma_f32_16x16x32_bf16 v[24:27], v[116:119], a[112:115], v[24:27]\n" \
  "s_lshr_b32 s45, s41, 4\n" \
  "s_lshl_b32 s45, s45, 13\n" \
  "v_mfma_f32_16x16x32_bf16 v[28:31], v[140:143], a[112:115], v[28:31]\n" \
  "s_lshl_b32 s47, s41, 9\n" \
  "s_add_u32 s47, s47, s45\n" \
  "v_mfma_f32_16x16x32_bf16 v[24:27], v[120:123], a[116:119], v[24:27]\n" \
  "s_cmp_lt_u32 s41, 64\n" \
  "s_cselect_b32 s47, s47, 1073741824\n" \
  "v_mfma_f32_16x16x32_bf16 v[28:31], v[144:147], a[116:119], v[28:31]\n" \
  "v_mfma_f32_16x16x32_bf16 v[32:35], v[100:103], a[120:123], 0\n" \
  "s_add_u32 m0, s51, 12288\n" \
  "s_nop 0\n" \
  "buffer_load_dwordx4 v1, s[36:39], s54 offen offset:0 sc1 lds\n" \
  "v_mfma_f32_16x16x32_bf16 v[36:39], v[124:127], a[120:123], 0\n" \
  "v_mfma_f32_16x16x32_bf16 v[32:35], v[104:107], a[124:127], v[32:35]\n" \
  "v_cvt_pk_bf16_f32 v236, v24, v25\n" \
  "v_mfma_f32_16x16x32_bf16 v[36:39], v[128:131], a[124:127], v[36:39]\n" \
  "v_cvt_pk_bf16_f32 v237, v26, v27\n" \
  "buffer_load_dwordx4 v1, s[36:39], s54 offen offset:1024 sc1 lds\n" \
  "v_mfma_f32_16x16x32_bf16 v[32:35], v[108:111], a[128:131], v[32:35]\n" \
  "v_cvt_pk_bf16_f32 v238, v28, v29\n" \
  "v_mfma_f32_16x16x32_bf16 v[36:39], v[132:135], a[128:131], v[36:39]\n" \
  "v_cvt_pk_bf16_f32 v239, v30, v31\n" \
  "v_mfma_f32_16x16x32_bf16 v[32:35], v[112:115], a[132:135], v[32:35]\n" \
  "buffer_store_dwordx4 v[236:239], v2, s[36:39], s43 offen sc1\n" \
  "v_mfma_f32_16x16x32_bf16 v[36:39], v[136:139], a[132:135], v[36:39]\n" \
  "s_add_u32 s43, s43, 0x40000\n" \
  "v_mfma_f32_16x16x32_bf16 v[32:35], v[116:119], a[136:139], v[32:35]\n" \
  "v_mfma_f32_16x16x32_bf16 v[36:39], v[140:143], a[136:139], v[36:39]\n" \
  "buffer_load_dwordx4 v1, s[36:39], s54 offen offset:2048 sc1 lds\n" \
  "v_mfma_f32_16x16x32_bf16 v[32:35], v[120:123], a[140:143], v[32:35]\n" \
  "v_mfma_f32_16x16x32_bf16 v[36:39], v[144:147], a[140:143], v[36:39]\n" \
  "v_mfma_f32_16x16x32_bf16 v[24:27], v[100:103], a[144:147], 0\n" \
  "s_add_u32 m0, s52, 16384\n" \
  "s_nop 0\n" \
  "buffer_load_dwordx4 v1, s[36:39], s55 offen offset:0 sc1 lds\n" \
  "v_mfma_f32_16x16x32_bf16 v[28:31], v[124:127], a[144:147], 0\n" \
  "v_mfma_f32_16x16x32_bf16 v[24:27], v[104:107], a[148:151], v[24:27]\n" \
  "v_cvt_pk_bf16_f32 v240, v32, v33\n" \
  "v_mfma_f32_16x16x32_bf16 v[28:31], v[128:131], a[148:151], v[28:31]\n" \
  "v_cvt_pk_bf16_f32 v241, v34, v35\n" \
  "buffer_load_dwordx4 v1, s[36:39], s55 offen offset:1024 sc1 lds\n" \
  "v_mfma_f32_16x16x32_bf16 v[24:27], v[108:111], a[152:155], v[24:27]\n" \
  "v_cvt_pk_bf16_f32 v242, v36, v37\n" \
  "v_mfma_f32_16x16x32_bf16 v[28:31], v[132:135], a[152:155], v[28:31]\n" \
  "v_cvt_pk_bf16_f32 v243, v38, v39\n" \
  "v_mfma_f32_16x16x32_bf16 v[24:27], v[112:115], a[156:159], v[24:27]\n" \
  "buffer_store_dwordx4 v[240:243], v2, s[36:39], s43 offen sc1\n" \
  "v_mfma_f32_16x16x32_bf16 v[28:31], v[136:139], a[156:159], v[28:31]\n" \
  "s_add_u32 s43, s43, 0x40000\n" \
  "v_mfma_f32_16x16x32_bf16 v[24:27], v[116:119], a[160:163], v[24:27]\n" \
  "v_mfma_f32_16x16x32_bf16 v[28:31], v[140:143], a[160:163], v[28:31]\n" \
  "buffer_load_dwordx4 v1, s[36:39], s55 offen offset:2048 sc1 lds\n" \
  "v_mfma_f32_16x16x32_bf16 v[24:27], v[120:123], a[164:167], v[24:27]\n" \
  "v_mfma_f32_16x16x32_bf16 v[28:31], v[144:147], a[164:167], v[28:31]\n" \
  "v_mfma_f32_16x16x32_bf16 v[32:35], v[100:103], a[168:171], 0\n" \
  "buffer_load_dwordx4 v14, s[36:39], s55 offen offset:3072 sc1 lds\n" \
  "v_mfma_f32_16x16x32_bf16 v[36:39], v[124:127], a[168:171], 0\n" \
  "v_mfma_f32_16x16x32_bf16 v[32:35], v[104:107], a[172:175], v[32:35]\n" \
  "v_cvt_pk_bf16_f32 v236, v24, v25\n" \
  "v_mfma_f32_16x16x32_bf16 v[36:39], v[128:131], a[172:175], v[36:39]\n" \
  "v_cvt_pk_bf16_f32 v237, v26, v27\n" \
  "v_mfma_f32_16x16x32_bf16 v[32:35], v[108:111], a[176:179], v[32:35]\n" \
  "v_cvt_pk_bf16_f32 v238, v28, v29\n" \
  "v_mfma_f32_16x16x32_bf16 v[36:39], v[132:135], a[176:179], v[36:39]\n" \
  "v_cvt_pk_bf16_f32 v239, v30, v31\n" \
  "v_mfma_f32_16x16x32_bf16 v[32:35], v[112:115], a[180:183], v[32:35]\n" \
  "buffer_store_dwordx4 v[236:239], v2, s[36:39], s43 offen sc1\n" \
  "v_mfma_f32_16x16x32_bf16 v[36:39], v[136:139], a[180:183], v[36:39]\n" \
  "s_add_u32 s43, s43, 0x40000\n" \
  "v_mfma_f32_16x16x32_bf16 v[32:35], v[116:119], a[184:187], v[32:35]\n" \
  "v_mfma_f32_16x16x32_bf16 v[36:39], v[140:143], a[184:187], v[36:39]\n" \
  "v_mfma_f32_16x16x32_bf16 v[32:35], v[120:123], a[188:191], v[32:35]\n" \
  "v_mfma_f32_16x16x32_bf16 v[36:39], v[144:147], a[188:191], v[36:39]\n" \
  "v_mfma_f32_16x16x32_bf16 v[24:27], v[100:103], a[192:195], 0\n" \
  "v_mfma_f32_16x16x32_bf16 v[28:31], v[124:127], a[192:195], 0\n" \
  "s_waitcnt vmcnt(11)\n" \
  "s_barrier\n" \
  "v_mfma_f32_16x16x32_bf16 v[24:27], v[104:107], a[196:199], v[24:27]\n" \
  "v_cvt_pk_bf16_f32 v240, v32, v33\n" \
  "ds_read_b128 v[40:43], v0 offset:0\n" \
  "v_mfma_f32_16x16x32_bf16 v[28:31], v[128:131], a[196:199], v[28:31]\n" \
  "v_cvt_pk_bf16_f32 v241, v34, v35\n" \
  "ds_read_b128 v[44:47], v0 offset:3072\n" \
  "v_mfma_f32_16x16x32_bf16 v[24:27], v[108:111], a[200:203], v[24:27]\n" \
  "v_cvt_pk_bf16_f32 v242, v36, v37\n" \
  "ds_read_b128 v[48:51], v0 offset:6144\n" \
  "v_mfma_f32_16x16x32_bf16 v[28:31], v[132:135], a[200:203], v[28:31]\n" \
  "v_cvt_pk_bf16_f32 v243, v38, v39\n" \
  "ds_read_b128 v[52:55], v0 offset:9216\n" \
  "v_mfma_f32_16x16x32_bf16 v[24:27], v[112:115], a[204:207], v[24:27]\n" \
  "buffer_store_dwordx4 v[240:243], v2, s[36:39], s43 offen sc1\n" \
  "v_mfma_f32_16x16x32_bf16 v[28:31], v[136:139], a[204:207], v[28:31]\n" \
  "s_add_u32 s43, s43, 0x40000\n" \
  "ds_read_b64 v[56:57], v0 offset:2048\n" \
  "v_mfma_f32_16x16x32_bf16 v[24:27], v[116:119], a[208:211], v[24:27]\n" \
  "ds_read_b64 v[58:59], v0 offset:5120\n" \
  "v_mfma_f32_16x16x32_bf16 v[28:31], v[140:143], a[208:211], v[28:31]\n" \
  "ds_read_b64 v[60:61], v0 offset:8192\n" \
  "v_mfma_f32_16x16x32_bf16 v[24:27], v[120:123], a[212:215], v[24:27]\n" \
  "ds_read_b64 v[62:63], v0 offset:11264\n" \
  "v_mfma_f32_16x16x32_bf16 v[28:31], v[144:147], a[212:215], v[28:31]\n" \
  "ds_read_b128 v[64:67], v0 offset:1024\n" \
  "v_mfma_f32_16x16x32_bf16 v[32:35], v[100:103], a[216:219], 0\n" \
  "ds_read_b128 v[68:71], v0 offset:4096\n" \
  "v_mfma_f32_16x16x32_bf16 v[36:39], v[124:127], a[216:219], 0\n" \
  "ds_read_b128 v[72:75], v0 offset:7168\n" \
  "v_mfma_f32_16x16x32_bf16 v[32:35], v[104:107], a[220:223], v[32:35]\n" \
  "v_cvt_pk_bf16_f32 v236, v24, v25\n" \
  "ds_read_b128 v[76:79], v0 offset:10240\n" \
  "v_mfma_f32_16x16x32_bf16 v[36:39], v[128:131], a[220:223], v[36:39]\n" \
  "v_cvt_pk_bf16_f32 v237, v26, v27\n" \
  "ds_read_b64 v[80:81], v0 offset:2056\n" \
  "v_mfma_f32_16x16x32_bf16 v[32:35], v[108:111], a[224:227], v[32:35]\n" \
  "v_cvt_pk_bf16_f32 v238, v28, v29\n" \
  "ds_read_b64 v[82:83], v0 offset:5128\n" \
  "v_mfma_f32_16x16x32_bf16 v[36:39], v[132:135], a[224:227], v[36:39]\n" \
  "v_cvt_pk_bf16_f32 v239, v30, v31\n" \
  "ds_read_b64 v[84:85], v0 offset:8200\n" \
  "v_mfma_f32_16x16x32_bf16 v[32:35], v[112:115], a[228:231], v[32:35]\n" \
  "buffer_store_dwordx4 v[236:239], v3, s[36:39], s44 offen sc1\n" \
  "v_mfma_f32_16x16x32_bf16 v[36:39], v[136:139], a[228:231], v[36:39]\n" \
  "s_add_u32 s44, s44, 0x40000\n" \
  "ds_read_b64 v[86:87], v0 offset:11272\n" \
  "v_mfma_f32_16x16x32_bf16 v[32:35], v[116:119], a[232:235], v[32:35]\n" \
  "v_mfma_f32_16x16x32_bf16 v[36:39], v[140:143], a[232:235], v[36:39]\n" \
  "v_mfma_f32_16x16x32_bf16 v[32:35], v[120:123], a[236:239], v[32:35]\n" \
  "v_mfma_f32_16x16x32_bf16 v[36:39], v[144:147], a[236:239], v[36:39]\n" \
  "s_add_u32 s41, s41, 1\n" \
  "s_waitcnt lgkmcnt(0)\n" \
  "s_cmp_lt_u32 s41, 8\n" \
  "s_cbranch_scc1 LL_%=\n" \
  "s_waitcnt vmcnt(3)\n" \
  "s_barrier\n" \
  "ds_read_b128 v[148:151], v15 offset:0\n" \
  "ds_read_b128 v[152:155], v18 offset:0\n" \
  "ds_read_b128 v[156:159], v17 offset:0\n" \
  "ds_read_b128 v[160:163], v17 offset:1024\n" \
  "ds_read_b128 v[164:167], v15 offset:3072\n" \
  "ds_read_b128 v[168:171], v18 offset:4096\n" \
  "ds_read_b128 v[172:175], v17 offset:4096\n" \
  "ds_read_b128 v[176:179], v17 offset:5120\n" \
  "ds_read_b128 v[180:183], v15 offset:6144\n" \
  "ds_read_b128 v[184:187], v18 offset:8192\n" \
  "ds_read_b128 v[188:191], v17 offset:8192\n" \
  "ds_read_b128 v[192:195], v17 offset:9216\n" \
  "s_lshr_b32 s45, s40, 1\n" \
  "s_add_u32 s45, s45, s41\n" \
  "s_sub_u32 s45, s45, 8\n" \
  "v_readlane_b32 s54, v5, s45\n" \
  "s_xor_b32 s54, s54, 0x2000\n" \
  "v_mov_b32 v226, 0x3d93cd3a\n" \
  "v_mov_b32 v227, 0x3d93cd3a\n" \
  "s_waitcnt lgkmcnt(10)\n" \
  "v_mfma_f32_16x16x32_bf16 v[196:199], v[152:155], v[148:151], v[208:211]\n" \
  "s_waitcnt lgkmcnt(9)\n" \
  "v_mfma_f32_16x16x32_bf16 v[200:203], v[156:159], v[148:151], v[212:215]\n" \
  "s_waitcnt lgkmcnt(8)\n" \
  "v_mfma_f32_16x16x32_bf16 v[204:207], v[160:163], v[148:151], v[216:219]\n" \
  "ds_read_b128 v[148:151], v15 offset:9216\n" \
  "ds_read_b128 v[152:155], v18 offset:12288\n" \
  "ds_read_b128 v[156:159], v17 offset:12288\n" \
  "ds_read_b128 v[160:163], v17 offset:13312\n" \
  "s_waitcnt lgkmcnt(10)\n" \
  "v_mfma_f32_16x16x32_bf16 v[196:199], v[168:171], v[164:167], v[196:199]\n" \
  "s_waitcnt lgkmcnt(9)\n" \
  "v_mfma_f32_16x16x32_bf16 v[200:203], v[172:175], v[164:167], v[200:203]\n" \
  "s_waitcnt lgkmcnt(8)\n" \
  "v_mfma_f32_16x16x32_bf16 v[204:207], v[176:179], v[164:167], v[204:207]\n" \
  "ds_read_b64 v[164:165], v16 offset:0\n" \
  "ds_read_b64 v[168:169], v19 offset:0\n" \
  "ds_read_b64 v[172:173], v17 offset:2048\n" \
  "ds_read_b64 v[176:177], v17 offset:2056\n" \
  "ds_read_b64 v[166:167], v16 offset:3072\n" \
  "ds_read_b64 v[170:171], v19 offset:4096\n" \
  "ds_read_b64 v[174:175], v17 offset:6144\n" \
  "s_waitcnt lgkmcnt(14)\n" \
  "ds_read_b64 v[178:179], v17 offset:6152\n" \
  "s_waitcnt lgkmcnt(14)\n" \
  "v_mfma_f32_16x16x32_bf16 v[196:199], v[184:187], v[180:183], v[196:199]\n" \
  "s_waitcnt lgkmcnt(13)\n" \
  "v_mfma_f32_16x16x32_bf16 v[200:203], v[188:191], v[180:183], v[200:203]\n" \
  "s_waitcnt lgkmcnt(12)\n" \
  "v_mfma_f32_16x16x32_bf16 v[204:207], v[192:195], v[180:183], v[204:207]\n" \
  "ds_read_b64 v[180:181], v16 offset:6144\n" \
  "ds_read_b64 v[184:185], v19 offset:8192\n" \
  "ds_read_b64 v[188:189], v17 offset:10240\n" \
  "s_waitcnt lgkmcnt(14)\n" \
  "ds_read_b64 v[192:193], v17 offset:10248\n" \
  "s_waitcnt lgkmcnt(14)\n" \
  "ds_read_b64 v[182:183], v16 offset:9216\n" \
  "s_waitcnt lgkmcnt(14)\n" \
  "ds_read_b64 v[186:187], v19 offset:12288\n" \
  "s_waitcnt lgkmcnt(14)\n" \
  "ds_read_b64 v[190:191], v17 offset:14336\n" \
  "s_waitcnt lgkmcnt(14)\n" \
  "ds_read_b64 v[194:195], v17 offset:14344\n" \
  "v_mfma_f32_16x16x32_bf16 v[196:199], v[152:155], v[148:151], v[196:199]\n" \
  "v_mfma_f32_16x16x32_bf16 v[200:203], v[156:159], v[148:151], v[200:203]\n" \
  "v_mfma_f32_16x16x32_bf16 v[204:207], v[160:163], v[148:151], v[204:207]\n" \
  "s_waitcnt lgkmcnt(10)\n" \
  "v_mfma_f32_16x16x32_bf16 v[196:199], v[168:171], v[164:167], v[196:199]\n" \
  "s_waitcnt lgkmcnt(9)\n" \
  "v_mfma_f32_16x16x32_bf16 v[200:203], v[172:175], v[164:167], v[200:203]\n" \
  "s_waitcnt lgkmcnt(8)\n" \
  "v_mfma_f32_16x16x32_bf16 v[204:207], v[176:179], v[164:167], v[204:207]\n" \
  "s_waitcnt lgkmcnt(2)\n" \
  "v_mfma_f32_16x16x32_bf16 v[196:199], v[184:187], v[180:183], v[196:199]\n" \
  "s_waitcnt lgkmcnt(1)\n" \
  "v_mfma_f32_16x16x32_bf16 v[200:203], v[188:191], v[180:183], v[200:203]\n" \
  "s_waitcnt lgkmcnt(0)\n" \
  "v_mfma_f32_16x16x32_bf16 v[204:207], v[192:195], v[180:183], v[204:207]\n" \
  "v_cvt_pk_bf16_f32 v220, v196, v197\n" \
  "v_cvt_pk_bf16_f32 v221, v198, v199\n" \
  "s_nop 3\n" \
  "v_cvt_pk_bf16_f32 v222, v200, v201\n" \
  "v_cvt_pk_bf16_f32 v223, v202, v203\n" \
  "v_cvt_pk_bf16_f32 v224, v204, v205\n" \
  "v_cvt_pk_bf16_f32 v225, v206, v207\n" \
  "v_lshlrev_b32 v196, 16, v220\n" \
  "v_and_b32 v197, 0xffff0000, v220\n" \
  "v_pk_mul_f32 v[196:197], v[196:197], v[226:227]\n" \
  "v_cvt_pk_bf16_f32 v220, v196, v197\n" \
  "v_lshlrev_b32 v196, 16, v220\n" \
  "v_and_b32 v197, 0xffff0000, v220\n" \
  "v_lshlrev_b32 v198, 16, v221\n" \
  "v_and_b32 v199, 0xffff0000, v221\n" \
  "v_pk_mul_f32 v[198:199], v[198:199], v[226:227]\n" \
  "v_cvt_pk_bf16_f32 v221, v198, v199\n" \
  "v_lshlrev_b32 v198, 16, v221\n" \
  "v_and_b32 v199, 0xffff0000, v221\n" \
  "v_lshlrev_b32 v200, 16, v222\n" \
  "v_and_b32 v201, 0xffff0000, v222\n" \
  "v_pk_mul_f32 v[200:201], v[200:201], v[226:227]\n" \
  "v_cvt_pk_bf16_f32 v222, v200, v201\n" \
  "v_lshlrev_b32 v200, 16, v222\n" \
  "v_and_b32 v201, 0xffff0000, v222\n" \
  "v_lshlrev_b32 v202, 16, v223\n" \
  "v_and_b32 v203, 0xffff0000, v223\n" \
  "v_pk_mul_f32 v[202:203], v[202:203], v[226:227]\n" \
  "v_cvt_pk_bf16_f32 v223, v202, v203\n" \
  "v_lshlrev_b32 v202, 16, v223\n" \
  "v_and_b32 v203, 0xffff0000, v223\n" \
  "v_lshlrev_b32 v204, 16, v224\n" \
  "v_and_b32 v205, 0xffff0000, v224\n" \
  "v_pk_mul_f32 v[204:205], v[204:205], v[226:227]\n" \
  "v_cvt_pk_bf16_f32 v224, v204, v205\n" \
  "v_lshlrev_b32 v204, 16, v224\n" \
  "v_and_b32 v205, 0xffff0000, v224\n" \
  "v_lshlrev_b32 v206, 16, v225\n" \
  "v_and_b32 v207, 0xffff0000, v225\n" \
  "v_pk_mul_f32 v[206:207], v[206:207], v[226:227]\n" \
  "v_cvt_pk_bf16_f32 v225, v206, v207\n" \
  "v_lshlrev_b32 v206, 16, v225\n" \
  "v_and_b32 v207, 0xffff0000, v225\n" \
  "v_max3_f32 v220, v196, v197, v198\n" \
  "v_max3_f32 v220, v220, v199, v200\n" \
  "v_max3_f32 v220, v220, v201, v202\n" \
  "v_max3_f32 v220, v220, v203, v204\n" \
  "v_max3_f32 v220, v220, v205, v206\n" \
  "v_max_f32 v220, v220, v207\n" \
  "v_mov_b32 v221, v220\n" \
  "s_nop 1\n" \
  "v_permlane16_swap_b32 v220, v221\n" \
  "v_max_f32 v220, v220, v221\n" \
  "v_mov_b32 v221, v220\n" \
  "s_nop 1\n" \
  "v_permlane32_swap_b32 v220, v221\n" \
  "v_max_f32 v220, v220, v221\n" \
  "v_mov_b32 v222, 0x3fb8aa3b\n" \
  "v_mov_b32 v223, 0x3fb8aa3b\n" \
  "v_pk_add_f32 v[196:197], v[196:197], v[220:221] op_sel_hi:[1,0] neg_lo:[0,1] neg_hi:[0,1]\n" \
  "v_pk_mul_f32 v[196:197], v[196:197], v[222:223]\n" \
  "v_pk_add_f32 v[198:199], v[198:199], v[220:221] op_sel_hi:[1,0] neg_lo:[0,1] neg_hi:[0,1]\n" \
  "v_pk_mul_f32 v[198:199], v[198:199], v[222:223]\n" \
  "v_pk_add_f32 v[200:201], v[200:201], v[220:221] op_sel_hi:[1,0] neg_lo:[0,1] neg_hi:[0,1]\n" \
  "v_pk_mul_f32 v[200:201], v[200:201], v[222:223]\n" \
  "v_pk_add_f32 v[202:203], v[202:203], v[220:221] op_sel_hi:[1,0] neg_lo:[0,1] neg_hi:[0,1]\n" \
  "v_pk_mul_f32 v[202:203], v[202:203], v[222:223]\n" \
  "v_pk_add_f32 v[204:205], v[204:205], v[220:221] op_sel_hi:[1,0] neg_lo:[0,1] neg_hi:[0,1]\n" \
  "v_pk_mul_f32 v[204:205], v[204:205], v[222:223]\n" \
  "v_pk_add_f32 v[206:207], v[206:207], v[220:221] op_sel_hi:[1,0] neg_lo:[0,1] neg_hi:[0,1]\n" \
  "v_pk_mul_f32 v[206:207], v[206:207], v[222:223]\n" \
  "v_exp_f32 v196, v196\n" \
  "v_exp_f32 v197, v197\n" \
  "v_exp_f32 v198, v198\n" \
  "v_exp_f32 v199, v199\n" \
  "v_exp_f32 v200, v200\n" \
  "v_exp_f32 v201, v201\n" \
  "v_exp_f32 v202, v202\n" \
  "v_exp_f32 v203, v203\n" \
  "v_exp_f32 v204, v204\n" \
  "v_exp_f32 v205, v205\n" \
  "v_exp_f32 v206, v206\n" \
  "v_exp_f32 v207, v207\n" \
  "v_pk_add_f32 v[220:221], v[196:197], v[198:199]\n" \
  "v_pk_add_f32 v[222:223], v[200:201], v[202:203]\n" \
  "v_pk_add_f32 v[224:225], v[204:205], v[206:207]\n" \
  "v_pk_add_f32 v[220:221], v[220:221], v[222:223]\n" \
  "v_pk_add_f32 v[220:221], v[220:221], v[224:225]\n" \
  "v_add_f32 v220, v220, v221\n" \
  "v_mov_b32 v221, v220\n" \
  "s_nop 1\n" \
  "v_permlane16_swap_b32 v220, v221\n" \
  "v_add_f32 v220, v220, v221\n" \
  "v_mov_b32 v221, v220\n" \
  "s_nop 1\n" \
  "v_permlane32_swap_b32 v220, v221\n" \
  "v_add_f32 v220, v220, v221\n" \
  "v_rcp_f32 v220, v220\n" \
  "s_nop 0\n" \
  "v_pk_mul_f32 v[200:201], v[200:201], v[220:221] op_sel_hi:[1,0]\n" \
  "v_pk_mul_f32 v[202:203], v[202:203], v[220:221] op_sel_hi:[1,0]\n" \
  "v_pk_mul_f32 v[204:205], v[204:205], v[220:221] op_sel_hi:[1,0]\n" \
  "v_pk_mul_f32 v[206:207], v[206:207], v[220:221] op_sel_hi:[1,0]\n" \
  "v_pk_mul_f32 v[196:197], v[196:197], v[220:221] op_sel_hi:[1,0]\n" \
  "v_pk_mul_f32 v[198:199], v[198:199], v[220:221] op_sel_hi:[1,0]\n" \
  "v_cvt_pk_bf16_f32 v228, v200, v201\n" \
  "v_cvt_pk_bf16_f32 v229, v202, v203\n" \
  "v_cvt_pk_bf16_f32 v230, v204, v205\n" \
  "v_cvt_pk_bf16_f32 v231, v206, v207\n" \
  "v_cvt_pk_bf16_f32 v232, v196, v197\n" \
  "v_cvt_pk_bf16_f32 v233, v198, v199\n" \
  "buffer_store_dwordx4 v[228:231], v20, s[36:39], s54 offen sc1\n" \
  "buffer_store_dwordx2 v[232:233], v21, s[36:39], s54 offen sc1\n" \
  "s_mov_b32 s53, 0\n" \
  "s_cmp_lt_u32 s41, 66\n" \
  "s_cbranch_scc1 LL_%=\n" \
  "s_waitcnt vmcnt(0)\n" \
  "s_barrier\n" \
  "buffer_atomic_add v92, v13, s[36:39], s53 offen\n" \
  "s_branch LX_%=\n" \
  "LPS_%=:\n" \
  "s_add_u32 s48, s48, 1\n" \
  "s_cmp_gt_u32 s48, 262144\n" \
  "s_cbranch_scc1 LKS_%=\n" \
  "s_sleep 4\n" \
  "buffer_load_dword v90, v88, s[36:39], 0 offen sc1\n" \
  "s_waitcnt vmcnt(0)\n" \
  "v_cmp_gt_i32 vcc, v89, v90\n" \
  "s_cbranch_vccnz LPS_%=\n" \
  "s_branch LKS_%=\n" \
  "LP0_%=:\n" \
  "s_add_u32 s48, s48, 1\n" \
  "s_cmp_gt_u32 s48, 262144\n" \
  "s_cbranch_scc1 LK0_%=\n" \
  "s_sleep 4\n" \
  "buffer_load_dword v90, v88, s[36:39], 0 offen sc1\n" \
  "s_waitcnt vmcnt(0)\n" \
  "v_cmp_gt_i32 vcc, v89, v90\n" \
  "s_cbranch_vccnz LP0_%=\n" \
  "s_branch LK0_%=\n" \
  "LP1_%=:\n" \
  "s_add_u32 s48, s48, 1\n" \
  "s_cmp_gt_u32 s48, 262144\n" \
  "s_cbranch_scc1 LK1_%=\n" \
  "s_sleep 4\n" \
  "buffer_load_dword v90, v88, s[36:39], 0 offen sc1\n" \
  "s_waitcnt vmcnt(0)\n" \
  "v_cmp_gt_i32 vcc, v89, v90\n" \
  "s_cbranch_vccnz LP1_%=\n" \
  "s_branch LK1_%=\n" \
  "LX_%=:\n"

__device__ __forceinline__ int fresh_lane() {
  int ln;
  asm volatile("v_mbcnt_lo_u32_b32 %0, -1, 0\n\tv_mbcnt_hi_u32_b32 %0, -1, %0" : "=v"(ln));
  __builtin_assume(ln >= 0 && ln < 64);
  return ln;
}
__device__ __forceinline__ unsigned now_ticks() { return (unsigned)__builtin_amdgcn_s_memrealtime(); }
template <int SLEEP>
__device__ __forceinline__ void wait_ge(gu32* ctrl, long word, unsigned need) {
#pragma unroll 1
  for (int spin = 0;; ++spin) {
    const unsigned x = __hip_atomic_load(ctrl + word, RLX_AGENT);
    if (__builtin_amdgcn_ballot_w64(x < need) == 0) return;
    if (spin > SPIN_CAP || ((spin & 1023) == 1023 && rfl(__hip_atomic_load(ctrl + C_ERR, RLX_AGENT)))) { __hip_atomic_store(ctrl + C_ERR, 1u, RLX_AGENT); return; }
    __builtin_amdgcn_s_sleep(SLEEP);
  }
}
__device__ __forceinline__ void wait_tag(gu32* ctrl, int word, unsigned tag) {
#pragma unroll 1
  for (int spin = 0; spin <= SPIN_CAP; ++spin) {
    if (__builtin_amdgcn_ballot_w64(__hip_atomic_load(ctrl + word, RLX_AGENT) != tag) == 0) return;
    __builtin_amdgcn_s_sleep(3);
  }
}
__device__ __forceinline__ unsigned long long wait_init(gu32* ctrl, unsigned tag, int lane) {
  unsigned x;
#pragma unroll 1
  for (int spin = 0;; ++spin) {
    x = __hip_atomic_load(ctrl + C_INIT + (lane & 31) * 32, RLX_AGENT);
    if (__builtin_amdgcn_ballot_w64((x & ~3u) != tag) == 0 || spin > SPIN_CAP) break;
    __builtin_amdgcn_s_sleep(4);
  }
  const unsigned long long m = __builtin_amdgcn_ballot_w64((x & (1u + (lane >> 5))) != 0), alt = 0x5555555555555555ull;
  const int d = __builtin_popcountll(m ^ alt);
  return d <= 26 ? alt : d >= 38 ? ~alt : m;
}
template <typename VT>
__device__ __forceinline__ void set_rsrc(VT& sr, int i, const void* ptr, unsigned bytes) {
  const unsigned long long a = uni64((unsigned long long)ptr);
  sr[4 * i] = (unsigned)a; sr[4 * i + 1] = (unsigned)(a >> 32) & 0xffffu; sr[4 * i + 2] = bytes; sr[4 * i + 3] = 0x00020000u;
}
__device__ __forceinline__ int so_near(unsigned long long par, int blk) { return (blk << 21) | ((int)(par >> blk & 1) << 13); }
__device__ __forceinline__ int so_far(unsigned long long par, int blk) { return so_near(par, blk) ^ 0x2000; }
__device__ __forceinline__ long nword(unsigned long long par, int off) { return ((long)so_near(par, XB) + lmap(off) - (long)CTRL_OFF) / 4; }
__device__ __forceinline__ v4u ld16s(rsrc_t r, int vo, int so) { return __builtin_amdgcn_raw_buffer_load_b128(r, vo, so, 16); }
__device__ __forceinline__ v2u ld8s(rsrc_t r, int vo, int so) { return __builtin_amdgcn_raw_buffer_load_b64(r, vo, so, 16); }
__device__ __forceinline__ void st16s(v4u x, rsrc_t r, int vo, int so) { __builtin_amdgcn_raw_buffer_store_b128(x, r, vo, so, 16); }
#define TS(cond, i)
#define OPAQUE(v) asm volatile("" : "=v"(v))
#define W240_IN(w) "{a[0:31]}"(w[0]), "{a[32:63]}"(w[1]), "{a[64:95]}"(w[2]), "{a[96:127]}"(w[3]), "{a[128:159]}"(w[4]), "{a[160:191]}"(w[5]), \
                   "{a[192:223]}"(w[6]), "{a[224:239]}"(w##7)
#define W240_SET(w, i, k, val) do { if ((i) < 224) w[(i) >> 5][((i) & 31) + (k)] = (val); else w##7[(i) - 224 + (k)] = (val); } while (0)

__device__ __forceinline__ unsigned wave_max_u32(unsigned m) {
  m = max(m, (unsigned)__builtin_amdgcn_update_dpp(0, (int)m, 0xb1, 0xf, 0xf, true)); m = max(m, (unsigned)__builtin_amdgcn_update_dpp(0, (int)m, 0x4e, 0xf, 0xf, true));
  m = max(m, (unsigned)__builtin_amdgcn_update_dpp(0, (int)m, 0x124, 0xf, 0xf, true)); m = max(m, ror8u(m));
  auto a = __builtin_amdgcn_permlane16_swap(m, m, false, false); m = max(a[0], a[1]);
  auto b = __builtin_amdgcn_permlane32_swap(m, m, false, false); return max(b[0], b[1]);
}
__device__ __forceinline__ void select_rows(gu32* ctrl, rsrc_t rT, int lane) {
  unsigned key[32];
  v4u rr[8];
#pragma unroll
  for (int i = 0; i < 8; ++i) rr[i] = ld16s(rT, lane * 16 + i * 1024, 2 * (int)HF_BYTES);
  __builtin_amdgcn_sched_barrier(0);
#pragma unroll
  for (int i = 0; i < 8; ++i) {
    const v4u r = rr[i];
#pragma unroll
    for (int k = 0; k < 4; ++k) {
      unsigned u = f2u(u2f(r[k]) + 0.f);
      u = (u & 0x80000000u) ? ~u : (u | 0x80000000u);
      key[4 * i + k] = (u & 0xffff0000u) | (unsigned)(S - 1 - (i * 256 + lane * 4 + k));
    }
  }
  unsigned mine = 0, bound = 0;
#pragma unroll 1
  for (int it = 0; it < TOPK; ++it) {
    unsigned m = 0;
#pragma unroll
    for (int i = 0; i < 32; ++i) m = max(m, key[i] - bound);
    bound += wave_max_u32(m);
    if ((lane & 7) == it) mine = S - (bound & 0xffffu);
  }
#pragma unroll
  for (int i = 0; i < 2; ++i) __hip_atomic_store(ctrl + C_IDX + ((lane >> 3) + 8 * i) * 32 + (lane & 7), mine, RLX_AGENT);
}

__device__ __forceinline__ void ffn_role(const Params& p, int role, int lane, int wave) {
  gu32* ctrl = (gu32*)(p.ws + CTRL_OFF);
  const int g = lane >> 4, n = lane & 15, q = (lane >> 2) & 3, pp = lane & 3;
  const bool up = role < 96, down = role >= 88;
  const int c = role - 88;
  v4u wg[5], wu[5], wd[12];
  {
    const rsrc_t rg = mkrsrc(p.wg, DM * FF * 2), ru = mkrsrc(p.wu, DM * FF * 2), rd = mkrsrc(p.wd, FF * DM * 2);
#pragma unroll
    for (int i = 0; i < 5; ++i) {
      const int vo = up ? ((wave * 160 + i * 32 + (lane >> 1)) * FF + role * 16) * 2 + (lane & 1) * 16 : OOB;
      dma16(rg, vo, 0, wave * 22528 + i * 1024); dma16(ru, vo, 0, wave * 22528 + 5120 + i * 1024);
    }
#pragma unroll
    for (int i = 0; i < 12; ++i) dma16(rd, down ? ((wave * 384 + i * 32 + (lane >> 1)) * DM + c * 16) * 2 + (lane & 1) * 16 : OOB, 0, wave * 22528 + 10240 + i * 1024);
    VMCNT(0);
    const unsigned a = wave * 22528 + (8 * g + q) * 32 + pp * 8;
#pragma unroll
    for (int i = 0; i < 22; ++i) {
      const v2u lo = trread(a + i * 1024), hi = trread(a + i * 1024 + 128);
      const v4u x = {lo[0], lo[1], hi[0], hi[1]};
      if (i < 5) wg[i] = x; else if (i < 10) wu[i - 5] = x; else wd[i - 10] = x;
    }
  }
  const rsrc_t rT = mkrsrc(p.ws + X1_OFF, (unsigned)(WS_END - X1_OFF));
  constexpr int SO_H2 = DM * 2, SO_R = 2 * (int)HF_BYTES, SO_ACT = (int)(ACT_OFF - X1_OFF), RED = 98304;
  if (wave == 0) {
    wait_ge<3>(ctrl, C_IDX + (role & 15) * 32 + (lane & 7), 1u);
    if (lane < 8) *(LAS unsigned*)(L_MISC + lane * 4) = __hip_atomic_load(ctrl + C_IDX + (role & 15) * 32 + lane, RLX_AGENT);
  }
  __syncthreads();
  const unsigned t0 = now_ticks();
  const int row = (*(LAS int*)(L_MISC + (n & 7) * 4) - 1) & (S - 1);
  v2u x1, h2, gb; unsigned rv;
  if (down && wave == 0) { { const v4u t = ld16s(mkrsrc(p.ws + CTRL_OFF + C_MOD * 4, 15360), (5 * DM + c * 16 + 4 * g) * 4, 0); gb = v2u{t[0], t[2]}; } x1 = ld8s(rT, (row * 2 * DM + c * 16 + 4 * g) * 2, 0); h2 = ld8s(rT, (row * 2 * DM + c * 16 + 4 * g) * 2, SO_H2); rv = __builtin_amdgcn_raw_buffer_load_b32(rT, row * 4, SO_R, 16); }
  if (up) {
    v4u xb[5];
#pragma unroll
    for (int i = 0; i < 5; ++i) xb[i] = ld16s(rT, (row * 2 * DM + wave * 160 + i * 32 + 8 * g) * 2, SO_H2);
    __builtin_amdgcn_sched_barrier(0);
    f4 ag = {}, au = {};
#pragma unroll
    for (int i = 0; i < 5; ++i) { ag = mfma32(wg[i], xb[i], ag); au = mfma32(wu[i], xb[i], au); }
    *(LAS f4*)(RED + wave * 2048 + lane * 16) = ag; *(LAS f4*)(RED + wave * 2048 + 1024 + lane * 16) = au;
    __syncthreads();
    if (wave == 0) {
#pragma unroll
      for (int w = 1; w < 4; ++w) { ag += *(LAS f4*)(RED + w * 2048 + lane * 16); au += *(LAS f4*)(RED + w * 2048 + 1024 + lane * 16); }
      float a[4];
#pragma unroll
      for (int r = 0; r < 4; ++r) { const float gs = rbf(ag[r]); a[r] = rbf(gs / (1.f + __expf(-gs))) * rbf(au[r]); }
      __builtin_amdgcn_raw_buffer_store_b64(v2u{unsent(pack2(a[0], a[1])), unsent(pack2(a[2], a[3]))}, rT, n < 8 ? role * 256 + (g >> 1) * 128 + n * 16 + (g & 1) * 8 : OOB, SO_ACT, 16);
    }
  }
  if (!down) return;
  while ((int)(now_ticks() - t0) < FFN_WAIT) __builtin_amdgcn_s_sleep(1);
  v4u ab[12];
  for (int spin = 0;; ++spin) {
    asm volatile("" ::: "memory");
    unsigned m = 0;
#pragma unroll
    for (int i = 0; i < 12; ++i) ab[i] = ld16s(rT, (wave * 12 + i) * 512 + g * 128 + (n & 7) * 16, SO_ACT);
    __builtin_amdgcn_sched_barrier(0);
#pragma unroll
    for (int i = 0; i < 12; ++i) m = max(max(m, max(ab[i][0], ab[i][1])), max(ab[i][2], ab[i][3]));
    if (__builtin_amdgcn_ballot_w64(m == ~0u) == 0) break;
    if (spin > SPIN_CAP) { __hip_atomic_store(ctrl + C_ERR, 1u, RLX_AGENT); break; }
    __builtin_amdgcn_s_sleep(4);
  }
  f4 y = {};
#pragma unroll
  for (int i = 0; i < 12; ++i) y = mfma32(wd[i], ab[i], y);
  *(LAS f4*)(RED + 8192 + wave * 1024 + lane * 16) = y;
  __syncthreads();
  if (wave == 0 && n < 8) {
#pragma unroll
    for (int w = 1; w < 4; ++w) y += *(LAS f4*)(RED + 8192 + w * 1024 + lane * 16);
    const float rs = rbf(1.f / (1.f + __expf(-u2f(rv))));
    float o[4];
#pragma unroll
    for (int r = 0; r < 4; ++r) {
      const float hv = (r & 1) ? hi2f(h2[r >> 1]) : lo2f(h2[r >> 1]), xv = (r & 1) ? hi2f(x1[r >> 1]) : lo2f(x1[r >> 1]);
      o[r] = xv + rbf(((r & 1) ? hi2f(gb[r >> 1]) : lo2f(gb[r >> 1])) * rbf(hv + rbf(rbf(y[r]) * rs)));
    }
    *(v2u*)(p.out + (size_t)row * DM + c * 16 + 4 * g) = v2u{pack2(o[0], o[1]), pack2(o[2], o[3])};
  }
}

constexpr int LP = 0, L_PR = 2560, L_MOD = 16384;
__device__ __forceinline__ v4u ln1_piece(const Params& p, int row0, int i, int lane) {
  return __builtin_amdgcn_raw_buffer_load_b128(mkrsrc(p.x, S * DM * 2), (row0 + (lane & 7)) * 1280 + (lane >> 3) * 16, i * 128, 0);
}
__device__ __forceinline__ void ln1_stats(const v4u (&a)[10], float& mb, float& isd) {
  float s = 0.f, q = 0.f;
  const bf2 one = {(__bf16)1.f, (__bf16)1.f};
#pragma unroll
  for (int i = 0; i < 10; ++i)
#pragma unroll
    for (int k = 0; k < 4; ++k) { const unsigned w = a[i][k]; s = __builtin_amdgcn_fdot2_f32_bf16(__builtin_bit_cast(bf2, w), one, s, false); }
  s += ror8f(s); s = swapsum16(s, s); s = swapsum32(s, s);
  const float mean = s * (1.f / DM);
#pragma unroll
  for (int i = 0; i < 10; ++i)
#pragma unroll
    for (int k = 0; k < 4; ++k) { const float d0 = lo2f(a[i][k]) - mean, d1 = hi2f(a[i][k]) - mean; q = fmaf(d0, d0, fmaf(d1, d1, q)); }
  q += ror8f(q); q = swapsum16(q, q); q = swapsum32(q, q);
  mb = rbf(mean); isd = __builtin_amdgcn_rcpf(rbf(__builtin_amdgcn_sqrtf(rbf(rbf(q * (1.f / DM)) + 1e-5f))));
}
__device__ __forceinline__ unsigned ln_norm(unsigned a, float mb, float isd) { const unsigned d = pack2(lo2f(a) - mb, hi2f(a) - mb); return pack2(lo2f(d) * isd, hi2f(d) * isd); }
__device__ __forceinline__ unsigned ln_mod(unsigned n, unsigned sc, unsigned sh) { const unsigned m = pack2(lo2f(n) * lo2f(sc), hi2f(n) * hi2f(sc)); return pack2(lo2f(m) + lo2f(sh), hi2f(m) + hi2f(sh)); }
__device__ __forceinline__ void ln1_store(const Params& p, v4u o, int row0, int i, int lane) {
  st16s(o, mkrsrc(p.ws + HF_OFF, (unsigned)HF_BYTES), (row0 >> 4) * 20480 + (lane >> 3) * 256 + ((row0 & 8) + (lane & 7)) * 16, i * 2048);
}
template <typename B>
__device__ __forceinline__ void mod_unit(const Params& p, int u, int tid, unsigned tag, B between) {
  const rsrc_t rw = mkrsrc(p.wmod, DM * 3840 * 2), rc = mkrsrc(p.c, DM * 2);
  LAS float* sv = (LAS float*)L_MOD; LAS float* red = sv + 768;
  u16 cv[3]; v4u w[20];
#pragma unroll
  for (int i = 0; i < 3; ++i) cv[i] = __builtin_amdgcn_raw_buffer_load_b16(rc, tid * 2, i * 512, 0);
#pragma unroll
  for (int i = 0; i < 20; ++i) w[i] = __builtin_amdgcn_raw_buffer_load_b128(rw, u < 60 ? (tid >> 3) * 7680 + u * 128 + (tid & 7) * 16 : OOB, i * 32 * 7680, 0);
  between();
  __builtin_amdgcn_sched_barrier(0);
#pragma unroll
  for (int i = 0; i < 3; ++i) { const float c = bf2f(cv[i]); sv[i * 256 + tid] = rbf(c / (1.f + __expf(-c))); }
  LGKM0(); BAR();
  float acc[8] = {};
#pragma unroll
  for (int i = 0; i < 20; ++i) {
    const float x = sv[i * 32 + (tid >> 3)];
#pragma unroll
    for (int k = 0; k < 4; ++k) { acc[2 * k] += x * lo2f(w[i][k]); acc[2 * k + 1] += x * hi2f(w[i][k]); }
  }
#pragma unroll
  for (int k = 0; k < 8; ++k) red[(tid >> 3) * 65 + (tid & 7) * 8 + k] = acc[k];
  LGKM0(); BAR();
  float sum = 0.f;
#pragma unroll
  for (int i = 0; i < 8; ++i) sum += red[((tid & 3) * 8 + i) * 65 + (tid >> 2)];
  sum += u2f(__builtin_amdgcn_update_dpp(0, (int)f2u(sum), 0xb1, 0xf, 0xf, true)); sum += u2f(__builtin_amdgcn_update_dpp(0, (int)f2u(sum), 0x4e, 0xf, 0xf, true));
  const unsigned y = pack2((u / 10) % 3 == 1 ? 1.f + rbf(sum) : sum, 0.f), z = (unsigned)__builtin_amdgcn_update_dpp(0, (int)y, 0x104, 0xf, 0xf, true);
  __builtin_amdgcn_raw_buffer_store_b64(v2u{y | z << 16, tag}, mkrsrc(p.ws + CTRL_OFF + C_MOD * 4, 15360), (tid & 7) == 0 ? u * 256 + tid : OOB, 0, 16);
  LGKM0(); BAR();
}
__device__ __forceinline__ void mark_slot(rsrc_t rH, int so, int hq, int lane, int wave) {
  const v4u m = {~0u, ~0u, ~0u, ~0u};
#pragma unroll
  for (int i = 0; i < 8; ++i) st16s(m, rH, lmap(P_OFF + (wave * 16 + (lane >> 5)) * 4096 + hq * 512 + (lane & 31) * 16) + i * 16384, so);
#pragma unroll
  for (int i = 0; i < 2; ++i) st16s(m, rH, lmap(P2_OFF + (wave * 16 + (lane >> 3)) * 1024 + hq * 128 + (lane & 7) * 16) + i * 16384, so);
#pragma unroll
  for (int i = 0; i < 2; ++i) st16s(m, rH, wave * 2 + i < 7 ? lmap((hq * 7 + wave * 2 + i) * 1024 + lane * 16) : OOB, so);
}
template <typename I, typename T>
__device__ __forceinline__ void stage4(I issue, T take) {
  issue(0, 0); issue(1, 1);
  VMCNT(15); BAR(); take(0, 0); LGKM0(); BAR(); issue(2, 0);
  VMCNT(15); BAR(); take(1, 1); LGKM0(); BAR(); issue(3, 1);
  VMCNT(15); BAR(); take(2, 0);
  VMCNT(0); BAR(); take(3, 1); LGKM0();
}

__device__ __forceinline__ void chain_role(const Params& p, int role, int lane, int w, unsigned tag) {
  const int ty = role % 3, h = role / 3, r = 2 * h + ty;
  const int g = lane >> 4, n = lane & 15, q = (lane >> 2) & 3, pp = lane & 3, par = n >> 3, i = n & 7, sel = q >> 1;
  constexpr int soX = XB << 21;
  v32u vc, vi;
  OPAQUE(vc); OPAQUE(vi);
#pragma unroll
  for (int k = 0; k < 10; ++k) {
    int o;
    if (ty == 1) { const int u = ((w & 1) * 8 + k) * 64 + lane, row = u >> 4; o = row * 12288 + ((u & 15) ^ (((row & 1) | ((row >> 1) & 2) | ((w >> 1) << 2)) << 1)) * 16; }
    else if (ty == 0 && w == 3) { const int u = k * 64 + lane, row = u >> 3; o = row * 12288 + 256 + ((u & 7) ^ (row & 4) ^ 2) * 16; }
    else { const int u = ((ty ? 6 : 8) * w + k) * 64 + lane, row = u / 24; o = row * 12288 + ((u % 24) ^ (row & 4)) * 16; }
    vi[k] = o + h * 384;
    const int u = (10 * w + k) * 64 + lane, row = u / 80;
    vi[10 + k] = (h * DH + row) * 1280 + ((u % 80) ^ (((row & 1) | ((row >> 1) & 2)) << 2)) * 16;
  }
  const int r0 = 4 * g + (q & 1), sw = (g & 1) << 6;
  if (ty == 0) {
    vi[20] = r0 * 384 + ((64 * w + 32 * sel + 8 * pp) ^ sw); vi[22] = 384;
    vi[21] = sel ? 24576 + r0 * 128 + ((32 * w + 8 * pp) ^ sw ^ 32) : r0 * 384 + ((256 + 32 * w + 8 * pp) ^ sw); vi[23] = sel ? 128 : 384;
  } else if (ty == 1) {
#pragma unroll
    for (int k = 0; k < 2; ++k) { vi[20 + k] = sel * 16384 + r0 * 256 + ((64 * w + 32 * k + 8 * pp) ^ (((q & 1) | ((g & 1) << 1) | (sel << 2)) << 5)); vi[22 + k] = 256; }
  } else {
    const int rv = 16 * (g >> 1) + 8 * sel + 4 * (g & 1) + (q & 1);
    vi[20] = rv * 384 + ((64 * w + 8 * pp) ^ sw); vi[21] = rv * 384 + ((256 + 32 * w + 8 * pp) ^ sw);
    vi[22] = r0 * 1280 + ((64 * w + 32 * sel + 8 * pp) ^ (((q & 1) | ((g & 1) << 1)) << 6));
  }
  vi[24] = (int)CTRL_OFF + (C_ANCH + 128) * 4;
  vi[25] = (int)CTRL_OFF + (C_ANCH + (role >> 5) * 32) * 4;
  vi[26] = lane < 4 ? (int)CTRL_OFF + (C_ANCH + lane * 32) * 4 : OOB;
  vi[27] = soX + lmap(N_PP) + pp * 128;
  vi[28] = w == 0 && lane == 0 ? vi[24] : OOB;
  vi[29] = (int)CTRL_OFF + (C_INIT + (lane & 31) * 32) * 4; vi[30] = 1 + (lane >> 5);
  vi[31] = lane == 0 ? soX + lmap(N_CF) + h * 128 + ty * 4 : OOB;
  vc[0] = n < 8 ? 0x4444 : 0xEEEE;
  const int tok = 16 * ((n >> 2) & 1) + 4 * (2 * w + par) + (n & 3);
  const int ko = L_K + (g * 16 + (tok & 15)) * 16;
  vc[1] = w == 2 ? lmap(M_OFF) + lane * 16 : ko + (tok >> 4) * 1024;
  vc[2] = w == 2 ? lmap(M_OFF) + 4096 + lane * 16 : ko + 2048 + (tok >> 4) * 8;
  vc[3] = lmap(w == 2 ? M_OFF + 8192 : w == 3 ? V2_OFF + r * 4096 : r * 16384 + w * 8192) + lane * 16;
  vc[4] = ty == 2 ? lmap(P2_OFF + (16 * w + 8 * (g >> 1) + (n >> 1)) * 1024 + h * 32 + (n & 1) * 16 + (g & 1) * 8) : h * 65536 + w * 32768 + 3072 + lane * 16;
  vc[5] = L_MEM + (ty < 2 ? g * 128 + i * 16 : lane * 16);
  vc[6] = L_MEM + 8192 + (ty < 2 ? g : 16 * (g >> 1) + 2 * (g & 1) + par) * 32 + i * 4; vc[7] = vc[6] + 1024;
  const int kmo = h * 65536 + w * 16384 + 6144;
  auto im4 = [](int u, int d) { return u * 128 + ((d ^ ((u & 2) << 2)) * 8); };
  if (ty == 1) {
    vc[8] = L_Q + w * 1024 + im4(g, n); vc[9] = vc[8] + 512;
    vc[10] = L_Q + im4(pp, 4 * g + q); vc[11] = vc[10] + 512; vc[12] = L_Q + w * 1024 + (g >> 1) * 512 + im4(pp, 8 * (g & 1) + q);
    vc[14] = par ? kmo + (g * 8 + i) * 16 : OOB;
  } else {
    vc[8] = L_Q + w * 512 + g * 128 + n * 8;
    vc[9] = ty ? L_Q + 2048 + w * 256 + (g & 1) * 128 + n * 8 + (g >> 1) * 4 : L_Q2 + w * 512 + im4(g, n);
    vc[10] = L_Q + (g >> 1) * 256 + (pp & 1) * 128 + (8 * (g & 1) + q) * 8;
    vc[11] = ty ? L_WO + w * 10240 + lane * 16 : L_Q2 + im4(w < 2 ? pp & 1 : pp, 4 * g + q);
    vc[12] = L_Q2 + w * 512 + im4(pp, 4 * g + q);
    vc[14] = par ? kmo + 512 + (g * 8 + i) * 8 : OOB;
  }
  vc[13] = ty == 2 ? lmap(P_OFF + ((4 * w + 2 * (g >> 1) + par) * 32 + h) * 128 + i * 16 + (g & 1) * 8)
         : ty == 0 && w < 2 ? lmap(E_OFF + h * 512 + (2 * w + (g >> 1)) * 128 + i * 16 + (g & 1) * 8 + par * 4)
         : g >= 2 && !par ? lmap(S_OFF + h * 256 + ty * 128 + i * 16 + (g - 2) * 8) : OOB;
  const int h0 = 8 * w + 4 * par;
  vc[15] = lmap(S_OFF + (h0 + g) * 256 + i * 16);
  vc[16] = lmap(E_OFF + h0 * 512 + g * 128 + i * 16);
  vc[17] = lmap(P_OFF + (r * 32 + h0 + g) * 128 + i * 16);
  vc[18] = i < 2 ? lmap(P2_OFF + r * 1024 + (h0 + g) * 32 + i * 16) : OOB;
  vc[19] = L_VO + h0 * 512 + g * 128 + i * 16;
  vc[20] = L_VO + 16384 + h0 * 128 + g * 32 + (i & 1) * 16;
  vc[21] = L_RED + w * 768 + (g & 1) * 256 + i * 16 + par * 8;
  vc[22] = L_RED + w * 768 + 512 + (g & 1) * 128 + i * 16 + par * 8;
  vc[23] = L_RED + min(lane, 47) * 16;
  vc[24] = lane < 32 ? lmap(M_OFF + r * 128 + i * 16 + (lane >> 3) * 4) : lane < 40 ? lmap(M_OFF + 8192 + tslot(r) * 32 + i * 4) : OOB;
  vc[25] = (3 * (lane % RING)) << 21; vc[26] = vc[25] + (2 << 21); vc[27] = vc[25] + (1 << 21) + VFAR;
  vc[28] = soX + lmap(N_PP) + pp * 128;
  vc[29] = soX + lmap(N_REC); vc[30] = role < 3 && lane == 0 ? vc[29] : OOB;
  vc[31] = lane == 0 ? (role * 4 + w) * 16 : OOB;
  v4u sh, s0, s1; v16u sc;
  set_rsrc(sh, 0, p.ws, (unsigned)HAND_BYTES + CTRL_BYTES);
  set_rsrc(s0, 0, ty == 2 ? p.wv : w >= 3 - ty ? p.wk : p.wq, DM * HD * 2); set_rsrc(s1, 0, p.wo, DM * HD * 2);
  sc[0] = ty * 4 + w; sc[1] = role; sc[2] = p.dB; sc[3] = p.dM; sc[4] = p.dS; sc[5] = p.lead; sc[6] = soX; sc[7] = (unsigned)HAND_BYTES + CTRL_BYTES;
  sc[8] = w * (ty == 2 ? 6144 : 8192); sc[9] = w * 10240; sc[10] = tag;
#pragma unroll
  for (int k = 0; k < 16; ++k) sc[k] = k < 11 ? rfl(sc[k]) : 0;
  asm volatile(CHAIN_ASM : "+{s[48:63]}"(sc), "+{v[0:31]}"(vc), "+{v[192:223]}"(vi), "+{s[36:39]}"(sh), "+{s[40:43]}"(s0), "+{s[44:47]}"(s1) :: "memory", "vcc", "scc", CHAIN_CLOBBERS);
  lane = fresh_lane();
  gu32* ct = (gu32*)(((unsigned long long)sh[0] | (unsigned long long)sh[1] << 32) + CTRL_OFF);
  if (lane == 0) {
    if (sc[12] > 20000u || sc[10] > 4000u || sc[11] > 4000u) __hip_atomic_store(ct + C_ERR, 1u, RLX_AGENT);
  }
}

__device__ __forceinline__ void load_wo(const Params& p, int h, int col0, int cstep, int lane, int wave, v32u (&wa)[7], v16u& wa7) {
  const int g = lane >> 4, q = (lane >> 2) & 3, pp = lane & 3;
  const rsrc_t ro = mkrsrc(p.wo, DM * HD * 2);
  stage4(
    [&](int m, int slot) {
#pragma unroll 1
      for (int i = 0; i < 15; ++i) {
        const int c = wave * 15 + i;
        dma16(ro, lane * 16, (h * DH + (c < 40 ? 32 * m : 96 + 16 * m)) * DM * 2 + c * 1024, slot * 61440 + c * 1024);
      }
    },
    [&](int m, int slot) {
#pragma unroll
      for (int c = 0; c < 10; ++c) {
        const unsigned col = slot * 61440 + (col0 + cstep * c + 4 * pp) * 2;
        const v2u lo = trread((8 * g + q) * 1280 + col), hi = trread((8 * g + 4 + q) * 1280 + col), tl = trread((32 + 4 * g + q) * 1280 + col);
        const int r = (c * 6 + m) * 4, r2 = (c * 6 + 4 + (m >> 1)) * 4 + 2 * (m & 1);
        W240_SET(wa, r, 0, lo[0]); W240_SET(wa, r, 1, lo[1]); W240_SET(wa, r, 2, hi[0]); W240_SET(wa, r, 3, hi[1]);
        W240_SET(wa, r2, 0, tl[0]); W240_SET(wa, r2, 1, tl[1]);
      }
    });
  BAR();
}

__device__ __forceinline__ void vo_role(const Params& p, int h, int lane, int wave, unsigned tag) {
  v32u wa[7]; v16u wa7;
  load_wo(p, h, 16 * wave, 64, lane, wave, wa, wa7);
  TS(lane == 0 && wave == 0, 256 + 96 + h)
  const unsigned long long parmask = wait_init((gu32*)(p.ws + CTRL_OFF), tag, lane);
  const int g = lane >> 4, n = lane & 15, b = 3 * (lane % RING);
  v16u vc; v8u vd; v12u init; v4u sh;
  OPAQUE(vc); OPAQUE(vd);
  vc[0] = lane * 16; vc[1] = h * 65536 + wave * 16384 + lane * 16;
  vc[2] = lmap((2 * wave + (n >> 3)) * 16384 + h * 512 + g * 128 + (n & 7) * 16);
  vc[3] = lmap(V2_OFF + (8 * wave + (n >> 1)) * 4096 + h * 128 + g * 32 + (n & 1) * 16);
  vc[4] = so_far(parmask, b + 2); vc[5] = so_near(parmask, b + 1) ^ VFAR; vc[6] = so_near(parmask, b) ^ VFAR;
  const int k = lane & 7, soX = so_near(parmask, XB);
  vc[7] = k < 3 ? (int)CTRL_OFF + (k == 1 ? C_CL : (k ? C_QP : C_VP) + h * 32) * 4 : soX + lmap(N_CF) + h * 128 + (k & 1) * 4;
  vc[8] = k == 1 ? 128 : 0; vc[9] = k == 0; vc[10] = k >= 2; vc[11] = k == 0 ? 1 : k == 1 ? 8 : -6;
  vc[12] = wave == 0 && lane == 0 ? soX + lmap(N_PP) + (h & 3) * 128 : OOB;
  vc[13] = wave == 0 && lane == 0 ? (int)CTRL_OFF + (C_OP + h * 32) * 4 : OOB;
  vc[14] = lane < 48 ? vc[1] : OOB;
  const int qt = wave & 1, lq = 24576 + (wave >> 1) * 12288, lk = 49152 + (wave >> 1) * 16384;
  vc[15] = lq + qt * 1024 + lane * 16; vd[0] = lq + 2048 + lane * 16 + qt * 8; vd[1] = lk + lane * 16;
  vd[2] = lk + 3072 + (g * 8 + (n & 7)) * 16; vd[3] = lk + 3584 + (g * 8 + (n & 7)) * 8;
  vd[4] = (h >> 2) * 32768 + ((h & 3) * 2 + qt) * 1024 + lane * 16;
  vd[5] = g < 2 ? (h >> 2) * 32768 + 16384 + qt * 1024 + (h & 3) * 256 + n * 16 + g * 8 : OOB;
  const unsigned neg = f2u(-1e30f);
#pragma unroll
  for (int r = 0; r < 4; ++r) { init[r] = g < 2 ? 0u : neg; init[4 + r] = 4 * g + r <= n + 16 * qt ? 0u : neg; init[8 + r] = 4 * g + r + 16 <= n + 16 * qt ? 0u : neg; }
  set_rsrc(sh, 0, p.ws, (unsigned)HAND_BYTES + CTRL_BYTES);
  asm volatile(VO_ASM :: "{v[0:15]}"(vc), "{v[16:23]}"(vd), "{v[208:219]}"(init), "{s[36:39]}"(sh), "{s40}"(wave), W240_IN(wa) : "memory", "vcc", "scc", VO_CLOBBERS);
}

__device__ __forceinline__ void set_up(const Params& p, int j, int blk, int lane, int wave, unsigned tag) {
  const rsrc_t rH = mkrsrc(p.ws, (unsigned)HAND_BYTES + CTRL_BYTES);
  const int tid = wave * 64 + lane;
  if (wave < 2) {
    unsigned best[2] = {1u << 30, 1u << 30};
#pragma unroll 1
    for (int rep = 0; rep < CALREP; ++rep) {
      const int off = lane < 8 ? ((j + 32 * wave) << 21) + 0x1FC000 + (rep & 1) * 8192 + lane * 16 : OOB;
      const unsigned long long t1 = __builtin_amdgcn_s_memtime();
      LGKM0();
      st16s(v4u{1u, 2u, 3u, 4u}, rH, off, 0);
      VMCNT(0);
      const unsigned d = (unsigned)(__builtin_amdgcn_s_memtime() - t1);
      if (rep & 1) best[1] = min(best[1], d); else best[0] = min(best[0], d);
    }
    const unsigned x = xcc_id();
    if (lane == 0) *(LAS unsigned*)(L_MISC + wave * 4) = ((best[1] < best[0] ? 1u : 0u) ^ (x >> 2) ^ ((x + 1 - blk) >> 2)) & 1u;
  } else {
    const int t = tid - 128, i = j * 128 + t;
    const v4u z = {0u, 0u, 0u, 0u};
    const int k = j * 80 + t, row = k / 320, cp = k % 320;
    const unsigned x = unsent(__builtin_amdgcn_raw_buffer_load_b32(mkrsrc(p.mem0, 8 * DM * 2), k * 4, 0, 0));
    st16s(z, rH, i < (C_Z1 - C_Z0) / 4 ? (int)CTRL_OFF + C_Z0 * 4 + i * 16 : OOB, 0);
#pragma unroll
    for (int u = 0; u < 2; ++u) st16s(z, rH, i + u * 4096 < (N_END - N_PP) / 8 ? (XB << 21) + 2 * N_PP + (i + u * 4096) * 16 : OOB, 0);
    st16s(v4u{~0u, ~0u, ~0u, ~0u}, mkrsrc(p.ws + ACT_OFF, TOPK * FF * 2), i * 16, 0);
    const int mo = t < 80 ? lmap(cp < 256 ? (cp >> 2) * 128 + row * 16 + (cp & 3) * 4 : 8192 + tslot(cp - 256) * 32 + row * 4) : OOB;
    __builtin_amdgcn_raw_buffer_store_b32(x, rH, mo, XB << 21, 16); __builtin_amdgcn_raw_buffer_store_b32(x, rH, mo, (XB << 21) + 8192, 16);
  }
  VMCNT(0); LGKM0(); BAR();
  if (tid == 0) __hip_atomic_store((gu32*)(p.ws + CTRL_OFF) + C_INIT + j * 32, tag | *(LAS unsigned*)L_MISC | *(LAS unsigned*)(L_MISC + 4) << 1, RLX_AGENT);
  TS(tid == 0, 768 + j)
  mark_slot(rH, so_near(wait_init((gu32*)(p.ws + CTRL_OFF), tag, lane), 3 * (j >> 3)), j & 7, lane, wave);
  VMCNT(0); BAR();
  if (tid == 0) __hip_atomic_fetch_add((gu32*)(p.ws + CTRL_OFF) + C_CL + (j >> 3) * 32, 1u, RLX_AGENT);
  TS(tid == 0, 960 + j)
  mod_unit(p, 28 + j, tid, tag, [] {});
}

__device__ __forceinline__ void proj_role(const Params& p, int type, int head, int blk, int lane, int wave, unsigned tag) {
  gu32* ctrl = (gu32*)(p.ws + CTRL_OFF);
  const int g = lane >> 4, q = (lane >> 2) & 3, pp = lane & 3;
  v32u wa[7]; v16u wa7;
  {
    if (type == 0) { set_up(p, head, blk, lane, wave, tag); __builtin_amdgcn_s_sleep(QSLEEP); }
    const rsrc_t rw = mkrsrc(type == 0 ? p.wq : type == 1 ? p.wk : p.wv, DM * HD * 2);
    stage4(
      [&](int m, int slot) {
#pragma unroll 1
        for (int i = 0; i < 15; ++i) {
          const int c = (wave * 15 + i) * 64 + lane;
          dma16(rw, ((m * 160 + c / 24) * HD + head * DH) * 2 + (c % 24) * 16, 0, slot * 61440 + (wave * 15 + i) * 1024);
        }
      },
      [&](int m, int slot) {
#pragma unroll
        for (int ks = 0; ks < 5; ++ks)
#pragma unroll
          for (int t = 0; t < 3; ++t) {
            const unsigned a = slot * 61440 + (32 * ks + 8 * g + q) * 384 + (t < 2 ? 32 * wave + 8 * pp + 4 * t : 128 + 16 * wave + 4 * pp) * 2;
            const v2u lo = trread(a), hi = trread(a + 4 * 384);
            const int r = (t * 20 + m * 5 + ks) * 4;
            W240_SET(wa, r, 0, lo[0]); W240_SET(wa, r, 1, lo[1]); W240_SET(wa, r, 2, hi[0]); W240_SET(wa, r, 3, hi[1]);
          }
      });
  }
  TS(lane == 0 && wave == 0, 256 + 160 + type * 32 + head)
  const unsigned long long parmask = wait_init(ctrl, tag, lane);
  if (wave < 2) wait_tag(ctrl, C_LNF + (wave * 64 + lane) * 8, tag);
  BAR();
  {
    const rsrc_t rh = mkrsrc(p.ws + HF_OFF, (unsigned)HF_BYTES);
#pragma unroll
    for (int k = 0; k < 20; ++k) dma16_sc1(rh, wave * 10240 + lane * 16, (k / 10) * 40960 + (k % 10) * 1024, (k / 10) * 40960 + wave * 10240 + (k % 10) * 1024);
  }
  v8u vc, sr; v4u sc;
  vc[0] = lane * 16; vc[1] = wave * 10240 + lane * 16;
  vc[2] = head * 65536 + wave * 16384 + (type == 2 ? 0 : 3072) + lane * 16;
  vc[3] = so_near(parmask, 3 * (lane % RING) + 2) ^ (type == 1 ? 0 : 0x2000);
  vc[4] = wave || lane ? OOB : type == 1 ? so_near(parmask, XB) + lmap(N_PP) + (head & 3) * 128 : (int)CTRL_OFF + ((type ? C_VP : C_QP) + head * 32) * 4;
  vc[5] = 0; vc[6] = 0; vc[7] = 0;
  set_rsrc(sr, 0, p.ws + HF_OFF, (unsigned)HF_BYTES); set_rsrc(sr, 1, p.ws, (unsigned)HAND_BYTES + CTRL_BYTES);
  sc[1] = wave * 10240; sc[2] = 0; sc[3] = type == 1 ? 0x2200 : 0;
#pragma unroll 1
  for (int k = 0; k < NSEG; k += CHUNK) {
    if (wave == 3 && k >= RING) wait_ge<8>(ctrl, C_OP + head * 32, k + CHUNK - RING + STAG * (2 - type));
    if (wave == 2 && k) wait_tag(ctrl, C_LNF + (24 + min(k + 2 + (lane >> 2) % CHUNK, NSEG - 1)) * 32 + (lane & 3) * 8, tag);
    sc[0] = k;
    TS(lane == 0 && wave == 0 && head == 0, 3100 + type * 16 + k / CHUNK)
    asm volatile(PROJ_ASM : "+{s[52:55]}"(sc) : "{v[0:7]}"(vc), "{s[36:43]}"(sr), W240_IN(wa) : "memory", "vcc", "scc", PROJ_CLOBBERS);
  }
}

constexpr int L_P = 16384;
struct Row { v4u a; unsigned b; __device__ __forceinline__ unsigned operator[](int i) const { return i < 4 ? a[i] : b; } };
__device__ __forceinline__ Row row_ld(rsrc_t r, int o, int so, int lane) { return Row{ld16s(r, o + lane * 16, so), __builtin_amdgcn_raw_buffer_load_b32(r, o + 1024 + lane * 4, so, 16)}; }
__device__ __forceinline__ void row_st(const unsigned (&x)[5], rsrc_t r, int o, int lane) {
  st16s(v4u{x[0], x[1], x[2], x[3]}, r, o + lane * 16, 0); __builtin_amdgcn_raw_buffer_store_b32(x[4], r, o + 1024 + lane * 4, 0, 16);
}
__device__ __forceinline__ void row_load(const Params& p, rsrc_t rH, int so, int row, int lane, Row (&r)[9]) {
  r[8] = row_ld(mkrsrc(p.x, S * DM * 2), row * 1280, 0, lane);
#pragma unroll
  for (int k = 0; k < 8; ++k) r[k] = row_ld(rH, ((row & 31) / 6) * 16384 + ((row & 31) % 6) * 1280, so + k * 98304, lane);
}
__device__ __forceinline__ void row_finish(const Params& p, int row, int lane, const Row (&r)[9]) {
  const rsrc_t rT = mkrsrc(p.ws + X1_OFF, (unsigned)(WS_END - X1_OFF)), rO = mkrsrc(p.out, S * DM * 2);
  Row pr[5];
#pragma unroll
  for (int k = 0; k < 5; ++k) pr[k] = Row{lds128(L_PR + k * 1280 + lane * 16), *(LAS unsigned*)(L_PR + k * 1280 + 1024 + lane * 4)};
  unsigned ul = 0x3f80u, uh = 0x3f800000u;
  asm("" : "+s"(ul), "+s"(uh));
  const bf2 lo = __builtin_bit_cast(bf2, ul), hi = __builtin_bit_cast(bf2, uh);
  unsigned xs[5], oh[5], o[5];
  float x1[10], sum = 0.f;
#pragma unroll
  for (int i = 0; i < 5; ++i) {
    float a = 0.f, c = 0.f;
#pragma unroll
    for (int k = 0; k < 8; ++k) { const unsigned w = r[k][i]; a = __builtin_amdgcn_fdot2_f32_bf16(__builtin_bit_cast(bf2, w), lo, a, false); c = __builtin_amdgcn_fdot2_f32_bf16(__builtin_bit_cast(bf2, w), hi, c, false); }
    const unsigned yb = pack2(a, c), ga = pack2(lo2f(pr[0][i]) * lo2f(yb), hi2f(pr[0][i]) * hi2f(yb));
    xs[i] = pack2(lo2f(r[8][i]) + lo2f(ga), hi2f(r[8][i]) + hi2f(ga));
    x1[2 * i] = lo2f(xs[i]); x1[2 * i + 1] = hi2f(xs[i]);
    sum += x1[2 * i] + x1[2 * i + 1];
  }
  const float mean = wave_sum(sum) * (1.f / DM);
  float sq = 0.f;
#pragma unroll
  for (int i = 0; i < 10; ++i) sq = fmaf(x1[i] - mean, x1[i] - mean, sq);
  const float mb = rbf(mean), isd = __builtin_amdgcn_rcpf(rbf(__builtin_amdgcn_sqrtf(rbf(rbf(wave_sum(sq) * (1.f / DM)) + 1e-5f))));
  float rs = 0.f;
#pragma unroll
  for (int i = 0; i < 5; ++i) {
    const unsigned d = pack2(x1[2 * i] - mb, x1[2 * i + 1] - mb), nn = pack2(lo2f(d) * isd, hi2f(d) * isd);
    const unsigned m = pack2(lo2f(nn) * lo2f(pr[2][i]), hi2f(nn) * hi2f(pr[2][i]));
    oh[i] = pack2(lo2f(m) + lo2f(pr[1][i]), hi2f(m) + hi2f(pr[1][i]));
    const float h0 = lo2f(oh[i]), h1 = hi2f(oh[i]);
    rs = fmaf(h0, lo2f(pr[4][i]), fmaf(h1, hi2f(pr[4][i]), rs));
    const unsigned gh = pack2(lo2f(pr[3][i]) * h0, hi2f(pr[3][i]) * h1);
    o[i] = pack2(x1[2 * i] + lo2f(gh), x1[2 * i + 1] + hi2f(gh));
  }
  rs = wave_sum(rs);
  __builtin_amdgcn_raw_buffer_store_b32(f2u(rbf(rs)), rT, lane == 0 ? row * 4 : OOB, 2 * (int)HF_BYTES, 16);
  row_st(oh, rT, row * 2560 + 1280, lane); row_st(xs, rT, row * 2560, lane); row_st(o, rO, row * 1280, lane);
}
template <bool LAST>
__device__ __forceinline__ void tok_unit(const Params& p, unsigned long long parmask, int s, int hq, int wave) {
  gu32* ctrl = (gu32*)(p.ws + CTRL_OFF);
  const rsrc_t rH = mkrsrc(p.ws, (unsigned)HAND_BYTES + CTRL_BYTES);
  const int lane = fresh_lane(), g = lane >> 4, n = lane & 15, h = hq * 4 + wave, b = 3 * (s % RING);
  const int sH = so_near(parmask, b), sV = so_near(parmask, b + 1) ^ VFAR, sK = so_near(parmask, b + 2), sP = so_far(parmask, 3 * ((s + RING - 4) % RING));
  const long cfw = nword(parmask, N_CF + (hq * 4 + g) * 128 + min(lane & 3, 2) * 4);
  const int k4 = (lane >> 2) & 3;
  wait_ge<4>(ctrl, k4 >= 2 ? C_AF + max(s + 4 - RING, 0) * 32 : k4 ? (LAST ? C_QP + h * 32 : C_OP + (hq * 4 + g) * 32) : LAST ? nword(parmask, N_PP + s * 512 + (lane & 3) * 128) : cfw,
             k4 >= 2 ? (s + 4 < RING ? 0 : 8) : k4 ? s + 1 : LAST ? 16 : s + 2);
  TS(lane == 0 && wave == 0 && hq == 0, 1024 + s * 8)
  v4u um[10], vo[10][4], pb[2][5];
  Row ri[9];
  const int row = s < 4 ? 0x80000 : (s - 4) * 32 + h;
  auto load_um = [&](int c) {
    um[c] = c < 8 ? ld16s(rH, lmap(P_OFF + (16 * wave + (n >> 3)) * 4096 + (hq * 4 + g) * 128 + (n & 7) * 16), sH + c * 16384)
                  : ld16s(rH, lmap(P2_OFF + (16 * wave + (n >> 1)) * 1024 + (hq * 4 + g) * 32 + (n & 1) * 16), sH + (c - 8) * 16384);
  };
  const int vm = lmap((16 * wave + (n >> 3)) * 16384 + hq * 2048 + g * 128 + (n & 7) * 16), vt = lmap(V2_OFF + (16 * wave + (n >> 1)) * 4096 + hq * 512 + g * 32 + (n & 1) * 16);
  auto load_vo = [&](int c0, int c1) {
#pragma unroll
    for (int c = c0; c < c1; ++c) {
#pragma unroll
      for (int k = 0; k < 4; ++k) vo[c][k] = c < 8 ? ld16s(rH, vm + k * 512, sV + c * 65536) : ld16s(rH, vt + k * 128, (sH ^ VFAR) + (c - 8) * 65536);
      if (!LAST) load_um(c);
    }
  };
  if (LAST) {
    v4u qf[2][6], kf[2][6], qx[4], kx[4], km[6];
    wait_ge<2>(ctrl, C_AF + (s - 4) * 32, 8u);
    row_load(p, rH, sP, row, lane, ri);
    const int fo = h * 65536 + 3072 + lane * 16;
#pragma unroll
    for (int w = 0; w < 4; ++w) {
      qf[0][w] = ld16s(rH, fo + w * 16384, sK ^ 0x2000); kf[0][w] = ld16s(rH, fo + w * 16384, sK);
      qf[1][w] = ld16s(rH, fo + w * 16384 + 1024, sK ^ 0x2000); kf[1][w] = ld16s(rH, fo + w * 16384 + 1024, sK);
      qx[w] = ld16s(rH, fo + w * 16384 + 2048, sK ^ 0x2000); kx[w] = ld16s(rH, fo + w * 16384 + 2048, sK);
    }
    __builtin_amdgcn_sched_barrier(0);
    row_finish(p, row, lane, ri);
    __builtin_amdgcn_sched_barrier(0);
    load_vo(0, 10);
    __builtin_amdgcn_sched_barrier(0);
#pragma unroll
    for (int w = 0; w < 4; ++w)
#pragma unroll
      for (int t = 0; t < 2; ++t)
#pragma unroll
        for (int k = 0; k < 2; ++k) { qf[t][4 + (w >> 1)][2 * (w & 1) + k] = qx[w][2 * t + k]; kf[t][4 + (w >> 1)][2 * (w & 1) + k] = kx[w][2 * t + k]; }
    f4 sc[2][3] = {};
#pragma unroll
    for (int f = 0; f < 6; ++f) { sc[0][1] = mfma32(kf[0][f], qf[0][f], sc[0][1]); sc[1][1] = mfma32(kf[0][f], qf[1][f], sc[1][1]); sc[1][2] = mfma32(kf[1][f], qf[1][f], sc[1][2]); }
    wait_ge<1>(ctrl, cfw, s + 1);
#pragma unroll
    for (int w = 0; w < 4; ++w) {
      km[w] = ld16s(rH, h * 65536 + 6144 + (g * 8 + (n & 7)) * 16 + w * 16384, sK);
      const v2u a = ld8s(rH, h * 65536 + 6656 + (g * 8 + (n & 7)) * 8 + w * 16384, sK);
      km[4 + (w >> 1)][2 * (w & 1)] = a[0]; km[4 + (w >> 1)][2 * (w & 1) + 1] = a[1];
    }
#pragma unroll
    for (int c = 0; c < 10; ++c) load_um(c);
    __builtin_amdgcn_sched_barrier(0);
    TS(lane == 0 && wave == 0 && hq == 0, 1025 + s * 8)
#pragma unroll
    for (int f = 0; f < 6; ++f) { sc[0][0] = mfma32(km[f], qf[0][f], sc[0][0]); sc[1][0] = mfma32(km[f], qf[1][f], sc[1][0]); }
#pragma unroll
    for (int t = 0; t < 2; ++t) {
      float e[12], mx = -3e38f, sum = 0.f;
#pragma unroll
      for (int k = 0; k < 8 + 4 * t; k += 2) {
        const unsigned u = pack2(sc[t][k >> 2][k & 3], sc[t][k >> 2][(k & 3) + 1]), w = pack2(lo2f(u) * 0.07216878364870322f, hi2f(u) * 0.07216878364870322f);
#pragma unroll
        for (int i = 0; i < 2; ++i) {
          const bool on = k < 4 ? g < 2 : (k >> 2) - 1 < t || 4 * g + (k & 3) + i <= n;
          e[k + i] = on ? (i ? hi2f(w) : lo2f(w)) : -3e38f;
          mx = fmaxf(mx, e[k + i]);
        }
      }
      mx = max_x32(max_x16(mx));
#pragma unroll
      for (int k = 0; k < 8 + 4 * t; ++k) { e[k] = __builtin_amdgcn_exp2f((e[k] - mx) * 1.4426950408889634f); sum += e[k]; }
      sum = swapsum16(sum, sum); sum = swapsum32(sum, sum);
      const float r = __builtin_amdgcn_rcpf(sum);
      *(LAS v4u*)(L_P + (wave * 2 + t) * 1024 + lane * 16) = v4u{pack2(e[4] * r, e[5] * r), pack2(e[6] * r, e[7] * r), t ? pack2(e[8] * r, e[9] * r) : 0u, t ? pack2(e[10] * r, e[11] * r) : 0u};
      *(LAS v2u*)(L_P + 8192 + (g >> 1) * 2048 + t * 1024 + wave * 256 + n * 16 + (g & 1) * 8) = v2u{pack2(e[0] * r, e[1] * r), pack2(e[2] * r, e[3] * r)};
    }
    LGKM0();
  } else {
#pragma unroll
    for (int k = 0; k < 3; ++k) dma16_sc1(rH, hq * 32768 + lane * 16, (sV ^ 0x2000) + (k < 2 ? k * 4096 : 16384) + wave * 1024, L_P + k * 4096 + wave * 1024);
    load_vo(0, 2);
    __builtin_amdgcn_sched_barrier(0);
    VMCNT(10);
  }
  BAR();
#pragma unroll
  for (int k = 0; k < 10; ++k) pb[k & 1][k >> 1] = lds128(L_P + k * 1024 + lane * 16);
  TS(lane == 0 && wave == 0 && hq == 0, 1026 + s * 8)
  int so[2];
#pragma unroll
  for (int t = 0; t < 2; ++t) so[t] = hq * 98304 + ((16 * t + n) / 6) * 16384 + ((16 * t + n) % 6) * 1280 + (g & 1) * 32 + (g >> 1) * 16;
  f4 d[2][2][2];
  unsigned af = 0;
#pragma unroll
  for (int c = 0; c < 12; c += 2) {
    if (!LAST) {
      if (c == 4) af = __hip_atomic_load(ctrl + C_AF + max(s - 4, 0) * 32, RLX_AGENT);
      if (c < 8) load_vo(c + 2, c + 4);
      if (c == 8) {
        if (s >= 4 && rfl(af) < 8) wait_ge<2>(ctrl, C_AF + (s - 4) * 32, 8u);
        row_load(p, rH, sP, row, lane, ri);
      }
      __builtin_amdgcn_sched_barrier(0);
    }
    if (c < 10) {
#pragma unroll
      for (int k = 0; k < 4; ++k) asm volatile("" : "+a"(vo[c][k]), "+a"(vo[c + 1][k]));
      asm volatile("" : "+a"(um[c]), "+a"(um[c + 1]));
#pragma unroll
      for (int t = 0; t < 2; ++t)
#pragma unroll
        for (int k = 0; k < 5; ++k)
#pragma unroll
          for (int u = 0; u < 2; ++u) d[(c >> 1) & 1][t][u] = mfma32(k < 4 ? vo[c + u][k] : um[c + u], pb[t][k], k ? d[(c >> 1) & 1][t][u] : f4{});
    }
    if (c > 0)
#pragma unroll
      for (int t = 0; t < 2; ++t) {
        const f4 x = d[~(c >> 1) & 1][t][0], y = d[~(c >> 1) & 1][t][1];
        auto lo = __builtin_amdgcn_permlane16_swap(pack2(x[0], x[1]), pack2(y[0], y[1]), false, false);
        auto hi = __builtin_amdgcn_permlane16_swap(pack2(x[2], x[3]), pack2(y[2], y[3]), false, false);
        st16s(v4u{lo[0], hi[0], lo[1], hi[1]}, rH, so[t] + (c < 10 ? 32 * (c - 2) : 1024), (sH ^ 0x2000) + (c < 10 ? 256 : 64) * wave);
      }
    __builtin_amdgcn_sched_barrier(0);
  }
  TS(lane == 0 && wave == 0 && hq == 0, 1027 + s * 8)
  const bool rel = !LAST && s + RING < NSEG;
  if (!LAST) {
    mark_slot(rH, rel ? sH : OOB, hq, lane, wave);
    __builtin_amdgcn_sched_barrier(0);
    row_finish(p, row, lane, ri);
    __builtin_amdgcn_sched_barrier(0);
    VMCNT(7);
  } else VMCNT(0);
  BAR();
  if (wave == 0 && lane < (rel ? 2 : 1)) __hip_atomic_fetch_add(ctrl + (lane ? C_CL + (s + RING) * 32 : C_AF + s * 32), 1u, RLX_AGENT);
  TS(lane == 0 && wave == 0 && hq == 0, 1028 + s * 8)
}

__device__ __forceinline__ void tok_role(const Params& p, int j, int lane, int wave, unsigned tag) {
  gu32* ctrl = (gu32*)(p.ws + CTRL_OFF);
  const rsrc_t rH = mkrsrc(p.ws, (unsigned)HAND_BYTES + CTRL_BYTES);
  const int tid = wave * 64 + lane, hq = j & 7, grp = j >> 3;
  const rsrc_t rm = mkrsrc(p.ws + CTRL_OFF + C_MOD * 4, 15360);
  {
    v4u a[10], own[3];
    float mb, isd;
    mod_unit(p, j < 28 ? j : 60, tid, tag, [&] {
#pragma unroll
      for (int i = 0; i < 10; ++i) a[i] = ln1_piece(p, j * 8, i, lane);
#pragma unroll
      for (int i = 0; i < 3; ++i) own[i] = ln1_piece(p, j * 8, min(wave + 4 * i, 9), lane);
    });
    TS(tid == 0, 896 + j)
    ln1_stats(a, mb, isd);
#pragma unroll
    for (int i = 0; i < 3; ++i)
#pragma unroll
      for (int k = 0; k < 4; ++k) own[i][k] = ln_norm(own[i][k], mb, isd);
    v4u g[3][4];
#pragma unroll 1
    for (int spin = 0; spin <= SPIN_CAP; ++spin) {
      bool bad = false;
#pragma unroll
      for (int i = 0; i < 3; ++i)
#pragma unroll
        for (int k = 0; k < 4; ++k) g[i][k] = ld16s(rm, (lane >> 3) * 32 + (k & 1) * 16 + (k >> 1) * 2560, min(wave + 4 * i, 9) * 256);
#pragma unroll
      for (int i = 0; i < 12; ++i) bad |= g[i >> 2][i & 3][1] != tag || g[i >> 2][i & 3][3] != tag;
      if (__builtin_amdgcn_ballot_w64(bad) == 0) break;
      __builtin_amdgcn_s_sleep(2);
    }
    TS(tid == 0, 928 + j)
#pragma unroll
    for (int i = 0; i < 3; ++i) {
#pragma unroll
      for (int k = 0; k < 4; ++k) own[i][k] = ln_mod(own[i][k], g[i][2 + (k >> 1)][2 * (k & 1)], g[i][k >> 1][2 * (k & 1)]);
      if (wave + 4 * i < 10) ln1_store(p, own[i], j * 8, wave + 4 * i, lane);
    }
    VMCNT(0);
    if (lane == 0) __hip_atomic_store(ctrl + C_LNF + j * 32 + wave * 8, tag, RLX_AGENT);
    TS(tid == 0, 800 + j)
  }
  auto gather = [&](int n0, int n1) {
#pragma unroll 1
    for (int n = n0; n < n1; ++n)
#pragma unroll 1
      for (int spin = 0; spin <= SPIN_CAP; ++spin) {
        const int o = min(n * 256 + tid, 959);
        const v4u a = ld16s(rm, o * 16, 0);
        *(LAS v2u*)(LP + o * 8) = v2u{a[0], a[2]};
        if (__builtin_amdgcn_ballot_w64(a[1] != tag || a[3] != tag) == 0) break;
        __builtin_amdgcn_s_sleep(8);
      }
    LGKM0(); BAR();
  };
  gather(0, 2);
  const unsigned long long parmask = wait_init(ctrl, tag, lane);
#pragma unroll 1
  for (int s = 4 + grp; s < 20; s += 4) {
    const int seg = s < 12 ? 8 + j + ((s >> 2) - 1) * 32 : NSEG, row0 = seg * 32 + wave * 8, lane = fresh_lane();
    v4u a[10];
    if (seg < NSEG)
#pragma unroll
      for (int i = 0; i < 10; ++i) a[i] = ln1_piece(p, row0, i, lane);
    mark_slot(rH, s < RING ? so_near(parmask, 3 * s) : OOB, hq, lane, wave);
    if (seg < NSEG) {
      float mb, isd;
      ln1_stats(a, mb, isd);
#pragma unroll
      for (int i = 0; i < 10; ++i) {
        const v4u sh = lds128(LP + (lane >> 3) * 16 + i * 128), sc = lds128(LP + 1280 + (lane >> 3) * 16 + i * 128);
#pragma unroll
        for (int k = 0; k < 4; ++k) a[i][k] = ln_mod(ln_norm(a[i][k], mb, isd), sc[k], sh[k]);
        ln1_store(p, a[i], row0, i, lane);
      }
    }
    VMCNT(0);
    if (seg < NSEG && lane == 0) __hip_atomic_store(ctrl + C_LNF + (24 + seg) * 32 + wave * 8, tag, RLX_AGENT);
    BAR();
    if (tid == 0 && s < RING) __hip_atomic_fetch_add(ctrl + C_CL + s * 32, 1u, RLX_AGENT);
  }
  gather(2, 4);
  TS(tid == 0, 832 + j)
  if (wave < 2) dma16(mkrsrc(p.wr, 1280), wave * 1024 + lane * 16, 0, LP + 7680 + wave * 1024);
#pragma unroll 1
  for (int s = grp; s < NSEG - 4; s += 4) tok_unit<false>(p, parmask, s, hq, wave);
  const int s = NSEG - 4 + grp;
  tok_unit<true>(p, parmask, s, hq, wave);
  lane = fresh_lane();
  wait_ge<1>(ctrl, C_AF + s * 32, 8u);
  {
    Row ri[9];
    row_load(p, rH, so_far(parmask, 3 * (s % RING)), s * 32 + hq * 4 + wave, lane, ri);
    row_finish(p, s * 32 + hq * 4 + wave, lane, ri);
    VMCNT(0);
  }
  const rsrc_t rT = mkrsrc(p.ws + X1_OFF, (unsigned)(WS_END - X1_OFF));
  lane = fresh_lane();
  unsigned t = 0;
  if (lane == 0) t = __hip_atomic_fetch_add(ctrl + C_FIN, 1u, RLX_AGENT);
  if (rfl(t) == 127) {
    TS(lane == 0, 3074)
    select_rows(ctrl, rT, lane);
    TS(lane == 0, 3075)
    if (lane == 0) __hip_atomic_store(ctrl + C_EPOCH, tag, RLX_AGENT);
  }
}

__global__ void __launch_bounds__(256, 1) k_main(Params p) {
  extern __shared__ __attribute__((aligned(16))) unsigned char smem[];
  const int tid = threadIdx.x, wave = rfl(tid >> 6), blk = blockIdx.x;
  {
    unsigned long long pc;
    asm volatile("s_getpc_b64 s[40:41]\n\ts_add_u32 s40, s40, _Z6k_main6Params@rel32@lo+4\n\ts_addc_u32 s41, s41, _Z6k_main6Params@rel32@hi+12" : "={s[40:41]}"(pc) :: "scc");
    dma16(mkrsrc((const void*)pc, CODE_BYTES), (((blk >> 3) & 3) * 256 + tid) * 128, 0, ((blk & 7) < 3 ? 12288 : L_MISC + 1024) + wave * 1024);
  }
  const unsigned tag = (*(const __attribute__((address_space(4))) unsigned*)uni64((unsigned long long)(p.ws + CTRL_OFF)) + 4u) & ~3u;
  const int x = blk & 7, t = blk >> 3;
  const int idx = (x - 3) * 32 + t, role = x < 3 ? x * 32 + t : 96 + (idx % 5) * 32 + idx / 5;
  TS(tid == 0, role)
  if (role < 128) {
    if (role < 96) chain_role(p, role, fresh_lane(), wave, tag);
    else vo_role(p, role - 96, fresh_lane(), wave, tag);
    const __attribute__((address_space(4))) Params* pk = (const __attribute__((address_space(4))) Params*)__builtin_amdgcn_kernarg_segment_ptr();
    asm volatile("" : "+s"(pk) :: "memory");
    const int ln = fresh_lane();
    Params p2 = {};
    p2.wg = pk->wg; p2.wu = pk->wu; p2.wd = pk->wd; p2.out = pk->out; p2.ws = pk->ws;
    ffn_role(p2, role, ln, wave);
  }
  else if (role < 160) tok_role(p, role - 128, fresh_lane(), wave, tag);
  else proj_role(p, (role - 160) >> 5, role & 31, blk, fresh_lane(), wave, tag);
  TS(wave == 0 && fresh_lane() == 0, 2048 + role)
}

#define SCHED_DB 90
#define SCHED_DM 78
#define SCHED_DS 100
#define SCHED_LEAD 250

extern "C" void kernel_launch(void* const* d_in, const int* in_sizes, int n_in, void* d_out, int out_size, void* d_ws, size_t ws_size,
                              hipStream_t stream) {
  Params p;
  p.x = (const u16*)d_in[0]; p.mem0 = (const u16*)d_in[1]; p.wq = (const u16*)d_in[2]; p.wk = (const u16*)d_in[3];
  p.wv = (const u16*)d_in[4]; p.wo = (const u16*)d_in[5]; p.wr = (const u16*)d_in[8]; p.wg = (const u16*)d_in[9];
  p.wu = (const u16*)d_in[10]; p.wd = (const u16*)d_in[11]; p.c = (const u16*)d_in[12]; p.wmod = (const u16*)d_in[13];
  p.out = (u16*)d_out;
  p.ws = (unsigned char*)(((uintptr_t)d_ws + 2 * MB - 1) & ~(uintptr_t)(2 * MB - 1));
  p.dB = SCHED_DB; p.dM = SCHED_DM; p.dS = SCHED_DS; p.lead = SCHED_LEAD;
  (void)hipFuncSetAttribute((const void*)k_main, hipFuncAttributeMaxDynamicSharedMemorySize, MAIN_LDS);
  k_main<<<256, 256, MAIN_LDS, stream>>>(p);
}
```

```cpp
#include <hip/hip_runtime.h>
#include <hip/hip_bf16.h>
#include <stdint.h>

#define GAS __attribute__((address_space(1)))
#define LAS __attribute__((address_space(3)))
#define RLX_AGENT __ATOMIC_RELAXED, __HIP_MEMORY_SCOPE_AGENT
typedef unsigned short u16;
typedef unsigned v4u __attribute__((ext_vector_type(4)));
typedef unsigned v2u __attribute__((ext_vector_type(2)));
typedef unsigned v8u __attribute__((ext_vector_type(8)));
typedef float f4 __attribute__((ext_vector_type(4)));
typedef __bf16 bf2 __attribute__((ext_vector_type(2)));
typedef __bf16 bf8 __attribute__((ext_vector_type(8)));
typedef __bf16 bf16v __attribute__((ext_vector_type(16)));
typedef short s4 __attribute__((ext_vector_type(4)));
typedef GAS unsigned gu32;
typedef __amdgpu_buffer_rsrc_t rsrc_t;

constexpr int S = 2048, DM = 640, NH = 32, DH = 192, HD = NH * DH, NSEG = 64, FF = 1536, TOPK = 8;

constexpr size_t MB = 1u << 20;
constexpr int RING = 20, CHUNK = 6, XB = 3 * RING, QB = 62, HB = 64;
constexpr int HS = 56;
constexpr size_t HAND_BYTES = 192 * MB;
constexpr size_t CTRL_OFF = HAND_BYTES;
constexpr unsigned CTRL_BYTES = 131072;
constexpr size_t HF_OFF = CTRL_OFF + CTRL_BYTES;
constexpr size_t HF_BYTES = (size_t)S * DM * 2;
constexpr size_t X1_OFF = HF_OFF + HF_BYTES;
constexpr size_t R_OFF = X1_OFF + 2 * HF_BYTES;
constexpr size_t ACT_OFF = R_OFF + S * 4;
constexpr size_t WS_END = ACT_OFF + TOPK * FF * 2;
static_assert(WS_END + 2 * MB <= 256 * MB, "workspace");

constexpr int C_EPOCH = 0;
constexpr int C_INIT = 64;
constexpr int C_LNF = 24576;
constexpr int C_MOD = 27648;
constexpr int C_Z0 = 2048;
constexpr int C_ANCH = 2048;
constexpr int C_ERR = 2304;
constexpr int C_FIN = 2336;
constexpr int C_IDX = 2560;
constexpr int C_AF = 4096;
constexpr int C_CL = 6144;
constexpr int C_VP = 8192;
constexpr int C_QP = 9216;
constexpr int C_OP = 10240;
constexpr int C_WP = 11264;
constexpr int C_Z1 = 12288;
constexpr int C_STAT = 12288;

#define QSLEEP 60
#define STAG 1
#define CALREP 10
#define MAP 0
#define XP 0
#define TFAR 0x2000
#define TPOL 16
#define UML ld16s
#define PACE 0
#define LPOL 16
#define LPOLL 3
#define FFN_WAIT 100
constexpr int M_OFF = 0, E_OFF = 16384, S_OFF = 32768, CPY = 40960, P_OFF = 4 * CPY, P2_OFF = P_OFF + 64 * 4096, V2_OFF = 393216, PR_OFF = 655360;
constexpr int N_PP = 0x80000, N_CF = N_PP + 32768, N_REC = N_CF + 8192, N_END = N_REC + 16384;
static_assert(P_OFF + 80 * 4096 == 120 * 4096 && P_OFF % 8192 == 0, "block");
__host__ __device__ constexpr int lmap(int off) { return ((off >> 13) << 14) | (off & 8191); }
constexpr int OOB = 0x40000000;
__host__ __device__ constexpr int tslot(int r) { return (r & ~15) | ((r & 3) << 2) | ((r >> 2) & 3); }

__device__ __forceinline__ float u2f(unsigned v) { return __builtin_bit_cast(float, v); }
__device__ __forceinline__ unsigned f2u(float v) { return __builtin_bit_cast(unsigned, v); }
__device__ __forceinline__ float bf2f(u16 v) { return __builtin_bit_cast(float, (unsigned)v << 16); }
__device__ __forceinline__ float lo2f(unsigned v) { return __builtin_bit_cast(float, v << 16); }
__device__ __forceinline__ float hi2f(unsigned v) { return __builtin_bit_cast(float, v & 0xffff0000u); }
__device__ __forceinline__ unsigned pack2(float a, float b) {
  bf2 v = {(__bf16)a, (__bf16)b};
  unsigned r = __builtin_bit_cast(unsigned, v);
  asm("" : "+v"(r));
  return r;
}
__device__ __forceinline__ float rbf(float a) { return (float)(__bf16)a; }
__device__ __forceinline__ unsigned long long uni64(unsigned long long a) {
  return (unsigned)__builtin_amdgcn_readfirstlane((unsigned)a) | (unsigned long long)(unsigned)__builtin_amdgcn_readfirstlane((unsigned)(a >> 32)) << 32;
}
__device__ __forceinline__ rsrc_t mkrsrc(const void* p, unsigned bytes) { return __builtin_amdgcn_make_buffer_rsrc((void*)uni64((unsigned long long)p), 0, bytes, 0x00020000); }
__device__ __forceinline__ unsigned xcc_id() { return (unsigned)__builtin_amdgcn_s_getreg(20 | (3 << 11)) & 0xfu; }
__device__ __forceinline__ int rfl(int v) { return __builtin_amdgcn_readfirstlane(v); }
__device__ __forceinline__ f4 mfma32(v4u a, v4u b, f4 c) { return __builtin_amdgcn_mfma_f32_16x16x32_bf16(__builtin_bit_cast(bf8, a), __builtin_bit_cast(bf8, b), c, 0, 0, 0); }
__device__ __forceinline__ f4 mfma16(v2u a, v2u b, f4 c) { return mfma32(v4u{a[0], a[1], 0u, 0u}, v4u{b[0], b[1], 0u, 0u}, c); }
__device__ __forceinline__ f4 smfma(v4u a, v8u b, f4 c, int idx) { return __builtin_amdgcn_smfmac_f32_16x16x64_bf16(__builtin_bit_cast(bf8, a), __builtin_bit_cast(bf16v, b), c, idx, 0, 0); }
__device__ __forceinline__ v2u trread(unsigned addr) { return __builtin_bit_cast(v2u, __builtin_amdgcn_ds_read_tr16_b64_v4i16((LAS s4*)addr)); }
__device__ __forceinline__ v4u lds128(unsigned addr) { return *(LAS v4u*)addr; }
__device__ __forceinline__ v2u lds64(unsigned addr) { return *(LAS v2u*)addr; }
__device__ __forceinline__ void dma16(rsrc_t r, int voff, int soff, unsigned ldsaddr) {
  asm volatile("s_mov_b32 m0, %0\n\ts_nop 3\n\tbuffer_load_dwordx4 %1, %2, %3 offen lds" ::"s"(ldsaddr), "v"(voff), "s"(r), "s"(soff) : "memory");
}
__device__ __forceinline__ void dma16_sc1(rsrc_t r, int voff, int soff, unsigned ldsaddr) {
  asm volatile("s_mov_b32 m0, %0\n\ts_nop 3\n\tbuffer_load_dwordx4 %1, %2, %3 offen sc1 lds" ::"s"(ldsaddr), "v"(voff), "s"(r), "s"(soff) : "memory");
}
#define VMCNT(n) asm volatile("s_waitcnt vmcnt(" #n ")" ::: "memory")
#define LGKM0() asm volatile("s_waitcnt lgkmcnt(0)" ::: "memory")
#define BAR() asm volatile("s_barrier" ::: "memory")
__device__ __forceinline__ float swapsum32(float a, float b) {
  auto r = __builtin_amdgcn_permlane32_swap(__builtin_bit_cast(unsigned, a), __builtin_bit_cast(unsigned, b), false, false);
  return u2f(r[0]) + u2f(r[1]);
}
__device__ __forceinline__ float swapsum16(float a, float b) {
  auto r = __builtin_amdgcn_permlane16_swap(__builtin_bit_cast(unsigned, a), __builtin_bit_cast(unsigned, b), false, false);
  return u2f(r[0]) + u2f(r[1]);
}
__device__ __forceinline__ float max_x32(float a) {
  auto r = __builtin_amdgcn_permlane32_swap(__builtin_bit_cast(unsigned, a), __builtin_bit_cast(unsigned, a), false, false);
  return fmaxf(u2f(r[0]), u2f(r[1]));
}
__device__ __forceinline__ float max_x16(float a) {
  auto r = __builtin_amdgcn_permlane16_swap(__builtin_bit_cast(unsigned, a), __builtin_bit_cast(unsigned, a), false, false);
  return fmaxf(u2f(r[0]), u2f(r[1]));
}
__device__ __forceinline__ unsigned ror8u(unsigned v) { return (unsigned)__builtin_amdgcn_update_dpp(0, (int)v, 0x128, 0xf, 0xf, true); }
__device__ __forceinline__ float ror8f(float v) { return __builtin_bit_cast(float, ror8u(__builtin_bit_cast(unsigned, v))); }
__device__ __forceinline__ float wave_sum(float v) {
  v += u2f(__builtin_amdgcn_update_dpp(0, (int)f2u(v), 0xb1, 0xf, 0xf, true)); v += u2f(__builtin_amdgcn_update_dpp(0, (int)f2u(v), 0x4e, 0xf, 0xf, true));
  v += u2f(__builtin_amdgcn_update_dpp(0, (int)f2u(v), 0x124, 0xf, 0xf, true)); v += ror8f(v);
  v = swapsum16(v, v);
  return swapsum32(v, v);
}
typedef unsigned short us2 __attribute__((ext_vector_type(2)));
__device__ __forceinline__ unsigned unsent(unsigned w) { return __builtin_bit_cast(unsigned, __builtin_elementwise_min(__builtin_bit_cast(us2, w), us2{0xfffe, 0xfffe})); }

constexpr unsigned CODE_BYTES = 141312;
struct Params {
  const u16 *x, *mem0, *wq, *wk, *wv, *wo, *wr, *wg, *wu, *wd, *c, *wmod;
  u16* out;
  unsigned char* ws;
  int dB, dM, dS, lead;
};

constexpr int L_MEM = 0, L_Q = 10240, L_RED = 16384, L_WO = 20480, L_VO = 20480, L_K = 40960, L_Q2 = L_Q + 2048;
constexpr int L_MISC = 122880;
constexpr int MAIN_LDS = 163840;
constexpr int SPIN_CAP = 1 << 18;
typedef unsigned v12u __attribute__((ext_vector_type(12)));
typedef unsigned v16u __attribute__((ext_vector_type(16)));
typedef unsigned v32u __attribute__((ext_vector_type(32)));

#define CHAIN_CLOBBERS \
  "v32", "v33", "v34", "v35", "v36", "v37", "v38", "v39", "v40", "v41", "v42", "v43", "v44", "v45", "v46", "v47", "v48", "v49", "v50", "v51", "v52", \
  "v53", "v54", "v55", "v56", "v57", "v58", "v59", "v60", "v61", "v62", "v63", "v64", "v65", "v66", "v67", "v68", "v69", "v70", "v71", "v72", "v73", \
  "v74", "v75", "v76", "v77", "v78", "v79", "v80", "v81", "v82", "v83", "v84", "v85", "v86", "v87", "v88", "v89", "v90", "v91", "v92", "v93", "v94", \
  "v95", "v100", "v101", "v102", "v103", "v104", "v105", "v106", "v107", "v108", "v109", "v110", "v111", "v112", "v113", "v114", "v115", "v116", "v117", \
  "v118", "v119", "v120", "v121", "v122", "v123", "v124", "v125", "v126", "v127", "v128", "v129", "v130", "v131", "v132", "v133", "v134", "v135", \
  "v136", "v137", "v138", "v139", "v140", "v141", "v142", "v143", "v144", "v145", "v146", "v147", "v148", "v149", "v150", "v151", "v152", "v153", \
  "v154", "v155", "v156", "v157", "v158", "v159", "v160", "v161", "v162", "v163", "v164", "v165", "v166", "v167", "v168", "v169", "v170", "v171", \
  "v172", "v173", "v174", "v175", "v176", "v177", "v178", "v179", "v180", "v181", "v182", "v183", "v184", "v185", "v186", "v187", "v188", "v189", \
  "v190", "v191", "v224", "v225", "v226", "v227", "v228", "v229", "v230", "v231", "v232", "v233", "v234", "v235", "v236", "v237", "v238", "v239", \
  "v240", "v241", "v242", "v243", "v244", "v245", "v246", "v247", "v248", "v249", "v250", "v251", "v252", "v253", "v254", "v255", "s18", "s19", "s20", \
  "s21", "s22", "s23", "s24", "s25", "s26", "s27", "s28", "s29", "s30", "s31", "s34", "s35", "s64", "s65", "s66", "s67", "s68", "s69", "s70", "s71", \
  "s72", "s73", "s74", "s75", "s76", "s77", "s78", "s79", "s80", "s81", "s82", "s83", "s84", "s85", "s86", "s87", "s88", "s89", "s90", "s91", "s92", \
  "s93", "s94", "s95", "s96", "s97", "s98", "s99", "a0", "a1", "a2", "a3", "a4", "a5", "a6", "a7", "a8", "a9", "a10", "a11", "a12", "a13", "a14", "a15", \
  "a16", "a17", "a18", "a19", "a20", "a21", "a22", "a23", "a24", "a25", "a26", "a27", "a28", "a29", "a30", "a31", "a32", "a33", "a34", "a35", "a36", \
  "a37", "a38", "a39", "a40", "a41", "a42", "a43", "a44", "a45", "a46", "a47", "a48", "a49", "a50", "a51", "a52", "a53", "a54", "a55", "a56", "a57", \
  "a58", "a59", "a60", "a61", "a62", "a63", "a64", "a65", "a66", "a67", "a68", "a69", "a70", "a71", "a72", "a73", "a74", "a75", "a76", "a77", "a78", \
  "a79", "a80", "a81", "a82", "a83", "a84", "a85", "a86", "a87", "a88", "a89", "a90", "a91", "a92", "a93", "a94", "a95", "a96", "a97", "a98", "a99", \
  "a100", "a101", "a102", "a103", "a104", "a105", "a106", "a107", "a108", "a109", "a110", "a111", "a112", "a113", "a114", "a115", "a116", "a117", \
  "a118", "a119", "a120", "a121", "a122", "a123", "a124", "a125", "a126", "a127", "a128", "a129", "a130", "a131", "a132", "a133", "a134", "a135", \
  "a136", "a137", "a138", "a139", "a140", "a141", "a142", "a143", "a144", "a145", "a146", "a147", "a148", "a149", "a150", "a151", "a152", "a153", \
  "a154", "a155", "a156", "a157", "a158", "a159", "a160", "a161", "a162", "a163", "a164", "a165", "a166", "a167", "a168", "a169", "a170", "a171", \
  "a172", "a173", "a174", "a175", "a176", "a177", "a178", "a179", "a180", "a181", "a182", "a183", "a184", "a185", "a186", "a187", "a188", "a189", \
  "a190", "a191", "a192", "a193", "a194", "a195", "a196", "a197", "a198", "a199", "a200", "a201", "a202", "a203", "a204", "a205", "a206", "a207", \
  "a208", "a209", "a210", "a211", "a212", "a213", "a214", "a215", "a216", "a217", "a218", "a219", "a220", "a221", "a222", "a223", "a224", "a225", \
  "a226", "a227", "a228", "a229", "a230", "a231", "a232", "a233", "a234", "a235", "a236", "a237", "a238", "a239", "a240", "a241", "a242", "a243", \
  "a244", "a245", "a246", "a247", "a248", "a249", "a250", "a251", "a252", "a253", "a254", "a255"
#define CHAIN_ASM \
  "s_and_b32 s71, s48, 3\n" \
  "s_cmp_eq_u32 s71, 2\n" \
  "s_cbranch_scc1 LF2_%=\n" \
  "s_cmp_ge_u32 s48, 8\n" \
  "s_cbranch_scc1 LF1_%=\n" \
  "s_mov_b32 s71, 0\n" \
  "s_add_u32 m0, s56, 57344\n" \
  "s_nop 0\n" \
  "buffer_load_dwordx4 v192, s[40:43], s71 offen lds\n" \
  "s_add_u32 m0, s56, 58368\n" \
  "s_nop 0\n" \
  "buffer_load_dwordx4 v193, s[40:43], s71 offen lds\n" \
  "s_add_u32 m0, s56, 59392\n" \
  "s_nop 0\n" \
  "buffer_load_dwordx4 v194, s[40:43], s71 offen lds\n" \
  "s_add_u32 m0, s56, 60416\n" \
  "s_nop 0\n" \
  "buffer_load_dwordx4 v195, s[40:43], s71 offen lds\n" \
  "s_add_u32 m0, s56, 61440\n" \
  "s_nop 0\n" \
  "buffer_load_dwordx4 v196, s[40:43], s71 offen lds\n" \
  "s_add_u32 m0, s56, 62464\n" \
  "s_nop 0\n" \
  "buffer_load_dwordx4 v197, s[40:43], s71 offen lds\n" \
  "s_add_u32 m0, s56, 63488\n" \
  "s_nop 0\n" \
  "buffer_load_dwordx4 v198, s[40:43], s71 offen lds\n" \
  "s_add_u32 m0, s56, 64512\n" \
  "s_nop 0\n" \
  "buffer_load_dwordx4 v199, s[40:43], s71 offen lds\n" \
  "s_mov_b32 s71, 786432\n" \
  "s_add_u32 m0, s56, 90112\n" \
  "s_nop 0\n" \
  "buffer_load_dwordx4 v192, s[40:43], s71 offen lds\n" \
  "s_add_u32 m0, s56, 91136\n" \
  "s_nop 0\n" \
  "buffer_load_dwordx4 v193, s[40:43], s71 offen lds\n" \
  "s_add_u32 m0, s56, 92160\n" \
  "s_nop 0\n" \
  "buffer_load_dwordx4 v194, s[40:43], s71 offen lds\n" \
  "s_add_u32 m0, s56, 93184\n" \
  "s_nop 0\n" \
  "buffer_load_dwordx4 v195, s[40:43], s71 offen lds\n" \
  "s_add_u32 m0, s56, 94208\n" \
  "s_nop 0\n" \
  "buffer_load_dwordx4 v196, s[40:43], s71 offen lds\n" \
  "s_add_u32 m0, s56, 95232\n" \
  "s_nop 0\n" \
  "buffer_load_dwordx4 v197, s[40:43], s71 offen lds\n" \
  "s_add_u32 m0, s56, 96256\n" \
  "s_nop 0\n" \
  "buffer_load_dwordx4 v198, s[40:43], s71 offen lds\n" \
  "s_add_u32 m0, s56, 97280\n" \
  "s_nop 0\n" \
  "buffer_load_dwordx4 v199, s[40:43], s71 offen lds\n" \
  "s_mov_b32 s71, 1572864\n" \
  "s_add_u32 m0, s56, 122880\n" \
  "s_nop 0\n" \
  "buffer_load_dwordx4 v192, s[40:43], s71 offen lds\n" \
  "s_add_u32 m0, s56, 123904\n" \
  "s_nop 0\n" \
  "buffer_load_dwordx4 v193, s[40:43], s71 offen lds\n" \
  "s_add_u32 m0, s56, 124928\n" \
  "s_nop 0\n" \
  "buffer_load_dwordx4 v194, s[40:43], s71 offen lds\n" \
  "s_add_u32 m0, s56, 125952\n" \
  "s_nop 0\n" \
  "buffer_load_dwordx4 v195, s[40:43], s71 offen lds\n" \
  "s_add_u32 m0, s56, 126976\n" \
  "s_nop 0\n" \
  "buffer_load_dwordx4 v196, s[40:43], s71 offen lds\n" \
  "s_add_u32 m0, s56, 128000\n" \
  "s_nop 0\n" \
  "buffer_load_dwordx4 v197, s[40:43], s71 offen lds\n" \
  "s_add_u32 m0, s56, 129024\n" \
  "s_nop 0\n" \
  "buffer_load_dwordx4 v198, s[40:43], s71 offen lds\n" \
  "s_add_u32 m0, s56, 130048\n" \
  "s_nop 0\n" \
  "buffer_load_dwordx4 v199, s[40:43], s71 offen lds\n" \
  "s_branch LF2_%=\n" \
  "LF1_%=:\n" \
  "s_mov_b32 s71, 0\n" \
  "s_add_u32 m0, s56, 16384\n" \
  "s_nop 0\n" \
  "buffer_load_dwordx4 v192, s[40:43], s71 offen lds\n" \
  "s_add_u32 m0, s56, 17408\n" \
  "s_nop 0\n" \
  "buffer_load_dwordx4 v193, s[40:43], s71 offen lds\n" \
  "s_add_u32 m0, s56, 18432\n" \
  "s_nop 0\n" \
  "buffer_load_dwordx4 v194, s[40:43], s71 offen lds\n" \
  "s_add_u32 m0, s56, 19456\n" \
  "s_nop 0\n" \
  "buffer_load_dwordx4 v195, s[40:43], s71 offen lds\n" \
  "s_add_u32 m0, s56, 20480\n" \
  "s_nop 0\n" \
  "buffer_load_dwordx4 v196, s[40:43], s71 offen lds\n" \
  "s_add_u32 m0, s56, 21504\n" \
  "s_nop 0\n" \
  "buffer_load_dwordx4 v197, s[40:43], s71 offen lds\n" \
  "s_mov_b32 s71, 786432\n" \
  "s_add_u32 m0, s56, 40960\n" \
  "s_nop 0\n" \
  "buffer_load_dwordx4 v192, s[40:43], s71 offen lds\n" \
  "s_add_u32 m0, s56, 41984\n" \
  "s_nop 0\n" \
  "buffer_load_dwordx4 v193, s[40:43], s71 offen lds\n" \
  "s_add_u32 m0, s56, 43008\n" \
  "s_nop 0\n" \
  "buffer_load_dwordx4 v194, s[40:43], s71 offen lds\n" \
  "s_add_u32 m0, s56, 44032\n" \
  "s_nop 0\n" \
  "buffer_load_dwordx4 v195, s[40:43], s71 offen lds\n" \
  "s_add_u32 m0, s56, 45056\n" \
  "s_nop 0\n" \
  "buffer_load_dwordx4 v196, s[40:43], s71 offen lds\n" \
  "s_add_u32 m0, s56, 46080\n" \
  "s_nop 0\n" \
  "buffer_load_dwordx4 v197, s[40:43], s71 offen lds\n" \
  "s_mov_b32 s71, 1572864\n" \
  "s_add_u32 m0, s56, 65536\n" \
  "s_nop 0\n" \
  "buffer_load_dwordx4 v192, s[40:43], s71 offen lds\n" \
  "s_add_u32 m0, s56, 66560\n" \
  "s_nop 0\n" \
  "buffer_load_dwordx4 v193, s[40:43], s71 offen lds\n" \
  "s_add_u32 m0, s56, 67584\n" \
  "s_nop 0\n" \
  "buffer_load_dwordx4 v194, s[40:43], s71 offen lds\n" \
  "s_add_u32 m0, s56, 68608\n" \
  "s_nop 0\n" \
  "buffer_load_dwordx4 v195, s[40:43], s71 offen lds\n" \
  "s_add_u32 m0, s56, 69632\n" \
  "s_nop 0\n" \
  "buffer_load_dwordx4 v196, s[40:43], s71 offen lds\n" \
  "s_add_u32 m0, s56, 70656\n" \
  "s_nop 0\n" \
  "buffer_load_dwordx4 v197, s[40:43], s71 offen lds\n" \
  "s_mov_b32 s71, 2359296\n" \
  "s_add_u32 m0, s56, 90112\n" \
  "s_nop 0\n" \
  "buffer_load_dwordx4 v192, s[40:43], s71 offen lds\n" \
  "s_add_u32 m0, s56, 91136\n" \
  "s_nop 0\n" \
  "buffer_load_dwordx4 v193, s[40:43], s71 offen lds\n" \
  "s_add_u32 m0, s56, 92160\n" \
  "s_nop 0\n" \
  "buffer_load_dwordx4 v194, s[40:43], s71 offen lds\n" \
  "s_add_u32 m0, s56, 93184\n" \
  "s_nop 0\n" \
  "buffer_load_dwordx4 v195, s[40:43], s71 offen lds\n" \
  "s_add_u32 m0, s56, 94208\n" \
  "s_nop 0\n" \
  "buffer_load_dwordx4 v196, s[40:43], s71 offen lds\n" \
  "s_add_u32 m0, s56, 95232\n" \
  "s_nop 0\n" \
  "buffer_load_dwordx4 v197, s[40:43], s71 offen lds\n" \
  "LF2_%=:\n" \
  "v_writelane_b32 v224, s50, 0\n" \
  "v_writelane_b32 v224, s51, 1\n" \
  "v_writelane_b32 v224, s52, 2\n" \
  "v_writelane_b32 v224, s58, 3\n" \
  "v_writelane_b32 v224, s40, 4\n" \
  "v_writelane_b32 v224, s41, 5\n" \
  "v_writelane_b32 v224, s42, 6\n" \
  "v_writelane_b32 v224, s43, 7\n" \
  "v_writelane_b32 v224, s44, 8\n" \
  "v_writelane_b32 v224, s45, 9\n" \
  "v_writelane_b32 v224, s46, 10\n" \
  "v_writelane_b32 v224, s47, 11\n" \
  "s_memrealtime s[74:75]\n" \
  "s_memtime s[76:77]\n" \
  "s_mov_b32 s73, 0\n" \
  "s_mov_b32 s38, 0\n" \
  "s_mov_b32 s64, 62\n" \
  "s_mov_b32 s60, 20001\n" \
  "s_mov_b32 s50, 0\n" \
  "s_mov_b32 s51, 0\n" \
  "s_mov_b32 s52, 4000\n" \
  "s_mov_b32 s90, 5900\n" \
  "s_waitcnt lgkmcnt(0)\n" \
  "s_mov_b32 s88, s74\n" \
  "v_mov_b32 v225, v25\n" \
  "v_mov_b32 v226, v26\n" \
  "s_cmp_ge_u32 s48, 8\n" \
  "s_cbranch_scc1 LDV_%=\n" \
  "LCN_%=:\n" \
  "s_mov_b32 s28, 0xffff0000\n" \
  "s_mov_b32 s29, 0x0\n" \
  "s_mov_b32 s26, 0x0\n" \
  "s_mov_b32 s27, 0xffff\n" \
  "s_mov_b32 s24, 0x0\n" \
  "s_mov_b32 s25, 0xffff0000\n" \
  "s_mov_b32 s46, 0x3030303\n" \
  "s_mov_b32 s47, 0x3030303\n" \
  "s_mov_b32 s30, 0xff00ff\n" \
  "s_mov_b32 s31, 0xff00ff00\n" \
  "s_mov_b32 s20, 0xff00ff\n" \
  "s_mov_b32 s21, 0xff00ff\n" \
  "s_mov_b32 s42, 0x3dd53b94\n" \
  "s_mov_b32 s43, 0x3dd53b94\n" \
  "s_mov_b32 s22, 0x3f80\n" \
  "s_mov_b32 s23, 0x3f800000\n" \
  "v_mov_b32 v56, 0x3f803f80\n" \
  "v_mov_b32 v72, 0\n" \
  "v_mov_b32 v57, 0x3f803f80\n" \
  "v_mov_b32 v73, 0\n" \
  "v_mov_b32 v58, 0x3f803f80\n" \
  "v_mov_b32 v74, 0\n" \
  "v_mov_b32 v59, 0x3f803f80\n" \
  "v_mov_b32 v75, 0\n" \
  "v_mov_b32 v60, 0x3f803f80\n" \
  "v_mov_b32 v76, 0\n" \
  "v_mov_b32 v61, 0x3f803f80\n" \
  "v_mov_b32 v77, 0\n" \
  "v_mov_b32 v62, 0x3f803f80\n" \
  "v_mov_b32 v78, 0\n" \
  "v_mov_b32 v63, 0x3f803f80\n" \
  "v_mov_b32 v79, 0\n" \
  "v_mov_b32 v64, 0x3f803f80\n" \
  "v_mov_b32 v80, 0\n" \
  "v_mov_b32 v65, 0x3f803f80\n" \
  "v_mov_b32 v81, 0\n" \
  "v_mov_b32 v66, 0x3f803f80\n" \
  "v_mov_b32 v82, 0\n" \
  "v_mov_b32 v67, 0x3f803f80\n" \
  "v_mov_b32 v83, 0\n" \
  "v_mov_b32 v68, 0x3f803f80\n" \
  "v_mov_b32 v84, 0\n" \
  "v_mov_b32 v69, 0x3f803f80\n" \
  "v_mov_b32 v85, 0\n" \
  "v_mov_b32 v70, 0x3f803f80\n" \
  "v_mov_b32 v86, 0\n" \
  "v_mov_b32 v71, 0x3f803f80\n" \
  "v_mov_b32 v87, 0\n" \
  "s_mov_b32 exec_lo, 0xfcfc\n" \
  "s_mov_b32 exec_hi, 0x0\n" \
  "v_mov_b32 v72, 0x3f803f80\n" \
  "v_mov_b32 v73, 0x3f803f80\n" \
  "v_mov_b32 v74, 0x3f803f80\n" \
  "v_mov_b32 v75, 0x3f803f80\n" \
  "s_mov_b32 exec_lo, 0xfcfc0000\n" \
  "s_mov_b32 exec_hi, 0x0\n" \
  "v_mov_b32 v76, 0x3f803f80\n" \
  "v_mov_b32 v77, 0x3f803f80\n" \
  "v_mov_b32 v78, 0x3f803f80\n" \
  "v_mov_b32 v79, 0x3f803f80\n" \
  "s_mov_b32 exec_lo, 0x0\n" \
  "s_mov_b32 exec_hi, 0xfcfc\n" \
  "v_mov_b32 v80, 0x3f803f80\n" \
  "v_mov_b32 v81, 0x3f803f80\n" \
  "v_mov_b32 v82, 0x3f803f80\n" \
  "v_mov_b32 v83, 0x3f803f80\n" \
  "s_mov_b32 exec_lo, 0x0\n" \
  "s_mov_b32 exec_hi, 0xfcfc0000\n" \
  "v_mov_b32 v84, 0x3f803f80\n" \
  "v_mov_b32 v85, 0x3f803f80\n" \
  "v_mov_b32 v86, 0x3f803f80\n" \
  "v_mov_b32 v87, 0x3f803f80\n" \
  "s_mov_b64 exec, -1\n" \
  "v_mov_b32 v33, 19456\n" \
  "v_mov_b32 v36, 0\n" \
  "ds_write_b32 v33, v36\n" \
  "s_cmp_eq_u32 s73, 1\n" \
  "s_cbranch_scc1 LSW_%=\n" \
  "s_branch LPASS_%=\n" \
  "LDV_%=:\n" \
  "s_mov_b32 s64, 63\n" \
  "LPASS_%=:\n" \
  "s_mul_i32 s93, s52, 24\n" \
  "s_cmp_eq_u32 s73, 0\n" \
  "s_cselect_b32 s94, -1073741824, -120\n" \
  "s_mov_b32 s97, 0\n" \
  "s_mov_b32 s98, 0\n" \
  "s_mov_b32 s19, 0\n" \
  "s_mov_b32 s34, 0\n" \
  "s_mov_b32 s35, 0\n" \
  "s_mul_i32 s58, s64, 100\n" \
  "s_mov_b32 s59, s58\n" \
  "s_mov_b32 s61, 64\n" \
  "s_mov_b32 s99, 3\n" \
  "s_mov_b32 s18, 16\n" \
  "s_mov_b32 s67, s54\n" \
  "s_add_u32 s50, s50, 16\n" \
  "s_mul_i32 s91, s50, 24\n" \
  "s_add_u32 s51, s51, 16\n" \
  "s_mul_i32 s92, s51, 24\n" \
  "s_memrealtime s[74:75]\n" \
  "s_memtime s[76:77]\n" \
  "s_waitcnt lgkmcnt(0)\n" \
  "s_add_u32 s89, s76, 0x1000000\n" \
  "s_mov_b32 s95, s74\n" \
  "s_mov_b32 s96, s76\n" \
  "v_readlane_b32 s66, v25, s64\n" \
  "v_readlane_b32 s68, v26, s64\n" \
  "s_nop 3\n" \
  "s_and_b32 s71, s48, 3\n" \
  "s_cmp_eq_u32 s71, 2\n" \
  "s_cbranch_scc0 LPA_%=\n" \
  "buffer_load_dword v34, v29, s[36:39], 0 offen sc1\n" \
  "s_waitcnt vmcnt(0)\n" \
  "LPA_%=:\n" \
  "s_cmp_lt_u32 s48, 2\n" \
  "s_cbranch_scc0 LPB_%=\n" \
  "s_add_u32 s91, s68, 16384\n" \
  "s_add_u32 s92, s68, 32768\n" \
  "s_add_u32 s93, s68, 49152\n" \
  "buffer_load_dwordx4 v[88:91], v1, s[36:39], s68 offen sc1\n" \
  "buffer_load_dwordx4 v[92:95], v1, s[36:39], s91 offen sc1\n" \
  "buffer_load_dwordx4 v[36:39], v1, s[36:39], s92 offen sc1\n" \
  "buffer_load_dwordx4 v[100:103], v1, s[36:39], s93 offen sc1\n" \
  "buffer_load_dwordx2 v[104:105], v2, s[36:39], s68 offen sc1\n" \
  "buffer_load_dwordx2 v[106:107], v2, s[36:39], s91 offen sc1\n" \
  "buffer_load_dwordx2 v[108:109], v2, s[36:39], s92 offen sc1\n" \
  "buffer_load_dwordx2 v[110:111], v2, s[36:39], s93 offen sc1\n" \
  "LPB_%=:\n" \
  "s_cmp_eq_u32 s48, 1\n" \
  "s_cbranch_scc1 LSTART01_%=\n" \
  "s_cmp_eq_u32 s48, 2\n" \
  "s_cbranch_scc1 LSTART02_%=\n" \
  "s_cmp_eq_u32 s48, 3\n" \
  "s_cbranch_scc1 LSTART03_%=\n" \
  "s_cmp_eq_u32 s48, 4\n" \
  "s_cbranch_scc1 LSTART10_%=\n" \
  "s_cmp_eq_u32 s48, 5\n" \
  "s_cbranch_scc1 LSTART11_%=\n" \
  "s_cmp_eq_u32 s48, 6\n" \
  "s_cbranch_scc1 LSTART12_%=\n" \
  "s_cmp_eq_u32 s48, 7\n" \
  "s_cbranch_scc1 LSTART13_%=\n" \
  "s_cmp_eq_u32 s48, 8\n" \
  "s_cbranch_scc1 LSTART20_%=\n" \
  "s_cmp_eq_u32 s48, 9\n" \
  "s_cbranch_scc1 LSTART21_%=\n" \
  "s_cmp_eq_u32 s48, 10\n" \
  "s_cbranch_scc1 LSTART22_%=\n" \
  "s_cmp_eq_u32 s48, 11\n" \
  "s_cbranch_scc1 LSTART23_%=\n" \
  "LSTART00_%=:\n" \
  "LOOP00_%=:\n" \
  "v_readlane_b32 s70, v27, s64\n" \
  "s_mov_b32 s65, s66\n" \
  "s_xor_b32 s69, s66, 0x2000\n" \
  "s_add_u32 s64, s64, 1\n" \
  "v_mov_b32 v176, 0\n" \
  "v_mov_b32 v177, 0\n" \
  "v_mov_b32 v178, 0\n" \
  "v_mov_b32 v179, 0\n" \
  "v_mov_b32 v180, 0\n" \
  "v_mov_b32 v181, 0\n" \
  "v_mov_b32 v182, 0\n" \
  "v_mov_b32 v183, 0\n" \
  "v_readlane_b32 s66, v25, s64\n" \
  "v_readlane_b32 s68, v26, s64\n" \
  "s_add_u32 s91, s65, 81920\n" \
  "s_add_u32 s92, s65, 163840\n" \
  "s_mov_b32 m0, 20480\n" \
  "s_add_u32 s72, s70, 4096\n" \
  "s_barrier\n" \
  "buffer_load_dwordx4 v3, s[36:39], s70 offen sc1 lds\n" \
  "buffer_load_dwordx4 v3, s[36:39], s70 offen offset:1024 sc1 lds\n" \
  "buffer_load_dwordx4 v3, s[36:39], s70 offen offset:2048 sc1 lds\n" \
  "buffer_load_dwordx4 v3, s[36:39], s70 offen offset:3072 sc1 lds\n" \
  "LM00_%=:\n" \
  "s_barrier\n" \
  "ds_read_b128 v[112:115], v5\n" \
  "ds_read_b128 v[116:119], v5 offset:512\n" \
  "s_barrier\n" \
  "ds_read_b128 v[120:123], v5 offset:1024\n" \
  "ds_read_b128 v[124:127], v5 offset:1536\n" \
  "s_waitcnt lgkmcnt(3)\n" \
  "v_smfmac_f32_16x16x64_bf16 v[176:179], v[112:115], a[0:7], v0\n" \
  "v_smfmac_f32_16x16x64_bf16 v[180:183], v[112:115], a[160:167], v0\n" \
  "s_waitcnt lgkmcnt(2)\n" \
  "v_smfmac_f32_16x16x64_bf16 v[176:179], v[116:119], a[8:15], v0\n" \
  "s_barrier\n" \
  "ds_read_b128 v[128:131], v5 offset:2048\n" \
  "v_smfmac_f32_16x16x64_bf16 v[180:183], v[116:119], a[168:175], v0\n" \
  "s_waitcnt lgkmcnt(2)\n" \
  "ds_read_b128 v[132:135], v5 offset:2560\n" \
  "v_smfmac_f32_16x16x64_bf16 v[176:179], v[120:123], a[16:23], v0\n" \
  "ds_read_b128 v[136:139], v5 offset:3072\n" \
  "v_smfmac_f32_16x16x64_bf16 v[180:183], v[120:123], a[176:183], v0\n" \
  "s_waitcnt lgkmcnt(3)\n" \
  "ds_read_b128 v[140:143], v5 offset:3584\n" \
  "v_smfmac_f32_16x16x64_bf16 v[176:179], v[124:127], a[24:31], v0\n" \
  "s_barrier\n" \
  "s_waitcnt vmcnt(4)\n" \
  "ds_read_b128 v[144:147], v5 offset:4096\n" \
  "v_smfmac_f32_16x16x64_bf16 v[180:183], v[124:127], a[184:191], v0\n" \
  "s_mov_b32 m0, 24576\n" \
  "s_waitcnt lgkmcnt(4)\n" \
  "ds_read_b128 v[148:151], v5 offset:4608\n" \
  "v_smfmac_f32_16x16x64_bf16 v[176:179], v[128:131], a[32:39], v0\n" \
  "ds_read_b128 v[152:155], v5 offset:5120\n" \
  "buffer_load_dwordx4 v3, s[36:39], s72 offen sc1 lds\n" \
  "v_smfmac_f32_16x16x64_bf16 v[180:183], v[128:131], a[192:199], v0\n" \
  "s_waitcnt lgkmcnt(5)\n" \
  "ds_read_b128 v[156:159], v5 offset:5632\n" \
  "v_smfmac_f32_16x16x64_bf16 v[176:179], v[132:135], a[40:47], v0\n" \
  "s_barrier\n" \
  "ds_read_b128 v[160:163], v5 offset:6144\n" \
  "buffer_load_dwordx4 v3, s[36:39], s72 offen offset:1024 sc1 lds\n" \
  "v_smfmac_f32_16x16x64_bf16 v[180:183], v[132:135], a[200:207], v0\n" \
  "s_waitcnt lgkmcnt(6)\n" \
  "ds_read_b128 v[164:167], v5 offset:6656\n" \
  "v_smfmac_f32_16x16x64_bf16 v[176:179], v[136:139], a[48:55], v0\n" \
  "ds_read_b128 v[168:171], v5 offset:7168\n" \
  "buffer_load_dwordx4 v3, s[36:39], s72 offen offset:2048 sc1 lds\n" \
  "v_smfmac_f32_16x16x64_bf16 v[180:183], v[136:139], a[208:215], v0\n" \
  "s_waitcnt lgkmcnt(7)\n" \
  "ds_read_b128 v[172:175], v5 offset:7680\n" \
  "v_smfmac_f32_16x16x64_bf16 v[176:179], v[140:143], a[56:63], v0\n" \
  "s_barrier\n" \
  "ds_read2_b32 v[112:113], v6 offset0:0 offset1:32\n" \
  "buffer_load_dwordx4 v3, s[36:39], s72 offen offset:3072 sc1 lds\n" \
  "v_smfmac_f32_16x16x64_bf16 v[180:183], v[140:143], a[216:223], v0\n" \
  "s_waitcnt lgkmcnt(8)\n" \
  "ds_read2_b32 v[114:115], v6 offset0:64 offset1:96\n" \
  "v_smfmac_f32_16x16x64_bf16 v[176:179], v[144:147], a[64:71], v0\n" \
  "s_waitcnt lgkmcnt(8)\n" \
  "ds_read2_b32 v[116:117], v6 offset0:128 offset1:160\n" \
  "v_smfmac_f32_16x16x64_bf16 v[176:179], v[148:151], a[72:79], v0\n" \
  "s_waitcnt lgkmcnt(8)\n" \
  "ds_read2_b32 v[118:119], v6 offset0:192 offset1:224\n" \
  "v_smfmac_f32_16x16x64_bf16 v[176:179], v[152:155], a[80:87], v0\n" \
  "s_waitcnt lgkmcnt(8)\n" \
  "ds_read2_b32 v[120:121], v7 offset0:0 offset1:32\n" \
  "v_smfmac_f32_16x16x64_bf16 v[176:179], v[156:159], a[88:95], v0\n" \
  "s_waitcnt lgkmcnt(8)\n" \
  "ds_read2_b32 v[122:123], v7 offset0:64 offset1:96\n" \
  "v_smfmac_f32_16x16x64_bf16 v[176:179], v[160:163], a[96:103], v0\n" \
  "s_waitcnt lgkmcnt(8)\n" \
  "ds_read2_b32 v[124:125], v7 offset0:128 offset1:160\n" \
  "v_smfmac_f32_16x16x64_bf16 v[176:179], v[164:167], a[104:111], v0\n" \
  "s_waitcnt lgkmcnt(8)\n" \
  "ds_read2_b32 v[126:127], v7 offset0:192 offset1:224\n" \
  "v_smfmac_f32_16x16x64_bf16 v[176:179], v[168:171], a[112:119], v0\n" \
  "s_waitcnt lgkmcnt(8)\n" \
  "v_smfmac_f32_16x16x64_bf16 v[176:179], v[172:175], a[120:127], v0\n" \
  "s_waitcnt lgkmcnt(6)\n" \
  "v_smfmac_f32_16x16x64_bf16 v[176:179], v[112:115], a[128:135], v0\n" \
  "s_waitcnt lgkmcnt(4)\n" \
  "v_smfmac_f32_16x16x64_bf16 v[176:179], v[116:119], a[136:143], v0\n" \
  "s_waitcnt lgkmcnt(2)\n" \
  "v_smfmac_f32_16x16x64_bf16 v[176:179], v[120:123], a[144:151], v0\n" \
  "s_waitcnt lgkmcnt(0)\n" \
  "v_smfmac_f32_16x16x64_bf16 v[176:179], v[124:127], a[152:159], v0\n" \
  "v_smfmac_f32_16x16x64_bf16 v[180:183], v[144:147], a[224:231], v0\n" \
  "v_smfmac_f32_16x16x64_bf16 v[180:183], v[148:151], a[232:239], v0\n" \
  "v_cvt_pk_bf16_f32 v184, v176, v177\n" \
  "v_smfmac_f32_16x16x64_bf16 v[180:183], v[152:155], a[240:247], v0\n" \
  "v_cvt_pk_bf16_f32 v185, v178, v179\n" \
  "v_smfmac_f32_16x16x64_bf16 v[180:183], v[156:159], a[248:255], v0\n" \
  "ds_write_b64 v8, v[184:185]\n" \
  "v_add_f32 v186, v176, v177\n" \
  "v_smfmac_f32_16x16x64_bf16 v[180:183], v[160:163], v[192:199], v0\n" \
  "v_add_f32 v187, v178, v179\n" \
  "v_smfmac_f32_16x16x64_bf16 v[180:183], v[164:167], v[200:207], v0\n" \
  "v_cmp_u_f32 vcc, v186, v187\n" \
  "v_smfmac_f32_16x16x64_bf16 v[180:183], v[168:171], v[208:215], v0\n" \
  "v_smfmac_f32_16x16x64_bf16 v[180:183], v[172:175], v[216:223], v0\n" \
  "s_cbranch_vccnz LMF00_%=\n" \
  "LMOK00_%=:\n" \
  "v_smfmac_f32_16x16x64_bf16 v[180:183], v[112:115], v[224:231], v0\n" \
  "s_waitcnt lgkmcnt(0)\n" \
  "s_barrier\n" \
  "ds_read_b64_tr_b16 v[128:129], v10\n" \
  "v_smfmac_f32_16x16x64_bf16 v[180:183], v[116:119], v[232:239], v0\n" \
  "ds_read_b64_tr_b16 v[130:131], v10 offset:32\n" \
  "ds_read_b64_tr_b16 v[132:133], v10 offset:512\n" \
  "v_smfmac_f32_16x16x64_bf16 v[180:183], v[120:123], v[240:247], v0\n" \
  "ds_read_b64_tr_b16 v[134:135], v10 offset:544\n" \
  "ds_read_b64_tr_b16 v[136:137], v10 offset:1024\n" \
  "v_smfmac_f32_16x16x64_bf16 v[180:183], v[124:127], v[248:255], v0\n" \
  "s_nop 0\n" \
  "ds_read_b64_tr_b16 v[138:139], v10 offset:1056\n" \
  "ds_read_b64_tr_b16 v[140:141], v10 offset:1536\n" \
  "ds_read_b64_tr_b16 v[142:143], v10 offset:1568\n" \
  "v_mov_b32 v31, v31\n" \
  "s_nop 2\n" \
  "v_cvt_pk_bf16_f32 v186, v180, v181\n" \
  "v_cvt_pk_bf16_f32 v187, v182, v183\n" \
  "ds_write_b64 v9, v[186:187]\n" \
  "s_waitcnt lgkmcnt(7)\n" \
  "v_mfma_f32_16x16x32_bf16 v[188:191], v[88:91], v[128:131], 0\n" \
  "s_waitcnt lgkmcnt(5)\n" \
  "v_mfma_f32_16x16x32_bf16 v[188:191], v[92:95], v[132:135], v[188:191]\n" \
  "s_waitcnt lgkmcnt(3)\n" \
  "v_mfma_f32_16x16x32_bf16 v[188:191], v[36:39], v[136:139], v[188:191]\n" \
  "s_waitcnt lgkmcnt(1)\n" \
  "v_mfma_f32_16x16x32_bf16 v[188:191], v[100:103], v[140:143], v[188:191]\n" \
  "s_waitcnt lgkmcnt(0)\n" \
  "s_barrier\n" \
  "ds_read_b64_tr_b16 v[144:145], v11\n" \
  "ds_read_b64_tr_b16 v[146:147], v11 offset:512\n" \
  "ds_read_b64_tr_b16 v[148:149], v11 offset:1024\n" \
  "ds_read_b64_tr_b16 v[150:151], v11 offset:1536\n" \
  "v_mov_b32 v31, v31\n" \
  "s_nop 2\n" \
  "v_mov_b32 v31, v31\n" \
  "s_nop 2\n" \
  "v_mov_b32 v31, v31\n" \
  "s_nop 0\n" \
  "s_waitcnt lgkmcnt(2)\n" \
  "v_mfma_f32_16x16x32_bf16 v[188:191], v[104:107], v[144:147], v[188:191]\n" \
  "s_waitcnt lgkmcnt(0)\n" \
  "v_mfma_f32_16x16x32_bf16 v[188:191], v[108:111], v[148:151], v[188:191]\n" \
  "s_nop 0\n" \
  "ds_read_b64_tr_b16 v[34:35], v12\n" \
  "s_nop 1\n" \
  "v_mov_b32 v31, v31\n" \
  "s_nop 2\n" \
  "v_cndmask_b32 v152, v190, v188, s[20:21]\n" \
  "v_cndmask_b32 v153, v191, v189, s[20:21]\n" \
  "v_mul_f32 v152, 0x3dd53b94, v152\n" \
  "v_mul_f32 v153, 0x3dd53b94, v153\n" \
  "v_max_f32 v156, |v152|, |v153|\n" \
  "v_cmp_lt_f32 vcc, 0x42800000, v156\n" \
  "v_exp_f32 v154, v152\n" \
  "v_exp_f32 v155, v153\n" \
  "s_cbranch_vccnz LMX00_%=\n" \
  "v_cvt_pk_bf16_f32 v154, v154, v155\n" \
  "LMY00_%=:\n" \
  "buffer_store_dword v154, v13, s[36:39], s65 offen sc1\n" \
  "buffer_store_dword v154, v13, s[36:39], s91 offen sc1\n" \
  "buffer_store_dword v154, v13, s[36:39], s92 offen sc1\n" \
  "s_mov_b32 s67, s66\n" \
  "s_cmp_eq_u32 s64, 64\n" \
  "s_cbranch_scc1 LKE00_%=\n" \
  "LB00_%=:\n" \
  "s_barrier\n" \
  "buffer_load_dwordx4 v[116:119], v15, s[36:39], s65 offen offset:128 sc1\n" \
  "s_barrier\n" \
  "buffer_load_dwordx4 v[112:115], v15, s[36:39], s65 offen sc1\n" \
  "s_barrier\n" \
  "buffer_load_dwordx4 v[120:123], v16, s[36:39], s65 offen sc1\n" \
  "s_barrier\n" \
  "buffer_load_dwordx4 v[124:127], v16, s[36:39], s65 offen offset:512 sc1\n" \
  "s_barrier\n" \
  "buffer_load_dwordx4 v[128:131], v16, s[36:39], s65 offen offset:1024 sc1\n" \
  "s_waitcnt vmcnt(8)\n" \
  "s_barrier\n" \
  "buffer_load_dwordx4 v[132:135], v16, s[36:39], s65 offen offset:1536 sc1\n" \
  "ds_read_b128 v[40:43], v19\n" \
  "ds_read_b128 v[44:47], v19 offset:512\n" \
  "ds_read_b128 v[48:51], v19 offset:1024\n" \
  "ds_read_b128 v[52:55], v19 offset:1536\n" \
  "s_mov_b64 exec, s[46:47]\n" \
  "ds_read_b128 v[56:59], v20\n" \
  "ds_read_b128 v[60:63], v20 offset:128\n" \
  "ds_read_b128 v[64:67], v20 offset:256\n" \
  "ds_read_b128 v[68:71], v20 offset:384\n" \
  "s_mov_b64 exec, -1\n" \
  "s_nop 15\n" \
  "s_nop 1\n" \
  "buffer_load_dwordx4 v[136:139], v17, s[36:39], s65 offen sc1\n" \
  "buffer_load_dwordx4 v[140:143], v18, s[36:39], s65 offen sc1\n" \
  "s_barrier\n" \
  "s_waitcnt lgkmcnt(0)\n" \
  "buffer_store_dwordx2 v[34:35], v14, s[36:39], s69 offen sc1\n" \
  "s_add_u32 s91, s68, 16384\n" \
  "s_add_u32 s92, s68, 32768\n" \
  "s_add_u32 s93, s68, 49152\n" \
  "buffer_load_dwordx4 v[88:91], v1, s[36:39], s68 offen sc1\n" \
  "buffer_load_dwordx4 v[92:95], v1, s[36:39], s91 offen sc1\n" \
  "buffer_load_dwordx4 v[36:39], v1, s[36:39], s92 offen sc1\n" \
  "buffer_load_dwordx4 v[100:103], v1, s[36:39], s93 offen sc1\n" \
  "s_waitcnt lgkmcnt(0)\n" \
  "s_getreg_b32 s71, hwreg(HW_REG_IB_STS, 0, 4)\n" \
  "s_cmp_le_u32 s71, 11\n" \
  "v_mfma_f32_16x16x32_bf16 v[144:147], v[72:75], v[72:75], 0\n" \
  "s_cbranch_scc1 LHXS00_%=\n" \
  "s_getreg_b32 s71, hwreg(HW_REG_IB_STS, 0, 4)\n" \
  "s_cmp_le_u32 s71, 11\n" \
  "v_mfma_f32_16x16x32_bf16 v[144:147], v[72:75], v[72:75], 0\n" \
  "s_cbranch_scc1 LHXS00_%=\n" \
  "s_getreg_b32 s71, hwreg(HW_REG_IB_STS, 0, 4)\n" \
  "s_cmp_le_u32 s71, 11\n" \
  "v_mfma_f32_16x16x32_bf16 v[144:147], v[72:75], v[72:75], 0\n" \
  "s_cbranch_scc1 LHXS00_%=\n" \
  "s_getreg_b32 s71, hwreg(HW_REG_IB_STS, 0, 4)\n" \
  "s_cmp_le_u32 s71, 11\n" \
  "v_mfma_f32_16x16x32_bf16 v[144:147], v[72:75], v[72:75], 0\n" \
  "s_cbranch_scc1 LHXS00_%=\n" \
  "s_getreg_b32 s71, hwreg(HW_REG_IB_STS, 0, 4)\n" \
  "s_cmp_le_u32 s71, 11\n" \
  "v_mfma_f32_16x16x32_bf16 v[144:147], v[72:75], v[72:75], 0\n" \
  "s_cbranch_scc1 LHXS00_%=\n" \
  "s_getreg_b32 s71, hwreg(HW_REG_IB_STS, 0, 4)\n" \
  "s_cmp_le_u32 s71, 11\n" \
  "v_mfma_f32_16x16x32_bf16 v[144:147], v[72:75], v[72:75], 0\n" \
  "s_cbranch_scc1 LHXS00_%=\n" \
  "s_getreg_b32 s71, hwreg(HW_REG_IB_STS, 0, 4)\n" \
  "s_cmp_le_u32 s71, 11\n" \
  "v_mfma_f32_16x16x32_bf16 v[144:147], v[72:75], v[72:75], 0\n" \
  "s_cbranch_scc1 LHXS00_%=\n" \
  "s_getreg_b32 s71, hwreg(HW_REG_IB_STS, 0, 4)\n" \
  "s_cmp_le_u32 s71, 11\n" \
  "v_mfma_f32_16x16x32_bf16 v[144:147], v[72:75], v[72:75], 0\n" \
  "s_cbranch_scc1 LHXS00_%=\n" \
  "s_getreg_b32 s71, hwreg(HW_REG_IB_STS, 0, 4)\n" \
  "s_cmp_le_u32 s71, 11\n" \
  "v_mfma_f32_16x16x32_bf16 v[144:147], v[72:75], v[72:75], 0\n" \
  "s_cbranch_scc1 LHXS00_%=\n" \
  "s_getreg_b32 s71, hwreg(HW_REG_IB_STS, 0, 4)\n" \
  "s_cmp_le_u32 s71, 11\n" \
  "v_mfma_f32_16x16x32_bf16 v[144:147], v[72:75], v[72:75], 0\n" \
  "s_cbranch_scc1 LHXS00_%=\n" \
  "s_getreg_b32 s71, hwreg(HW_REG_IB_STS, 0, 4)\n" \
  "s_cmp_le_u32 s71, 11\n" \
  "v_mfma_f32_16x16x32_bf16 v[144:147], v[72:75], v[72:75], 0\n" \
  "s_cbranch_scc1 LHXS00_%=\n" \
  "s_getreg_b32 s71, hwreg(HW_REG_IB_STS, 0, 4)\n" \
  "s_cmp_le_u32 s71, 11\n" \
  "v_mfma_f32_16x16x32_bf16 v[144:147], v[72:75], v[72:75], 0\n" \
  "s_cbranch_scc1 LHXS00_%=\n" \
  "s_getreg_b32 s71, hwreg(HW_REG_IB_STS, 0, 4)\n" \
  "s_cmp_le_u32 s71, 11\n" \
  "v_mfma_f32_16x16x32_bf16 v[144:147], v[72:75], v[72:75], 0\n" \
  "s_cbranch_scc1 LHXS00_%=\n" \
  "s_getreg_b32 s71, hwreg(HW_REG_IB_STS, 0, 4)\n" \
  "s_cmp_le_u32 s71, 11\n" \
  "v_mfma_f32_16x16x32_bf16 v[144:147], v[72:75], v[72:75], 0\n" \
  "s_cbranch_scc1 LHXS00_%=\n" \
  "s_getreg_b32 s71, hwreg(HW_REG_IB_STS, 0, 4)\n" \
  "s_cmp_le_u32 s71, 11\n" \
  "v_mfma_f32_16x16x32_bf16 v[144:147], v[72:75], v[72:75], 0\n" \
  "s_cbranch_scc1 LHXS00_%=\n" \
  "s_getreg_b32 s71, hwreg(HW_REG_IB_STS, 0, 4)\n" \
  "s_cmp_le_u32 s71, 11\n" \
  "v_mfma_f32_16x16x32_bf16 v[144:147], v[72:75], v[72:75], 0\n" \
  "s_cbranch_scc1 LHXS00_%=\n" \
  "s_getreg_b32 s71, hwreg(HW_REG_IB_STS, 0, 4)\n" \
  "s_cmp_le_u32 s71, 11\n" \
  "v_mfma_f32_16x16x32_bf16 v[144:147], v[72:75], v[72:75], 0\n" \
  "s_cbranch_scc1 LHXS00_%=\n" \
  "s_getreg_b32 s71, hwreg(HW_REG_IB_STS, 0, 4)\n" \
  "s_cmp_le_u32 s71, 11\n" \
  "v_mfma_f32_16x16x32_bf16 v[144:147], v[72:75], v[72:75], 0\n" \
  "s_cbranch_scc1 LHXS00_%=\n" \
  "s_getreg_b32 s71, hwreg(HW_REG_IB_STS, 0, 4)\n" \
  "s_cmp_le_u32 s71, 11\n" \
  "v_mfma_f32_16x16x32_bf16 v[144:147], v[72:75], v[72:75], 0\n" \
  "s_cbranch_scc1 LHXS00_%=\n" \
  "s_getreg_b32 s71, hwreg(HW_REG_IB_STS, 0, 4)\n" \
  "s_cmp_le_u32 s71, 11\n" \
  "v_mfma_f32_16x16x32_bf16 v[144:147], v[72:75], v[72:75], 0\n" \
  "s_cbranch_scc1 LHXS00_%=\n" \
  "LHXS00_%=:\n" \
  "s_waitcnt vmcnt(11)\n" \
  "LS200_%=:\n" \
  "v_dot2_f32_bf16 v176, v112, s22, 0\n" \
  "v_dot2_f32_bf16 v177, v112, s23, 0\n" \
  "v_dot2_f32_bf16 v178, v113, s22, 0\n" \
  "v_dot2_f32_bf16 v179, v113, s23, 0\n" \
  "v_dot2_f32_bf16 v180, v114, s22, 0\n" \
  "v_dot2_f32_bf16 v181, v114, s23, 0\n" \
  "v_dot2_f32_bf16 v182, v115, s22, 0\n" \
  "v_dot2_f32_bf16 v183, v115, s23, 0\n" \
  "v_dot2_f32_bf16 v176, v116, s22, v176\n" \
  "v_dot2_f32_bf16 v177, v116, s23, v177\n" \
  "v_dot2_f32_bf16 v178, v117, s22, v178\n" \
  "v_dot2_f32_bf16 v179, v117, s23, v179\n" \
  "v_dot2_f32_bf16 v180, v118, s22, v180\n" \
  "v_dot2_f32_bf16 v181, v118, s23, v181\n" \
  "v_dot2_f32_bf16 v182, v119, s22, v182\n" \
  "v_dot2_f32_bf16 v183, v119, s23, v183\n" \
  "v_exp_f32 v176, v176\n" \
  "v_exp_f32 v177, v177\n" \
  "v_exp_f32 v178, v178\n" \
  "v_exp_f32 v179, v179\n" \
  "s_waitcnt vmcnt(10)\n" \
  "v_mfma_f32_16x16x32_bf16 v[160:163], v[56:59], v[120:123], 0\n" \
  "v_exp_f32 v180, v180\n" \
  "v_mfma_f32_16x16x32_bf16 v[144:147], v[40:43], v[120:123], 0\n" \
  "v_exp_f32 v181, v181\n" \
  "v_exp_f32 v182, v182\n" \
  "v_exp_f32 v183, v183\n" \
  "v_cvt_pk_bf16_f32 v184, v176, v177\n" \
  "v_cvt_pk_bf16_f32 v185, v178, v179\n" \
  "v_cvt_pk_bf16_f32 v186, v180, v181\n" \
  "v_cvt_pk_bf16_f32 v187, v182, v183\n" \
  "s_nop 1\n" \
  "v_mfma_f32_16x16x32_bf16 v[160:163], v[72:75], v[184:187], v[160:163]\n" \
  "v_mfma_f32_16x16x32_bf16 v[164:167], v[76:79], v[184:187], 0\n" \
  "s_waitcnt vmcnt(9)\n" \
  "v_mfma_f32_16x16x32_bf16 v[164:167], v[60:63], v[124:127], v[164:167]\n" \
  "v_rcp_f32 v188, v162\n" \
  "v_mfma_f32_16x16x32_bf16 v[148:151], v[44:47], v[124:127], 0\n" \
  "v_mul_f32 v144, v188, v144\n" \
  "v_mul_f32 v145, v188, v145\n" \
  "v_mfma_f32_16x16x32_bf16 v[168:171], v[80:83], v[184:187], 0\n" \
  "s_waitcnt vmcnt(8)\n" \
  "v_rcp_f32 v189, v166\n" \
  "v_mfma_f32_16x16x32_bf16 v[168:171], v[64:67], v[128:131], v[168:171]\n" \
  "v_mul_f32 v146, v188, v146\n" \
  "v_mul_f32 v147, v188, v147\n" \
  "v_mfma_f32_16x16x32_bf16 v[152:155], v[48:51], v[128:131], 0\n" \
  "v_mul_f32 v160, v188, v160\n" \
  "v_mul_f32 v161, v188, v161\n" \
  "v_mfma_f32_16x16x32_bf16 v[172:175], v[84:87], v[184:187], 0\n" \
  "s_waitcnt vmcnt(7)\n" \
  "v_rcp_f32 v190, v170\n" \
  "v_mfma_f32_16x16x32_bf16 v[172:175], v[68:71], v[132:135], v[172:175]\n" \
  "v_cndmask_b32 v167, v188, v189, s[28:29]\n" \
  "v_cndmask_b32 v167, v167, v190, s[26:27]\n" \
  "v_mfma_f32_16x16x32_bf16 v[156:159], v[52:55], v[132:135], 0\n" \
  "v_fmac_f32 v144, v189, v148\n" \
  "v_fmac_f32 v145, v189, v149\n" \
  "v_fmac_f32 v146, v189, v150\n" \
  "v_fmac_f32 v147, v189, v151\n" \
  "v_rcp_f32 v191, v174\n" \
  "v_fmac_f32 v160, v189, v164\n" \
  "v_cndmask_b32 v167, v167, v191, s[24:25]\n" \
  "v_pk_mul_f32 v[176:177], v[176:177], v[166:167] op_sel:[0,1] op_sel_hi:[1,1]\n" \
  "v_pk_mul_f32 v[178:179], v[178:179], v[166:167] op_sel:[0,1] op_sel_hi:[1,1]\n" \
  "v_pk_mul_f32 v[180:181], v[180:181], v[166:167] op_sel:[0,1] op_sel_hi:[1,1]\n" \
  "v_pk_mul_f32 v[182:183], v[182:183], v[166:167] op_sel:[0,1] op_sel_hi:[1,1]\n" \
  "v_cmp_nlt_f32 vcc, 0x0d800000, v167\n" \
  "v_cvt_pk_bf16_f32 v184, v176, v177\n" \
  "v_cvt_pk_bf16_f32 v185, v178, v179\n" \
  "v_cvt_pk_bf16_f32 v186, v180, v181\n" \
  "v_cvt_pk_bf16_f32 v187, v182, v183\n" \
  "v_fmac_f32 v161, v189, v165\n" \
  "v_pk_fma_f32 v[160:161], v[168:169], v[190:191], v[160:161] op_sel:[0,0,0] op_sel_hi:[1,0,1]\n" \
  "v_pk_fma_f32 v[160:161], v[172:173], v[190:191], v[160:161] op_sel:[0,1,0] op_sel_hi:[1,1,1]\n" \
  "v_pk_fma_f32 v[144:145], v[152:153], v[190:191], v[144:145] op_sel:[0,0,0] op_sel_hi:[1,0,1]\n" \
  "v_pk_fma_f32 v[146:147], v[154:155], v[190:191], v[146:147] op_sel:[0,0,0] op_sel_hi:[1,0,1]\n" \
  "v_pk_fma_f32 v[144:145], v[156:157], v[190:191], v[144:145] op_sel:[0,1,0] op_sel_hi:[1,1,1]\n" \
  "v_pk_fma_f32 v[146:147], v[158:159], v[190:191], v[146:147] op_sel:[0,1,0] op_sel_hi:[1,1,1]\n" \
  "s_waitcnt vmcnt(5)\n" \
  "v_mfma_f32_16x16x32_bf16 v[160:163], v[140:143], v[184:187], v[160:163]\n" \
  "s_mov_b64 s[80:81], vcc\n" \
  "v_mfma_f32_16x16x32_bf16 v[144:147], v[136:139], v[184:187], v[144:147]\n" \
  "s_mov_b64 exec, s[30:31]\n" \
  "s_nop 2\n" \
  "ds_write_b64 v22, v[160:161]\n" \
  "v_cmp_u_f32 s[78:79], v160, v161\n" \
  "s_nop 1\n" \
  "ds_write2_b64 v21, v[144:145], v[146:147] offset1:16\n" \
  "v_add_f32 v188, v144, v145\n" \
  "v_add_f32 v189, v146, v147\n" \
  "v_cmp_u_f32 vcc, v188, v189\n" \
  "s_or_b64 s[80:81], s[80:81], s[78:79]\n" \
  "s_or_b64 vcc, vcc, s[80:81]\n" \
  "s_cbranch_vccnz LSF00_%=\n" \
  "LSOK00_%=:\n" \
  "s_mov_b64 exec, -1\n" \
  "s_waitcnt lgkmcnt(0)\n" \
  "s_barrier\n" \
  "ds_read_b32 v188, v33\n" \
  "s_waitcnt lgkmcnt(0)\n" \
  "v_cmp_ne_u32 vcc, 0, v188\n" \
  "s_cbranch_vccnz LSR00_%=\n" \
  "s_nop 15\n" \
  "s_nop 3\n" \
  "buffer_load_dwordx2 v[104:105], v2, s[36:39], s68 offen sc1\n" \
  "buffer_load_dwordx2 v[106:107], v2, s[36:39], s91 offen sc1\n" \
  "buffer_load_dwordx2 v[108:109], v2, s[36:39], s92 offen sc1\n" \
  "buffer_load_dwordx2 v[110:111], v2, s[36:39], s93 offen sc1\n" \
  "LNL00_%=:\n" \
  "s_waitcnt lgkmcnt(0)\n" \
  "s_cmp_eq_u32 s73, 1\n" \
  "s_cbranch_scc1 LOOP00_%=\n" \
  "s_branch LDONE_%=\n" \
  "LMF00_%=:\n" \
  "v_mbcnt_lo_u32_b32 v130, -1, 0\n" \
  "v_mbcnt_hi_u32_b32 v130, -1, v130\n" \
  "v_lshlrev_b32 v130, 4, v130\n" \
  "v_mov_b32 v128, 0\n" \
  "ds_read_b128 v[132:135], v130 offset:0\n" \
  "ds_read_b128 v[136:139], v130 offset:1024\n" \
  "ds_read_b128 v[140:143], v130 offset:2048\n" \
  "ds_read_b128 v[144:147], v130 offset:3072\n" \
  "ds_read_b128 v[148:151], v130 offset:4096\n" \
  "s_waitcnt lgkmcnt(0)\n" \
  "v_max3_u32 v128, v128, v132, v133\n" \
  "v_max3_u32 v128, v128, v134, v135\n" \
  "v_max3_u32 v128, v128, v136, v137\n" \
  "v_max3_u32 v128, v128, v138, v139\n" \
  "v_max3_u32 v128, v128, v140, v141\n" \
  "v_max3_u32 v128, v128, v142, v143\n" \
  "v_max3_u32 v128, v128, v144, v145\n" \
  "v_max3_u32 v128, v128, v146, v147\n" \
  "v_max3_u32 v128, v128, v148, v149\n" \
  "v_max3_u32 v128, v128, v150, v151\n" \
  "ds_read_b128 v[132:135], v130 offset:5120\n" \
  "ds_read_b128 v[136:139], v130 offset:6144\n" \
  "ds_read_b128 v[140:143], v130 offset:7168\n" \
  "ds_read_b128 v[144:147], v130 offset:8192\n" \
  "ds_read_b128 v[148:151], v130 offset:9216\n" \
  "s_waitcnt lgkmcnt(0)\n" \
  "v_max3_u32 v128, v128, v132, v133\n" \
  "v_max3_u32 v128, v128, v134, v135\n" \
  "v_max3_u32 v128, v128, v136, v137\n" \
  "v_max3_u32 v128, v128, v138, v139\n" \
  "v_max3_u32 v128, v128, v140, v141\n" \
  "v_max3_u32 v128, v128, v142, v143\n" \
  "v_max3_u32 v128, v128, v144, v145\n" \
  "v_max3_u32 v128, v128, v146, v147\n" \
  "v_max3_u32 v128, v128, v148, v149\n" \
  "v_max3_u32 v128, v128, v150, v151\n" \
  "v_cmp_eq_u32 vcc, -1, v128\n" \
  "s_cbranch_vccz LMOK00_%=\n" \
  "s_cmp_gt_u32 s58, 4000\n" \
  "s_cbranch_scc1 LMOK00_%=\n" \
  "s_add_u32 s58, s58, 1\n" \
  "s_sub_u32 s64, s64, 1\n" \
  "s_mov_b32 s66, s65\n" \
  "s_waitcnt vmcnt(0) lgkmcnt(0)\n" \
  "s_barrier\n" \
  "s_branch LOOP00_%=\n" \
  "LKE00_%=:\n" \
  "s_waitcnt lgkmcnt(0)\n" \
  "buffer_store_dwordx2 v[34:35], v14, s[36:39], s69 offen sc1\n" \
  "s_branch LDONE_%=\n" \
  "LMX00_%=:\n" \
  "v_cvt_pk_bf16_f32 v155, v152, v153\n" \
  "v_pk_min_u16 v155, v155, -2 op_sel_hi:[1,0]\n" \
  "s_add_u32 s71, s65, 245760\n" \
  "buffer_store_dword v155, v13, s[36:39], s71 offen sc1\n" \
  "v_mov_b32 v154, 0x7fd57fd5\n" \
  "s_branch LMY00_%=\n" \
  "LSF00_%=:\n" \
  "s_getpc_b64 s[78:79]\n" \
  "s_branch LSF_%=\n" \
  "s_branch LSOK00_%=\n" \
  "LSR00_%=:\n" \
  "v_mov_b32 v188, 0\n" \
  "s_add_u32 s59, s59, 1\n" \
  "s_barrier\n" \
  "ds_write_b32 v33, v188\n" \
  "s_waitcnt lgkmcnt(0)\n" \
  "s_branch LB00_%=\n" \
  "LSTART01_%=:\n" \
  "LOOP01_%=:\n" \
  "v_readlane_b32 s70, v27, s64\n" \
  "s_mov_b32 s65, s66\n" \
  "s_xor_b32 s69, s66, 0x2000\n" \
  "s_add_u32 s64, s64, 1\n" \
  "v_mov_b32 v176, 0\n" \
  "v_mov_b32 v177, 0\n" \
  "v_mov_b32 v178, 0\n" \
  "v_mov_b32 v179, 0\n" \
  "v_mov_b32 v180, 0\n" \
  "v_mov_b32 v181, 0\n" \
  "v_mov_b32 v182, 0\n" \
  "v_mov_b32 v183, 0\n" \
  "v_readlane_b32 s66, v25, s64\n" \
  "v_readlane_b32 s68, v26, s64\n" \
  "s_add_u32 s91, s65, 81920\n" \
  "s_add_u32 s92, s65, 163840\n" \
  "s_mov_b32 m0, 28672\n" \
  "s_add_u32 s72, s70, 4096\n" \
  "s_barrier\n" \
  "buffer_load_dwordx4 v3, s[36:39], s70 offen sc1 lds\n" \
  "buffer_load_dwordx4 v3, s[36:39], s70 offen offset:1024 sc1 lds\n" \
  "buffer_load_dwordx4 v3, s[36:39], s70 offen offset:2048 sc1 lds\n" \
  "buffer_load_dwordx4 v3, s[36:39], s70 offen offset:3072 sc1 lds\n" \
  "LM01_%=:\n" \
  "s_barrier\n" \
  "ds_read_b128 v[112:115], v5\n" \
  "ds_read_b128 v[116:119], v5 offset:512\n" \
  "s_barrier\n" \
  "ds_read_b128 v[120:123], v5 offset:1024\n" \
  "ds_read_b128 v[124:127], v5 offset:1536\n" \
  "s_waitcnt lgkmcnt(3)\n" \
  "v_smfmac_f32_16x16x64_bf16 v[176:179], v[112:115], a[0:7], v0\n" \
  "v_smfmac_f32_16x16x64_bf16 v[180:183], v[112:115], a[160:167], v0\n" \
  "s_waitcnt lgkmcnt(2)\n" \
  "v_smfmac_f32_16x16x64_bf16 v[176:179], v[116:119], a[8:15], v0\n" \
  "s_barrier\n" \
  "ds_read_b128 v[128:131], v5 offset:2048\n" \
  "v_smfmac_f32_16x16x64_bf16 v[180:183], v[116:119], a[168:175], v0\n" \
  "s_waitcnt lgkmcnt(2)\n" \
  "ds_read_b128 v[132:135], v5 offset:2560\n" \
  "v_smfmac_f32_16x16x64_bf16 v[176:179], v[120:123], a[16:23], v0\n" \
  "ds_read_b128 v[136:139], v5 offset:3072\n" \
  "v_smfmac_f32_16x16x64_bf16 v[180:183], v[120:123], a[176:183], v0\n" \
  "s_waitcnt lgkmcnt(3)\n" \
  "ds_read_b128 v[140:143], v5 offset:3584\n" \
  "v_smfmac_f32_16x16x64_bf16 v[176:179], v[124:127], a[24:31], v0\n" \
  "s_barrier\n" \
  "s_waitcnt vmcnt(4)\n" \
  "ds_read_b128 v[144:147], v5 offset:4096\n" \
  "v_smfmac_f32_16x16x64_bf16 v[180:183], v[124:127], a[184:191], v0\n" \
  "s_mov_b32 m0, 32768\n" \
  "s_waitcnt lgkmcnt(4)\n" \
  "ds_read_b128 v[148:151], v5 offset:4608\n" \
  "v_smfmac_f32_16x16x64_bf16 v[176:179], v[128:131], a[32:39], v0\n" \
  "ds_read_b128 v[152:155], v5 offset:5120\n" \
  "v_smfmac_f32_16x16x64_bf16 v[180:183], v[128:131], a[192:199], v0\n" \
  "ds_read_b128 v[156:159], v5 offset:5632\n" \
  "buffer_load_dwordx4 v3, s[36:39], s72 offen sc1 lds\n" \
  "s_waitcnt lgkmcnt(6)\n" \
  "v_smfmac_f32_16x16x64_bf16 v[176:179], v[132:135], a[40:47], v0\n" \
  "s_barrier\n" \
  "ds_read_b128 v[160:163], v5 offset:6144\n" \
  "v_smfmac_f32_16x16x64_bf16 v[180:183], v[132:135], a[200:207], v0\n" \
  "ds_read_b128 v[164:167], v5 offset:6656\n" \
  "buffer_load_dwordx4 v3, s[36:39], s72 offen offset:1024 sc1 lds\n" \
  "s_waitcnt lgkmcnt(7)\n" \
  "v_smfmac_f32_16x16x64_bf16 v[176:179], v[136:139], a[48:55], v0\n" \
  "ds_read_b128 v[168:171], v5 offset:7168\n" \
  "v_smfmac_f32_16x16x64_bf16 v[180:183], v[136:139], a[208:215], v0\n" \
  "ds_read_b128 v[172:175], v5 offset:7680\n" \
  "buffer_load_dwordx4 v3, s[36:39], s72 offen offset:2048 sc1 lds\n" \
  "s_waitcnt lgkmcnt(8)\n" \
  "v_smfmac_f32_16x16x64_bf16 v[176:179], v[140:143], a[56:63], v0\n" \
  "s_barrier\n" \
  "ds_read2_b32 v[112:113], v6 offset0:0 offset1:32\n" \
  "v_smfmac_f32_16x16x64_bf16 v[180:183], v[140:143], a[216:223], v0\n" \
  "ds_read2_b32 v[114:115], v6 offset0:64 offset1:96\n" \
  "buffer_load_dwordx4 v3, s[36:39], s72 offen offset:3072 sc1 lds\n" \
  "s_waitcnt lgkmcnt(9)\n" \
  "v_smfmac_f32_16x16x64_bf16 v[176:179], v[144:147], a[64:71], v0\n" \
  "s_waitcnt lgkmcnt(8)\n" \
  "ds_read2_b32 v[116:117], v6 offset0:128 offset1:160\n" \
  "v_smfmac_f32_16x16x64_bf16 v[176:179], v[148:151], a[72:79], v0\n" \
  "s_waitcnt lgkmcnt(8)\n" \
  "ds_read2_b32 v[118:119], v6 offset0:192 offset1:224\n" \
  "v_smfmac_f32_16x16x64_bf16 v[176:179], v[152:155], a[80:87], v0\n" \
  "s_waitcnt lgkmcnt(8)\n" \
  "ds_read2_b32 v[120:121], v7 offset0:0 offset1:32\n" \
  "v_smfmac_f32_16x16x64_bf16 v[176:179], v[156:159], a[88:95], v0\n" \
  "s_waitcnt lgkmcnt(8)\n" \
  "ds_read2_b32 v[122:123], v7 offset0:64 offset1:96\n" \
  "v_smfmac_f32_16x16x64_bf16 v[176:179], v[160:163], a[96:103], v0\n" \
  "s_waitcnt lgkmcnt(8)\n" \
  "ds_read2_b32 v[124:125], v7 offset0:128 offset1:160\n" \
  "v_smfmac_f32_16x16x64_bf16 v[176:179], v[164:167], a[104:111], v0\n" \
  "s_waitcnt lgkmcnt(8)\n" \
  "ds_read2_b32 v[126:127], v7 offset0:192 offset1:224\n" \
  "v_smfmac_f32_16x16x64_bf16 v[176:179], v[168:171], a[112:119], v0\n" \
  "s_waitcnt lgkmcnt(8)\n" \
  "v_smfmac_f32_16x16x64_bf16 v[176:179], v[172:175], a[120:127], v0\n" \
  "s_waitcnt lgkmcnt(6)\n" \
  "v_smfmac_f32_16x16x64_bf16 v[176:179], v[112:115], a[128:135], v0\n" \
  "s_waitcnt lgkmcnt(4)\n" \
  "v_smfmac_f32_16x16x64_bf16 v[176:179], v[116:119], a[136:143], v0\n" \
  "s_waitcnt lgkmcnt(2)\n" \
  "v_smfmac_f32_16x16x64_bf16 v[176:179], v[120:123], a[144:151], v0\n" \
  "s_waitcnt lgkmcnt(0)\n" \
  "v_smfmac_f32_16x16x64_bf16 v[176:179], v[124:127], a[152:159], v0\n" \
  "v_smfmac_f32_16x16x64_bf16 v[180:183], v[144:147], a[224:231], v0\n" \
  "v_smfmac_f32_16x16x64_bf16 v[180:183], v[148:151], a[232:239], v0\n" \
  "v_cvt_pk_bf16_f32 v184, v176, v177\n" \
  "v_smfmac_f32_16x16x64_bf16 v[180:183], v[152:155], a[240:247], v0\n" \
  "v_cvt_pk_bf16_f32 v185, v178, v179\n" \
  "v_smfmac_f32_16x16x64_bf16 v[180:183], v[156:159], a[248:255], v0\n" \
  "ds_write_b64 v8, v[184:185]\n" \
  "v_add_f32 v186, v176, v177\n" \
  "v_smfmac_f32_16x16x64_bf16 v[180:183], v[160:163], v[192:199], v0\n" \
  "v_add_f32 v187, v178, v179\n" \
  "v_smfmac_f32_16x16x64_bf16 v[180:183], v[164:167], v[200:207], v0\n" \
  "v_cmp_u_f32 vcc, v186, v187\n" \
  "v_smfmac_f32_16x16x64_bf16 v[180:183], v[168:171], v[208:215], v0\n" \
  "v_smfmac_f32_16x16x64_bf16 v[180:183], v[172:175], v[216:223], v0\n" \
  "s_cbranch_vccnz LMF01_%=\n" \
  "LMOK01_%=:\n" \
  "v_smfmac_f32_16x16x64_bf16 v[180:183], v[112:115], v[224:231], v0\n" \
  "s_waitcnt lgkmcnt(0)\n" \
  "s_barrier\n" \
  "ds_read_b64_tr_b16 v[128:129], v10\n" \
  "v_smfmac_f32_16x16x64_bf16 v[180:183], v[116:119], v[232:239], v0\n" \
  "ds_read_b64_tr_b16 v[130:131], v10 offset:32\n" \
  "ds_read_b64_tr_b16 v[132:133], v10 offset:512\n" \
  "v_smfmac_f32_16x16x64_bf16 v[180:183], v[120:123], v[240:247], v0\n" \
  "ds_read_b64_tr_b16 v[134:135], v10 offset:544\n" \
  "ds_read_b64_tr_b16 v[136:137], v10 offset:1024\n" \
  "v_smfmac_f32_16x16x64_bf16 v[180:183], v[124:127], v[248:255], v0\n" \
  "s_nop 0\n" \
  "ds_read_b64_tr_b16 v[138:139], v10 offset:1056\n" \
  "ds_read_b64_tr_b16 v[140:141], v10 offset:1536\n" \
  "ds_read_b64_tr_b16 v[142:143], v10 offset:1568\n" \
  "v_mov_b32 v31, v31\n" \
  "s_nop 2\n" \
  "v_cvt_pk_bf16_f32 v186, v180, v181\n" \
  "v_cvt_pk_bf16_f32 v187, v182, v183\n" \
  "ds_write_b64 v9, v[186:187]\n" \
  "s_waitcnt lgkmcnt(7)\n" \
  "v_mfma_f32_16x16x32_bf16 v[188:191], v[88:91], v[128:131], 0\n" \
  "s_waitcnt lgkmcnt(5)\n" \
  "v_mfma_f32_16x16x32_bf16 v[188:191], v[92:95], v[132:135], v[188:191]\n" \
  "s_waitcnt lgkmcnt(3)\n" \
  "v_mfma_f32_16x16x32_bf16 v[188:191], v[36:39], v[136:139], v[188:191]\n" \
  "s_waitcnt lgkmcnt(1)\n" \
  "v_mfma_f32_16x16x32_bf16 v[188:191], v[100:103], v[140:143], v[188:191]\n" \
  "s_waitcnt lgkmcnt(0)\n" \
  "s_barrier\n" \
  "ds_read_b64_tr_b16 v[144:145], v11\n" \
  "ds_read_b64_tr_b16 v[146:147], v11 offset:512\n" \
  "ds_read_b64_tr_b16 v[148:149], v11 offset:1024\n" \
  "ds_read_b64_tr_b16 v[150:151], v11 offset:1536\n" \
  "v_mov_b32 v31, v31\n" \
  "s_nop 2\n" \
  "v_mov_b32 v31, v31\n" \
  "s_nop 2\n" \
  "v_mov_b32 v31, v31\n" \
  "s_nop 0\n" \
  "s_waitcnt lgkmcnt(2)\n" \
  "v_mfma_f32_16x16x32_bf16 v[188:191], v[104:107], v[144:147], v[188:191]\n" \
  "s_waitcnt lgkmcnt(0)\n" \
  "v_mfma_f32_16x16x32_bf16 v[188:191], v[108:111], v[148:151], v[188:191]\n" \
  "s_nop 0\n" \
  "ds_read_b64_tr_b16 v[34:35], v12\n" \
  "s_nop 1\n" \
  "v_mov_b32 v31, v31\n" \
  "s_nop 2\n" \
  "v_cndmask_b32 v152, v190, v188, s[20:21]\n" \
  "v_cndmask_b32 v153, v191, v189, s[20:21]\n" \
  "v_mul_f32 v152, 0x3dd53b94, v152\n" \
  "v_mul_f32 v153, 0x3dd53b94, v153\n" \
  "v_max_f32 v156, |v152|, |v153|\n" \
  "v_cmp_lt_f32 vcc, 0x42800000, v156\n" \
  "v_exp_f32 v154, v152\n" \
  "v_exp_f32 v155, v153\n" \
  "s_cbranch_vccnz LMX01_%=\n" \
  "v_cvt_pk_bf16_f32 v154, v154, v155\n" \
  "LMY01_%=:\n" \
  "buffer_store_dword v154, v13, s[36:39], s65 offen sc1\n" \
  "buffer_store_dword v154, v13, s[36:39], s91 offen sc1\n" \
  "buffer_store_dword v154, v13, s[36:39], s92 offen sc1\n" \
  "s_mov_b32 s67, s66\n" \
  "s_cmp_eq_u32 s64, 64\n" \
  "s_cbranch_scc1 LKE01_%=\n" \
  "LB01_%=:\n" \
  "s_barrier\n" \
  "buffer_load_dwordx4 v[116:119], v15, s[36:39], s65 offen offset:128 sc1\n" \
  "s_barrier\n" \
  "buffer_load_dwordx4 v[112:115], v15, s[36:39], s65 offen sc1\n" \
  "s_barrier\n" \
  "buffer_load_dwordx4 v[120:123], v16, s[36:39], s65 offen sc1\n" \
  "s_barrier\n" \
  "buffer_load_dwordx4 v[124:127], v16, s[36:39], s65 offen offset:512 sc1\n" \
  "s_barrier\n" \
  "buffer_load_dwordx4 v[128:131], v16, s[36:39], s65 offen offset:1024 sc1\n" \
  "s_waitcnt vmcnt(8)\n" \
  "s_barrier\n" \
  "buffer_load_dwordx4 v[132:135], v16, s[36:39], s65 offen offset:1536 sc1\n" \
  "ds_read_b128 v[40:43], v19\n" \
  "ds_read_b128 v[44:47], v19 offset:512\n" \
  "ds_read_b128 v[48:51], v19 offset:1024\n" \
  "ds_read_b128 v[52:55], v19 offset:1536\n" \
  "s_mov_b64 exec, s[46:47]\n" \
  "ds_read_b128 v[56:59], v20\n" \
  "ds_read_b128 v[60:63], v20 offset:128\n" \
  "ds_read_b128 v[64:67], v20 offset:256\n" \
  "ds_read_b128 v[68:71], v20 offset:384\n" \
  "s_mov_b64 exec, -1\n" \
  "s_nop 15\n" \
  "s_nop 1\n" \
  "buffer_load_dwordx4 v[136:139], v17, s[36:39], s65 offen sc1\n" \
  "buffer_load_dwordx4 v[140:143], v18, s[36:39], s65 offen sc1\n" \
  "s_barrier\n" \
  "s_waitcnt lgkmcnt(0)\n" \
  "buffer_store_dwordx2 v[34:35], v14, s[36:39], s69 offen sc1\n" \
  "s_add_u32 s91, s68, 16384\n" \
  "s_add_u32 s92, s68, 32768\n" \
  "s_add_u32 s93, s68, 49152\n" \
  "buffer_load_dwordx4 v[88:91], v1, s[36:39], s68 offen sc1\n" \
  "buffer_load_dwordx4 v[92:95], v1, s[36:39], s91 offen sc1\n" \
  "buffer_load_dwordx4 v[36:39], v1, s[36:39], s92 offen sc1\n" \
  "buffer_load_dwordx4 v[100:103], v1, s[36:39], s93 offen sc1\n" \
  "s_waitcnt lgkmcnt(0)\n" \
  "s_getreg_b32 s71, hwreg(HW_REG_IB_STS, 0, 4)\n" \
  "s_cmp_le_u32 s71, 11\n" \
  "v_mfma_f32_16x16x32_bf16 v[144:147], v[72:75], v[72:75], 0\n" \
  "s_cbranch_scc1 LHXS01_%=\n" \
  "s_getreg_b32 s71, hwreg(HW_REG_IB_STS, 0, 4)\n" \
  "s_cmp_le_u32 s71, 11\n" \
  "v_mfma_f32_16x16x32_bf16 v[144:147], v[72:75], v[72:75], 0\n" \
  "s_cbranch_scc1 LHXS01_%=\n" \
  "s_getreg_b32 s71, hwreg(HW_REG_IB_STS, 0, 4)\n" \
  "s_cmp_le_u32 s71, 11\n" \
  "v_mfma_f32_16x16x32_bf16 v[144:147], v[72:75], v[72:75], 0\n" \
  "s_cbranch_scc1 LHXS01_%=\n" \
  "s_getreg_b32 s71, hwreg(HW_REG_IB_STS, 0, 4)\n" \
  "s_cmp_le_u32 s71, 11\n" \
  "v_mfma_f32_16x16x32_bf16 v[144:147], v[72:75], v[72:75], 0\n" \
  "s_cbranch_scc1 LHXS01_%=\n" \
  "s_getreg_b32 s71, hwreg(HW_REG_IB_STS, 0, 4)\n" \
  "s_cmp_le_u32 s71, 11\n" \
  "v_mfma_f32_16x16x32_bf16 v[144:147], v[72:75], v[72:75], 0\n" \
  "s_cbranch_scc1 LHXS01_%=\n" \
  "s_getreg_b32 s71, hwreg(HW_REG_IB_STS, 0, 4)\n" \
  "s_cmp_le_u32 s71, 11\n" \
  "v_mfma_f32_16x16x32_bf16 v[144:147], v[72:75], v[72:75], 0\n" \
  "s_cbranch_scc1 LHXS01_%=\n" \
  "s_getreg_b32 s71, hwreg(HW_REG_IB_STS, 0, 4)\n" \
  "s_cmp_le_u32 s71, 11\n" \
  "v_mfma_f32_16x16x32_bf16 v[144:147], v[72:75], v[72:75], 0\n" \
  "s_cbranch_scc1 LHXS01_%=\n" \
  "s_getreg_b32 s71, hwreg(HW_REG_IB_STS, 0, 4)\n" \
  "s_cmp_le_u32 s71, 11\n" \
  "v_mfma_f32_16x16x32_bf16 v[144:147], v[72:75], v[72:75], 0\n" \
  "s_cbranch_scc1 LHXS01_%=\n" \
  "s_getreg_b32 s71, hwreg(HW_REG_IB_STS, 0, 4)\n" \
  "s_cmp_le_u32 s71, 11\n" \
  "v_mfma_f32_16x16x32_bf16 v[144:147], v[72:75], v[72:75], 0\n" \
  "s_cbranch_scc1 LHXS01_%=\n" \
  "s_getreg_b32 s71, hwreg(HW_REG_IB_STS, 0, 4)\n" \
  "s_cmp_le_u32 s71, 11\n" \
  "v_mfma_f32_16x16x32_bf16 v[144:147], v[72:75], v[72:75], 0\n" \
  "s_cbranch_scc1 LHXS01_%=\n" \
  "s_getreg_b32 s71, hwreg(HW_REG_IB_STS, 0, 4)\n" \
  "s_cmp_le_u32 s71, 11\n" \
  "v_mfma_f32_16x16x32_bf16 v[144:147], v[72:75], v[72:75], 0\n" \
  "s_cbranch_scc1 LHXS01_%=\n" \
  "s_getreg_b32 s71, hwreg(HW_REG_IB_STS, 0, 4)\n" \
  "s_cmp_le_u32 s71, 11\n" \
  "v_mfma_f32_16x16x32_bf16 v[144:147], v[72:75], v[72:75], 0\n" \
  "s_cbranch_scc1 LHXS01_%=\n" \
  "s_getreg_b32 s71, hwreg(HW_REG_IB_STS, 0, 4)\n" \
  "s_cmp_le_u32 s71, 11\n" \
  "v_mfma_f32_16x16x32_bf16 v[144:147], v[72:75], v[72:75], 0\n" \
  "s_cbranch_scc1 LHXS01_%=\n" \
  "s_getreg_b32 s71, hwreg(HW_REG_IB_STS, 0, 4)\n" \
  "s_cmp_le_u32 s71, 11\n" \
  "v_mfma_f32_16x16x32_bf16 v[144:147], v[72:75], v[72:75], 0\n" \
  "s_cbranch_scc1 LHXS01_%=\n" \
  "s_getreg_b32 s71, hwreg(HW_REG_IB_STS, 0, 4)\n" \
  "s_cmp_le_u32 s71, 11\n" \
  "v_mfma_f32_16x16x32_bf16 v[144:147], v[72:75], v[72:75], 0\n" \
  "s_cbranch_scc1 LHXS01_%=\n" \
  "s_getreg_b32 s71, hwreg(HW_REG_IB_STS, 0, 4)\n" \
  "s_cmp_le_u32 s71, 11\n" \
  "v_mfma_f32_16x16x32_bf16 v[144:147], v[72:75], v[72:75], 0\n" \
  "s_cbranch_scc1 LHXS01_%=\n" \
  "s_getreg_b32 s71, hwreg(HW_REG_IB_STS, 0, 4)\n" \
  "s_cmp_le_u32 s71, 11\n" \
  "v_mfma_f32_16x16x32_bf16 v[144:147], v[72:75], v[72:75], 0\n" \
  "s_cbranch_scc1 LHXS01_%=\n" \
  "s_getreg_b32 s71, hwreg(HW_REG_IB_STS, 0, 4)\n" \
  "s_cmp_le_u32 s71, 11\n" \
  "v_mfma_f32_16x16x32_bf16 v[144:147], v[72:75], v[72:75], 0\n" \
  "s_cbranch_scc1 LHXS01_%=\n" \
  "s_getreg_b32 s71, hwreg(HW_REG_IB_STS, 0, 4)\n" \
  "s_cmp_le_u32 s71, 11\n" \
  "v_mfma_f32_16x16x32_bf16 v[144:147], v[72:75], v[72:75], 0\n" \
  "s_cbranch_scc1 LHXS01_%=\n" \
  "s_getreg_b32 s71, hwreg(HW_REG_IB_STS, 0, 4)\n" \
  "s_cmp_le_u32 s71, 11\n" \
  "v_mfma_f32_16x16x32_bf16 v[144:147], v[72:75], v[72:75], 0\n" \
  "s_cbranch_scc1 LHXS01_%=\n" \
  "LHXS01_%=:\n" \
  "s_waitcnt vmcnt(11)\n" \
  "LS201_%=:\n" \
  "v_dot2_f32_bf16 v176, v112, s22, 0\n" \
  "v_dot2_f32_bf16 v177, v112, s23, 0\n" \
  "v_dot2_f32_bf16 v178, v113, s22, 0\n" \
  "v_dot2_f32_bf16 v179, v113, s23, 0\n" \
  "v_dot2_f32_bf16 v180, v114, s22, 0\n" \
  "v_dot2_f32_bf16 v181, v114, s23, 0\n" \
  "v_dot2_f32_bf16 v182, v115, s22, 0\n" \
  "v_dot2_f32_bf16 v183, v115, s23, 0\n" \
  "v_dot2_f32_bf16 v176, v116, s22, v176\n" \
  "v_dot2_f32_bf16 v177, v116, s23, v177\n" \
  "v_dot2_f32_bf16 v178, v117, s22, v178\n" \
  "v_dot2_f32_bf16 v179, v117, s23, v179\n" \
  "v_dot2_f32_bf16 v180, v118, s22, v180\n" \
  "v_dot2_f32_bf16 v181, v118, s23, v181\n" \
  "v_dot2_f32_bf16 v182, v119, s22, v182\n" \
  "v_dot2_f32_bf16 v183, v119, s23, v183\n" \
  "v_exp_f32 v176, v176\n" \
  "v_exp_f32 v177, v177\n" \
  "v_exp_f32 v178, v178\n" \
  "v_exp_f32 v179, v179\n" \
  "s_waitcnt vmcnt(10)\n" \
  "v_mfma_f32_16x16x32_bf16 v[160:163], v[56:59], v[120:123], 0\n" \
  "v_exp_f32 v180, v180\n" \
  "v_mfma_f32_16x16x32_bf16 v[144:147], v[40:43], v[120:123], 0\n" \
  "v_exp_f32 v181, v181\n" \
  "v_exp_f32 v182, v182\n" \
  "v_exp_f32 v183, v183\n" \
  "v_cvt_pk_bf16_f32 v184, v176, v177\n" \
  "v_cvt_pk_bf16_f32 v185, v178, v179\n" \
  "v_cvt_pk_bf16_f32 v186, v180, v181\n" \
  "v_cvt_pk_bf16_f32 v187, v182, v183\n" \
  "s_nop 1\n" \
  "v_mfma_f32_16x16x32_bf16 v[160:163], v[72:75], v[184:187], v[160:163]\n" \
  "v_mfma_f32_16x16x32_bf16 v[164:167], v[76:79], v[184:187], 0\n" \
  "s_waitcnt vmcnt(9)\n" \
  "v_mfma_f32_16x16x32_bf16 v[164:167], v[60:63], v[124:127], v[164:167]\n" \
  "v_rcp_f32 v188, v162\n" \
  "v_mfma_f32_16x16x32_bf16 v[148:151], v[44:47], v[124:127], 0\n" \
  "v_mul_f32 v144, v188, v144\n" \
  "v_mul_f32 v145, v188, v145\n" \
  "v_mfma_f32_16x16x32_bf16 v[168:171], v[80:83], v[184:187], 0\n" \
  "s_waitcnt vmcnt(8)\n" \
  "v_rcp_f32 v189, v166\n" \
  "v_mfma_f32_16x16x32_bf16 v[168:171], v[64:67], v[128:131], v[168:171]\n" \
  "v_mul_f32 v146, v188, v146\n" \
  "v_mul_f32 v147, v188, v147\n" \
  "v_mfma_f32_16x16x32_bf16 v[152:155], v[48:51], v[128:131], 0\n" \
  "v_mul_f32 v160, v188, v160\n" \
  "v_mul_f32 v161, v188, v161\n" \
  "v_mfma_f32_16x16x32_bf16 v[172:175], v[84:87], v[184:187], 0\n" \
  "s_waitcnt vmcnt(7)\n" \
  "v_rcp_f32 v190, v170\n" \
  "v_mfma_f32_16x16x32_bf16 v[172:175], v[68:71], v[132:135], v[172:175]\n" \
  "v_cndmask_b32 v167, v188, v189, s[28:29]\n" \
  "v_cndmask_b32 v167, v167, v190, s[26:27]\n" \
  "v_mfma_f32_16x16x32_bf16 v[156:159], v[52:55], v[132:135], 0\n" \
  "v_fmac_f32 v144, v189, v148\n" \
  "v_fmac_f32 v145, v189, v149\n" \
  "v_fmac_f32 v146, v189, v150\n" \
  "v_fmac_f32 v147, v189, v151\n" \
  "v_rcp_f32 v191, v174\n" \
  "v_fmac_f32 v160, v189, v164\n" \
  "v_cndmask_b32 v167, v167, v191, s[24:25]\n" \
  "v_pk_mul_f32 v[176:177], v[176:177], v[166:167] op_sel:[0,1] op_sel_hi:[1,1]\n" \
  "v_pk_mul_f32 v[178:179], v[178:179], v[166:167] op_sel:[0,1] op_sel_hi:[1,1]\n" \
  "v_pk_mul_f32 v[180:181], v[180:181], v[166:167] op_sel:[0,1] op_sel_hi:[1,1]\n" \
  "v_pk_mul_f32 v[182:183], v[182:183], v[166:167] op_sel:[0,1] op_sel_hi:[1,1]\n" \
  "v_cmp_nlt_f32 vcc, 0x0d800000, v167\n" \
  "v_cvt_pk_bf16_f32 v184, v176, v177\n" \
  "v_cvt_pk_bf16_f32 v185, v178, v179\n" \
  "v_cvt_pk_bf16_f32 v186, v180, v181\n" \
  "v_cvt_pk_bf16_f32 v187, v182, v183\n" \
  "v_fmac_f32 v161, v189, v165\n" \
  "v_pk_fma_f32 v[160:161], v[168:169], v[190:191], v[160:161] op_sel:[0,0,0] op_sel_hi:[1,0,1]\n" \
  "v_pk_fma_f32 v[160:161], v[172:173], v[190:191], v[160:161] op_sel:[0,1,0] op_sel_hi:[1,1,1]\n" \
  "v_pk_fma_f32 v[144:145], v[152:153], v[190:191], v[144:145] op_sel:[0,0,0] op_sel_hi:[1,0,1]\n" \
  "v_pk_fma_f32 v[146:147], v[154:155], v[190:191], v[146:147] op_sel:[0,0,0] op_sel_hi:[1,0,1]\n" \
  "v_pk_fma_f32 v[144:145], v[156:157], v[190:191], v[144:145] op_sel:[0,1,0] op_sel_hi:[1,1,1]\n" \
  "v_pk_fma_f32 v[146:147], v[158:159], v[190:191], v[146:147] op_sel:[0,1,0] op_sel_hi:[1,1,1]\n" \
  "s_waitcnt vmcnt(5)\n" \
  "v_mfma_f32_16x16x32_bf16 v[160:163], v[140:143], v[184:187], v[160:163]\n" \
  "s_mov_b64 s[80:81], vcc\n" \
  "v_mfma_f32_16x16x32_bf16 v[144:147], v[136:139], v[184:187], v[144:147]\n" \
  "s_mov_b64 exec, s[30:31]\n" \
  "s_nop 2\n" \
  "ds_write_b64 v22, v[160:161]\n" \
  "v_cmp_u_f32 s[78:79], v160, v161\n" \
  "s_nop 1\n" \
  "ds_write2_b64 v21, v[144:145], v[146:147] offset1:16\n" \
  "v_add_f32 v188, v144, v145\n" \
  "v_add_f32 v189, v146, v147\n" \
  "v_cmp_u_f32 vcc, v188, v189\n" \
  "s_or_b64 s[80:81], s[80:81], s[78:79]\n" \
  "s_or_b64 vcc, vcc, s[80:81]\n" \
  "s_cbranch_vccnz LSF01_%=\n" \
  "LSOK01_%=:\n" \
  "s_mov_b64 exec, -1\n" \
  "s_waitcnt lgkmcnt(0)\n" \
  "s_barrier\n" \
  "ds_read_b32 v188, v33\n" \
  "s_waitcnt lgkmcnt(0)\n" \
  "v_cmp_ne_u32 vcc, 0, v188\n" \
  "s_cbranch_vccnz LSR01_%=\n" \
  "s_nop 15\n" \
  "s_nop 3\n" \
  "buffer_load_dwordx2 v[104:105], v2, s[36:39], s68 offen sc1\n" \
  "buffer_load_dwordx2 v[106:107], v2, s[36:39], s91 offen sc1\n" \
  "buffer_load_dwordx2 v[108:109], v2, s[36:39], s92 offen sc1\n" \
  "buffer_load_dwordx2 v[110:111], v2, s[36:39], s93 offen sc1\n" \
  "LNL01_%=:\n" \
  "s_waitcnt lgkmcnt(0)\n" \
  "s_cmp_eq_u32 s73, 1\n" \
  "s_cbranch_scc1 LOOP01_%=\n" \
  "s_branch LDONE_%=\n" \
  "LMF01_%=:\n" \
  "v_mbcnt_lo_u32_b32 v130, -1, 0\n" \
  "v_mbcnt_hi_u32_b32 v130, -1, v130\n" \
  "v_lshlrev_b32 v130, 4, v130\n" \
  "v_mov_b32 v128, 0\n" \
  "ds_read_b128 v[132:135], v130 offset:0\n" \
  "ds_read_b128 v[136:139], v130 offset:1024\n" \
  "ds_read_b128 v[140:143], v130 offset:2048\n" \
  "ds_read_b128 v[144:147], v130 offset:3072\n" \
  "ds_read_b128 v[148:151], v130 offset:4096\n" \
  "s_waitcnt lgkmcnt(0)\n" \
  "v_max3_u32 v128, v128, v132, v133\n" \
  "v_max3_u32 v128, v128, v134, v135\n" \
  "v_max3_u32 v128, v128, v136, v137\n" \
  "v_max3_u32 v128, v128, v138, v139\n" \
  "v_max3_u32 v128, v128, v140, v141\n" \
  "v_max3_u32 v128, v128, v142, v143\n" \
  "v_max3_u32 v128, v128, v144, v145\n" \
  "v_max3_u32 v128, v128, v146, v147\n" \
  "v_max3_u32 v128, v128, v148, v149\n" \
  "v_max3_u32 v128, v128, v150, v151\n" \
  "ds_read_b128 v[132:135], v130 offset:5120\n" \
  "ds_read_b128 v[136:139], v130 offset:6144\n" \
  "ds_read_b128 v[140:143], v130 offset:7168\n" \
  "ds_read_b128 v[144:147], v130 offset:8192\n" \
  "ds_read_b128 v[148:151], v130 offset:9216\n" \
  "s_waitcnt lgkmcnt(0)\n" \
  "v_max3_u32 v128, v128, v132, v133\n" \
  "v_max3_u32 v128, v128, v134, v135\n" \
  "v_max3_u32 v128, v128, v136, v137\n" \
  "v_max3_u32 v128, v128, v138, v139\n" \
  "v_max3_u32 v128, v128, v140, v141\n" \
  "v_max3_u32 v128, v128, v142, v143\n" \
  "v_max3_u32 v128, v128, v144, v145\n" \
  "v_max3_u32 v128, v128, v146, v147\n" \
  "v_max3_u32 v128, v128, v148, v149\n" \
  "v_max3_u32 v128, v128, v150, v151\n" \
  "v_cmp_eq_u32 vcc, -1, v128\n" \
  "s_cbranch_vccz LMOK01_%=\n" \
  "s_cmp_gt_u32 s58, 4000\n" \
  "s_cbranch_scc1 LMOK01_%=\n" \
  "s_add_u32 s58, s58, 1\n" \
  "s_sub_u32 s64, s64, 1\n" \
  "s_mov_b32 s66, s65\n" \
  "s_waitcnt vmcnt(0) lgkmcnt(0)\n" \
  "s_barrier\n" \
  "s_branch LOOP01_%=\n" \
  "LKE01_%=:\n" \
  "s_waitcnt lgkmcnt(0)\n" \
  "buffer_store_dwordx2 v[34:35], v14, s[36:39], s69 offen sc1\n" \
  "s_branch LDONE_%=\n" \
  "LMX01_%=:\n" \
  "v_cvt_pk_bf16_f32 v155, v152, v153\n" \
  "v_pk_min_u16 v155, v155, -2 op_sel_hi:[1,0]\n" \
  "s_add_u32 s71, s65, 245760\n" \
  "buffer_store_dword v155, v13, s[36:39], s71 offen sc1\n" \
  "v_mov_b32 v154, 0x7fd57fd5\n" \
  "s_branch LMY01_%=\n" \
  "LSF01_%=:\n" \
  "s_getpc_b64 s[78:79]\n" \
  "s_branch LSF_%=\n" \
  "s_branch LSOK01_%=\n" \
  "LSR01_%=:\n" \
  "v_mov_b32 v188, 0\n" \
  "s_add_u32 s59, s59, 1\n" \
  "s_barrier\n" \
  "ds_write_b32 v33, v188\n" \
  "s_waitcnt lgkmcnt(0)\n" \
  "s_branch LB01_%=\n" \
  "LSTART02_%=:\n" \
  "s_getpc_b64 s[80:81]\n" \
  "LPC02_%=:\n" \
  "s_add_u32 s82, s80, LEA02_%=-LPC02_%=+24\n" \
  "s_addc_u32 s83, s81, 0\n" \
  "s_add_u32 s84, s80, LEB02_%=-LPC02_%=+24\n" \
  "s_addc_u32 s85, s81, 0\n" \
  "LOOP02_%=:\n" \
  "v_readfirstlane_b32 s86, v34\n" \
  "s_add_u32 s71, s64, 1\n" \
  "s_min_u32 s71, s71, 63\n" \
  "s_lshr_b32 s72, s71, 4\n" \
  "s_lshl_b32 s72, s72, 13\n" \
  "s_lshl_b32 s71, s71, 9\n" \
  "s_add_u32 s71, s71, s72\n" \
  "s_mov_b32 s87, s71\n" \
  "s_lshl_b32 s40, s64, 7\n" \
  "s_mov_b32 m0, 0\n" \
  "LWA02_%=:\n" \
  "s_memrealtime s[74:75]\n" \
  "s_memtime s[76:77]\n" \
  "s_waitcnt lgkmcnt(0)\n" \
  "s_sub_i32 s72, s88, s74\n" \
  "s_mul_i32 s72, s72, s90\n" \
  "s_ashr_i32 s72, s72, 8\n" \
  "s_sub_i32 s71, s89, s76\n" \
  "s_min_i32 s71, s71, s72\n" \
  "s_sub_i32 s72, s72, 92\n" \
  "s_max_i32 s71, s71, s72\n" \
  "s_add_u32 s89, s71, s76\n" \
  "s_cmp_gt_i32 s71, 168\n" \
  "s_cbranch_scc1 LWA02_%=\n" \
  "s_cmp_lt_i32 s71, s94\n" \
  "s_cbranch_scc1 LSA02_%=\n" \
  "s_max_i32 s71, s71, 48\n" \
  "s_lshr_b32 s71, s71, 3\n" \
  "s_lshl_b32 s71, s71, 2\n" \
  "s_sub_u32 s78, s82, s71\n" \
  "s_subb_u32 s79, s83, 0\n" \
  "s_setpc_b64 s[78:79]\n" \
  "s_nop 1\n" \
  "s_nop 1\n" \
  "s_nop 1\n" \
  "s_nop 1\n" \
  "s_nop 1\n" \
  "s_nop 1\n" \
  "s_nop 1\n" \
  "s_nop 1\n" \
  "s_nop 1\n" \
  "s_nop 1\n" \
  "s_nop 1\n" \
  "s_nop 1\n" \
  "s_nop 1\n" \
  "s_nop 1\n" \
  "s_nop 1\n" \
  "s_nop 1\n" \
  "LEA02_%=:\n" \
  "ds_read_b32 v188, v33\n" \
  "buffer_load_dwordx4 v1, s[36:39], s67 offen sc1 lds\n" \
  "buffer_load_dwordx4 v1, s[36:39], s67 offen offset:1024 sc1 lds\n" \
  "buffer_load_dwordx4 v1, s[36:39], s67 offen offset:2048 sc1 lds\n" \
  "buffer_load_dwordx4 v1, s[36:39], s67 offen offset:3072 sc1 lds\n" \
  "s_mov_b32 m0, 4096\n" \
  "s_nop 0\n" \
  "buffer_load_dwordx4 v2, s[36:39], s67 offen sc1 lds\n" \
  "buffer_load_dwordx4 v2, s[36:39], s67 offen offset:1024 sc1 lds\n" \
  "buffer_load_dwordx4 v2, s[36:39], s67 offen offset:2048 sc1 lds\n" \
  "buffer_load_dwordx4 v2, s[36:39], s67 offen offset:3072 sc1 lds\n" \
  "s_mov_b32 m0, 8192\n" \
  "s_nop 0\n" \
  "buffer_load_dwordx4 v3, s[36:39], s67 offen sc1 lds\n" \
  "buffer_load_dwordx4 v3, s[36:39], s67 offen offset:1024 sc1 lds\n" \
  "s_waitcnt lgkmcnt(0)\n" \
  "v_cmp_ne_u32 vcc, 0, v188\n" \
  "s_cbranch_vccnz LSR02_%=\n" \
  "buffer_load_dword v35, v28, s[36:39], s87 offen sc1\n" \
  "buffer_load_dword v34, v29, s[36:39], s40 offen offset:128 sc1\n" \
  "v_readlane_b32 s70, v27, s64\n" \
  "s_mov_b32 s65, s66\n" \
  "s_xor_b32 s69, s66, 0x2000\n" \
  "s_add_u32 s64, s64, 1\n" \
  "s_barrier\n" \
  "v_mov_b32 v176, 0\n" \
  "v_mov_b32 v177, 0\n" \
  "v_mov_b32 v178, 0\n" \
  "v_mov_b32 v179, 0\n" \
  "v_mov_b32 v180, 0\n" \
  "v_mov_b32 v181, 0\n" \
  "v_mov_b32 v182, 0\n" \
  "v_mov_b32 v183, 0\n" \
  "v_readlane_b32 s66, v25, s64\n" \
  "v_readlane_b32 s68, v26, s64\n" \
  "s_cmp_eq_u32 s34, s64\n" \
  "s_cbranch_scc1 LRT02_%=\n" \
  "s_mov_b32 s34, s64\n" \
  "s_mov_b32 s71, s86\n" \
  "s_max_u32 s19, s19, s71\n" \
  "s_and_b32 s72, s71, 0xff\n" \
  "s_lshr_b32 s71, s71, 8\n" \
  "s_cselect_b32 s71, s71, s61\n" \
  "s_sub_i32 s72, s64, s72\n" \
  "s_sub_i32 s71, s71, s61\n" \
  "s_cmp_ge_i32 s72, 0\n" \
  "s_cselect_b32 s71, s71, 0\n" \
  "s_add_u32 s61, s61, s71\n" \
  "s_mul_i32 s72, s72, s71\n" \
  "s_lshl_b32 s72, s72, 1\n" \
  "s_add_u32 s88, s88, s72\n" \
  "s_mul_i32 s72, s72, s90\n" \
  "s_ashr_i32 s72, s72, 8\n" \
  "s_add_u32 s89, s89, s72\n" \
  "s_lshr_b32 s72, s18, 1\n" \
  "s_sub_u32 s80, s72, s18\n" \
  "s_mov_b32 s18, s72\n" \
  "s_add_u32 s71, s71, s80\n" \
  "s_add_u32 s50, s50, s71\n" \
  "s_mul_i32 s91, s50, s90\n" \
  "s_lshr_b32 s91, s91, 8\n" \
  "s_add_u32 s51, s51, s71\n" \
  "s_mul_i32 s92, s51, s90\n" \
  "s_lshr_b32 s92, s92, 8\n" \
  "LRT02_%=:\n" \
  "LM02_%=:\n" \
  "s_waitcnt vmcnt(11)\n" \
  "s_barrier\n" \
  "ds_read_b128 v[112:115], v5\n" \
  "ds_read_b128 v[116:119], v5 offset:512\n" \
  "s_waitcnt vmcnt(10)\n" \
  "s_barrier\n" \
  "ds_read_b128 v[120:123], v5 offset:1024\n" \
  "ds_read_b128 v[124:127], v5 offset:1536\n" \
  "s_waitcnt lgkmcnt(3)\n" \
  "v_smfmac_f32_16x16x64_bf16 v[176:179], v[112:115], a[0:7], v0\n" \
  "v_smfmac_f32_16x16x64_bf16 v[180:183], v[112:115], a[160:167], v0\n" \
  "s_waitcnt vmcnt(8)\n" \
  "s_waitcnt lgkmcnt(2)\n" \
  "v_smfmac_f32_16x16x64_bf16 v[176:179], v[116:119], a[8:15], v0\n" \
  "s_barrier\n" \
  "ds_read_b128 v[128:131], v5 offset:2048\n" \
  "v_smfmac_f32_16x16x64_bf16 v[180:183], v[116:119], a[168:175], v0\n" \
  "s_waitcnt lgkmcnt(2)\n" \
  "ds_read_b128 v[132:135], v5 offset:2560\n" \
  "v_smfmac_f32_16x16x64_bf16 v[176:179], v[120:123], a[16:23], v0\n" \
  "ds_read_b128 v[136:139], v5 offset:3072\n" \
  "v_smfmac_f32_16x16x64_bf16 v[180:183], v[120:123], a[176:183], v0\n" \
  "s_waitcnt vmcnt(6)\n" \
  "s_waitcnt lgkmcnt(3)\n" \
  "ds_read_b128 v[140:143], v5 offset:3584\n" \
  "v_smfmac_f32_16x16x64_bf16 v[176:179], v[124:127], a[24:31], v0\n" \
  "s_barrier\n" \
  "ds_read_b128 v[144:147], v5 offset:4096\n" \
  "v_smfmac_f32_16x16x64_bf16 v[180:183], v[124:127], a[184:191], v0\n" \
  "s_waitcnt lgkmcnt(4)\n" \
  "ds_read_b128 v[148:151], v5 offset:4608\n" \
  "v_smfmac_f32_16x16x64_bf16 v[176:179], v[128:131], a[32:39], v0\n" \
  "ds_read_b128 v[152:155], v5 offset:5120\n" \
  "v_smfmac_f32_16x16x64_bf16 v[180:183], v[128:131], a[192:199], v0\n" \
  "s_waitcnt vmcnt(4)\n" \
  "s_waitcnt lgkmcnt(5)\n" \
  "ds_read_b128 v[156:159], v5 offset:5632\n" \
  "v_smfmac_f32_16x16x64_bf16 v[176:179], v[132:135], a[40:47], v0\n" \
  "s_barrier\n" \
  "ds_read_b128 v[160:163], v5 offset:6144\n" \
  "v_smfmac_f32_16x16x64_bf16 v[180:183], v[132:135], a[200:207], v0\n" \
  "s_waitcnt lgkmcnt(6)\n" \
  "ds_read_b128 v[164:167], v5 offset:6656\n" \
  "v_smfmac_f32_16x16x64_bf16 v[176:179], v[136:139], a[48:55], v0\n" \
  "ds_read_b128 v[168:171], v5 offset:7168\n" \
  "v_smfmac_f32_16x16x64_bf16 v[180:183], v[136:139], a[208:215], v0\n" \
  "s_waitcnt vmcnt(2)\n" \
  "s_waitcnt lgkmcnt(7)\n" \
  "ds_read_b128 v[172:175], v5 offset:7680\n" \
  "v_smfmac_f32_16x16x64_bf16 v[176:179], v[140:143], a[56:63], v0\n" \
  "s_barrier\n" \
  "ds_read2_b32 v[112:113], v6 offset0:0 offset1:32\n" \
  "v_smfmac_f32_16x16x64_bf16 v[180:183], v[140:143], a[216:223], v0\n" \
  "s_waitcnt lgkmcnt(8)\n" \
  "ds_read2_b32 v[114:115], v6 offset0:64 offset1:96\n" \
  "v_smfmac_f32_16x16x64_bf16 v[176:179], v[144:147], a[64:71], v0\n" \
  "s_waitcnt lgkmcnt(8)\n" \
  "ds_read2_b32 v[116:117], v6 offset0:128 offset1:160\n" \
  "v_smfmac_f32_16x16x64_bf16 v[176:179], v[148:151], a[72:79], v0\n" \
  "s_waitcnt lgkmcnt(8)\n" \
  "ds_read2_b32 v[118:119], v6 offset0:192 offset1:224\n" \
  "v_smfmac_f32_16x16x64_bf16 v[176:179], v[152:155], a[80:87], v0\n" \
  "s_waitcnt lgkmcnt(8)\n" \
  "ds_read2_b32 v[120:121], v7 offset0:0 offset1:32\n" \
  "v_smfmac_f32_16x16x64_bf16 v[176:179], v[156:159], a[88:95], v0\n" \
  "s_waitcnt lgkmcnt(8)\n" \
  "ds_read2_b32 v[122:123], v7 offset0:64 offset1:96\n" \
  "v_smfmac_f32_16x16x64_bf16 v[176:179], v[160:163], a[96:103], v0\n" \
  "s_waitcnt lgkmcnt(8)\n" \
  "ds_read2_b32 v[124:125], v7 offset0:128 offset1:160\n" \
  "v_smfmac_f32_16x16x64_bf16 v[176:179], v[164:167], a[104:111], v0\n" \
  "s_waitcnt lgkmcnt(8)\n" \
  "ds_read2_b32 v[126:127], v7 offset0:192 offset1:224\n" \
  "v_smfmac_f32_16x16x64_bf16 v[176:179], v[168:171], a[112:119], v0\n" \
  "s_waitcnt lgkmcnt(8)\n" \
  "v_smfmac_f32_16x16x64_bf16 v[176:179], v[172:175], a[120:127], v0\n" \
  "s_waitcnt lgkmcnt(6)\n" \
  "v_smfmac_f32_16x16x64_bf16 v[176:179], v[112:115], a[128:135], v0\n" \
  "s_waitcnt lgkmcnt(4)\n" \
  "v_smfmac_f32_16x16x64_bf16 v[176:179], v[116:119], a[136:143], v0\n" \
  "s_waitcnt lgkmcnt(2)\n" \
  "v_smfmac_f32_16x16x64_bf16 v[176:179], v[120:123], a[144:151], v0\n" \
  "s_waitcnt lgkmcnt(0)\n" \
  "v_smfmac_f32_16x16x64_bf16 v[176:179], v[124:127], a[152:159], v0\n" \
  "v_smfmac_f32_16x16x64_bf16 v[180:183], v[144:147], a[224:231], v0\n" \
  "v_smfmac_f32_16x16x64_bf16 v[180:183], v[148:151], a[232:239], v0\n" \
  "v_cvt_pk_bf16_f32 v184, v176, v177\n" \
  "v_smfmac_f32_16x16x64_bf16 v[180:183], v[152:155], a[240:247], v0\n" \
  "v_cvt_pk_bf16_f32 v185, v178, v179\n" \
  "v_smfmac_f32_16x16x64_bf16 v[180:183], v[156:159], a[248:255], v0\n" \
  "ds_write_b64 v8, v[184:185]\n" \
  "v_add_f32 v186, v176, v177\n" \
  "v_smfmac_f32_16x16x64_bf16 v[180:183], v[160:163], v[192:199], v0\n" \
  "v_add_f32 v187, v178, v179\n" \
  "v_smfmac_f32_16x16x64_bf16 v[180:183], v[164:167], v[200:207], v0\n" \
  "v_cmp_u_f32 vcc, v186, v187\n" \
  "v_smfmac_f32_16x16x64_bf16 v[180:183], v[168:171], v[208:215], v0\n" \
  "v_smfmac_f32_16x16x64_bf16 v[180:183], v[172:175], v[216:223], v0\n" \
  "s_cbranch_vccnz LMF02_%=\n" \
  "LMOK02_%=:\n" \
  "v_smfmac_f32_16x16x64_bf16 v[180:183], v[112:115], v[224:231], v0\n" \
  "s_waitcnt lgkmcnt(0)\n" \
  "s_barrier\n" \
  "v_smfmac_f32_16x16x64_bf16 v[180:183], v[116:119], v[232:239], v0\n" \
  "v_smfmac_f32_16x16x64_bf16 v[180:183], v[120:123], v[240:247], v0\n" \
  "v_smfmac_f32_16x16x64_bf16 v[180:183], v[124:127], v[248:255], v0\n" \
  "s_nop 3\n" \
  "v_mov_b32 v31, v31\n" \
  "s_nop 2\n" \
  "v_cvt_pk_bf16_f32 v186, v180, v181\n" \
  "v_cvt_pk_bf16_f32 v187, v182, v183\n" \
  "ds_write_b64 v9, v[186:187]\n" \
  "s_waitcnt lgkmcnt(0)\n" \
  "s_barrier\n" \
  "s_nop 0\n" \
  "ds_read_b64_tr_b16 v[88:89], v12\n" \
  "s_mov_b32 s67, s66\n" \
  "s_cmp_eq_u32 s64, 64\n" \
  "s_cbranch_scc1 LKE02_%=\n" \
  "s_add_u32 s88, s88, s50\n" \
  "s_add_u32 s89, s89, s91\n" \
  "s_waitcnt vmcnt(0)\n" \
  "v_cmp_gt_u32 vcc, 16, v35\n" \
  "s_cbranch_vccnz LFF02_%=\n" \
  "LFOK02_%=:\n" \
  "LB02_%=:\n" \
  "LWB02_%=:\n" \
  "s_memrealtime s[74:75]\n" \
  "s_memtime s[76:77]\n" \
  "s_waitcnt lgkmcnt(0)\n" \
  "s_sub_i32 s72, s88, s74\n" \
  "s_mul_i32 s72, s72, s90\n" \
  "s_ashr_i32 s72, s72, 8\n" \
  "s_sub_i32 s71, s89, s76\n" \
  "s_min_i32 s71, s71, s72\n" \
  "s_sub_i32 s72, s72, 92\n" \
  "s_max_i32 s71, s71, s72\n" \
  "s_add_u32 s89, s71, s76\n" \
  "s_cmp_gt_i32 s71, 168\n" \
  "s_cbranch_scc1 LWB02_%=\n" \
  "s_cmp_lt_i32 s71, s94\n" \
  "s_cbranch_scc1 LSB02_%=\n" \
  "s_max_i32 s71, s71, 48\n" \
  "s_lshr_b32 s71, s71, 3\n" \
  "s_lshl_b32 s71, s71, 2\n" \
  "s_sub_u32 s78, s84, s71\n" \
  "s_subb_u32 s79, s85, 0\n" \
  "s_setpc_b64 s[78:79]\n" \
  "s_nop 1\n" \
  "s_nop 1\n" \
  "s_nop 1\n" \
  "s_nop 1\n" \
  "s_nop 1\n" \
  "s_nop 1\n" \
  "s_nop 1\n" \
  "s_nop 1\n" \
  "s_nop 1\n" \
  "s_nop 1\n" \
  "s_nop 1\n" \
  "s_nop 1\n" \
  "s_nop 1\n" \
  "s_nop 1\n" \
  "s_nop 1\n" \
  "s_nop 1\n" \
  "LEB02_%=:\n" \
  "s_barrier\n" \
  "buffer_load_dwordx4 v[116:119], v15, s[36:39], s65 offen offset:128 sc1\n" \
  "s_barrier\n" \
  "buffer_load_dwordx4 v[112:115], v15, s[36:39], s65 offen sc1\n" \
  "s_add_u32 s88, s88, s51\n" \
  "s_add_u32 s89, s89, s92\n" \
  "s_barrier\n" \
  "buffer_load_dwordx4 v[120:123], v16, s[36:39], s65 offen sc1\n" \
  "s_barrier\n" \
  "buffer_load_dwordx4 v[124:127], v16, s[36:39], s65 offen offset:512 sc1\n" \
  "s_barrier\n" \
  "buffer_load_dwordx4 v[128:131], v16, s[36:39], s65 offen offset:1024 sc1\n" \
  "s_barrier\n" \
  "buffer_load_dwordx4 v[132:135], v16, s[36:39], s65 offen offset:1536 sc1\n" \
  "ds_read_b128 v[40:43], v19\n" \
  "ds_read_b128 v[44:47], v19 offset:512\n" \
  "ds_read_b128 v[48:51], v19 offset:1024\n" \
  "ds_read_b128 v[52:55], v19 offset:1536\n" \
  "s_mov_b64 exec, s[46:47]\n" \
  "ds_read_b128 v[56:59], v20\n" \
  "ds_read_b128 v[60:63], v20 offset:128\n" \
  "ds_read_b128 v[64:67], v20 offset:256\n" \
  "ds_read_b128 v[68:71], v20 offset:384\n" \
  "s_mov_b64 exec, -1\n" \
  "v_readfirstlane_b32 s86, v34\n" \
  "s_add_u32 s71, s64, 1\n" \
  "s_min_u32 s71, s71, 63\n" \
  "s_lshr_b32 s72, s71, 4\n" \
  "s_lshl_b32 s72, s72, 13\n" \
  "s_lshl_b32 s71, s71, 9\n" \
  "s_add_u32 s71, s71, s72\n" \
  "s_mov_b32 s87, s71\n" \
  "s_lshl_b32 s40, s64, 7\n" \
  "s_mov_b32 m0, 0\n" \
  "s_nop 7\n" \
  "buffer_load_dwordx4 v[136:139], v17, s[36:39], s65 offen sc1\n" \
  "buffer_load_dwordx4 v[140:143], v18, s[36:39], s65 offen sc1\n" \
  "s_barrier\n" \
  "s_waitcnt lgkmcnt(0)\n" \
  "buffer_store_dwordx2 v[88:89], v14, s[36:39], s69 offen sc1\n" \
  "s_sub_u32 s72, s74, s95\n" \
  "s_cmp_lt_u32 s64, 3\n" \
  "s_cbranch_scc1 LRB02_%=\n" \
  "s_cmp_gt_u32 s72, 900\n" \
  "s_cbranch_scc0 LRC02_%=\n" \
  "s_sub_u32 s71, s76, s96\n" \
  "s_sub_u32 s72, s74, s95\n" \
  "v_cvt_f32_u32 v36, s71\n" \
  "v_cvt_f32_u32 v37, s72\n" \
  "v_rcp_f32 v37, v37\n" \
  "v_mul_f32 v36, 0x43820000, v36\n" \
  "v_mul_f32 v36, v36, v37\n" \
  "v_cvt_u32_f32 v36, v36\n" \
  "s_nop 0\n" \
  "v_readfirstlane_b32 s71, v36\n" \
  "s_min_u32 s90, s71, 6400\n" \
  "LRB02_%=:\n" \
  "s_mov_b32 s95, s74\n" \
  "s_mov_b32 s96, s76\n" \
  "LRC02_%=:\n" \
  "s_waitcnt lgkmcnt(0)\n" \
  "s_getreg_b32 s71, hwreg(HW_REG_IB_STS, 0, 4)\n" \
  "s_cmp_le_u32 s71, 7\n" \
  "v_mfma_f32_16x16x32_bf16 v[144:147], v[72:75], v[72:75], 0\n" \
  "s_cbranch_scc1 LHXS02_%=\n" \
  "s_getreg_b32 s71, hwreg(HW_REG_IB_STS, 0, 4)\n" \
  "s_cmp_le_u32 s71, 7\n" \
  "v_mfma_f32_16x16x32_bf16 v[144:147], v[72:75], v[72:75], 0\n" \
  "s_cbranch_scc1 LHXS02_%=\n" \
  "s_getreg_b32 s71, hwreg(HW_REG_IB_STS, 0, 4)\n" \
  "s_cmp_le_u32 s71, 7\n" \
  "v_mfma_f32_16x16x32_bf16 v[144:147], v[72:75], v[72:75], 0\n" \
  "s_cbranch_scc1 LHXS02_%=\n" \
  "s_getreg_b32 s71, hwreg(HW_REG_IB_STS, 0, 4)\n" \
  "s_cmp_le_u32 s71, 7\n" \
  "v_mfma_f32_16x16x32_bf16 v[144:147], v[72:75], v[72:75], 0\n" \
  "s_cbranch_scc1 LHXS02_%=\n" \
  "s_getreg_b32 s71, hwreg(HW_REG_IB_STS, 0, 4)\n" \
  "s_cmp_le_u32 s71, 7\n" \
  "v_mfma_f32_16x16x32_bf16 v[144:147], v[72:75], v[72:75], 0\n" \
  "s_cbranch_scc1 LHXS02_%=\n" \
  "s_getreg_b32 s71, hwreg(HW_REG_IB_STS, 0, 4)\n" \
  "s_cmp_le_u32 s71, 7\n" \
  "v_mfma_f32_16x16x32_bf16 v[144:147], v[72:75], v[72:75], 0\n" \
  "s_cbranch_scc1 LHXS02_%=\n" \
  "s_getreg_b32 s71, hwreg(HW_REG_IB_STS, 0, 4)\n" \
  "s_cmp_le_u32 s71, 7\n" \
  "v_mfma_f32_16x16x32_bf16 v[144:147], v[72:75], v[72:75], 0\n" \
  "s_cbranch_scc1 LHXS02_%=\n" \
  "s_getreg_b32 s71, hwreg(HW_REG_IB_STS, 0, 4)\n" \
  "s_cmp_le_u32 s71, 7\n" \
  "v_mfma_f32_16x16x32_bf16 v[144:147], v[72:75], v[72:75], 0\n" \
  "s_cbranch_scc1 LHXS02_%=\n" \
  "s_getreg_b32 s71, hwreg(HW_REG_IB_STS, 0, 4)\n" \
  "s_cmp_le_u32 s71, 7\n" \
  "v_mfma_f32_16x16x32_bf16 v[144:147], v[72:75], v[72:75], 0\n" \
  "s_cbranch_scc1 LHXS02_%=\n" \
  "s_getreg_b32 s71, hwreg(HW_REG_IB_STS, 0, 4)\n" \
  "s_cmp_le_u32 s71, 7\n" \
  "v_mfma_f32_16x16x32_bf16 v[144:147], v[72:75], v[72:75], 0\n" \
  "s_cbranch_scc1 LHXS02_%=\n" \
  "s_getreg_b32 s71, hwreg(HW_REG_IB_STS, 0, 4)\n" \
  "s_cmp_le_u32 s71, 7\n" \
  "v_mfma_f32_16x16x32_bf16 v[144:147], v[72:75], v[72:75], 0\n" \
  "s_cbranch_scc1 LHXS02_%=\n" \
  "s_getreg_b32 s71, hwreg(HW_REG_IB_STS, 0, 4)\n" \
  "s_cmp_le_u32 s71, 7\n" \
  "v_mfma_f32_16x16x32_bf16 v[144:147], v[72:75], v[72:75], 0\n" \
  "s_cbranch_scc1 LHXS02_%=\n" \
  "s_getreg_b32 s71, hwreg(HW_REG_IB_STS, 0, 4)\n" \
  "s_cmp_le_u32 s71, 7\n" \
  "v_mfma_f32_16x16x32_bf16 v[144:147], v[72:75], v[72:75], 0\n" \
  "s_cbranch_scc1 LHXS02_%=\n" \
  "s_getreg_b32 s71, hwreg(HW_REG_IB_STS, 0, 4)\n" \
  "s_cmp_le_u32 s71, 7\n" \
  "v_mfma_f32_16x16x32_bf16 v[144:147], v[72:75], v[72:75], 0\n" \
  "s_cbranch_scc1 LHXS02_%=\n" \
  "s_getreg_b32 s71, hwreg(HW_REG_IB_STS, 0, 4)\n" \
  "s_cmp_le_u32 s71, 7\n" \
  "v_mfma_f32_16x16x32_bf16 v[144:147], v[72:75], v[72:75], 0\n" \
  "s_cbranch_scc1 LHXS02_%=\n" \
  "s_getreg_b32 s71, hwreg(HW_REG_IB_STS, 0, 4)\n" \
  "s_cmp_le_u32 s71, 7\n" \
  "v_mfma_f32_16x16x32_bf16 v[144:147], v[72:75], v[72:75], 0\n" \
  "s_cbranch_scc1 LHXS02_%=\n" \
  "s_getreg_b32 s71, hwreg(HW_REG_IB_STS, 0, 4)\n" \
  "s_cmp_le_u32 s71, 7\n" \
  "v_mfma_f32_16x16x32_bf16 v[144:147], v[72:75], v[72:75], 0\n" \
  "s_cbranch_scc1 LHXS02_%=\n" \
  "s_getreg_b32 s71, hwreg(HW_REG_IB_STS, 0, 4)\n" \
  "s_cmp_le_u32 s71, 7\n" \
  "v_mfma_f32_16x16x32_bf16 v[144:147], v[72:75], v[72:75], 0\n" \
  "s_cbranch_scc1 LHXS02_%=\n" \
  "s_getreg_b32 s71, hwreg(HW_REG_IB_STS, 0, 4)\n" \
  "s_cmp_le_u32 s71, 7\n" \
  "v_mfma_f32_16x16x32_bf16 v[144:147], v[72:75], v[72:75], 0\n" \
  "s_cbranch_scc1 LHXS02_%=\n" \
  "s_getreg_b32 s71, hwreg(HW_REG_IB_STS, 0, 4)\n" \
  "s_cmp_le_u32 s71, 7\n" \
  "v_mfma_f32_16x16x32_bf16 v[144:147], v[72:75], v[72:75], 0\n" \
  "s_cbranch_scc1 LHXS02_%=\n" \
  "LHXS02_%=:\n" \
  "s_waitcnt vmcnt(7)\n" \
  "LS202_%=:\n" \
  "v_dot2_f32_bf16 v176, v112, s22, 0\n" \
  "v_dot2_f32_bf16 v177, v112, s23, 0\n" \
  "v_dot2_f32_bf16 v178, v113, s22, 0\n" \
  "v_dot2_f32_bf16 v179, v113, s23, 0\n" \
  "v_dot2_f32_bf16 v180, v114, s22, 0\n" \
  "v_dot2_f32_bf16 v181, v114, s23, 0\n" \
  "v_dot2_f32_bf16 v182, v115, s22, 0\n" \
  "v_dot2_f32_bf16 v183, v115, s23, 0\n" \
  "v_dot2_f32_bf16 v176, v116, s22, v176\n" \
  "v_dot2_f32_bf16 v177, v116, s23, v177\n" \
  "v_dot2_f32_bf16 v178, v117, s22, v178\n" \
  "v_dot2_f32_bf16 v179, v117, s23, v179\n" \
  "v_dot2_f32_bf16 v180, v118, s22, v180\n" \
  "v_dot2_f32_bf16 v181, v118, s23, v181\n" \
  "v_dot2_f32_bf16 v182, v119, s22, v182\n" \
  "v_dot2_f32_bf16 v183, v119, s23, v183\n" \
  "v_exp_f32 v176, v176\n" \
  "v_exp_f32 v177, v177\n" \
  "v_exp_f32 v178, v178\n" \
  "v_exp_f32 v179, v179\n" \
  "s_waitcnt vmcnt(6)\n" \
  "v_mfma_f32_16x16x32_bf16 v[160:163], v[56:59], v[120:123], 0\n" \
  "v_exp_f32 v180, v180\n" \
  "v_mfma_f32_16x16x32_bf16 v[144:147], v[40:43], v[120:123], 0\n" \
  "v_exp_f32 v181, v181\n" \
  "v_exp_f32 v182, v182\n" \
  "v_exp_f32 v183, v183\n" \
  "v_cvt_pk_bf16_f32 v184, v176, v177\n" \
  "v_cvt_pk_bf16_f32 v185, v178, v179\n" \
  "v_cvt_pk_bf16_f32 v186, v180, v181\n" \
  "v_cvt_pk_bf16_f32 v187, v182, v183\n" \
  "s_nop 1\n" \
  "v_mfma_f32_16x16x32_bf16 v[160:163], v[72:75], v[184:187], v[160:163]\n" \
  "v_mfma_f32_16x16x32_bf16 v[164:167], v[76:79], v[184:187], 0\n" \
  "s_waitcnt vmcnt(5)\n" \
  "v_mfma_f32_16x16x32_bf16 v[164:167], v[60:63], v[124:127], v[164:167]\n" \
  "v_rcp_f32 v188, v162\n" \
  "v_mfma_f32_16x16x32_bf16 v[148:151], v[44:47], v[124:127], 0\n" \
  "v_mul_f32 v144, v188, v144\n" \
  "v_mul_f32 v145, v188, v145\n" \
  "v_mfma_f32_16x16x32_bf16 v[168:171], v[80:83], v[184:187], 0\n" \
  "s_waitcnt vmcnt(4)\n" \
  "v_rcp_f32 v189, v166\n" \
  "v_mfma_f32_16x16x32_bf16 v[168:171], v[64:67], v[128:131], v[168:171]\n" \
  "v_mul_f32 v146, v188, v146\n" \
  "v_mul_f32 v147, v188, v147\n" \
  "v_mfma_f32_16x16x32_bf16 v[152:155], v[48:51], v[128:131], 0\n" \
  "v_mul_f32 v160, v188, v160\n" \
  "v_mul_f32 v161, v188, v161\n" \
  "v_mfma_f32_16x16x32_bf16 v[172:175], v[84:87], v[184:187], 0\n" \
  "s_waitcnt vmcnt(3)\n" \
  "v_rcp_f32 v190, v170\n" \
  "v_mfma_f32_16x16x32_bf16 v[172:175], v[68:71], v[132:135], v[172:175]\n" \
  "v_cndmask_b32 v167, v188, v189, s[28:29]\n" \
  "v_cndmask_b32 v167, v167, v190, s[26:27]\n" \
  "v_mfma_f32_16x16x32_bf16 v[156:159], v[52:55], v[132:135], 0\n" \
  "v_fmac_f32 v144, v189, v148\n" \
  "v_fmac_f32 v145, v189, v149\n" \
  "v_fmac_f32 v146, v189, v150\n" \
  "v_fmac_f32 v147, v189, v151\n" \
  "v_rcp_f32 v191, v174\n" \
  "v_fmac_f32 v160, v189, v164\n" \
  "v_cndmask_b32 v167, v167, v191, s[24:25]\n" \
  "v_pk_mul_f32 v[176:177], v[176:177], v[166:167] op_sel:[0,1] op_sel_hi:[1,1]\n" \
  "v_pk_mul_f32 v[178:179], v[178:179], v[166:167] op_sel:[0,1] op_sel_hi:[1,1]\n" \
  "v_pk_mul_f32 v[180:181], v[180:181], v[166:167] op_sel:[0,1] op_sel_hi:[1,1]\n" \
  "v_pk_mul_f32 v[182:183], v[182:183], v[166:167] op_sel:[0,1] op_sel_hi:[1,1]\n" \
  "v_cmp_nlt_f32 vcc, 0x0d800000, v167\n" \
  "v_cvt_pk_bf16_f32 v184, v176, v177\n" \
  "v_cvt_pk_bf16_f32 v185, v178, v179\n" \
  "v_cvt_pk_bf16_f32 v186, v180, v181\n" \
  "v_cvt_pk_bf16_f32 v187, v182, v183\n" \
  "v_fmac_f32 v161, v189, v165\n" \
  "v_pk_fma_f32 v[160:161], v[168:169], v[190:191], v[160:161] op_sel:[0,0,0] op_sel_hi:[1,0,1]\n" \
  "v_pk_fma_f32 v[160:161], v[172:173], v[190:191], v[160:161] op_sel:[0,1,0] op_sel_hi:[1,1,1]\n" \
  "v_pk_fma_f32 v[144:145], v[152:153], v[190:191], v[144:145] op_sel:[0,0,0] op_sel_hi:[1,0,1]\n" \
  "v_pk_fma_f32 v[146:147], v[154:155], v[190:191], v[146:147] op_sel:[0,0,0] op_sel_hi:[1,0,1]\n" \
  "v_pk_fma_f32 v[144:145], v[156:157], v[190:191], v[144:145] op_sel:[0,1,0] op_sel_hi:[1,1,1]\n" \
  "v_pk_fma_f32 v[146:147], v[158:159], v[190:191], v[146:147] op_sel:[0,1,0] op_sel_hi:[1,1,1]\n" \
  "s_waitcnt vmcnt(1)\n" \
  "v_mfma_f32_16x16x32_bf16 v[160:163], v[140:143], v[184:187], v[160:163]\n" \
  "s_mov_b64 s[80:81], vcc\n" \
  "v_mfma_f32_16x16x32_bf16 v[144:147], v[136:139], v[184:187], v[144:147]\n" \
  "s_mov_b64 exec, s[30:31]\n" \
  "s_nop 2\n" \
  "ds_write_b64 v22, v[160:161]\n" \
  "v_cmp_u_f32 s[78:79], v160, v161\n" \
  "s_nop 1\n" \
  "ds_write2_b64 v21, v[144:145], v[146:147] offset1:16\n" \
  "v_add_f32 v188, v144, v145\n" \
  "v_add_f32 v189, v146, v147\n" \
  "v_cmp_u_f32 vcc, v188, v189\n" \
  "s_or_b64 s[80:81], s[80:81], s[78:79]\n" \
  "s_or_b64 vcc, vcc, s[80:81]\n" \
  "s_cbranch_vccnz LSF02_%=\n" \
  "LSOK02_%=:\n" \
  "s_mov_b64 exec, -1\n" \
  "s_waitcnt lgkmcnt(0)\n" \
  "s_barrier\n" \
  "s_cmp_eq_u32 s73, 1\n" \
  "s_cbranch_scc1 LWA02_%=\n" \
  "s_branch LDONE_%=\n" \
  "ds_read_b32 v188, v33\n" \
  "s_waitcnt lgkmcnt(0)\n" \
  "v_cmp_ne_u32 vcc, 0, v188\n" \
  "s_cbranch_vccnz LSR02_%=\n" \
  "LNL02_%=:\n" \
  "s_waitcnt lgkmcnt(0)\n" \
  "s_cmp_eq_u32 s73, 1\n" \
  "s_cbranch_scc1 LOOP02_%=\n" \
  "s_branch LDONE_%=\n" \
  "LSA02_%=:\n" \
  "ds_read_b32 v188, v33\n" \
  "s_waitcnt lgkmcnt(0)\n" \
  "v_cmp_ne_u32 vcc, 0, v188\n" \
  "s_cbranch_vccnz LSR02_%=\n" \
  "s_add_u32 s88, s88, s52\n" \
  "s_add_u32 s89, s89, s93\n" \
  "s_add_u32 s97, s97, 1\n" \
  "s_branch LWA02_%=\n" \
  "LSB02_%=:\n" \
  "s_add_u32 s88, s88, s52\n" \
  "s_add_u32 s89, s89, s93\n" \
  "s_add_u32 s97, s97, 1\n" \
  "s_branch LWB02_%=\n" \
  "LMF02_%=:\n" \
  "v_mbcnt_lo_u32_b32 v130, -1, 0\n" \
  "v_mbcnt_hi_u32_b32 v130, -1, v130\n" \
  "v_lshlrev_b32 v130, 4, v130\n" \
  "v_mov_b32 v128, 0\n" \
  "ds_read_b128 v[132:135], v130 offset:0\n" \
  "ds_read_b128 v[136:139], v130 offset:1024\n" \
  "ds_read_b128 v[140:143], v130 offset:2048\n" \
  "ds_read_b128 v[144:147], v130 offset:3072\n" \
  "ds_read_b128 v[148:151], v130 offset:4096\n" \
  "s_waitcnt lgkmcnt(0)\n" \
  "v_max3_u32 v128, v128, v132, v133\n" \
  "v_max3_u32 v128, v128, v134, v135\n" \
  "v_max3_u32 v128, v128, v136, v137\n" \
  "v_max3_u32 v128, v128, v138, v139\n" \
  "v_max3_u32 v128, v128, v140, v141\n" \
  "v_max3_u32 v128, v128, v142, v143\n" \
  "v_max3_u32 v128, v128, v144, v145\n" \
  "v_max3_u32 v128, v128, v146, v147\n" \
  "v_max3_u32 v128, v128, v148, v149\n" \
  "v_max3_u32 v128, v128, v150, v151\n" \
  "ds_read_b128 v[132:135], v130 offset:5120\n" \
  "ds_read_b128 v[136:139], v130 offset:6144\n" \
  "ds_read_b128 v[140:143], v130 offset:7168\n" \
  "ds_read_b128 v[144:147], v130 offset:8192\n" \
  "ds_read_b128 v[148:151], v130 offset:9216\n" \
  "s_waitcnt lgkmcnt(0)\n" \
  "v_max3_u32 v128, v128, v132, v133\n" \
  "v_max3_u32 v128, v128, v134, v135\n" \
  "v_max3_u32 v128, v128, v136, v137\n" \
  "v_max3_u32 v128, v128, v138, v139\n" \
  "v_max3_u32 v128, v128, v140, v141\n" \
  "v_max3_u32 v128, v128, v142, v143\n" \
  "v_max3_u32 v128, v128, v144, v145\n" \
  "v_max3_u32 v128, v128, v146, v147\n" \
  "v_max3_u32 v128, v128, v148, v149\n" \
  "v_max3_u32 v128, v128, v150, v151\n" \
  "v_cmp_eq_u32 vcc, -1, v128\n" \
  "s_cbranch_vccz LMOK02_%=\n" \
  "s_cmp_gt_u32 s58, 4000\n" \
  "s_cbranch_scc1 LMOK02_%=\n" \
  "s_add_u32 s58, s58, 1\n" \
  "s_sub_u32 s64, s64, 1\n" \
  "s_mov_b32 s66, s65\n" \
  "s_add_u32 s88, s88, s52\n" \
  "s_add_u32 s89, s89, s93\n" \
  "s_add_u32 s97, s97, 1\n" \
  "s_waitcnt vmcnt(0) lgkmcnt(0)\n" \
  "s_barrier\n" \
  "s_branch LOOP02_%=\n" \
  "LKE02_%=:\n" \
  "s_waitcnt lgkmcnt(0)\n" \
  "buffer_store_dwordx2 v[88:89], v14, s[36:39], s69 offen sc1\n" \
  "s_branch LDONE_%=\n" \
  "LFF02_%=:\n" \
  "s_add_u32 s71, s64, 0\n" \
  "s_min_u32 s71, s71, 63\n" \
  "s_lshr_b32 s72, s71, 4\n" \
  "s_lshl_b32 s72, s72, 13\n" \
  "s_lshl_b32 s71, s71, 9\n" \
  "s_add_u32 s71, s71, s72\n" \
  "LOF02_%=:\n" \
  "buffer_load_dword v35, v28, s[36:39], s71 offen sc1\n" \
  "s_waitcnt vmcnt(0)\n" \
  "v_cmp_gt_u32 vcc, 16, v35\n" \
  "s_cbranch_vccz LOXF02_%=\n" \
  "s_add_u32 s60, s60, 1\n" \
  "s_cmp_gt_u32 s60, 20000\n" \
  "s_cbranch_scc1 LOXF02_%=\n" \
  "s_sleep 16\n" \
  "s_branch LOF02_%=\n" \
  "LOXF02_%=:\n" \
  "s_branch LFOK02_%=\n" \
  "LSF02_%=:\n" \
  "s_getpc_b64 s[78:79]\n" \
  "s_branch LSF_%=\n" \
  "s_branch LSOK02_%=\n" \
  "LSR02_%=:\n" \
  "v_mov_b32 v188, 0\n" \
  "s_add_u32 s59, s59, 1\n" \
  "s_barrier\n" \
  "ds_write_b32 v33, v188\n" \
  "s_sub_u32 s88, s88, s51\n" \
  "s_sub_u32 s89, s89, s92\n" \
  "s_add_u32 s88, s88, s52\n" \
  "s_add_u32 s89, s89, s93\n" \
  "s_add_u32 s97, s97, 1\n" \
  "s_waitcnt lgkmcnt(0)\n" \
  "s_branch LB02_%=\n" \
  "LSTART03_%=:\n" \
  "LOOP03_%=:\n" \
  "v_readlane_b32 s70, v27, s64\n" \
  "s_mov_b32 s65, s66\n" \
  "s_xor_b32 s69, s66, 0x2000\n" \
  "s_add_u32 s64, s64, 1\n" \
  "v_mov_b32 v176, 0\n" \
  "v_mov_b32 v177, 0\n" \
  "v_mov_b32 v178, 0\n" \
  "v_mov_b32 v179, 0\n" \
  "v_mov_b32 v180, 0\n" \
  "v_mov_b32 v181, 0\n" \
  "v_mov_b32 v182, 0\n" \
  "v_mov_b32 v183, 0\n" \
  "v_readlane_b32 s66, v25, s64\n" \
  "v_readlane_b32 s68, v26, s64\n" \
  "s_add_u32 s91, s65, 81920\n" \
  "s_add_u32 s92, s65, 163840\n" \
  "s_add_u32 s93, s66, 81920\n" \
  "s_add_u32 s94, s66, 163840\n" \
  "s_mov_b32 m0, 36864\n" \
  "s_nop 0\n" \
  "s_barrier\n" \
  "buffer_load_dwordx4 v3, s[36:39], s70 offen sc1 lds\n" \
  "buffer_load_dwordx4 v3, s[36:39], s70 offen offset:1024 sc1 lds\n" \
  "buffer_load_dwordx4 v3, s[36:39], s70 offen offset:2048 sc1 lds\n" \
  "buffer_load_dwordx4 v3, s[36:39], s70 offen offset:3072 sc1 lds\n" \
  "LM03_%=:\n" \
  "s_barrier\n" \
  "ds_read_b128 v[112:115], v5\n" \
  "ds_read_b128 v[116:119], v5 offset:512\n" \
  "s_barrier\n" \
  "ds_read_b128 v[120:123], v5 offset:1024\n" \
  "ds_read_b128 v[124:127], v5 offset:1536\n" \
  "s_waitcnt lgkmcnt(3)\n" \
  "v_smfmac_f32_16x16x64_bf16 v[176:179], v[112:115], a[0:7], v0\n" \
  "v_smfmac_f32_16x16x64_bf16 v[180:183], v[112:115], a[160:167], v0\n" \
  "s_waitcnt lgkmcnt(2)\n" \
  "v_smfmac_f32_16x16x64_bf16 v[176:179], v[116:119], a[8:15], v0\n" \
  "s_barrier\n" \
  "ds_read_b128 v[128:131], v5 offset:2048\n" \
  "v_smfmac_f32_16x16x64_bf16 v[180:183], v[116:119], a[168:175], v0\n" \
  "s_waitcnt lgkmcnt(2)\n" \
  "ds_read_b128 v[132:135], v5 offset:2560\n" \
  "v_smfmac_f32_16x16x64_bf16 v[176:179], v[120:123], a[16:23], v0\n" \
  "ds_read_b128 v[136:139], v5 offset:3072\n" \
  "v_smfmac_f32_16x16x64_bf16 v[180:183], v[120:123], a[176:183], v0\n" \
  "s_waitcnt lgkmcnt(3)\n" \
  "ds_read_b128 v[140:143], v5 offset:3584\n" \
  "v_smfmac_f32_16x16x64_bf16 v[176:179], v[124:127], a[24:31], v0\n" \
  "s_barrier\n" \
  "ds_read_b128 v[144:147], v5 offset:4096\n" \
  "v_smfmac_f32_16x16x64_bf16 v[180:183], v[124:127], a[184:191], v0\n" \
  "s_waitcnt lgkmcnt(4)\n" \
  "ds_read_b128 v[148:151], v5 offset:4608\n" \
  "v_smfmac_f32_16x16x64_bf16 v[176:179], v[128:131], a[32:39], v0\n" \
  "ds_read_b128 v[152:155], v5 offset:5120\n" \
  "v_smfmac_f32_16x16x64_bf16 v[180:183], v[128:131], a[192:199], v0\n" \
  "s_waitcnt lgkmcnt(5)\n" \
  "ds_read_b128 v[156:159], v5 offset:5632\n" \
  "v_smfmac_f32_16x16x64_bf16 v[176:179], v[132:135], a[40:47], v0\n" \
  "s_barrier\n" \
  "ds_read_b128 v[160:163], v5 offset:6144\n" \
  "v_smfmac_f32_16x16x64_bf16 v[180:183], v[132:135], a[200:207], v0\n" \
  "s_waitcnt lgkmcnt(6)\n" \
  "ds_read_b128 v[164:167], v5 offset:6656\n" \
  "v_smfmac_f32_16x16x64_bf16 v[176:179], v[136:139], a[48:55], v0\n" \
  "ds_read_b128 v[168:171], v5 offset:7168\n" \
  "v_smfmac_f32_16x16x64_bf16 v[180:183], v[136:139], a[208:215], v0\n" \
  "s_waitcnt lgkmcnt(7)\n" \
  "ds_read_b128 v[172:175], v5 offset:7680\n" \
  "v_smfmac_f32_16x16x64_bf16 v[176:179], v[140:143], a[56:63], v0\n" \
  "s_barrier\n" \
  "ds_read2_b32 v[112:113], v6 offset0:0 offset1:32\n" \
  "v_smfmac_f32_16x16x64_bf16 v[180:183], v[140:143], a[216:223], v0\n" \
  "s_waitcnt lgkmcnt(8)\n" \
  "ds_read2_b32 v[114:115], v6 offset0:64 offset1:96\n" \
  "v_smfmac_f32_16x16x64_bf16 v[176:179], v[144:147], a[64:71], v0\n" \
  "s_waitcnt lgkmcnt(8)\n" \
  "ds_read2_b32 v[116:117], v6 offset0:128 offset1:160\n" \
  "v_smfmac_f32_16x16x64_bf16 v[176:179], v[148:151], a[72:79], v0\n" \
  "s_waitcnt lgkmcnt(8)\n" \
  "ds_read2_b32 v[118:119], v6 offset0:192 offset1:224\n" \
  "v_smfmac_f32_16x16x64_bf16 v[176:179], v[152:155], a[80:87], v0\n" \
  "s_waitcnt lgkmcnt(8)\n" \
  "ds_read2_b32 v[120:121], v7 offset0:0 offset1:32\n" \
  "v_smfmac_f32_16x16x64_bf16 v[176:179], v[156:159], a[88:95], v0\n" \
  "s_waitcnt lgkmcnt(8)\n" \
  "ds_read2_b32 v[122:123], v7 offset0:64 offset1:96\n" \
  "v_smfmac_f32_16x16x64_bf16 v[176:179], v[160:163], a[96:103], v0\n" \
  "s_waitcnt lgkmcnt(8)\n" \
  "ds_read2_b32 v[124:125], v7 offset0:128 offset1:160\n" \
  "v_smfmac_f32_16x16x64_bf16 v[176:179], v[164:167], a[104:111], v0\n" \
  "s_waitcnt lgkmcnt(8)\n" \
  "ds_read2_b32 v[126:127], v7 offset0:192 offset1:224\n" \
  "v_smfmac_f32_16x16x64_bf16 v[176:179], v[168:171], a[112:119], v0\n" \
  "s_waitcnt lgkmcnt(8)\n" \
  "v_smfmac_f32_16x16x64_bf16 v[176:179], v[172:175], a[120:127], v0\n" \
  "s_waitcnt lgkmcnt(6)\n" \
  "v_smfmac_f32_16x16x64_bf16 v[176:179], v[112:115], a[128:135], v0\n" \
  "s_waitcnt lgkmcnt(4)\n" \
  "v_smfmac_f32_16x16x64_bf16 v[176:179], v[116:119], a[136:143], v0\n" \
  "s_waitcnt lgkmcnt(2)\n" \
  "v_smfmac_f32_16x16x64_bf16 v[176:179], v[120:123], a[144:151], v0\n" \
  "s_waitcnt lgkmcnt(0)\n" \
  "v_smfmac_f32_16x16x64_bf16 v[176:179], v[124:127], a[152:159], v0\n" \
  "v_smfmac_f32_16x16x64_bf16 v[180:183], v[144:147], a[224:231], v0\n" \
  "v_smfmac_f32_16x16x64_bf16 v[180:183], v[148:151], a[232:239], v0\n" \
  "v_cvt_pk_bf16_f32 v184, v176, v177\n" \
  "v_smfmac_f32_16x16x64_bf16 v[180:183], v[152:155], a[240:247], v0\n" \
  "v_cvt_pk_bf16_f32 v185, v178, v179\n" \
  "v_smfmac_f32_16x16x64_bf16 v[180:183], v[156:159], a[248:255], v0\n" \
  "ds_write_b64 v8, v[184:185]\n" \
  "v_add_f32 v186, v176, v177\n" \
  "v_smfmac_f32_16x16x64_bf16 v[180:183], v[160:163], v[192:199], v0\n" \
  "v_add_f32 v187, v178, v179\n" \
  "v_smfmac_f32_16x16x64_bf16 v[180:183], v[164:167], v[200:207], v0\n" \
  "v_cmp_u_f32 vcc, v186, v187\n" \
  "v_smfmac_f32_16x16x64_bf16 v[180:183], v[168:171], v[208:215], v0\n" \
  "v_smfmac_f32_16x16x64_bf16 v[180:183], v[172:175], v[216:223], v0\n" \
  "s_cbranch_vccnz LMF03_%=\n" \
  "LMOK03_%=:\n" \
  "v_smfmac_f32_16x16x64_bf16 v[180:183], v[112:115], v[224:231], v0\n" \
  "s_waitcnt lgkmcnt(0)\n" \
  "s_barrier\n" \
  "v_smfmac_f32_16x16x64_bf16 v[180:183], v[116:119], v[232:239], v0\n" \
  "v_smfmac_f32_16x16x64_bf16 v[180:183], v[120:123], v[240:247], v0\n" \
  "v_smfmac_f32_16x16x64_bf16 v[180:183], v[124:127], v[248:255], v0\n" \
  "s_nop 3\n" \
  "v_mov_b32 v31, v31\n" \
  "s_nop 2\n" \
  "v_cvt_pk_bf16_f32 v186, v180, v181\n" \
  "v_cvt_pk_bf16_f32 v187, v182, v183\n" \
  "ds_write_b64 v9, v[186:187]\n" \
  "v_mov_b32 v31, v31\n" \
  "s_nop 2\n" \
  "v_mov_b32 v31, v31\n" \
  "s_nop 2\n" \
  "v_mov_b32 v31, v31\n" \
  "s_nop 2\n" \
  "s_waitcnt lgkmcnt(0)\n" \
  "s_barrier\n" \
  "ds_read_b64_tr_b16 v[144:145], v11\n" \
  "ds_read_b64_tr_b16 v[146:147], v11 offset:512\n" \
  "ds_read_b64_tr_b16 v[148:149], v11 offset:1024\n" \
  "ds_read_b64_tr_b16 v[150:151], v11 offset:1536\n" \
  "v_mov_b32 v31, v31\n" \
  "s_nop 2\n" \
  "v_mov_b32 v31, v31\n" \
  "s_nop 2\n" \
  "v_mov_b32 v31, v31\n" \
  "s_nop 0\n" \
  "s_waitcnt lgkmcnt(2)\n" \
  "v_mfma_f32_16x16x32_bf16 v[188:191], v[144:147], v[144:147], 0\n" \
  "s_waitcnt lgkmcnt(0)\n" \
  "v_mfma_f32_16x16x32_bf16 v[188:191], v[148:151], v[148:151], v[188:191]\n" \
  "s_nop 0\n" \
  "ds_read_b64_tr_b16 v[88:89], v12\n" \
  "s_nop 1\n" \
  "v_mov_b32 v31, v31\n" \
  "s_nop 2\n" \
  "v_pk_mul_f32 v[188:189], v[188:189], s[42:43]\n" \
  "v_pk_mul_f32 v[190:191], v[190:191], s[42:43]\n" \
  "v_cvt_pk_bf16_f32 v152, v188, v189\n" \
  "v_cvt_pk_bf16_f32 v153, v190, v191\n" \
  "buffer_store_dwordx2 v[152:153], v13, s[36:39], s65 offen sc1\n" \
  "buffer_store_dwordx2 v[152:153], v13, s[36:39], s91 offen sc1\n" \
  "buffer_store_dwordx2 v[152:153], v13, s[36:39], s92 offen sc1\n" \
  "s_mov_b32 s67, s66\n" \
  "s_cmp_eq_u32 s64, 64\n" \
  "s_cbranch_scc1 LKE03_%=\n" \
  "LB03_%=:\n" \
  "s_barrier\n" \
  "buffer_load_dwordx4 v[116:119], v15, s[36:39], s65 offen offset:128 sc1\n" \
  "s_barrier\n" \
  "buffer_load_dwordx4 v[112:115], v15, s[36:39], s65 offen sc1\n" \
  "s_barrier\n" \
  "buffer_load_dwordx4 v[120:123], v16, s[36:39], s65 offen sc1\n" \
  "s_barrier\n" \
  "buffer_load_dwordx4 v[124:127], v16, s[36:39], s65 offen offset:512 sc1\n" \
  "s_barrier\n" \
  "buffer_load_dwordx4 v[128:131], v16, s[36:39], s65 offen offset:1024 sc1\n" \
  "s_waitcnt vmcnt(8)\n" \
  "s_barrier\n" \
  "buffer_load_dwordx4 v[132:135], v16, s[36:39], s65 offen offset:1536 sc1\n" \
  "ds_read_b128 v[40:43], v19\n" \
  "ds_read_b128 v[44:47], v19 offset:512\n" \
  "ds_read_b128 v[48:51], v19 offset:1024\n" \
  "ds_read_b128 v[52:55], v19 offset:1536\n" \
  "s_mov_b64 exec, s[46:47]\n" \
  "ds_read_b128 v[56:59], v20\n" \
  "ds_read_b128 v[60:63], v20 offset:128\n" \
  "ds_read_b128 v[64:67], v20 offset:256\n" \
  "ds_read_b128 v[68:71], v20 offset:384\n" \
  "s_mov_b64 exec, -1\n" \
  "s_nop 15\n" \
  "s_nop 1\n" \
  "buffer_load_dwordx4 v[136:139], v17, s[36:39], s65 offen sc1\n" \
  "buffer_load_dwordx4 v[140:143], v18, s[36:39], s65 offen sc1\n" \
  "s_barrier\n" \
  "s_waitcnt lgkmcnt(0)\n" \
  "buffer_store_dwordx2 v[88:89], v14, s[36:39], s69 offen sc1\n" \
  "s_waitcnt lgkmcnt(0)\n" \
  "s_getreg_b32 s71, hwreg(HW_REG_IB_STS, 0, 4)\n" \
  "s_cmp_le_u32 s71, 7\n" \
  "v_mfma_f32_16x16x32_bf16 v[144:147], v[72:75], v[72:75], 0\n" \
  "s_cbranch_scc1 LHXS03_%=\n" \
  "s_getreg_b32 s71, hwreg(HW_REG_IB_STS, 0, 4)\n" \
  "s_cmp_le_u32 s71, 7\n" \
  "v_mfma_f32_16x16x32_bf16 v[144:147], v[72:75], v[72:75], 0\n" \
  "s_cbranch_scc1 LHXS03_%=\n" \
  "s_getreg_b32 s71, hwreg(HW_REG_IB_STS, 0, 4)\n" \
  "s_cmp_le_u32 s71, 7\n" \
  "v_mfma_f32_16x16x32_bf16 v[144:147], v[72:75], v[72:75], 0\n" \
  "s_cbranch_scc1 LHXS03_%=\n" \
  "s_getreg_b32 s71, hwreg(HW_REG_IB_STS, 0, 4)\n" \
  "s_cmp_le_u32 s71, 7\n" \
  "v_mfma_f32_16x16x32_bf16 v[144:147], v[72:75], v[72:75], 0\n" \
  "s_cbranch_scc1 LHXS03_%=\n" \
  "s_getreg_b32 s71, hwreg(HW_REG_IB_STS, 0, 4)\n" \
  "s_cmp_le_u32 s71, 7\n" \
  "v_mfma_f32_16x16x32_bf16 v[144:147], v[72:75], v[72:75], 0\n" \
  "s_cbranch_scc1 LHXS03_%=\n" \
  "s_getreg_b32 s71, hwreg(HW_REG_IB_STS, 0, 4)\n" \
  "s_cmp_le_u32 s71, 7\n" \
  "v_mfma_f32_16x16x32_bf16 v[144:147], v[72:75], v[72:75], 0\n" \
  "s_cbranch_scc1 LHXS03_%=\n" \
  "s_getreg_b32 s71, hwreg(HW_REG_IB_STS, 0, 4)\n" \
  "s_cmp_le_u32 s71, 7\n" \
  "v_mfma_f32_16x16x32_bf16 v[144:147], v[72:75], v[72:75], 0\n" \
  "s_cbranch_scc1 LHXS03_%=\n" \
  "s_getreg_b32 s71, hwreg(HW_REG_IB_STS, 0, 4)\n" \
  "s_cmp_le_u32 s71, 7\n" \
  "v_mfma_f32_16x16x32_bf16 v[144:147], v[72:75], v[72:75], 0\n" \
  "s_cbranch_scc1 LHXS03_%=\n" \
  "s_getreg_b32 s71, hwreg(HW_REG_IB_STS, 0, 4)\n" \
  "s_cmp_le_u32 s71, 7\n" \
  "v_mfma_f32_16x16x32_bf16 v[144:147], v[72:75], v[72:75], 0\n" \
  "s_cbranch_scc1 LHXS03_%=\n" \
  "s_getreg_b32 s71, hwreg(HW_REG_IB_STS, 0, 4)\n" \
  "s_cmp_le_u32 s71, 7\n" \
  "v_mfma_f32_16x16x32_bf16 v[144:147], v[72:75], v[72:75], 0\n" \
  "s_cbranch_scc1 LHXS03_%=\n" \
  "s_getreg_b32 s71, hwreg(HW_REG_IB_STS, 0, 4)\n" \
  "s_cmp_le_u32 s71, 7\n" \
  "v_mfma_f32_16x16x32_bf16 v[144:147], v[72:75], v[72:75], 0\n" \
  "s_cbranch_scc1 LHXS03_%=\n" \
  "s_getreg_b32 s71, hwreg(HW_REG_IB_STS, 0, 4)\n" \
  "s_cmp_le_u32 s71, 7\n" \
  "v_mfma_f32_16x16x32_bf16 v[144:147], v[72:75], v[72:75], 0\n" \
  "s_cbranch_scc1 LHXS03_%=\n" \
  "s_getreg_b32 s71, hwreg(HW_REG_IB_STS, 0, 4)\n" \
  "s_cmp_le_u32 s71, 7\n" \
  "v_mfma_f32_16x16x32_bf16 v[144:147], v[72:75], v[72:75], 0\n" \
  "s_cbranch_scc1 LHXS03_%=\n" \
  "s_getreg_b32 s71, hwreg(HW_REG_IB_STS, 0, 4)\n" \
  "s_cmp_le_u32 s71, 7\n" \
  "v_mfma_f32_16x16x32_bf16 v[144:147], v[72:75], v[72:75], 0\n" \
  "s_cbranch_scc1 LHXS03_%=\n" \
  "s_getreg_b32 s71, hwreg(HW_REG_IB_STS, 0, 4)\n" \
  "s_cmp_le_u32 s71, 7\n" \
  "v_mfma_f32_16x16x32_bf16 v[144:147], v[72:75], v[72:75], 0\n" \
  "s_cbranch_scc1 LHXS03_%=\n" \
  "s_getreg_b32 s71, hwreg(HW_REG_IB_STS, 0, 4)\n" \
  "s_cmp_le_u32 s71, 7\n" \
  "v_mfma_f32_16x16x32_bf16 v[144:147], v[72:75], v[72:75], 0\n" \
  "s_cbranch_scc1 LHXS03_%=\n" \
  "s_getreg_b32 s71, hwreg(HW_REG_IB_STS, 0, 4)\n" \
  "s_cmp_le_u32 s71, 7\n" \
  "v_mfma_f32_16x16x32_bf16 v[144:147], v[72:75], v[72:75], 0\n" \
  "s_cbranch_scc1 LHXS03_%=\n" \
  "s_getreg_b32 s71, hwreg(HW_REG_IB_STS, 0, 4)\n" \
  "s_cmp_le_u32 s71, 7\n" \
  "v_mfma_f32_16x16x32_bf16 v[144:147], v[72:75], v[72:75], 0\n" \
  "s_cbranch_scc1 LHXS03_%=\n" \
  "s_getreg_b32 s71, hwreg(HW_REG_IB_STS, 0, 4)\n" \
  "s_cmp_le_u32 s71, 7\n" \
  "v_mfma_f32_16x16x32_bf16 v[144:147], v[72:75], v[72:75], 0\n" \
  "s_cbranch_scc1 LHXS03_%=\n" \
  "s_getreg_b32 s71, hwreg(HW_REG_IB_STS, 0, 4)\n" \
  "s_cmp_le_u32 s71, 7\n" \
  "v_mfma_f32_16x16x32_bf16 v[144:147], v[72:75], v[72:75], 0\n" \
  "s_cbranch_scc1 LHXS03_%=\n" \
  "LHXS03_%=:\n" \
  "s_waitcnt vmcnt(7)\n" \
  "LS203_%=:\n" \
  "v_dot2_f32_bf16 v176, v112, s22, 0\n" \
  "v_dot2_f32_bf16 v177, v112, s23, 0\n" \
  "v_dot2_f32_bf16 v178, v113, s22, 0\n" \
  "v_dot2_f32_bf16 v179, v113, s23, 0\n" \
  "v_dot2_f32_bf16 v180, v114, s22, 0\n" \
  "v_dot2_f32_bf16 v181, v114, s23, 0\n" \
  "v_dot2_f32_bf16 v182, v115, s22, 0\n" \
  "v_dot2_f32_bf16 v183, v115, s23, 0\n" \
  "v_dot2_f32_bf16 v176, v116, s22, v176\n" \
  "v_dot2_f32_bf16 v177, v116, s23, v177\n" \
  "v_dot2_f32_bf16 v178, v117, s22, v178\n" \
  "v_dot2_f32_bf16 v179, v117, s23, v179\n" \
  "v_dot2_f32_bf16 v180, v118, s22, v180\n" \
  "v_dot2_f32_bf16 v181, v118, s23, v181\n" \
  "v_dot2_f32_bf16 v182, v119, s22, v182\n" \
  "v_dot2_f32_bf16 v183, v119, s23, v183\n" \
  "v_exp_f32 v176, v176\n" \
  "v_exp_f32 v177, v177\n" \
  "v_exp_f32 v178, v178\n" \
  "v_exp_f32 v179, v179\n" \
  "s_waitcnt vmcnt(6)\n" \
  "v_mfma_f32_16x16x32_bf16 v[160:163], v[56:59], v[120:123], 0\n" \
  "v_exp_f32 v180, v180\n" \
  "v_mfma_f32_16x16x32_bf16 v[144:147], v[40:43], v[120:123], 0\n" \
  "v_exp_f32 v181, v181\n" \
  "v_exp_f32 v182, v182\n" \
  "v_exp_f32 v183, v183\n" \
  "v_cvt_pk_bf16_f32 v184, v176, v177\n" \
  "v_cvt_pk_bf16_f32 v185, v178, v179\n" \
  "v_cvt_pk_bf16_f32 v186, v180, v181\n" \
  "v_cvt_pk_bf16_f32 v187, v182, v183\n" \
  "s_nop 1\n" \
  "v_mfma_f32_16x16x32_bf16 v[160:163], v[72:75], v[184:187], v[160:163]\n" \
  "v_mfma_f32_16x16x32_bf16 v[164:167], v[76:79], v[184:187], 0\n" \
  "s_waitcnt vmcnt(5)\n" \
  "v_mfma_f32_16x16x32_bf16 v[164:167], v[60:63], v[124:127], v[164:167]\n" \
  "v_rcp_f32 v188, v162\n" \
  "v_mfma_f32_16x16x32_bf16 v[148:151], v[44:47], v[124:127], 0\n" \
  "v_mul_f32 v144, v188, v144\n" \
  "v_mul_f32 v145, v188, v145\n" \
  "v_mfma_f32_16x16x32_bf16 v[168:171], v[80:83], v[184:187], 0\n" \
  "s_waitcnt vmcnt(4)\n" \
  "v_rcp_f32 v189, v166\n" \
  "v_mfma_f32_16x16x32_bf16 v[168:171], v[64:67], v[128:131], v[168:171]\n" \
  "v_mul_f32 v146, v188, v146\n" \
  "v_mul_f32 v147, v188, v147\n" \
  "v_mfma_f32_16x16x32_bf16 v[152:155], v[48:51], v[128:131], 0\n" \
  "v_mul_f32 v160, v188, v160\n" \
  "v_mul_f32 v161, v188, v161\n" \
  "v_mfma_f32_16x16x32_bf16 v[172:175], v[84:87], v[184:187], 0\n" \
  "s_waitcnt vmcnt(3)\n" \
  "v_rcp_f32 v190, v170\n" \
  "v_mfma_f32_16x16x32_bf16 v[172:175], v[68:71], v[132:135], v[172:175]\n" \
  "v_cndmask_b32 v167, v188, v189, s[28:29]\n" \
  "v_cndmask_b32 v167, v167, v190, s[26:27]\n" \
  "v_mfma_f32_16x16x32_bf16 v[156:159], v[52:55], v[132:135], 0\n" \
  "v_fmac_f32 v144, v189, v148\n" \
  "v_fmac_f32 v145, v189, v149\n" \
  "v_fmac_f32 v146, v189, v150\n" \
  "v_fmac_f32 v147, v189, v151\n" \
  "v_rcp_f32 v191, v174\n" \
  "v_fmac_f32 v160, v189, v164\n" \
  "v_cndmask_b32 v167, v167, v191, s[24:25]\n" \
  "v_pk_mul_f32 v[176:177], v[176:177], v[166:167] op_sel:[0,1] op_sel_hi:[1,1]\n" \
  "v_pk_mul_f32 v[178:179], v[178:179], v[166:167] op_sel:[0,1] op_sel_hi:[1,1]\n" \
  "v_pk_mul_f32 v[180:181], v[180:181], v[166:167] op_sel:[0,1] op_sel_hi:[1,1]\n" \
  "v_pk_mul_f32 v[182:183], v[182:183], v[166:167] op_sel:[0,1] op_sel_hi:[1,1]\n" \
  "v_cmp_nlt_f32 vcc, 0x0d800000, v167\n" \
  "v_cvt_pk_bf16_f32 v184, v176, v177\n" \
  "v_cvt_pk_bf16_f32 v185, v178, v179\n" \
  "v_cvt_pk_bf16_f32 v186, v180, v181\n" \
  "v_cvt_pk_bf16_f32 v187, v182, v183\n" \
  "v_fmac_f32 v161, v189, v165\n" \
  "v_pk_fma_f32 v[160:161], v[168:169], v[190:191], v[160:161] op_sel:[0,0,0] op_sel_hi:[1,0,1]\n" \
  "v_pk_fma_f32 v[160:161], v[172:173], v[190:191], v[160:161] op_sel:[0,1,0] op_sel_hi:[1,1,1]\n" \
  "v_pk_fma_f32 v[144:145], v[152:153], v[190:191], v[144:145] op_sel:[0,0,0] op_sel_hi:[1,0,1]\n" \
  "v_pk_fma_f32 v[146:147], v[154:155], v[190:191], v[146:147] op_sel:[0,0,0] op_sel_hi:[1,0,1]\n" \
  "v_pk_fma_f32 v[144:145], v[156:157], v[190:191], v[144:145] op_sel:[0,1,0] op_sel_hi:[1,1,1]\n" \
  "v_pk_fma_f32 v[146:147], v[158:159], v[190:191], v[146:147] op_sel:[0,1,0] op_sel_hi:[1,1,1]\n" \
  "s_waitcnt vmcnt(1)\n" \
  "v_mfma_f32_16x16x32_bf16 v[160:163], v[140:143], v[184:187], v[160:163]\n" \
  "s_mov_b64 s[80:81], vcc\n" \
  "v_mfma_f32_16x16x32_bf16 v[144:147], v[136:139], v[184:187], v[144:147]\n" \
  "s_mov_b64 exec, s[30:31]\n" \
  "s_nop 2\n" \
  "ds_write_b64 v22, v[160:161]\n" \
  "v_cmp_u_f32 s[78:79], v160, v161\n" \
  "s_nop 1\n" \
  "ds_write2_b64 v21, v[144:145], v[146:147] offset1:16\n" \
  "v_add_f32 v188, v144, v145\n" \
  "v_add_f32 v189, v146, v147\n" \
  "v_cmp_u_f32 vcc, v188, v189\n" \
  "s_or_b64 s[80:81], s[80:81], s[78:79]\n" \
  "s_or_b64 vcc, vcc, s[80:81]\n" \
  "s_cbranch_vccnz LSF03_%=\n" \
  "LSOK03_%=:\n" \
  "s_mov_b64 exec, -1\n" \
  "s_waitcnt lgkmcnt(0)\n" \
  "s_barrier\n" \
  "ds_read_b32 v188, v33\n" \
  "ds_read_b128 v[120:123], v23\n" \
  "ds_read_b128 v[124:127], v23 offset:768\n" \
  "ds_read_b128 v[128:131], v23 offset:1536\n" \
  "ds_read_b128 v[132:135], v23 offset:2304\n" \
  "s_waitcnt lgkmcnt(4)\n" \
  "v_cmp_ne_u32 vcc, 0, v188\n" \
  "s_waitcnt lgkmcnt(2)\n" \
  "v_pk_add_f32 v[120:121], v[120:121], v[122:123]\n" \
  "v_pk_add_f32 v[124:125], v[124:125], v[126:127]\n" \
  "v_pk_add_f32 v[120:121], v[120:121], v[124:125]\n" \
  "s_waitcnt lgkmcnt(1)\n" \
  "v_pk_add_f32 v[128:129], v[128:129], v[130:131]\n" \
  "v_pk_add_f32 v[120:121], v[120:121], v[128:129]\n" \
  "s_waitcnt lgkmcnt(0)\n" \
  "v_pk_add_f32 v[132:133], v[132:133], v[134:135]\n" \
  "v_pk_add_f32 v[120:121], v[120:121], v[132:133]\n" \
  "v_cvt_pk_bf16_f32 v120, v120, v121\n" \
  "s_cbranch_vccnz LSR03_%=\n" \
  "buffer_store_dword v120, v24, s[36:39], s66 offen sc1\n" \
  "buffer_store_dword v120, v24, s[36:39], s93 offen sc1\n" \
  "buffer_store_dword v120, v24, s[36:39], s94 offen sc1\n" \
  "LNL03_%=:\n" \
  "s_waitcnt lgkmcnt(0)\n" \
  "s_cmp_eq_u32 s73, 1\n" \
  "s_cbranch_scc1 LOOP03_%=\n" \
  "s_branch LDONE_%=\n" \
  "LMF03_%=:\n" \
  "v_mbcnt_lo_u32_b32 v130, -1, 0\n" \
  "v_mbcnt_hi_u32_b32 v130, -1, v130\n" \
  "v_lshlrev_b32 v130, 4, v130\n" \
  "v_mov_b32 v128, 0\n" \
  "ds_read_b128 v[132:135], v130 offset:0\n" \
  "ds_read_b128 v[136:139], v130 offset:1024\n" \
  "ds_read_b128 v[140:143], v130 offset:2048\n" \
  "ds_read_b128 v[144:147], v130 offset:3072\n" \
  "ds_read_b128 v[148:151], v130 offset:4096\n" \
  "s_waitcnt lgkmcnt(0)\n" \
  "v_max3_u32 v128, v128, v132, v133\n" \
  "v_max3_u32 v128, v128, v134, v135\n" \
  "v_max3_u32 v128, v128, v136, v137\n" \
  "v_max3_u32 v128, v128, v138, v139\n" \
  "v_max3_u32 v128, v128, v140, v141\n" \
  "v_max3_u32 v128, v128, v142, v143\n" \
  "v_max3_u32 v128, v128, v144, v145\n" \
  "v_max3_u32 v128, v128, v146, v147\n" \
  "v_max3_u32 v128, v128, v148, v149\n" \
  "v_max3_u32 v128, v128, v150, v151\n" \
  "ds_read_b128 v[132:135], v130 offset:5120\n" \
  "ds_read_b128 v[136:139], v130 offset:6144\n" \
  "ds_read_b128 v[140:143], v130 offset:7168\n" \
  "ds_read_b128 v[144:147], v130 offset:8192\n" \
  "ds_read_b128 v[148:151], v130 offset:9216\n" \
  "s_waitcnt lgkmcnt(0)\n" \
  "v_max3_u32 v128, v128, v132, v133\n" \
  "v_max3_u32 v128, v128, v134, v135\n" \
  "v_max3_u32 v128, v128, v136, v137\n" \
  "v_max3_u32 v128, v128, v138, v139\n" \
  "v_max3_u32 v128, v128, v140, v141\n" \
  "v_max3_u32 v128, v128, v142, v143\n" \
  "v_max3_u32 v128, v128, v144, v145\n" \
  "v_max3_u32 v128, v128, v146, v147\n" \
  "v_max3_u32 v128, v128, v148, v149\n" \
  "v_max3_u32 v128, v128, v150, v151\n" \
  "v_cmp_eq_u32 vcc, -1, v128\n" \
  "s_cbranch_vccz LMOK03_%=\n" \
  "s_cmp_gt_u32 s58, 4000\n" \
  "s_cbranch_scc1 LMOK03_%=\n" \
  "s_add_u32 s58, s58, 1\n" \
  "s_sub_u32 s64, s64, 1\n" \
  "s_mov_b32 s66, s65\n" \
  "s_waitcnt vmcnt(0) lgkmcnt(0)\n" \
  "s_barrier\n" \
  "s_branch LOOP03_%=\n" \
  "LKE03_%=:\n" \
  "s_waitcnt lgkmcnt(0)\n" \
  "buffer_store_dwordx2 v[88:89], v14, s[36:39], s69 offen sc1\n" \
  "s_branch LDONE_%=\n" \
  "LSF03_%=:\n" \
  "s_getpc_b64 s[78:79]\n" \
  "s_branch LSF_%=\n" \
  "s_branch LSOK03_%=\n" \
  "LSR03_%=:\n" \
  "v_mov_b32 v188, 0\n" \
  "s_add_u32 s59, s59, 1\n" \
  "s_barrier\n" \
  "ds_write_b32 v33, v188\n" \
  "s_waitcnt lgkmcnt(0)\n" \
  "s_branch LB03_%=\n" \
  "LSTART10_%=:\n" \
  "LOOP10_%=:\n" \
  "v_readlane_b32 s70, v27, s64\n" \
  "s_mov_b32 s65, s66\n" \
  "s_xor_b32 s69, s66, 0x2000\n" \
  "s_add_u32 s64, s64, 1\n" \
  "v_mov_b32 v176, 0\n" \
  "v_mov_b32 v177, 0\n" \
  "v_mov_b32 v178, 0\n" \
  "v_mov_b32 v179, 0\n" \
  "v_mov_b32 v180, 0\n" \
  "v_mov_b32 v181, 0\n" \
  "v_mov_b32 v182, 0\n" \
  "v_mov_b32 v183, 0\n" \
  "v_readlane_b32 s66, v25, s64\n" \
  "v_readlane_b32 s68, v26, s64\n" \
  "s_add_u32 s91, s65, 81920\n" \
  "s_add_u32 s92, s65, 163840\n" \
  "s_mov_b32 m0, 20480\n" \
  "s_add_u32 s72, s70, 4096\n" \
  "s_barrier\n" \
  "buffer_load_dwordx4 v3, s[36:39], s70 offen sc1 lds\n" \
  "buffer_load_dwordx4 v3, s[36:39], s70 offen offset:1024 sc1 lds\n" \
  "buffer_load_dwordx4 v3, s[36:39], s70 offen offset:2048 sc1 lds\n" \
  "buffer_load_dwordx4 v3, s[36:39], s70 offen offset:3072 sc1 lds\n" \
  "LM10_%=:\n" \
  "s_barrier\n" \
  "ds_read_b128 v[112:115], v5\n" \
  "ds_read_b128 v[116:119], v5 offset:512\n" \
  "s_barrier\n" \
  "ds_read_b128 v[120:123], v5 offset:1024\n" \
  "ds_read_b128 v[124:127], v5 offset:1536\n" \
  "s_waitcnt lgkmcnt(3)\n" \
  "v_smfmac_f32_16x16x64_bf16 v[176:179], v[112:115], a[0:7], v0\n" \
  "v_smfmac_f32_16x16x64_bf16 v[180:183], v[112:115], a[160:167], v0\n" \
  "s_waitcnt lgkmcnt(2)\n" \
  "v_smfmac_f32_16x16x64_bf16 v[176:179], v[116:119], a[8:15], v0\n" \
  "s_barrier\n" \
  "ds_read_b128 v[128:131], v5 offset:2048\n" \
  "v_smfmac_f32_16x16x64_bf16 v[180:183], v[116:119], a[168:175], v0\n" \
  "s_waitcnt lgkmcnt(2)\n" \
  "ds_read_b128 v[132:135], v5 offset:2560\n" \
  "v_smfmac_f32_16x16x64_bf16 v[176:179], v[120:123], a[16:23], v0\n" \
  "ds_read_b128 v[136:139], v5 offset:3072\n" \
  "v_smfmac_f32_16x16x64_bf16 v[180:183], v[120:123], a[176:183], v0\n" \
  "s_waitcnt lgkmcnt(3)\n" \
  "ds_read_b128 v[140:143], v5 offset:3584\n" \
  "v_smfmac_f32_16x16x64_bf16 v[176:179], v[124:127], a[24:31], v0\n" \
  "s_barrier\n" \
  "ds_read_b128 v[144:147], v5 offset:4096\n" \
  "v_smfmac_f32_16x16x64_bf16 v[180:183], v[124:127], a[184:191], v0\n" \
  "s_mov_b32 m0, 24576\n" \
  "s_waitcnt lgkmcnt(4)\n" \
  "ds_read_b128 v[148:151], v5 offset:4608\n" \
  "v_smfmac_f32_16x16x64_bf16 v[176:179], v[128:131], a[32:39], v0\n" \
  "ds_read_b128 v[152:155], v5 offset:5120\n" \
  "buffer_load_dwordx4 v3, s[36:39], s72 offen sc1 lds\n" \
  "v_smfmac_f32_16x16x64_bf16 v[180:183], v[128:131], a[192:199], v0\n" \
  "s_waitcnt lgkmcnt(5)\n" \
  "ds_read_b128 v[156:159], v5 offset:5632\n" \
  "v_smfmac_f32_16x16x64_bf16 v[176:179], v[132:135], a[40:47], v0\n" \
  "s_barrier\n" \
  "ds_read_b128 v[160:163], v5 offset:6144\n" \
  "buffer_load_dwordx4 v3, s[36:39], s72 offen offset:1024 sc1 lds\n" \
  "v_smfmac_f32_16x16x64_bf16 v[180:183], v[132:135], a[200:207], v0\n" \
  "s_waitcnt lgkmcnt(6)\n" \
  "ds_read_b128 v[164:167], v5 offset:6656\n" \
  "v_smfmac_f32_16x16x64_bf16 v[176:179], v[136:139], a[48:55], v0\n" \
  "ds_read_b128 v[168:171], v5 offset:7168\n" \
  "buffer_load_dwordx4 v3, s[36:39], s72 offen offset:2048 sc1 lds\n" \
  "v_smfmac_f32_16x16x64_bf16 v[180:183], v[136:139], a[208:215], v0\n" \
  "s_waitcnt lgkmcnt(7)\n" \
  "ds_read_b128 v[172:175], v5 offset:7680\n" \
  "v_smfmac_f32_16x16x64_bf16 v[176:179], v[140:143], a[56:63], v0\n" \
  "s_barrier\n" \
  "ds_read2_b32 v[112:113], v6 offset0:0 offset1:32\n" \
  "buffer_load_dwordx4 v3, s[36:39], s72 offen offset:3072 sc1 lds\n" \
  "v_smfmac_f32_16x16x64_bf16 v[180:183], v[140:143], a[216:223], v0\n" \
  "s_waitcnt lgkmcnt(8)\n" \
  "ds_read2_b32 v[114:115], v6 offset0:64 offset1:96\n" \
  "v_smfmac_f32_16x16x64_bf16 v[176:179], v[144:147], a[64:71], v0\n" \
  "s_waitcnt lgkmcnt(8)\n" \
  "ds_read2_b32 v[116:117], v6 offset0:128 offset1:160\n" \
  "v_smfmac_f32_16x16x64_bf16 v[176:179], v[148:151], a[72:79], v0\n" \
  "s_waitcnt lgkmcnt(8)\n" \
  "ds_read2_b32 v[118:119], v6 offset0:192 offset1:224\n" \
  "v_smfmac_f32_16x16x64_bf16 v[176:179], v[152:155], a[80:87], v0\n" \
  "s_waitcnt lgkmcnt(8)\n" \
  "ds_read2_b32 v[120:121], v7 offset0:0 offset1:32\n" \
  "v_smfmac_f32_16x16x64_bf16 v[176:179], v[156:159], a[88:95], v0\n" \
  "s_waitcnt lgkmcnt(8)\n" \
  "ds_read2_b32 v[122:123], v7 offset0:64 offset1:96\n" \
  "v_smfmac_f32_16x16x64_bf16 v[176:179], v[160:163], a[96:103], v0\n" \
  "s_waitcnt lgkmcnt(8)\n" \
  "ds_read2_b32 v[124:125], v7 offset0:128 offset1:160\n" \
  "v_smfmac_f32_16x16x64_bf16 v[176:179], v[164:167], a[104:111], v0\n" \
  "s_waitcnt lgkmcnt(8)\n" \
  "ds_read2_b32 v[126:127], v7 offset0:192 offset1:224\n" \
  "v_smfmac_f32_16x16x64_bf16 v[176:179], v[168:171], a[112:119], v0\n" \
  "s_waitcnt lgkmcnt(8)\n" \
  "v_smfmac_f32_16x16x64_bf16 v[176:179], v[172:175], a[120:127], v0\n" \
  "s_waitcnt lgkmcnt(6)\n" \
  "v_smfmac_f32_16x16x64_bf16 v[176:179], v[112:115], a[128:135], v0\n" \
  "s_waitcnt lgkmcnt(4)\n" \
  "v_smfmac_f32_16x16x64_bf16 v[176:179], v[116:119], a[136:143], v0\n" \
  "s_waitcnt lgkmcnt(2)\n" \
  "v_smfmac_f32_16x16x64_bf16 v[176:179], v[120:123], a[144:151], v0\n" \
  "s_waitcnt lgkmcnt(0)\n" \
  "v_smfmac_f32_16x16x64_bf16 v[176:179], v[124:127], a[152:159], v0\n" \
  "v_smfmac_f32_16x16x64_bf16 v[180:183], v[144:147], a[224:231], v0\n" \
  "v_smfmac_f32_16x16x64_bf16 v[180:183], v[148:151], a[232:239], v0\n" \
  "v_cvt_pk_bf16_f32 v184, v176, v177\n" \
  "v_smfmac_f32_16x16x64_bf16 v[180:183], v[152:155], a[240:247], v0\n" \
  "v_cvt_pk_bf16_f32 v185, v178, v179\n" \
  "v_smfmac_f32_16x16x64_bf16 v[180:183], v[156:159], a[248:255], v0\n" \
  "ds_write_b64 v8, v[184:185]\n" \
  "v_add_f32 v186, v176, v177\n" \
  "v_smfmac_f32_16x16x64_bf16 v[180:183], v[160:163], v[192:199], v0\n" \
  "v_add_f32 v187, v178, v179\n" \
  "v_smfmac_f32_16x16x64_bf16 v[180:183], v[164:167], v[200:207], v0\n" \
  "v_cmp_u_f32 vcc, v186, v187\n" \
  "v_smfmac_f32_16x16x64_bf16 v[180:183], v[168:171], v[208:215], v0\n" \
  "v_smfmac_f32_16x16x64_bf16 v[180:183], v[172:175], v[216:223], v0\n" \
  "s_cbranch_vccnz LMF10_%=\n" \
  "LMOK10_%=:\n" \
  "v_smfmac_f32_16x16x64_bf16 v[180:183], v[112:115], v[224:231], v0\n" \
  "s_waitcnt lgkmcnt(0)\n" \
  "s_barrier\n" \
  "ds_read_b64_tr_b16 v[128:129], v10\n" \
  "v_smfmac_f32_16x16x64_bf16 v[180:183], v[116:119], v[232:239], v0\n" \
  "ds_read_b64_tr_b16 v[130:131], v10 offset:1024\n" \
  "ds_read_b64_tr_b16 v[132:133], v10 offset:2048\n" \
  "v_smfmac_f32_16x16x64_bf16 v[180:183], v[120:123], v[240:247], v0\n" \
  "ds_read_b64_tr_b16 v[134:135], v10 offset:3072\n" \
  "v_smfmac_f32_16x16x64_bf16 v[180:183], v[124:127], v[248:255], v0\n" \
  "s_nop 3\n" \
  "v_mov_b32 v31, v31\n" \
  "s_nop 2\n" \
  "v_cvt_pk_bf16_f32 v186, v180, v181\n" \
  "v_cvt_pk_bf16_f32 v187, v182, v183\n" \
  "ds_write_b64 v9, v[186:187]\n" \
  "s_waitcnt lgkmcnt(3)\n" \
  "v_mfma_f32_16x16x32_bf16 v[188:191], v[128:131], v[128:131], 0\n" \
  "s_waitcnt lgkmcnt(1)\n" \
  "v_mfma_f32_16x16x32_bf16 v[188:191], v[132:135], v[132:135], v[188:191]\n" \
  "s_waitcnt lgkmcnt(0)\n" \
  "s_barrier\n" \
  "ds_read_b64_tr_b16 v[144:145], v11\n" \
  "ds_read_b64_tr_b16 v[146:147], v11 offset:1024\n" \
  "ds_read_b64_tr_b16 v[148:149], v11 offset:2048\n" \
  "ds_read_b64_tr_b16 v[150:151], v11 offset:3072\n" \
  "s_waitcnt lgkmcnt(2)\n" \
  "v_mfma_f32_16x16x32_bf16 v[188:191], v[144:147], v[144:147], v[188:191]\n" \
  "s_waitcnt lgkmcnt(0)\n" \
  "v_mfma_f32_16x16x32_bf16 v[188:191], v[148:151], v[148:151], v[188:191]\n" \
  "s_nop 0\n" \
  "ds_read_b64_tr_b16 v[88:89], v12\n" \
  "ds_read_b64_tr_b16 v[90:91], v12 offset:32\n" \
  "s_nop 1\n" \
  "v_mov_b32 v31, v31\n" \
  "s_nop 1\n" \
  "v_pk_mul_f32 v[188:189], v[188:189], s[42:43]\n" \
  "v_pk_mul_f32 v[190:191], v[190:191], s[42:43]\n" \
  "v_cvt_pk_bf16_f32 v152, v188, v189\n" \
  "v_cvt_pk_bf16_f32 v153, v190, v191\n" \
  "buffer_store_dwordx2 v[152:153], v13, s[36:39], s65 offen sc1\n" \
  "buffer_store_dwordx2 v[152:153], v13, s[36:39], s91 offen sc1\n" \
  "buffer_store_dwordx2 v[152:153], v13, s[36:39], s92 offen sc1\n" \
  "s_mov_b32 s67, s66\n" \
  "s_cmp_eq_u32 s64, 64\n" \
  "s_cbranch_scc1 LKE10_%=\n" \
  "LB10_%=:\n" \
  "s_barrier\n" \
  "buffer_load_dwordx4 v[116:119], v15, s[36:39], s65 offen offset:128 sc1\n" \
  "s_barrier\n" \
  "buffer_load_dwordx4 v[112:115], v15, s[36:39], s65 offen sc1\n" \
  "s_barrier\n" \
  "buffer_load_dwordx4 v[120:123], v16, s[36:39], s65 offen sc1\n" \
  "s_barrier\n" \
  "buffer_load_dwordx4 v[124:127], v16, s[36:39], s65 offen offset:512 sc1\n" \
  "s_barrier\n" \
  "buffer_load_dwordx4 v[128:131], v16, s[36:39], s65 offen offset:1024 sc1\n" \
  "s_waitcnt vmcnt(8)\n" \
  "s_barrier\n" \
  "buffer_load_dwordx4 v[132:135], v16, s[36:39], s65 offen offset:1536 sc1\n" \
  "ds_read_b128 v[40:43], v19\n" \
  "ds_read_b128 v[44:47], v19 offset:512\n" \
  "ds_read_b128 v[48:51], v19 offset:1024\n" \
  "ds_read_b128 v[52:55], v19 offset:1536\n" \
  "s_mov_b64 exec, s[46:47]\n" \
  "ds_read_b128 v[56:59], v20\n" \
  "ds_read_b128 v[60:63], v20 offset:128\n" \
  "ds_read_b128 v[64:67], v20 offset:256\n" \
  "ds_read_b128 v[68:71], v20 offset:384\n" \
  "s_mov_b64 exec, -1\n" \
  "s_nop 15\n" \
  "s_nop 1\n" \
  "buffer_load_dwordx4 v[136:139], v17, s[36:39], s65 offen sc1\n" \
  "buffer_load_dwordx4 v[140:143], v18, s[36:39], s65 offen sc1\n" \
  "s_barrier\n" \
  "s_waitcnt lgkmcnt(0)\n" \
  "buffer_store_dwordx4 v[88:91], v14, s[36:39], s69 offen sc1\n" \
  "s_waitcnt lgkmcnt(0)\n" \
  "s_getreg_b32 s71, hwreg(HW_REG_IB_STS, 0, 4)\n" \
  "s_cmp_le_u32 s71, 7\n" \
  "v_mfma_f32_16x16x32_bf16 v[144:147], v[72:75], v[72:75], 0\n" \
  "s_cbranch_scc1 LHXS10_%=\n" \
  "s_getreg_b32 s71, hwreg(HW_REG_IB_STS, 0, 4)\n" \
  "s_cmp_le_u32 s71, 7\n" \
  "v_mfma_f32_16x16x32_bf16 v[144:147], v[72:75], v[72:75], 0\n" \
  "s_cbranch_scc1 LHXS10_%=\n" \
  "s_getreg_b32 s71, hwreg(HW_REG_IB_STS, 0, 4)\n" \
  "s_cmp_le_u32 s71, 7\n" \
  "v_mfma_f32_16x16x32_bf16 v[144:147], v[72:75], v[72:75], 0\n" \
  "s_cbranch_scc1 LHXS10_%=\n" \
  "s_getreg_b32 s71, hwreg(HW_REG_IB_STS, 0, 4)\n" \
  "s_cmp_le_u32 s71, 7\n" \
  "v_mfma_f32_16x16x32_bf16 v[144:147], v[72:75], v[72:75], 0\n" \
  "s_cbranch_scc1 LHXS10_%=\n" \
  "s_getreg_b32 s71, hwreg(HW_REG_IB_STS, 0, 4)\n" \
  "s_cmp_le_u32 s71, 7\n" \
  "v_mfma_f32_16x16x32_bf16 v[144:147], v[72:75], v[72:75], 0\n" \
  "s_cbranch_scc1 LHXS10_%=\n" \
  "s_getreg_b32 s71, hwreg(HW_REG_IB_STS, 0, 4)\n" \
  "s_cmp_le_u32 s71, 7\n" \
  "v_mfma_f32_16x16x32_bf16 v[144:147], v[72:75], v[72:75], 0\n" \
  "s_cbranch_scc1 LHXS10_%=\n" \
  "s_getreg_b32 s71, hwreg(HW_REG_IB_STS, 0, 4)\n" \
  "s_cmp_le_u32 s71, 7\n" \
  "v_mfma_f32_16x16x32_bf16 v[144:147], v[72:75], v[72:75], 0\n" \
  "s_cbranch_scc1 LHXS10_%=\n" \
  "s_getreg_b32 s71, hwreg(HW_REG_IB_STS, 0, 4)\n" \
  "s_cmp_le_u32 s71, 7\n" \
  "v_mfma_f32_16x16x32_bf16 v[144:147], v[72:75], v[72:75], 0\n" \
  "s_cbranch_scc1 LHXS10_%=\n" \
  "s_getreg_b32 s71, hwreg(HW_REG_IB_STS, 0, 4)\n" \
  "s_cmp_le_u32 s71, 7\n" \
  "v_mfma_f32_16x16x32_bf16 v[144:147], v[72:75], v[72:75], 0\n" \
  "s_cbranch_scc1 LHXS10_%=\n" \
  "s_getreg_b32 s71, hwreg(HW_REG_IB_STS, 0, 4)\n" \
  "s_cmp_le_u32 s71, 7\n" \
  "v_mfma_f32_16x16x32_bf16 v[144:147], v[72:75], v[72:75], 0\n" \
  "s_cbranch_scc1 LHXS10_%=\n" \
  "s_getreg_b32 s71, hwreg(HW_REG_IB_STS, 0, 4)\n" \
  "s_cmp_le_u32 s71, 7\n" \
  "v_mfma_f32_16x16x32_bf16 v[144:147], v[72:75], v[72:75], 0\n" \
  "s_cbranch_scc1 LHXS10_%=\n" \
  "s_getreg_b32 s71, hwreg(HW_REG_IB_STS, 0, 4)\n" \
  "s_cmp_le_u32 s71, 7\n" \
  "v_mfma_f32_16x16x32_bf16 v[144:147], v[72:75], v[72:75], 0\n" \
  "s_cbranch_scc1 LHXS10_%=\n" \
  "s_getreg_b32 s71, hwreg(HW_REG_IB_STS, 0, 4)\n" \
  "s_cmp_le_u32 s71, 7\n" \
  "v_mfma_f32_16x16x32_bf16 v[144:147], v[72:75], v[72:75], 0\n" \
  "s_cbranch_scc1 LHXS10_%=\n" \
  "s_getreg_b32 s71, hwreg(HW_REG_IB_STS, 0, 4)\n" \
  "s_cmp_le_u32 s71, 7\n" \
  "v_mfma_f32_16x16x32_bf16 v[144:147], v[72:75], v[72:75], 0\n" \
  "s_cbranch_scc1 LHXS10_%=\n" \
  "s_getreg_b32 s71, hwreg(HW_REG_IB_STS, 0, 4)\n" \
  "s_cmp_le_u32 s71, 7\n" \
  "v_mfma_f32_16x16x32_bf16 v[144:147], v[72:75], v[72:75], 0\n" \
  "s_cbranch_scc1 LHXS10_%=\n" \
  "s_getreg_b32 s71, hwreg(HW_REG_IB_STS, 0, 4)\n" \
  "s_cmp_le_u32 s71, 7\n" \
  "v_mfma_f32_16x16x32_bf16 v[144:147], v[72:75], v[72:75], 0\n" \
  "s_cbranch_scc1 LHXS10_%=\n" \
  "s_getreg_b32 s71, hwreg(HW_REG_IB_STS, 0, 4)\n" \
  "s_cmp_le_u32 s71, 7\n" \
  "v_mfma_f32_16x16x32_bf16 v[144:147], v[72:75], v[72:75], 0\n" \
  "s_cbranch_scc1 LHXS10_%=\n" \
  "s_getreg_b32 s71, hwreg(HW_REG_IB_STS, 0, 4)\n" \
  "s_cmp_le_u32 s71, 7\n" \
  "v_mfma_f32_16x16x32_bf16 v[144:147], v[72:75], v[72:75], 0\n" \
  "s_cbranch_scc1 LHXS10_%=\n" \
  "s_getreg_b32 s71, hwreg(HW_REG_IB_STS, 0, 4)\n" \
  "s_cmp_le_u32 s71, 7\n" \
  "v_mfma_f32_16x16x32_bf16 v[144:147], v[72:75], v[72:75], 0\n" \
  "s_cbranch_scc1 LHXS10_%=\n" \
  "s_getreg_b32 s71, hwreg(HW_REG_IB_STS, 0, 4)\n" \
  "s_cmp_le_u32 s71, 7\n" \
  "v_mfma_f32_16x16x32_bf16 v[144:147], v[72:75], v[72:75], 0\n" \
  "s_cbranch_scc1 LHXS10_%=\n" \
  "LHXS10_%=:\n" \
  "s_waitcnt vmcnt(7)\n" \
  "LS210_%=:\n" \
  "v_dot2_f32_bf16 v176, v112, s22, 0\n" \
  "v_dot2_f32_bf16 v177, v112, s23, 0\n" \
  "v_dot2_f32_bf16 v178, v113, s22, 0\n" \
  "v_dot2_f32_bf16 v179, v113, s23, 0\n" \
  "v_dot2_f32_bf16 v180, v114, s22, 0\n" \
  "v_dot2_f32_bf16 v181, v114, s23, 0\n" \
  "v_dot2_f32_bf16 v182, v115, s22, 0\n" \
  "v_dot2_f32_bf16 v183, v115, s23, 0\n" \
  "v_dot2_f32_bf16 v176, v116, s22, v176\n" \
  "v_dot2_f32_bf16 v177, v116, s23, v177\n" \
  "v_dot2_f32_bf16 v178, v117, s22, v178\n" \
  "v_dot2_f32_bf16 v179, v117, s23, v179\n" \
  "v_dot2_f32_bf16 v180, v118, s22, v180\n" \
  "v_dot2_f32_bf16 v181, v118, s23, v181\n" \
  "v_dot2_f32_bf16 v182, v119, s22, v182\n" \
  "v_dot2_f32_bf16 v183, v119, s23, v183\n" \
  "v_exp_f32 v176, v176\n" \
  "v_exp_f32 v177, v177\n" \
  "v_exp_f32 v178, v178\n" \
  "v_exp_f32 v179, v179\n" \
  "s_waitcnt vmcnt(6)\n" \
  "v_mfma_f32_16x16x32_bf16 v[160:163], v[56:59], v[120:123], 0\n" \
  "v_exp_f32 v180, v180\n" \
  "v_mfma_f32_16x16x32_bf16 v[144:147], v[40:43], v[120:123], 0\n" \
  "v_exp_f32 v181, v181\n" \
  "v_exp_f32 v182, v182\n" \
  "v_exp_f32 v183, v183\n" \
  "v_cvt_pk_bf16_f32 v184, v176, v177\n" \
  "v_cvt_pk_bf16_f32 v185, v178, v179\n" \
  "v_cvt_pk_bf16_f32 v186, v180, v181\n" \
  "v_cvt_pk_bf16_f32 v187, v182, v183\n" \
  "s_nop 1\n" \
  "v_mfma_f32_16x16x32_bf16 v[160:163], v[72:75], v[184:187], v[160:163]\n" \
  "v_mfma_f32_16x16x32_bf16 v[164:167], v[76:79], v[184:187], 0\n" \
  "s_waitcnt vmcnt(5)\n" \
  "v_mfma_f32_16x16x32_bf16 v[164:167], v[60:63], v[124:127], v[164:167]\n" \
  "v_rcp_f32 v188, v162\n" \
  "v_mfma_f32_16x16x32_bf16 v[148:151], v[44:47], v[124:127], 0\n" \
  "v_mul_f32 v144, v188, v144\n" \
  "v_mul_f32 v145, v188, v145\n" \
  "v_mfma_f32_16x16x32_bf16 v[168:171], v[80:83], v[184:187], 0\n" \
  "s_waitcnt vmcnt(4)\n" \
  "v_rcp_f32 v189, v166\n" \
  "v_mfma_f32_16x16x32_bf16 v[168:171], v[64:67], v[128:131], v[168:171]\n" \
  "v_mul_f32 v146, v188, v146\n" \
  "v_mul_f32 v147, v188, v147\n" \
  "v_mfma_f32_16x16x32_bf16 v[152:155], v[48:51], v[128:131], 0\n" \
  "v_mul_f32 v160, v188, v160\n" \
  "v_mul_f32 v161, v188, v161\n" \
  "v_mfma_f32_16x16x32_bf16 v[172:175], v[84:87], v[184:187], 0\n" \
  "s_waitcnt vmcnt(3)\n" \
  "v_rcp_f32 v190, v170\n" \
  "v_mfma_f32_16x16x32_bf16 v[172:175], v[68:71], v[132:135], v[172:175]\n" \
  "v_cndmask_b32 v167, v188, v189, s[28:29]\n" \
  "v_cndmask_b32 v167, v167, v190, s[26:27]\n" \
  "v_mfma_f32_16x16x32_bf16 v[156:159], v[52:55], v[132:135], 0\n" \
  "v_fmac_f32 v144, v189, v148\n" \
  "v_fmac_f32 v145, v189, v149\n" \
  "v_fmac_f32 v146, v189, v150\n" \
  "v_fmac_f32 v147, v189, v151\n" \
  "v_rcp_f32 v191, v174\n" \
  "v_fmac_f32 v160, v189, v164\n" \
  "v_cndmask_b32 v167, v167, v191, s[24:25]\n" \
  "v_pk_mul_f32 v[176:177], v[176:177], v[166:167] op_sel:[0,1] op_sel_hi:[1,1]\n" \
  "v_pk_mul_f32 v[178:179], v[178:179], v[166:167] op_sel:[0,1] op_sel_hi:[1,1]\n" \
  "v_pk_mul_f32 v[180:181], v[180:181], v[166:167] op_sel:[0,1] op_sel_hi:[1,1]\n" \
  "v_pk_mul_f32 v[182:183], v[182:183], v[166:167] op_sel:[0,1] op_sel_hi:[1,1]\n" \
  "v_cmp_nlt_f32 vcc, 0x0d800000, v167\n" \
  "v_cvt_pk_bf16_f32 v184, v176, v177\n" \
  "v_cvt_pk_bf16_f32 v185, v178, v179\n" \
  "v_cvt_pk_bf16_f32 v186, v180, v181\n" \
  "v_cvt_pk_bf16_f32 v187, v182, v183\n" \
  "v_fmac_f32 v161, v189, v165\n" \
  "v_pk_fma_f32 v[160:161], v[168:169], v[190:191], v[160:161] op_sel:[0,0,0] op_sel_hi:[1,0,1]\n" \
  "v_pk_fma_f32 v[160:161], v[172:173], v[190:191], v[160:161] op_sel:[0,1,0] op_sel_hi:[1,1,1]\n" \
  "v_pk_fma_f32 v[144:145], v[152:153], v[190:191], v[144:145] op_sel:[0,0,0] op_sel_hi:[1,0,1]\n" \
  "v_pk_fma_f32 v[146:147], v[154:155], v[190:191], v[146:147] op_sel:[0,0,0] op_sel_hi:[1,0,1]\n" \
  "v_pk_fma_f32 v[144:145], v[156:157], v[190:191], v[144:145] op_sel:[0,1,0] op_sel_hi:[1,1,1]\n" \
  "v_pk_fma_f32 v[146:147], v[158:159], v[190:191], v[146:147] op_sel:[0,1,0] op_sel_hi:[1,1,1]\n" \
  "s_waitcnt vmcnt(1)\n" \
  "v_mfma_f32_16x16x32_bf16 v[160:163], v[140:143], v[184:187], v[160:163]\n" \
  "s_mov_b64 s[80:81], vcc\n" \
  "v_mfma_f32_16x16x32_bf16 v[144:147], v[136:139], v[184:187], v[144:147]\n" \
  "s_mov_b64 exec, s[30:31]\n" \
  "s_nop 2\n" \
  "ds_write_b64 v22, v[160:161]\n" \
  "v_cmp_u_f32 s[78:79], v160, v161\n" \
  "s_nop 1\n" \
  "ds_write2_b64 v21, v[144:145], v[146:147] offset1:16\n" \
  "v_add_f32 v188, v144, v145\n" \
  "v_add_f32 v189, v146, v147\n" \
  "v_cmp_u_f32 vcc, v188, v189\n" \
  "s_or_b64 s[80:81], s[80:81], s[78:79]\n" \
  "s_or_b64 vcc, vcc, s[80:81]\n" \
  "s_cbranch_vccnz LSF10_%=\n" \
  "LSOK10_%=:\n" \
  "s_mov_b64 exec, -1\n" \
  "s_waitcnt lgkmcnt(0)\n" \
  "s_barrier\n" \
  "ds_read_b32 v188, v33\n" \
  "s_waitcnt lgkmcnt(0)\n" \
  "v_cmp_ne_u32 vcc, 0, v188\n" \
  "s_cbranch_vccnz LSR10_%=\n" \
  "LNL10_%=:\n" \
  "s_waitcnt lgkmcnt(0)\n" \
  "s_cmp_eq_u32 s73, 1\n" \
  "s_cbranch_scc1 LOOP10_%=\n" \
  "s_branch LDONE_%=\n" \
  "LMF10_%=:\n" \
  "v_mbcnt_lo_u32_b32 v130, -1, 0\n" \
  "v_mbcnt_hi_u32_b32 v130, -1, v130\n" \
  "v_lshlrev_b32 v130, 4, v130\n" \
  "v_mov_b32 v128, 0\n" \
  "ds_read_b128 v[132:135], v130 offset:0\n" \
  "ds_read_b128 v[136:139], v130 offset:1024\n" \
  "ds_read_b128 v[140:143], v130 offset:2048\n" \
  "ds_read_b128 v[144:147], v130 offset:3072\n" \
  "ds_read_b128 v[148:151], v130 offset:4096\n" \
  "s_waitcnt lgkmcnt(0)\n" \
  "v_max3_u32 v128, v128, v132, v133\n" \
  "v_max3_u32 v128, v128, v134, v135\n" \
  "v_max3_u32 v128, v128, v136, v137\n" \
  "v_max3_u32 v128, v128, v138, v139\n" \
  "v_max3_u32 v128, v128, v140, v141\n" \
  "v_max3_u32 v128, v128, v142, v143\n" \
  "v_max3_u32 v128, v128, v144, v145\n" \
  "v_max3_u32 v128, v128, v146, v147\n" \
  "v_max3_u32 v128, v128, v148, v149\n" \
  "v_max3_u32 v128, v128, v150, v151\n" \
  "ds_read_b128 v[132:135], v130 offset:5120\n" \
  "ds_read_b128 v[136:139], v130 offset:6144\n" \
  "ds_read_b128 v[140:143], v130 offset:7168\n" \
  "ds_read_b128 v[144:147], v130 offset:8192\n" \
  "ds_read_b128 v[148:151], v130 offset:9216\n" \
  "s_waitcnt lgkmcnt(0)\n" \
  "v_max3_u32 v128, v128, v132, v133\n" \
  "v_max3_u32 v128, v128, v134, v135\n" \
  "v_max3_u32 v128, v128, v136, v137\n" \
  "v_max3_u32 v128, v128, v138, v139\n" \
  "v_max3_u32 v128, v128, v140, v141\n" \
  "v_max3_u32 v128, v128, v142, v143\n" \
  "v_max3_u32 v128, v128, v144, v145\n" \
  "v_max3_u32 v128, v128, v146, v147\n" \
  "v_max3_u32 v128, v128, v148, v149\n" \
  "v_max3_u32 v128, v128, v150, v151\n" \
  "v_cmp_eq_u32 vcc, -1, v128\n" \
  "s_cbranch_vccz LMOK10_%=\n" \
  "s_cmp_gt_u32 s58, 4000\n" \
  "s_cbranch_scc1 LMOK10_%=\n" \
  "s_add_u32 s58, s58, 1\n" \
  "s_sub_u32 s64, s64, 1\n" \
  "s_mov_b32 s66, s65\n" \
  "s_waitcnt vmcnt(0) lgkmcnt(0)\n" \
  "s_barrier\n" \
  "s_branch LOOP10_%=\n" \
  "LKE10_%=:\n" \
  "s_waitcnt lgkmcnt(0)\n" \
  "buffer_store_dwordx4 v[88:91], v14, s[36:39], s69 offen sc1\n" \
  "s_branch LDONE_%=\n" \
  "LSF10_%=:\n" \
  "s_getpc_b64 s[78:79]\n" \
  "s_branch LSF_%=\n" \
  "s_branch LSOK10_%=\n" \
  "LSR10_%=:\n" \
  "v_mov_b32 v188, 0\n" \
  "s_add_u32 s59, s59, 1\n" \
  "s_barrier\n" \
  "ds_write_b32 v33, v188\n" \
  "s_waitcnt lgkmcnt(0)\n" \
  "s_branch LB10_%=\n" \
  "LSTART11_%=:\n" \
  "LOOP11_%=:\n" \
  "v_readlane_b32 s70, v27, s64\n" \
  "s_mov_b32 s65, s66\n" \
  "s_xor_b32 s69, s66, 0x2000\n" \
  "s_add_u32 s64, s64, 1\n" \
  "v_mov_b32 v176, 0\n" \
  "v_mov_b32 v177, 0\n" \
  "v_mov_b32 v178, 0\n" \
  "v_mov_b32 v179, 0\n" \
  "v_mov_b32 v180, 0\n" \
  "v_mov_b32 v181, 0\n" \
  "v_mov_b32 v182, 0\n" \
  "v_mov_b32 v183, 0\n" \
  "v_readlane_b32 s66, v25, s64\n" \
  "v_readlane_b32 s68, v26, s64\n" \
  "s_mov_b32 m0, 28672\n" \
  "s_add_u32 s72, s70, 4096\n" \
  "s_barrier\n" \
  "buffer_load_dwordx4 v3, s[36:39], s70 offen sc1 lds\n" \
  "buffer_load_dwordx4 v3, s[36:39], s70 offen offset:1024 sc1 lds\n" \
  "buffer_load_dwordx4 v3, s[36:39], s70 offen offset:2048 sc1 lds\n" \
  "buffer_load_dwordx4 v3, s[36:39], s70 offen offset:3072 sc1 lds\n" \
  "LM11_%=:\n" \
  "s_barrier\n" \
  "ds_read_b128 v[112:115], v5\n" \
  "ds_read_b128 v[116:119], v5 offset:512\n" \
  "s_barrier\n" \
  "ds_read_b128 v[120:123], v5 offset:1024\n" \
  "ds_read_b128 v[124:127], v5 offset:1536\n" \
  "s_waitcnt lgkmcnt(3)\n" \
  "v_smfmac_f32_16x16x64_bf16 v[176:179], v[112:115], a[0:7], v0\n" \
  "v_smfmac_f32_16x16x64_bf16 v[180:183], v[112:115], a[160:167], v0\n" \
  "s_waitcnt lgkmcnt(2)\n" \
  "v_smfmac_f32_16x16x64_bf16 v[176:179], v[116:119], a[8:15], v0\n" \
  "s_barrier\n" \
  "ds_read_b128 v[128:131], v5 offset:2048\n" \
  "v_smfmac_f32_16x16x64_bf16 v[180:183], v[116:119], a[168:175], v0\n" \
  "s_waitcnt lgkmcnt(2)\n" \
  "ds_read_b128 v[132:135], v5 offset:2560\n" \
  "v_smfmac_f32_16x16x64_bf16 v[176:179], v[120:123], a[16:23], v0\n" \
  "ds_read_b128 v[136:139], v5 offset:3072\n" \
  "v_smfmac_f32_16x16x64_bf16 v[180:183], v[120:123], a[176:183], v0\n" \
  "s_waitcnt lgkmcnt(3)\n" \
  "ds_read_b128 v[140:143], v5 offset:3584\n" \
  "v_smfmac_f32_16x16x64_bf16 v[176:179], v[124:127], a[24:31], v0\n" \
  "s_barrier\n" \
  "ds_read_b128 v[144:147], v5 offset:4096\n" \
  "v_smfmac_f32_16x16x64_bf16 v[180:183], v[124:127], a[184:191], v0\n" \
  "s_mov_b32 m0, 32768\n" \
  "s_waitcnt lgkmcnt(4)\n" \
  "ds_read_b128 v[148:151], v5 offset:4608\n" \
  "v_smfmac_f32_16x16x64_bf16 v[176:179], v[128:131], a[32:39], v0\n" \
  "ds_read_b128 v[152:155], v5 offset:5120\n" \
  "v_smfmac_f32_16x16x64_bf16 v[180:183], v[128:131], a[192:199], v0\n" \
  "ds_read_b128 v[156:159], v5 offset:5632\n" \
  "buffer_load_dwordx4 v3, s[36:39], s72 offen sc1 lds\n" \
  "s_waitcnt lgkmcnt(6)\n" \
  "v_smfmac_f32_16x16x64_bf16 v[176:179], v[132:135], a[40:47], v0\n" \
  "s_barrier\n" \
  "ds_read_b128 v[160:163], v5 offset:6144\n" \
  "v_smfmac_f32_16x16x64_bf16 v[180:183], v[132:135], a[200:207], v0\n" \
  "ds_read_b128 v[164:167], v5 offset:6656\n" \
  "buffer_load_dwordx4 v3, s[36:39], s72 offen offset:1024 sc1 lds\n" \
  "s_waitcnt lgkmcnt(7)\n" \
  "v_smfmac_f32_16x16x64_bf16 v[176:179], v[136:139], a[48:55], v0\n" \
  "ds_read_b128 v[168:171], v5 offset:7168\n" \
  "v_smfmac_f32_16x16x64_bf16 v[180:183], v[136:139], a[208:215], v0\n" \
  "ds_read_b128 v[172:175], v5 offset:7680\n" \
  "buffer_load_dwordx4 v3, s[36:39], s72 offen offset:2048 sc1 lds\n" \
  "s_waitcnt lgkmcnt(8)\n" \
  "v_smfmac_f32_16x16x64_bf16 v[176:179], v[140:143], a[56:63], v0\n" \
  "s_barrier\n" \
  "ds_read2_b32 v[112:113], v6 offset0:0 offset1:32\n" \
  "v_smfmac_f32_16x16x64_bf16 v[180:183], v[140:143], a[216:223], v0\n" \
  "ds_read2_b32 v[114:115], v6 offset0:64 offset1:96\n" \
  "buffer_load_dwordx4 v3, s[36:39], s72 offen offset:3072 sc1 lds\n" \
  "s_waitcnt lgkmcnt(9)\n" \
  "v_smfmac_f32_16x16x64_bf16 v[176:179], v[144:147], a[64:71], v0\n" \
  "s_waitcnt lgkmcnt(8)\n" \
  "ds_read2_b32 v[116:117], v6 offset0:128 offset1:160\n" \
  "v_smfmac_f32_16x16x64_bf16 v[176:179], v[148:151], a[72:79], v0\n" \
  "s_waitcnt lgkmcnt(8)\n" \
  "ds_read2_b32 v[118:119], v6 offset0:192 offset1:224\n" \
  "v_smfmac_f32_16x16x64_bf16 v[176:179], v[152:155], a[80:87], v0\n" \
  "s_waitcnt lgkmcnt(8)\n" \
  "ds_read2_b32 v[120:121], v7 offset0:0 offset1:32\n" \
  "v_smfmac_f32_16x16x64_bf16 v[176:179], v[156:159], a[88:95], v0\n" \
  "s_waitcnt lgkmcnt(8)\n" \
  "ds_read2_b32 v[122:123], v7 offset0:64 offset1:96\n" \
  "v_smfmac_f32_16x16x64_bf16 v[176:179], v[160:163], a[96:103], v0\n" \
  "s_waitcnt lgkmcnt(8)\n" \
  "ds_read2_b32 v[124:125], v7 offset0:128 offset1:160\n" \
  "v_smfmac_f32_16x16x64_bf16 v[176:179], v[164:167], a[104:111], v0\n" \
  "s_waitcnt lgkmcnt(8)\n" \
  "ds_read2_b32 v[126:127], v7 offset0:192 offset1:224\n" \
  "v_smfmac_f32_16x16x64_bf16 v[176:179], v[168:171], a[112:119], v0\n" \
  "s_waitcnt lgkmcnt(8)\n" \
  "v_smfmac_f32_16x16x64_bf16 v[176:179], v[172:175], a[120:127], v0\n" \
  "s_waitcnt lgkmcnt(6)\n" \
  "v_smfmac_f32_16x16x64_bf16 v[176:179], v[112:115], a[128:135], v0\n" \
  "s_waitcnt lgkmcnt(4)\n" \
  "v_smfmac_f32_16x16x64_bf16 v[176:179], v[116:119], a[136:143], v0\n" \
  "s_waitcnt lgkmcnt(2)\n" \
  "v_smfmac_f32_16x16x64_bf16 v[176:179], v[120:123], a[144:151], v0\n" \
  "s_waitcnt lgkmcnt(0)\n" \
  "v_smfmac_f32_16x16x64_bf16 v[176:179], v[124:127], a[152:159], v0\n" \
  "v_smfmac_f32_16x16x64_bf16 v[180:183], v[144:147], a[224:231], v0\n" \
  "v_smfmac_f32_16x16x64_bf16 v[180:183], v[148:151], a[232:239], v0\n" \
  "v_cvt_pk_bf16_f32 v184, v176, v177\n" \
  "v_smfmac_f32_16x16x64_bf16 v[180:183], v[152:155], a[240:247], v0\n" \
  "v_cvt_pk_bf16_f32 v185, v178, v179\n" \
  "v_smfmac_f32_16x16x64_bf16 v[180:183], v[156:159], a[248:255], v0\n" \
  "ds_write_b64 v8, v[184:185]\n" \
  "v_add_f32 v186, v176, v177\n" \
  "v_smfmac_f32_16x16x64_bf16 v[180:183], v[160:163], v[192:199], v0\n" \
  "v_add_f32 v187, v178, v179\n" \
  "v_smfmac_f32_16x16x64_bf16 v[180:183], v[164:167], v[200:207], v0\n" \
  "v_cmp_u_f32 vcc, v186, v187\n" \
  "v_smfmac_f32_16x16x64_bf16 v[180:183], v[168:171], v[208:215], v0\n" \
  "v_smfmac_f32_16x16x64_bf16 v[180:183], v[172:175], v[216:223], v0\n" \
  "s_cbranch_vccnz LMF11_%=\n" \
  "LMOK11_%=:\n" \
  "v_smfmac_f32_16x16x64_bf16 v[180:183], v[112:115], v[224:231], v0\n" \
  "s_waitcnt lgkmcnt(0)\n" \
  "s_barrier\n" \
  "v_smfmac_f32_16x16x64_bf16 v[180:183], v[116:119], v[232:239], v0\n" \
  "v_smfmac_f32_16x16x64_bf16 v[180:183], v[120:123], v[240:247], v0\n" \
  "v_smfmac_f32_16x16x64_bf16 v[180:183], v[124:127], v[248:255], v0\n" \
  "s_nop 3\n" \
  "v_mov_b32 v31, v31\n" \
  "s_nop 2\n" \
  "v_cvt_pk_bf16_f32 v186, v180, v181\n" \
  "v_cvt_pk_bf16_f32 v187, v182, v183\n" \
  "ds_write_b64 v9, v[186:187]\n" \
  "s_waitcnt lgkmcnt(0)\n" \
  "s_barrier\n" \
  "s_nop 15\n" \
  "s_nop 0\n" \
  "ds_read_b64_tr_b16 v[88:89], v12\n" \
  "ds_read_b64_tr_b16 v[90:91], v12 offset:32\n" \
  "s_mov_b32 s67, s66\n" \
  "s_cmp_eq_u32 s64, 64\n" \
  "s_cbranch_scc1 LKE11_%=\n" \
  "LB11_%=:\n" \
  "s_barrier\n" \
  "buffer_load_dwordx4 v[116:119], v15, s[36:39], s65 offen offset:128 sc1\n" \
  "s_barrier\n" \
  "buffer_load_dwordx4 v[112:115], v15, s[36:39], s65 offen sc1\n" \
  "s_barrier\n" \
  "buffer_load_dwordx4 v[120:123], v16, s[36:39], s65 offen sc1\n" \
  "s_barrier\n" \
  "buffer_load_dwordx4 v[124:127], v16, s[36:39], s65 offen offset:512 sc1\n" \
  "s_barrier\n" \
  "buffer_load_dwordx4 v[128:131], v16, s[36:39], s65 offen offset:1024 sc1\n" \
  "s_waitcnt vmcnt(5)\n" \
  "s_barrier\n" \
  "buffer_load_dwordx4 v[132:135], v16, s[36:39], s65 offen offset:1536 sc1\n" \
  "ds_read_b128 v[40:43], v19\n" \
  "ds_read_b128 v[44:47], v19 offset:512\n" \
  "ds_read_b128 v[48:51], v19 offset:1024\n" \
  "ds_read_b128 v[52:55], v19 offset:1536\n" \
  "s_mov_b64 exec, s[46:47]\n" \
  "ds_read_b128 v[56:59], v20\n" \
  "ds_read_b128 v[60:63], v20 offset:128\n" \
  "ds_read_b128 v[64:67], v20 offset:256\n" \
  "ds_read_b128 v[68:71], v20 offset:384\n" \
  "s_mov_b64 exec, -1\n" \
  "s_nop 15\n" \
  "s_nop 1\n" \
  "buffer_load_dwordx4 v[136:139], v17, s[36:39], s65 offen sc1\n" \
  "buffer_load_dwordx4 v[140:143], v18, s[36:39], s65 offen sc1\n" \
  "s_barrier\n" \
  "s_waitcnt lgkmcnt(0)\n" \
  "buffer_store_dwordx4 v[88:91], v14, s[36:39], s69 offen sc1\n" \
  "s_waitcnt lgkmcnt(0)\n" \
  "s_getreg_b32 s71, hwreg(HW_REG_IB_STS, 0, 4)\n" \
  "s_cmp_le_u32 s71, 7\n" \
  "v_mfma_f32_16x16x32_bf16 v[144:147], v[72:75], v[72:75], 0\n" \
  "s_cbranch_scc1 LHXS11_%=\n" \
  "s_getreg_b32 s71, hwreg(HW_REG_IB_STS, 0, 4)\n" \
  "s_cmp_le_u32 s71, 7\n" \
  "v_mfma_f32_16x16x32_bf16 v[144:147], v[72:75], v[72:75], 0\n" \
  "s_cbranch_scc1 LHXS11_%=\n" \
  "s_getreg_b32 s71, hwreg(HW_REG_IB_STS, 0, 4)\n" \
  "s_cmp_le_u32 s71, 7\n" \
  "v_mfma_f32_16x16x32_bf16 v[144:147], v[72:75], v[72:75], 0\n" \
  "s_cbranch_scc1 LHXS11_%=\n" \
  "s_getreg_b32 s71, hwreg(HW_REG_IB_STS, 0, 4)\n" \
  "s_cmp_le_u32 s71, 7\n" \
  "v_mfma_f32_16x16x32_bf16 v[144:147], v[72:75], v[72:75], 0\n" \
  "s_cbranch_scc1 LHXS11_%=\n" \
  "s_getreg_b32 s71, hwreg(HW_REG_IB_STS, 0, 4)\n" \
  "s_cmp_le_u32 s71, 7\n" \
  "v_mfma_f32_16x16x32_bf16 v[144:147], v[72:75], v[72:75], 0\n" \
  "s_cbranch_scc1 LHXS11_%=\n" \
  "s_getreg_b32 s71, hwreg(HW_REG_IB_STS, 0, 4)\n" \
  "s_cmp_le_u32 s71, 7\n" \
  "v_mfma_f32_16x16x32_bf16 v[144:147], v[72:75], v[72:75], 0\n" \
  "s_cbranch_scc1 LHXS11_%=\n" \
  "s_getreg_b32 s71, hwreg(HW_REG_IB_STS, 0, 4)\n" \
  "s_cmp_le_u32 s71, 7\n" \
  "v_mfma_f32_16x16x32_bf16 v[144:147], v[72:75], v[72:75], 0\n" \
  "s_cbranch_scc1 LHXS11_%=\n" \
  "s_getreg_b32 s71, hwreg(HW_REG_IB_STS, 0, 4)\n" \
  "s_cmp_le_u32 s71, 7\n" \
  "v_mfma_f32_16x16x32_bf16 v[144:147], v[72:75], v[72:75], 0\n" \
  "s_cbranch_scc1 LHXS11_%=\n" \
  "s_getreg_b32 s71, hwreg(HW_REG_IB_STS, 0, 4)\n" \
  "s_cmp_le_u32 s71, 7\n" \
  "v_mfma_f32_16x16x32_bf16 v[144:147], v[72:75], v[72:75], 0\n" \
  "s_cbranch_scc1 LHXS11_%=\n" \
  "s_getreg_b32 s71, hwreg(HW_REG_IB_STS, 0, 4)\n" \
  "s_cmp_le_u32 s71, 7\n" \
  "v_mfma_f32_16x16x32_bf16 v[144:147], v[72:75], v[72:75], 0\n" \
  "s_cbranch_scc1 LHXS11_%=\n" \
  "s_getreg_b32 s71, hwreg(HW_REG_IB_STS, 0, 4)\n" \
  "s_cmp_le_u32 s71, 7\n" \
  "v_mfma_f32_16x16x32_bf16 v[144:147], v[72:75], v[72:75], 0\n" \
  "s_cbranch_scc1 LHXS11_%=\n" \
  "s_getreg_b32 s71, hwreg(HW_REG_IB_STS, 0, 4)\n" \
  "s_cmp_le_u32 s71, 7\n" \
  "v_mfma_f32_16x16x32_bf16 v[144:147], v[72:75], v[72:75], 0\n" \
  "s_cbranch_scc1 LHXS11_%=\n" \
  "s_getreg_b32 s71, hwreg(HW_REG_IB_STS, 0, 4)\n" \
  "s_cmp_le_u32 s71, 7\n" \
  "v_mfma_f32_16x16x32_bf16 v[144:147], v[72:75], v[72:75], 0\n" \
  "s_cbranch_scc1 LHXS11_%=\n" \
  "s_getreg_b32 s71, hwreg(HW_REG_IB_STS, 0, 4)\n" \
  "s_cmp_le_u32 s71, 7\n" \
  "v_mfma_f32_16x16x32_bf16 v[144:147], v[72:75], v[72:75], 0\n" \
  "s_cbranch_scc1 LHXS11_%=\n" \
  "s_getreg_b32 s71, hwreg(HW_REG_IB_STS, 0, 4)\n" \
  "s_cmp_le_u32 s71, 7\n" \
  "v_mfma_f32_16x16x32_bf16 v[144:147], v[72:75], v[72:75], 0\n" \
  "s_cbranch_scc1 LHXS11_%=\n" \
  "s_getreg_b32 s71, hwreg(HW_REG_IB_STS, 0, 4)\n" \
  "s_cmp_le_u32 s71, 7\n" \
  "v_mfma_f32_16x16x32_bf16 v[144:147], v[72:75], v[72:75], 0\n" \
  "s_cbranch_scc1 LHXS11_%=\n" \
  "s_getreg_b32 s71, hwreg(HW_REG_IB_STS, 0, 4)\n" \
  "s_cmp_le_u32 s71, 7\n" \
  "v_mfma_f32_16x16x32_bf16 v[144:147], v[72:75], v[72:75], 0\n" \
  "s_cbranch_scc1 LHXS11_%=\n" \
  "s_getreg_b32 s71, hwreg(HW_REG_IB_STS, 0, 4)\n" \
  "s_cmp_le_u32 s71, 7\n" \
  "v_mfma_f32_16x16x32_bf16 v[144:147], v[72:75], v[72:75], 0\n" \
  "s_cbranch_scc1 LHXS11_%=\n" \
  "s_getreg_b32 s71, hwreg(HW_REG_IB_STS, 0, 4)\n" \
  "s_cmp_le_u32 s71, 7\n" \
  "v_mfma_f32_16x16x32_bf16 v[144:147], v[72:75], v[72:75], 0\n" \
  "s_cbranch_scc1 LHXS11_%=\n" \
  "s_getreg_b32 s71, hwreg(HW_REG_IB_STS, 0, 4)\n" \
  "s_cmp_le_u32 s71, 7\n" \
  "v_mfma_f32_16x16x32_bf16 v[144:147], v[72:75], v[72:75], 0\n" \
  "s_cbranch_scc1 LHXS11_%=\n" \
  "LHXS11_%=:\n" \
  "s_waitcnt vmcnt(7)\n" \
  "LS211_%=:\n" \
  "v_dot2_f32_bf16 v176, v112, s22, 0\n" \
  "v_dot2_f32_bf16 v177, v112, s23, 0\n" \
  "v_dot2_f32_bf16 v178, v113, s22, 0\n" \
  "v_dot2_f32_bf16 v179, v113, s23, 0\n" \
  "v_dot2_f32_bf16 v180, v114, s22, 0\n" \
  "v_dot2_f32_bf16 v181, v114, s23, 0\n" \
  "v_dot2_f32_bf16 v182, v115, s22, 0\n" \
  "v_dot2_f32_bf16 v183, v115, s23, 0\n" \
  "v_dot2_f32_bf16 v176, v116, s22, v176\n" \
  "v_dot2_f32_bf16 v177, v116, s23, v177\n" \
  "v_dot2_f32_bf16 v178, v117, s22, v178\n" \
  "v_dot2_f32_bf16 v179, v117, s23, v179\n" \
  "v_dot2_f32_bf16 v180, v118, s22, v180\n" \
  "v_dot2_f32_bf16 v181, v118, s23, v181\n" \
  "v_dot2_f32_bf16 v182, v119, s22, v182\n" \
  "v_dot2_f32_bf16 v183, v119, s23, v183\n" \
  "v_exp_f32 v176, v176\n" \
  "v_exp_f32 v177, v177\n" \
  "v_exp_f32 v178, v178\n" \
  "v_exp_f32 v179, v179\n" \
  "s_waitcnt vmcnt(6)\n" \
  "v_mfma_f32_16x16x32_bf16 v[160:163], v[56:59], v[120:123], 0\n" \
  "v_exp_f32 v180, v180\n" \
  "v_mfma_f32_16x16x32_bf16 v[144:147], v[40:43], v[120:123], 0\n" \
  "v_exp_f32 v181, v181\n" \
  "v_exp_f32 v182, v182\n" \
  "v_exp_f32 v183, v183\n" \
  "v_cvt_pk_bf16_f32 v184, v176, v177\n" \
  "v_cvt_pk_bf16_f32 v185, v178, v179\n" \
  "v_cvt_pk_bf16_f32 v186, v180, v181\n" \
  "v_cvt_pk_bf16_f32 v187, v182, v183\n" \
  "s_nop 1\n" \
  "v_mfma_f32_16x16x32_bf16 v[160:163], v[72:75], v[184:187], v[160:163]\n" \
  "v_mfma_f32_16x16x32_bf16 v[164:167], v[76:79], v[184:187], 0\n" \
  "s_waitcnt vmcnt(5)\n" \
  "v_mfma_f32_16x16x32_bf16 v[164:167], v[60:63], v[124:127], v[164:167]\n" \
  "v_rcp_f32 v188, v162\n" \
  "v_mfma_f32_16x16x32_bf16 v[148:151], v[44:47], v[124:127], 0\n" \
  "v_mul_f32 v144, v188, v144\n" \
  "v_mul_f32 v145, v188, v145\n" \
  "v_mfma_f32_16x16x32_bf16 v[168:171], v[80:83], v[184:187], 0\n" \
  "s_waitcnt vmcnt(4)\n" \
  "v_rcp_f32 v189, v166\n" \
  "v_mfma_f32_16x16x32_bf16 v[168:171], v[64:67], v[128:131], v[168:171]\n" \
  "v_mul_f32 v146, v188, v146\n" \
  "v_mul_f32 v147, v188, v147\n" \
  "v_mfma_f32_16x16x32_bf16 v[152:155], v[48:51], v[128:131], 0\n" \
  "v_mul_f32 v160, v188, v160\n" \
  "v_mul_f32 v161, v188, v161\n" \
  "v_mfma_f32_16x16x32_bf16 v[172:175], v[84:87], v[184:187], 0\n" \
  "s_waitcnt vmcnt(3)\n" \
  "v_rcp_f32 v190, v170\n" \
  "v_mfma_f32_16x16x32_bf16 v[172:175], v[68:71], v[132:135], v[172:175]\n" \
  "v_cndmask_b32 v167, v188, v189, s[28:29]\n" \
  "v_cndmask_b32 v167, v167, v190, s[26:27]\n" \
  "v_mfma_f32_16x16x32_bf16 v[156:159], v[52:55], v[132:135], 0\n" \
  "v_fmac_f32 v144, v189, v148\n" \
  "v_fmac_f32 v145, v189, v149\n" \
  "v_fmac_f32 v146, v189, v150\n" \
  "v_fmac_f32 v147, v189, v151\n" \
  "v_rcp_f32 v191, v174\n" \
  "v_fmac_f32 v160, v189, v164\n" \
  "v_cndmask_b32 v167, v167, v191, s[24:25]\n" \
  "v_pk_mul_f32 v[176:177], v[176:177], v[166:167] op_sel:[0,1] op_sel_hi:[1,1]\n" \
  "v_pk_mul_f32 v[178:179], v[178:179], v[166:167] op_sel:[0,1] op_sel_hi:[1,1]\n" \
  "v_pk_mul_f32 v[180:181], v[180:181], v[166:167] op_sel:[0,1] op_sel_hi:[1,1]\n" \
  "v_pk_mul_f32 v[182:183], v[182:183], v[166:167] op_sel:[0,1] op_sel_hi:[1,1]\n" \
  "v_cmp_nlt_f32 vcc, 0x0d800000, v167\n" \
  "v_cvt_pk_bf16_f32 v184, v176, v177\n" \
  "v_cvt_pk_bf16_f32 v185, v178, v179\n" \
  "v_cvt_pk_bf16_f32 v186, v180, v181\n" \
  "v_cvt_pk_bf16_f32 v187, v182, v183\n" \
  "v_fmac_f32 v161, v189, v165\n" \
  "v_pk_fma_f32 v[160:161], v[168:169], v[190:191], v[160:161] op_sel:[0,0,0] op_sel_hi:[1,0,1]\n" \
  "v_pk_fma_f32 v[160:161], v[172:173], v[190:191], v[160:161] op_sel:[0,1,0] op_sel_hi:[1,1,1]\n" \
  "v_pk_fma_f32 v[144:145], v[152:153], v[190:191], v[144:145] op_sel:[0,0,0] op_sel_hi:[1,0,1]\n" \
  "v_pk_fma_f32 v[146:147], v[154:155], v[190:191], v[146:147] op_sel:[0,0,0] op_sel_hi:[1,0,1]\n" \
  "v_pk_fma_f32 v[144:145], v[156:157], v[190:191], v[144:145] op_sel:[0,1,0] op_sel_hi:[1,1,1]\n" \
  "v_pk_fma_f32 v[146:147], v[158:159], v[190:191], v[146:147] op_sel:[0,1,0] op_sel_hi:[1,1,1]\n" \
  "s_waitcnt vmcnt(1)\n" \
  "v_mfma_f32_16x16x32_bf16 v[160:163], v[140:143], v[184:187], v[160:163]\n" \
  "s_mov_b64 s[80:81], vcc\n" \
  "v_mfma_f32_16x16x32_bf16 v[144:147], v[136:139], v[184:187], v[144:147]\n" \
  "s_mov_b64 exec, s[30:31]\n" \
  "s_nop 2\n" \
  "ds_write_b64 v22, v[160:161]\n" \
  "v_cmp_u_f32 s[78:79], v160, v161\n" \
  "s_nop 1\n" \
  "ds_write2_b64 v21, v[144:145], v[146:147] offset1:16\n" \
  "v_add_f32 v188, v144, v145\n" \
  "v_add_f32 v189, v146, v147\n" \
  "v_cmp_u_f32 vcc, v188, v189\n" \
  "s_or_b64 s[80:81], s[80:81], s[78:79]\n" \
  "s_or_b64 vcc, vcc, s[80:81]\n" \
  "s_cbranch_vccnz LSF11_%=\n" \
  "LSOK11_%=:\n" \
  "s_mov_b64 exec, -1\n" \
  "s_waitcnt lgkmcnt(0)\n" \
  "s_barrier\n" \
  "ds_read_b32 v188, v33\n" \
  "s_waitcnt lgkmcnt(0)\n" \
  "v_cmp_ne_u32 vcc, 0, v188\n" \
  "s_cbranch_vccnz LSR11_%=\n" \
  "LNL11_%=:\n" \
  "s_waitcnt lgkmcnt(0)\n" \
  "s_cmp_eq_u32 s73, 1\n" \
  "s_cbranch_scc1 LOOP11_%=\n" \
  "s_branch LDONE_%=\n" \
  "LMF11_%=:\n" \
  "v_mbcnt_lo_u32_b32 v130, -1, 0\n" \
  "v_mbcnt_hi_u32_b32 v130, -1, v130\n" \
  "v_lshlrev_b32 v130, 4, v130\n" \
  "v_mov_b32 v128, 0\n" \
  "ds_read_b128 v[132:135], v130 offset:0\n" \
  "ds_read_b128 v[136:139], v130 offset:1024\n" \
  "ds_read_b128 v[140:143], v130 offset:2048\n" \
  "ds_read_b128 v[144:147], v130 offset:3072\n" \
  "ds_read_b128 v[148:151], v130 offset:4096\n" \
  "s_waitcnt lgkmcnt(0)\n" \
  "v_max3_u32 v128, v128, v132, v133\n" \
  "v_max3_u32 v128, v128, v134, v135\n" \
  "v_max3_u32 v128, v128, v136, v137\n" \
  "v_max3_u32 v128, v128, v138, v139\n" \
  "v_max3_u32 v128, v128, v140, v141\n" \
  "v_max3_u32 v128, v128, v142, v143\n" \
  "v_max3_u32 v128, v128, v144, v145\n" \
  "v_max3_u32 v128, v128, v146, v147\n" \
  "v_max3_u32 v128, v128, v148, v149\n" \
  "v_max3_u32 v128, v128, v150, v151\n" \
  "ds_read_b128 v[132:135], v130 offset:5120\n" \
  "ds_read_b128 v[136:139], v130 offset:6144\n" \
  "ds_read_b128 v[140:143], v130 offset:7168\n" \
  "ds_read_b128 v[144:147], v130 offset:8192\n" \
  "ds_read_b128 v[148:151], v130 offset:9216\n" \
  "s_waitcnt lgkmcnt(0)\n" \
  "v_max3_u32 v128, v128, v132, v133\n" \
  "v_max3_u32 v128, v128, v134, v135\n" \
  "v_max3_u32 v128, v128, v136, v137\n" \
  "v_max3_u32 v128, v128, v138, v139\n" \
  "v_max3_u32 v128, v128, v140, v141\n" \
  "v_max3_u32 v128, v128, v142, v143\n" \
  "v_max3_u32 v128, v128, v144, v145\n" \
  "v_max3_u32 v128, v128, v146, v147\n" \
  "v_max3_u32 v128, v128, v148, v149\n" \
  "v_max3_u32 v128, v128, v150, v151\n" \
  "v_cmp_eq_u32 vcc, -1, v128\n" \
  "s_cbranch_vccz LMOK11_%=\n" \
  "s_cmp_gt_u32 s58, 4000\n" \
  "s_cbranch_scc1 LMOK11_%=\n" \
  "s_add_u32 s58, s58, 1\n" \
  "s_sub_u32 s64, s64, 1\n" \
  "s_mov_b32 s66, s65\n" \
  "s_waitcnt vmcnt(0) lgkmcnt(0)\n" \
  "s_barrier\n" \
  "s_branch LOOP11_%=\n" \
  "LKE11_%=:\n" \
  "s_waitcnt lgkmcnt(0)\n" \
  "buffer_store_dwordx4 v[88:91], v14, s[36:39], s69 offen sc1\n" \
  "s_branch LDONE_%=\n" \
  "LSF11_%=:\n" \
  "s_getpc_b64 s[78:79]\n" \
  "s_branch LSF_%=\n" \
  "s_branch LSOK11_%=\n" \
  "LSR11_%=:\n" \
  "v_mov_b32 v188, 0\n" \
  "s_add_u32 s59, s59, 1\n" \
  "s_barrier\n" \
  "ds_write_b32 v33, v188\n" \
  "s_waitcnt lgkmcnt(0)\n" \
  "s_branch LB11_%=\n" \
  "LSTART12_%=:\n" \
  "s_getpc_b64 s[80:81]\n" \
  "LPC12_%=:\n" \
  "s_add_u32 s82, s80, LEA12_%=-LPC12_%=+24\n" \
  "s_addc_u32 s83, s81, 0\n" \
  "s_add_u32 s84, s80, LEB12_%=-LPC12_%=+24\n" \
  "s_addc_u32 s85, s81, 0\n" \
  "LOOP12_%=:\n" \
  "v_readfirstlane_b32 s86, v34\n" \
  "s_add_u32 s71, s64, 1\n" \
  "s_min_u32 s71, s71, 63\n" \
  "s_lshr_b32 s72, s71, 4\n" \
  "s_lshl_b32 s72, s72, 13\n" \
  "s_lshl_b32 s71, s71, 9\n" \
  "s_add_u32 s71, s71, s72\n" \
  "s_mov_b32 s87, s71\n" \
  "s_lshl_b32 s40, s64, 7\n" \
  "s_mov_b32 m0, 0\n" \
  "LWA12_%=:\n" \
  "s_memrealtime s[74:75]\n" \
  "s_memtime s[76:77]\n" \
  "s_waitcnt lgkmcnt(0)\n" \
  "s_sub_i32 s72, s88, s74\n" \
  "s_mul_i32 s72, s72, s90\n" \
  "s_ashr_i32 s72, s72, 8\n" \
  "s_sub_i32 s71, s89, s76\n" \
  "s_min_i32 s71, s71, s72\n" \
  "s_sub_i32 s72, s72, 92\n" \
  "s_max_i32 s71, s71, s72\n" \
  "s_add_u32 s89, s71, s76\n" \
  "s_cmp_gt_i32 s71, 168\n" \
  "s_cbranch_scc1 LWA12_%=\n" \
  "s_cmp_lt_i32 s71, s94\n" \
  "s_cbranch_scc1 LSA12_%=\n" \
  "s_max_i32 s71, s71, 48\n" \
  "s_lshr_b32 s71, s71, 3\n" \
  "s_lshl_b32 s71, s71, 2\n" \
  "s_sub_u32 s78, s82, s71\n" \
  "s_subb_u32 s79, s83, 0\n" \
  "s_setpc_b64 s[78:79]\n" \
  "s_nop 1\n" \
  "s_nop 1\n" \
  "s_nop 1\n" \
  "s_nop 1\n" \
  "s_nop 1\n" \
  "s_nop 1\n" \
  "s_nop 1\n" \
  "s_nop 1\n" \
  "s_nop 1\n" \
  "s_nop 1\n" \
  "s_nop 1\n" \
  "s_nop 1\n" \
  "s_nop 1\n" \
  "s_nop 1\n" \
  "s_nop 1\n" \
  "s_nop 1\n" \
  "LEA12_%=:\n" \
  "ds_read_b32 v188, v33\n" \
  "buffer_load_dwordx4 v1, s[36:39], s67 offen sc1 lds\n" \
  "buffer_load_dwordx4 v1, s[36:39], s67 offen offset:1024 sc1 lds\n" \
  "buffer_load_dwordx4 v1, s[36:39], s67 offen offset:2048 sc1 lds\n" \
  "buffer_load_dwordx4 v1, s[36:39], s67 offen offset:3072 sc1 lds\n" \
  "s_mov_b32 m0, 4096\n" \
  "s_nop 0\n" \
  "buffer_load_dwordx4 v2, s[36:39], s67 offen sc1 lds\n" \
  "buffer_load_dwordx4 v2, s[36:39], s67 offen offset:1024 sc1 lds\n" \
  "buffer_load_dwordx4 v2, s[36:39], s67 offen offset:2048 sc1 lds\n" \
  "buffer_load_dwordx4 v2, s[36:39], s67 offen offset:3072 sc1 lds\n" \
  "s_mov_b32 m0, 8192\n" \
  "s_nop 0\n" \
  "buffer_load_dwordx4 v3, s[36:39], s67 offen sc1 lds\n" \
  "buffer_load_dwordx4 v3, s[36:39], s67 offen offset:1024 sc1 lds\n" \
  "s_waitcnt lgkmcnt(0)\n" \
  "v_cmp_ne_u32 vcc, 0, v188\n" \
  "s_cbranch_vccnz LSR12_%=\n" \
  "buffer_load_dword v35, v28, s[36:39], s87 offen sc1\n" \
  "buffer_load_dword v34, v29, s[36:39], s40 offen offset:128 sc1\n" \
  "v_readlane_b32 s70, v27, s64\n" \
  "s_mov_b32 s65, s66\n" \
  "s_xor_b32 s69, s66, 0x2000\n" \
  "s_add_u32 s64, s64, 1\n" \
  "s_barrier\n" \
  "v_mov_b32 v176, 0\n" \
  "v_mov_b32 v177, 0\n" \
  "v_mov_b32 v178, 0\n" \
  "v_mov_b32 v179, 0\n" \
  "v_mov_b32 v180, 0\n" \
  "v_mov_b32 v181, 0\n" \
  "v_mov_b32 v182, 0\n" \
  "v_mov_b32 v183, 0\n" \
  "v_readlane_b32 s66, v25, s64\n" \
  "v_readlane_b32 s68, v26, s64\n" \
  "s_cmp_eq_u32 s34, s64\n" \
  "s_cbranch_scc1 LRT12_%=\n" \
  "s_mov_b32 s34, s64\n" \
  "s_mov_b32 s71, s86\n" \
  "s_max_u32 s19, s19, s71\n" \
  "s_and_b32 s72, s71, 0xff\n" \
  "s_lshr_b32 s71, s71, 8\n" \
  "s_cselect_b32 s71, s71, s61\n" \
  "s_sub_i32 s72, s64, s72\n" \
  "s_sub_i32 s71, s71, s61\n" \
  "s_cmp_ge_i32 s72, 0\n" \
  "s_cselect_b32 s71, s71, 0\n" \
  "s_add_u32 s61, s61, s71\n" \
  "s_mul_i32 s72, s72, s71\n" \
  "s_lshl_b32 s72, s72, 1\n" \
  "s_add_u32 s88, s88, s72\n" \
  "s_mul_i32 s72, s72, s90\n" \
  "s_ashr_i32 s72, s72, 8\n" \
  "s_add_u32 s89, s89, s72\n" \
  "s_lshr_b32 s72, s18, 1\n" \
  "s_sub_u32 s80, s72, s18\n" \
  "s_mov_b32 s18, s72\n" \
  "s_add_u32 s71, s71, s80\n" \
  "s_add_u32 s50, s50, s71\n" \
  "s_mul_i32 s91, s50, s90\n" \
  "s_lshr_b32 s91, s91, 8\n" \
  "s_add_u32 s51, s51, s71\n" \
  "s_mul_i32 s92, s51, s90\n" \
  "s_lshr_b32 s92, s92, 8\n" \
  "LRT12_%=:\n" \
  "LM12_%=:\n" \
  "s_waitcnt vmcnt(11)\n" \
  "s_barrier\n" \
  "ds_read_b128 v[112:115], v5\n" \
  "ds_read_b128 v[116:119], v5 offset:512\n" \
  "s_waitcnt vmcnt(10)\n" \
  "s_barrier\n" \
  "ds_read_b128 v[120:123], v5 offset:1024\n" \
  "ds_read_b128 v[124:127], v5 offset:1536\n" \
  "s_waitcnt lgkmcnt(3)\n" \
  "v_smfmac_f32_16x16x64_bf16 v[176:179], v[112:115], a[0:7], v0\n" \
  "v_smfmac_f32_16x16x64_bf16 v[180:183], v[112:115], a[160:167], v0\n" \
  "s_waitcnt vmcnt(8)\n" \
  "s_waitcnt lgkmcnt(2)\n" \
  "v_smfmac_f32_16x16x64_bf16 v[176:179], v[116:119], a[8:15], v0\n" \
  "s_barrier\n" \
  "ds_read_b128 v[128:131], v5 offset:2048\n" \
  "v_smfmac_f32_16x16x64_bf16 v[180:183], v[116:119], a[168:175], v0\n" \
  "s_waitcnt lgkmcnt(2)\n" \
  "ds_read_b128 v[132:135], v5 offset:2560\n" \
  "v_smfmac_f32_16x16x64_bf16 v[176:179], v[120:123], a[16:23], v0\n" \
  "ds_read_b128 v[136:139], v5 offset:3072\n" \
  "v_smfmac_f32_16x16x64_bf16 v[180:183], v[120:123], a[176:183], v0\n" \
  "s_waitcnt vmcnt(6)\n" \
  "s_waitcnt lgkmcnt(3)\n" \
  "ds_read_b128 v[140:143], v5 offset:3584\n" \
  "v_smfmac_f32_16x16x64_bf16 v[176:179], v[124:127], a[24:31], v0\n" \
  "s_barrier\n" \
  "ds_read_b128 v[144:147], v5 offset:4096\n" \
  "v_smfmac_f32_16x16x64_bf16 v[180:183], v[124:127], a[184:191], v0\n" \
  "s_waitcnt lgkmcnt(4)\n" \
  "ds_read_b128 v[148:151], v5 offset:4608\n" \
  "v_smfmac_f32_16x16x64_bf16 v[176:179], v[128:131], a[32:39], v0\n" \
  "ds_read_b128 v[152:155], v5 offset:5120\n" \
  "v_smfmac_f32_16x16x64_bf16 v[180:183], v[128:131], a[192:199], v0\n" \
  "s_waitcnt vmcnt(4)\n" \
  "s_waitcnt lgkmcnt(5)\n" \
  "ds_read_b128 v[156:159], v5 offset:5632\n" \
  "v_smfmac_f32_16x16x64_bf16 v[176:179], v[132:135], a[40:47], v0\n" \
  "s_barrier\n" \
  "ds_read_b128 v[160:163], v5 offset:6144\n" \
  "v_smfmac_f32_16x16x64_bf16 v[180:183], v[132:135], a[200:207], v0\n" \
  "s_waitcnt lgkmcnt(6)\n" \
  "ds_read_b128 v[164:167], v5 offset:6656\n" \
  "v_smfmac_f32_16x16x64_bf16 v[176:179], v[136:139], a[48:55], v0\n" \
  "ds_read_b128 v[168:171], v5 offset:7168\n" \
  "v_smfmac_f32_16x16x64_bf16 v[180:183], v[136:139], a[208:215], v0\n" \
  "s_waitcnt vmcnt(2)\n" \
  "s_waitcnt lgkmcnt(7)\n" \
  "ds_read_b128 v[172:175], v5 offset:7680\n" \
  "v_smfmac_f32_16x16x64_bf16 v[176:179], v[140:143], a[56:63], v0\n" \
  "s_barrier\n" \
  "ds_read2_b32 v[112:113], v6 offset0:0 offset1:32\n" \
  "v_smfmac_f32_16x16x64_bf16 v[180:183], v[140:143], a[216:223], v0\n" \
  "s_waitcnt lgkmcnt(8)\n" \
  "ds_read2_b32 v[114:115], v6 offset0:64 offset1:96\n" \
  "v_smfmac_f32_16x16x64_bf16 v[176:179], v[144:147], a[64:71], v0\n" \
  "s_waitcnt lgkmcnt(8)\n" \
  "ds_read2_b32 v[116:117], v6 offset0:128 offset1:160\n" \
  "v_smfmac_f32_16x16x64_bf16 v[176:179], v[148:151], a[72:79], v0\n" \
  "s_waitcnt lgkmcnt(8)\n" \
  "ds_read2_b32 v[118:119], v6 offset0:192 offset1:224\n" \
  "v_smfmac_f32_16x16x64_bf16 v[176:179], v[152:155], a[80:87], v0\n" \
  "s_waitcnt lgkmcnt(8)\n" \
  "ds_read2_b32 v[120:121], v7 offset0:0 offset1:32\n" \
  "v_smfmac_f32_16x16x64_bf16 v[176:179], v[156:159], a[88:95], v0\n" \
  "s_waitcnt lgkmcnt(8)\n" \
  "ds_read2_b32 v[122:123], v7 offset0:64 offset1:96\n" \
  "v_smfmac_f32_16x16x64_bf16 v[176:179], v[160:163], a[96:103], v0\n" \
  "s_waitcnt lgkmcnt(8)\n" \
  "ds_read2_b32 v[124:125], v7 offset0:128 offset1:160\n" \
  "v_smfmac_f32_16x16x64_bf16 v[176:179], v[164:167], a[104:111], v0\n" \
  "s_waitcnt lgkmcnt(8)\n" \
  "ds_read2_b32 v[126:127], v7 offset0:192 offset1:224\n" \
  "v_smfmac_f32_16x16x64_bf16 v[176:179], v[168:171], a[112:119], v0\n" \
  "s_waitcnt lgkmcnt(8)\n" \
  "v_smfmac_f32_16x16x64_bf16 v[176:179], v[172:175], a[120:127], v0\n" \
  "s_waitcnt lgkmcnt(6)\n" \
  "v_smfmac_f32_16x16x64_bf16 v[176:179], v[112:115], a[128:135], v0\n" \
  "s_waitcnt lgkmcnt(4)\n" \
  "v_smfmac_f32_16x16x64_bf16 v[176:179], v[116:119], a[136:143], v0\n" \
  "s_waitcnt lgkmcnt(2)\n" \
  "v_smfmac_f32_16x16x64_bf16 v[176:179], v[120:123], a[144:151], v0\n" \
  "s_waitcnt lgkmcnt(0)\n" \
  "v_smfmac_f32_16x16x64_bf16 v[176:179], v[124:127], a[152:159], v0\n" \
  "v_smfmac_f32_16x16x64_bf16 v[180:183], v[144:147], a[224:231], v0\n" \
  "v_smfmac_f32_16x16x64_bf16 v[180:183], v[148:151], a[232:239], v0\n" \
  "v_cvt_pk_bf16_f32 v184, v176, v177\n" \
  "v_smfmac_f32_16x16x64_bf16 v[180:183], v[152:155], a[240:247], v0\n" \
  "v_cvt_pk_bf16_f32 v185, v178, v179\n" \
  "v_smfmac_f32_16x16x64_bf16 v[180:183], v[156:159], a[248:255], v0\n" \
  "ds_write_b64 v8, v[184:185]\n" \
  "v_add_f32 v186, v176, v177\n" \
  "v_smfmac_f32_16x16x64_bf16 v[180:183], v[160:163], v[192:199], v0\n" \
  "v_add_f32 v187, v178, v179\n" \
  "v_smfmac_f32_16x16x64_bf16 v[180:183], v[164:167], v[200:207], v0\n" \
  "v_cmp_u_f32 vcc, v186, v187\n" \
  "v_smfmac_f32_16x16x64_bf16 v[180:183], v[168:171], v[208:215], v0\n" \
  "v_smfmac_f32_16x16x64_bf16 v[180:183], v[172:175], v[216:223], v0\n" \
  "s_cbranch_vccnz LMF12_%=\n" \
  "LMOK12_%=:\n" \
  "v_smfmac_f32_16x16x64_bf16 v[180:183], v[112:115], v[224:231], v0\n" \
  "s_waitcnt lgkmcnt(0)\n" \
  "s_barrier\n" \
  "v_smfmac_f32_16x16x64_bf16 v[180:183], v[116:119], v[232:239], v0\n" \
  "v_smfmac_f32_16x16x64_bf16 v[180:183], v[120:123], v[240:247], v0\n" \
  "v_smfmac_f32_16x16x64_bf16 v[180:183], v[124:127], v[248:255], v0\n" \
  "s_nop 3\n" \
  "v_mov_b32 v31, v31\n" \
  "s_nop 2\n" \
  "v_cvt_pk_bf16_f32 v186, v180, v181\n" \
  "v_cvt_pk_bf16_f32 v187, v182, v183\n" \
  "ds_write_b64 v9, v[186:187]\n" \
  "s_waitcnt lgkmcnt(0)\n" \
  "s_barrier\n" \
  "s_nop 0\n" \
  "ds_read_b64_tr_b16 v[88:89], v12\n" \
  "ds_read_b64_tr_b16 v[90:91], v12 offset:32\n" \
  "s_mov_b32 s67, s66\n" \
  "s_cmp_eq_u32 s64, 64\n" \
  "s_cbranch_scc1 LKE12_%=\n" \
  "s_add_u32 s88, s88, s50\n" \
  "s_add_u32 s89, s89, s91\n" \
  "s_waitcnt vmcnt(0)\n" \
  "v_cmp_gt_u32 vcc, 16, v35\n" \
  "s_cbranch_vccnz LFF12_%=\n" \
  "LFOK12_%=:\n" \
  "LB12_%=:\n" \
  "LWB12_%=:\n" \
  "s_memrealtime s[74:75]\n" \
  "s_memtime s[76:77]\n" \
  "s_waitcnt lgkmcnt(0)\n" \
  "s_sub_i32 s72, s88, s74\n" \
  "s_mul_i32 s72, s72, s90\n" \
  "s_ashr_i32 s72, s72, 8\n" \
  "s_sub_i32 s71, s89, s76\n" \
  "s_min_i32 s71, s71, s72\n" \
  "s_sub_i32 s72, s72, 92\n" \
  "s_max_i32 s71, s71, s72\n" \
  "s_add_u32 s89, s71, s76\n" \
  "s_cmp_gt_i32 s71, 168\n" \
  "s_cbranch_scc1 LWB12_%=\n" \
  "s_cmp_lt_i32 s71, s94\n" \
  "s_cbranch_scc1 LSB12_%=\n" \
  "s_max_i32 s71, s71, 48\n" \
  "s_lshr_b32 s71, s71, 3\n" \
  "s_lshl_b32 s71, s71, 2\n" \
  "s_sub_u32 s78, s84, s71\n" \
  "s_subb_u32 s79, s85, 0\n" \
  "s_setpc_b64 s[78:79]\n" \
  "s_nop 1\n" \
  "s_nop 1\n" \
  "s_nop 1\n" \
  "s_nop 1\n" \
  "s_nop 1\n" \
  "s_nop 1\n" \
  "s_nop 1\n" \
  "s_nop 1\n" \
  "s_nop 1\n" \
  "s_nop 1\n" \
  "s_nop 1\n" \
  "s_nop 1\n" \
  "s_nop 1\n" \
  "s_nop 1\n" \
  "s_nop 1\n" \
  "s_nop 1\n" \
  "LEB12_%=:\n" \
  "s_barrier\n" \
  "buffer_load_dwordx4 v[116:119], v15, s[36:39], s65 offen offset:128 sc1\n" \
  "s_barrier\n" \
  "buffer_load_dwordx4 v[112:115], v15, s[36:39], s65 offen sc1\n" \
  "s_add_u32 s88, s88, s51\n" \
  "s_add_u32 s89, s89, s92\n" \
  "s_barrier\n" \
  "buffer_load_dwordx4 v[120:123], v16, s[36:39], s65 offen sc1\n" \
  "s_barrier\n" \
  "buffer_load_dwordx4 v[124:127], v16, s[36:39], s65 offen offset:512 sc1\n" \
  "s_barrier\n" \
  "buffer_load_dwordx4 v[128:131], v16, s[36:39], s65 offen offset:1024 sc1\n" \
  "s_barrier\n" \
  "buffer_load_dwordx4 v[132:135], v16, s[36:39], s65 offen offset:1536 sc1\n" \
  "ds_read_b128 v[40:43], v19\n" \
  "ds_read_b128 v[44:47], v19 offset:512\n" \
  "ds_read_b128 v[48:51], v19 offset:1024\n" \
  "ds_read_b128 v[52:55], v19 offset:1536\n" \
  "s_mov_b64 exec, s[46:47]\n" \
  "ds_read_b128 v[56:59], v20\n" \
  "ds_read_b128 v[60:63], v20 offset:128\n" \
  "ds_read_b128 v[64:67], v20 offset:256\n" \
  "ds_read_b128 v[68:71], v20 offset:384\n" \
  "s_mov_b64 exec, -1\n" \
  "v_readfirstlane_b32 s86, v34\n" \
  "s_add_u32 s71, s64, 1\n" \
  "s_min_u32 s71, s71, 63\n" \
  "s_lshr_b32 s72, s71, 4\n" \
  "s_lshl_b32 s72, s72, 13\n" \
  "s_lshl_b32 s71, s71, 9\n" \
  "s_add_u32 s71, s71, s72\n" \
  "s_mov_b32 s87, s71\n" \
  "s_lshl_b32 s40, s64, 7\n" \
  "s_mov_b32 m0, 0\n" \
  "s_nop 7\n" \
  "buffer_load_dwordx4 v[136:139], v17, s[36:39], s65 offen sc1\n" \
  "buffer_load_dwordx4 v[140:143], v18, s[36:39], s65 offen sc1\n" \
  "s_barrier\n" \
  "s_waitcnt lgkmcnt(0)\n" \
  "buffer_store_dwordx4 v[88:91], v14, s[36:39], s69 offen sc1\n" \
  "s_sub_u32 s72, s74, s95\n" \
  "s_cmp_lt_u32 s64, 3\n" \
  "s_cbranch_scc1 LRB12_%=\n" \
  "s_cmp_gt_u32 s72, 900\n" \
  "s_cbranch_scc0 LRC12_%=\n" \
  "s_sub_u32 s71, s76, s96\n" \
  "s_sub_u32 s72, s74, s95\n" \
  "v_cvt_f32_u32 v36, s71\n" \
  "v_cvt_f32_u32 v37, s72\n" \
  "v_rcp_f32 v37, v37\n" \
  "v_mul_f32 v36, 0x43820000, v36\n" \
  "v_mul_f32 v36, v36, v37\n" \
  "v_cvt_u32_f32 v36, v36\n" \
  "s_nop 0\n" \
  "v_readfirstlane_b32 s71, v36\n" \
  "s_min_u32 s90, s71, 6400\n" \
  "LRB12_%=:\n" \
  "s_mov_b32 s95, s74\n" \
  "s_mov_b32 s96, s76\n" \
  "LRC12_%=:\n" \
  "s_waitcnt lgkmcnt(0)\n" \
  "s_getreg_b32 s71, hwreg(HW_REG_IB_STS, 0, 4)\n" \
  "s_cmp_le_u32 s71, 7\n" \
  "v_mfma_f32_16x16x32_bf16 v[144:147], v[72:75], v[72:75], 0\n" \
  "s_cbranch_scc1 LHXS12_%=\n" \
  "s_getreg_b32 s71, hwreg(HW_REG_IB_STS, 0, 4)\n" \
  "s_cmp_le_u32 s71, 7\n" \
  "v_mfma_f32_16x16x32_bf16 v[144:147], v[72:75], v[72:75], 0\n" \
  "s_cbranch_scc1 LHXS12_%=\n" \
  "s_getreg_b32 s71, hwreg(HW_REG_IB_STS, 0, 4)\n" \
  "s_cmp_le_u32 s71, 7\n" \
  "v_mfma_f32_16x16x32_bf16 v[144:147], v[72:75], v[72:75], 0\n" \
  "s_cbranch_scc1 LHXS12_%=\n" \
  "s_getreg_b32 s71, hwreg(HW_REG_IB_STS, 0, 4)\n" \
  "s_cmp_le_u32 s71, 7\n" \
  "v_mfma_f32_16x16x32_bf16 v[144:147], v[72:75], v[72:75], 0\n" \
  "s_cbranch_scc1 LHXS12_%=\n" \
  "s_getreg_b32 s71, hwreg(HW_REG_IB_STS, 0, 4)\n" \
  "s_cmp_le_u32 s71, 7\n" \
  "v_mfma_f32_16x16x32_bf16 v[144:147], v[72:75], v[72:75], 0\n" \
  "s_cbranch_scc1 LHXS12_%=\n" \
  "s_getreg_b32 s71, hwreg(HW_REG_IB_STS, 0, 4)\n" \
  "s_cmp_le_u32 s71, 7\n" \
  "v_mfma_f32_16x16x32_bf16 v[144:147], v[72:75], v[72:75], 0\n" \
  "s_cbranch_scc1 LHXS12_%=\n" \
  "s_getreg_b32 s71, hwreg(HW_REG_IB_STS, 0, 4)\n" \
  "s_cmp_le_u32 s71, 7\n" \
  "v_mfma_f32_16x16x32_bf16 v[144:147], v[72:75], v[72:75], 0\n" \
  "s_cbranch_scc1 LHXS12_%=\n" \
  "s_getreg_b32 s71, hwreg(HW_REG_IB_STS, 0, 4)\n" \
  "s_cmp_le_u32 s71, 7\n" \
  "v_mfma_f32_16x16x32_bf16 v[144:147], v[72:75], v[72:75], 0\n" \
  "s_cbranch_scc1 LHXS12_%=\n" \
  "s_getreg_b32 s71, hwreg(HW_REG_IB_STS, 0, 4)\n" \
  "s_cmp_le_u32 s71, 7\n" \
  "v_mfma_f32_16x16x32_bf16 v[144:147], v[72:75], v[72:75], 0\n" \
  "s_cbranch_scc1 LHXS12_%=\n" \
  "s_getreg_b32 s71, hwreg(HW_REG_IB_STS, 0, 4)\n" \
  "s_cmp_le_u32 s71, 7\n" \
  "v_mfma_f32_16x16x32_bf16 v[144:147], v[72:75], v[72:75], 0\n" \
  "s_cbranch_scc1 LHXS12_%=\n" \
  "s_getreg_b32 s71, hwreg(HW_REG_IB_STS, 0, 4)\n" \
  "s_cmp_le_u32 s71, 7\n" \
  "v_mfma_f32_16x16x32_bf16 v[144:147], v[72:75], v[72:75], 0\n" \
  "s_cbranch_scc1 LHXS12_%=\n" \
  "s_getreg_b32 s71, hwreg(HW_REG_IB_STS, 0, 4)\n" \
  "s_cmp_le_u32 s71, 7\n" \
  "v_mfma_f32_16x16x32_bf16 v[144:147], v[72:75], v[72:75], 0\n" \
  "s_cbranch_scc1 LHXS12_%=\n" \
  "s_getreg_b32 s71, hwreg(HW_REG_IB_STS, 0, 4)\n" \
  "s_cmp_le_u32 s71, 7\n" \
  "v_mfma_f32_16x16x32_bf16 v[144:147], v[72:75], v[72:75], 0\n" \
  "s_cbranch_scc1 LHXS12_%=\n" \
  "s_getreg_b32 s71, hwreg(HW_REG_IB_STS, 0, 4)\n" \
  "s_cmp_le_u32 s71, 7\n" \
  "v_mfma_f32_16x16x32_bf16 v[144:147], v[72:75], v[72:75], 0\n" \
  "s_cbranch_scc1 LHXS12_%=\n" \
  "s_getreg_b32 s71, hwreg(HW_REG_IB_STS, 0, 4)\n" \
  "s_cmp_le_u32 s71, 7\n" \
  "v_mfma_f32_16x16x32_bf16 v[144:147], v[72:75], v[72:75], 0\n" \
  "s_cbranch_scc1 LHXS12_%=\n" \
  "s_getreg_b32 s71, hwreg(HW_REG_IB_STS, 0, 4)\n" \
  "s_cmp_le_u32 s71, 7\n" \
  "v_mfma_f32_16x16x32_bf16 v[144:147], v[72:75], v[72:75], 0\n" \
  "s_cbranch_scc1 LHXS12_%=\n" \
  "s_getreg_b32 s71, hwreg(HW_REG_IB_STS, 0, 4)\n" \
  "s_cmp_le_u32 s71, 7\n" \
  "v_mfma_f32_16x16x32_bf16 v[144:147], v[72:75], v[72:75], 0\n" \
  "s_cbranch_scc1 LHXS12_%=\n" \
  "s_getreg_b32 s71, hwreg(HW_REG_IB_STS, 0, 4)\n" \
  "s_cmp_le_u32 s71, 7\n" \
  "v_mfma_f32_16x16x32_bf16 v[144:147], v[72:75], v[72:75], 0\n" \
  "s_cbranch_scc1 LHXS12_%=\n" \
  "s_getreg_b32 s71, hwreg(HW_REG_IB_STS, 0, 4)\n" \
  "s_cmp_le_u32 s71, 7\n" \
  "v_mfma_f32_16x16x32_bf16 v[144:147], v[72:75], v[72:75], 0\n" \
  "s_cbranch_scc1 LHXS12_%=\n" \
  "s_getreg_b32 s71, hwreg(HW_REG_IB_STS, 0, 4)\n" \
  "s_cmp_le_u32 s71, 7\n" \
  "v_mfma_f32_16x16x32_bf16 v[144:147], v[72:75], v[72:75], 0\n" \
  "s_cbranch_scc1 LHXS12_%=\n" \
  "LHXS12_%=:\n" \
  "s_waitcnt vmcnt(7)\n" \
  "LS212_%=:\n" \
  "v_dot2_f32_bf16 v176, v112, s22, 0\n" \
  "v_dot2_f32_bf16 v177, v112, s23, 0\n" \
  "v_dot2_f32_bf16 v178, v113, s22, 0\n" \
  "v_dot2_f32_bf16 v179, v113, s23, 0\n" \
  "v_dot2_f32_bf16 v180, v114, s22, 0\n" \
  "v_dot2_f32_bf16 v181, v114, s23, 0\n" \
  "v_dot2_f32_bf16 v182, v115, s22, 0\n" \
  "v_dot2_f32_bf16 v183, v115, s23, 0\n" \
  "v_dot2_f32_bf16 v176, v116, s22, v176\n" \
  "v_dot2_f32_bf16 v177, v116, s23, v177\n" \
  "v_dot2_f32_bf16 v178, v117, s22, v178\n" \
  "v_dot2_f32_bf16 v179, v117, s23, v179\n" \
  "v_dot2_f32_bf16 v180, v118, s22, v180\n" \
  "v_dot2_f32_bf16 v181, v118, s23, v181\n" \
  "v_dot2_f32_bf16 v182, v119, s22, v182\n" \
  "v_dot2_f32_bf16 v183, v119, s23, v183\n" \
  "v_exp_f32 v176, v176\n" \
  "v_exp_f32 v177, v177\n" \
  "v_exp_f32 v178, v178\n" \
  "v_exp_f32 v179, v179\n" \
  "s_waitcnt vmcnt(6)\n" \
  "v_mfma_f32_16x16x32_bf16 v[160:163], v[56:59], v[120:123], 0\n" \
  "v_exp_f32 v180, v180\n" \
  "v_mfma_f32_16x16x32_bf16 v[144:147], v[40:43], v[120:123], 0\n" \
  "v_exp_f32 v181, v181\n" \
  "v_exp_f32 v182, v182\n" \
  "v_exp_f32 v183, v183\n" \
  "v_cvt_pk_bf16_f32 v184, v176, v177\n" \
  "v_cvt_pk_bf16_f32 v185, v178, v179\n" \
  "v_cvt_pk_bf16_f32 v186, v180, v181\n" \
  "v_cvt_pk_bf16_f32 v187, v182, v183\n" \
  "s_nop 1\n" \
  "v_mfma_f32_16x16x32_bf16 v[160:163], v[72:75], v[184:187], v[160:163]\n" \
  "v_mfma_f32_16x16x32_bf16 v[164:167], v[76:79], v[184:187], 0\n" \
  "s_waitcnt vmcnt(5)\n" \
  "v_mfma_f32_16x16x32_bf16 v[164:167], v[60:63], v[124:127], v[164:167]\n" \
  "v_rcp_f32 v188, v162\n" \
  "v_mfma_f32_16x16x32_bf16 v[148:151], v[44:47], v[124:127], 0\n" \
  "v_mul_f32 v144, v188, v144\n" \
  "v_mul_f32 v145, v188, v145\n" \
  "v_mfma_f32_16x16x32_bf16 v[168:171], v[80:83], v[184:187], 0\n" \
  "s_waitcnt vmcnt(4)\n" \
  "v_rcp_f32 v189, v166\n" \
  "v_mfma_f32_16x16x32_bf16 v[168:171], v[64:67], v[128:131], v[168:171]\n" \
  "v_mul_f32 v146, v188, v146\n" \
  "v_mul_f32 v147, v188, v147\n" \
  "v_mfma_f32_16x16x32_bf16 v[152:155], v[48:51], v[128:131], 0\n" \
  "v_mul_f32 v160, v188, v160\n" \
  "v_mul_f32 v161, v188, v161\n" \
  "v_mfma_f32_16x16x32_bf16 v[172:175], v[84:87], v[184:187], 0\n" \
  "s_waitcnt vmcnt(3)\n" \
  "v_rcp_f32 v190, v170\n" \
  "v_mfma_f32_16x16x32_bf16 v[172:175], v[68:71], v[132:135], v[172:175]\n" \
  "v_cndmask_b32 v167, v188, v189, s[28:29]\n" \
  "v_cndmask_b32 v167, v167, v190, s[26:27]\n" \
  "v_mfma_f32_16x16x32_bf16 v[156:159], v[52:55], v[132:135], 0\n" \
  "v_fmac_f32 v144, v189, v148\n" \
  "v_fmac_f32 v145, v189, v149\n" \
  "v_fmac_f32 v146, v189, v150\n" \
  "v_fmac_f32 v147, v189, v151\n" \
  "v_rcp_f32 v191, v174\n" \
  "v_fmac_f32 v160, v189, v164\n" \
  "v_cndmask_b32 v167, v167, v191, s[24:25]\n" \
  "v_pk_mul_f32 v[176:177], v[176:177], v[166:167] op_sel:[0,1] op_sel_hi:[1,1]\n" \
  "v_pk_mul_f32 v[178:179], v[178:179], v[166:167] op_sel:[0,1] op_sel_hi:[1,1]\n" \
  "v_pk_mul_f32 v[180:181], v[180:181], v[166:167] op_sel:[0,1] op_sel_hi:[1,1]\n" \
  "v_pk_mul_f32 v[182:183], v[182:183], v[166:167] op_sel:[0,1] op_sel_hi:[1,1]\n" \
  "v_cmp_nlt_f32 vcc, 0x0d800000, v167\n" \
  "v_cvt_pk_bf16_f32 v184, v176, v177\n" \
  "v_cvt_pk_bf16_f32 v185, v178, v179\n" \
  "v_cvt_pk_bf16_f32 v186, v180, v181\n" \
  "v_cvt_pk_bf16_f32 v187, v182, v183\n" \
  "v_fmac_f32 v161, v189, v165\n" \
  "v_pk_fma_f32 v[160:161], v[168:169], v[190:191], v[160:161] op_sel:[0,0,0] op_sel_hi:[1,0,1]\n" \
  "v_pk_fma_f32 v[160:161], v[172:173], v[190:191], v[160:161] op_sel:[0,1,0] op_sel_hi:[1,1,1]\n" \
  "v_pk_fma_f32 v[144:145], v[152:153], v[190:191], v[144:145] op_sel:[0,0,0] op_sel_hi:[1,0,1]\n" \
  "v_pk_fma_f32 v[146:147], v[154:155], v[190:191], v[146:147] op_sel:[0,0,0] op_sel_hi:[1,0,1]\n" \
  "v_pk_fma_f32 v[144:145], v[156:157], v[190:191], v[144:145] op_sel:[0,1,0] op_sel_hi:[1,1,1]\n" \
  "v_pk_fma_f32 v[146:147], v[158:159], v[190:191], v[146:147] op_sel:[0,1,0] op_sel_hi:[1,1,1]\n" \
  "s_waitcnt vmcnt(1)\n" \
  "v_mfma_f32_16x16x32_bf16 v[160:163], v[140:143], v[184:187], v[160:163]\n" \
  "s_mov_b64 s[80:81], vcc\n" \
  "v_mfma_f32_16x16x32_bf16 v[144:147], v[136:139], v[184:187], v[144:147]\n" \
  "s_mov_b64 exec, s[30:31]\n" \
  "s_nop 2\n" \
  "ds_write_b64 v22, v[160:161]\n" \
  "v_cmp_u_f32 s[78:79], v160, v161\n" \
  "s_nop 1\n" \
  "ds_write2_b64 v21, v[144:145], v[146:147] offset1:16\n" \
  "v_add_f32 v188, v144, v145\n" \
  "v_add_f32 v189, v146, v147\n" \
  "v_cmp_u_f32 vcc, v188, v189\n" \
  "s_or_b64 s[80:81], s[80:81], s[78:79]\n" \
  "s_or_b64 vcc, vcc, s[80:81]\n" \
  "s_cbranch_vccnz LSF12_%=\n" \
  "LSOK12_%=:\n" \
  "s_mov_b64 exec, -1\n" \
  "s_waitcnt lgkmcnt(0)\n" \
  "s_barrier\n" \
  "s_cmp_eq_u32 s73, 1\n" \
  "s_cbranch_scc1 LWA12_%=\n" \
  "s_branch LDONE_%=\n" \
  "ds_read_b32 v188, v33\n" \
  "s_waitcnt lgkmcnt(0)\n" \
  "v_cmp_ne_u32 vcc, 0, v188\n" \
  "s_cbranch_vccnz LSR12_%=\n" \
  "LNL12_%=:\n" \
  "s_waitcnt lgkmcnt(0)\n" \
  "s_cmp_eq_u32 s73, 1\n" \
  "s_cbranch_scc1 LOOP12_%=\n" \
  "s_branch LDONE_%=\n" \
  "LSA12_%=:\n" \
  "ds_read_b32 v188, v33\n" \
  "s_waitcnt lgkmcnt(0)\n" \
  "v_cmp_ne_u32 vcc, 0, v188\n" \
  "s_cbranch_vccnz LSR12_%=\n" \
  "s_add_u32 s88, s88, s52\n" \
  "s_add_u32 s89, s89, s93\n" \
  "s_add_u32 s97, s97, 1\n" \
  "s_branch LWA12_%=\n" \
  "LSB12_%=:\n" \
  "s_add_u32 s88, s88, s52\n" \
  "s_add_u32 s89, s89, s93\n" \
  "s_add_u32 s97, s97, 1\n" \
  "s_branch LWB12_%=\n" \
  "LMF12_%=:\n" \
  "v_mbcnt_lo_u32_b32 v130, -1, 0\n" \
  "v_mbcnt_hi_u32_b32 v130, -1, v130\n" \
  "v_lshlrev_b32 v130, 4, v130\n" \
  "v_mov_b32 v128, 0\n" \
  "ds_read_b128 v[132:135], v130 offset:0\n" \
  "ds_read_b128 v[136:139], v130 offset:1024\n" \
  "ds_read_b128 v[140:143], v130 offset:2048\n" \
  "ds_read_b128 v[144:147], v130 offset:3072\n" \
  "ds_read_b128 v[148:151], v130 offset:4096\n" \
  "s_waitcnt lgkmcnt(0)\n" \
  "v_max3_u32 v128, v128, v132, v133\n" \
  "v_max3_u32 v128, v128, v134, v135\n" \
  "v_max3_u32 v128, v128, v136, v137\n" \
  "v_max3_u32 v128, v128, v138, v139\n" \
  "v_max3_u32 v128, v128, v140, v141\n" \
  "v_max3_u32 v128, v128, v142, v143\n" \
  "v_max3_u32 v128, v128, v144, v145\n" \
  "v_max3_u32 v128, v128, v146, v147\n" \
  "v_max3_u32 v128, v128, v148, v149\n" \
  "v_max3_u32 v128, v128, v150, v151\n" \
  "ds_read_b128 v[132:135], v130 offset:5120\n" \
  "ds_read_b128 v[136:139], v130 offset:6144\n" \
  "ds_read_b128 v[140:143], v130 offset:7168\n" \
  "ds_read_b128 v[144:147], v130 offset:8192\n" \
  "ds_read_b128 v[148:151], v130 offset:9216\n" \
  "s_waitcnt lgkmcnt(0)\n" \
  "v_max3_u32 v128, v128, v132, v133\n" \
  "v_max3_u32 v128, v128, v134, v135\n" \
  "v_max3_u32 v128, v128, v136, v137\n" \
  "v_max3_u32 v128, v128, v138, v139\n" \
  "v_max3_u32 v128, v128, v140, v141\n" \
  "v_max3_u32 v128, v128, v142, v143\n" \
  "v_max3_u32 v128, v128, v144, v145\n" \
  "v_max3_u32 v128, v128, v146, v147\n" \
  "v_max3_u32 v128, v128, v148, v149\n" \
  "v_max3_u32 v128, v128, v150, v151\n" \
  "v_cmp_eq_u32 vcc, -1, v128\n" \
  "s_cbranch_vccz LMOK12_%=\n" \
  "s_cmp_gt_u32 s58, 4000\n" \
  "s_cbranch_scc1 LMOK12_%=\n" \
  "s_add_u32 s58, s58, 1\n" \
  "s_sub_u32 s64, s64, 1\n" \
  "s_mov_b32 s66, s65\n" \
  "s_add_u32 s88, s88, s52\n" \
  "s_add_u32 s89, s89, s93\n" \
  "s_add_u32 s97, s97, 1\n" \
  "s_waitcnt vmcnt(0) lgkmcnt(0)\n" \
  "s_barrier\n" \
  "s_branch LOOP12_%=\n" \
  "LKE12_%=:\n" \
  "s_waitcnt lgkmcnt(0)\n" \
  "buffer_store_dwordx4 v[88:91], v14, s[36:39], s69 offen sc1\n" \
  "s_branch LDONE_%=\n" \
  "LFF12_%=:\n" \
  "s_add_u32 s71, s64, 0\n" \
  "s_min_u32 s71, s71, 63\n" \
  "s_lshr_b32 s72, s71, 4\n" \
  "s_lshl_b32 s72, s72, 13\n" \
  "s_lshl_b32 s71, s71, 9\n" \
  "s_add_u32 s71, s71, s72\n" \
  "LOF12_%=:\n" \
  "buffer_load_dword v35, v28, s[36:39], s71 offen sc1\n" \
  "s_waitcnt vmcnt(0)\n" \
  "v_cmp_gt_u32 vcc, 16, v35\n" \
  "s_cbranch_vccz LOXF12_%=\n" \
  "s_add_u32 s60, s60, 1\n" \
  "s_cmp_gt_u32 s60, 20000\n" \
  "s_cbranch_scc1 LOXF12_%=\n" \
  "s_sleep 16\n" \
  "s_branch LOF12_%=\n" \
  "LOXF12_%=:\n" \
  "s_branch LFOK12_%=\n" \
  "LSF12_%=:\n" \
  "s_getpc_b64 s[78:79]\n" \
  "s_branch LSF_%=\n" \
  "s_branch LSOK12_%=\n" \
  "LSR12_%=:\n" \
  "v_mov_b32 v188, 0\n" \
  "s_add_u32 s59, s59, 1\n" \
  "s_barrier\n" \
  "ds_write_b32 v33, v188\n" \
  "s_sub_u32 s88, s88, s51\n" \
  "s_sub_u32 s89, s89, s92\n" \
  "s_add_u32 s88, s88, s52\n" \
  "s_add_u32 s89, s89, s93\n" \
  "s_add_u32 s97, s97, 1\n" \
  "s_waitcnt lgkmcnt(0)\n" \
  "s_branch LB12_%=\n" \
  "LSTART13_%=:\n" \
  "LOOP13_%=:\n" \
  "v_readlane_b32 s70, v27, s64\n" \
  "s_mov_b32 s65, s66\n" \
  "s_xor_b32 s69, s66, 0x2000\n" \
  "s_add_u32 s64, s64, 1\n" \
  "v_mov_b32 v176, 0\n" \
  "v_mov_b32 v177, 0\n" \
  "v_mov_b32 v178, 0\n" \
  "v_mov_b32 v179, 0\n" \
  "v_mov_b32 v180, 0\n" \
  "v_mov_b32 v181, 0\n" \
  "v_mov_b32 v182, 0\n" \
  "v_mov_b32 v183, 0\n" \
  "v_readlane_b32 s66, v25, s64\n" \
  "v_readlane_b32 s68, v26, s64\n" \
  "s_add_u32 s93, s66, 81920\n" \
  "s_add_u32 s94, s66, 163840\n" \
  "s_mov_b32 m0, 36864\n" \
  "s_nop 0\n" \
  "s_barrier\n" \
  "buffer_load_dwordx4 v3, s[36:39], s70 offen sc1 lds\n" \
  "buffer_load_dwordx4 v3, s[36:39], s70 offen offset:1024 sc1 lds\n" \
  "buffer_load_dwordx4 v3, s[36:39], s70 offen offset:2048 sc1 lds\n" \
  "buffer_load_dwordx4 v3, s[36:39], s70 offen offset:3072 sc1 lds\n" \
  "LM13_%=:\n" \
  "s_barrier\n" \
  "ds_read_b128 v[112:115], v5\n" \
  "ds_read_b128 v[116:119], v5 offset:512\n" \
  "s_barrier\n" \
  "ds_read_b128 v[120:123], v5 offset:1024\n" \
  "ds_read_b128 v[124:127], v5 offset:1536\n" \
  "s_waitcnt lgkmcnt(3)\n" \
  "v_smfmac_f32_16x16x64_bf16 v[176:179], v[112:115], a[0:7], v0\n" \
  "v_smfmac_f32_16x16x64_bf16 v[180:183], v[112:115], a[160:167], v0\n" \
  "s_waitcnt lgkmcnt(2)\n" \
  "v_smfmac_f32_16x16x64_bf16 v[176:179], v[116:119], a[8:15], v0\n" \
  "s_barrier\n" \
  "ds_read_b128 v[128:131], v5 offset:2048\n" \
  "v_smfmac_f32_16x16x64_bf16 v[180:183], v[116:119], a[168:175], v0\n" \
  "s_waitcnt lgkmcnt(2)\n" \
  "ds_read_b128 v[132:135], v5 offset:2560\n" \
  "v_smfmac_f32_16x16x64_bf16 v[176:179], v[120:123], a[16:23], v0\n" \
  "ds_read_b128 v[136:139], v5 offset:3072\n" \
  "v_smfmac_f32_16x16x64_bf16 v[180:183], v[120:123], a[176:183], v0\n" \
  "s_waitcnt lgkmcnt(3)\n" \
  "ds_read_b128 v[140:143], v5 offset:3584\n" \
  "v_smfmac_f32_16x16x64_bf16 v[176:179], v[124:127], a[24:31], v0\n" \
  "s_barrier\n" \
  "ds_read_b128 v[144:147], v5 offset:4096\n" \
  "v_smfmac_f32_16x16x64_bf16 v[180:183], v[124:127], a[184:191], v0\n" \
  "s_waitcnt lgkmcnt(4)\n" \
  "ds_read_b128 v[148:151], v5 offset:4608\n" \
  "v_smfmac_f32_16x16x64_bf16 v[176:179], v[128:131], a[32:39], v0\n" \
  "ds_read_b128 v[152:155], v5 offset:5120\n" \
  "v_smfmac_f32_16x16x64_bf16 v[180:183], v[128:131], a[192:199], v0\n" \
  "s_waitcnt lgkmcnt(5)\n" \
  "ds_read_b128 v[156:159], v5 offset:5632\n" \
  "v_smfmac_f32_16x16x64_bf16 v[176:179], v[132:135], a[40:47], v0\n" \
  "s_barrier\n" \
  "ds_read_b128 v[160:163], v5 offset:6144\n" \
  "v_smfmac_f32_16x16x64_bf16 v[180:183], v[132:135], a[200:207], v0\n" \
  "s_waitcnt lgkmcnt(6)\n" \
  "ds_read_b128 v[164:167], v5 offset:6656\n" \
  "v_smfmac_f32_16x16x64_bf16 v[176:179], v[136:139], a[48:55], v0\n" \
  "ds_read_b128 v[168:171], v5 offset:7168\n" \
  "v_smfmac_f32_16x16x64_bf16 v[180:183], v[136:139], a[208:215], v0\n" \
  "s_waitcnt lgkmcnt(7)\n" \
  "ds_read_b128 v[172:175], v5 offset:7680\n" \
  "v_smfmac_f32_16x16x64_bf16 v[176:179], v[140:143], a[56:63], v0\n" \
  "s_barrier\n" \
  "ds_read2_b32 v[112:113], v6 offset0:0 offset1:32\n" \
  "v_smfmac_f32_16x16x64_bf16 v[180:183], v[140:143], a[216:223], v0\n" \
  "s_waitcnt lgkmcnt(8)\n" \
  "ds_read2_b32 v[114:115], v6 offset0:64 offset1:96\n" \
  "v_smfmac_f32_16x16x64_bf16 v[176:179], v[144:147], a[64:71], v0\n" \
  "s_waitcnt lgkmcnt(8)\n" \
  "ds_read2_b32 v[116:117], v6 offset0:128 offset1:160\n" \
  "v_smfmac_f32_16x16x64_bf16 v[176:179], v[148:151], a[72:79], v0\n" \
  "s_waitcnt lgkmcnt(8)\n" \
  "ds_read2_b32 v[118:119], v6 offset0:192 offset1:224\n" \
  "v_smfmac_f32_16x16x64_bf16 v[176:179], v[152:155], a[80:87], v0\n" \
  "s_waitcnt lgkmcnt(8)\n" \
  "ds_read2_b32 v[120:121], v7 offset0:0 offset1:32\n" \
  "v_smfmac_f32_16x16x64_bf16 v[176:179], v[156:159], a[88:95], v0\n" \
  "s_waitcnt lgkmcnt(8)\n" \
  "ds_read2_b32 v[122:123], v7 offset0:64 offset1:96\n" \
  "v_smfmac_f32_16x16x64_bf16 v[176:179], v[160:163], a[96:103], v0\n" \
  "s_waitcnt lgkmcnt(8)\n" \
  "ds_read2_b32 v[124:125], v7 offset0:128 offset1:160\n" \
  "v_smfmac_f32_16x16x64_bf16 v[176:179], v[164:167], a[104:111], v0\n" \
  "s_waitcnt lgkmcnt(8)\n" \
  "ds_read2_b32 v[126:127], v7 offset0:192 offset1:224\n" \
  "v_smfmac_f32_16x16x64_bf16 v[176:179], v[168:171], a[112:119], v0\n" \
  "s_waitcnt lgkmcnt(8)\n" \
  "v_smfmac_f32_16x16x64_bf16 v[176:179], v[172:175], a[120:127], v0\n" \
  "s_waitcnt lgkmcnt(6)\n" \
  "v_smfmac_f32_16x16x64_bf16 v[176:179], v[112:115], a[128:135], v0\n" \
  "s_waitcnt lgkmcnt(4)\n" \
  "v_smfmac_f32_16x16x64_bf16 v[176:179], v[116:119], a[136:143], v0\n" \
  "s_waitcnt lgkmcnt(2)\n" \
  "v_smfmac_f32_16x16x64_bf16 v[176:179], v[120:123], a[144:151], v0\n" \
  "s_waitcnt lgkmcnt(0)\n" \
  "v_smfmac_f32_16x16x64_bf16 v[176:179], v[124:127], a[152:159], v0\n" \
  "v_smfmac_f32_16x16x64_bf16 v[180:183], v[144:147], a[224:231], v0\n" \
  "v_smfmac_f32_16x16x64_bf16 v[180:183], v[148:151], a[232:239], v0\n" \
  "v_cvt_pk_bf16_f32 v184, v176, v177\n" \
  "v_smfmac_f32_16x16x64_bf16 v[180:183], v[152:155], a[240:247], v0\n" \
  "v_cvt_pk_bf16_f32 v185, v178, v179\n" \
  "v_smfmac_f32_16x16x64_bf16 v[180:183], v[156:159], a[248:255], v0\n" \
  "ds_write_b64 v8, v[184:185]\n" \
  "v_add_f32 v186, v176, v177\n" \
  "v_smfmac_f32_16x16x64_bf16 v[180:183], v[160:163], v[192:199], v0\n" \
  "v_add_f32 v187, v178, v179\n" \
  "v_smfmac_f32_16x16x64_bf16 v[180:183], v[164:167], v[200:207], v0\n" \
  "v_cmp_u_f32 vcc, v186, v187\n" \
  "v_smfmac_f32_16x16x64_bf16 v[180:183], v[168:171], v[208:215], v0\n" \
  "v_smfmac_f32_16x16x64_bf16 v[180:183], v[172:175], v[216:223], v0\n" \
  "s_cbranch_vccnz LMF13_%=\n" \
  "LMOK13_%=:\n" \
  "v_smfmac_f32_16x16x64_bf16 v[180:183], v[112:115], v[224:231], v0\n" \
  "s_waitcnt lgkmcnt(0)\n" \
  "s_barrier\n" \
  "v_smfmac_f32_16x16x64_bf16 v[180:183], v[116:119], v[232:239], v0\n" \
  "v_smfmac_f32_16x16x64_bf16 v[180:183], v[120:123], v[240:247], v0\n" \
  "v_smfmac_f32_16x16x64_bf16 v[180:183], v[124:127], v[248:255], v0\n" \
  "s_nop 3\n" \
  "v_mov_b32 v31, v31\n" \
  "s_nop 2\n" \
  "v_cvt_pk_bf16_f32 v186, v180, v181\n" \
  "v_cvt_pk_bf16_f32 v187, v182, v183\n" \
  "ds_write_b64 v9, v[186:187]\n" \
  "s_waitcnt lgkmcnt(0)\n" \
  "s_barrier\n" \
  "s_nop 15\n" \
  "s_nop 0\n" \
  "ds_read_b64_tr_b16 v[88:89], v12\n" \
  "ds_read_b64_tr_b16 v[90:91], v12 offset:32\n" \
  "s_mov_b32 s67, s66\n" \
  "s_cmp_eq_u32 s64, 64\n" \
  "s_cbranch_scc1 LKE13_%=\n" \
  "LB13_%=:\n" \
  "s_barrier\n" \
  "buffer_load_dwordx4 v[116:119], v15, s[36:39], s65 offen offset:128 sc1\n" \
  "s_barrier\n" \
  "buffer_load_dwordx4 v[112:115], v15, s[36:39], s65 offen sc1\n" \
  "s_barrier\n" \
  "buffer_load_dwordx4 v[120:123], v16, s[36:39], s65 offen sc1\n" \
  "s_barrier\n" \
  "buffer_load_dwordx4 v[124:127], v16, s[36:39], s65 offen offset:512 sc1\n" \
  "s_barrier\n" \
  "buffer_load_dwordx4 v[128:131], v16, s[36:39], s65 offen offset:1024 sc1\n" \
  "s_waitcnt vmcnt(5)\n" \
  "s_barrier\n" \
  "buffer_load_dwordx4 v[132:135], v16, s[36:39], s65 offen offset:1536 sc1\n" \
  "ds_read_b128 v[40:43], v19\n" \
  "ds_read_b128 v[44:47], v19 offset:512\n" \
  "ds_read_b128 v[48:51], v19 offset:1024\n" \
  "ds_read_b128 v[52:55], v19 offset:1536\n" \
  "s_mov_b64 exec, s[46:47]\n" \
  "ds_read_b128 v[56:59], v20\n" \
  "ds_read_b128 v[60:63], v20 offset:128\n" \
  "ds_read_b128 v[64:67], v20 offset:256\n" \
  "ds_read_b128 v[68:71], v20 offset:384\n" \
  "s_mov_b64 exec, -1\n" \
  "s_nop 15\n" \
  "s_nop 1\n" \
  "buffer_load_dwordx4 v[136:139], v17, s[36:39], s65 offen sc1\n" \
  "buffer_load_dwordx4 v[140:143], v18, s[36:39], s65 offen sc1\n" \
  "s_barrier\n" \
  "s_waitcnt lgkmcnt(0)\n" \
  "buffer_store_dwordx4 v[88:91], v14, s[36:39], s69 offen sc1\n" \
  "s_waitcnt lgkmcnt(0)\n" \
  "s_getreg_b32 s71, hwreg(HW_REG_IB_STS, 0, 4)\n" \
  "s_cmp_le_u32 s71, 7\n" \
  "v_mfma_f32_16x16x32_bf16 v[144:147], v[72:75], v[72:75], 0\n" \
  "s_cbranch_scc1 LHXS13_%=\n" \
  "s_getreg_b32 s71, hwreg(HW_REG_IB_STS, 0, 4)\n" \
  "s_cmp_le_u32 s71, 7\n" \
  "v_mfma_f32_16x16x32_bf16 v[144:147], v[72:75], v[72:75], 0\n" \
  "s_cbranch_scc1 LHXS13_%=\n" \
  "s_getreg_b32 s71, hwreg(HW_REG_IB_STS, 0, 4)\n" \
  "s_cmp_le_u32 s71, 7\n" \
  "v_mfma_f32_16x16x32_bf16 v[144:147], v[72:75], v[72:75], 0\n" \
  "s_cbranch_scc1 LHXS13_%=\n" \
  "s_getreg_b32 s71, hwreg(HW_REG_IB_STS, 0, 4)\n" \
  "s_cmp_le_u32 s71, 7\n" \
  "v_mfma_f32_16x16x32_bf16 v[144:147], v[72:75], v[72:75], 0\n" \
  "s_cbranch_scc1 LHXS13_%=\n" \
  "s_getreg_b32 s71, hwreg(HW_REG_IB_STS, 0, 4)\n" \
  "s_cmp_le_u32 s71, 7\n" \
  "v_mfma_f32_16x16x32_bf16 v[144:147], v[72:75], v[72:75], 0\n" \
  "s_cbranch_scc1 LHXS13_%=\n" \
  "s_getreg_b32 s71, hwreg(HW_REG_IB_STS, 0, 4)\n" \
  "s_cmp_le_u32 s71, 7\n" \
  "v_mfma_f32_16x16x32_bf16 v[144:147], v[72:75], v[72:75], 0\n" \
  "s_cbranch_scc1 LHXS13_%=\n" \
  "s_getreg_b32 s71, hwreg(HW_REG_IB_STS, 0, 4)\n" \
  "s_cmp_le_u32 s71, 7\n" \
  "v_mfma_f32_16x16x32_bf16 v[144:147], v[72:75], v[72:75], 0\n" \
  "s_cbranch_scc1 LHXS13_%=\n" \
  "s_getreg_b32 s71, hwreg(HW_REG_IB_STS, 0, 4)\n" \
  "s_cmp_le_u32 s71, 7\n" \
  "v_mfma_f32_16x16x32_bf16 v[144:147], v[72:75], v[72:75], 0\n" \
  "s_cbranch_scc1 LHXS13_%=\n" \
  "s_getreg_b32 s71, hwreg(HW_REG_IB_STS, 0, 4)\n" \
  "s_cmp_le_u32 s71, 7\n" \
  "v_mfma_f32_16x16x32_bf16 v[144:147], v[72:75], v[72:75], 0\n" \
  "s_cbranch_scc1 LHXS13_%=\n" \
  "s_getreg_b32 s71, hwreg(HW_REG_IB_STS, 0, 4)\n" \
  "s_cmp_le_u32 s71, 7\n" \
  "v_mfma_f32_16x16x32_bf16 v[144:147], v[72:75], v[72:75], 0\n" \
  "s_cbranch_scc1 LHXS13_%=\n" \
  "s_getreg_b32 s71, hwreg(HW_REG_IB_STS, 0, 4)\n" \
  "s_cmp_le_u32 s71, 7\n" \
  "v_mfma_f32_16x16x32_bf16 v[144:147], v[72:75], v[72:75], 0\n" \
  "s_cbranch_scc1 LHXS13_%=\n" \
  "s_getreg_b32 s71, hwreg(HW_REG_IB_STS, 0, 4)\n" \
  "s_cmp_le_u32 s71, 7\n" \
  "v_mfma_f32_16x16x32_bf16 v[144:147], v[72:75], v[72:75], 0\n" \
  "s_cbranch_scc1 LHXS13_%=\n" \
  "s_getreg_b32 s71, hwreg(HW_REG_IB_STS, 0, 4)\n" \
  "s_cmp_le_u32 s71, 7\n" \
  "v_mfma_f32_16x16x32_bf16 v[144:147], v[72:75], v[72:75], 0\n" \
  "s_cbranch_scc1 LHXS13_%=\n" \
  "s_getreg_b32 s71, hwreg(HW_REG_IB_STS, 0, 4)\n" \
  "s_cmp_le_u32 s71, 7\n" \
  "v_mfma_f32_16x16x32_bf16 v[144:147], v[72:75], v[72:75], 0\n" \
  "s_cbranch_scc1 LHXS13_%=\n" \
  "s_getreg_b32 s71, hwreg(HW_REG_IB_STS, 0, 4)\n" \
  "s_cmp_le_u32 s71, 7\n" \
  "v_mfma_f32_16x16x32_bf16 v[144:147], v[72:75], v[72:75], 0\n" \
  "s_cbranch_scc1 LHXS13_%=\n" \
  "s_getreg_b32 s71, hwreg(HW_REG_IB_STS, 0, 4)\n" \
  "s_cmp_le_u32 s71, 7\n" \
  "v_mfma_f32_16x16x32_bf16 v[144:147], v[72:75], v[72:75], 0\n" \
  "s_cbranch_scc1 LHXS13_%=\n" \
  "s_getreg_b32 s71, hwreg(HW_REG_IB_STS, 0, 4)\n" \
  "s_cmp_le_u32 s71, 7\n" \
  "v_mfma_f32_16x16x32_bf16 v[144:147], v[72:75], v[72:75], 0\n" \
  "s_cbranch_scc1 LHXS13_%=\n" \
  "s_getreg_b32 s71, hwreg(HW_REG_IB_STS, 0, 4)\n" \
  "s_cmp_le_u32 s71, 7\n" \
  "v_mfma_f32_16x16x32_bf16 v[144:147], v[72:75], v[72:75], 0\n" \
  "s_cbranch_scc1 LHXS13_%=\n" \
  "s_getreg_b32 s71, hwreg(HW_REG_IB_STS, 0, 4)\n" \
  "s_cmp_le_u32 s71, 7\n" \
  "v_mfma_f32_16x16x32_bf16 v[144:147], v[72:75], v[72:75], 0\n" \
  "s_cbranch_scc1 LHXS13_%=\n" \
  "s_getreg_b32 s71, hwreg(HW_REG_IB_STS, 0, 4)\n" \
  "s_cmp_le_u32 s71, 7\n" \
  "v_mfma_f32_16x16x32_bf16 v[144:147], v[72:75], v[72:75], 0\n" \
  "s_cbranch_scc1 LHXS13_%=\n" \
  "LHXS13_%=:\n" \
  "s_waitcnt vmcnt(7)\n" \
  "LS213_%=:\n" \
  "v_dot2_f32_bf16 v176, v112, s22, 0\n" \
  "v_dot2_f32_bf16 v177, v112, s23, 0\n" \
  "v_dot2_f32_bf16 v178, v113, s22, 0\n" \
  "v_dot2_f32_bf16 v179, v113, s23, 0\n" \
  "v_dot2_f32_bf16 v180, v114, s22, 0\n" \
  "v_dot2_f32_bf16 v181, v114, s23, 0\n" \
  "v_dot2_f32_bf16 v182, v115, s22, 0\n" \
  "v_dot2_f32_bf16 v183, v115, s23, 0\n" \
  "v_dot2_f32_bf16 v176, v116, s22, v176\n" \
  "v_dot2_f32_bf16 v177, v116, s23, v177\n" \
  "v_dot2_f32_bf16 v178, v117, s22, v178\n" \
  "v_dot2_f32_bf16 v179, v117, s23, v179\n" \
  "v_dot2_f32_bf16 v180, v118, s22, v180\n" \
  "v_dot2_f32_bf16 v181, v118, s23, v181\n" \
  "v_dot2_f32_bf16 v182, v119, s22, v182\n" \
  "v_dot2_f32_bf16 v183, v119, s23, v183\n" \
  "v_exp_f32 v176, v176\n" \
  "v_exp_f32 v177, v177\n" \
  "v_exp_f32 v178, v178\n" \
  "v_exp_f32 v179, v179\n" \
  "s_waitcnt vmcnt(6)\n" \
  "v_mfma_f32_16x16x32_bf16 v[160:163], v[56:59], v[120:123], 0\n" \
  "v_exp_f32 v180, v180\n" \
  "v_mfma_f32_16x16x32_bf16 v[144:147], v[40:43], v[120:123], 0\n" \
  "v_exp_f32 v181, v181\n" \
  "v_exp_f32 v182, v182\n" \
  "v_exp_f32 v183, v183\n" \
  "v_cvt_pk_bf16_f32 v184, v176, v177\n" \
  "v_cvt_pk_bf16_f32 v185, v178, v179\n" \
  "v_cvt_pk_bf16_f32 v186, v180, v181\n" \
  "v_cvt_pk_bf16_f32 v187, v182, v183\n" \
  "s_nop 1\n" \
  "v_mfma_f32_16x16x32_bf16 v[160:163], v[72:75], v[184:187], v[160:163]\n" \
  "v_mfma_f32_16x16x32_bf16 v[164:167], v[76:79], v[184:187], 0\n" \
  "s_waitcnt vmcnt(5)\n" \
  "v_mfma_f32_16x16x32_bf16 v[164:167], v[60:63], v[124:127], v[164:167]\n" \
  "v_rcp_f32 v188, v162\n" \
  "v_mfma_f32_16x16x32_bf16 v[148:151], v[44:47], v[124:127], 0\n" \
  "v_mul_f32 v144, v188, v144\n" \
  "v_mul_f32 v145, v188, v145\n" \
  "v_mfma_f32_16x16x32_bf16 v[168:171], v[80:83], v[184:187], 0\n" \
  "s_waitcnt vmcnt(4)\n" \
  "v_rcp_f32 v189, v166\n" \
  "v_mfma_f32_16x16x32_bf16 v[168:171], v[64:67], v[128:131], v[168:171]\n" \
  "v_mul_f32 v146, v188, v146\n" \
  "v_mul_f32 v147, v188, v147\n" \
  "v_mfma_f32_16x16x32_bf16 v[152:155], v[48:51], v[128:131], 0\n" \
  "v_mul_f32 v160, v188, v160\n" \
  "v_mul_f32 v161, v188, v161\n" \
  "v_mfma_f32_16x16x32_bf16 v[172:175], v[84:87], v[184:187], 0\n" \
  "s_waitcnt vmcnt(3)\n" \
  "v_rcp_f32 v190, v170\n" \
  "v_mfma_f32_16x16x32_bf16 v[172:175], v[68:71], v[132:135], v[172:175]\n" \
  "v_cndmask_b32 v167, v188, v189, s[28:29]\n" \
  "v_cndmask_b32 v167, v167, v190, s[26:27]\n" \
  "v_mfma_f32_16x16x32_bf16 v[156:159], v[52:55], v[132:135], 0\n" \
  "v_fmac_f32 v144, v189, v148\n" \
  "v_fmac_f32 v145, v189, v149\n" \
  "v_fmac_f32 v146, v189, v150\n" \
  "v_fmac_f32 v147, v189, v151\n" \
  "v_rcp_f32 v191, v174\n" \
  "v_fmac_f32 v160, v189, v164\n" \
  "v_cndmask_b32 v167, v167, v191, s[24:25]\n" \
  "v_pk_mul_f32 v[176:177], v[176:177], v[166:167] op_sel:[0,1] op_sel_hi:[1,1]\n" \
  "v_pk_mul_f32 v[178:179], v[178:179], v[166:167] op_sel:[0,1] op_sel_hi:[1,1]\n" \
  "v_pk_mul_f32 v[180:181], v[180:181], v[166:167] op_sel:[0,1] op_sel_hi:[1,1]\n" \
  "v_pk_mul_f32 v[182:183], v[182:183], v[166:167] op_sel:[0,1] op_sel_hi:[1,1]\n" \
  "v_cmp_nlt_f32 vcc, 0x0d800000, v167\n" \
  "v_cvt_pk_bf16_f32 v184, v176, v177\n" \
  "v_cvt_pk_bf16_f32 v185, v178, v179\n" \
  "v_cvt_pk_bf16_f32 v186, v180, v181\n" \
  "v_cvt_pk_bf16_f32 v187, v182, v183\n" \
  "v_fmac_f32 v161, v189, v165\n" \
  "v_pk_fma_f32 v[160:161], v[168:169], v[190:191], v[160:161] op_sel:[0,0,0] op_sel_hi:[1,0,1]\n" \
  "v_pk_fma_f32 v[160:161], v[172:173], v[190:191], v[160:161] op_sel:[0,1,0] op_sel_hi:[1,1,1]\n" \
  "v_pk_fma_f32 v[144:145], v[152:153], v[190:191], v[144:145] op_sel:[0,0,0] op_sel_hi:[1,0,1]\n" \
  "v_pk_fma_f32 v[146:147], v[154:155], v[190:191], v[146:147] op_sel:[0,0,0] op_sel_hi:[1,0,1]\n" \
  "v_pk_fma_f32 v[144:145], v[156:157], v[190:191], v[144:145] op_sel:[0,1,0] op_sel_hi:[1,1,1]\n" \
  "v_pk_fma_f32 v[146:147], v[158:159], v[190:191], v[146:147] op_sel:[0,1,0] op_sel_hi:[1,1,1]\n" \
  "s_waitcnt vmcnt(1)\n" \
  "v_mfma_f32_16x16x32_bf16 v[160:163], v[140:143], v[184:187], v[160:163]\n" \
  "s_mov_b64 s[80:81], vcc\n" \
  "v_mfma_f32_16x16x32_bf16 v[144:147], v[136:139], v[184:187], v[144:147]\n" \
  "s_mov_b64 exec, s[30:31]\n" \
  "s_nop 2\n" \
  "ds_write_b64 v22, v[160:161]\n" \
  "v_cmp_u_f32 s[78:79], v160, v161\n" \
  "s_nop 1\n" \
  "ds_write2_b64 v21, v[144:145], v[146:147] offset1:16\n" \
  "v_add_f32 v188, v144, v145\n" \
  "v_add_f32 v189, v146, v147\n" \
  "v_cmp_u_f32 vcc, v188, v189\n" \
  "s_or_b64 s[80:81], s[80:81], s[78:79]\n" \
  "s_or_b64 vcc, vcc, s[80:81]\n" \
  "s_cbranch_vccnz LSF13_%=\n" \
  "LSOK13_%=:\n" \
  "s_mov_b64 exec, -1\n" \
  "s_waitcnt lgkmcnt(0)\n" \
  "s_barrier\n" \
  "ds_read_b32 v188, v33\n" \
  "ds_read_b128 v[120:123], v23\n" \
  "ds_read_b128 v[124:127], v23 offset:768\n" \
  "ds_read_b128 v[128:131], v23 offset:1536\n" \
  "ds_read_b128 v[132:135], v23 offset:2304\n" \
  "s_waitcnt lgkmcnt(4)\n" \
  "v_cmp_ne_u32 vcc, 0, v188\n" \
  "s_waitcnt lgkmcnt(2)\n" \
  "v_pk_add_f32 v[120:121], v[120:121], v[122:123]\n" \
  "v_pk_add_f32 v[124:125], v[124:125], v[126:127]\n" \
  "v_pk_add_f32 v[120:121], v[120:121], v[124:125]\n" \
  "s_waitcnt lgkmcnt(1)\n" \
  "v_pk_add_f32 v[128:129], v[128:129], v[130:131]\n" \
  "v_pk_add_f32 v[120:121], v[120:121], v[128:129]\n" \
  "s_waitcnt lgkmcnt(0)\n" \
  "v_pk_add_f32 v[132:133], v[132:133], v[134:135]\n" \
  "v_pk_add_f32 v[120:121], v[120:121], v[132:133]\n" \
  "v_cvt_pk_bf16_f32 v120, v120, v121\n" \
  "s_cbranch_vccnz LSR13_%=\n" \
  "buffer_store_dword v120, v24, s[36:39], s66 offen sc1\n" \
  "buffer_store_dword v120, v24, s[36:39], s93 offen sc1\n" \
  "buffer_store_dword v120, v24, s[36:39], s94 offen sc1\n" \
  "LNL13_%=:\n" \
  "s_waitcnt lgkmcnt(0)\n" \
  "s_cmp_eq_u32 s73, 1\n" \
  "s_cbranch_scc1 LOOP13_%=\n" \
  "s_branch LDONE_%=\n" \
  "LMF13_%=:\n" \
  "v_mbcnt_lo_u32_b32 v130, -1, 0\n" \
  "v_mbcnt_hi_u32_b32 v130, -1, v130\n" \
  "v_lshlrev_b32 v130, 4, v130\n" \
  "v_mov_b32 v128, 0\n" \
  "ds_read_b128 v[132:135], v130 offset:0\n" \
  "ds_read_b128 v[136:139], v130 offset:1024\n" \
  "ds_read_b128 v[140:143], v130 offset:2048\n" \
  "ds_read_b128 v[144:147], v130 offset:3072\n" \
  "ds_read_b128 v[148:151], v130 offset:4096\n" \
  "s_waitcnt lgkmcnt(0)\n" \
  "v_max3_u32 v128, v128, v132, v133\n" \
  "v_max3_u32 v128, v128, v134, v135\n" \
  "v_max3_u32 v128, v128, v136, v137\n" \
  "v_max3_u32 v128, v128, v138, v139\n" \
  "v_max3_u32 v128, v128, v140, v141\n" \
  "v_max3_u32 v128, v128, v142, v143\n" \
  "v_max3_u32 v128, v128, v144, v145\n" \
  "v_max3_u32 v128, v128, v146, v147\n" \
  "v_max3_u32 v128, v128, v148, v149\n" \
  "v_max3_u32 v128, v128, v150, v151\n" \
  "ds_read_b128 v[132:135], v130 offset:5120\n" \
  "ds_read_b128 v[136:139], v130 offset:6144\n" \
  "ds_read_b128 v[140:143], v130 offset:7168\n" \
  "ds_read_b128 v[144:147], v130 offset:8192\n" \
  "ds_read_b128 v[148:151], v130 offset:9216\n" \
  "s_waitcnt lgkmcnt(0)\n" \
  "v_max3_u32 v128, v128, v132, v133\n" \
  "v_max3_u32 v128, v128, v134, v135\n" \
  "v_max3_u32 v128, v128, v136, v137\n" \
  "v_max3_u32 v128, v128, v138, v139\n" \
  "v_max3_u32 v128, v128, v140, v141\n" \
  "v_max3_u32 v128, v128, v142, v143\n" \
  "v_max3_u32 v128, v128, v144, v145\n" \
  "v_max3_u32 v128, v128, v146, v147\n" \
  "v_max3_u32 v128, v128, v148, v149\n" \
  "v_max3_u32 v128, v128, v150, v151\n" \
  "v_cmp_eq_u32 vcc, -1, v128\n" \
  "s_cbranch_vccz LMOK13_%=\n" \
  "s_cmp_gt_u32 s58, 4000\n" \
  "s_cbranch_scc1 LMOK13_%=\n" \
  "s_add_u32 s58, s58, 1\n" \
  "s_sub_u32 s64, s64, 1\n" \
  "s_mov_b32 s66, s65\n" \
  "s_waitcnt vmcnt(0) lgkmcnt(0)\n" \
  "s_barrier\n" \
  "s_branch LOOP13_%=\n" \
  "LKE13_%=:\n" \
  "s_waitcnt lgkmcnt(0)\n" \
  "buffer_store_dwordx4 v[88:91], v14, s[36:39], s69 offen sc1\n" \
  "s_branch LDONE_%=\n" \
  "LSF13_%=:\n" \
  "s_getpc_b64 s[78:79]\n" \
  "s_branch LSF_%=\n" \
  "s_branch LSOK13_%=\n" \
  "LSR13_%=:\n" \
  "v_mov_b32 v188, 0\n" \
  "s_add_u32 s59, s59, 1\n" \
  "s_barrier\n" \
  "ds_write_b32 v33, v188\n" \
  "s_waitcnt lgkmcnt(0)\n" \
  "s_branch LB13_%=\n" \
  "LSF_%=:\n" \
  "s_mov_b64 exec, -1\n" \
  "v_mov_b32 v188, 0\n" \
  "v_max3_u32 v188, v188, v112, v113\n" \
  "v_max3_u32 v188, v188, v114, v115\n" \
  "v_max3_u32 v188, v188, v116, v117\n" \
  "v_max3_u32 v188, v188, v118, v119\n" \
  "v_max3_u32 v188, v188, v120, v121\n" \
  "v_max3_u32 v188, v188, v122, v123\n" \
  "v_max3_u32 v188, v188, v124, v125\n" \
  "v_max3_u32 v188, v188, v126, v127\n" \
  "v_max3_u32 v188, v188, v128, v129\n" \
  "v_max3_u32 v188, v188, v130, v131\n" \
  "v_max3_u32 v188, v188, v132, v133\n" \
  "v_max3_u32 v188, v188, v134, v135\n" \
  "v_max3_u32 v188, v188, v136, v137\n" \
  "v_max3_u32 v188, v188, v138, v139\n" \
  "v_max3_u32 v188, v188, v140, v141\n" \
  "v_max3_u32 v188, v188, v142, v143\n" \
  "v_cmp_eq_u32 vcc, -1, v188\n" \
  "s_cbranch_vccz LSG_%=\n" \
  "s_cmp_gt_u32 s59, 4000\n" \
  "s_cbranch_scc1 LSG_%=\n" \
  "ds_write_b32 v33, v32\n" \
  "s_add_u32 s78, s78, 4\n" \
  "s_addc_u32 s79, s79, 0\n" \
  "s_setpc_b64 s[78:79]\n" \
  "LSG_%=:\n" \
  "v_dot2_f32_bf16 v176, v112, s22, 0\n" \
  "v_dot2_f32_bf16 v176, v116, s22, v176\n" \
  "v_dot2_f32_bf16 v177, v112, s23, 0\n" \
  "v_dot2_f32_bf16 v177, v116, s23, v177\n" \
  "v_dot2_f32_bf16 v178, v113, s22, 0\n" \
  "v_dot2_f32_bf16 v178, v117, s22, v178\n" \
  "v_dot2_f32_bf16 v179, v113, s23, 0\n" \
  "v_dot2_f32_bf16 v179, v117, s23, v179\n" \
  "v_dot2_f32_bf16 v180, v114, s22, 0\n" \
  "v_dot2_f32_bf16 v180, v118, s22, v180\n" \
  "v_dot2_f32_bf16 v181, v114, s23, 0\n" \
  "v_dot2_f32_bf16 v181, v118, s23, v181\n" \
  "v_dot2_f32_bf16 v182, v115, s22, 0\n" \
  "v_dot2_f32_bf16 v182, v119, s22, v182\n" \
  "v_dot2_f32_bf16 v183, v115, s23, 0\n" \
  "v_dot2_f32_bf16 v183, v119, s23, v183\n" \
  "s_nop 2\n" \
  "v_max3_f32 v158, v176, v177, v178\n" \
  "v_max3_f32 v158, v158, v179, v180\n" \
  "v_max3_f32 v158, v158, v181, v182\n" \
  "v_max_f32 v158, v158, v183\n" \
  "s_lshr_b32 s72, s49, 5\n" \
  "s_sub_u32 s72, 3, s72\n" \
  "s_mul_i32 s72, s72, 81920\n" \
  "s_add_u32 s72, s65, s72\n" \
  "v_mov_b32 v157, 0x7fd57fd5\n" \
  "v_cmp_eq_u32 s[80:81], v120, v157\n" \
  "LSP0_%=:\n" \
  "buffer_load_dwordx4 v[152:155], v16, s[36:39], s72 offen sc1\n" \
  "s_waitcnt vmcnt(0)\n" \
  "v_max3_u32 v156, v152, v153, v154\n" \
  "v_max_u32 v156, v156, v155\n" \
  "v_cmp_eq_u32 vcc, -1, v156\n" \
  "s_and_b64 vcc, vcc, s[80:81]\n" \
  "s_cbranch_vccz LSQ0_%=\n" \
  "s_add_u32 s60, s60, 1\n" \
  "s_cmp_gt_u32 s60, 20000\n" \
  "s_cbranch_scc1 LSQ0_%=\n" \
  "s_sleep 8\n" \
  "s_branch LSP0_%=\n" \
  "LSQ0_%=:\n" \
  "v_lshlrev_b32 v144, 16, v120\n" \
  "v_log_f32 v144, v144\n" \
  "v_lshlrev_b32 v157, 16, v152\n" \
  "v_cndmask_b32 v144, v144, v157, s[80:81]\n" \
  "v_and_b32 v145, 0xffff0000, v120\n" \
  "v_log_f32 v145, v145\n" \
  "v_and_b32 v157, 0xffff0000, v152\n" \
  "v_cndmask_b32 v145, v145, v157, s[80:81]\n" \
  "v_lshlrev_b32 v146, 16, v121\n" \
  "v_log_f32 v146, v146\n" \
  "v_lshlrev_b32 v157, 16, v153\n" \
  "v_cndmask_b32 v146, v146, v157, s[80:81]\n" \
  "v_and_b32 v147, 0xffff0000, v121\n" \
  "v_log_f32 v147, v147\n" \
  "v_and_b32 v157, 0xffff0000, v153\n" \
  "v_cndmask_b32 v147, v147, v157, s[80:81]\n" \
  "v_lshlrev_b32 v148, 16, v122\n" \
  "v_log_f32 v148, v148\n" \
  "v_lshlrev_b32 v157, 16, v154\n" \
  "v_cndmask_b32 v148, v148, v157, s[80:81]\n" \
  "v_and_b32 v149, 0xffff0000, v122\n" \
  "v_log_f32 v149, v149\n" \
  "v_and_b32 v157, 0xffff0000, v154\n" \
  "v_cndmask_b32 v149, v149, v157, s[80:81]\n" \
  "v_lshlrev_b32 v150, 16, v123\n" \
  "v_log_f32 v150, v150\n" \
  "v_lshlrev_b32 v157, 16, v155\n" \
  "v_cndmask_b32 v150, v150, v157, s[80:81]\n" \
  "v_and_b32 v151, 0xffff0000, v123\n" \
  "v_log_f32 v151, v151\n" \
  "v_and_b32 v157, 0xffff0000, v155\n" \
  "v_cndmask_b32 v151, v151, v157, s[80:81]\n" \
  "v_max3_f32 v156, v144, v145, v146\n" \
  "v_max3_f32 v156, v156, v147, v148\n" \
  "v_max3_f32 v156, v156, v149, v150\n" \
  "v_max_f32 v156, v156, v151\n" \
  "s_mov_b64 exec, 0xffff\n" \
  "v_max_f32 v156, v156, v158\n" \
  "s_mov_b64 exec, -1\n" \
  "v_mov_b32 v157, v156\n" \
  "s_nop 1\n" \
  "v_permlane16_swap_b32 v156, v157\n" \
  "v_max_f32 v156, v156, v157\n" \
  "v_mov_b32 v157, v156\n" \
  "s_nop 1\n" \
  "v_permlane32_swap_b32 v156, v157\n" \
  "v_max_f32 v156, v156, v157\n" \
  "s_mov_b64 exec, 0xffff\n" \
  "v_mov_b32 v188, v156\n" \
  "s_mov_b64 exec, -1\n" \
  "v_sub_f32 v144, v144, v156\n" \
  "v_exp_f32 v144, v144\n" \
  "v_sub_f32 v145, v145, v156\n" \
  "v_exp_f32 v145, v145\n" \
  "v_sub_f32 v146, v146, v156\n" \
  "v_exp_f32 v146, v146\n" \
  "v_sub_f32 v147, v147, v156\n" \
  "v_exp_f32 v147, v147\n" \
  "v_sub_f32 v148, v148, v156\n" \
  "v_exp_f32 v148, v148\n" \
  "v_sub_f32 v149, v149, v156\n" \
  "v_exp_f32 v149, v149\n" \
  "v_sub_f32 v150, v150, v156\n" \
  "v_exp_f32 v150, v150\n" \
  "v_sub_f32 v151, v151, v156\n" \
  "v_exp_f32 v151, v151\n" \
  "v_cvt_pk_bf16_f32 v120, v144, v145\n" \
  "v_cvt_pk_bf16_f32 v121, v146, v147\n" \
  "v_cvt_pk_bf16_f32 v122, v148, v149\n" \
  "v_cvt_pk_bf16_f32 v123, v150, v151\n" \
  "v_mov_b32 v157, 0x7fd57fd5\n" \
  "v_cmp_eq_u32 s[80:81], v124, v157\n" \
  "LSP1_%=:\n" \
  "buffer_load_dwordx4 v[152:155], v16, s[36:39], s72 offen offset:512 sc1\n" \
  "s_waitcnt vmcnt(0)\n" \
  "v_max3_u32 v156, v152, v153, v154\n" \
  "v_max_u32 v156, v156, v155\n" \
  "v_cmp_eq_u32 vcc, -1, v156\n" \
  "s_and_b64 vcc, vcc, s[80:81]\n" \
  "s_cbranch_vccz LSQ1_%=\n" \
  "s_add_u32 s60, s60, 1\n" \
  "s_cmp_gt_u32 s60, 20000\n" \
  "s_cbranch_scc1 LSQ1_%=\n" \
  "s_sleep 8\n" \
  "s_branch LSP1_%=\n" \
  "LSQ1_%=:\n" \
  "v_lshlrev_b32 v144, 16, v124\n" \
  "v_log_f32 v144, v144\n" \
  "v_lshlrev_b32 v157, 16, v152\n" \
  "v_cndmask_b32 v144, v144, v157, s[80:81]\n" \
  "v_and_b32 v145, 0xffff0000, v124\n" \
  "v_log_f32 v145, v145\n" \
  "v_and_b32 v157, 0xffff0000, v152\n" \
  "v_cndmask_b32 v145, v145, v157, s[80:81]\n" \
  "v_lshlrev_b32 v146, 16, v125\n" \
  "v_log_f32 v146, v146\n" \
  "v_lshlrev_b32 v157, 16, v153\n" \
  "v_cndmask_b32 v146, v146, v157, s[80:81]\n" \
  "v_and_b32 v147, 0xffff0000, v125\n" \
  "v_log_f32 v147, v147\n" \
  "v_and_b32 v157, 0xffff0000, v153\n" \
  "v_cndmask_b32 v147, v147, v157, s[80:81]\n" \
  "v_lshlrev_b32 v148, 16, v126\n" \
  "v_log_f32 v148, v148\n" \
  "v_lshlrev_b32 v157, 16, v154\n" \
  "v_cndmask_b32 v148, v148, v157, s[80:81]\n" \
  "v_and_b32 v149, 0xffff0000, v126\n" \
  "v_log_f32 v149, v149\n" \
  "v_and_b32 v157, 0xffff0000, v154\n" \
  "v_cndmask_b32 v149, v149, v157, s[80:81]\n" \
  "v_lshlrev_b32 v150, 16, v127\n" \
  "v_log_f32 v150, v150\n" \
  "v_lshlrev_b32 v157, 16, v155\n" \
  "v_cndmask_b32 v150, v150, v157, s[80:81]\n" \
  "v_and_b32 v151, 0xffff0000, v127\n" \
  "v_log_f32 v151, v151\n" \
  "v_and_b32 v157, 0xffff0000, v155\n" \
  "v_cndmask_b32 v151, v151, v157, s[80:81]\n" \
  "v_max3_f32 v156, v144, v145, v146\n" \
  "v_max3_f32 v156, v156, v147, v148\n" \
  "v_max3_f32 v156, v156, v149, v150\n" \
  "v_max_f32 v156, v156, v151\n" \
  "s_mov_b64 exec, s[28:29]\n" \
  "v_max_f32 v156, v156, v158\n" \
  "s_mov_b64 exec, -1\n" \
  "v_mov_b32 v157, v156\n" \
  "s_nop 1\n" \
  "v_permlane16_swap_b32 v156, v157\n" \
  "v_max_f32 v156, v156, v157\n" \
  "v_mov_b32 v157, v156\n" \
  "s_nop 1\n" \
  "v_permlane32_swap_b32 v156, v157\n" \
  "v_max_f32 v156, v156, v157\n" \
  "s_mov_b64 exec, s[28:29]\n" \
  "v_mov_b32 v188, v156\n" \
  "s_mov_b64 exec, -1\n" \
  "v_sub_f32 v144, v144, v156\n" \
  "v_exp_f32 v144, v144\n" \
  "v_sub_f32 v145, v145, v156\n" \
  "v_exp_f32 v145, v145\n" \
  "v_sub_f32 v146, v146, v156\n" \
  "v_exp_f32 v146, v146\n" \
  "v_sub_f32 v147, v147, v156\n" \
  "v_exp_f32 v147, v147\n" \
  "v_sub_f32 v148, v148, v156\n" \
  "v_exp_f32 v148, v148\n" \
  "v_sub_f32 v149, v149, v156\n" \
  "v_exp_f32 v149, v149\n" \
  "v_sub_f32 v150, v150, v156\n" \
  "v_exp_f32 v150, v150\n" \
  "v_sub_f32 v151, v151, v156\n" \
  "v_exp_f32 v151, v151\n" \
  "v_cvt_pk_bf16_f32 v124, v144, v145\n" \
  "v_cvt_pk_bf16_f32 v125, v146, v147\n" \
  "v_cvt_pk_bf16_f32 v126, v148, v149\n" \
  "v_cvt_pk_bf16_f32 v127, v150, v151\n" \
  "v_mov_b32 v157, 0x7fd57fd5\n" \
  "v_cmp_eq_u32 s[80:81], v128, v157\n" \
  "LSP2_%=:\n" \
  "buffer_load_dwordx4 v[152:155], v16, s[36:39], s72 offen offset:1024 sc1\n" \
  "s_waitcnt vmcnt(0)\n" \
  "v_max3_u32 v156, v152, v153, v154\n" \
  "v_max_u32 v156, v156, v155\n" \
  "v_cmp_eq_u32 vcc, -1, v156\n" \
  "s_and_b64 vcc, vcc, s[80:81]\n" \
  "s_cbranch_vccz LSQ2_%=\n" \
  "s_add_u32 s60, s60, 1\n" \
  "s_cmp_gt_u32 s60, 20000\n" \
  "s_cbranch_scc1 LSQ2_%=\n" \
  "s_sleep 8\n" \
  "s_branch LSP2_%=\n" \
  "LSQ2_%=:\n" \
  "v_lshlrev_b32 v144, 16, v128\n" \
  "v_log_f32 v144, v144\n" \
  "v_lshlrev_b32 v157, 16, v152\n" \
  "v_cndmask_b32 v144, v144, v157, s[80:81]\n" \
  "v_and_b32 v145, 0xffff0000, v128\n" \
  "v_log_f32 v145, v145\n" \
  "v_and_b32 v157, 0xffff0000, v152\n" \
  "v_cndmask_b32 v145, v145, v157, s[80:81]\n" \
  "v_lshlrev_b32 v146, 16, v129\n" \
  "v_log_f32 v146, v146\n" \
  "v_lshlrev_b32 v157, 16, v153\n" \
  "v_cndmask_b32 v146, v146, v157, s[80:81]\n" \
  "v_and_b32 v147, 0xffff0000, v129\n" \
  "v_log_f32 v147, v147\n" \
  "v_and_b32 v157, 0xffff0000, v153\n" \
  "v_cndmask_b32 v147, v147, v157, s[80:81]\n" \
  "v_lshlrev_b32 v148, 16, v130\n" \
  "v_log_f32 v148, v148\n" \
  "v_lshlrev_b32 v157, 16, v154\n" \
  "v_cndmask_b32 v148, v148, v157, s[80:81]\n" \
  "v_and_b32 v149, 0xffff0000, v130\n" \
  "v_log_f32 v149, v149\n" \
  "v_and_b32 v157, 0xffff0000, v154\n" \
  "v_cndmask_b32 v149, v149, v157, s[80:81]\n" \
  "v_lshlrev_b32 v150, 16, v131\n" \
  "v_log_f32 v150, v150\n" \
  "v_lshlrev_b32 v157, 16, v155\n" \
  "v_cndmask_b32 v150, v150, v157, s[80:81]\n" \
  "v_and_b32 v151, 0xffff0000, v131\n" \
  "v_log_f32 v151, v151\n" \
  "v_and_b32 v157, 0xffff0000, v155\n" \
  "v_cndmask_b32 v151, v151, v157, s[80:81]\n" \
  "v_max3_f32 v156, v144, v145, v146\n" \
  "v_max3_f32 v156, v156, v147, v148\n" \
  "v_max3_f32 v156, v156, v149, v150\n" \
  "v_max_f32 v156, v156, v151\n" \
  "s_mov_b64 exec, s[26:27]\n" \
  "v_max_f32 v156, v156, v158\n" \
  "s_mov_b64 exec, -1\n" \
  "v_mov_b32 v157, v156\n" \
  "s_nop 1\n" \
  "v_permlane16_swap_b32 v156, v157\n" \
  "v_max_f32 v156, v156, v157\n" \
  "v_mov_b32 v157, v156\n" \
  "s_nop 1\n" \
  "v_permlane32_swap_b32 v156, v157\n" \
  "v_max_f32 v156, v156, v157\n" \
  "s_mov_b64 exec, s[26:27]\n" \
  "v_mov_b32 v188, v156\n" \
  "s_mov_b64 exec, -1\n" \
  "v_sub_f32 v144, v144, v156\n" \
  "v_exp_f32 v144, v144\n" \
  "v_sub_f32 v145, v145, v156\n" \
  "v_exp_f32 v145, v145\n" \
  "v_sub_f32 v146, v146, v156\n" \
  "v_exp_f32 v146, v146\n" \
  "v_sub_f32 v147, v147, v156\n" \
  "v_exp_f32 v147, v147\n" \
  "v_sub_f32 v148, v148, v156\n" \
  "v_exp_f32 v148, v148\n" \
  "v_sub_f32 v149, v149, v156\n" \
  "v_exp_f32 v149, v149\n" \
  "v_sub_f32 v150, v150, v156\n" \
  "v_exp_f32 v150, v150\n" \
  "v_sub_f32 v151, v151, v156\n" \
  "v_exp_f32 v151, v151\n" \
  "v_cvt_pk_bf16_f32 v128, v144, v145\n" \
  "v_cvt_pk_bf16_f32 v129, v146, v147\n" \
  "v_cvt_pk_bf16_f32 v130, v148, v149\n" \
  "v_cvt_pk_bf16_f32 v131, v150, v151\n" \
  "v_mov_b32 v157, 0x7fd57fd5\n" \
  "v_cmp_eq_u32 s[80:81], v132, v157\n" \
  "LSP3_%=:\n" \
  "buffer_load_dwordx4 v[152:155], v16, s[36:39], s72 offen offset:1536 sc1\n" \
  "s_waitcnt vmcnt(0)\n" \
  "v_max3_u32 v156, v152, v153, v154\n" \
  "v_max_u32 v156, v156, v155\n" \
  "v_cmp_eq_u32 vcc, -1, v156\n" \
  "s_and_b64 vcc, vcc, s[80:81]\n" \
  "s_cbranch_vccz LSQ3_%=\n" \
  "s_add_u32 s60, s60, 1\n" \
  "s_cmp_gt_u32 s60, 20000\n" \
  "s_cbranch_scc1 LSQ3_%=\n" \
  "s_sleep 8\n" \
  "s_branch LSP3_%=\n" \
  "LSQ3_%=:\n" \
  "v_lshlrev_b32 v144, 16, v132\n" \
  "v_log_f32 v144, v144\n" \
  "v_lshlrev_b32 v157, 16, v152\n" \
  "v_cndmask_b32 v144, v144, v157, s[80:81]\n" \
  "v_and_b32 v145, 0xffff0000, v132\n" \
  "v_log_f32 v145, v145\n" \
  "v_and_b32 v157, 0xffff0000, v152\n" \
  "v_cndmask_b32 v145, v145, v157, s[80:81]\n" \
  "v_lshlrev_b32 v146, 16, v133\n" \
  "v_log_f32 v146, v146\n" \
  "v_lshlrev_b32 v157, 16, v153\n" \
  "v_cndmask_b32 v146, v146, v157, s[80:81]\n" \
  "v_and_b32 v147, 0xffff0000, v133\n" \
  "v_log_f32 v147, v147\n" \
  "v_and_b32 v157, 0xffff0000, v153\n" \
  "v_cndmask_b32 v147, v147, v157, s[80:81]\n" \
  "v_lshlrev_b32 v148, 16, v134\n" \
  "v_log_f32 v148, v148\n" \
  "v_lshlrev_b32 v157, 16, v154\n" \
  "v_cndmask_b32 v148, v148, v157, s[80:81]\n" \
  "v_and_b32 v149, 0xffff0000, v134\n" \
  "v_log_f32 v149, v149\n" \
  "v_and_b32 v157, 0xffff0000, v154\n" \
  "v_cndmask_b32 v149, v149, v157, s[80:81]\n" \
  "v_lshlrev_b32 v150, 16, v135\n" \
  "v_log_f32 v150, v150\n" \
  "v_lshlrev_b32 v157, 16, v155\n" \
  "v_cndmask_b32 v150, v150, v157, s[80:81]\n" \
  "v_and_b32 v151, 0xffff0000, v135\n" \
  "v_log_f32 v151, v151\n" \
  "v_and_b32 v157, 0xffff0000, v155\n" \
  "v_cndmask_b32 v151, v151, v157, s[80:81]\n" \
  "v_max3_f32 v156, v144, v145, v146\n" \
  "v_max3_f32 v156, v156, v147, v148\n" \
  "v_max3_f32 v156, v156, v149, v150\n" \
  "v_max_f32 v156, v156, v151\n" \
  "s_mov_b64 exec, s[24:25]\n" \
  "v_max_f32 v156, v156, v158\n" \
  "s_mov_b64 exec, -1\n" \
  "v_mov_b32 v157, v156\n" \
  "s_nop 1\n" \
  "v_permlane16_swap_b32 v156, v157\n" \
  "v_max_f32 v156, v156, v157\n" \
  "v_mov_b32 v157, v156\n" \
  "s_nop 1\n" \
  "v_permlane32_swap_b32 v156, v157\n" \
  "v_max_f32 v156, v156, v157\n" \
  "s_mov_b64 exec, s[24:25]\n" \
  "v_mov_b32 v188, v156\n" \
  "s_mov_b64 exec, -1\n" \
  "v_sub_f32 v144, v144, v156\n" \
  "v_exp_f32 v144, v144\n" \
  "v_sub_f32 v145, v145, v156\n" \
  "v_exp_f32 v145, v145\n" \
  "v_sub_f32 v146, v146, v156\n" \
  "v_exp_f32 v146, v146\n" \
  "v_sub_f32 v147, v147, v156\n" \
  "v_exp_f32 v147, v147\n" \
  "v_sub_f32 v148, v148, v156\n" \
  "v_exp_f32 v148, v148\n" \
  "v_sub_f32 v149, v149, v156\n" \
  "v_exp_f32 v149, v149\n" \
  "v_sub_f32 v150, v150, v156\n" \
  "v_exp_f32 v150, v150\n" \
  "v_sub_f32 v151, v151, v156\n" \
  "v_exp_f32 v151, v151\n" \
  "v_cvt_pk_bf16_f32 v132, v144, v145\n" \
  "v_cvt_pk_bf16_f32 v133, v146, v147\n" \
  "v_cvt_pk_bf16_f32 v134, v148, v149\n" \
  "v_cvt_pk_bf16_f32 v135, v150, v151\n" \
  "v_sub_f32 v176, v176, v188\n" \
  "v_exp_f32 v176, v176\n" \
  "v_sub_f32 v177, v177, v188\n" \
  "v_exp_f32 v177, v177\n" \
  "v_sub_f32 v178, v178, v188\n" \
  "v_exp_f32 v178, v178\n" \
  "v_sub_f32 v179, v179, v188\n" \
  "v_exp_f32 v179, v179\n" \
  "v_sub_f32 v180, v180, v188\n" \
  "v_exp_f32 v180, v180\n" \
  "v_sub_f32 v181, v181, v188\n" \
  "v_exp_f32 v181, v181\n" \
  "v_sub_f32 v182, v182, v188\n" \
  "v_exp_f32 v182, v182\n" \
  "v_sub_f32 v183, v183, v188\n" \
  "v_exp_f32 v183, v183\n" \
  "v_cvt_pk_bf16_f32 v184, v176, v177\n" \
  "v_cvt_pk_bf16_f32 v185, v178, v179\n" \
  "v_cvt_pk_bf16_f32 v186, v180, v181\n" \
  "v_cvt_pk_bf16_f32 v187, v182, v183\n" \
  "v_mfma_f32_16x16x32_bf16 v[144:147], v[40:43], v[120:123], 0\n" \
  "v_mfma_f32_16x16x32_bf16 v[160:163], v[56:59], v[120:123], 0\n" \
  "v_mfma_f32_16x16x32_bf16 v[160:163], v[72:75], v[184:187], v[160:163]\n" \
  "v_mfma_f32_16x16x32_bf16 v[148:151], v[44:47], v[124:127], 0\n" \
  "v_mfma_f32_16x16x32_bf16 v[164:167], v[60:63], v[124:127], 0\n" \
  "v_mfma_f32_16x16x32_bf16 v[164:167], v[76:79], v[184:187], v[164:167]\n" \
  "v_mfma_f32_16x16x32_bf16 v[152:155], v[48:51], v[128:131], 0\n" \
  "v_mfma_f32_16x16x32_bf16 v[168:171], v[64:67], v[128:131], 0\n" \
  "v_mfma_f32_16x16x32_bf16 v[168:171], v[80:83], v[184:187], v[168:171]\n" \
  "v_mfma_f32_16x16x32_bf16 v[156:159], v[52:55], v[132:135], 0\n" \
  "v_mfma_f32_16x16x32_bf16 v[172:175], v[68:71], v[132:135], 0\n" \
  "v_mfma_f32_16x16x32_bf16 v[172:175], v[84:87], v[184:187], v[172:175]\n" \
  "v_rcp_f32 v188, v162\n" \
  "s_nop 0\n" \
  "v_pk_mul_f32 v[144:145], v[144:145], v[188:189] op_sel_hi:[1,0]\n" \
  "v_pk_mul_f32 v[146:147], v[146:147], v[188:189] op_sel_hi:[1,0]\n" \
  "v_pk_mul_f32 v[160:161], v[160:161], v[188:189] op_sel_hi:[1,0]\n" \
  "v_rcp_f32 v189, v166\n" \
  "s_nop 0\n" \
  "v_cndmask_b32 v167, v188, v189, s[28:29]\n" \
  "v_pk_fma_f32 v[144:145], v[148:149], v[188:189], v[144:145] op_sel:[0,1,0] op_sel_hi:[1,1,1]\n" \
  "v_pk_fma_f32 v[146:147], v[150:151], v[188:189], v[146:147] op_sel:[0,1,0] op_sel_hi:[1,1,1]\n" \
  "v_pk_fma_f32 v[160:161], v[164:165], v[188:189], v[160:161] op_sel:[0,1,0] op_sel_hi:[1,1,1]\n" \
  "v_rcp_f32 v190, v170\n" \
  "s_nop 0\n" \
  "v_cndmask_b32 v167, v167, v190, s[26:27]\n" \
  "v_pk_fma_f32 v[144:145], v[152:153], v[190:191], v[144:145] op_sel:[0,0,0] op_sel_hi:[1,0,1]\n" \
  "v_pk_fma_f32 v[146:147], v[154:155], v[190:191], v[146:147] op_sel:[0,0,0] op_sel_hi:[1,0,1]\n" \
  "v_pk_fma_f32 v[160:161], v[168:169], v[190:191], v[160:161] op_sel:[0,0,0] op_sel_hi:[1,0,1]\n" \
  "v_rcp_f32 v191, v174\n" \
  "s_nop 0\n" \
  "v_cndmask_b32 v167, v167, v191, s[24:25]\n" \
  "v_pk_fma_f32 v[144:145], v[156:157], v[190:191], v[144:145] op_sel:[0,1,0] op_sel_hi:[1,1,1]\n" \
  "v_pk_fma_f32 v[146:147], v[158:159], v[190:191], v[146:147] op_sel:[0,1,0] op_sel_hi:[1,1,1]\n" \
  "v_pk_fma_f32 v[160:161], v[172:173], v[190:191], v[160:161] op_sel:[0,1,0] op_sel_hi:[1,1,1]\n" \
  "v_pk_mul_f32 v[176:177], v[176:177], v[166:167] op_sel:[0,1] op_sel_hi:[1,1]\n" \
  "v_pk_mul_f32 v[178:179], v[178:179], v[166:167] op_sel:[0,1] op_sel_hi:[1,1]\n" \
  "v_pk_mul_f32 v[180:181], v[180:181], v[166:167] op_sel:[0,1] op_sel_hi:[1,1]\n" \
  "v_pk_mul_f32 v[182:183], v[182:183], v[166:167] op_sel:[0,1] op_sel_hi:[1,1]\n" \
  "v_cvt_pk_bf16_f32 v184, v176, v177\n" \
  "v_cvt_pk_bf16_f32 v185, v178, v179\n" \
  "v_cvt_pk_bf16_f32 v186, v180, v181\n" \
  "v_cvt_pk_bf16_f32 v187, v182, v183\n" \
  "s_nop 1\n" \
  "v_mfma_f32_16x16x32_bf16 v[144:147], v[136:139], v[184:187], v[144:147]\n" \
  "v_mfma_f32_16x16x32_bf16 v[160:163], v[140:143], v[184:187], v[160:163]\n" \
  "s_mov_b64 exec, s[30:31]\n" \
  "s_nop 6\n" \
  "ds_write_b64 v22, v[160:161]\n" \
  "ds_write2_b64 v21, v[144:145], v[146:147] offset1:16\n" \
  "s_add_u32 s78, s78, 4\n" \
  "s_addc_u32 s79, s79, 0\n" \
  "s_setpc_b64 s[78:79]\n" \
  "LSTART20_%=:\n" \
  "LOOP20_%=:\n" \
  "v_mov_b32 v80, 0\n" \
  "v_mov_b32 v81, 0\n" \
  "v_mov_b32 v82, 0\n" \
  "v_mov_b32 v83, 0\n" \
  "v_mov_b32 v84, 0\n" \
  "v_mov_b32 v85, 0\n" \
  "v_mov_b32 v86, 0\n" \
  "v_mov_b32 v87, 0\n" \
  "v_mov_b32 v88, 0\n" \
  "v_mov_b32 v89, 0\n" \
  "v_mov_b32 v90, 0\n" \
  "v_mov_b32 v91, 0\n" \
  "v_mov_b32 v16, 0\n" \
  "v_mov_b32 v17, 0\n" \
  "v_mov_b32 v18, 0\n" \
  "v_mov_b32 v19, 0\n" \
  "v_mov_b32 v20, 0\n" \
  "v_mov_b32 v21, 0\n" \
  "v_mov_b32 v22, 0\n" \
  "v_mov_b32 v23, 0\n" \
  "v_mov_b32 v24, 0\n" \
  "v_mov_b32 v25, 0\n" \
  "v_mov_b32 v26, 0\n" \
  "v_mov_b32 v27, 0\n" \
  "v_mov_b32 v104, 0\n" \
  "v_mov_b32 v105, 0\n" \
  "v_mov_b32 v106, 0\n" \
  "v_mov_b32 v107, 0\n" \
  "s_mov_b32 s65, s66\n" \
  "s_xor_b32 s69, s66, 0x2000\n" \
  "s_add_u32 s64, s64, 1\n" \
  "v_readlane_b32 s66, v28, s64\n" \
  "s_add_u32 s40, s65, 131072\n" \
  "s_add_u32 s41, s65, 262144\n" \
  "s_add_u32 s42, s65, 393216\n" \
  "s_barrier\n" \
  "LM20_%=:\n" \
  "s_barrier\n" \
  "ds_read_b128 v[40:43], v5\n" \
  "s_barrier\n" \
  "ds_read_b128 v[44:47], v5 offset:1024\n" \
  "s_waitcnt lgkmcnt(1)\n" \
  "v_smfmac_f32_16x16x64_bf16 v[80:83], v[40:43], a[0:7], v0\n" \
  "v_smfmac_f32_16x16x64_bf16 v[84:87], v[40:43], a[80:87], v0\n" \
  "s_barrier\n" \
  "ds_read_b128 v[48:51], v5 offset:2048\n" \
  "v_smfmac_f32_16x16x64_bf16 v[88:91], v[40:43], a[160:167], v0\n" \
  "s_waitcnt lgkmcnt(1)\n" \
  "ds_read_b128 v[52:55], v5 offset:3072\n" \
  "v_smfmac_f32_16x16x64_bf16 v[80:83], v[44:47], a[8:15], v0\n" \
  "v_smfmac_f32_16x16x64_bf16 v[84:87], v[44:47], a[88:95], v0\n" \
  "v_smfmac_f32_16x16x64_bf16 v[88:91], v[44:47], a[168:175], v0\n" \
  "s_barrier\n" \
  "s_waitcnt lgkmcnt(1)\n" \
  "ds_read_b128 v[56:59], v5 offset:4096\n" \
  "v_smfmac_f32_16x16x64_bf16 v[80:83], v[48:51], a[16:23], v0\n" \
  "ds_read_b128 v[60:63], v5 offset:5120\n" \
  "v_smfmac_f32_16x16x64_bf16 v[84:87], v[48:51], a[96:103], v0\n" \
  "v_smfmac_f32_16x16x64_bf16 v[88:91], v[48:51], a[176:183], v0\n" \
  "s_waitcnt lgkmcnt(2)\n" \
  "v_smfmac_f32_16x16x64_bf16 v[80:83], v[52:55], a[24:31], v0\n" \
  "s_barrier\n" \
  "ds_read_b128 v[64:67], v5 offset:6144\n" \
  "v_smfmac_f32_16x16x64_bf16 v[84:87], v[52:55], a[104:111], v0\n" \
  "s_waitcnt lgkmcnt(2)\n" \
  "ds_read_b128 v[68:71], v5 offset:7168\n" \
  "v_smfmac_f32_16x16x64_bf16 v[80:83], v[56:59], a[32:39], v0\n" \
  "v_smfmac_f32_16x16x64_bf16 v[84:87], v[56:59], a[112:119], v0\n" \
  "s_waitcnt lgkmcnt(2)\n" \
  "v_smfmac_f32_16x16x64_bf16 v[80:83], v[60:63], a[40:47], v0\n" \
  "s_barrier\n" \
  "ds_read2_b32 v[72:73], v6 offset0:0 offset1:32\n" \
  "ds_read2_b32 v[74:75], v6 offset0:64 offset1:96\n" \
  "v_smfmac_f32_16x16x64_bf16 v[84:87], v[60:63], a[120:127], v0\n" \
  "s_waitcnt lgkmcnt(3)\n" \
  "ds_read2_b32 v[76:77], v7 offset0:0 offset1:32\n" \
  "ds_read2_b32 v[78:79], v7 offset0:64 offset1:96\n" \
  "v_smfmac_f32_16x16x64_bf16 v[80:83], v[64:67], a[48:55], v0\n" \
  "v_smfmac_f32_16x16x64_bf16 v[84:87], v[64:67], a[128:135], v0\n" \
  "s_waitcnt lgkmcnt(4)\n" \
  "v_smfmac_f32_16x16x64_bf16 v[80:83], v[68:71], a[56:63], v0\n" \
  "v_smfmac_f32_16x16x64_bf16 v[84:87], v[68:71], a[136:143], v0\n" \
  "s_waitcnt lgkmcnt(2)\n" \
  "v_smfmac_f32_16x16x64_bf16 v[80:83], v[72:75], a[64:71], v0\n" \
  "v_smfmac_f32_16x16x64_bf16 v[84:87], v[72:75], a[144:151], v0\n" \
  "s_waitcnt lgkmcnt(0)\n" \
  "ds_read_b128 v[40:43], v11 offset:8192\n" \
  "v_smfmac_f32_16x16x64_bf16 v[80:83], v[76:79], a[72:79], v0\n" \
  "ds_read_b128 v[44:47], v11 offset:9216\n" \
  "v_smfmac_f32_16x16x64_bf16 v[84:87], v[76:79], a[152:159], v0\n" \
  "v_smfmac_f32_16x16x64_bf16 v[88:91], v[52:55], a[184:191], v0\n" \
  "ds_read_b128 v[48:51], v11 offset:49152\n" \
  "v_smfmac_f32_16x16x64_bf16 v[88:91], v[56:59], a[192:199], v0\n" \
  "ds_read_b128 v[52:55], v11 offset:50176\n" \
  "v_permlane32_swap_b32 v80, v84\n" \
  "v_smfmac_f32_16x16x64_bf16 v[88:91], v[60:63], a[200:207], v0\n" \
  "ds_read_b128 v[56:59], v11 offset:0\n" \
  "v_permlane32_swap_b32 v81, v85\n" \
  "v_smfmac_f32_16x16x64_bf16 v[88:91], v[64:67], a[208:215], v0\n" \
  "ds_read_b128 v[60:63], v11 offset:1024\n" \
  "v_permlane32_swap_b32 v82, v86\n" \
  "v_smfmac_f32_16x16x64_bf16 v[88:91], v[68:71], a[216:223], v0\n" \
  "ds_read_b128 v[64:67], v11 offset:40960\n" \
  "v_permlane32_swap_b32 v83, v87\n" \
  "v_smfmac_f32_16x16x64_bf16 v[88:91], v[72:75], a[224:231], v0\n" \
  "ds_read_b128 v[68:71], v11 offset:41984\n" \
  "v_add_f32 v108, v80, v84\n" \
  "v_smfmac_f32_16x16x64_bf16 v[88:91], v[76:79], a[232:239], v0\n" \
  "s_nop 1\n" \
  "v_add_f32 v109, v81, v85\n" \
  "v_add_f32 v110, v82, v86\n" \
  "v_add_f32 v111, v83, v87\n" \
  "v_cvt_pk_bf16_f32 v32, v108, v109\n" \
  "v_cvt_pk_bf16_f32 v33, v110, v111\n" \
  "s_nop 0\n" \
  "v_permlane32_swap_b32 v88, v90\n" \
  "v_permlane32_swap_b32 v89, v91\n" \
  "v_pk_add_f32 v[80:81], v[88:89], v[90:91]\n" \
  "v_cvt_pk_bf16_f32 v12, v80, v81\n" \
  "v_cmp_u_f32 vcc, v80, v81\n" \
  "ds_write_b64 v8, v[32:33]\n" \
  "ds_write_b32 v9, v12\n" \
  "s_cbranch_vccnz LMF20_%=\n" \
  "LMOK20_%=:\n" \
  "s_waitcnt lgkmcnt(0)\n" \
  "s_barrier\n" \
  "ds_read_b64_tr_b16 v[80:81], v10\n" \
  "ds_read_b64_tr_b16 v[82:83], v10 offset:32\n" \
  "ds_read_b64_tr_b16 v[84:85], v10 offset:512\n" \
  "ds_read_b64_tr_b16 v[86:87], v10 offset:544\n" \
  "ds_read_b64_tr_b16 v[88:89], v10 offset:1024\n" \
  "ds_read_b64_tr_b16 v[90:91], v10 offset:1056\n" \
  "ds_read_b64_tr_b16 v[92:93], v10 offset:1536\n" \
  "ds_read_b64_tr_b16 v[94:95], v10 offset:1568\n" \
  "ds_read_b64_tr_b16 v[36:37], v10 offset:2048\n" \
  "ds_read_b64_tr_b16 v[38:39], v10 offset:2080\n" \
  "ds_read_b64_tr_b16 v[100:101], v10 offset:2560\n" \
  "ds_read_b64_tr_b16 v[102:103], v10 offset:2592\n" \
  "s_waitcnt lgkmcnt(10)\n" \
  "v_smfmac_f32_16x16x64_bf16 v[16:19], v[80:83], v[240:247], v0\n" \
  "s_waitcnt lgkmcnt(8)\n" \
  "v_smfmac_f32_16x16x64_bf16 v[16:19], v[84:87], v[248:255], v0\n" \
  "s_waitcnt lgkmcnt(6)\n" \
  "v_smfmac_f32_16x16x64_bf16 v[16:19], v[88:91], a[240:247], v0\n" \
  "s_waitcnt lgkmcnt(4)\n" \
  "ds_read_b128 v[72:75], v11 offset:2048\n" \
  "v_smfmac_f32_16x16x64_bf16 v[16:19], v[92:95], a[248:255], v0\n" \
  "s_waitcnt lgkmcnt(3)\n" \
  "ds_read_b128 v[76:79], v11 offset:3072\n" \
  "v_smfmac_f32_16x16x64_bf16 v[16:19], v[36:39], v[40:47], v0\n" \
  "s_waitcnt lgkmcnt(2)\n" \
  "ds_read_b128 v[40:43], v11 offset:43008\n" \
  "v_smfmac_f32_16x16x64_bf16 v[16:19], v[100:103], v[48:55], v0\n" \
  "ds_read_b128 v[44:47], v11 offset:44032\n" \
  "v_smfmac_f32_16x16x64_bf16 v[20:23], v[80:83], v[112:119], v0\n" \
  "ds_read_b128 v[48:51], v11 offset:4096\n" \
  "v_smfmac_f32_16x16x64_bf16 v[20:23], v[84:87], v[120:127], v0\n" \
  "ds_read_b128 v[52:55], v11 offset:5120\n" \
  "v_cvt_pk_bf16_f32 v108, v16, v17\n" \
  "v_smfmac_f32_16x16x64_bf16 v[20:23], v[88:91], v[128:135], v0\n" \
  "v_cvt_pk_bf16_f32 v109, v18, v19\n" \
  "v_smfmac_f32_16x16x64_bf16 v[20:23], v[92:95], v[136:143], v0\n" \
  "buffer_store_dwordx2 v[108:109], v4, s[36:39], s65 offen sc1\n" \
  "v_smfmac_f32_16x16x64_bf16 v[20:23], v[36:39], v[56:63], v0\n" \
  "ds_read_b128 v[56:59], v11 offset:45056\n" \
  "v_smfmac_f32_16x16x64_bf16 v[20:23], v[100:103], v[64:71], v0\n" \
  "ds_read_b128 v[60:63], v11 offset:46080\n" \
  "v_smfmac_f32_16x16x64_bf16 v[104:107], v[80:83], v[144:151], v0\n" \
  "ds_read_b128 v[64:67], v11 offset:6144\n" \
  "v_smfmac_f32_16x16x64_bf16 v[104:107], v[84:87], v[152:159], v0\n" \
  "ds_read_b128 v[68:71], v11 offset:7168\n" \
  "v_cvt_pk_bf16_f32 v110, v20, v21\n" \
  "v_smfmac_f32_16x16x64_bf16 v[104:107], v[88:91], v[160:167], v0\n" \
  "v_cvt_pk_bf16_f32 v111, v22, v23\n" \
  "v_smfmac_f32_16x16x64_bf16 v[104:107], v[92:95], v[168:175], v0\n" \
  "buffer_store_dwordx2 v[110:111], v13, s[36:39], s65 offen sc1\n" \
  "v_mov_b32 v16, 0\n" \
  "s_waitcnt lgkmcnt(8)\n" \
  "v_smfmac_f32_16x16x64_bf16 v[104:107], v[36:39], v[72:79], v0\n" \
  "s_waitcnt lgkmcnt(6)\n" \
  "ds_read_b128 v[72:75], v11 offset:47104\n" \
  "v_mov_b32 v17, 0\n" \
  "v_smfmac_f32_16x16x64_bf16 v[104:107], v[100:103], v[40:47], v0\n" \
  "ds_read_b128 v[76:79], v11 offset:48128\n" \
  "v_mov_b32 v18, 0\n" \
  "v_smfmac_f32_16x16x64_bf16 v[24:27], v[80:83], v[176:183], v0\n" \
  "v_mov_b32 v19, 0\n" \
  "v_smfmac_f32_16x16x64_bf16 v[24:27], v[84:87], v[184:191], v0\n" \
  "v_cvt_pk_bf16_f32 v108, v104, v105\n" \
  "v_smfmac_f32_16x16x64_bf16 v[24:27], v[88:91], v[192:199], v0\n" \
  "v_cvt_pk_bf16_f32 v109, v106, v107\n" \
  "v_smfmac_f32_16x16x64_bf16 v[24:27], v[92:95], v[200:207], v0\n" \
  "buffer_store_dwordx2 v[108:109], v13, s[36:39], s40 offen sc1\n" \
  "s_waitcnt lgkmcnt(6)\n" \
  "v_smfmac_f32_16x16x64_bf16 v[24:27], v[36:39], v[48:55], v0\n" \
  "s_waitcnt lgkmcnt(4)\n" \
  "v_smfmac_f32_16x16x64_bf16 v[24:27], v[100:103], v[56:63], v0\n" \
  "v_smfmac_f32_16x16x64_bf16 v[16:19], v[80:83], v[208:215], v0\n" \
  "v_smfmac_f32_16x16x64_bf16 v[16:19], v[84:87], v[216:223], v0\n" \
  "v_cvt_pk_bf16_f32 v110, v24, v25\n" \
  "v_smfmac_f32_16x16x64_bf16 v[16:19], v[88:91], v[224:231], v0\n" \
  "v_cvt_pk_bf16_f32 v111, v26, v27\n" \
  "v_smfmac_f32_16x16x64_bf16 v[16:19], v[92:95], v[232:239], v0\n" \
  "buffer_store_dwordx2 v[110:111], v13, s[36:39], s41 offen sc1\n" \
  "s_waitcnt lgkmcnt(2)\n" \
  "v_smfmac_f32_16x16x64_bf16 v[16:19], v[36:39], v[64:71], v0\n" \
  "s_waitcnt lgkmcnt(0)\n" \
  "v_smfmac_f32_16x16x64_bf16 v[16:19], v[100:103], v[72:79], v0\n" \
  "s_nop 3\n" \
  "v_mov_b32 v31, v31\n" \
  "s_nop 2\n" \
  "v_cvt_pk_bf16_f32 v108, v16, v17\n" \
  "v_cvt_pk_bf16_f32 v109, v18, v19\n" \
  "buffer_store_dwordx2 v[108:109], v13, s[36:39], s42 offen sc1\n" \
  "s_mov_b32 s67, s66\n" \
  "s_cmp_eq_u32 s64, 64\n" \
  "s_cbranch_scc1 LDONE_%=\n" \
  "s_waitcnt vmcnt(0)\n" \
  "s_branch LOOP20_%=\n" \
  "LMF20_%=:\n" \
  "v_mbcnt_lo_u32_b32 v94, -1, 0\n" \
  "v_mbcnt_hi_u32_b32 v94, -1, v94\n" \
  "v_lshlrev_b32 v94, 4, v94\n" \
  "v_mov_b32 v92, 0\n" \
  "ds_read_b128 v[72:75], v94 offset:0\n" \
  "ds_read_b128 v[76:79], v94 offset:1024\n" \
  "ds_read_b128 v[80:83], v94 offset:2048\n" \
  "ds_read_b128 v[84:87], v94 offset:3072\n" \
  "ds_read_b128 v[88:91], v94 offset:4096\n" \
  "s_waitcnt lgkmcnt(0)\n" \
  "v_max3_u32 v92, v92, v72, v73\n" \
  "v_max3_u32 v92, v92, v74, v75\n" \
  "v_max3_u32 v92, v92, v76, v77\n" \
  "v_max3_u32 v92, v92, v78, v79\n" \
  "v_max3_u32 v92, v92, v80, v81\n" \
  "v_max3_u32 v92, v92, v82, v83\n" \
  "v_max3_u32 v92, v92, v84, v85\n" \
  "v_max3_u32 v92, v92, v86, v87\n" \
  "v_max3_u32 v92, v92, v88, v89\n" \
  "v_max3_u32 v92, v92, v90, v91\n" \
  "ds_read_b128 v[72:75], v94 offset:5120\n" \
  "ds_read_b128 v[76:79], v94 offset:6144\n" \
  "ds_read_b128 v[80:83], v94 offset:7168\n" \
  "ds_read_b128 v[84:87], v94 offset:8192\n" \
  "ds_read_b128 v[88:91], v94 offset:9216\n" \
  "s_waitcnt lgkmcnt(0)\n" \
  "v_max3_u32 v92, v92, v72, v73\n" \
  "v_max3_u32 v92, v92, v74, v75\n" \
  "v_max3_u32 v92, v92, v76, v77\n" \
  "v_max3_u32 v92, v92, v78, v79\n" \
  "v_max3_u32 v92, v92, v80, v81\n" \
  "v_max3_u32 v92, v92, v82, v83\n" \
  "v_max3_u32 v92, v92, v84, v85\n" \
  "v_max3_u32 v92, v92, v86, v87\n" \
  "v_max3_u32 v92, v92, v88, v89\n" \
  "v_max3_u32 v92, v92, v90, v91\n" \
  "v_cmp_eq_u32 vcc, -1, v92\n" \
  "s_cbranch_vccz LMOK20_%=\n" \
  "s_cmp_gt_u32 s58, 4000\n" \
  "s_cbranch_scc1 LMOK20_%=\n" \
  "s_add_u32 s58, s58, 1\n" \
  "s_sub_u32 s64, s64, 1\n" \
  "s_mov_b32 s66, s65\n" \
  "s_waitcnt vmcnt(0) lgkmcnt(0)\n" \
  "s_barrier\n" \
  "s_branch LOOP20_%=\n" \
  "LSTART21_%=:\n" \
  "LOOP21_%=:\n" \
  "v_mov_b32 v80, 0\n" \
  "v_mov_b32 v81, 0\n" \
  "v_mov_b32 v82, 0\n" \
  "v_mov_b32 v83, 0\n" \
  "v_mov_b32 v84, 0\n" \
  "v_mov_b32 v85, 0\n" \
  "v_mov_b32 v86, 0\n" \
  "v_mov_b32 v87, 0\n" \
  "v_mov_b32 v88, 0\n" \
  "v_mov_b32 v89, 0\n" \
  "v_mov_b32 v90, 0\n" \
  "v_mov_b32 v91, 0\n" \
  "v_mov_b32 v16, 0\n" \
  "v_mov_b32 v17, 0\n" \
  "v_mov_b32 v18, 0\n" \
  "v_mov_b32 v19, 0\n" \
  "v_mov_b32 v20, 0\n" \
  "v_mov_b32 v21, 0\n" \
  "v_mov_b32 v22, 0\n" \
  "v_mov_b32 v23, 0\n" \
  "v_mov_b32 v24, 0\n" \
  "v_mov_b32 v25, 0\n" \
  "v_mov_b32 v26, 0\n" \
  "v_mov_b32 v27, 0\n" \
  "v_mov_b32 v104, 0\n" \
  "v_mov_b32 v105, 0\n" \
  "v_mov_b32 v106, 0\n" \
  "v_mov_b32 v107, 0\n" \
  "s_mov_b32 s65, s66\n" \
  "s_xor_b32 s69, s66, 0x2000\n" \
  "s_add_u32 s64, s64, 1\n" \
  "v_readlane_b32 s66, v28, s64\n" \
  "s_add_u32 s40, s65, 131072\n" \
  "s_add_u32 s41, s65, 262144\n" \
  "s_add_u32 s42, s65, 393216\n" \
  "s_barrier\n" \
  "LM21_%=:\n" \
  "s_barrier\n" \
  "ds_read_b128 v[40:43], v5\n" \
  "s_barrier\n" \
  "ds_read_b128 v[44:47], v5 offset:1024\n" \
  "s_waitcnt lgkmcnt(1)\n" \
  "v_smfmac_f32_16x16x64_bf16 v[80:83], v[40:43], a[0:7], v0\n" \
  "v_smfmac_f32_16x16x64_bf16 v[84:87], v[40:43], a[80:87], v0\n" \
  "s_barrier\n" \
  "ds_read_b128 v[48:51], v5 offset:2048\n" \
  "v_smfmac_f32_16x16x64_bf16 v[88:91], v[40:43], a[160:167], v0\n" \
  "s_waitcnt lgkmcnt(1)\n" \
  "ds_read_b128 v[52:55], v5 offset:3072\n" \
  "v_smfmac_f32_16x16x64_bf16 v[80:83], v[44:47], a[8:15], v0\n" \
  "v_smfmac_f32_16x16x64_bf16 v[84:87], v[44:47], a[88:95], v0\n" \
  "v_smfmac_f32_16x16x64_bf16 v[88:91], v[44:47], a[168:175], v0\n" \
  "s_barrier\n" \
  "s_waitcnt lgkmcnt(1)\n" \
  "ds_read_b128 v[56:59], v5 offset:4096\n" \
  "v_smfmac_f32_16x16x64_bf16 v[80:83], v[48:51], a[16:23], v0\n" \
  "ds_read_b128 v[60:63], v5 offset:5120\n" \
  "v_smfmac_f32_16x16x64_bf16 v[84:87], v[48:51], a[96:103], v0\n" \
  "v_smfmac_f32_16x16x64_bf16 v[88:91], v[48:51], a[176:183], v0\n" \
  "s_waitcnt lgkmcnt(2)\n" \
  "v_smfmac_f32_16x16x64_bf16 v[80:83], v[52:55], a[24:31], v0\n" \
  "s_barrier\n" \
  "ds_read_b128 v[64:67], v5 offset:6144\n" \
  "v_smfmac_f32_16x16x64_bf16 v[84:87], v[52:55], a[104:111], v0\n" \
  "s_waitcnt lgkmcnt(2)\n" \
  "ds_read_b128 v[68:71], v5 offset:7168\n" \
  "v_smfmac_f32_16x16x64_bf16 v[80:83], v[56:59], a[32:39], v0\n" \
  "v_smfmac_f32_16x16x64_bf16 v[84:87], v[56:59], a[112:119], v0\n" \
  "s_waitcnt lgkmcnt(2)\n" \
  "v_smfmac_f32_16x16x64_bf16 v[80:83], v[60:63], a[40:47], v0\n" \
  "s_barrier\n" \
  "ds_read2_b32 v[72:73], v6 offset0:0 offset1:32\n" \
  "ds_read2_b32 v[74:75], v6 offset0:64 offset1:96\n" \
  "v_smfmac_f32_16x16x64_bf16 v[84:87], v[60:63], a[120:127], v0\n" \
  "s_waitcnt lgkmcnt(3)\n" \
  "ds_read2_b32 v[76:77], v7 offset0:0 offset1:32\n" \
  "ds_read2_b32 v[78:79], v7 offset0:64 offset1:96\n" \
  "v_smfmac_f32_16x16x64_bf16 v[80:83], v[64:67], a[48:55], v0\n" \
  "v_smfmac_f32_16x16x64_bf16 v[84:87], v[64:67], a[128:135], v0\n" \
  "s_waitcnt lgkmcnt(4)\n" \
  "v_smfmac_f32_16x16x64_bf16 v[80:83], v[68:71], a[56:63], v0\n" \
  "v_smfmac_f32_16x16x64_bf16 v[84:87], v[68:71], a[136:143], v0\n" \
  "s_waitcnt lgkmcnt(2)\n" \
  "v_smfmac_f32_16x16x64_bf16 v[80:83], v[72:75], a[64:71], v0\n" \
  "v_smfmac_f32_16x16x64_bf16 v[84:87], v[72:75], a[144:151], v0\n" \
  "s_waitcnt lgkmcnt(0)\n" \
  "ds_read_b128 v[40:43], v11 offset:8192\n" \
  "v_smfmac_f32_16x16x64_bf16 v[80:83], v[76:79], a[72:79], v0\n" \
  "ds_read_b128 v[44:47], v11 offset:9216\n" \
  "v_smfmac_f32_16x16x64_bf16 v[84:87], v[76:79], a[152:159], v0\n" \
  "v_smfmac_f32_16x16x64_bf16 v[88:91], v[52:55], a[184:191], v0\n" \
  "ds_read_b128 v[48:51], v11 offset:49152\n" \
  "v_smfmac_f32_16x16x64_bf16 v[88:91], v[56:59], a[192:199], v0\n" \
  "ds_read_b128 v[52:55], v11 offset:50176\n" \
  "v_permlane32_swap_b32 v80, v84\n" \
  "v_smfmac_f32_16x16x64_bf16 v[88:91], v[60:63], a[200:207], v0\n" \
  "ds_read_b128 v[56:59], v11 offset:0\n" \
  "v_permlane32_swap_b32 v81, v85\n" \
  "v_smfmac_f32_16x16x64_bf16 v[88:91], v[64:67], a[208:215], v0\n" \
  "ds_read_b128 v[60:63], v11 offset:1024\n" \
  "v_permlane32_swap_b32 v82, v86\n" \
  "v_smfmac_f32_16x16x64_bf16 v[88:91], v[68:71], a[216:223], v0\n" \
  "ds_read_b128 v[64:67], v11 offset:40960\n" \
  "v_permlane32_swap_b32 v83, v87\n" \
  "v_smfmac_f32_16x16x64_bf16 v[88:91], v[72:75], a[224:231], v0\n" \
  "ds_read_b128 v[68:71], v11 offset:41984\n" \
  "v_add_f32 v108, v80, v84\n" \
  "v_smfmac_f32_16x16x64_bf16 v[88:91], v[76:79], a[232:239], v0\n" \
  "s_nop 1\n" \
  "v_add_f32 v109, v81, v85\n" \
  "v_add_f32 v110, v82, v86\n" \
  "v_add_f32 v111, v83, v87\n" \
  "v_cvt_pk_bf16_f32 v32, v108, v109\n" \
  "v_cvt_pk_bf16_f32 v33, v110, v111\n" \
  "s_nop 0\n" \
  "v_permlane32_swap_b32 v88, v90\n" \
  "v_permlane32_swap_b32 v89, v91\n" \
  "v_pk_add_f32 v[80:81], v[88:89], v[90:91]\n" \
  "v_cvt_pk_bf16_f32 v12, v80, v81\n" \
  "v_cmp_u_f32 vcc, v80, v81\n" \
  "ds_write_b64 v8, v[32:33]\n" \
  "ds_write_b32 v9, v12\n" \
  "s_cbranch_vccnz LMF21_%=\n" \
  "LMOK21_%=:\n" \
  "s_waitcnt lgkmcnt(0)\n" \
  "s_barrier\n" \
  "ds_read_b64_tr_b16 v[80:81], v10\n" \
  "ds_read_b64_tr_b16 v[82:83], v10 offset:32\n" \
  "ds_read_b64_tr_b16 v[84:85], v10 offset:512\n" \
  "ds_read_b64_tr_b16 v[86:87], v10 offset:544\n" \
  "ds_read_b64_tr_b16 v[88:89], v10 offset:1024\n" \
  "ds_read_b64_tr_b16 v[90:91], v10 offset:1056\n" \
  "ds_read_b64_tr_b16 v[92:93], v10 offset:1536\n" \
  "ds_read_b64_tr_b16 v[94:95], v10 offset:1568\n" \
  "ds_read_b64_tr_b16 v[36:37], v10 offset:2048\n" \
  "ds_read_b64_tr_b16 v[38:39], v10 offset:2080\n" \
  "ds_read_b64_tr_b16 v[100:101], v10 offset:2560\n" \
  "ds_read_b64_tr_b16 v[102:103], v10 offset:2592\n" \
  "s_waitcnt lgkmcnt(10)\n" \
  "v_smfmac_f32_16x16x64_bf16 v[16:19], v[80:83], v[240:247], v0\n" \
  "s_waitcnt lgkmcnt(8)\n" \
  "v_smfmac_f32_16x16x64_bf16 v[16:19], v[84:87], v[248:255], v0\n" \
  "s_waitcnt lgkmcnt(6)\n" \
  "v_smfmac_f32_16x16x64_bf16 v[16:19], v[88:91], a[240:247], v0\n" \
  "s_waitcnt lgkmcnt(4)\n" \
  "ds_read_b128 v[72:75], v11 offset:2048\n" \
  "v_smfmac_f32_16x16x64_bf16 v[16:19], v[92:95], a[248:255], v0\n" \
  "s_waitcnt lgkmcnt(3)\n" \
  "ds_read_b128 v[76:79], v11 offset:3072\n" \
  "v_smfmac_f32_16x16x64_bf16 v[16:19], v[36:39], v[40:47], v0\n" \
  "s_waitcnt lgkmcnt(2)\n" \
  "ds_read_b128 v[40:43], v11 offset:43008\n" \
  "v_smfmac_f32_16x16x64_bf16 v[16:19], v[100:103], v[48:55], v0\n" \
  "ds_read_b128 v[44:47], v11 offset:44032\n" \
  "v_smfmac_f32_16x16x64_bf16 v[20:23], v[80:83], v[112:119], v0\n" \
  "ds_read_b128 v[48:51], v11 offset:4096\n" \
  "v_smfmac_f32_16x16x64_bf16 v[20:23], v[84:87], v[120:127], v0\n" \
  "ds_read_b128 v[52:55], v11 offset:5120\n" \
  "v_cvt_pk_bf16_f32 v108, v16, v17\n" \
  "v_smfmac_f32_16x16x64_bf16 v[20:23], v[88:91], v[128:135], v0\n" \
  "v_cvt_pk_bf16_f32 v109, v18, v19\n" \
  "v_smfmac_f32_16x16x64_bf16 v[20:23], v[92:95], v[136:143], v0\n" \
  "buffer_store_dwordx2 v[108:109], v4, s[36:39], s65 offen sc1\n" \
  "v_smfmac_f32_16x16x64_bf16 v[20:23], v[36:39], v[56:63], v0\n" \
  "ds_read_b128 v[56:59], v11 offset:45056\n" \
  "v_smfmac_f32_16x16x64_bf16 v[20:23], v[100:103], v[64:71], v0\n" \
  "ds_read_b128 v[60:63], v11 offset:46080\n" \
  "v_smfmac_f32_16x16x64_bf16 v[104:107], v[80:83], v[144:151], v0\n" \
  "ds_read_b128 v[64:67], v11 offset:6144\n" \
  "v_smfmac_f32_16x16x64_bf16 v[104:107], v[84:87], v[152:159], v0\n" \
  "ds_read_b128 v[68:71], v11 offset:7168\n" \
  "v_cvt_pk_bf16_f32 v110, v20, v21\n" \
  "v_smfmac_f32_16x16x64_bf16 v[104:107], v[88:91], v[160:167], v0\n" \
  "v_cvt_pk_bf16_f32 v111, v22, v23\n" \
  "v_smfmac_f32_16x16x64_bf16 v[104:107], v[92:95], v[168:175], v0\n" \
  "buffer_store_dwordx2 v[110:111], v13, s[36:39], s65 offen sc1\n" \
  "v_mov_b32 v16, 0\n" \
  "s_waitcnt lgkmcnt(8)\n" \
  "v_smfmac_f32_16x16x64_bf16 v[104:107], v[36:39], v[72:79], v0\n" \
  "s_waitcnt lgkmcnt(6)\n" \
  "ds_read_b128 v[72:75], v11 offset:47104\n" \
  "v_mov_b32 v17, 0\n" \
  "v_smfmac_f32_16x16x64_bf16 v[104:107], v[100:103], v[40:47], v0\n" \
  "ds_read_b128 v[76:79], v11 offset:48128\n" \
  "v_mov_b32 v18, 0\n" \
  "v_smfmac_f32_16x16x64_bf16 v[24:27], v[80:83], v[176:183], v0\n" \
  "v_mov_b32 v19, 0\n" \
  "v_smfmac_f32_16x16x64_bf16 v[24:27], v[84:87], v[184:191], v0\n" \
  "v_cvt_pk_bf16_f32 v108, v104, v105\n" \
  "v_smfmac_f32_16x16x64_bf16 v[24:27], v[88:91], v[192:199], v0\n" \
  "v_cvt_pk_bf16_f32 v109, v106, v107\n" \
  "v_smfmac_f32_16x16x64_bf16 v[24:27], v[92:95], v[200:207], v0\n" \
  "buffer_store_dwordx2 v[108:109], v13, s[36:39], s40 offen sc1\n" \
  "s_waitcnt lgkmcnt(6)\n" \
  "v_smfmac_f32_16x16x64_bf16 v[24:27], v[36:39], v[48:55], v0\n" \
  "s_waitcnt lgkmcnt(4)\n" \
  "v_smfmac_f32_16x16x64_bf16 v[24:27], v[100:103], v[56:63], v0\n" \
  "v_smfmac_f32_16x16x64_bf16 v[16:19], v[80:83], v[208:215], v0\n" \
  "v_smfmac_f32_16x16x64_bf16 v[16:19], v[84:87], v[216:223], v0\n" \
  "v_cvt_pk_bf16_f32 v110, v24, v25\n" \
  "v_smfmac_f32_16x16x64_bf16 v[16:19], v[88:91], v[224:231], v0\n" \
  "v_cvt_pk_bf16_f32 v111, v26, v27\n" \
  "v_smfmac_f32_16x16x64_bf16 v[16:19], v[92:95], v[232:239], v0\n" \
  "buffer_store_dwordx2 v[110:111], v13, s[36:39], s41 offen sc1\n" \
  "s_waitcnt lgkmcnt(2)\n" \
  "v_smfmac_f32_16x16x64_bf16 v[16:19], v[36:39], v[64:71], v0\n" \
  "s_waitcnt lgkmcnt(0)\n" \
  "v_smfmac_f32_16x16x64_bf16 v[16:19], v[100:103], v[72:79], v0\n" \
  "s_nop 3\n" \
  "v_mov_b32 v31, v31\n" \
  "s_nop 2\n" \
  "v_cvt_pk_bf16_f32 v108, v16, v17\n" \
  "v_cvt_pk_bf16_f32 v109, v18, v19\n" \
  "buffer_store_dwordx2 v[108:109], v13, s[36:39], s42 offen sc1\n" \
  "s_mov_b32 s67, s66\n" \
  "s_cmp_eq_u32 s64, 64\n" \
  "s_cbranch_scc1 LDONE_%=\n" \
  "s_waitcnt vmcnt(0)\n" \
  "s_branch LOOP21_%=\n" \
  "LMF21_%=:\n" \
  "v_mbcnt_lo_u32_b32 v94, -1, 0\n" \
  "v_mbcnt_hi_u32_b32 v94, -1, v94\n" \
  "v_lshlrev_b32 v94, 4, v94\n" \
  "v_mov_b32 v92, 0\n" \
  "ds_read_b128 v[72:75], v94 offset:0\n" \
  "ds_read_b128 v[76:79], v94 offset:1024\n" \
  "ds_read_b128 v[80:83], v94 offset:2048\n" \
  "ds_read_b128 v[84:87], v94 offset:3072\n" \
  "ds_read_b128 v[88:91], v94 offset:4096\n" \
  "s_waitcnt lgkmcnt(0)\n" \
  "v_max3_u32 v92, v92, v72, v73\n" \
  "v_max3_u32 v92, v92, v74, v75\n" \
  "v_max3_u32 v92, v92, v76, v77\n" \
  "v_max3_u32 v92, v92, v78, v79\n" \
  "v_max3_u32 v92, v92, v80, v81\n" \
  "v_max3_u32 v92, v92, v82, v83\n" \
  "v_max3_u32 v92, v92, v84, v85\n" \
  "v_max3_u32 v92, v92, v86, v87\n" \
  "v_max3_u32 v92, v92, v88, v89\n" \
  "v_max3_u32 v92, v92, v90, v91\n" \
  "ds_read_b128 v[72:75], v94 offset:5120\n" \
  "ds_read_b128 v[76:79], v94 offset:6144\n" \
  "ds_read_b128 v[80:83], v94 offset:7168\n" \
  "ds_read_b128 v[84:87], v94 offset:8192\n" \
  "ds_read_b128 v[88:91], v94 offset:9216\n" \
  "s_waitcnt lgkmcnt(0)\n" \
  "v_max3_u32 v92, v92, v72, v73\n" \
  "v_max3_u32 v92, v92, v74, v75\n" \
  "v_max3_u32 v92, v92, v76, v77\n" \
  "v_max3_u32 v92, v92, v78, v79\n" \
  "v_max3_u32 v92, v92, v80, v81\n" \
  "v_max3_u32 v92, v92, v82, v83\n" \
  "v_max3_u32 v92, v92, v84, v85\n" \
  "v_max3_u32 v92, v92, v86, v87\n" \
  "v_max3_u32 v92, v92, v88, v89\n" \
  "v_max3_u32 v92, v92, v90, v91\n" \
  "v_cmp_eq_u32 vcc, -1, v92\n" \
  "s_cbranch_vccz LMOK21_%=\n" \
  "s_cmp_gt_u32 s58, 4000\n" \
  "s_cbranch_scc1 LMOK21_%=\n" \
  "s_add_u32 s58, s58, 1\n" \
  "s_sub_u32 s64, s64, 1\n" \
  "s_mov_b32 s66, s65\n" \
  "s_waitcnt vmcnt(0) lgkmcnt(0)\n" \
  "s_barrier\n" \
  "s_branch LOOP21_%=\n" \
  "LSTART22_%=:\n" \
  "s_getpc_b64 s[80:81]\n" \
  "LPC22_%=:\n" \
  "s_add_u32 s82, s80, LEA22_%=-LPC22_%=+24\n" \
  "s_addc_u32 s83, s81, 0\n" \
  "LOOP22_%=:\n" \
  "v_mov_b32 v80, 0\n" \
  "v_mov_b32 v81, 0\n" \
  "v_mov_b32 v82, 0\n" \
  "v_mov_b32 v83, 0\n" \
  "v_mov_b32 v84, 0\n" \
  "v_mov_b32 v85, 0\n" \
  "v_mov_b32 v86, 0\n" \
  "v_mov_b32 v87, 0\n" \
  "v_mov_b32 v88, 0\n" \
  "v_mov_b32 v89, 0\n" \
  "v_mov_b32 v90, 0\n" \
  "v_mov_b32 v91, 0\n" \
  "v_mov_b32 v16, 0\n" \
  "v_mov_b32 v17, 0\n" \
  "v_mov_b32 v18, 0\n" \
  "v_mov_b32 v19, 0\n" \
  "v_mov_b32 v20, 0\n" \
  "v_mov_b32 v21, 0\n" \
  "v_mov_b32 v22, 0\n" \
  "v_mov_b32 v23, 0\n" \
  "v_mov_b32 v24, 0\n" \
  "v_mov_b32 v25, 0\n" \
  "v_mov_b32 v26, 0\n" \
  "v_mov_b32 v27, 0\n" \
  "v_mov_b32 v104, 0\n" \
  "v_mov_b32 v105, 0\n" \
  "v_mov_b32 v106, 0\n" \
  "v_mov_b32 v107, 0\n" \
  "v_readfirstlane_b32 s86, v34\n" \
  "s_mov_b32 m0, 0\n" \
  "LWA22_%=:\n" \
  "s_memrealtime s[74:75]\n" \
  "s_memtime s[76:77]\n" \
  "s_waitcnt lgkmcnt(0)\n" \
  "s_sub_i32 s72, s88, s74\n" \
  "s_mul_i32 s72, s72, s90\n" \
  "s_ashr_i32 s72, s72, 8\n" \
  "s_sub_i32 s71, s89, s76\n" \
  "s_min_i32 s71, s71, s72\n" \
  "s_sub_i32 s72, s72, 92\n" \
  "s_max_i32 s71, s71, s72\n" \
  "s_add_u32 s89, s71, s76\n" \
  "s_cmp_gt_i32 s71, 168\n" \
  "s_cbranch_scc1 LWA22_%=\n" \
  "s_cmp_lt_i32 s71, s94\n" \
  "s_cbranch_scc1 LSA22_%=\n" \
  "s_max_i32 s71, s71, 48\n" \
  "s_lshr_b32 s71, s71, 3\n" \
  "s_lshl_b32 s71, s71, 2\n" \
  "s_sub_u32 s78, s82, s71\n" \
  "s_subb_u32 s79, s83, 0\n" \
  "s_setpc_b64 s[78:79]\n" \
  "s_nop 1\n" \
  "s_nop 1\n" \
  "s_nop 1\n" \
  "s_nop 1\n" \
  "s_nop 1\n" \
  "s_nop 1\n" \
  "s_nop 1\n" \
  "s_nop 1\n" \
  "s_nop 1\n" \
  "s_nop 1\n" \
  "s_nop 1\n" \
  "s_nop 1\n" \
  "s_nop 1\n" \
  "s_nop 1\n" \
  "s_nop 1\n" \
  "s_nop 1\n" \
  "LEA22_%=:\n" \
  "buffer_load_dwordx4 v1, s[36:39], s67 offen sc1 lds\n" \
  "buffer_load_dwordx4 v1, s[36:39], s67 offen offset:1024 sc1 lds\n" \
  "buffer_load_dwordx4 v1, s[36:39], s67 offen offset:2048 sc1 lds\n" \
  "buffer_load_dwordx4 v1, s[36:39], s67 offen offset:3072 sc1 lds\n" \
  "s_mov_b32 m0, 4096\n" \
  "s_nop 0\n" \
  "buffer_load_dwordx4 v2, s[36:39], s67 offen sc1 lds\n" \
  "buffer_load_dwordx4 v2, s[36:39], s67 offen offset:1024 sc1 lds\n" \
  "buffer_load_dwordx4 v2, s[36:39], s67 offen offset:2048 sc1 lds\n" \
  "buffer_load_dwordx4 v2, s[36:39], s67 offen offset:3072 sc1 lds\n" \
  "s_mov_b32 m0, 8192\n" \
  "s_nop 0\n" \
  "buffer_load_dwordx4 v3, s[36:39], s67 offen sc1 lds\n" \
  "buffer_load_dwordx4 v3, s[36:39], s67 offen offset:1024 sc1 lds\n" \
  "s_mov_b32 s65, s66\n" \
  "s_xor_b32 s69, s66, 0x2000\n" \
  "s_add_u32 s64, s64, 1\n" \
  "s_barrier\n" \
  "v_readlane_b32 s66, v28, s64\n" \
  "s_add_u32 s40, s65, 131072\n" \
  "s_add_u32 s41, s65, 262144\n" \
  "s_add_u32 s42, s65, 393216\n" \
  "s_cmp_eq_u32 s34, s64\n" \
  "s_cbranch_scc1 LRT22_%=\n" \
  "s_mov_b32 s34, s64\n" \
  "s_mov_b32 s71, s86\n" \
  "s_max_u32 s19, s19, s71\n" \
  "s_and_b32 s72, s71, 0xff\n" \
  "s_lshr_b32 s71, s71, 8\n" \
  "s_cselect_b32 s71, s71, s61\n" \
  "s_sub_i32 s72, s64, s72\n" \
  "s_sub_i32 s71, s71, s61\n" \
  "s_cmp_ge_i32 s72, 0\n" \
  "s_cselect_b32 s71, s71, 0\n" \
  "s_add_u32 s61, s61, s71\n" \
  "s_mul_i32 s72, s72, s71\n" \
  "s_lshl_b32 s72, s72, 1\n" \
  "s_add_u32 s88, s88, s72\n" \
  "s_mul_i32 s72, s72, s90\n" \
  "s_ashr_i32 s72, s72, 8\n" \
  "s_add_u32 s89, s89, s72\n" \
  "s_lshr_b32 s72, s18, 1\n" \
  "s_sub_u32 s80, s72, s18\n" \
  "s_mov_b32 s18, s72\n" \
  "s_add_u32 s71, s71, s80\n" \
  "s_add_u32 s50, s50, s71\n" \
  "s_mul_i32 s91, s50, s90\n" \
  "s_lshr_b32 s91, s91, 8\n" \
  "s_add_u32 s51, s51, s71\n" \
  "s_mul_i32 s92, s51, s90\n" \
  "s_lshr_b32 s92, s92, 8\n" \
  "LRT22_%=:\n" \
  "LM22_%=:\n" \
  "s_waitcnt vmcnt(9)\n" \
  "s_barrier\n" \
  "ds_read_b128 v[40:43], v5\n" \
  "s_waitcnt vmcnt(8)\n" \
  "s_barrier\n" \
  "ds_read_b128 v[44:47], v5 offset:1024\n" \
  "s_waitcnt lgkmcnt(1)\n" \
  "v_smfmac_f32_16x16x64_bf16 v[80:83], v[40:43], a[0:7], v0\n" \
  "s_waitcnt vmcnt(6)\n" \
  "v_smfmac_f32_16x16x64_bf16 v[84:87], v[40:43], a[80:87], v0\n" \
  "s_barrier\n" \
  "ds_read_b128 v[48:51], v5 offset:2048\n" \
  "v_smfmac_f32_16x16x64_bf16 v[88:91], v[40:43], a[160:167], v0\n" \
  "s_waitcnt lgkmcnt(1)\n" \
  "ds_read_b128 v[52:55], v5 offset:3072\n" \
  "v_smfmac_f32_16x16x64_bf16 v[80:83], v[44:47], a[8:15], v0\n" \
  "v_smfmac_f32_16x16x64_bf16 v[84:87], v[44:47], a[88:95], v0\n" \
  "s_waitcnt vmcnt(4)\n" \
  "v_smfmac_f32_16x16x64_bf16 v[88:91], v[44:47], a[168:175], v0\n" \
  "s_barrier\n" \
  "s_waitcnt lgkmcnt(1)\n" \
  "ds_read_b128 v[56:59], v5 offset:4096\n" \
  "v_smfmac_f32_16x16x64_bf16 v[80:83], v[48:51], a[16:23], v0\n" \
  "ds_read_b128 v[60:63], v5 offset:5120\n" \
  "v_smfmac_f32_16x16x64_bf16 v[84:87], v[48:51], a[96:103], v0\n" \
  "v_smfmac_f32_16x16x64_bf16 v[88:91], v[48:51], a[176:183], v0\n" \
  "s_waitcnt vmcnt(2)\n" \
  "s_waitcnt lgkmcnt(2)\n" \
  "v_smfmac_f32_16x16x64_bf16 v[80:83], v[52:55], a[24:31], v0\n" \
  "s_barrier\n" \
  "ds_read_b128 v[64:67], v5 offset:6144\n" \
  "v_smfmac_f32_16x16x64_bf16 v[84:87], v[52:55], a[104:111], v0\n" \
  "s_waitcnt lgkmcnt(2)\n" \
  "ds_read_b128 v[68:71], v5 offset:7168\n" \
  "v_smfmac_f32_16x16x64_bf16 v[80:83], v[56:59], a[32:39], v0\n" \
  "v_smfmac_f32_16x16x64_bf16 v[84:87], v[56:59], a[112:119], v0\n" \
  "s_waitcnt vmcnt(0)\n" \
  "s_waitcnt lgkmcnt(2)\n" \
  "v_smfmac_f32_16x16x64_bf16 v[80:83], v[60:63], a[40:47], v0\n" \
  "s_barrier\n" \
  "ds_read2_b32 v[72:73], v6 offset0:0 offset1:32\n" \
  "ds_read2_b32 v[74:75], v6 offset0:64 offset1:96\n" \
  "v_smfmac_f32_16x16x64_bf16 v[84:87], v[60:63], a[120:127], v0\n" \
  "s_waitcnt lgkmcnt(3)\n" \
  "ds_read2_b32 v[76:77], v7 offset0:0 offset1:32\n" \
  "ds_read2_b32 v[78:79], v7 offset0:64 offset1:96\n" \
  "v_smfmac_f32_16x16x64_bf16 v[80:83], v[64:67], a[48:55], v0\n" \
  "v_smfmac_f32_16x16x64_bf16 v[84:87], v[64:67], a[128:135], v0\n" \
  "s_waitcnt lgkmcnt(4)\n" \
  "v_smfmac_f32_16x16x64_bf16 v[80:83], v[68:71], a[56:63], v0\n" \
  "v_smfmac_f32_16x16x64_bf16 v[84:87], v[68:71], a[136:143], v0\n" \
  "s_waitcnt lgkmcnt(2)\n" \
  "v_smfmac_f32_16x16x64_bf16 v[80:83], v[72:75], a[64:71], v0\n" \
  "v_smfmac_f32_16x16x64_bf16 v[84:87], v[72:75], a[144:151], v0\n" \
  "s_waitcnt lgkmcnt(0)\n" \
  "ds_read_b128 v[40:43], v11 offset:8192\n" \
  "v_smfmac_f32_16x16x64_bf16 v[80:83], v[76:79], a[72:79], v0\n" \
  "ds_read_b128 v[44:47], v11 offset:9216\n" \
  "v_smfmac_f32_16x16x64_bf16 v[84:87], v[76:79], a[152:159], v0\n" \
  "v_smfmac_f32_16x16x64_bf16 v[88:91], v[52:55], a[184:191], v0\n" \
  "ds_read_b128 v[48:51], v11 offset:49152\n" \
  "v_smfmac_f32_16x16x64_bf16 v[88:91], v[56:59], a[192:199], v0\n" \
  "ds_read_b128 v[52:55], v11 offset:50176\n" \
  "v_permlane32_swap_b32 v80, v84\n" \
  "v_smfmac_f32_16x16x64_bf16 v[88:91], v[60:63], a[200:207], v0\n" \
  "ds_read_b128 v[56:59], v11 offset:0\n" \
  "v_permlane32_swap_b32 v81, v85\n" \
  "v_smfmac_f32_16x16x64_bf16 v[88:91], v[64:67], a[208:215], v0\n" \
  "ds_read_b128 v[60:63], v11 offset:1024\n" \
  "v_permlane32_swap_b32 v82, v86\n" \
  "v_smfmac_f32_16x16x64_bf16 v[88:91], v[68:71], a[216:223], v0\n" \
  "ds_read_b128 v[64:67], v11 offset:40960\n" \
  "v_permlane32_swap_b32 v83, v87\n" \
  "v_smfmac_f32_16x16x64_bf16 v[88:91], v[72:75], a[224:231], v0\n" \
  "ds_read_b128 v[68:71], v11 offset:41984\n" \
  "v_add_f32 v108, v80, v84\n" \
  "v_smfmac_f32_16x16x64_bf16 v[88:91], v[76:79], a[232:239], v0\n" \
  "s_nop 1\n" \
  "v_add_f32 v109, v81, v85\n" \
  "v_add_f32 v110, v82, v86\n" \
  "v_add_f32 v111, v83, v87\n" \
  "v_cvt_pk_bf16_f32 v32, v108, v109\n" \
  "v_cvt_pk_bf16_f32 v33, v110, v111\n" \
  "s_nop 0\n" \
  "v_permlane32_swap_b32 v88, v90\n" \
  "v_permlane32_swap_b32 v89, v91\n" \
  "v_pk_add_f32 v[80:81], v[88:89], v[90:91]\n" \
  "v_cvt_pk_bf16_f32 v12, v80, v81\n" \
  "v_cmp_u_f32 vcc, v80, v81\n" \
  "ds_write_b64 v8, v[32:33]\n" \
  "ds_write_b32 v9, v12\n" \
  "s_cbranch_vccnz LMF22_%=\n" \
  "LMOK22_%=:\n" \
  "s_waitcnt lgkmcnt(0)\n" \
  "s_barrier\n" \
  "ds_read_b64_tr_b16 v[80:81], v10\n" \
  "ds_read_b64_tr_b16 v[82:83], v10 offset:32\n" \
  "ds_read_b64_tr_b16 v[84:85], v10 offset:512\n" \
  "ds_read_b64_tr_b16 v[86:87], v10 offset:544\n" \
  "ds_read_b64_tr_b16 v[88:89], v10 offset:1024\n" \
  "ds_read_b64_tr_b16 v[90:91], v10 offset:1056\n" \
  "ds_read_b64_tr_b16 v[92:93], v10 offset:1536\n" \
  "ds_read_b64_tr_b16 v[94:95], v10 offset:1568\n" \
  "ds_read_b64_tr_b16 v[36:37], v10 offset:2048\n" \
  "ds_read_b64_tr_b16 v[38:39], v10 offset:2080\n" \
  "ds_read_b64_tr_b16 v[100:101], v10 offset:2560\n" \
  "ds_read_b64_tr_b16 v[102:103], v10 offset:2592\n" \
  "s_waitcnt lgkmcnt(10)\n" \
  "v_smfmac_f32_16x16x64_bf16 v[16:19], v[80:83], v[240:247], v0\n" \
  "s_waitcnt lgkmcnt(8)\n" \
  "v_smfmac_f32_16x16x64_bf16 v[16:19], v[84:87], v[248:255], v0\n" \
  "s_waitcnt lgkmcnt(6)\n" \
  "v_smfmac_f32_16x16x64_bf16 v[16:19], v[88:91], a[240:247], v0\n" \
  "s_waitcnt lgkmcnt(4)\n" \
  "ds_read_b128 v[72:75], v11 offset:2048\n" \
  "v_smfmac_f32_16x16x64_bf16 v[16:19], v[92:95], a[248:255], v0\n" \
  "s_waitcnt lgkmcnt(3)\n" \
  "ds_read_b128 v[76:79], v11 offset:3072\n" \
  "v_smfmac_f32_16x16x64_bf16 v[16:19], v[36:39], v[40:47], v0\n" \
  "s_waitcnt lgkmcnt(2)\n" \
  "ds_read_b128 v[40:43], v11 offset:43008\n" \
  "v_smfmac_f32_16x16x64_bf16 v[16:19], v[100:103], v[48:55], v0\n" \
  "ds_read_b128 v[44:47], v11 offset:44032\n" \
  "v_smfmac_f32_16x16x64_bf16 v[20:23], v[80:83], v[112:119], v0\n" \
  "ds_read_b128 v[48:51], v11 offset:4096\n" \
  "v_smfmac_f32_16x16x64_bf16 v[20:23], v[84:87], v[120:127], v0\n" \
  "ds_read_b128 v[52:55], v11 offset:5120\n" \
  "v_cvt_pk_bf16_f32 v108, v16, v17\n" \
  "v_smfmac_f32_16x16x64_bf16 v[20:23], v[88:91], v[128:135], v0\n" \
  "v_cvt_pk_bf16_f32 v109, v18, v19\n" \
  "v_smfmac_f32_16x16x64_bf16 v[20:23], v[92:95], v[136:143], v0\n" \
  "buffer_store_dwordx2 v[108:109], v4, s[36:39], s65 offen sc1\n" \
  "v_smfmac_f32_16x16x64_bf16 v[20:23], v[36:39], v[56:63], v0\n" \
  "ds_read_b128 v[56:59], v11 offset:45056\n" \
  "v_smfmac_f32_16x16x64_bf16 v[20:23], v[100:103], v[64:71], v0\n" \
  "ds_read_b128 v[60:63], v11 offset:46080\n" \
  "v_smfmac_f32_16x16x64_bf16 v[104:107], v[80:83], v[144:151], v0\n" \
  "ds_read_b128 v[64:67], v11 offset:6144\n" \
  "v_smfmac_f32_16x16x64_bf16 v[104:107], v[84:87], v[152:159], v0\n" \
  "ds_read_b128 v[68:71], v11 offset:7168\n" \
  "v_cvt_pk_bf16_f32 v110, v20, v21\n" \
  "v_smfmac_f32_16x16x64_bf16 v[104:107], v[88:91], v[160:167], v0\n" \
  "v_cvt_pk_bf16_f32 v111, v22, v23\n" \
  "v_smfmac_f32_16x16x64_bf16 v[104:107], v[92:95], v[168:175], v0\n" \
  "buffer_store_dwordx2 v[110:111], v13, s[36:39], s65 offen sc1\n" \
  "v_mov_b32 v16, 0\n" \
  "s_waitcnt lgkmcnt(8)\n" \
  "v_smfmac_f32_16x16x64_bf16 v[104:107], v[36:39], v[72:79], v0\n" \
  "s_waitcnt lgkmcnt(6)\n" \
  "ds_read_b128 v[72:75], v11 offset:47104\n" \
  "v_mov_b32 v17, 0\n" \
  "v_smfmac_f32_16x16x64_bf16 v[104:107], v[100:103], v[40:47], v0\n" \
  "ds_read_b128 v[76:79], v11 offset:48128\n" \
  "v_mov_b32 v18, 0\n" \
  "v_smfmac_f32_16x16x64_bf16 v[24:27], v[80:83], v[176:183], v0\n" \
  "v_mov_b32 v19, 0\n" \
  "v_smfmac_f32_16x16x64_bf16 v[24:27], v[84:87], v[184:191], v0\n" \
  "v_cvt_pk_bf16_f32 v108, v104, v105\n" \
  "v_smfmac_f32_16x16x64_bf16 v[24:27], v[88:91], v[192:199], v0\n" \
  "v_cvt_pk_bf16_f32 v109, v106, v107\n" \
  "v_smfmac_f32_16x16x64_bf16 v[24:27], v[92:95], v[200:207], v0\n" \
  "buffer_store_dwordx2 v[108:109], v13, s[36:39], s40 offen sc1\n" \
  "s_waitcnt lgkmcnt(6)\n" \
  "v_smfmac_f32_16x16x64_bf16 v[24:27], v[36:39], v[48:55], v0\n" \
  "s_waitcnt lgkmcnt(4)\n" \
  "v_smfmac_f32_16x16x64_bf16 v[24:27], v[100:103], v[56:63], v0\n" \
  "v_smfmac_f32_16x16x64_bf16 v[16:19], v[80:83], v[208:215], v0\n" \
  "v_smfmac_f32_16x16x64_bf16 v[16:19], v[84:87], v[216:223], v0\n" \
  "v_cvt_pk_bf16_f32 v110, v24, v25\n" \
  "v_smfmac_f32_16x16x64_bf16 v[16:19], v[88:91], v[224:231], v0\n" \
  "v_cvt_pk_bf16_f32 v111, v26, v27\n" \
  "v_smfmac_f32_16x16x64_bf16 v[16:19], v[92:95], v[232:239], v0\n" \
  "buffer_store_dwordx2 v[110:111], v13, s[36:39], s41 offen sc1\n" \
  "s_waitcnt lgkmcnt(2)\n" \
  "v_smfmac_f32_16x16x64_bf16 v[16:19], v[36:39], v[64:71], v0\n" \
  "s_waitcnt lgkmcnt(0)\n" \
  "v_smfmac_f32_16x16x64_bf16 v[16:19], v[100:103], v[72:79], v0\n" \
  "s_nop 3\n" \
  "v_mov_b32 v31, v31\n" \
  "s_nop 2\n" \
  "v_cvt_pk_bf16_f32 v108, v16, v17\n" \
  "v_cvt_pk_bf16_f32 v109, v18, v19\n" \
  "buffer_store_dwordx2 v[108:109], v13, s[36:39], s42 offen sc1\n" \
  "s_mov_b32 s67, s66\n" \
  "s_cmp_eq_u32 s64, 64\n" \
  "s_cbranch_scc1 LDONE_%=\n" \
  "s_add_u32 s88, s88, s50\n" \
  "s_add_u32 s89, s89, s91\n" \
  "s_add_u32 s88, s88, s51\n" \
  "s_add_u32 s89, s89, s92\n" \
  "s_sub_u32 s72, s74, s95\n" \
  "s_cmp_lt_u32 s64, 3\n" \
  "s_cbranch_scc1 LRB22_%=\n" \
  "s_cmp_gt_u32 s72, 900\n" \
  "s_cbranch_scc0 LRC22_%=\n" \
  "s_sub_u32 s71, s76, s96\n" \
  "s_sub_u32 s72, s74, s95\n" \
  "v_cvt_f32_u32 v110, s71\n" \
  "v_cvt_f32_u32 v111, s72\n" \
  "v_rcp_f32 v111, v111\n" \
  "v_mul_f32 v110, 0x43820000, v110\n" \
  "v_mul_f32 v110, v110, v111\n" \
  "v_cvt_u32_f32 v110, v110\n" \
  "s_nop 0\n" \
  "v_readfirstlane_b32 s71, v110\n" \
  "s_min_u32 s90, s71, 6400\n" \
  "LRB22_%=:\n" \
  "s_mov_b32 s95, s74\n" \
  "s_mov_b32 s96, s76\n" \
  "LRC22_%=:\n" \
  "v_cvt_f32_u32 v110, s90\n" \
  "v_rcp_f32 v110, v110\n" \
  "v_mov_b32 v111, 0xc0b00000\n" \
  "v_fmac_f32 v111, 0x48cc6500, v110\n" \
  "v_cvt_u32_f32 v111, v111\n" \
  "s_cmp_lt_u32 s64, s99\n" \
  "s_cselect_b32 s98, s97, s98\n" \
  "s_cselect_b32 s80, 0, -1\n" \
  "s_cmp_lg_u32 s98, s97\n" \
  "s_addc_u32 s72, s61, 0\n" \
  "s_mov_b32 s98, s97\n" \
  "v_readfirstlane_b32 s71, v111\n" \
  "s_max_u32 s71, s71, s72\n" \
  "s_and_b32 s71, s71, s80\n" \
  "s_add_u32 s72, s64, 2\n" \
  "s_lshl_b32 s80, s71, 8\n" \
  "s_or_b32 s80, s80, s72\n" \
  "s_add_u32 s72, s64, 3\n" \
  "s_cmp_gt_u32 s71, s61\n" \
  "s_cselect_b32 s19, s80, s19\n" \
  "s_cselect_b32 s99, s72, s99\n" \
  "v_mov_b32 v110, s19\n" \
  "s_lshl_b32 s72, s64, 7\n" \
  "buffer_store_dword v110, v30, s[36:39], s72 offen offset:128 sc1\n" \
  "buffer_load_dword v34, v29, s[36:39], s72 offen sc1\n" \
  "s_waitcnt vmcnt(0)\n" \
  "s_branch LOOP22_%=\n" \
  "LSA22_%=:\n" \
  "s_add_u32 s88, s88, s52\n" \
  "s_add_u32 s89, s89, s93\n" \
  "s_add_u32 s97, s97, 1\n" \
  "s_branch LWA22_%=\n" \
  "LMF22_%=:\n" \
  "v_mbcnt_lo_u32_b32 v94, -1, 0\n" \
  "v_mbcnt_hi_u32_b32 v94, -1, v94\n" \
  "v_lshlrev_b32 v94, 4, v94\n" \
  "v_mov_b32 v92, 0\n" \
  "ds_read_b128 v[72:75], v94 offset:0\n" \
  "ds_read_b128 v[76:79], v94 offset:1024\n" \
  "ds_read_b128 v[80:83], v94 offset:2048\n" \
  "ds_read_b128 v[84:87], v94 offset:3072\n" \
  "ds_read_b128 v[88:91], v94 offset:4096\n" \
  "s_waitcnt lgkmcnt(0)\n" \
  "v_max3_u32 v92, v92, v72, v73\n" \
  "v_max3_u32 v92, v92, v74, v75\n" \
  "v_max3_u32 v92, v92, v76, v77\n" \
  "v_max3_u32 v92, v92, v78, v79\n" \
  "v_max3_u32 v92, v92, v80, v81\n" \
  "v_max3_u32 v92, v92, v82, v83\n" \
  "v_max3_u32 v92, v92, v84, v85\n" \
  "v_max3_u32 v92, v92, v86, v87\n" \
  "v_max3_u32 v92, v92, v88, v89\n" \
  "v_max3_u32 v92, v92, v90, v91\n" \
  "ds_read_b128 v[72:75], v94 offset:5120\n" \
  "ds_read_b128 v[76:79], v94 offset:6144\n" \
  "ds_read_b128 v[80:83], v94 offset:7168\n" \
  "ds_read_b128 v[84:87], v94 offset:8192\n" \
  "ds_read_b128 v[88:91], v94 offset:9216\n" \
  "s_waitcnt lgkmcnt(0)\n" \
  "v_max3_u32 v92, v92, v72, v73\n" \
  "v_max3_u32 v92, v92, v74, v75\n" \
  "v_max3_u32 v92, v92, v76, v77\n" \
  "v_max3_u32 v92, v92, v78, v79\n" \
  "v_max3_u32 v92, v92, v80, v81\n" \
  "v_max3_u32 v92, v92, v82, v83\n" \
  "v_max3_u32 v92, v92, v84, v85\n" \
  "v_max3_u32 v92, v92, v86, v87\n" \
  "v_max3_u32 v92, v92, v88, v89\n" \
  "v_max3_u32 v92, v92, v90, v91\n" \
  "v_cmp_eq_u32 vcc, -1, v92\n" \
  "s_cbranch_vccz LMOK22_%=\n" \
  "s_cmp_gt_u32 s58, 4000\n" \
  "s_cbranch_scc1 LMOK22_%=\n" \
  "s_add_u32 s58, s58, 1\n" \
  "s_sub_u32 s64, s64, 1\n" \
  "s_mov_b32 s66, s65\n" \
  "s_add_u32 s88, s88, s52\n" \
  "s_add_u32 s89, s89, s93\n" \
  "s_add_u32 s97, s97, 1\n" \
  "s_waitcnt vmcnt(0) lgkmcnt(0)\n" \
  "s_barrier\n" \
  "s_branch LOOP22_%=\n" \
  "LSTART23_%=:\n" \
  "LOOP23_%=:\n" \
  "v_mov_b32 v80, 0\n" \
  "v_mov_b32 v81, 0\n" \
  "v_mov_b32 v82, 0\n" \
  "v_mov_b32 v83, 0\n" \
  "v_mov_b32 v84, 0\n" \
  "v_mov_b32 v85, 0\n" \
  "v_mov_b32 v86, 0\n" \
  "v_mov_b32 v87, 0\n" \
  "v_mov_b32 v88, 0\n" \
  "v_mov_b32 v89, 0\n" \
  "v_mov_b32 v90, 0\n" \
  "v_mov_b32 v91, 0\n" \
  "v_mov_b32 v16, 0\n" \
  "v_mov_b32 v17, 0\n" \
  "v_mov_b32 v18, 0\n" \
  "v_mov_b32 v19, 0\n" \
  "v_mov_b32 v20, 0\n" \
  "v_mov_b32 v21, 0\n" \
  "v_mov_b32 v22, 0\n" \
  "v_mov_b32 v23, 0\n" \
  "v_mov_b32 v24, 0\n" \
  "v_mov_b32 v25, 0\n" \
  "v_mov_b32 v26, 0\n" \
  "v_mov_b32 v27, 0\n" \
  "v_mov_b32 v104, 0\n" \
  "v_mov_b32 v105, 0\n" \
  "v_mov_b32 v106, 0\n" \
  "v_mov_b32 v107, 0\n" \
  "s_mov_b32 s65, s66\n" \
  "s_xor_b32 s69, s66, 0x2000\n" \
  "v_mov_b32 v34, s64\n" \
  "s_add_u32 s64, s64, 1\n" \
  "v_readlane_b32 s66, v28, s64\n" \
  "s_add_u32 s40, s65, 131072\n" \
  "s_add_u32 s41, s65, 262144\n" \
  "s_add_u32 s42, s65, 393216\n" \
  "s_barrier\n" \
  "LM23_%=:\n" \
  "s_barrier\n" \
  "ds_read_b128 v[40:43], v5\n" \
  "s_barrier\n" \
  "ds_read_b128 v[44:47], v5 offset:1024\n" \
  "s_waitcnt lgkmcnt(1)\n" \
  "v_smfmac_f32_16x16x64_bf16 v[80:83], v[40:43], a[0:7], v0\n" \
  "v_smfmac_f32_16x16x64_bf16 v[84:87], v[40:43], a[80:87], v0\n" \
  "s_barrier\n" \
  "ds_read_b128 v[48:51], v5 offset:2048\n" \
  "v_smfmac_f32_16x16x64_bf16 v[88:91], v[40:43], a[160:167], v0\n" \
  "s_waitcnt lgkmcnt(1)\n" \
  "ds_read_b128 v[52:55], v5 offset:3072\n" \
  "v_smfmac_f32_16x16x64_bf16 v[80:83], v[44:47], a[8:15], v0\n" \
  "v_smfmac_f32_16x16x64_bf16 v[84:87], v[44:47], a[88:95], v0\n" \
  "v_smfmac_f32_16x16x64_bf16 v[88:91], v[44:47], a[168:175], v0\n" \
  "s_barrier\n" \
  "s_waitcnt lgkmcnt(1)\n" \
  "ds_read_b128 v[56:59], v5 offset:4096\n" \
  "v_smfmac_f32_16x16x64_bf16 v[80:83], v[48:51], a[16:23], v0\n" \
  "ds_read_b128 v[60:63], v5 offset:5120\n" \
  "v_smfmac_f32_16x16x64_bf16 v[84:87], v[48:51], a[96:103], v0\n" \
  "v_smfmac_f32_16x16x64_bf16 v[88:91], v[48:51], a[176:183], v0\n" \
  "s_waitcnt lgkmcnt(2)\n" \
  "v_smfmac_f32_16x16x64_bf16 v[80:83], v[52:55], a[24:31], v0\n" \
  "s_barrier\n" \
  "ds_read_b128 v[64:67], v5 offset:6144\n" \
  "v_smfmac_f32_16x16x64_bf16 v[84:87], v[52:55], a[104:111], v0\n" \
  "s_waitcnt lgkmcnt(2)\n" \
  "ds_read_b128 v[68:71], v5 offset:7168\n" \
  "v_smfmac_f32_16x16x64_bf16 v[80:83], v[56:59], a[32:39], v0\n" \
  "v_smfmac_f32_16x16x64_bf16 v[84:87], v[56:59], a[112:119], v0\n" \
  "s_waitcnt lgkmcnt(2)\n" \
  "v_smfmac_f32_16x16x64_bf16 v[80:83], v[60:63], a[40:47], v0\n" \
  "s_barrier\n" \
  "ds_read2_b32 v[72:73], v6 offset0:0 offset1:32\n" \
  "ds_read2_b32 v[74:75], v6 offset0:64 offset1:96\n" \
  "v_smfmac_f32_16x16x64_bf16 v[84:87], v[60:63], a[120:127], v0\n" \
  "s_waitcnt lgkmcnt(3)\n" \
  "ds_read2_b32 v[76:77], v7 offset0:0 offset1:32\n" \
  "ds_read2_b32 v[78:79], v7 offset0:64 offset1:96\n" \
  "v_smfmac_f32_16x16x64_bf16 v[80:83], v[64:67], a[48:55], v0\n" \
  "v_smfmac_f32_16x16x64_bf16 v[84:87], v[64:67], a[128:135], v0\n" \
  "s_waitcnt lgkmcnt(4)\n" \
  "v_smfmac_f32_16x16x64_bf16 v[80:83], v[68:71], a[56:63], v0\n" \
  "v_smfmac_f32_16x16x64_bf16 v[84:87], v[68:71], a[136:143], v0\n" \
  "s_waitcnt lgkmcnt(2)\n" \
  "v_smfmac_f32_16x16x64_bf16 v[80:83], v[72:75], a[64:71], v0\n" \
  "v_smfmac_f32_16x16x64_bf16 v[84:87], v[72:75], a[144:151], v0\n" \
  "s_waitcnt lgkmcnt(0)\n" \
  "ds_read_b128 v[40:43], v11 offset:8192\n" \
  "v_smfmac_f32_16x16x64_bf16 v[80:83], v[76:79], a[72:79], v0\n" \
  "ds_read_b128 v[44:47], v11 offset:9216\n" \
  "v_smfmac_f32_16x16x64_bf16 v[84:87], v[76:79], a[152:159], v0\n" \
  "v_smfmac_f32_16x16x64_bf16 v[88:91], v[52:55], a[184:191], v0\n" \
  "ds_read_b128 v[48:51], v11 offset:49152\n" \
  "v_smfmac_f32_16x16x64_bf16 v[88:91], v[56:59], a[192:199], v0\n" \
  "ds_read_b128 v[52:55], v11 offset:50176\n" \
  "v_permlane32_swap_b32 v80, v84\n" \
  "v_smfmac_f32_16x16x64_bf16 v[88:91], v[60:63], a[200:207], v0\n" \
  "ds_read_b128 v[56:59], v11 offset:0\n" \
  "v_permlane32_swap_b32 v81, v85\n" \
  "v_smfmac_f32_16x16x64_bf16 v[88:91], v[64:67], a[208:215], v0\n" \
  "ds_read_b128 v[60:63], v11 offset:1024\n" \
  "v_permlane32_swap_b32 v82, v86\n" \
  "v_smfmac_f32_16x16x64_bf16 v[88:91], v[68:71], a[216:223], v0\n" \
  "ds_read_b128 v[64:67], v11 offset:40960\n" \
  "v_permlane32_swap_b32 v83, v87\n" \
  "v_smfmac_f32_16x16x64_bf16 v[88:91], v[72:75], a[224:231], v0\n" \
  "ds_read_b128 v[68:71], v11 offset:41984\n" \
  "v_add_f32 v108, v80, v84\n" \
  "v_smfmac_f32_16x16x64_bf16 v[88:91], v[76:79], a[232:239], v0\n" \
  "s_nop 1\n" \
  "v_add_f32 v109, v81, v85\n" \
  "v_add_f32 v110, v82, v86\n" \
  "v_add_f32 v111, v83, v87\n" \
  "v_cvt_pk_bf16_f32 v32, v108, v109\n" \
  "v_cvt_pk_bf16_f32 v33, v110, v111\n" \
  "s_nop 0\n" \
  "v_permlane32_swap_b32 v88, v90\n" \
  "v_permlane32_swap_b32 v89, v91\n" \
  "v_pk_add_f32 v[80:81], v[88:89], v[90:91]\n" \
  "v_cvt_pk_bf16_f32 v12, v80, v81\n" \
  "v_cmp_u_f32 vcc, v80, v81\n" \
  "ds_write_b64 v8, v[32:33]\n" \
  "ds_write_b32 v9, v12\n" \
  "s_cbranch_vccnz LMF23_%=\n" \
  "LMOK23_%=:\n" \
  "s_waitcnt lgkmcnt(0)\n" \
  "s_barrier\n" \
  "ds_read_b64_tr_b16 v[80:81], v10\n" \
  "ds_read_b64_tr_b16 v[82:83], v10 offset:32\n" \
  "ds_read_b64_tr_b16 v[84:85], v10 offset:512\n" \
  "ds_read_b64_tr_b16 v[86:87], v10 offset:544\n" \
  "ds_read_b64_tr_b16 v[88:89], v10 offset:1024\n" \
  "ds_read_b64_tr_b16 v[90:91], v10 offset:1056\n" \
  "ds_read_b64_tr_b16 v[92:93], v10 offset:1536\n" \
  "ds_read_b64_tr_b16 v[94:95], v10 offset:1568\n" \
  "ds_read_b64_tr_b16 v[36:37], v10 offset:2048\n" \
  "ds_read_b64_tr_b16 v[38:39], v10 offset:2080\n" \
  "ds_read_b64_tr_b16 v[100:101], v10 offset:2560\n" \
  "ds_read_b64_tr_b16 v[102:103], v10 offset:2592\n" \
  "s_waitcnt lgkmcnt(10)\n" \
  "v_smfmac_f32_16x16x64_bf16 v[16:19], v[80:83], v[240:247], v0\n" \
  "s_waitcnt lgkmcnt(8)\n" \
  "v_smfmac_f32_16x16x64_bf16 v[16:19], v[84:87], v[248:255], v0\n" \
  "s_waitcnt lgkmcnt(6)\n" \
  "v_smfmac_f32_16x16x64_bf16 v[16:19], v[88:91], a[240:247], v0\n" \
  "s_waitcnt lgkmcnt(4)\n" \
  "ds_read_b128 v[72:75], v11 offset:2048\n" \
  "v_smfmac_f32_16x16x64_bf16 v[16:19], v[92:95], a[248:255], v0\n" \
  "s_waitcnt lgkmcnt(3)\n" \
  "ds_read_b128 v[76:79], v11 offset:3072\n" \
  "v_smfmac_f32_16x16x64_bf16 v[16:19], v[36:39], v[40:47], v0\n" \
  "s_waitcnt lgkmcnt(2)\n" \
  "ds_read_b128 v[40:43], v11 offset:43008\n" \
  "v_smfmac_f32_16x16x64_bf16 v[16:19], v[100:103], v[48:55], v0\n" \
  "ds_read_b128 v[44:47], v11 offset:44032\n" \
  "v_smfmac_f32_16x16x64_bf16 v[20:23], v[80:83], v[112:119], v0\n" \
  "ds_read_b128 v[48:51], v11 offset:4096\n" \
  "v_smfmac_f32_16x16x64_bf16 v[20:23], v[84:87], v[120:127], v0\n" \
  "ds_read_b128 v[52:55], v11 offset:5120\n" \
  "v_cvt_pk_bf16_f32 v108, v16, v17\n" \
  "v_smfmac_f32_16x16x64_bf16 v[20:23], v[88:91], v[128:135], v0\n" \
  "v_cvt_pk_bf16_f32 v109, v18, v19\n" \
  "v_smfmac_f32_16x16x64_bf16 v[20:23], v[92:95], v[136:143], v0\n" \
  "buffer_store_dwordx2 v[108:109], v4, s[36:39], s65 offen sc1\n" \
  "v_smfmac_f32_16x16x64_bf16 v[20:23], v[36:39], v[56:63], v0\n" \
  "ds_read_b128 v[56:59], v11 offset:45056\n" \
  "v_smfmac_f32_16x16x64_bf16 v[20:23], v[100:103], v[64:71], v0\n" \
  "ds_read_b128 v[60:63], v11 offset:46080\n" \
  "v_smfmac_f32_16x16x64_bf16 v[104:107], v[80:83], v[144:151], v0\n" \
  "ds_read_b128 v[64:67], v11 offset:6144\n" \
  "v_smfmac_f32_16x16x64_bf16 v[104:107], v[84:87], v[152:159], v0\n" \
  "ds_read_b128 v[68:71], v11 offset:7168\n" \
  "v_cvt_pk_bf16_f32 v110, v20, v21\n" \
  "v_smfmac_f32_16x16x64_bf16 v[104:107], v[88:91], v[160:167], v0\n" \
  "v_cvt_pk_bf16_f32 v111, v22, v23\n" \
  "v_smfmac_f32_16x16x64_bf16 v[104:107], v[92:95], v[168:175], v0\n" \
  "buffer_store_dwordx2 v[110:111], v13, s[36:39], s65 offen sc1\n" \
  "v_mov_b32 v16, 0\n" \
  "s_waitcnt lgkmcnt(8)\n" \
  "v_smfmac_f32_16x16x64_bf16 v[104:107], v[36:39], v[72:79], v0\n" \
  "s_waitcnt lgkmcnt(6)\n" \
  "ds_read_b128 v[72:75], v11 offset:47104\n" \
  "v_mov_b32 v17, 0\n" \
  "v_smfmac_f32_16x16x64_bf16 v[104:107], v[100:103], v[40:47], v0\n" \
  "ds_read_b128 v[76:79], v11 offset:48128\n" \
  "v_mov_b32 v18, 0\n" \
  "v_smfmac_f32_16x16x64_bf16 v[24:27], v[80:83], v[176:183], v0\n" \
  "v_mov_b32 v19, 0\n" \
  "v_smfmac_f32_16x16x64_bf16 v[24:27], v[84:87], v[184:191], v0\n" \
  "v_cvt_pk_bf16_f32 v108, v104, v105\n" \
  "v_smfmac_f32_16x16x64_bf16 v[24:27], v[88:91], v[192:199], v0\n" \
  "v_cvt_pk_bf16_f32 v109, v106, v107\n" \
  "v_smfmac_f32_16x16x64_bf16 v[24:27], v[92:95], v[200:207], v0\n" \
  "buffer_store_dwordx2 v[108:109], v13, s[36:39], s40 offen sc1\n" \
  "s_waitcnt lgkmcnt(6)\n" \
  "v_smfmac_f32_16x16x64_bf16 v[24:27], v[36:39], v[48:55], v0\n" \
  "s_waitcnt lgkmcnt(4)\n" \
  "v_smfmac_f32_16x16x64_bf16 v[24:27], v[100:103], v[56:63], v0\n" \
  "v_smfmac_f32_16x16x64_bf16 v[16:19], v[80:83], v[208:215], v0\n" \
  "v_smfmac_f32_16x16x64_bf16 v[16:19], v[84:87], v[216:223], v0\n" \
  "v_cvt_pk_bf16_f32 v110, v24, v25\n" \
  "v_smfmac_f32_16x16x64_bf16 v[16:19], v[88:91], v[224:231], v0\n" \
  "v_cvt_pk_bf16_f32 v111, v26, v27\n" \
  "v_smfmac_f32_16x16x64_bf16 v[16:19], v[92:95], v[232:239], v0\n" \
  "buffer_store_dwordx2 v[110:111], v13, s[36:39], s41 offen sc1\n" \
  "s_waitcnt lgkmcnt(2)\n" \
  "v_smfmac_f32_16x16x64_bf16 v[16:19], v[36:39], v[64:71], v0\n" \
  "s_waitcnt lgkmcnt(0)\n" \
  "v_smfmac_f32_16x16x64_bf16 v[16:19], v[100:103], v[72:79], v0\n" \
  "s_nop 3\n" \
  "v_mov_b32 v31, v31\n" \
  "s_nop 2\n" \
  "v_cvt_pk_bf16_f32 v108, v16, v17\n" \
  "v_cvt_pk_bf16_f32 v109, v18, v19\n" \
  "buffer_store_dwordx2 v[108:109], v13, s[36:39], s42 offen sc1\n" \
  "buffer_store_dword v34, v1, s[36:39], 0 offen sc1\n" \
  "s_mov_b32 s67, s66\n" \
  "s_cmp_eq_u32 s64, 64\n" \
  "s_cbranch_scc1 LDONE_%=\n" \
  "s_waitcnt vmcnt(0)\n" \
  "s_branch LOOP23_%=\n" \
  "LMF23_%=:\n" \
  "v_mbcnt_lo_u32_b32 v94, -1, 0\n" \
  "v_mbcnt_hi_u32_b32 v94, -1, v94\n" \
  "v_lshlrev_b32 v94, 4, v94\n" \
  "v_mov_b32 v92, 0\n" \
  "ds_read_b128 v[72:75], v94 offset:0\n" \
  "ds_read_b128 v[76:79], v94 offset:1024\n" \
  "ds_read_b128 v[80:83], v94 offset:2048\n" \
  "ds_read_b128 v[84:87], v94 offset:3072\n" \
  "ds_read_b128 v[88:91], v94 offset:4096\n" \
  "s_waitcnt lgkmcnt(0)\n" \
  "v_max3_u32 v92, v92, v72, v73\n" \
  "v_max3_u32 v92, v92, v74, v75\n" \
  "v_max3_u32 v92, v92, v76, v77\n" \
  "v_max3_u32 v92, v92, v78, v79\n" \
  "v_max3_u32 v92, v92, v80, v81\n" \
  "v_max3_u32 v92, v92, v82, v83\n" \
  "v_max3_u32 v92, v92, v84, v85\n" \
  "v_max3_u32 v92, v92, v86, v87\n" \
  "v_max3_u32 v92, v92, v88, v89\n" \
  "v_max3_u32 v92, v92, v90, v91\n" \
  "ds_read_b128 v[72:75], v94 offset:5120\n" \
  "ds_read_b128 v[76:79], v94 offset:6144\n" \
  "ds_read_b128 v[80:83], v94 offset:7168\n" \
  "ds_read_b128 v[84:87], v94 offset:8192\n" \
  "ds_read_b128 v[88:91], v94 offset:9216\n" \
  "s_waitcnt lgkmcnt(0)\n" \
  "v_max3_u32 v92, v92, v72, v73\n" \
  "v_max3_u32 v92, v92, v74, v75\n" \
  "v_max3_u32 v92, v92, v76, v77\n" \
  "v_max3_u32 v92, v92, v78, v79\n" \
  "v_max3_u32 v92, v92, v80, v81\n" \
  "v_max3_u32 v92, v92, v82, v83\n" \
  "v_max3_u32 v92, v92, v84, v85\n" \
  "v_max3_u32 v92, v92, v86, v87\n" \
  "v_max3_u32 v92, v92, v88, v89\n" \
  "v_max3_u32 v92, v92, v90, v91\n" \
  "v_cmp_eq_u32 vcc, -1, v92\n" \
  "s_cbranch_vccz LMOK23_%=\n" \
  "s_cmp_gt_u32 s58, 4000\n" \
  "s_cbranch_scc1 LMOK23_%=\n" \
  "s_add_u32 s58, s58, 1\n" \
  "s_sub_u32 s64, s64, 1\n" \
  "s_mov_b32 s66, s65\n" \
  "s_waitcnt vmcnt(0) lgkmcnt(0)\n" \
  "s_barrier\n" \
  "s_branch LOOP23_%=\n" \
  "LDONE_%=:\n" \
  "s_cmp_eq_u32 s73, 1\n" \
  "s_cbranch_scc1 LFIN_%=\n" \
  "s_mov_b32 s73, 1\n" \
  "s_mov_b32 s38, s55\n" \
  "s_mov_b32 s64, 0\n" \
  "s_mov_b32 s60, 0\n" \
  "s_memrealtime s[74:75]\n" \
  "s_memtime s[76:77]\n" \
  "v_readlane_b32 s50, v224, 0\n" \
  "v_readlane_b32 s51, v224, 1\n" \
  "v_readlane_b32 s52, v224, 2\n" \
  "v_readlane_b32 s58, v224, 3\n" \
  "v_readlane_b32 s40, v224, 4\n" \
  "v_readlane_b32 s41, v224, 5\n" \
  "v_readlane_b32 s42, v224, 6\n" \
  "v_readlane_b32 s43, v224, 7\n" \
  "v_readlane_b32 s44, v224, 8\n" \
  "v_readlane_b32 s45, v224, 9\n" \
  "v_readlane_b32 s46, v224, 10\n" \
  "v_readlane_b32 s47, v224, 11\n" \
  "v_mov_b32 v40, v192\n" \
  "v_mov_b32 v41, v193\n" \
  "v_mov_b32 v42, v194\n" \
  "v_mov_b32 v43, v195\n" \
  "v_mov_b32 v44, v196\n" \
  "v_mov_b32 v45, v197\n" \
  "v_mov_b32 v46, v198\n" \
  "v_mov_b32 v47, v199\n" \
  "v_mov_b32 v48, v200\n" \
  "v_mov_b32 v49, v201\n" \
  "v_mov_b32 v50, v202\n" \
  "v_mov_b32 v51, v203\n" \
  "v_mov_b32 v52, v204\n" \
  "v_mov_b32 v53, v205\n" \
  "v_mov_b32 v54, v206\n" \
  "v_mov_b32 v55, v207\n" \
  "v_mov_b32 v56, v208\n" \
  "v_mov_b32 v57, v209\n" \
  "v_mov_b32 v58, v210\n" \
  "v_mov_b32 v59, v211\n" \
  "v_mov_b32 v60, v212\n" \
  "v_mov_b32 v61, v213\n" \
  "v_mov_b32 v62, v214\n" \
  "v_mov_b32 v63, v215\n" \
  "v_mov_b32 v64, v216\n" \
  "v_mov_b32 v65, v217\n" \
  "v_mov_b32 v66, v218\n" \
  "v_mov_b32 v67, v219\n" \
  "v_mov_b32 v68, v220\n" \
  "v_mov_b32 v69, v221\n" \
  "v_mov_b32 v70, v222\n" \
  "v_mov_b32 v71, v223\n" \
  "v_mov_b32 v25, v225\n" \
  "v_mov_b32 v26, v226\n" \
  "v_mov_b32 v32, 1\n" \
  "s_waitcnt lgkmcnt(0)\n" \
  "s_mov_b32 s55, s74\n" \
  "s_mov_b32 s62, s74\n" \
  "s_mov_b32 s86, s76\n" \
  "s_cmp_ge_u32 s48, 8\n" \
  "s_cbranch_scc1 LSV_%=\n" \
  "v_mad_u32_u24 v112, v62, 0, v60\n" \
  "v_add_u32 v128, 90112, v112\n" \
  "v_mad_u32_u24 v113, v62, 2, v60\n" \
  "v_add_u32 v129, 90112, v113\n" \
  "v_mad_u32_u24 v114, v62, 16, v60\n" \
  "v_add_u32 v130, 90112, v114\n" \
  "v_mad_u32_u24 v115, v62, 18, v60\n" \
  "v_add_u32 v131, 90112, v115\n" \
  "v_mad_u32_u24 v116, v62, 32, v60\n" \
  "v_add_u32 v132, 90112, v116\n" \
  "v_mad_u32_u24 v117, v62, 34, v60\n" \
  "v_add_u32 v133, 90112, v117\n" \
  "v_mad_u32_u24 v118, v62, 48, v60\n" \
  "v_add_u32 v134, 90112, v118\n" \
  "v_mad_u32_u24 v119, v62, 50, v60\n" \
  "v_add_u32 v135, 90112, v119\n" \
  "v_mad_u32_u24 v120, v63, 0, v61\n" \
  "v_add_u32 v136, 90112, v120\n" \
  "v_mad_u32_u24 v121, v63, 2, v61\n" \
  "v_add_u32 v137, 90112, v121\n" \
  "v_mad_u32_u24 v122, v63, 16, v61\n" \
  "v_add_u32 v138, 90112, v122\n" \
  "v_mad_u32_u24 v123, v63, 18, v61\n" \
  "v_add_u32 v139, 90112, v123\n" \
  "v_mad_u32_u24 v124, v63, 32, v61\n" \
  "v_add_u32 v140, 90112, v124\n" \
  "v_mad_u32_u24 v125, v63, 34, v61\n" \
  "v_add_u32 v141, 90112, v125\n" \
  "v_mad_u32_u24 v126, v63, 48, v61\n" \
  "v_add_u32 v142, 90112, v126\n" \
  "v_mad_u32_u24 v127, v63, 50, v61\n" \
  "v_add_u32 v143, 90112, v127\n" \
  "s_and_b32 s71, s48, 3\n" \
  "s_cmp_eq_u32 s71, 2\n" \
  "s_cbranch_scc0 LGQ_%=\n" \
  "s_mov_b32 s71, 0\n" \
  "s_add_u32 m0, s56, 57344\n" \
  "s_nop 0\n" \
  "buffer_load_dwordx4 v40, s[40:43], s71 offen lds\n" \
  "s_add_u32 m0, s56, 58368\n" \
  "s_nop 0\n" \
  "buffer_load_dwordx4 v41, s[40:43], s71 offen lds\n" \
  "s_add_u32 m0, s56, 59392\n" \
  "s_nop 0\n" \
  "buffer_load_dwordx4 v42, s[40:43], s71 offen lds\n" \
  "s_add_u32 m0, s56, 60416\n" \
  "s_nop 0\n" \
  "buffer_load_dwordx4 v43, s[40:43], s71 offen lds\n" \
  "s_add_u32 m0, s56, 61440\n" \
  "s_nop 0\n" \
  "buffer_load_dwordx4 v44, s[40:43], s71 offen lds\n" \
  "s_add_u32 m0, s56, 62464\n" \
  "s_nop 0\n" \
  "buffer_load_dwordx4 v45, s[40:43], s71 offen lds\n" \
  "s_add_u32 m0, s56, 63488\n" \
  "s_nop 0\n" \
  "buffer_load_dwordx4 v46, s[40:43], s71 offen lds\n" \
  "s_add_u32 m0, s56, 64512\n" \
  "s_nop 0\n" \
  "buffer_load_dwordx4 v47, s[40:43], s71 offen lds\n" \
  "s_mov_b32 s71, 786432\n" \
  "s_add_u32 m0, s56, 90112\n" \
  "s_nop 0\n" \
  "buffer_load_dwordx4 v40, s[40:43], s71 offen lds\n" \
  "s_add_u32 m0, s56, 91136\n" \
  "s_nop 0\n" \
  "buffer_load_dwordx4 v41, s[40:43], s71 offen lds\n" \
  "s_add_u32 m0, s56, 92160\n" \
  "s_nop 0\n" \
  "buffer_load_dwordx4 v42, s[40:43], s71 offen lds\n" \
  "s_add_u32 m0, s56, 93184\n" \
  "s_nop 0\n" \
  "buffer_load_dwordx4 v43, s[40:43], s71 offen lds\n" \
  "s_add_u32 m0, s56, 94208\n" \
  "s_nop 0\n" \
  "buffer_load_dwordx4 v44, s[40:43], s71 offen lds\n" \
  "s_add_u32 m0, s56, 95232\n" \
  "s_nop 0\n" \
  "buffer_load_dwordx4 v45, s[40:43], s71 offen lds\n" \
  "s_add_u32 m0, s56, 96256\n" \
  "s_nop 0\n" \
  "buffer_load_dwordx4 v46, s[40:43], s71 offen lds\n" \
  "s_add_u32 m0, s56, 97280\n" \
  "s_nop 0\n" \
  "buffer_load_dwordx4 v47, s[40:43], s71 offen lds\n" \
  "s_mov_b32 s71, 1572864\n" \
  "s_add_u32 m0, s56, 122880\n" \
  "s_nop 0\n" \
  "buffer_load_dwordx4 v40, s[40:43], s71 offen lds\n" \
  "s_add_u32 m0, s56, 123904\n" \
  "s_nop 0\n" \
  "buffer_load_dwordx4 v41, s[40:43], s71 offen lds\n" \
  "s_add_u32 m0, s56, 124928\n" \
  "s_nop 0\n" \
  "buffer_load_dwordx4 v42, s[40:43], s71 offen lds\n" \
  "s_add_u32 m0, s56, 125952\n" \
  "s_nop 0\n" \
  "buffer_load_dwordx4 v43, s[40:43], s71 offen lds\n" \
  "s_add_u32 m0, s56, 126976\n" \
  "s_nop 0\n" \
  "buffer_load_dwordx4 v44, s[40:43], s71 offen lds\n" \
  "s_add_u32 m0, s56, 128000\n" \
  "s_nop 0\n" \
  "buffer_load_dwordx4 v45, s[40:43], s71 offen lds\n" \
  "s_add_u32 m0, s56, 129024\n" \
  "s_nop 0\n" \
  "buffer_load_dwordx4 v46, s[40:43], s71 offen lds\n" \
  "s_add_u32 m0, s56, 130048\n" \
  "s_nop 0\n" \
  "buffer_load_dwordx4 v47, s[40:43], s71 offen lds\n" \
  "LGQ_%=:\n" \
  "s_waitcnt vmcnt(16) lgkmcnt(0)\n" \
  "s_barrier\n" \
  "s_mov_b32 s71, 2359296\n" \
  "s_add_u32 m0, s56, 0\n" \
  "s_nop 0\n" \
  "buffer_load_dwordx4 v40, s[40:43], s71 offen lds\n" \
  "s_add_u32 m0, s56, 1024\n" \
  "s_nop 0\n" \
  "buffer_load_dwordx4 v41, s[40:43], s71 offen lds\n" \
  "s_add_u32 m0, s56, 2048\n" \
  "s_nop 0\n" \
  "buffer_load_dwordx4 v42, s[40:43], s71 offen lds\n" \
  "s_add_u32 m0, s56, 3072\n" \
  "s_nop 0\n" \
  "buffer_load_dwordx4 v43, s[40:43], s71 offen lds\n" \
  "s_add_u32 m0, s56, 4096\n" \
  "s_nop 0\n" \
  "buffer_load_dwordx4 v44, s[40:43], s71 offen lds\n" \
  "s_add_u32 m0, s56, 5120\n" \
  "s_nop 0\n" \
  "buffer_load_dwordx4 v45, s[40:43], s71 offen lds\n" \
  "s_add_u32 m0, s56, 6144\n" \
  "s_nop 0\n" \
  "buffer_load_dwordx4 v46, s[40:43], s71 offen lds\n" \
  "s_add_u32 m0, s56, 7168\n" \
  "s_nop 0\n" \
  "buffer_load_dwordx4 v47, s[40:43], s71 offen lds\n" \
  "ds_read_b64_tr_b16 a[0:1], v112 offset:57344\n" \
  "ds_read_b64_tr_b16 a[2:3], v113 offset:57344\n" \
  "ds_read_b64_tr_b16 a[4:5], v114 offset:57344\n" \
  "ds_read_b64_tr_b16 a[6:7], v115 offset:57344\n" \
  "ds_read_b64_tr_b16 a[8:9], v116 offset:57344\n" \
  "ds_read_b64_tr_b16 a[10:11], v117 offset:57344\n" \
  "ds_read_b64_tr_b16 a[12:13], v118 offset:57344\n" \
  "ds_read_b64_tr_b16 a[14:15], v119 offset:57344\n" \
  "s_waitcnt lgkmcnt(0)\n" \
  "ds_read_b64_tr_b16 a[160:161], v120 offset:57344\n" \
  "ds_read_b64_tr_b16 a[162:163], v121 offset:57344\n" \
  "ds_read_b64_tr_b16 a[164:165], v122 offset:57344\n" \
  "ds_read_b64_tr_b16 a[166:167], v123 offset:57344\n" \
  "ds_read_b64_tr_b16 a[168:169], v124 offset:57344\n" \
  "ds_read_b64_tr_b16 a[170:171], v125 offset:57344\n" \
  "ds_read_b64_tr_b16 a[172:173], v126 offset:57344\n" \
  "ds_read_b64_tr_b16 a[174:175], v127 offset:57344\n" \
  "s_waitcnt lgkmcnt(0)\n" \
  "s_waitcnt vmcnt(16) lgkmcnt(0)\n" \
  "s_barrier\n" \
  "s_mov_b32 s71, 3145728\n" \
  "s_add_u32 m0, s56, 57344\n" \
  "s_nop 0\n" \
  "buffer_load_dwordx4 v40, s[40:43], s71 offen lds\n" \
  "s_add_u32 m0, s56, 58368\n" \
  "s_nop 0\n" \
  "buffer_load_dwordx4 v41, s[40:43], s71 offen lds\n" \
  "s_add_u32 m0, s56, 59392\n" \
  "s_nop 0\n" \
  "buffer_load_dwordx4 v42, s[40:43], s71 offen lds\n" \
  "s_add_u32 m0, s56, 60416\n" \
  "s_nop 0\n" \
  "buffer_load_dwordx4 v43, s[40:43], s71 offen lds\n" \
  "s_add_u32 m0, s56, 61440\n" \
  "s_nop 0\n" \
  "buffer_load_dwordx4 v44, s[40:43], s71 offen lds\n" \
  "s_add_u32 m0, s56, 62464\n" \
  "s_nop 0\n" \
  "buffer_load_dwordx4 v45, s[40:43], s71 offen lds\n" \
  "s_add_u32 m0, s56, 63488\n" \
  "s_nop 0\n" \
  "buffer_load_dwordx4 v46, s[40:43], s71 offen lds\n" \
  "s_add_u32 m0, s56, 64512\n" \
  "s_nop 0\n" \
  "buffer_load_dwordx4 v47, s[40:43], s71 offen lds\n" \
  "ds_read_b64_tr_b16 a[16:17], v128\n" \
  "ds_read_b64_tr_b16 a[18:19], v129\n" \
  "ds_read_b64_tr_b16 a[20:21], v130\n" \
  "ds_read_b64_tr_b16 a[22:23], v131\n" \
  "ds_read_b64_tr_b16 a[24:25], v132\n" \
  "ds_read_b64_tr_b16 a[26:27], v133\n" \
  "ds_read_b64_tr_b16 a[28:29], v134\n" \
  "ds_read_b64_tr_b16 a[30:31], v135\n" \
  "s_waitcnt lgkmcnt(0)\n" \
  "ds_read_b64_tr_b16 a[176:177], v136\n" \
  "ds_read_b64_tr_b16 a[178:179], v137\n" \
  "ds_read_b64_tr_b16 a[180:181], v138\n" \
  "ds_read_b64_tr_b16 a[182:183], v139\n" \
  "ds_read_b64_tr_b16 a[184:185], v140\n" \
  "ds_read_b64_tr_b16 a[186:187], v141\n" \
  "ds_read_b64_tr_b16 a[188:189], v142\n" \
  "ds_read_b64_tr_b16 a[190:191], v143\n" \
  "s_waitcnt lgkmcnt(0)\n" \
  "s_waitcnt vmcnt(16) lgkmcnt(0)\n" \
  "s_barrier\n" \
  "s_mov_b32 s71, 3932160\n" \
  "s_add_u32 m0, s56, 90112\n" \
  "s_nop 0\n" \
  "buffer_load_dwordx4 v40, s[40:43], s71 offen lds\n" \
  "s_add_u32 m0, s56, 91136\n" \
  "s_nop 0\n" \
  "buffer_load_dwordx4 v41, s[40:43], s71 offen lds\n" \
  "s_add_u32 m0, s56, 92160\n" \
  "s_nop 0\n" \
  "buffer_load_dwordx4 v42, s[40:43], s71 offen lds\n" \
  "s_add_u32 m0, s56, 93184\n" \
  "s_nop 0\n" \
  "buffer_load_dwordx4 v43, s[40:43], s71 offen lds\n" \
  "s_add_u32 m0, s56, 94208\n" \
  "s_nop 0\n" \
  "buffer_load_dwordx4 v44, s[40:43], s71 offen lds\n" \
  "s_add_u32 m0, s56, 95232\n" \
  "s_nop 0\n" \
  "buffer_load_dwordx4 v45, s[40:43], s71 offen lds\n" \
  "s_add_u32 m0, s56, 96256\n" \
  "s_nop 0\n" \
  "buffer_load_dwordx4 v46, s[40:43], s71 offen lds\n" \
  "s_add_u32 m0, s56, 97280\n" \
  "s_nop 0\n" \
  "buffer_load_dwordx4 v47, s[40:43], s71 offen lds\n" \
  "ds_read_b64_tr_b16 a[32:33], v128 offset:32768\n" \
  "ds_read_b64_tr_b16 a[34:35], v129 offset:32768\n" \
  "ds_read_b64_tr_b16 a[36:37], v130 offset:32768\n" \
  "ds_read_b64_tr_b16 a[38:39], v131 offset:32768\n" \
  "ds_read_b64_tr_b16 a[40:41], v132 offset:32768\n" \
  "ds_read_b64_tr_b16 a[42:43], v133 offset:32768\n" \
  "ds_read_b64_tr_b16 a[44:45], v134 offset:32768\n" \
  "ds_read_b64_tr_b16 a[46:47], v135 offset:32768\n" \
  "s_waitcnt lgkmcnt(0)\n" \
  "ds_read_b64_tr_b16 a[192:193], v136 offset:32768\n" \
  "ds_read_b64_tr_b16 a[194:195], v137 offset:32768\n" \
  "ds_read_b64_tr_b16 a[196:197], v138 offset:32768\n" \
  "ds_read_b64_tr_b16 a[198:199], v139 offset:32768\n" \
  "ds_read_b64_tr_b16 a[200:201], v140 offset:32768\n" \
  "ds_read_b64_tr_b16 a[202:203], v141 offset:32768\n" \
  "ds_read_b64_tr_b16 a[204:205], v142 offset:32768\n" \
  "ds_read_b64_tr_b16 a[206:207], v143 offset:32768\n" \
  "s_waitcnt lgkmcnt(0)\n" \
  "s_waitcnt vmcnt(16) lgkmcnt(0)\n" \
  "s_barrier\n" \
  "s_mov_b32 s71, 4718592\n" \
  "s_add_u32 m0, s56, 122880\n" \
  "s_nop 0\n" \
  "buffer_load_dwordx4 v40, s[40:43], s71 offen lds\n" \
  "s_add_u32 m0, s56, 123904\n" \
  "s_nop 0\n" \
  "buffer_load_dwordx4 v41, s[40:43], s71 offen lds\n" \
  "s_add_u32 m0, s56, 124928\n" \
  "s_nop 0\n" \
  "buffer_load_dwordx4 v42, s[40:43], s71 offen lds\n" \
  "s_add_u32 m0, s56, 125952\n" \
  "s_nop 0\n" \
  "buffer_load_dwordx4 v43, s[40:43], s71 offen lds\n" \
  "s_add_u32 m0, s56, 126976\n" \
  "s_nop 0\n" \
  "buffer_load_dwordx4 v44, s[40:43], s71 offen lds\n" \
  "s_add_u32 m0, s56, 128000\n" \
  "s_nop 0\n" \
  "buffer_load_dwordx4 v45, s[40:43], s71 offen lds\n" \
  "s_add_u32 m0, s56, 129024\n" \
  "s_nop 0\n" \
  "buffer_load_dwordx4 v46, s[40:43], s71 offen lds\n" \
  "s_add_u32 m0, s56, 130048\n" \
  "s_nop 0\n" \
  "buffer_load_dwordx4 v47, s[40:43], s71 offen lds\n" \
  "ds_read_b64_tr_b16 a[48:49], v112\n" \
  "ds_read_b64_tr_b16 a[50:51], v113\n" \
  "ds_read_b64_tr_b16 a[52:53], v114\n" \
  "ds_read_b64_tr_b16 a[54:55], v115\n" \
  "ds_read_b64_tr_b16 a[56:57], v116\n" \
  "ds_read_b64_tr_b16 a[58:59], v117\n" \
  "ds_read_b64_tr_b16 a[60:61], v118\n" \
  "ds_read_b64_tr_b16 a[62:63], v119\n" \
  "s_waitcnt lgkmcnt(0)\n" \
  "ds_read_b64_tr_b16 a[208:209], v120\n" \
  "ds_read_b64_tr_b16 a[210:211], v121\n" \
  "ds_read_b64_tr_b16 a[212:213], v122\n" \
  "ds_read_b64_tr_b16 a[214:215], v123\n" \
  "ds_read_b64_tr_b16 a[216:217], v124\n" \
  "ds_read_b64_tr_b16 a[218:219], v125\n" \
  "ds_read_b64_tr_b16 a[220:221], v126\n" \
  "ds_read_b64_tr_b16 a[222:223], v127\n" \
  "s_waitcnt lgkmcnt(0)\n" \
  "s_waitcnt vmcnt(16) lgkmcnt(0)\n" \
  "s_barrier\n" \
  "s_mov_b32 s71, 5505024\n" \
  "s_add_u32 m0, s56, 0\n" \
  "s_nop 0\n" \
  "buffer_load_dwordx4 v40, s[40:43], s71 offen lds\n" \
  "s_add_u32 m0, s56, 1024\n" \
  "s_nop 0\n" \
  "buffer_load_dwordx4 v41, s[40:43], s71 offen lds\n" \
  "s_add_u32 m0, s56, 2048\n" \
  "s_nop 0\n" \
  "buffer_load_dwordx4 v42, s[40:43], s71 offen lds\n" \
  "s_add_u32 m0, s56, 3072\n" \
  "s_nop 0\n" \
  "buffer_load_dwordx4 v43, s[40:43], s71 offen lds\n" \
  "s_add_u32 m0, s56, 4096\n" \
  "s_nop 0\n" \
  "buffer_load_dwordx4 v44, s[40:43], s71 offen lds\n" \
  "s_add_u32 m0, s56, 5120\n" \
  "s_nop 0\n" \
  "buffer_load_dwordx4 v45, s[40:43], s71 offen lds\n" \
  "s_add_u32 m0, s56, 6144\n" \
  "s_nop 0\n" \
  "buffer_load_dwordx4 v46, s[40:43], s71 offen lds\n" \
  "s_add_u32 m0, s56, 7168\n" \
  "s_nop 0\n" \
  "buffer_load_dwordx4 v47, s[40:43], s71 offen lds\n" \
  "ds_read_b64_tr_b16 a[64:65], v112 offset:57344\n" \
  "ds_read_b64_tr_b16 a[66:67], v113 offset:57344\n" \
  "ds_read_b64_tr_b16 a[68:69], v114 offset:57344\n" \
  "ds_read_b64_tr_b16 a[70:71], v115 offset:57344\n" \
  "ds_read_b64_tr_b16 a[72:73], v116 offset:57344\n" \
  "ds_read_b64_tr_b16 a[74:75], v117 offset:57344\n" \
  "ds_read_b64_tr_b16 a[76:77], v118 offset:57344\n" \
  "ds_read_b64_tr_b16 a[78:79], v119 offset:57344\n" \
  "s_waitcnt lgkmcnt(0)\n" \
  "ds_read_b64_tr_b16 a[224:225], v120 offset:57344\n" \
  "ds_read_b64_tr_b16 a[226:227], v121 offset:57344\n" \
  "ds_read_b64_tr_b16 a[228:229], v122 offset:57344\n" \
  "ds_read_b64_tr_b16 a[230:231], v123 offset:57344\n" \
  "ds_read_b64_tr_b16 a[232:233], v124 offset:57344\n" \
  "ds_read_b64_tr_b16 a[234:235], v125 offset:57344\n" \
  "ds_read_b64_tr_b16 a[236:237], v126 offset:57344\n" \
  "ds_read_b64_tr_b16 a[238:239], v127 offset:57344\n" \
  "s_waitcnt lgkmcnt(0)\n" \
  "s_waitcnt vmcnt(16) lgkmcnt(0)\n" \
  "s_barrier\n" \
  "s_mov_b32 s71, 6291456\n" \
  "s_add_u32 m0, s56, 57344\n" \
  "s_nop 0\n" \
  "buffer_load_dwordx4 v40, s[40:43], s71 offen lds\n" \
  "s_add_u32 m0, s56, 58368\n" \
  "s_nop 0\n" \
  "buffer_load_dwordx4 v41, s[40:43], s71 offen lds\n" \
  "s_add_u32 m0, s56, 59392\n" \
  "s_nop 0\n" \
  "buffer_load_dwordx4 v42, s[40:43], s71 offen lds\n" \
  "s_add_u32 m0, s56, 60416\n" \
  "s_nop 0\n" \
  "buffer_load_dwordx4 v43, s[40:43], s71 offen lds\n" \
  "s_add_u32 m0, s56, 61440\n" \
  "s_nop 0\n" \
  "buffer_load_dwordx4 v44, s[40:43], s71 offen lds\n" \
  "s_add_u32 m0, s56, 62464\n" \
  "s_nop 0\n" \
  "buffer_load_dwordx4 v45, s[40:43], s71 offen lds\n" \
  "s_add_u32 m0, s56, 63488\n" \
  "s_nop 0\n" \
  "buffer_load_dwordx4 v46, s[40:43], s71 offen lds\n" \
  "s_add_u32 m0, s56, 64512\n" \
  "s_nop 0\n" \
  "buffer_load_dwordx4 v47, s[40:43], s71 offen lds\n" \
  "ds_read_b64_tr_b16 a[80:81], v128\n" \
  "ds_read_b64_tr_b16 a[82:83], v129\n" \
  "ds_read_b64_tr_b16 a[84:85], v130\n" \
  "ds_read_b64_tr_b16 a[86:87], v131\n" \
  "ds_read_b64_tr_b16 a[88:89], v132\n" \
  "ds_read_b64_tr_b16 a[90:91], v133\n" \
  "ds_read_b64_tr_b16 a[92:93], v134\n" \
  "ds_read_b64_tr_b16 a[94:95], v135\n" \
  "s_waitcnt lgkmcnt(0)\n" \
  "ds_read_b64_tr_b16 a[240:241], v136\n" \
  "ds_read_b64_tr_b16 a[242:243], v137\n" \
  "ds_read_b64_tr_b16 a[244:245], v138\n" \
  "ds_read_b64_tr_b16 a[246:247], v139\n" \
  "ds_read_b64_tr_b16 a[248:249], v140\n" \
  "ds_read_b64_tr_b16 a[250:251], v141\n" \
  "ds_read_b64_tr_b16 a[252:253], v142\n" \
  "ds_read_b64_tr_b16 a[254:255], v143\n" \
  "s_waitcnt lgkmcnt(0)\n" \
  "s_waitcnt vmcnt(16) lgkmcnt(0)\n" \
  "s_barrier\n" \
  "s_mov_b32 s71, 7077888\n" \
  "s_add_u32 m0, s56, 90112\n" \
  "s_nop 0\n" \
  "buffer_load_dwordx4 v40, s[40:43], s71 offen lds\n" \
  "s_add_u32 m0, s56, 91136\n" \
  "s_nop 0\n" \
  "buffer_load_dwordx4 v41, s[40:43], s71 offen lds\n" \
  "s_add_u32 m0, s56, 92160\n" \
  "s_nop 0\n" \
  "buffer_load_dwordx4 v42, s[40:43], s71 offen lds\n" \
  "s_add_u32 m0, s56, 93184\n" \
  "s_nop 0\n" \
  "buffer_load_dwordx4 v43, s[40:43], s71 offen lds\n" \
  "s_add_u32 m0, s56, 94208\n" \
  "s_nop 0\n" \
  "buffer_load_dwordx4 v44, s[40:43], s71 offen lds\n" \
  "s_add_u32 m0, s56, 95232\n" \
  "s_nop 0\n" \
  "buffer_load_dwordx4 v45, s[40:43], s71 offen lds\n" \
  "s_add_u32 m0, s56, 96256\n" \
  "s_nop 0\n" \
  "buffer_load_dwordx4 v46, s[40:43], s71 offen lds\n" \
  "s_add_u32 m0, s56, 97280\n" \
  "s_nop 0\n" \
  "buffer_load_dwordx4 v47, s[40:43], s71 offen lds\n" \
  "ds_read_b64_tr_b16 a[96:97], v128 offset:32768\n" \
  "ds_read_b64_tr_b16 a[98:99], v129 offset:32768\n" \
  "ds_read_b64_tr_b16 a[100:101], v130 offset:32768\n" \
  "ds_read_b64_tr_b16 a[102:103], v131 offset:32768\n" \
  "ds_read_b64_tr_b16 a[104:105], v132 offset:32768\n" \
  "ds_read_b64_tr_b16 a[106:107], v133 offset:32768\n" \
  "ds_read_b64_tr_b16 a[108:109], v134 offset:32768\n" \
  "ds_read_b64_tr_b16 a[110:111], v135 offset:32768\n" \
  "s_waitcnt lgkmcnt(0)\n" \
  "ds_read_b64_tr_b16 v[192:193], v136 offset:32768\n" \
  "ds_read_b64_tr_b16 v[194:195], v137 offset:32768\n" \
  "ds_read_b64_tr_b16 v[196:197], v138 offset:32768\n" \
  "ds_read_b64_tr_b16 v[198:199], v139 offset:32768\n" \
  "ds_read_b64_tr_b16 v[200:201], v140 offset:32768\n" \
  "ds_read_b64_tr_b16 v[202:203], v141 offset:32768\n" \
  "ds_read_b64_tr_b16 v[204:205], v142 offset:32768\n" \
  "ds_read_b64_tr_b16 v[206:207], v143 offset:32768\n" \
  "s_waitcnt lgkmcnt(0)\n" \
  "s_waitcnt vmcnt(16) lgkmcnt(0)\n" \
  "s_barrier\n" \
  "ds_read_b64_tr_b16 a[112:113], v112\n" \
  "ds_read_b64_tr_b16 a[114:115], v113\n" \
  "ds_read_b64_tr_b16 a[116:117], v114\n" \
  "ds_read_b64_tr_b16 a[118:119], v115\n" \
  "ds_read_b64_tr_b16 a[120:121], v116\n" \
  "ds_read_b64_tr_b16 a[122:123], v117\n" \
  "ds_read_b64_tr_b16 a[124:125], v118\n" \
  "ds_read_b64_tr_b16 a[126:127], v119\n" \
  "s_waitcnt lgkmcnt(0)\n" \
  "ds_read_b64_tr_b16 v[208:209], v120\n" \
  "ds_read_b64_tr_b16 v[210:211], v121\n" \
  "ds_read_b64_tr_b16 v[212:213], v122\n" \
  "ds_read_b64_tr_b16 v[214:215], v123\n" \
  "ds_read_b64_tr_b16 v[216:217], v124\n" \
  "ds_read_b64_tr_b16 v[218:219], v125\n" \
  "ds_read_b64_tr_b16 v[220:221], v126\n" \
  "ds_read_b64_tr_b16 v[222:223], v127\n" \
  "s_waitcnt lgkmcnt(0)\n" \
  "s_waitcnt vmcnt(8) lgkmcnt(0)\n" \
  "s_barrier\n" \
  "ds_read_b64_tr_b16 a[128:129], v112 offset:57344\n" \
  "ds_read_b64_tr_b16 a[130:131], v113 offset:57344\n" \
  "ds_read_b64_tr_b16 a[132:133], v114 offset:57344\n" \
  "ds_read_b64_tr_b16 a[134:135], v115 offset:57344\n" \
  "ds_read_b64_tr_b16 a[136:137], v116 offset:57344\n" \
  "ds_read_b64_tr_b16 a[138:139], v117 offset:57344\n" \
  "ds_read_b64_tr_b16 a[140:141], v118 offset:57344\n" \
  "ds_read_b64_tr_b16 a[142:143], v119 offset:57344\n" \
  "s_waitcnt lgkmcnt(0)\n" \
  "ds_read_b64_tr_b16 v[224:225], v120 offset:57344\n" \
  "ds_read_b64_tr_b16 v[226:227], v121 offset:57344\n" \
  "ds_read_b64_tr_b16 v[228:229], v122 offset:57344\n" \
  "ds_read_b64_tr_b16 v[230:231], v123 offset:57344\n" \
  "ds_read_b64_tr_b16 v[232:233], v124 offset:57344\n" \
  "ds_read_b64_tr_b16 v[234:235], v125 offset:57344\n" \
  "ds_read_b64_tr_b16 v[236:237], v126 offset:57344\n" \
  "ds_read_b64_tr_b16 v[238:239], v127 offset:57344\n" \
  "s_waitcnt lgkmcnt(0)\n" \
  "s_waitcnt vmcnt(0) lgkmcnt(0)\n" \
  "s_barrier\n" \
  "ds_read_b64_tr_b16 a[144:145], v128\n" \
  "ds_read_b64_tr_b16 a[146:147], v129\n" \
  "ds_read_b64_tr_b16 a[148:149], v130\n" \
  "ds_read_b64_tr_b16 a[150:151], v131\n" \
  "ds_read_b64_tr_b16 a[152:153], v132\n" \
  "ds_read_b64_tr_b16 a[154:155], v133\n" \
  "ds_read_b64_tr_b16 a[156:157], v134\n" \
  "ds_read_b64_tr_b16 a[158:159], v135\n" \
  "s_waitcnt lgkmcnt(0)\n" \
  "ds_read_b64_tr_b16 v[240:241], v136\n" \
  "ds_read_b64_tr_b16 v[242:243], v137\n" \
  "ds_read_b64_tr_b16 v[244:245], v138\n" \
  "ds_read_b64_tr_b16 v[246:247], v139\n" \
  "ds_read_b64_tr_b16 v[248:249], v140\n" \
  "ds_read_b64_tr_b16 v[250:251], v141\n" \
  "ds_read_b64_tr_b16 v[252:253], v142\n" \
  "ds_read_b64_tr_b16 v[254:255], v143\n" \
  "s_waitcnt lgkmcnt(0)\n" \
  "s_waitcnt lgkmcnt(0)\n" \
  "s_barrier\n" \
  "LIQ_%=:\n" \
  "buffer_load_dword v92, v69, s[36:39], 0 offen sc1\n" \
  "s_waitcnt vmcnt(0)\n" \
  "v_and_b32 v93, -4, v92\n" \
  "v_cmp_ne_u32 vcc, s58, v93\n" \
  "s_cbranch_vccz LIXQ_%=\n" \
  "s_add_u32 s60, s60, 1\n" \
  "s_cmp_gt_u32 s60, 20000\n" \
  "s_cbranch_scc1 LIXQ_%=\n" \
  "s_sleep 8\n" \
  "s_branch LIQ_%=\n" \
  "LIXQ_%=:\n" \
  "v_and_b32 v92, v70, v92\n" \
  "v_cmp_ne_u32 s[80:81], 0, v92\n" \
  "s_mov_b32 s78, 0x55555555\n" \
  "s_mov_b32 s79, 0x55555555\n" \
  "s_xor_b64 s[82:83], s[80:81], s[78:79]\n" \
  "s_bcnt1_i32_b64 s71, s[82:83]\n" \
  "s_cmp_le_u32 s71, 26\n" \
  "s_cselect_b64 s[80:81], s[78:79], s[80:81]\n" \
  "s_not_b64 s[78:79], s[78:79]\n" \
  "s_cmp_ge_u32 s71, 38\n" \
  "s_cselect_b64 s[80:81], s[78:79], s[80:81]\n" \
  "v_lshrrev_b32 v92, 21, v25\n" \
  "v_lshrrev_b64 v[92:93], v92, s[80:81]\n" \
  "v_and_b32 v92, 1, v92\n" \
  "v_lshlrev_b32 v92, 13, v92\n" \
  "v_xor_b32 v25, v92, v25\n" \
  "v_lshrrev_b32 v92, 21, v26\n" \
  "v_lshrrev_b64 v[92:93], v92, s[80:81]\n" \
  "v_and_b32 v92, 1, v92\n" \
  "v_lshlrev_b32 v92, 13, v92\n" \
  "v_xor_b32 v26, v92, v26\n" \
  "v_lshrrev_b32 v92, 21, v27\n" \
  "v_lshrrev_b64 v[92:93], v92, s[80:81]\n" \
  "v_and_b32 v92, 1, v92\n" \
  "v_lshlrev_b32 v92, 13, v92\n" \
  "v_xor_b32 v27, v92, v27\n" \
  "v_lshrrev_b32 v92, 21, v28\n" \
  "v_lshrrev_b64 v[92:93], v92, s[80:81]\n" \
  "v_and_b32 v92, 1, v92\n" \
  "v_lshlrev_b32 v92, 13, v92\n" \
  "v_xor_b32 v28, v92, v28\n" \
  "v_lshrrev_b32 v92, 21, v29\n" \
  "v_lshrrev_b64 v[92:93], v92, s[80:81]\n" \
  "v_and_b32 v92, 1, v92\n" \
  "v_lshlrev_b32 v92, 13, v92\n" \
  "v_xor_b32 v29, v92, v29\n" \
  "v_lshrrev_b32 v92, 21, v30\n" \
  "v_lshrrev_b64 v[92:93], v92, s[80:81]\n" \
  "v_and_b32 v92, 1, v92\n" \
  "v_lshlrev_b32 v92, 13, v92\n" \
  "v_xor_b32 v30, v92, v30\n" \
  "v_lshrrev_b32 v92, 21, v67\n" \
  "v_lshrrev_b64 v[92:93], v92, s[80:81]\n" \
  "v_and_b32 v92, 1, v92\n" \
  "v_lshlrev_b32 v92, 13, v92\n" \
  "v_xor_b32 v67, v92, v67\n" \
  "v_lshrrev_b32 v92, 21, v71\n" \
  "v_lshrrev_b64 v[92:93], v92, s[80:81]\n" \
  "v_and_b32 v92, 1, v92\n" \
  "v_lshlrev_b32 v92, 13, v92\n" \
  "v_xor_b32 v71, v92, v71\n" \
  "s_lshr_b32 s71, s54, 21\n" \
  "s_lshr_b64 s[80:81], s[80:81], s71\n" \
  "s_and_b32 s71, s80, 1\n" \
  "s_lshl_b32 s71, s71, 13\n" \
  "s_or_b32 s54, s54, s71\n" \
  "s_and_b32 s71, s48, 3\n" \
  "s_cmp_eq_u32 s71, 3\n" \
  "s_cbranch_scc0 LIYQ_%=\n" \
  "v_mov_b32 v1, v71\n" \
  "LIYQ_%=:\n" \
  "buffer_atomic_add v32, v68, s[36:39], 0 offen\n" \
  "v_mov_b32 v104, v64\n" \
  "v_mov_b32 v105, v65\n" \
  "v_mov_b32 v106, v66\n" \
  "v_mov_b32 v107, v67\n" \
  "s_branch LCN_%=\n" \
  "LSV_%=:\n" \
  "v_add_u32 v72, 16384, v60\n" \
  "v_add_u32 v73, 16384, v61\n" \
  "v_add_u32 v74, 16384, v62\n" \
  "v_add_u32 v76, 65536, v60\n" \
  "v_add_u32 v77, 65536, v61\n" \
  "v_add_u32 v78, 65536, v62\n" \
  "v_add_u32 v80, 114688, v60\n" \
  "v_add_u32 v81, 114688, v61\n" \
  "v_add_u32 v82, 114688, v62\n" \
  "s_and_b32 s71, s48, 3\n" \
  "s_cmp_eq_u32 s71, 2\n" \
  "s_cbranch_scc0 LGV_%=\n" \
  "s_mov_b32 s71, 0\n" \
  "s_add_u32 m0, s56, 16384\n" \
  "s_nop 0\n" \
  "buffer_load_dwordx4 v40, s[40:43], s71 offen lds\n" \
  "s_add_u32 m0, s56, 17408\n" \
  "s_nop 0\n" \
  "buffer_load_dwordx4 v41, s[40:43], s71 offen lds\n" \
  "s_add_u32 m0, s56, 18432\n" \
  "s_nop 0\n" \
  "buffer_load_dwordx4 v42, s[40:43], s71 offen lds\n" \
  "s_add_u32 m0, s56, 19456\n" \
  "s_nop 0\n" \
  "buffer_load_dwordx4 v43, s[40:43], s71 offen lds\n" \
  "s_add_u32 m0, s56, 20480\n" \
  "s_nop 0\n" \
  "buffer_load_dwordx4 v44, s[40:43], s71 offen lds\n" \
  "s_add_u32 m0, s56, 21504\n" \
  "s_nop 0\n" \
  "buffer_load_dwordx4 v45, s[40:43], s71 offen lds\n" \
  "s_mov_b32 s71, 786432\n" \
  "s_add_u32 m0, s56, 40960\n" \
  "s_nop 0\n" \
  "buffer_load_dwordx4 v40, s[40:43], s71 offen lds\n" \
  "s_add_u32 m0, s56, 41984\n" \
  "s_nop 0\n" \
  "buffer_load_dwordx4 v41, s[40:43], s71 offen lds\n" \
  "s_add_u32 m0, s56, 43008\n" \
  "s_nop 0\n" \
  "buffer_load_dwordx4 v42, s[40:43], s71 offen lds\n" \
  "s_add_u32 m0, s56, 44032\n" \
  "s_nop 0\n" \
  "buffer_load_dwordx4 v43, s[40:43], s71 offen lds\n" \
  "s_add_u32 m0, s56, 45056\n" \
  "s_nop 0\n" \
  "buffer_load_dwordx4 v44, s[40:43], s71 offen lds\n" \
  "s_add_u32 m0, s56, 46080\n" \
  "s_nop 0\n" \
  "buffer_load_dwordx4 v45, s[40:43], s71 offen lds\n" \
  "s_mov_b32 s71, 1572864\n" \
  "s_add_u32 m0, s56, 65536\n" \
  "s_nop 0\n" \
  "buffer_load_dwordx4 v40, s[40:43], s71 offen lds\n" \
  "s_add_u32 m0, s56, 66560\n" \
  "s_nop 0\n" \
  "buffer_load_dwordx4 v41, s[40:43], s71 offen lds\n" \
  "s_add_u32 m0, s56, 67584\n" \
  "s_nop 0\n" \
  "buffer_load_dwordx4 v42, s[40:43], s71 offen lds\n" \
  "s_add_u32 m0, s56, 68608\n" \
  "s_nop 0\n" \
  "buffer_load_dwordx4 v43, s[40:43], s71 offen lds\n" \
  "s_add_u32 m0, s56, 69632\n" \
  "s_nop 0\n" \
  "buffer_load_dwordx4 v44, s[40:43], s71 offen lds\n" \
  "s_add_u32 m0, s56, 70656\n" \
  "s_nop 0\n" \
  "buffer_load_dwordx4 v45, s[40:43], s71 offen lds\n" \
  "s_mov_b32 s71, 2359296\n" \
  "s_add_u32 m0, s56, 90112\n" \
  "s_nop 0\n" \
  "buffer_load_dwordx4 v40, s[40:43], s71 offen lds\n" \
  "s_add_u32 m0, s56, 91136\n" \
  "s_nop 0\n" \
  "buffer_load_dwordx4 v41, s[40:43], s71 offen lds\n" \
  "s_add_u32 m0, s56, 92160\n" \
  "s_nop 0\n" \
  "buffer_load_dwordx4 v42, s[40:43], s71 offen lds\n" \
  "s_add_u32 m0, s56, 93184\n" \
  "s_nop 0\n" \
  "buffer_load_dwordx4 v43, s[40:43], s71 offen lds\n" \
  "s_add_u32 m0, s56, 94208\n" \
  "s_nop 0\n" \
  "buffer_load_dwordx4 v44, s[40:43], s71 offen lds\n" \
  "s_add_u32 m0, s56, 95232\n" \
  "s_nop 0\n" \
  "buffer_load_dwordx4 v45, s[40:43], s71 offen lds\n" \
  "LGV_%=:\n" \
  "s_waitcnt vmcnt(12) lgkmcnt(0)\n" \
  "s_barrier\n" \
  "s_mov_b32 s71, 3145728\n" \
  "s_add_u32 m0, s56, 114688\n" \
  "s_nop 0\n" \
  "buffer_load_dwordx4 v40, s[40:43], s71 offen lds\n" \
  "s_add_u32 m0, s56, 115712\n" \
  "s_nop 0\n" \
  "buffer_load_dwordx4 v41, s[40:43], s71 offen lds\n" \
  "s_add_u32 m0, s56, 116736\n" \
  "s_nop 0\n" \
  "buffer_load_dwordx4 v42, s[40:43], s71 offen lds\n" \
  "s_add_u32 m0, s56, 117760\n" \
  "s_nop 0\n" \
  "buffer_load_dwordx4 v43, s[40:43], s71 offen lds\n" \
  "s_add_u32 m0, s56, 118784\n" \
  "s_nop 0\n" \
  "buffer_load_dwordx4 v44, s[40:43], s71 offen lds\n" \
  "s_add_u32 m0, s56, 119808\n" \
  "s_nop 0\n" \
  "buffer_load_dwordx4 v45, s[40:43], s71 offen lds\n" \
  "s_mov_b32 s71, 3932160\n" \
  "s_add_u32 m0, s56, 139264\n" \
  "s_nop 0\n" \
  "buffer_load_dwordx4 v40, s[40:43], s71 offen lds\n" \
  "s_add_u32 m0, s56, 140288\n" \
  "s_nop 0\n" \
  "buffer_load_dwordx4 v41, s[40:43], s71 offen lds\n" \
  "s_add_u32 m0, s56, 141312\n" \
  "s_nop 0\n" \
  "buffer_load_dwordx4 v42, s[40:43], s71 offen lds\n" \
  "s_add_u32 m0, s56, 142336\n" \
  "s_nop 0\n" \
  "buffer_load_dwordx4 v43, s[40:43], s71 offen lds\n" \
  "s_add_u32 m0, s56, 143360\n" \
  "s_nop 0\n" \
  "buffer_load_dwordx4 v44, s[40:43], s71 offen lds\n" \
  "s_add_u32 m0, s56, 144384\n" \
  "s_nop 0\n" \
  "buffer_load_dwordx4 v45, s[40:43], s71 offen lds\n" \
  "ds_read_b64_tr_b16 a[0:1], v72\n" \
  "ds_read_b64_tr_b16 a[2:3], v72 offset:768\n" \
  "ds_read_b64_tr_b16 a[4:5], v72 offset:12288\n" \
  "ds_read_b64_tr_b16 a[6:7], v72 offset:13056\n" \
  "ds_read_b64_tr_b16 a[80:81], v72 offset:32\n" \
  "ds_read_b64_tr_b16 a[82:83], v72 offset:800\n" \
  "ds_read_b64_tr_b16 a[84:85], v72 offset:12320\n" \
  "ds_read_b64_tr_b16 a[86:87], v72 offset:13088\n" \
  "ds_read_b64_tr_b16 a[160:161], v73\n" \
  "ds_read_b64_tr_b16 a[162:163], v73 offset:768\n" \
  "ds_read_b64_tr_b16 a[164:165], v73 offset:12288\n" \
  "ds_read_b64_tr_b16 a[166:167], v73 offset:13056\n" \
  "s_waitcnt lgkmcnt(0)\n" \
  "ds_read_b64_tr_b16 a[8:9], v72 offset:24576\n" \
  "ds_read_b64_tr_b16 a[10:11], v72 offset:25344\n" \
  "ds_read_b64_tr_b16 a[12:13], v72 offset:36864\n" \
  "ds_read_b64_tr_b16 a[14:15], v72 offset:37632\n" \
  "ds_read_b64_tr_b16 a[88:89], v72 offset:24608\n" \
  "ds_read_b64_tr_b16 a[90:91], v72 offset:25376\n" \
  "ds_read_b64_tr_b16 a[92:93], v72 offset:36896\n" \
  "ds_read_b64_tr_b16 a[94:95], v72 offset:37664\n" \
  "ds_read_b64_tr_b16 a[168:169], v73 offset:24576\n" \
  "ds_read_b64_tr_b16 a[170:171], v73 offset:25344\n" \
  "ds_read_b64_tr_b16 a[172:173], v73 offset:36864\n" \
  "ds_read_b64_tr_b16 a[174:175], v73 offset:37632\n" \
  "s_waitcnt lgkmcnt(0)\n" \
  "s_waitcnt vmcnt(12) lgkmcnt(0)\n" \
  "s_barrier\n" \
  "s_mov_b32 s71, 4718592\n" \
  "s_add_u32 m0, s56, 16384\n" \
  "s_nop 0\n" \
  "buffer_load_dwordx4 v40, s[40:43], s71 offen lds\n" \
  "s_add_u32 m0, s56, 17408\n" \
  "s_nop 0\n" \
  "buffer_load_dwordx4 v41, s[40:43], s71 offen lds\n" \
  "s_add_u32 m0, s56, 18432\n" \
  "s_nop 0\n" \
  "buffer_load_dwordx4 v42, s[40:43], s71 offen lds\n" \
  "s_add_u32 m0, s56, 19456\n" \
  "s_nop 0\n" \
  "buffer_load_dwordx4 v43, s[40:43], s71 offen lds\n" \
  "s_add_u32 m0, s56, 20480\n" \
  "s_nop 0\n" \
  "buffer_load_dwordx4 v44, s[40:43], s71 offen lds\n" \
  "s_add_u32 m0, s56, 21504\n" \
  "s_nop 0\n" \
  "buffer_load_dwordx4 v45, s[40:43], s71 offen lds\n" \
  "s_mov_b32 s71, 5505024\n" \
  "s_add_u32 m0, s56, 40960\n" \
  "s_nop 0\n" \
  "buffer_load_dwordx4 v40, s[40:43], s71 offen lds\n" \
  "s_add_u32 m0, s56, 41984\n" \
  "s_nop 0\n" \
  "buffer_load_dwordx4 v41, s[40:43], s71 offen lds\n" \
  "s_add_u32 m0, s56, 43008\n" \
  "s_nop 0\n" \
  "buffer_load_dwordx4 v42, s[40:43], s71 offen lds\n" \
  "s_add_u32 m0, s56, 44032\n" \
  "s_nop 0\n" \
  "buffer_load_dwordx4 v43, s[40:43], s71 offen lds\n" \
  "s_add_u32 m0, s56, 45056\n" \
  "s_nop 0\n" \
  "buffer_load_dwordx4 v44, s[40:43], s71 offen lds\n" \
  "s_add_u32 m0, s56, 46080\n" \
  "s_nop 0\n" \
  "buffer_load_dwordx4 v45, s[40:43], s71 offen lds\n" \
  "ds_read_b64_tr_b16 a[16:17], v76\n" \
  "ds_read_b64_tr_b16 a[18:19], v76 offset:768\n" \
  "ds_read_b64_tr_b16 a[20:21], v76 offset:12288\n" \
  "ds_read_b64_tr_b16 a[22:23], v76 offset:13056\n" \
  "ds_read_b64_tr_b16 a[96:97], v76 offset:32\n" \
  "ds_read_b64_tr_b16 a[98:99], v76 offset:800\n" \
  "ds_read_b64_tr_b16 a[100:101], v76 offset:12320\n" \
  "ds_read_b64_tr_b16 a[102:103], v76 offset:13088\n" \
  "ds_read_b64_tr_b16 a[176:177], v77\n" \
  "ds_read_b64_tr_b16 a[178:179], v77 offset:768\n" \
  "ds_read_b64_tr_b16 a[180:181], v77 offset:12288\n" \
  "ds_read_b64_tr_b16 a[182:183], v77 offset:13056\n" \
  "s_waitcnt lgkmcnt(0)\n" \
  "ds_read_b64_tr_b16 a[24:25], v76 offset:24576\n" \
  "ds_read_b64_tr_b16 a[26:27], v76 offset:25344\n" \
  "ds_read_b64_tr_b16 a[28:29], v76 offset:36864\n" \
  "ds_read_b64_tr_b16 a[30:31], v76 offset:37632\n" \
  "ds_read_b64_tr_b16 a[104:105], v76 offset:24608\n" \
  "ds_read_b64_tr_b16 a[106:107], v76 offset:25376\n" \
  "ds_read_b64_tr_b16 a[108:109], v76 offset:36896\n" \
  "ds_read_b64_tr_b16 a[110:111], v76 offset:37664\n" \
  "ds_read_b64_tr_b16 a[184:185], v77 offset:24576\n" \
  "ds_read_b64_tr_b16 a[186:187], v77 offset:25344\n" \
  "ds_read_b64_tr_b16 a[188:189], v77 offset:36864\n" \
  "ds_read_b64_tr_b16 a[190:191], v77 offset:37632\n" \
  "s_waitcnt lgkmcnt(0)\n" \
  "s_waitcnt vmcnt(12) lgkmcnt(0)\n" \
  "s_barrier\n" \
  "s_mov_b32 s71, 6291456\n" \
  "s_add_u32 m0, s56, 65536\n" \
  "s_nop 0\n" \
  "buffer_load_dwordx4 v40, s[40:43], s71 offen lds\n" \
  "s_add_u32 m0, s56, 66560\n" \
  "s_nop 0\n" \
  "buffer_load_dwordx4 v41, s[40:43], s71 offen lds\n" \
  "s_add_u32 m0, s56, 67584\n" \
  "s_nop 0\n" \
  "buffer_load_dwordx4 v42, s[40:43], s71 offen lds\n" \
  "s_add_u32 m0, s56, 68608\n" \
  "s_nop 0\n" \
  "buffer_load_dwordx4 v43, s[40:43], s71 offen lds\n" \
  "s_add_u32 m0, s56, 69632\n" \
  "s_nop 0\n" \
  "buffer_load_dwordx4 v44, s[40:43], s71 offen lds\n" \
  "s_add_u32 m0, s56, 70656\n" \
  "s_nop 0\n" \
  "buffer_load_dwordx4 v45, s[40:43], s71 offen lds\n" \
  "s_mov_b32 s71, 7077888\n" \
  "s_add_u32 m0, s56, 90112\n" \
  "s_nop 0\n" \
  "buffer_load_dwordx4 v40, s[40:43], s71 offen lds\n" \
  "s_add_u32 m0, s56, 91136\n" \
  "s_nop 0\n" \
  "buffer_load_dwordx4 v41, s[40:43], s71 offen lds\n" \
  "s_add_u32 m0, s56, 92160\n" \
  "s_nop 0\n" \
  "buffer_load_dwordx4 v42, s[40:43], s71 offen lds\n" \
  "s_add_u32 m0, s56, 93184\n" \
  "s_nop 0\n" \
  "buffer_load_dwordx4 v43, s[40:43], s71 offen lds\n" \
  "s_add_u32 m0, s56, 94208\n" \
  "s_nop 0\n" \
  "buffer_load_dwordx4 v44, s[40:43], s71 offen lds\n" \
  "s_add_u32 m0, s56, 95232\n" \
  "s_nop 0\n" \
  "buffer_load_dwordx4 v45, s[40:43], s71 offen lds\n" \
  "ds_read_b64_tr_b16 a[32:33], v80\n" \
  "ds_read_b64_tr_b16 a[34:35], v80 offset:768\n" \
  "ds_read_b64_tr_b16 a[36:37], v80 offset:12288\n" \
  "ds_read_b64_tr_b16 a[38:39], v80 offset:13056\n" \
  "ds_read_b64_tr_b16 a[112:113], v80 offset:32\n" \
  "ds_read_b64_tr_b16 a[114:115], v80 offset:800\n" \
  "ds_read_b64_tr_b16 a[116:117], v80 offset:12320\n" \
  "ds_read_b64_tr_b16 a[118:119], v80 offset:13088\n" \
  "ds_read_b64_tr_b16 a[192:193], v81\n" \
  "ds_read_b64_tr_b16 a[194:195], v81 offset:768\n" \
  "ds_read_b64_tr_b16 a[196:197], v81 offset:12288\n" \
  "ds_read_b64_tr_b16 a[198:199], v81 offset:13056\n" \
  "s_waitcnt lgkmcnt(0)\n" \
  "ds_read_b64_tr_b16 a[40:41], v80 offset:24576\n" \
  "ds_read_b64_tr_b16 a[42:43], v80 offset:25344\n" \
  "ds_read_b64_tr_b16 a[44:45], v80 offset:36864\n" \
  "ds_read_b64_tr_b16 a[46:47], v80 offset:37632\n" \
  "ds_read_b64_tr_b16 a[120:121], v80 offset:24608\n" \
  "ds_read_b64_tr_b16 a[122:123], v80 offset:25376\n" \
  "ds_read_b64_tr_b16 a[124:125], v80 offset:36896\n" \
  "ds_read_b64_tr_b16 a[126:127], v80 offset:37664\n" \
  "ds_read_b64_tr_b16 a[200:201], v81 offset:24576\n" \
  "ds_read_b64_tr_b16 a[202:203], v81 offset:25344\n" \
  "ds_read_b64_tr_b16 a[204:205], v81 offset:36864\n" \
  "ds_read_b64_tr_b16 a[206:207], v81 offset:37632\n" \
  "s_waitcnt lgkmcnt(0)\n" \
  "s_waitcnt vmcnt(12) lgkmcnt(0)\n" \
  "s_barrier\n" \
  "s_mov_b32 s71, 0\n" \
  "s_add_u32 m0, s57, 114688\n" \
  "s_nop 0\n" \
  "buffer_load_dwordx4 v50, s[44:47], s71 offen lds\n" \
  "s_add_u32 m0, s57, 115712\n" \
  "s_nop 0\n" \
  "buffer_load_dwordx4 v51, s[44:47], s71 offen lds\n" \
  "s_add_u32 m0, s57, 116736\n" \
  "s_nop 0\n" \
  "buffer_load_dwordx4 v52, s[44:47], s71 offen lds\n" \
  "s_add_u32 m0, s57, 117760\n" \
  "s_nop 0\n" \
  "buffer_load_dwordx4 v53, s[44:47], s71 offen lds\n" \
  "s_add_u32 m0, s57, 118784\n" \
  "s_nop 0\n" \
  "buffer_load_dwordx4 v54, s[44:47], s71 offen lds\n" \
  "s_add_u32 m0, s57, 119808\n" \
  "s_nop 0\n" \
  "buffer_load_dwordx4 v55, s[44:47], s71 offen lds\n" \
  "s_add_u32 m0, s57, 120832\n" \
  "s_nop 0\n" \
  "buffer_load_dwordx4 v56, s[44:47], s71 offen lds\n" \
  "s_add_u32 m0, s57, 121856\n" \
  "s_nop 0\n" \
  "buffer_load_dwordx4 v57, s[44:47], s71 offen lds\n" \
  "s_add_u32 m0, s57, 122880\n" \
  "s_nop 0\n" \
  "buffer_load_dwordx4 v58, s[44:47], s71 offen lds\n" \
  "s_add_u32 m0, s57, 123904\n" \
  "s_nop 0\n" \
  "buffer_load_dwordx4 v59, s[44:47], s71 offen lds\n" \
  "ds_read_b64_tr_b16 a[48:49], v72\n" \
  "ds_read_b64_tr_b16 a[50:51], v72 offset:768\n" \
  "ds_read_b64_tr_b16 a[52:53], v72 offset:12288\n" \
  "ds_read_b64_tr_b16 a[54:55], v72 offset:13056\n" \
  "ds_read_b64_tr_b16 a[128:129], v72 offset:32\n" \
  "ds_read_b64_tr_b16 a[130:131], v72 offset:800\n" \
  "ds_read_b64_tr_b16 a[132:133], v72 offset:12320\n" \
  "ds_read_b64_tr_b16 a[134:135], v72 offset:13088\n" \
  "ds_read_b64_tr_b16 a[208:209], v73\n" \
  "ds_read_b64_tr_b16 a[210:211], v73 offset:768\n" \
  "ds_read_b64_tr_b16 a[212:213], v73 offset:12288\n" \
  "ds_read_b64_tr_b16 a[214:215], v73 offset:13056\n" \
  "s_waitcnt lgkmcnt(0)\n" \
  "ds_read_b64_tr_b16 a[56:57], v72 offset:24576\n" \
  "ds_read_b64_tr_b16 a[58:59], v72 offset:25344\n" \
  "ds_read_b64_tr_b16 a[60:61], v72 offset:36864\n" \
  "ds_read_b64_tr_b16 a[62:63], v72 offset:37632\n" \
  "ds_read_b64_tr_b16 a[136:137], v72 offset:24608\n" \
  "ds_read_b64_tr_b16 a[138:139], v72 offset:25376\n" \
  "ds_read_b64_tr_b16 a[140:141], v72 offset:36896\n" \
  "ds_read_b64_tr_b16 a[142:143], v72 offset:37664\n" \
  "ds_read_b64_tr_b16 a[216:217], v73 offset:24576\n" \
  "ds_read_b64_tr_b16 a[218:219], v73 offset:25344\n" \
  "ds_read_b64_tr_b16 a[220:221], v73 offset:36864\n" \
  "ds_read_b64_tr_b16 a[222:223], v73 offset:37632\n" \
  "s_waitcnt lgkmcnt(0)\n" \
  "s_waitcnt vmcnt(10) lgkmcnt(0)\n" \
  "s_barrier\n" \
  "s_mov_b32 s71, 40960\n" \
  "s_add_u32 m0, s57, 16384\n" \
  "s_nop 0\n" \
  "buffer_load_dwordx4 v50, s[44:47], s71 offen lds\n" \
  "s_add_u32 m0, s57, 17408\n" \
  "s_nop 0\n" \
  "buffer_load_dwordx4 v51, s[44:47], s71 offen lds\n" \
  "s_add_u32 m0, s57, 18432\n" \
  "s_nop 0\n" \
  "buffer_load_dwordx4 v52, s[44:47], s71 offen lds\n" \
  "s_add_u32 m0, s57, 19456\n" \
  "s_nop 0\n" \
  "buffer_load_dwordx4 v53, s[44:47], s71 offen lds\n" \
  "s_add_u32 m0, s57, 20480\n" \
  "s_nop 0\n" \
  "buffer_load_dwordx4 v54, s[44:47], s71 offen lds\n" \
  "s_add_u32 m0, s57, 21504\n" \
  "s_nop 0\n" \
  "buffer_load_dwordx4 v55, s[44:47], s71 offen lds\n" \
  "s_add_u32 m0, s57, 22528\n" \
  "s_nop 0\n" \
  "buffer_load_dwordx4 v56, s[44:47], s71 offen lds\n" \
  "s_add_u32 m0, s57, 23552\n" \
  "s_nop 0\n" \
  "buffer_load_dwordx4 v57, s[44:47], s71 offen lds\n" \
  "s_add_u32 m0, s57, 24576\n" \
  "s_nop 0\n" \
  "buffer_load_dwordx4 v58, s[44:47], s71 offen lds\n" \
  "s_add_u32 m0, s57, 25600\n" \
  "s_nop 0\n" \
  "buffer_load_dwordx4 v59, s[44:47], s71 offen lds\n" \
  "ds_read_b64_tr_b16 a[64:65], v76\n" \
  "ds_read_b64_tr_b16 a[66:67], v76 offset:768\n" \
  "ds_read_b64_tr_b16 a[68:69], v76 offset:12288\n" \
  "ds_read_b64_tr_b16 a[70:71], v76 offset:13056\n" \
  "ds_read_b64_tr_b16 a[144:145], v76 offset:32\n" \
  "ds_read_b64_tr_b16 a[146:147], v76 offset:800\n" \
  "ds_read_b64_tr_b16 a[148:149], v76 offset:12320\n" \
  "ds_read_b64_tr_b16 a[150:151], v76 offset:13088\n" \
  "ds_read_b64_tr_b16 a[224:225], v77\n" \
  "ds_read_b64_tr_b16 a[226:227], v77 offset:768\n" \
  "ds_read_b64_tr_b16 a[228:229], v77 offset:12288\n" \
  "ds_read_b64_tr_b16 a[230:231], v77 offset:13056\n" \
  "s_waitcnt lgkmcnt(0)\n" \
  "ds_read_b64_tr_b16 a[72:73], v76 offset:24576\n" \
  "ds_read_b64_tr_b16 a[74:75], v76 offset:25344\n" \
  "ds_read_b64_tr_b16 a[76:77], v76 offset:36864\n" \
  "ds_read_b64_tr_b16 a[78:79], v76 offset:37632\n" \
  "ds_read_b64_tr_b16 a[152:153], v76 offset:24608\n" \
  "ds_read_b64_tr_b16 a[154:155], v76 offset:25376\n" \
  "ds_read_b64_tr_b16 a[156:157], v76 offset:36896\n" \
  "ds_read_b64_tr_b16 a[158:159], v76 offset:37664\n" \
  "ds_read_b64_tr_b16 a[232:233], v77 offset:24576\n" \
  "ds_read_b64_tr_b16 a[234:235], v77 offset:25344\n" \
  "ds_read_b64_tr_b16 a[236:237], v77 offset:36864\n" \
  "ds_read_b64_tr_b16 a[238:239], v77 offset:37632\n" \
  "s_waitcnt lgkmcnt(0)\n" \
  "s_waitcnt vmcnt(10) lgkmcnt(0)\n" \
  "s_barrier\n" \
  "s_mov_b32 s71, 81920\n" \
  "s_add_u32 m0, s57, 65536\n" \
  "s_nop 0\n" \
  "buffer_load_dwordx4 v50, s[44:47], s71 offen lds\n" \
  "s_add_u32 m0, s57, 66560\n" \
  "s_nop 0\n" \
  "buffer_load_dwordx4 v51, s[44:47], s71 offen lds\n" \
  "s_add_u32 m0, s57, 67584\n" \
  "s_nop 0\n" \
  "buffer_load_dwordx4 v52, s[44:47], s71 offen lds\n" \
  "s_add_u32 m0, s57, 68608\n" \
  "s_nop 0\n" \
  "buffer_load_dwordx4 v53, s[44:47], s71 offen lds\n" \
  "s_add_u32 m0, s57, 69632\n" \
  "s_nop 0\n" \
  "buffer_load_dwordx4 v54, s[44:47], s71 offen lds\n" \
  "s_add_u32 m0, s57, 70656\n" \
  "s_nop 0\n" \
  "buffer_load_dwordx4 v55, s[44:47], s71 offen lds\n" \
  "s_add_u32 m0, s57, 71680\n" \
  "s_nop 0\n" \
  "buffer_load_dwordx4 v56, s[44:47], s71 offen lds\n" \
  "s_add_u32 m0, s57, 72704\n" \
  "s_nop 0\n" \
  "buffer_load_dwordx4 v57, s[44:47], s71 offen lds\n" \
  "s_add_u32 m0, s57, 73728\n" \
  "s_nop 0\n" \
  "buffer_load_dwordx4 v58, s[44:47], s71 offen lds\n" \
  "s_add_u32 m0, s57, 74752\n" \
  "s_nop 0\n" \
  "buffer_load_dwordx4 v59, s[44:47], s71 offen lds\n" \
  "ds_read_b64_tr_b16 v[112:113], v82\n" \
  "ds_read_b64_tr_b16 v[114:115], v82 offset:2560\n" \
  "ds_read_b64_tr_b16 v[116:117], v82 offset:20480\n" \
  "ds_read_b64_tr_b16 v[118:119], v82 offset:23040\n" \
  "ds_read_b64_tr_b16 v[144:145], v82 offset:256\n" \
  "ds_read_b64_tr_b16 v[146:147], v82 offset:2816\n" \
  "ds_read_b64_tr_b16 v[148:149], v82 offset:20736\n" \
  "ds_read_b64_tr_b16 v[150:151], v82 offset:23296\n" \
  "s_waitcnt lgkmcnt(0)\n" \
  "ds_read_b64_tr_b16 v[176:177], v82 offset:512\n" \
  "ds_read_b64_tr_b16 v[178:179], v82 offset:3072\n" \
  "ds_read_b64_tr_b16 v[180:181], v82 offset:20992\n" \
  "ds_read_b64_tr_b16 v[182:183], v82 offset:23552\n" \
  "ds_read_b64_tr_b16 v[208:209], v82 offset:768\n" \
  "ds_read_b64_tr_b16 v[210:211], v82 offset:3328\n" \
  "ds_read_b64_tr_b16 v[212:213], v82 offset:21248\n" \
  "ds_read_b64_tr_b16 v[214:215], v82 offset:23808\n" \
  "s_waitcnt lgkmcnt(0)\n" \
  "ds_read_b64_tr_b16 v[240:241], v82 offset:1024\n" \
  "ds_read_b64_tr_b16 v[242:243], v82 offset:3584\n" \
  "ds_read_b64_tr_b16 v[244:245], v82 offset:21504\n" \
  "ds_read_b64_tr_b16 v[246:247], v82 offset:24064\n" \
  "s_waitcnt vmcnt(10) lgkmcnt(0)\n" \
  "s_barrier\n" \
  "s_mov_b32 s71, 122880\n" \
  "s_add_u32 m0, s57, 114688\n" \
  "s_nop 0\n" \
  "buffer_load_dwordx4 v50, s[44:47], s71 offen lds\n" \
  "s_add_u32 m0, s57, 115712\n" \
  "s_nop 0\n" \
  "buffer_load_dwordx4 v51, s[44:47], s71 offen lds\n" \
  "s_add_u32 m0, s57, 116736\n" \
  "s_nop 0\n" \
  "buffer_load_dwordx4 v52, s[44:47], s71 offen lds\n" \
  "s_add_u32 m0, s57, 117760\n" \
  "s_nop 0\n" \
  "buffer_load_dwordx4 v53, s[44:47], s71 offen lds\n" \
  "s_add_u32 m0, s57, 118784\n" \
  "s_nop 0\n" \
  "buffer_load_dwordx4 v54, s[44:47], s71 offen lds\n" \
  "s_add_u32 m0, s57, 119808\n" \
  "s_nop 0\n" \
  "buffer_load_dwordx4 v55, s[44:47], s71 offen lds\n" \
  "s_add_u32 m0, s57, 120832\n" \
  "s_nop 0\n" \
  "buffer_load_dwordx4 v56, s[44:47], s71 offen lds\n" \
  "s_add_u32 m0, s57, 121856\n" \
  "s_nop 0\n" \
  "buffer_load_dwordx4 v57, s[44:47], s71 offen lds\n" \
  "s_add_u32 m0, s57, 122880\n" \
  "s_nop 0\n" \
  "buffer_load_dwordx4 v58, s[44:47], s71 offen lds\n" \
  "s_add_u32 m0, s57, 123904\n" \
  "s_nop 0\n" \
  "buffer_load_dwordx4 v59, s[44:47], s71 offen lds\n" \
  "ds_read_b64_tr_b16 v[120:121], v74\n" \
  "ds_read_b64_tr_b16 v[122:123], v74 offset:2560\n" \
  "ds_read_b64_tr_b16 v[124:125], v74 offset:20480\n" \
  "ds_read_b64_tr_b16 v[126:127], v74 offset:23040\n" \
  "ds_read_b64_tr_b16 v[152:153], v74 offset:256\n" \
  "ds_read_b64_tr_b16 v[154:155], v74 offset:2816\n" \
  "ds_read_b64_tr_b16 v[156:157], v74 offset:20736\n" \
  "ds_read_b64_tr_b16 v[158:159], v74 offset:23296\n" \
  "s_waitcnt lgkmcnt(0)\n" \
  "ds_read_b64_tr_b16 v[184:185], v74 offset:512\n" \
  "ds_read_b64_tr_b16 v[186:187], v74 offset:3072\n" \
  "ds_read_b64_tr_b16 v[188:189], v74 offset:20992\n" \
  "ds_read_b64_tr_b16 v[190:191], v74 offset:23552\n" \
  "ds_read_b64_tr_b16 v[216:217], v74 offset:768\n" \
  "ds_read_b64_tr_b16 v[218:219], v74 offset:3328\n" \
  "ds_read_b64_tr_b16 v[220:221], v74 offset:21248\n" \
  "ds_read_b64_tr_b16 v[222:223], v74 offset:23808\n" \
  "s_waitcnt lgkmcnt(0)\n" \
  "ds_read_b64_tr_b16 v[248:249], v74 offset:1024\n" \
  "ds_read_b64_tr_b16 v[250:251], v74 offset:3584\n" \
  "ds_read_b64_tr_b16 v[252:253], v74 offset:21504\n" \
  "ds_read_b64_tr_b16 v[254:255], v74 offset:24064\n" \
  "s_waitcnt vmcnt(10) lgkmcnt(0)\n" \
  "s_barrier\n" \
  "s_mov_b32 s71, 163840\n" \
  "s_add_u32 m0, s57, 16384\n" \
  "s_nop 0\n" \
  "buffer_load_dwordx4 v50, s[44:47], s71 offen lds\n" \
  "s_add_u32 m0, s57, 17408\n" \
  "s_nop 0\n" \
  "buffer_load_dwordx4 v51, s[44:47], s71 offen lds\n" \
  "s_add_u32 m0, s57, 18432\n" \
  "s_nop 0\n" \
  "buffer_load_dwordx4 v52, s[44:47], s71 offen lds\n" \
  "s_add_u32 m0, s57, 19456\n" \
  "s_nop 0\n" \
  "buffer_load_dwordx4 v53, s[44:47], s71 offen lds\n" \
  "s_add_u32 m0, s57, 20480\n" \
  "s_nop 0\n" \
  "buffer_load_dwordx4 v54, s[44:47], s71 offen lds\n" \
  "s_add_u32 m0, s57, 21504\n" \
  "s_nop 0\n" \
  "buffer_load_dwordx4 v55, s[44:47], s71 offen lds\n" \
  "s_add_u32 m0, s57, 22528\n" \
  "s_nop 0\n" \
  "buffer_load_dwordx4 v56, s[44:47], s71 offen lds\n" \
  "s_add_u32 m0, s57, 23552\n" \
  "s_nop 0\n" \
  "buffer_load_dwordx4 v57, s[44:47], s71 offen lds\n" \
  "s_add_u32 m0, s57, 24576\n" \
  "s_nop 0\n" \
  "buffer_load_dwordx4 v58, s[44:47], s71 offen lds\n" \
  "s_add_u32 m0, s57, 25600\n" \
  "s_nop 0\n" \
  "buffer_load_dwordx4 v59, s[44:47], s71 offen lds\n" \
  "ds_read_b64_tr_b16 v[128:129], v78\n" \
  "ds_read_b64_tr_b16 v[130:131], v78 offset:2560\n" \
  "ds_read_b64_tr_b16 v[132:133], v78 offset:20480\n" \
  "ds_read_b64_tr_b16 v[134:135], v78 offset:23040\n" \
  "ds_read_b64_tr_b16 v[160:161], v78 offset:256\n" \
  "ds_read_b64_tr_b16 v[162:163], v78 offset:2816\n" \
  "ds_read_b64_tr_b16 v[164:165], v78 offset:20736\n" \
  "ds_read_b64_tr_b16 v[166:167], v78 offset:23296\n" \
  "s_waitcnt lgkmcnt(0)\n" \
  "ds_read_b64_tr_b16 v[192:193], v78 offset:512\n" \
  "ds_read_b64_tr_b16 v[194:195], v78 offset:3072\n" \
  "ds_read_b64_tr_b16 v[196:197], v78 offset:20992\n" \
  "ds_read_b64_tr_b16 v[198:199], v78 offset:23552\n" \
  "ds_read_b64_tr_b16 v[224:225], v78 offset:768\n" \
  "ds_read_b64_tr_b16 v[226:227], v78 offset:3328\n" \
  "ds_read_b64_tr_b16 v[228:229], v78 offset:21248\n" \
  "ds_read_b64_tr_b16 v[230:231], v78 offset:23808\n" \
  "s_waitcnt lgkmcnt(0)\n" \
  "ds_read_b64_tr_b16 a[240:241], v78 offset:1024\n" \
  "ds_read_b64_tr_b16 a[242:243], v78 offset:3584\n" \
  "ds_read_b64_tr_b16 a[244:245], v78 offset:21504\n" \
  "ds_read_b64_tr_b16 a[246:247], v78 offset:24064\n" \
  "s_waitcnt vmcnt(10) lgkmcnt(0)\n" \
  "s_barrier\n" \
  "s_mov_b32 s71, 204800\n" \
  "s_add_u32 m0, s57, 65536\n" \
  "s_nop 0\n" \
  "buffer_load_dwordx4 v50, s[44:47], s71 offen lds\n" \
  "s_add_u32 m0, s57, 66560\n" \
  "s_nop 0\n" \
  "buffer_load_dwordx4 v51, s[44:47], s71 offen lds\n" \
  "s_add_u32 m0, s57, 67584\n" \
  "s_nop 0\n" \
  "buffer_load_dwordx4 v52, s[44:47], s71 offen lds\n" \
  "s_add_u32 m0, s57, 68608\n" \
  "s_nop 0\n" \
  "buffer_load_dwordx4 v53, s[44:47], s71 offen lds\n" \
  "s_add_u32 m0, s57, 69632\n" \
  "s_nop 0\n" \
  "buffer_load_dwordx4 v54, s[44:47], s71 offen lds\n" \
  "s_add_u32 m0, s57, 70656\n" \
  "s_nop 0\n" \
  "buffer_load_dwordx4 v55, s[44:47], s71 offen lds\n" \
  "s_add_u32 m0, s57, 71680\n" \
  "s_nop 0\n" \
  "buffer_load_dwordx4 v56, s[44:47], s71 offen lds\n" \
  "s_add_u32 m0, s57, 72704\n" \
  "s_nop 0\n" \
  "buffer_load_dwordx4 v57, s[44:47], s71 offen lds\n" \
  "s_add_u32 m0, s57, 73728\n" \
  "s_nop 0\n" \
  "buffer_load_dwordx4 v58, s[44:47], s71 offen lds\n" \
  "s_add_u32 m0, s57, 74752\n" \
  "s_nop 0\n" \
  "buffer_load_dwordx4 v59, s[44:47], s71 offen lds\n" \
  "ds_read_b64_tr_b16 v[136:137], v82\n" \
  "ds_read_b64_tr_b16 v[138:139], v82 offset:2560\n" \
  "ds_read_b64_tr_b16 v[140:141], v82 offset:20480\n" \
  "ds_read_b64_tr_b16 v[142:143], v82 offset:23040\n" \
  "ds_read_b64_tr_b16 v[168:169], v82 offset:256\n" \
  "ds_read_b64_tr_b16 v[170:171], v82 offset:2816\n" \
  "ds_read_b64_tr_b16 v[172:173], v82 offset:20736\n" \
  "ds_read_b64_tr_b16 v[174:175], v82 offset:23296\n" \
  "s_waitcnt lgkmcnt(0)\n" \
  "ds_read_b64_tr_b16 v[200:201], v82 offset:512\n" \
  "ds_read_b64_tr_b16 v[202:203], v82 offset:3072\n" \
  "ds_read_b64_tr_b16 v[204:205], v82 offset:20992\n" \
  "ds_read_b64_tr_b16 v[206:207], v82 offset:23552\n" \
  "ds_read_b64_tr_b16 v[232:233], v82 offset:768\n" \
  "ds_read_b64_tr_b16 v[234:235], v82 offset:3328\n" \
  "ds_read_b64_tr_b16 v[236:237], v82 offset:21248\n" \
  "ds_read_b64_tr_b16 v[238:239], v82 offset:23808\n" \
  "s_waitcnt lgkmcnt(0)\n" \
  "ds_read_b64_tr_b16 a[248:249], v82 offset:1024\n" \
  "ds_read_b64_tr_b16 a[250:251], v82 offset:3584\n" \
  "ds_read_b64_tr_b16 a[252:253], v82 offset:21504\n" \
  "ds_read_b64_tr_b16 a[254:255], v82 offset:24064\n" \
  "s_waitcnt vmcnt(10) lgkmcnt(0)\n" \
  "s_barrier\n" \
  "s_waitcnt vmcnt(0) lgkmcnt(0)\n" \
  "s_barrier\n" \
  "s_waitcnt lgkmcnt(0)\n" \
  "s_barrier\n" \
  "LIV_%=:\n" \
  "buffer_load_dword v92, v69, s[36:39], 0 offen sc1\n" \
  "s_waitcnt vmcnt(0)\n" \
  "v_and_b32 v93, -4, v92\n" \
  "v_cmp_ne_u32 vcc, s58, v93\n" \
  "s_cbranch_vccz LIXV_%=\n" \
  "s_add_u32 s60, s60, 1\n" \
  "s_cmp_gt_u32 s60, 20000\n" \
  "s_cbranch_scc1 LIXV_%=\n" \
  "s_sleep 8\n" \
  "s_branch LIV_%=\n" \
  "LIXV_%=:\n" \
  "v_and_b32 v92, v70, v92\n" \
  "v_cmp_ne_u32 s[80:81], 0, v92\n" \
  "s_mov_b32 s78, 0x55555555\n" \
  "s_mov_b32 s79, 0x55555555\n" \
  "s_xor_b64 s[82:83], s[80:81], s[78:79]\n" \
  "s_bcnt1_i32_b64 s71, s[82:83]\n" \
  "s_cmp_le_u32 s71, 26\n" \
  "s_cselect_b64 s[80:81], s[78:79], s[80:81]\n" \
  "s_not_b64 s[78:79], s[78:79]\n" \
  "s_cmp_ge_u32 s71, 38\n" \
  "s_cselect_b64 s[80:81], s[78:79], s[80:81]\n" \
  "v_lshrrev_b32 v92, 21, v25\n" \
  "v_lshrrev_b64 v[92:93], v92, s[80:81]\n" \
  "v_and_b32 v92, 1, v92\n" \
  "v_lshlrev_b32 v92, 13, v92\n" \
  "v_xor_b32 v25, v92, v25\n" \
  "v_lshrrev_b32 v92, 21, v26\n" \
  "v_lshrrev_b64 v[92:93], v92, s[80:81]\n" \
  "v_and_b32 v92, 1, v92\n" \
  "v_lshlrev_b32 v92, 13, v92\n" \
  "v_xor_b32 v26, v92, v26\n" \
  "v_lshrrev_b32 v92, 21, v27\n" \
  "v_lshrrev_b64 v[92:93], v92, s[80:81]\n" \
  "v_and_b32 v92, 1, v92\n" \
  "v_lshlrev_b32 v92, 13, v92\n" \
  "v_xor_b32 v27, v92, v27\n" \
  "v_lshrrev_b32 v92, 21, v28\n" \
  "v_lshrrev_b64 v[92:93], v92, s[80:81]\n" \
  "v_and_b32 v92, 1, v92\n" \
  "v_lshlrev_b32 v92, 13, v92\n" \
  "v_xor_b32 v28, v92, v28\n" \
  "v_lshrrev_b32 v92, 21, v29\n" \
  "v_lshrrev_b64 v[92:93], v92, s[80:81]\n" \
  "v_and_b32 v92, 1, v92\n" \
  "v_lshlrev_b32 v92, 13, v92\n" \
  "v_xor_b32 v29, v92, v29\n" \
  "v_lshrrev_b32 v92, 21, v30\n" \
  "v_lshrrev_b64 v[92:93], v92, s[80:81]\n" \
  "v_and_b32 v92, 1, v92\n" \
  "v_lshlrev_b32 v92, 13, v92\n" \
  "v_xor_b32 v30, v92, v30\n" \
  "v_lshrrev_b32 v92, 21, v67\n" \
  "v_lshrrev_b64 v[92:93], v92, s[80:81]\n" \
  "v_and_b32 v92, 1, v92\n" \
  "v_lshlrev_b32 v92, 13, v92\n" \
  "v_xor_b32 v67, v92, v67\n" \
  "v_lshrrev_b32 v92, 21, v71\n" \
  "v_lshrrev_b64 v[92:93], v92, s[80:81]\n" \
  "v_and_b32 v92, 1, v92\n" \
  "v_lshlrev_b32 v92, 13, v92\n" \
  "v_xor_b32 v71, v92, v71\n" \
  "s_lshr_b32 s71, s54, 21\n" \
  "s_lshr_b64 s[80:81], s[80:81], s71\n" \
  "s_and_b32 s71, s80, 1\n" \
  "s_lshl_b32 s71, s71, 13\n" \
  "s_or_b32 s54, s54, s71\n" \
  "s_and_b32 s71, s48, 3\n" \
  "s_cmp_eq_u32 s71, 3\n" \
  "s_cbranch_scc0 LIYV_%=\n" \
  "v_mov_b32 v1, v71\n" \
  "LIYV_%=:\n" \
  "buffer_atomic_add v32, v68, s[36:39], 0 offen\n" \
  "v_mov_b32 v104, v64\n" \
  "v_mov_b32 v105, v65\n" \
  "v_mov_b32 v106, v66\n" \
  "v_mov_b32 v107, v67\n" \
  "v_mov_b32 v28, v25\n" \
  "v_mov_b32 v88, v74\n" \
  "v_mov_b32 v89, v78\n" \
  "ds_read_b64_tr_b16 v[40:41], v88\n" \
  "ds_read_b64_tr_b16 v[42:43], v88 offset:2560\n" \
  "ds_read_b64_tr_b16 v[44:45], v88 offset:20480\n" \
  "ds_read_b64_tr_b16 v[46:47], v88 offset:23040\n" \
  "ds_read_b64_tr_b16 v[48:49], v88 offset:256\n" \
  "ds_read_b64_tr_b16 v[50:51], v88 offset:2816\n" \
  "ds_read_b64_tr_b16 v[52:53], v88 offset:20736\n" \
  "ds_read_b64_tr_b16 v[54:55], v88 offset:23296\n" \
  "s_waitcnt lgkmcnt(0)\n" \
  "ds_read_b64_tr_b16 v[56:57], v88 offset:512\n" \
  "ds_read_b64_tr_b16 v[58:59], v88 offset:3072\n" \
  "ds_read_b64_tr_b16 v[60:61], v88 offset:20992\n" \
  "ds_read_b64_tr_b16 v[62:63], v88 offset:23552\n" \
  "ds_read_b64_tr_b16 v[64:65], v88 offset:768\n" \
  "ds_read_b64_tr_b16 v[66:67], v88 offset:3328\n" \
  "ds_read_b64_tr_b16 v[68:69], v88 offset:21248\n" \
  "ds_read_b64_tr_b16 v[70:71], v88 offset:23808\n" \
  "s_waitcnt lgkmcnt(0)\n" \
  "ds_read_b64_tr_b16 v[72:73], v88 offset:1024\n" \
  "ds_read_b64_tr_b16 v[74:75], v88 offset:3584\n" \
  "ds_read_b64_tr_b16 v[76:77], v88 offset:21504\n" \
  "ds_read_b64_tr_b16 v[78:79], v88 offset:24064\n" \
  "s_waitcnt lgkmcnt(0)\n" \
  "s_barrier\n" \
  "ds_write_b128 v11, v[40:43] offset:0\n" \
  "ds_write_b128 v11, v[44:47] offset:1024\n" \
  "ds_write_b128 v11, v[48:51] offset:2048\n" \
  "ds_write_b128 v11, v[52:55] offset:3072\n" \
  "ds_write_b128 v11, v[56:59] offset:4096\n" \
  "ds_write_b128 v11, v[60:63] offset:5120\n" \
  "ds_write_b128 v11, v[64:67] offset:6144\n" \
  "ds_write_b128 v11, v[68:71] offset:7168\n" \
  "ds_write_b128 v11, v[72:75] offset:8192\n" \
  "ds_write_b128 v11, v[76:79] offset:9216\n" \
  "s_waitcnt lgkmcnt(0)\n" \
  "ds_read_b64_tr_b16 v[40:41], v89\n" \
  "ds_read_b64_tr_b16 v[42:43], v89 offset:2560\n" \
  "ds_read_b64_tr_b16 v[44:45], v89 offset:20480\n" \
  "ds_read_b64_tr_b16 v[46:47], v89 offset:23040\n" \
  "ds_read_b64_tr_b16 v[48:49], v89 offset:256\n" \
  "ds_read_b64_tr_b16 v[50:51], v89 offset:2816\n" \
  "ds_read_b64_tr_b16 v[52:53], v89 offset:20736\n" \
  "ds_read_b64_tr_b16 v[54:55], v89 offset:23296\n" \
  "s_waitcnt lgkmcnt(0)\n" \
  "ds_read_b64_tr_b16 v[56:57], v89 offset:512\n" \
  "ds_read_b64_tr_b16 v[58:59], v89 offset:3072\n" \
  "ds_read_b64_tr_b16 v[60:61], v89 offset:20992\n" \
  "ds_read_b64_tr_b16 v[62:63], v89 offset:23552\n" \
  "ds_read_b64_tr_b16 v[64:65], v89 offset:768\n" \
  "ds_read_b64_tr_b16 v[66:67], v89 offset:3328\n" \
  "ds_read_b64_tr_b16 v[68:69], v89 offset:21248\n" \
  "ds_read_b64_tr_b16 v[70:71], v89 offset:23808\n" \
  "s_waitcnt lgkmcnt(0)\n" \
  "ds_read_b64_tr_b16 v[72:73], v89 offset:1024\n" \
  "ds_read_b64_tr_b16 v[74:75], v89 offset:3584\n" \
  "ds_read_b64_tr_b16 v[76:77], v89 offset:21504\n" \
  "ds_read_b64_tr_b16 v[78:79], v89 offset:24064\n" \
  "s_waitcnt lgkmcnt(0)\n" \
  "s_barrier\n" \
  "ds_write_b128 v11, v[40:43] offset:40960\n" \
  "ds_write_b128 v11, v[44:47] offset:41984\n" \
  "ds_write_b128 v11, v[48:51] offset:43008\n" \
  "ds_write_b128 v11, v[52:55] offset:44032\n" \
  "ds_write_b128 v11, v[56:59] offset:45056\n" \
  "ds_write_b128 v11, v[60:63] offset:46080\n" \
  "ds_write_b128 v11, v[64:67] offset:47104\n" \
  "ds_write_b128 v11, v[68:71] offset:48128\n" \
  "ds_write_b128 v11, v[72:75] offset:49152\n" \
  "ds_write_b128 v11, v[76:79] offset:50176\n" \
  "s_waitcnt lgkmcnt(0)\n" \
  "s_barrier\n" \
  "LSW_%=:\n" \
  "s_memrealtime s[74:75]\n" \
  "s_waitcnt vmcnt(0) lgkmcnt(0)\n" \
  "s_mov_b32 s63, s74\n" \
  "s_and_b32 s71, s48, 3\n" \
  "s_cmp_eq_u32 s71, 2\n" \
  "s_cbranch_scc0 LHB_%=\n" \
  "s_cmp_eq_u32 s49, 0\n" \
  "s_cbranch_scc0 LHA_%=\n" \
  "LOA_%=:\n" \
  "buffer_load_dword v36, v104, s[36:39], 0 offen sc1\n" \
  "s_waitcnt vmcnt(0)\n" \
  "v_cmp_gt_u32 vcc, 96, v36\n" \
  "s_cbranch_vccz LOXA_%=\n" \
  "s_add_u32 s60, s60, 1\n" \
  "s_cmp_gt_u32 s60, 20000\n" \
  "s_cbranch_scc1 LOXA_%=\n" \
  "s_sleep 4\n" \
  "s_branch LOA_%=\n" \
  "LOXA_%=:\n" \
  "LOB_%=:\n" \
  "buffer_load_dword v36, v107, s[36:39], 0 offen sc1\n" \
  "s_waitcnt vmcnt(0)\n" \
  "v_cmp_gt_u32 vcc, 8, v36\n" \
  "s_cbranch_vccz LOXB_%=\n" \
  "s_add_u32 s60, s60, 1\n" \
  "s_cmp_gt_u32 s60, 20000\n" \
  "s_cbranch_scc1 LOXB_%=\n" \
  "s_sleep 2\n" \
  "s_branch LOB_%=\n" \
  "LOXB_%=:\n" \
  "v_cmp_gt_u32 vcc, 16, v36\n" \
  "s_cmp_eq_u64 vcc, 0\n" \
  "s_cselect_b32 s53, 135, s53\n" \
  "s_mov_b32 s95, s62\n" \
  "s_mov_b32 s96, s86\n" \
  "s_memrealtime s[74:75]\n" \
  "s_memtime s[76:77]\n" \
  "s_waitcnt lgkmcnt(0)\n" \
  "s_sub_u32 s71, s76, s96\n" \
  "s_sub_u32 s72, s74, s95\n" \
  "v_cvt_f32_u32 v36, s71\n" \
  "v_cvt_f32_u32 v37, s72\n" \
  "v_rcp_f32 v37, v37\n" \
  "v_mul_f32 v36, 0x43800000, v36\n" \
  "v_mul_f32 v36, v36, v37\n" \
  "v_cvt_u32_f32 v36, v36\n" \
  "s_nop 0\n" \
  "v_readfirstlane_b32 s71, v36\n" \
  "s_min_u32 s90, s71, 5950\n" \
  "v_cvt_f32_u32 v36, s90\n" \
  "v_rcp_f32 v36, v36\n" \
  "v_mov_b32 v37, 0xc0b00000\n" \
  "v_fmac_f32 v37, 0x48cc6500, v36\n" \
  "v_cvt_u32_f32 v37, v37\n" \
  "v_lshl_or_b32 v37, v37, 8, 1\n" \
  "buffer_store_dword v37, v30, s[36:39], 0 offen sc1\n" \
  "s_add_u32 s71, s74, s53\n" \
  "s_or_b32 s71, s71, 1\n" \
  "v_mov_b32 v36, s71\n" \
  "buffer_store_dword v36, v106, s[36:39], 0 offen sc1\n" \
  "s_nop 1\n" \
  "LHA_%=:\n" \
  "LOC_%=:\n" \
  "buffer_load_dword v104, v105, s[36:39], 0 offen sc1\n" \
  "s_waitcnt vmcnt(0)\n" \
  "v_cmp_gt_u32 vcc, 1, v104\n" \
  "s_cbranch_vccz LOXC_%=\n" \
  "s_add_u32 s60, s60, 1\n" \
  "s_cmp_gt_u32 s60, 20000\n" \
  "s_cbranch_scc1 LOXC_%=\n" \
  "s_sleep 4\n" \
  "s_branch LOC_%=\n" \
  "LOXC_%=:\n" \
  "s_mov_b32 s95, s62\n" \
  "s_mov_b32 s96, s86\n" \
  "s_memrealtime s[74:75]\n" \
  "s_memtime s[76:77]\n" \
  "s_waitcnt lgkmcnt(0)\n" \
  "s_sub_u32 s71, s76, s96\n" \
  "s_sub_u32 s72, s74, s95\n" \
  "v_cvt_f32_u32 v36, s71\n" \
  "v_cvt_f32_u32 v37, s72\n" \
  "v_rcp_f32 v37, v37\n" \
  "v_mul_f32 v36, 0x43800000, v36\n" \
  "v_mul_f32 v36, v36, v37\n" \
  "v_cvt_u32_f32 v36, v36\n" \
  "s_nop 0\n" \
  "v_readfirstlane_b32 s71, v36\n" \
  "s_min_u32 s90, s71, 5950\n" \
  "v_readfirstlane_b32 s88, v104\n" \
  "s_sub_u32 s71, s88, s74\n" \
  "s_add_u32 s71, s71, 100000\n" \
  "s_cmp_gt_u32 s71, 200000\n" \
  "s_cselect_b32 s88, s74, s88\n" \
  "s_mov_b32 s62, s88\n" \
  "LHD_%=:\n" \
  "s_memrealtime s[74:75]\n" \
  "s_waitcnt lgkmcnt(0)\n" \
  "s_sub_i32 s71, s88, s74\n" \
  "s_cmp_gt_i32 s71, 75\n" \
  "s_cbranch_scc1 LHD_%=\n" \
  "LOD_%=:\n" \
  "buffer_load_dword v36, v107, s[36:39], 0 offen sc1\n" \
  "s_waitcnt vmcnt(0)\n" \
  "v_cmp_gt_u32 vcc, 16, v36\n" \
  "s_cbranch_vccz LOXD_%=\n" \
  "s_add_u32 s60, s60, 1\n" \
  "s_cmp_gt_u32 s60, 20000\n" \
  "s_cbranch_scc1 LOXD_%=\n" \
  "s_sleep 2\n" \
  "s_branch LOD_%=\n" \
  "LOXD_%=:\n" \
  "LHB_%=:\n" \
  "s_barrier\n" \
  "s_branch LPASS_%=\n" \
  "LFIN_%=:\n" \
  "s_waitcnt vmcnt(0) lgkmcnt(0)\n" \
  "s_barrier\n" \
  "s_and_b32 s71, s48, 3\n" \
  "s_cmp_eq_u32 s71, 3\n" \
  "s_cbranch_scc0 LEND_%=\n" \
  "v_mov_b32 v34, 64\n" \
  "buffer_store_dword v34, v1, s[36:39], 0 offen sc1\n" \
  "LEND_%=:\n" \
  "s_mov_b32 s57, s35\n"
#define PROJQ_CLOBBERS \
  "v9", "v10", "v11", "v12", "v13", "v14", "v15", "v16", "v17", "v18", "v19", "v20", "v21", "v22", "v23", "v24", "v25", "v26", "v27", "v28", "v29", \
  "v30", "v31", "v32", "v33", "v34", "v35", "v36", "v37", "v38", "v39", "v40", "v41", "v42", "v43", "v44", "v45", "v46", "v47", "v48", "v49", "v50", \
  "v51", "v52", "v53", "v54", "v55", "v56", "v57", "v58", "v59", "v60", "v61", "v62", "v63", "v64", "v65", "v66", "v67", "v68", "v69", "v70", "v71", \
  "v72", "v73", "v74", "v75", "v76", "v77", "v78", "v79", "v80", "v81", "v82", "v83", "v84", "v85", "v86", "v87", "v88", "v89", "v90", "v91", "v92", \
  "v93", "v94", "v95", "v100", "v101", "v102", "v103", "v104", "v105", "v106", "v107", "v108", "v109", "v110", "v111", "v112", "v113", "v114", "v115", \
  "v116", "v117", "v118", "v119", "v120", "v121", "v122", "v123", "v144", "v145", "v146", "v147", "v148", "v149", "v150", "v151", "v152", "v153", \
  "v154", "v155", "v156", "v157", "v158", "v159", "v160", "v161", "v162", "v163", "v164", "v165", "v166", "v167", "v168", "v169", "v170", "v171", \
  "v172", "v173", "v174", "v175", "v176", "v177", "v178", "v179", "v180", "v181", "v182", "v183", "v184", "v185", "v186", "v187", "v188", "v189", \
  "v190", "v191", "v192", "v193", "v194", "v195", "v196", "v197", "v198", "v199", "v200", "v201", "v202", "v203", "v204", "v205", "v206", "v207", \
  "v208", "v209", "v210", "v211", "v212", "v213", "v214", "v215", "v216", "v217", "v218", "v219", "v220", "v221", "v222", "v223", "v224", "v225", \
  "v226", "v227", "v228", "v229", "v230", "v231", "v232", "v233", "v234", "v235", "v236", "v237", "v238", "v239", "v240", "v241", "v242", "v243", \
  "v244", "v245", "v246", "v247", "v248", "v249", "v250", "v251", "s52", "s56", "s57", "s58", "s59", "s60", "s61", "s62", "s63", "s64", "s65", "s68", \
  "s69", "s70", "s71", "s72", "s73"
#define PROJQ_ASM \
  "s_waitcnt vmcnt(0)\n" \
  "s_barrier\n" \
  "ds_read_b128 v[32:35], v0 offset:0\n" \
  "ds_read_b128 v[36:39], v0 offset:20480\n" \
  "ds_read_b128 v[40:43], v0 offset:1024\n" \
  "ds_read_b128 v[44:47], v0 offset:21504\n" \
  "v_mov_b32 v12, 2\n" \
  "v_add_u32 v9, 40960, v0\n" \
  "v_add_u32 v10, 81920, v0\n" \
  "s_mov_b32 s68, 81920\n" \
  "s_mov_b32 s52, 0\n" \
  "s_mov_b32 s65, 0\n" \
  "LL_%=:\n" \
  "s_waitcnt lgkmcnt(0)\n" \
  "v_mfma_f32_16x16x32_bf16 v[56:59], a[0:3], v[32:35], 0\n" \
  "s_add_u32 s63, s52, s54\n" \
  "ds_read_b128 v[48:51], v0 offset:2048\n" \
  "v_mfma_f32_16x16x32_bf16 v[68:71], a[0:3], v[36:39], 0\n" \
  "s_sub_u32 s64, s63, 2\n" \
  "ds_read_b128 v[52:55], v0 offset:22528\n" \
  "v_mfma_f32_16x16x32_bf16 v[60:63], a[80:83], v[32:35], 0\n" \
  "v_cvt_pk_bf16_f32 v16, v100, v101\n" \
  "v_cvt_pk_bf16_f32 v17, v102, v103\n" \
  "v_mfma_f32_16x16x32_bf16 v[72:75], a[80:83], v[36:39], 0\n" \
  "v_cvt_pk_bf16_f32 v18, v104, v105\n" \
  "v_cvt_pk_bf16_f32 v19, v106, v107\n" \
  "v_mfma_f32_16x16x32_bf16 v[64:67], a[160:163], v[32:35], 0\n" \
  "v_cvt_pk_bf16_f32 v20, v112, v113\n" \
  "v_cvt_pk_bf16_f32 v21, v114, v115\n" \
  "v_mfma_f32_16x16x32_bf16 v[76:79], a[160:163], v[36:39], 0\n" \
  "v_cvt_pk_bf16_f32 v22, v116, v117\n" \
  "v_cvt_pk_bf16_f32 v23, v118, v119\n" \
  "v_mfma_f32_16x16x32_bf16 v[56:59], a[4:7], v[40:43], v[56:59]\n" \
  "ds_read_b128 v[32:35], v0 offset:3072\n" \
  "v_mfma_f32_16x16x32_bf16 v[68:71], a[4:7], v[44:47], v[68:71]\n" \
  "s_barrier\n" \
  "ds_read_b128 v[36:39], v0 offset:23552\n" \
  "v_mfma_f32_16x16x32_bf16 v[60:63], a[84:87], v[40:43], v[60:63]\n" \
  "v_cvt_pk_bf16_f32 v24, v108, v109\n" \
  "v_cvt_pk_bf16_f32 v25, v110, v111\n" \
  "v_mfma_f32_16x16x32_bf16 v[72:75], a[84:87], v[44:47], v[72:75]\n" \
  "v_cvt_pk_bf16_f32 v26, v120, v121\n" \
  "v_cvt_pk_bf16_f32 v27, v122, v123\n" \
  "v_mfma_f32_16x16x32_bf16 v[64:67], a[164:167], v[40:43], v[64:67]\n" \
  "ds_write_b128 v2, v[16:19] offset:12288\n" \
  "v_mfma_f32_16x16x32_bf16 v[76:79], a[164:167], v[44:47], v[76:79]\n" \
  "s_waitcnt lgkmcnt(4)\n" \
  "v_mfma_f32_16x16x32_bf16 v[56:59], a[8:11], v[48:51], v[56:59]\n" \
  "s_waitcnt lgkmcnt(3)\n" \
  "ds_read_b128 v[40:43], v0 offset:4096\n" \
  "v_mfma_f32_16x16x32_bf16 v[68:71], a[8:11], v[52:55], v[68:71]\n" \
  "ds_read_b128 v[44:47], v0 offset:24576\n" \
  "v_mfma_f32_16x16x32_bf16 v[60:63], a[88:91], v[48:51], v[60:63]\n" \
  "ds_write_b128 v2, v[20:23] offset:13312\n" \
  "v_mfma_f32_16x16x32_bf16 v[72:75], a[88:91], v[52:55], v[72:75]\n" \
  "v_mfma_f32_16x16x32_bf16 v[64:67], a[168:171], v[48:51], v[64:67]\n" \
  "v_mfma_f32_16x16x32_bf16 v[76:79], a[168:171], v[52:55], v[76:79]\n" \
  "s_add_u32 s70, s68, 4096\n" \
  "s_add_u32 s71, s68, 8192\n" \
  "s_waitcnt lgkmcnt(5)\n" \
  "v_mfma_f32_16x16x32_bf16 v[56:59], a[12:15], v[32:35], v[56:59]\n" \
  "s_waitcnt lgkmcnt(4)\n" \
  "ds_write_b128 v2, v[24:27] offset:14336\n" \
  "ds_read_b128 v[48:51], v0 offset:5120\n" \
  "v_mfma_f32_16x16x32_bf16 v[68:71], a[12:15], v[36:39], v[68:71]\n" \
  "s_add_u32 m0, s53, 81920\n" \
  "ds_read_b128 v[52:55], v0 offset:25600\n" \
  "v_mfma_f32_16x16x32_bf16 v[60:63], a[92:95], v[32:35], v[60:63]\n" \
  "buffer_load_dwordx4 v1, s[36:39], s68 offen sc1 lds\n" \
  "v_mfma_f32_16x16x32_bf16 v[72:75], a[92:95], v[36:39], v[72:75]\n" \
  "v_mfma_f32_16x16x32_bf16 v[64:67], a[172:175], v[32:35], v[64:67]\n" \
  "v_mfma_f32_16x16x32_bf16 v[76:79], a[172:175], v[36:39], v[76:79]\n" \
  "s_waitcnt lgkmcnt(5)\n" \
  "v_mfma_f32_16x16x32_bf16 v[56:59], a[16:19], v[40:43], v[56:59]\n" \
  "s_waitcnt lgkmcnt(4)\n" \
  "ds_read_b128 v[32:35], v0 offset:6144\n" \
  "v_mfma_f32_16x16x32_bf16 v[68:71], a[16:19], v[44:47], v[68:71]\n" \
  "ds_read_b128 v[36:39], v0 offset:26624\n" \
  "v_mfma_f32_16x16x32_bf16 v[60:63], a[96:99], v[40:43], v[60:63]\n" \
  "buffer_load_dwordx4 v1, s[36:39], s68 offen offset:1024 sc1 lds\n" \
  "v_mfma_f32_16x16x32_bf16 v[72:75], a[96:99], v[44:47], v[72:75]\n" \
  "v_mfma_f32_16x16x32_bf16 v[64:67], a[176:179], v[40:43], v[64:67]\n" \
  "v_readlane_b32 s61, v126, s64\n" \
  "v_mfma_f32_16x16x32_bf16 v[76:79], a[176:179], v[44:47], v[76:79]\n" \
  "s_sub_u32 s64, s52, 2\n" \
  "s_cmp_lt_u32 s64, 56\n" \
  "s_waitcnt lgkmcnt(3)\n" \
  "v_mfma_f32_16x16x32_bf16 v[56:59], a[20:23], v[48:51], v[56:59]\n" \
  "s_cselect_b32 s61, s61, 1073741824\n" \
  "s_waitcnt lgkmcnt(2)\n" \
  "ds_read_b128 v[40:43], v0 offset:7168\n" \
  "v_mfma_f32_16x16x32_bf16 v[68:71], a[20:23], v[52:55], v[68:71]\n" \
  "ds_read_b128 v[44:47], v0 offset:27648\n" \
  "v_mfma_f32_16x16x32_bf16 v[60:63], a[100:103], v[48:51], v[60:63]\n" \
  "buffer_load_dwordx4 v1, s[36:39], s68 offen offset:2048 sc1 lds\n" \
  "v_mfma_f32_16x16x32_bf16 v[72:75], a[100:103], v[52:55], v[72:75]\n" \
  "s_cmp_ge_u32 s52, 4\n" \
  "s_cselect_b32 s62, 0, 1073741824\n" \
  "v_mfma_f32_16x16x32_bf16 v[64:67], a[180:183], v[48:51], v[64:67]\n" \
  "s_waitcnt vmcnt(3) lgkmcnt(4)\n" \
  "s_barrier\n" \
  "v_mfma_f32_16x16x32_bf16 v[76:79], a[180:183], v[52:55], v[76:79]\n" \
  "buffer_atomic_add v12, v129, s[40:43], s62 offen\n" \
  "ds_read_b128 v[216:219], v3 offset:0\n" \
  "s_waitcnt lgkmcnt(4)\n" \
  "v_mfma_f32_16x16x32_bf16 v[56:59], a[24:27], v[32:35], v[56:59]\n" \
  "s_waitcnt lgkmcnt(3)\n" \
  "ds_read_b128 v[48:51], v0 offset:8192\n" \
  "ds_read_b128 v[220:223], v3 offset:3072\n" \
  "v_mfma_f32_16x16x32_bf16 v[68:71], a[24:27], v[36:39], v[68:71]\n" \
  "ds_read_b128 v[52:55], v0 offset:28672\n" \
  "ds_read_b128 v[224:227], v3 offset:6144\n" \
  "v_mfma_f32_16x16x32_bf16 v[60:63], a[104:107], v[32:35], v[60:63]\n" \
  "buffer_load_dwordx4 v1, s[36:39], s68 offen offset:3072 sc1 lds\n" \
  "ds_read_b128 v[228:231], v3 offset:9216\n" \
  "v_mfma_f32_16x16x32_bf16 v[72:75], a[104:107], v[36:39], v[72:75]\n" \
  "ds_read_b64 v[232:233], v4 offset:0\n" \
  "ds_read_b64 v[234:235], v4 offset:3072\n" \
  "v_mfma_f32_16x16x32_bf16 v[64:67], a[184:187], v[32:35], v[64:67]\n" \
  "ds_read_b64 v[236:237], v4 offset:6144\n" \
  "ds_read_b64 v[238:239], v4 offset:9216\n" \
  "v_mfma_f32_16x16x32_bf16 v[76:79], a[184:187], v[36:39], v[76:79]\n" \
  "s_waitcnt lgkmcnt(11)\n" \
  "v_swap_b32 v162, v184\n" \
  "v_mfma_f32_16x16x32_bf16 v[56:59], a[28:31], v[40:43], v[56:59]\n" \
  "s_waitcnt lgkmcnt(10)\n" \
  "ds_read_b128 v[32:35], v0 offset:9216\n" \
  "v_mfma_f32_16x16x32_bf16 v[68:71], a[28:31], v[44:47], v[68:71]\n" \
  "s_add_u32 m0, s53, 86016\n" \
  "ds_read_b128 v[36:39], v0 offset:29696\n" \
  "v_mfma_f32_16x16x32_bf16 v[60:63], a[108:111], v[40:43], v[60:63]\n" \
  "buffer_load_dwordx4 v1, s[36:39], s70 offen sc1 lds\n" \
  "v_mfma_f32_16x16x32_bf16 v[72:75], a[108:111], v[44:47], v[72:75]\n" \
  "v_swap_b32 v163, v185\n" \
  "v_mfma_f32_16x16x32_bf16 v[64:67], a[188:191], v[40:43], v[64:67]\n" \
  "v_swap_b32 v166, v188\n" \
  "v_mfma_f32_16x16x32_bf16 v[76:79], a[188:191], v[44:47], v[76:79]\n" \
  "s_waitcnt lgkmcnt(10)\n" \
  "v_swap_b32 v167, v189\n" \
  "v_mfma_f32_16x16x32_bf16 v[56:59], a[32:35], v[48:51], v[56:59]\n" \
  "s_waitcnt lgkmcnt(8)\n" \
  "ds_read_b128 v[40:43], v0 offset:10240\n" \
  "v_mfma_f32_16x16x32_bf16 v[68:71], a[32:35], v[52:55], v[68:71]\n" \
  "ds_read_b128 v[44:47], v0 offset:30720\n" \
  "v_mfma_f32_16x16x32_bf16 v[240:243], v[192:195], v[216:219], v[132:135]\n" \
  "v_mfma_f32_16x16x32_bf16 v[244:247], v[144:147], v[216:219], v[136:139]\n" \
  "v_mfma_f32_16x16x32_bf16 v[248:251], v[168:171], v[216:219], v[140:143]\n" \
  "v_mfma_f32_16x16x32_bf16 v[60:63], a[112:115], v[48:51], v[60:63]\n" \
  "buffer_load_dwordx4 v1, s[36:39], s70 offen offset:1024 sc1 lds\n" \
  "v_mfma_f32_16x16x32_bf16 v[240:243], v[196:199], v[220:223], v[240:243]\n" \
  "v_mfma_f32_16x16x32_bf16 v[244:247], v[148:151], v[220:223], v[244:247]\n" \
  "v_mfma_f32_16x16x32_bf16 v[248:251], v[172:175], v[220:223], v[248:251]\n" \
  "v_mfma_f32_16x16x32_bf16 v[72:75], a[112:115], v[52:55], v[72:75]\n" \
  "s_waitcnt lgkmcnt(9)\n" \
  "v_mfma_f32_16x16x32_bf16 v[240:243], v[200:203], v[224:227], v[240:243]\n" \
  "v_mfma_f32_16x16x32_bf16 v[244:247], v[152:155], v[224:227], v[244:247]\n" \
  "v_mfma_f32_16x16x32_bf16 v[248:251], v[176:179], v[224:227], v[248:251]\n" \
  "v_mfma_f32_16x16x32_bf16 v[64:67], a[192:195], v[48:51], v[64:67]\n" \
  "s_waitcnt lgkmcnt(8)\n" \
  "v_mfma_f32_16x16x32_bf16 v[240:243], v[204:207], v[228:231], v[240:243]\n" \
  "v_mfma_f32_16x16x32_bf16 v[244:247], v[156:159], v[228:231], v[244:247]\n" \
  "v_mfma_f32_16x16x32_bf16 v[248:251], v[180:183], v[228:231], v[248:251]\n" \
  "v_mfma_f32_16x16x32_bf16 v[76:79], a[192:195], v[52:55], v[76:79]\n" \
  "s_waitcnt lgkmcnt(6)\n" \
  "v_mfma_f32_16x16x32_bf16 v[240:243], v[208:211], v[232:235], v[240:243]\n" \
  "v_mfma_f32_16x16x32_bf16 v[244:247], v[160:163], v[232:235], v[244:247]\n" \
  "v_mfma_f32_16x16x32_bf16 v[248:251], v[184:187], v[232:235], v[248:251]\n" \
  "s_waitcnt lgkmcnt(3)\n" \
  "v_mfma_f32_16x16x32_bf16 v[56:59], a[36:39], v[32:35], v[56:59]\n" \
  "ds_read_b128 v[48:51], v0 offset:11264\n" \
  "v_mfma_f32_16x16x32_bf16 v[240:243], v[212:215], v[236:239], v[240:243]\n" \
  "v_mfma_f32_16x16x32_bf16 v[244:247], v[164:167], v[236:239], v[244:247]\n" \
  "v_mfma_f32_16x16x32_bf16 v[248:251], v[188:191], v[236:239], v[248:251]\n" \
  "s_waitcnt lgkmcnt(3)\n" \
  "v_mfma_f32_16x16x32_bf16 v[68:71], a[36:39], v[36:39], v[68:71]\n" \
  "ds_read_b128 v[52:55], v0 offset:31744\n" \
  "v_mfma_f32_16x16x32_bf16 v[60:63], a[116:119], v[32:35], v[60:63]\n" \
  "buffer_load_dwordx4 v1, s[36:39], s70 offen offset:2048 sc1 lds\n" \
  "v_mfma_f32_16x16x32_bf16 v[72:75], a[116:119], v[36:39], v[72:75]\n" \
  "v_cvt_pk_bf16_f32 v80, v240, v241\n" \
  "v_cvt_pk_bf16_f32 v81, v242, v243\n" \
  "v_mfma_f32_16x16x32_bf16 v[64:67], a[196:199], v[32:35], v[64:67]\n" \
  "v_cvt_pk_bf16_f32 v82, v244, v245\n" \
  "v_cvt_pk_bf16_f32 v83, v246, v247\n" \
  "v_mfma_f32_16x16x32_bf16 v[76:79], a[196:199], v[36:39], v[76:79]\n" \
  "s_waitcnt lgkmcnt(3)\n" \
  "v_cvt_pk_bf16_f32 v84, v248, v249\n" \
  "v_cvt_pk_bf16_f32 v85, v250, v251\n" \
  "v_mfma_f32_16x16x32_bf16 v[56:59], a[40:43], v[40:43], v[56:59]\n" \
  "s_waitcnt lgkmcnt(2)\n" \
  "ds_read_b128 v[32:35], v0 offset:12288\n" \
  "v_lshlrev_b32 v240, 16, v80\n" \
  "v_mfma_f32_16x16x32_bf16 v[68:71], a[40:43], v[44:47], v[68:71]\n" \
  "ds_read_b128 v[36:39], v0 offset:32768\n" \
  "v_and_b32 v241, 0xffff0000, v80\n" \
  "v_mfma_f32_16x16x32_bf16 v[60:63], a[120:123], v[40:43], v[60:63]\n" \
  "buffer_load_dwordx4 v1, s[36:39], s70 offen offset:3072 sc1 lds\n" \
  "v_mul_f32 v240, 0x3d93cd3a, v240\n" \
  "v_mfma_f32_16x16x32_bf16 v[72:75], a[120:123], v[44:47], v[72:75]\n" \
  "v_mul_f32 v241, 0x3d93cd3a, v241\n" \
  "v_cvt_pk_bf16_f32 v80, v240, v241\n" \
  "v_mfma_f32_16x16x32_bf16 v[64:67], a[200:203], v[40:43], v[64:67]\n" \
  "v_lshlrev_b32 v240, 16, v80\n" \
  "v_and_b32 v241, 0xffff0000, v80\n" \
  "v_mfma_f32_16x16x32_bf16 v[76:79], a[200:203], v[44:47], v[76:79]\n" \
  "s_waitcnt lgkmcnt(3)\n" \
  "v_lshlrev_b32 v242, 16, v81\n" \
  "v_and_b32 v243, 0xffff0000, v81\n" \
  "v_mfma_f32_16x16x32_bf16 v[56:59], a[44:47], v[48:51], v[56:59]\n" \
  "s_waitcnt lgkmcnt(2)\n" \
  "ds_read_b128 v[40:43], v0 offset:13312\n" \
  "v_mul_f32 v242, 0x3d93cd3a, v242\n" \
  "v_mfma_f32_16x16x32_bf16 v[68:71], a[44:47], v[52:55], v[68:71]\n" \
  "s_add_u32 m0, s53, 90112\n" \
  "ds_read_b128 v[44:47], v0 offset:33792\n" \
  "v_mul_f32 v243, 0x3d93cd3a, v243\n" \
  "v_mfma_f32_16x16x32_bf16 v[60:63], a[124:127], v[48:51], v[60:63]\n" \
  "buffer_load_dwordx4 v1, s[36:39], s71 offen sc1 lds\n" \
  "v_cvt_pk_bf16_f32 v81, v242, v243\n" \
  "v_mfma_f32_16x16x32_bf16 v[72:75], a[124:127], v[52:55], v[72:75]\n" \
  "v_lshlrev_b32 v242, 16, v81\n" \
  "v_and_b32 v243, 0xffff0000, v81\n" \
  "v_mfma_f32_16x16x32_bf16 v[64:67], a[204:207], v[48:51], v[64:67]\n" \
  "v_readlane_b32 s56, v124, s63\n" \
  "v_mfma_f32_16x16x32_bf16 v[76:79], a[204:207], v[52:55], v[76:79]\n" \
  "s_waitcnt lgkmcnt(3)\n" \
  "v_lshlrev_b32 v244, 16, v82\n" \
  "v_and_b32 v245, 0xffff0000, v82\n" \
  "v_mfma_f32_16x16x32_bf16 v[56:59], a[48:51], v[32:35], v[56:59]\n" \
  "s_waitcnt lgkmcnt(2)\n" \
  "ds_read_b128 v[48:51], v0 offset:14336\n" \
  "v_mul_f32 v244, 0x3d93cd3a, v244\n" \
  "v_mfma_f32_16x16x32_bf16 v[68:71], a[48:51], v[36:39], v[68:71]\n" \
  "ds_read_b128 v[52:55], v0 offset:34816\n" \
  "v_mul_f32 v245, 0x3d93cd3a, v245\n" \
  "v_mfma_f32_16x16x32_bf16 v[60:63], a[128:131], v[32:35], v[60:63]\n" \
  "buffer_load_dwordx4 v1, s[36:39], s71 offen offset:1024 sc1 lds\n" \
  "v_cvt_pk_bf16_f32 v82, v244, v245\n" \
  "v_mfma_f32_16x16x32_bf16 v[72:75], a[128:131], v[36:39], v[72:75]\n" \
  "s_add_u32 s57, s56, 16384\n" \
  "v_lshlrev_b32 v244, 16, v82\n" \
  "v_and_b32 v245, 0xffff0000, v82\n" \
  "v_mfma_f32_16x16x32_bf16 v[64:67], a[208:211], v[32:35], v[64:67]\n" \
  "s_add_u32 s58, s56, 32768\n" \
  "v_lshlrev_b32 v246, 16, v83\n" \
  "v_and_b32 v247, 0xffff0000, v83\n" \
  "v_mfma_f32_16x16x32_bf16 v[76:79], a[208:211], v[36:39], v[76:79]\n" \
  "s_add_u32 s68, s68, 40960\n" \
  "s_add_u32 s59, s56, 49152\n" \
  "s_waitcnt lgkmcnt(3)\n" \
  "v_mul_f32 v246, 0x3d93cd3a, v246\n" \
  "v_mfma_f32_16x16x32_bf16 v[56:59], a[52:55], v[40:43], v[56:59]\n" \
  "s_waitcnt lgkmcnt(2)\n" \
  "ds_read_b128 v[32:35], v0 offset:15360\n" \
  "v_mul_f32 v247, 0x3d93cd3a, v247\n" \
  "v_mfma_f32_16x16x32_bf16 v[68:71], a[52:55], v[44:47], v[68:71]\n" \
  "ds_read_b128 v[36:39], v0 offset:35840\n" \
  "v_cvt_pk_bf16_f32 v83, v246, v247\n" \
  "v_mfma_f32_16x16x32_bf16 v[60:63], a[132:135], v[40:43], v[60:63]\n" \
  "v_lshlrev_b32 v246, 16, v83\n" \
  "v_and_b32 v247, 0xffff0000, v83\n" \
  "v_mfma_f32_16x16x32_bf16 v[72:75], a[132:135], v[44:47], v[72:75]\n" \
  "v_lshlrev_b32 v248, 16, v84\n" \
  "v_and_b32 v249, 0xffff0000, v84\n" \
  "v_mfma_f32_16x16x32_bf16 v[64:67], a[212:215], v[40:43], v[64:67]\n" \
  "v_mul_f32 v248, 0x3d93cd3a, v248\n" \
  "v_mul_f32 v249, 0x3d93cd3a, v249\n" \
  "v_mfma_f32_16x16x32_bf16 v[76:79], a[212:215], v[44:47], v[76:79]\n" \
  "buffer_load_dwordx4 v[144:147], v5, s[40:43], s56 offen offset:0 sc1\n" \
  "v_cvt_pk_bf16_f32 v84, v248, v249\n" \
  "s_waitcnt lgkmcnt(3)\n" \
  "v_mfma_f32_16x16x32_bf16 v[56:59], a[56:59], v[48:51], v[56:59]\n" \
  "s_waitcnt lgkmcnt(2)\n" \
  "ds_read_b128 v[40:43], v0 offset:16384\n" \
  "v_lshlrev_b32 v248, 16, v84\n" \
  "v_mfma_f32_16x16x32_bf16 v[68:71], a[56:59], v[52:55], v[68:71]\n" \
  "ds_read_b128 v[44:47], v0 offset:36864\n" \
  "v_and_b32 v249, 0xffff0000, v84\n" \
  "v_mfma_f32_16x16x32_bf16 v[60:63], a[136:139], v[48:51], v[60:63]\n" \
  "v_lshlrev_b32 v250, 16, v85\n" \
  "v_and_b32 v251, 0xffff0000, v85\n" \
  "v_mfma_f32_16x16x32_bf16 v[72:75], a[136:139], v[52:55], v[72:75]\n" \
  "v_mul_f32 v250, 0x3d93cd3a, v250\n" \
  "v_mul_f32 v251, 0x3d93cd3a, v251\n" \
  "v_mfma_f32_16x16x32_bf16 v[64:67], a[216:219], v[48:51], v[64:67]\n" \
  "v_cvt_pk_bf16_f32 v85, v250, v251\n" \
  "v_lshlrev_b32 v250, 16, v85\n" \
  "v_mfma_f32_16x16x32_bf16 v[76:79], a[216:219], v[52:55], v[76:79]\n" \
  "buffer_load_dwordx4 v[168:171], v5, s[40:43], s56 offen offset:1024 sc1\n" \
  "v_and_b32 v251, 0xffff0000, v85\n" \
  "s_waitcnt lgkmcnt(3)\n" \
  "v_mfma_f32_16x16x32_bf16 v[56:59], a[60:63], v[32:35], v[56:59]\n" \
  "s_waitcnt lgkmcnt(2)\n" \
  "ds_read_b128 v[48:51], v0 offset:17408\n" \
  "v_max3_f32 v80, v240, v241, v242\n" \
  "v_mfma_f32_16x16x32_bf16 v[68:71], a[60:63], v[36:39], v[68:71]\n" \
  "ds_read_b128 v[52:55], v0 offset:37888\n" \
  "v_max3_f32 v80, v80, v243, v244\n" \
  "v_mfma_f32_16x16x32_bf16 v[60:63], a[140:143], v[32:35], v[60:63]\n" \
  "v_max3_f32 v80, v80, v245, v246\n" \
  "v_max3_f32 v80, v80, v247, v248\n" \
  "v_mfma_f32_16x16x32_bf16 v[72:75], a[140:143], v[36:39], v[72:75]\n" \
  "v_max3_f32 v80, v80, v249, v250\n" \
  "v_max_f32 v80, v80, v251\n" \
  "v_mfma_f32_16x16x32_bf16 v[64:67], a[220:223], v[32:35], v[64:67]\n" \
  "v_mov_b32 v81, v80\n" \
  "v_mfma_f32_16x16x32_bf16 v[76:79], a[220:223], v[36:39], v[76:79]\n" \
  "buffer_load_dwordx4 v[148:151], v5, s[40:43], s57 offen offset:0 sc1\n" \
  "s_waitcnt lgkmcnt(3)\n" \
  "v_mfma_f32_16x16x32_bf16 v[56:59], a[64:67], v[40:43], v[56:59]\n" \
  "s_waitcnt lgkmcnt(2)\n" \
  "ds_read_b128 v[32:35], v0 offset:18432\n" \
  "v_mfma_f32_16x16x32_bf16 v[68:71], a[64:67], v[44:47], v[68:71]\n" \
  "ds_read_b128 v[36:39], v0 offset:38912\n" \
  "v_mfma_f32_16x16x32_bf16 v[60:63], a[144:147], v[40:43], v[60:63]\n" \
  "v_permlane16_swap_b32 v80, v81\n" \
  "v_mfma_f32_16x16x32_bf16 v[72:75], a[144:147], v[44:47], v[72:75]\n" \
  "v_max_f32 v80, v80, v81\n" \
  "v_mov_b32 v81, v80\n" \
  "v_mfma_f32_16x16x32_bf16 v[64:67], a[224:227], v[40:43], v[64:67]\n" \
  "v_mfma_f32_16x16x32_bf16 v[76:79], a[224:227], v[44:47], v[76:79]\n" \
  "buffer_load_dwordx4 v[172:175], v5, s[40:43], s57 offen offset:1024 sc1\n" \
  "s_waitcnt lgkmcnt(3)\n" \
  "v_mfma_f32_16x16x32_bf16 v[56:59], a[68:71], v[48:51], v[56:59]\n" \
  "s_waitcnt lgkmcnt(2)\n" \
  "ds_read_b128 v[40:43], v0 offset:19456\n" \
  "v_mfma_f32_16x16x32_bf16 v[68:71], a[68:71], v[52:55], v[68:71]\n" \
  "ds_read_b128 v[44:47], v0 offset:39936\n" \
  "v_mfma_f32_16x16x32_bf16 v[60:63], a[148:151], v[48:51], v[60:63]\n" \
  "v_permlane32_swap_b32 v80, v81\n" \
  "v_mfma_f32_16x16x32_bf16 v[72:75], a[148:151], v[52:55], v[72:75]\n" \
  "v_max_f32 v80, v80, v81\n" \
  "v_mul_f32 v80, 0x3fb8aa3b, v80\n" \
  "v_mfma_f32_16x16x32_bf16 v[64:67], a[228:231], v[48:51], v[64:67]\n" \
  "v_mov_b32 v82, 0x3fb8aa3b\n" \
  "v_fma_f32 v240, v240, v82, -v80\n" \
  "v_mfma_f32_16x16x32_bf16 v[76:79], a[228:231], v[52:55], v[76:79]\n" \
  "buffer_load_dwordx4 v[152:155], v5, s[40:43], s58 offen offset:0 sc1\n" \
  "v_fma_f32 v241, v241, v82, -v80\n" \
  "s_waitcnt vmcnt(24)\n" \
  "s_barrier\n" \
  "s_waitcnt lgkmcnt(3)\n" \
  "v_mfma_f32_16x16x32_bf16 v[56:59], a[72:75], v[32:35], v[56:59]\n" \
  "s_waitcnt lgkmcnt(2)\n" \
  "ds_read_b128 v[48:51], v9 offset:0\n" \
  "v_fma_f32 v242, v242, v82, -v80\n" \
  "v_mfma_f32_16x16x32_bf16 v[68:71], a[72:75], v[36:39], v[68:71]\n" \
  "ds_read_b128 v[52:55], v9 offset:20480\n" \
  "v_fma_f32 v243, v243, v82, -v80\n" \
  "v_mfma_f32_16x16x32_bf16 v[60:63], a[152:155], v[32:35], v[60:63]\n" \
  "v_fma_f32 v244, v244, v82, -v80\n" \
  "v_fma_f32 v245, v245, v82, -v80\n" \
  "v_mfma_f32_16x16x32_bf16 v[72:75], a[152:155], v[36:39], v[72:75]\n" \
  "v_fma_f32 v246, v246, v82, -v80\n" \
  "v_fma_f32 v247, v247, v82, -v80\n" \
  "v_mfma_f32_16x16x32_bf16 v[64:67], a[232:235], v[32:35], v[64:67]\n" \
  "v_fma_f32 v248, v248, v82, -v80\n" \
  "v_fma_f32 v249, v249, v82, -v80\n" \
  "v_mfma_f32_16x16x32_bf16 v[76:79], a[232:235], v[36:39], v[76:79]\n" \
  "buffer_load_dwordx4 v[176:179], v5, s[40:43], s58 offen offset:1024 sc1\n" \
  "v_fma_f32 v250, v250, v82, -v80\n" \
  "s_waitcnt lgkmcnt(3)\n" \
  "v_mfma_f32_16x16x32_bf16 v[56:59], a[76:79], v[40:43], v[56:59]\n" \
  "s_waitcnt lgkmcnt(2)\n" \
  "ds_read_b128 v[32:35], v9 offset:1024\n" \
  "v_fma_f32 v251, v251, v82, -v80\n" \
  "v_mfma_f32_16x16x32_bf16 v[68:71], a[76:79], v[44:47], v[68:71]\n" \
  "ds_read_b128 v[36:39], v9 offset:21504\n" \
  "v_mfma_f32_16x16x32_bf16 v[60:63], a[156:159], v[40:43], v[60:63]\n" \
  "v_exp_f32 v240, v240\n" \
  "v_mfma_f32_16x16x32_bf16 v[72:75], a[156:159], v[44:47], v[72:75]\n" \
  "v_exp_f32 v241, v241\n" \
  "v_mfma_f32_16x16x32_bf16 v[64:67], a[236:239], v[40:43], v[64:67]\n" \
  "v_exp_f32 v242, v242\n" \
  "v_mfma_f32_16x16x32_bf16 v[76:79], a[236:239], v[44:47], v[76:79]\n" \
  "v_exp_f32 v243, v243\n" \
  "s_add_u32 s52, s52, 1\n" \
  "s_waitcnt lgkmcnt(3)\n" \
  "v_mfma_f32_16x16x32_bf16 v[100:103], a[0:3], v[48:51], 0\n" \
  "s_waitcnt lgkmcnt(2)\n" \
  "ds_read_b128 v[40:43], v9 offset:2048\n" \
  "v_mfma_f32_16x16x32_bf16 v[112:115], a[0:3], v[52:55], 0\n" \
  "ds_read_b128 v[44:47], v9 offset:22528\n" \
  "v_mfma_f32_16x16x32_bf16 v[104:107], a[80:83], v[48:51], 0\n" \
  "v_cvt_pk_bf16_f32 v16, v56, v57\n" \
  "v_cvt_pk_bf16_f32 v17, v58, v59\n" \
  "v_mfma_f32_16x16x32_bf16 v[116:119], a[80:83], v[52:55], 0\n" \
  "v_cvt_pk_bf16_f32 v18, v60, v61\n" \
  "v_cvt_pk_bf16_f32 v19, v62, v63\n" \
  "v_mfma_f32_16x16x32_bf16 v[108:111], a[160:163], v[48:51], 0\n" \
  "v_cvt_pk_bf16_f32 v20, v68, v69\n" \
  "v_cvt_pk_bf16_f32 v21, v70, v71\n" \
  "v_mfma_f32_16x16x32_bf16 v[120:123], a[160:163], v[52:55], 0\n" \
  "v_cvt_pk_bf16_f32 v22, v72, v73\n" \
  "v_cvt_pk_bf16_f32 v23, v74, v75\n" \
  "buffer_load_dwordx4 v[156:159], v5, s[40:43], s59 offen offset:0 sc1\n" \
  "s_waitcnt lgkmcnt(3)\n" \
  "v_mfma_f32_16x16x32_bf16 v[100:103], a[4:7], v[32:35], v[100:103]\n" \
  "s_waitcnt lgkmcnt(2)\n" \
  "ds_read_b128 v[48:51], v9 offset:3072\n" \
  "v_mfma_f32_16x16x32_bf16 v[112:115], a[4:7], v[36:39], v[112:115]\n" \
  "s_barrier\n" \
  "ds_read_b128 v[52:55], v9 offset:23552\n" \
  "v_mfma_f32_16x16x32_bf16 v[104:107], a[84:87], v[32:35], v[104:107]\n" \
  "v_cvt_pk_bf16_f32 v24, v64, v65\n" \
  "v_cvt_pk_bf16_f32 v25, v66, v67\n" \
  "v_mfma_f32_16x16x32_bf16 v[116:119], a[84:87], v[36:39], v[116:119]\n" \
  "v_cvt_pk_bf16_f32 v26, v76, v77\n" \
  "v_cvt_pk_bf16_f32 v27, v78, v79\n" \
  "v_mfma_f32_16x16x32_bf16 v[108:111], a[164:167], v[32:35], v[108:111]\n" \
  "ds_write_b128 v2, v[16:19] offset:0\n" \
  "v_mfma_f32_16x16x32_bf16 v[120:123], a[164:167], v[36:39], v[120:123]\n" \
  "buffer_load_dwordx4 v[180:183], v5, s[40:43], s59 offen offset:1024 sc1\n" \
  "s_waitcnt lgkmcnt(4)\n" \
  "v_mfma_f32_16x16x32_bf16 v[100:103], a[8:11], v[40:43], v[100:103]\n" \
  "s_waitcnt lgkmcnt(3)\n" \
  "ds_read_b128 v[32:35], v9 offset:4096\n" \
  "v_mfma_f32_16x16x32_bf16 v[112:115], a[8:11], v[44:47], v[112:115]\n" \
  "ds_read_b128 v[36:39], v9 offset:24576\n" \
  "v_mfma_f32_16x16x32_bf16 v[104:107], a[88:91], v[40:43], v[104:107]\n" \
  "ds_write_b128 v2, v[20:23] offset:1024\n" \
  "v_mfma_f32_16x16x32_bf16 v[116:119], a[88:91], v[44:47], v[116:119]\n" \
  "v_exp_f32 v244, v244\n" \
  "v_mfma_f32_16x16x32_bf16 v[108:111], a[168:171], v[40:43], v[108:111]\n" \
  "v_exp_f32 v245, v245\n" \
  "v_mfma_f32_16x16x32_bf16 v[120:123], a[168:171], v[44:47], v[120:123]\n" \
  "s_add_u32 s70, s68, 4096\n" \
  "s_add_u32 s71, s68, 8192\n" \
  "buffer_load_dwordx4 v[160:163], v5, s[40:43], s56 offen offset:2048 sc1\n" \
  "s_waitcnt lgkmcnt(5)\n" \
  "v_mfma_f32_16x16x32_bf16 v[100:103], a[12:15], v[48:51], v[100:103]\n" \
  "s_waitcnt lgkmcnt(4)\n" \
  "ds_write_b128 v2, v[24:27] offset:2048\n" \
  "ds_read_b128 v[40:43], v9 offset:5120\n" \
  "v_mfma_f32_16x16x32_bf16 v[112:115], a[12:15], v[52:55], v[112:115]\n" \
  "s_add_u32 m0, s53, 0\n" \
  "ds_read_b128 v[44:47], v9 offset:25600\n" \
  "v_mfma_f32_16x16x32_bf16 v[104:107], a[92:95], v[48:51], v[104:107]\n" \
  "buffer_load_dwordx4 v1, s[36:39], s68 offen sc1 lds\n" \
  "v_mfma_f32_16x16x32_bf16 v[116:119], a[92:95], v[52:55], v[116:119]\n" \
  "v_exp_f32 v246, v246\n" \
  "v_mfma_f32_16x16x32_bf16 v[108:111], a[172:175], v[48:51], v[108:111]\n" \
  "v_exp_f32 v247, v247\n" \
  "v_mfma_f32_16x16x32_bf16 v[120:123], a[172:175], v[52:55], v[120:123]\n" \
  "buffer_load_dwordx4 v[184:187], v5, s[40:43], s57 offen offset:2048 sc1\n" \
  "s_waitcnt lgkmcnt(5)\n" \
  "v_mfma_f32_16x16x32_bf16 v[100:103], a[16:19], v[32:35], v[100:103]\n" \
  "s_waitcnt lgkmcnt(4)\n" \
  "ds_read_b128 v[48:51], v9 offset:6144\n" \
  "v_mfma_f32_16x16x32_bf16 v[112:115], a[16:19], v[36:39], v[112:115]\n" \
  "ds_read_b128 v[52:55], v9 offset:26624\n" \
  "v_mfma_f32_16x16x32_bf16 v[104:107], a[96:99], v[32:35], v[104:107]\n" \
  "buffer_load_dwordx4 v1, s[36:39], s68 offen offset:1024 sc1 lds\n" \
  "v_mfma_f32_16x16x32_bf16 v[116:119], a[96:99], v[36:39], v[116:119]\n" \
  "v_exp_f32 v248, v248\n" \
  "v_mfma_f32_16x16x32_bf16 v[108:111], a[176:179], v[32:35], v[108:111]\n" \
  "v_exp_f32 v249, v249\n" \
  "v_mfma_f32_16x16x32_bf16 v[120:123], a[176:179], v[36:39], v[120:123]\n" \
  "buffer_load_dwordx4 v[164:167], v5, s[40:43], s58 offen offset:2048 sc1\n" \
  "s_waitcnt lgkmcnt(3)\n" \
  "v_mfma_f32_16x16x32_bf16 v[100:103], a[20:23], v[40:43], v[100:103]\n" \
  "s_waitcnt lgkmcnt(2)\n" \
  "ds_read_b128 v[32:35], v9 offset:7168\n" \
  "v_mfma_f32_16x16x32_bf16 v[112:115], a[20:23], v[44:47], v[112:115]\n" \
  "ds_read_b128 v[36:39], v9 offset:27648\n" \
  "v_mfma_f32_16x16x32_bf16 v[104:107], a[100:103], v[40:43], v[104:107]\n" \
  "buffer_load_dwordx4 v1, s[36:39], s68 offen offset:2048 sc1 lds\n" \
  "v_mfma_f32_16x16x32_bf16 v[116:119], a[100:103], v[44:47], v[116:119]\n" \
  "v_exp_f32 v250, v250\n" \
  "v_mfma_f32_16x16x32_bf16 v[108:111], a[180:183], v[40:43], v[108:111]\n" \
  "v_exp_f32 v251, v251\n" \
  "v_mfma_f32_16x16x32_bf16 v[120:123], a[180:183], v[44:47], v[120:123]\n" \
  "buffer_load_dwordx4 v[188:191], v5, s[40:43], s59 offen offset:2048 sc1\n" \
  "v_add_f32 v80, v240, v241\n" \
  "s_waitcnt lgkmcnt(3)\n" \
  "v_mfma_f32_16x16x32_bf16 v[100:103], a[24:27], v[48:51], v[100:103]\n" \
  "s_waitcnt lgkmcnt(2)\n" \
  "ds_read_b128 v[40:43], v9 offset:8192\n" \
  "v_add_f32 v80, v80, v242\n" \
  "v_mfma_f32_16x16x32_bf16 v[112:115], a[24:27], v[52:55], v[112:115]\n" \
  "ds_read_b128 v[44:47], v9 offset:28672\n" \
  "v_add_f32 v80, v80, v243\n" \
  "v_mfma_f32_16x16x32_bf16 v[104:107], a[104:107], v[48:51], v[104:107]\n" \
  "buffer_load_dwordx4 v1, s[36:39], s68 offen offset:3072 sc1 lds\n" \
  "v_add_f32 v80, v80, v244\n" \
  "v_mfma_f32_16x16x32_bf16 v[116:119], a[104:107], v[52:55], v[116:119]\n" \
  "v_add_f32 v80, v80, v245\n" \
  "v_add_f32 v80, v80, v246\n" \
  "v_mfma_f32_16x16x32_bf16 v[108:111], a[184:187], v[48:51], v[108:111]\n" \
  "v_add_f32 v80, v80, v247\n" \
  "v_add_f32 v80, v80, v248\n" \
  "v_mfma_f32_16x16x32_bf16 v[120:123], a[184:187], v[52:55], v[120:123]\n" \
  "buffer_load_dword v11, v8, s[40:43], 0 offen sc1\n" \
  "v_add_f32 v80, v80, v249\n" \
  "s_waitcnt lgkmcnt(3)\n" \
  "v_mfma_f32_16x16x32_bf16 v[100:103], a[28:31], v[32:35], v[100:103]\n" \
  "s_waitcnt lgkmcnt(2)\n" \
  "ds_read_b128 v[48:51], v9 offset:9216\n" \
  "v_add_f32 v80, v80, v250\n" \
  "v_mfma_f32_16x16x32_bf16 v[112:115], a[28:31], v[36:39], v[112:115]\n" \
  "s_add_u32 m0, s53, 4096\n" \
  "ds_read_b128 v[52:55], v9 offset:29696\n" \
  "v_add_f32 v80, v80, v251\n" \
  "v_mfma_f32_16x16x32_bf16 v[104:107], a[108:111], v[32:35], v[104:107]\n" \
  "buffer_load_dwordx4 v1, s[36:39], s70 offen sc1 lds\n" \
  "v_mov_b32 v81, v80\n" \
  "v_mfma_f32_16x16x32_bf16 v[116:119], a[108:111], v[36:39], v[116:119]\n" \
  "v_mfma_f32_16x16x32_bf16 v[108:111], a[188:191], v[32:35], v[108:111]\n" \
  "v_readlane_b32 s60, v125, s63\n" \
  "v_mfma_f32_16x16x32_bf16 v[120:123], a[188:191], v[36:39], v[120:123]\n" \
  "s_waitcnt lgkmcnt(3)\n" \
  "v_permlane16_swap_b32 v80, v81\n" \
  "v_mfma_f32_16x16x32_bf16 v[100:103], a[32:35], v[40:43], v[100:103]\n" \
  "s_waitcnt lgkmcnt(2)\n" \
  "ds_read_b128 v[32:35], v9 offset:10240\n" \
  "v_add_f32 v80, v80, v81\n" \
  "v_mfma_f32_16x16x32_bf16 v[112:115], a[32:35], v[44:47], v[112:115]\n" \
  "ds_read_b128 v[36:39], v9 offset:30720\n" \
  "v_mov_b32 v81, v80\n" \
  "v_mfma_f32_16x16x32_bf16 v[104:107], a[112:115], v[40:43], v[104:107]\n" \
  "buffer_load_dwordx4 v1, s[36:39], s70 offen offset:1024 sc1 lds\n" \
  "v_mfma_f32_16x16x32_bf16 v[116:119], a[112:115], v[44:47], v[116:119]\n" \
  "v_permlane32_swap_b32 v80, v81\n" \
  "v_mfma_f32_16x16x32_bf16 v[108:111], a[192:195], v[40:43], v[108:111]\n" \
  "s_add_u32 s64, s52, 2\n" \
  "s_min_u32 s64, s64, 57\n" \
  "v_add_f32 v80, v80, v81\n" \
  "v_mfma_f32_16x16x32_bf16 v[120:123], a[192:195], v[44:47], v[120:123]\n" \
  "s_waitcnt lgkmcnt(3)\n" \
  "v_rcp_f32 v80, v80\n" \
  "v_mfma_f32_16x16x32_bf16 v[100:103], a[36:39], v[48:51], v[100:103]\n" \
  "s_waitcnt lgkmcnt(2)\n" \
  "ds_read_b128 v[40:43], v9 offset:11264\n" \
  "v_mul_f32 v244, v244, v80\n" \
  "v_mfma_f32_16x16x32_bf16 v[112:115], a[36:39], v[52:55], v[112:115]\n" \
  "ds_read_b128 v[44:47], v9 offset:31744\n" \
  "v_mul_f32 v245, v245, v80\n" \
  "v_mfma_f32_16x16x32_bf16 v[104:107], a[116:119], v[48:51], v[104:107]\n" \
  "buffer_load_dwordx4 v1, s[36:39], s70 offen offset:2048 sc1 lds\n" \
  "v_mul_f32 v246, v246, v80\n" \
  "v_mfma_f32_16x16x32_bf16 v[116:119], a[116:119], v[52:55], v[116:119]\n" \
  "v_mul_f32 v247, v247, v80\n" \
  "v_mul_f32 v248, v248, v80\n" \
  "v_mfma_f32_16x16x32_bf16 v[108:111], a[196:199], v[48:51], v[108:111]\n" \
  "v_mul_f32 v249, v249, v80\n" \
  "v_mul_f32 v250, v250, v80\n" \
  "v_mfma_f32_16x16x32_bf16 v[120:123], a[196:199], v[52:55], v[120:123]\n" \
  "s_waitcnt lgkmcnt(3)\n" \
  "v_mul_f32 v251, v251, v80\n" \
  "v_mul_f32 v240, v240, v80\n" \
  "v_mfma_f32_16x16x32_bf16 v[100:103], a[40:43], v[32:35], v[100:103]\n" \
  "s_waitcnt lgkmcnt(2)\n" \
  "ds_read_b128 v[48:51], v9 offset:12288\n" \
  "v_mul_f32 v241, v241, v80\n" \
  "v_mfma_f32_16x16x32_bf16 v[112:115], a[40:43], v[36:39], v[112:115]\n" \
  "ds_read_b128 v[52:55], v9 offset:32768\n" \
  "v_mul_f32 v242, v242, v80\n" \
  "v_mfma_f32_16x16x32_bf16 v[104:107], a[120:123], v[32:35], v[104:107]\n" \
  "buffer_load_dwordx4 v1, s[36:39], s70 offen offset:3072 sc1 lds\n" \
  "v_mul_f32 v243, v243, v80\n" \
  "v_mfma_f32_16x16x32_bf16 v[116:119], a[120:123], v[36:39], v[116:119]\n" \
  "v_cvt_pk_bf16_f32 v88, v244, v245\n" \
  "v_cvt_pk_bf16_f32 v89, v246, v247\n" \
  "v_mfma_f32_16x16x32_bf16 v[108:111], a[200:203], v[32:35], v[108:111]\n" \
  "v_cvt_pk_bf16_f32 v90, v248, v249\n" \
  "v_cvt_pk_bf16_f32 v91, v250, v251\n" \
  "v_mfma_f32_16x16x32_bf16 v[120:123], a[200:203], v[36:39], v[120:123]\n" \
  "s_waitcnt lgkmcnt(3)\n" \
  "v_cvt_pk_bf16_f32 v92, v240, v241\n" \
  "v_cvt_pk_bf16_f32 v93, v242, v243\n" \
  "v_mfma_f32_16x16x32_bf16 v[100:103], a[44:47], v[40:43], v[100:103]\n" \
  "ds_read_b128 v[32:35], v9 offset:13312\n" \
  "buffer_store_dwordx4 v[88:91], v127, s[40:43], s61 offen sc1\n" \
  "s_waitcnt lgkmcnt(3)\n" \
  "v_mfma_f32_16x16x32_bf16 v[112:115], a[44:47], v[44:47], v[112:115]\n" \
  "s_add_u32 m0, s53, 8192\n" \
  "ds_read_b128 v[36:39], v9 offset:33792\n" \
  "buffer_store_dwordx2 v[92:93], v128, s[40:43], s61 offen sc1\n" \
  "v_mfma_f32_16x16x32_bf16 v[104:107], a[124:127], v[40:43], v[104:107]\n" \
  "buffer_load_dwordx4 v1, s[36:39], s71 offen sc1 lds\n" \
  "v_mfma_f32_16x16x32_bf16 v[116:119], a[124:127], v[44:47], v[116:119]\n" \
  "v_mfma_f32_16x16x32_bf16 v[108:111], a[204:207], v[40:43], v[108:111]\n" \
  "v_mfma_f32_16x16x32_bf16 v[120:123], a[204:207], v[44:47], v[120:123]\n" \
  "s_waitcnt lgkmcnt(3)\n" \
  "v_mfma_f32_16x16x32_bf16 v[100:103], a[48:51], v[48:51], v[100:103]\n" \
  "s_waitcnt lgkmcnt(2)\n" \
  "ds_read_b128 v[40:43], v9 offset:14336\n" \
  "v_mfma_f32_16x16x32_bf16 v[112:115], a[48:51], v[52:55], v[112:115]\n" \
  "ds_read_b128 v[44:47], v9 offset:34816\n" \
  "v_mfma_f32_16x16x32_bf16 v[104:107], a[128:131], v[48:51], v[104:107]\n" \
  "buffer_load_dwordx4 v1, s[36:39], s71 offen offset:1024 sc1 lds\n" \
  "v_mfma_f32_16x16x32_bf16 v[116:119], a[128:131], v[52:55], v[116:119]\n" \
  "v_mfma_f32_16x16x32_bf16 v[108:111], a[208:211], v[48:51], v[108:111]\n" \
  "s_waitcnt vmcnt(8)\n" \
  "v_cmp_gt_u32 vcc, s64, v11\n" \
  "v_mfma_f32_16x16x32_bf16 v[120:123], a[208:211], v[52:55], v[120:123]\n" \
  "s_add_u32 s68, s68, 40960\n" \
  "s_waitcnt lgkmcnt(3)\n" \
  "v_mfma_f32_16x16x32_bf16 v[100:103], a[52:55], v[32:35], v[100:103]\n" \
  "s_waitcnt lgkmcnt(2)\n" \
  "ds_read_b128 v[48:51], v9 offset:15360\n" \
  "v_mfma_f32_16x16x32_bf16 v[112:115], a[52:55], v[36:39], v[112:115]\n" \
  "s_cbranch_vccnz LP1_%=\n" \
  "LK1_%=:\n" \
  "ds_read_b128 v[52:55], v9 offset:35840\n" \
  "v_mfma_f32_16x16x32_bf16 v[104:107], a[132:135], v[32:35], v[104:107]\n" \
  "v_mfma_f32_16x16x32_bf16 v[116:119], a[132:135], v[36:39], v[116:119]\n" \
  "v_mfma_f32_16x16x32_bf16 v[108:111], a[212:215], v[32:35], v[108:111]\n" \
  "v_mfma_f32_16x16x32_bf16 v[120:123], a[212:215], v[36:39], v[120:123]\n" \
  "buffer_load_dwordx4 v[192:195], v6, s[40:43], s60 offen offset:0 sc1\n" \
  "s_waitcnt lgkmcnt(3)\n" \
  "v_mfma_f32_16x16x32_bf16 v[100:103], a[56:59], v[40:43], v[100:103]\n" \
  "s_waitcnt lgkmcnt(2)\n" \
  "ds_read_b128 v[32:35], v9 offset:16384\n" \
  "v_mfma_f32_16x16x32_bf16 v[112:115], a[56:59], v[44:47], v[112:115]\n" \
  "ds_read_b128 v[36:39], v9 offset:36864\n" \
  "v_mfma_f32_16x16x32_bf16 v[104:107], a[136:139], v[40:43], v[104:107]\n" \
  "buffer_load_dwordx4 v[196:199], v6, s[40:43], s60 offen offset:1024 sc1\n" \
  "v_mfma_f32_16x16x32_bf16 v[116:119], a[136:139], v[44:47], v[116:119]\n" \
  "v_mfma_f32_16x16x32_bf16 v[108:111], a[216:219], v[40:43], v[108:111]\n" \
  "v_mfma_f32_16x16x32_bf16 v[120:123], a[216:219], v[44:47], v[120:123]\n" \
  "buffer_load_dwordx4 v[200:203], v6, s[40:43], s60 offen offset:2048 sc1\n" \
  "s_waitcnt lgkmcnt(3)\n" \
  "v_mfma_f32_16x16x32_bf16 v[100:103], a[60:63], v[48:51], v[100:103]\n" \
  "s_waitcnt lgkmcnt(2)\n" \
  "ds_read_b128 v[40:43], v9 offset:17408\n" \
  "v_mfma_f32_16x16x32_bf16 v[112:115], a[60:63], v[52:55], v[112:115]\n" \
  "ds_read_b128 v[44:47], v9 offset:37888\n" \
  "v_mfma_f32_16x16x32_bf16 v[104:107], a[140:143], v[48:51], v[104:107]\n" \
  "buffer_load_dwordx4 v[204:207], v6, s[40:43], s60 offen offset:3072 sc1\n" \
  "v_mfma_f32_16x16x32_bf16 v[116:119], a[140:143], v[52:55], v[116:119]\n" \
  "v_mfma_f32_16x16x32_bf16 v[108:111], a[220:223], v[48:51], v[108:111]\n" \
  "v_mfma_f32_16x16x32_bf16 v[120:123], a[220:223], v[52:55], v[120:123]\n" \
  "buffer_load_dwordx2 v[208:209], v7, s[40:43], s60 offen offset:0 sc1\n" \
  "s_waitcnt lgkmcnt(3)\n" \
  "v_mfma_f32_16x16x32_bf16 v[100:103], a[64:67], v[32:35], v[100:103]\n" \
  "s_waitcnt lgkmcnt(2)\n" \
  "ds_read_b128 v[48:51], v9 offset:18432\n" \
  "v_mfma_f32_16x16x32_bf16 v[112:115], a[64:67], v[36:39], v[112:115]\n" \
  "ds_read_b128 v[52:55], v9 offset:38912\n" \
  "v_mfma_f32_16x16x32_bf16 v[104:107], a[144:147], v[32:35], v[104:107]\n" \
  "buffer_load_dwordx2 v[210:211], v7, s[40:43], s60 offen offset:1024 sc1\n" \
  "v_mfma_f32_16x16x32_bf16 v[116:119], a[144:147], v[36:39], v[116:119]\n" \
  "v_mfma_f32_16x16x32_bf16 v[108:111], a[224:227], v[32:35], v[108:111]\n" \
  "v_mfma_f32_16x16x32_bf16 v[120:123], a[224:227], v[36:39], v[120:123]\n" \
  "buffer_load_dwordx2 v[212:213], v7, s[40:43], s60 offen offset:2048 sc1\n" \
  "s_waitcnt lgkmcnt(3)\n" \
  "v_mfma_f32_16x16x32_bf16 v[100:103], a[68:71], v[40:43], v[100:103]\n" \
  "s_waitcnt lgkmcnt(2)\n" \
  "ds_read_b128 v[32:35], v9 offset:19456\n" \
  "v_mfma_f32_16x16x32_bf16 v[112:115], a[68:71], v[44:47], v[112:115]\n" \
  "ds_read_b128 v[36:39], v9 offset:39936\n" \
  "v_mfma_f32_16x16x32_bf16 v[104:107], a[148:151], v[40:43], v[104:107]\n" \
  "buffer_load_dwordx2 v[214:215], v7, s[40:43], s60 offen offset:3072 sc1\n" \
  "v_mfma_f32_16x16x32_bf16 v[116:119], a[148:151], v[44:47], v[116:119]\n" \
  "v_mfma_f32_16x16x32_bf16 v[108:111], a[228:231], v[40:43], v[108:111]\n" \
  "v_mfma_f32_16x16x32_bf16 v[120:123], a[228:231], v[44:47], v[120:123]\n" \
  "s_waitcnt vmcnt(33)\n" \
  "s_barrier\n" \
  "s_waitcnt lgkmcnt(3)\n" \
  "v_mfma_f32_16x16x32_bf16 v[100:103], a[72:75], v[48:51], v[100:103]\n" \
  "s_waitcnt lgkmcnt(2)\n" \
  "ds_read_b128 v[40:43], v10 offset:0\n" \
  "v_mfma_f32_16x16x32_bf16 v[112:115], a[72:75], v[52:55], v[112:115]\n" \
  "ds_read_b128 v[44:47], v10 offset:20480\n" \
  "v_mfma_f32_16x16x32_bf16 v[104:107], a[152:155], v[48:51], v[104:107]\n" \
  "v_mfma_f32_16x16x32_bf16 v[116:119], a[152:155], v[52:55], v[116:119]\n" \
  "v_mfma_f32_16x16x32_bf16 v[108:111], a[232:235], v[48:51], v[108:111]\n" \
  "v_mfma_f32_16x16x32_bf16 v[120:123], a[232:235], v[52:55], v[120:123]\n" \
  "s_waitcnt lgkmcnt(3)\n" \
  "v_mfma_f32_16x16x32_bf16 v[100:103], a[76:79], v[32:35], v[100:103]\n" \
  "s_waitcnt lgkmcnt(2)\n" \
  "ds_read_b128 v[48:51], v10 offset:1024\n" \
  "v_mfma_f32_16x16x32_bf16 v[112:115], a[76:79], v[36:39], v[112:115]\n" \
  "ds_read_b128 v[52:55], v10 offset:21504\n" \
  "v_mfma_f32_16x16x32_bf16 v[104:107], a[156:159], v[32:35], v[104:107]\n" \
  "v_mfma_f32_16x16x32_bf16 v[116:119], a[156:159], v[36:39], v[116:119]\n" \
  "v_mfma_f32_16x16x32_bf16 v[108:111], a[236:239], v[32:35], v[108:111]\n" \
  "v_mfma_f32_16x16x32_bf16 v[120:123], a[236:239], v[36:39], v[120:123]\n" \
  "s_add_u32 s52, s52, 1\n" \
  "s_waitcnt lgkmcnt(3)\n" \
  "v_mfma_f32_16x16x32_bf16 v[56:59], a[0:3], v[40:43], 0\n" \
  "s_add_u32 s63, s52, s54\n" \
  "s_waitcnt lgkmcnt(2)\n" \
  "ds_read_b128 v[32:35], v10 offset:2048\n" \
  "v_mfma_f32_16x16x32_bf16 v[68:71], a[0:3], v[44:47], 0\n" \
  "s_sub_u32 s64, s63, 2\n" \
  "ds_read_b128 v[36:39], v10 offset:22528\n" \
  "v_mfma_f32_16x16x32_bf16 v[60:63], a[80:83], v[40:43], 0\n" \
  "v_cvt_pk_bf16_f32 v16, v100, v101\n" \
  "v_cvt_pk_bf16_f32 v17, v102, v103\n" \
  "v_mfma_f32_16x16x32_bf16 v[72:75], a[80:83], v[44:47], 0\n" \
  "v_cvt_pk_bf16_f32 v18, v104, v105\n" \
  "v_cvt_pk_bf16_f32 v19, v106, v107\n" \
  "v_mfma_f32_16x16x32_bf16 v[64:67], a[160:163], v[40:43], 0\n" \
  "v_cvt_pk_bf16_f32 v20, v112, v113\n" \
  "v_cvt_pk_bf16_f32 v21, v114, v115\n" \
  "v_mfma_f32_16x16x32_bf16 v[76:79], a[160:163], v[44:47], 0\n" \
  "s_waitcnt lgkmcnt(3)\n" \
  "v_cvt_pk_bf16_f32 v22, v116, v117\n" \
  "v_cvt_pk_bf16_f32 v23, v118, v119\n" \
  "v_mfma_f32_16x16x32_bf16 v[56:59], a[4:7], v[48:51], v[56:59]\n" \
  "s_waitcnt lgkmcnt(2)\n" \
  "ds_read_b128 v[40:43], v10 offset:3072\n" \
  "v_mfma_f32_16x16x32_bf16 v[68:71], a[4:7], v[52:55], v[68:71]\n" \
  "s_barrier\n" \
  "ds_read_b128 v[44:47], v10 offset:23552\n" \
  "v_mfma_f32_16x16x32_bf16 v[60:63], a[84:87], v[48:51], v[60:63]\n" \
  "v_cvt_pk_bf16_f32 v24, v108, v109\n" \
  "v_cvt_pk_bf16_f32 v25, v110, v111\n" \
  "v_mfma_f32_16x16x32_bf16 v[72:75], a[84:87], v[52:55], v[72:75]\n" \
  "v_cvt_pk_bf16_f32 v26, v120, v121\n" \
  "v_cvt_pk_bf16_f32 v27, v122, v123\n" \
  "v_mfma_f32_16x16x32_bf16 v[64:67], a[164:167], v[48:51], v[64:67]\n" \
  "ds_write_b128 v2, v[16:19] offset:12288\n" \
  "v_mfma_f32_16x16x32_bf16 v[76:79], a[164:167], v[52:55], v[76:79]\n" \
  "s_waitcnt lgkmcnt(4)\n" \
  "v_mfma_f32_16x16x32_bf16 v[56:59], a[8:11], v[32:35], v[56:59]\n" \
  "s_waitcnt lgkmcnt(3)\n" \
  "ds_read_b128 v[48:51], v10 offset:4096\n" \
  "v_mfma_f32_16x16x32_bf16 v[68:71], a[8:11], v[36:39], v[68:71]\n" \
  "ds_read_b128 v[52:55], v10 offset:24576\n" \
  "v_mfma_f32_16x16x32_bf16 v[60:63], a[88:91], v[32:35], v[60:63]\n" \
  "ds_write_b128 v2, v[20:23] offset:13312\n" \
  "v_mfma_f32_16x16x32_bf16 v[72:75], a[88:91], v[36:39], v[72:75]\n" \
  "v_mfma_f32_16x16x32_bf16 v[64:67], a[168:171], v[32:35], v[64:67]\n" \
  "v_mfma_f32_16x16x32_bf16 v[76:79], a[168:171], v[36:39], v[76:79]\n" \
  "s_add_u32 s70, s68, 4096\n" \
  "s_add_u32 s71, s68, 8192\n" \
  "s_waitcnt lgkmcnt(5)\n" \
  "v_mfma_f32_16x16x32_bf16 v[56:59], a[12:15], v[40:43], v[56:59]\n" \
  "s_waitcnt lgkmcnt(4)\n" \
  "ds_write_b128 v2, v[24:27] offset:14336\n" \
  "ds_read_b128 v[32:35], v10 offset:5120\n" \
  "v_mfma_f32_16x16x32_bf16 v[68:71], a[12:15], v[44:47], v[68:71]\n" \
  "s_add_u32 m0, s53, 40960\n" \
  "ds_read_b128 v[36:39], v10 offset:25600\n" \
  "v_mfma_f32_16x16x32_bf16 v[60:63], a[92:95], v[40:43], v[60:63]\n" \
  "buffer_load_dwordx4 v1, s[36:39], s68 offen sc1 lds\n" \
  "v_mfma_f32_16x16x32_bf16 v[72:75], a[92:95], v[44:47], v[72:75]\n" \
  "v_mfma_f32_16x16x32_bf16 v[64:67], a[172:175], v[40:43], v[64:67]\n" \
  "v_mfma_f32_16x16x32_bf16 v[76:79], a[172:175], v[44:47], v[76:79]\n" \
  "s_waitcnt lgkmcnt(5)\n" \
  "v_mfma_f32_16x16x32_bf16 v[56:59], a[16:19], v[48:51], v[56:59]\n" \
  "s_waitcnt lgkmcnt(4)\n" \
  "ds_read_b128 v[40:43], v10 offset:6144\n" \
  "v_mfma_f32_16x16x32_bf16 v[68:71], a[16:19], v[52:55], v[68:71]\n" \
  "ds_read_b128 v[44:47], v10 offset:26624\n" \
  "v_mfma_f32_16x16x32_bf16 v[60:63], a[96:99], v[48:51], v[60:63]\n" \
  "buffer_load_dwordx4 v1, s[36:39], s68 offen offset:1024 sc1 lds\n" \
  "v_mfma_f32_16x16x32_bf16 v[72:75], a[96:99], v[52:55], v[72:75]\n" \
  "v_mfma_f32_16x16x32_bf16 v[64:67], a[176:179], v[48:51], v[64:67]\n" \
  "v_readlane_b32 s61, v126, s64\n" \
  "v_mfma_f32_16x16x32_bf16 v[76:79], a[176:179], v[52:55], v[76:79]\n" \
  "s_sub_u32 s64, s52, 2\n" \
  "s_cmp_lt_u32 s64, 56\n" \
  "s_waitcnt lgkmcnt(3)\n" \
  "v_mfma_f32_16x16x32_bf16 v[56:59], a[20:23], v[32:35], v[56:59]\n" \
  "s_cselect_b32 s61, s61, 1073741824\n" \
  "s_waitcnt lgkmcnt(2)\n" \
  "ds_read_b128 v[48:51], v10 offset:7168\n" \
  "v_mfma_f32_16x16x32_bf16 v[68:71], a[20:23], v[36:39], v[68:71]\n" \
  "ds_read_b128 v[52:55], v10 offset:27648\n" \
  "v_mfma_f32_16x16x32_bf16 v[60:63], a[100:103], v[32:35], v[60:63]\n" \
  "buffer_load_dwordx4 v1, s[36:39], s68 offen offset:2048 sc1 lds\n" \
  "v_mfma_f32_16x16x32_bf16 v[72:75], a[100:103], v[36:39], v[72:75]\n" \
  "s_cmp_ge_u32 s52, 4\n" \
  "s_cselect_b32 s62, 0, 1073741824\n" \
  "v_mfma_f32_16x16x32_bf16 v[64:67], a[180:183], v[32:35], v[64:67]\n" \
  "s_waitcnt vmcnt(3) lgkmcnt(4)\n" \
  "s_barrier\n" \
  "v_mfma_f32_16x16x32_bf16 v[76:79], a[180:183], v[36:39], v[76:79]\n" \
  "buffer_atomic_add v12, v129, s[40:43], s62 offen\n" \
  "ds_read_b128 v[216:219], v3 offset:0\n" \
  "s_waitcnt lgkmcnt(4)\n" \
  "v_mfma_f32_16x16x32_bf16 v[56:59], a[24:27], v[40:43], v[56:59]\n" \
  "s_waitcnt lgkmcnt(3)\n" \
  "ds_read_b128 v[32:35], v10 offset:8192\n" \
  "ds_read_b128 v[220:223], v3 offset:3072\n" \
  "v_mfma_f32_16x16x32_bf16 v[68:71], a[24:27], v[44:47], v[68:71]\n" \
  "ds_read_b128 v[36:39], v10 offset:28672\n" \
  "ds_read_b128 v[224:227], v3 offset:6144\n" \
  "v_mfma_f32_16x16x32_bf16 v[60:63], a[104:107], v[40:43], v[60:63]\n" \
  "buffer_load_dwordx4 v1, s[36:39], s68 offen offset:3072 sc1 lds\n" \
  "ds_read_b128 v[228:231], v3 offset:9216\n" \
  "v_mfma_f32_16x16x32_bf16 v[72:75], a[104:107], v[44:47], v[72:75]\n" \
  "ds_read_b64 v[232:233], v4 offset:0\n" \
  "ds_read_b64 v[234:235], v4 offset:3072\n" \
  "v_mfma_f32_16x16x32_bf16 v[64:67], a[184:187], v[40:43], v[64:67]\n" \
  "ds_read_b64 v[236:237], v4 offset:6144\n" \
  "ds_read_b64 v[238:239], v4 offset:9216\n" \
  "v_mfma_f32_16x16x32_bf16 v[76:79], a[184:187], v[44:47], v[76:79]\n" \
  "s_waitcnt lgkmcnt(11)\n" \
  "v_swap_b32 v162, v184\n" \
  "v_mfma_f32_16x16x32_bf16 v[56:59], a[28:31], v[48:51], v[56:59]\n" \
  "s_waitcnt lgkmcnt(10)\n" \
  "ds_read_b128 v[40:43], v10 offset:9216\n" \
  "v_mfma_f32_16x16x32_bf16 v[68:71], a[28:31], v[52:55], v[68:71]\n" \
  "s_add_u32 m0, s53, 45056\n" \
  "ds_read_b128 v[44:47], v10 offset:29696\n" \
  "v_mfma_f32_16x16x32_bf16 v[60:63], a[108:111], v[48:51], v[60:63]\n" \
  "buffer_load_dwordx4 v1, s[36:39], s70 offen sc1 lds\n" \
  "v_mfma_f32_16x16x32_bf16 v[72:75], a[108:111], v[52:55], v[72:75]\n" \
  "v_swap_b32 v163, v185\n" \
  "v_mfma_f32_16x16x32_bf16 v[64:67], a[188:191], v[48:51], v[64:67]\n" \
  "v_swap_b32 v166, v188\n" \
  "v_mfma_f32_16x16x32_bf16 v[76:79], a[188:191], v[52:55], v[76:79]\n" \
  "s_waitcnt lgkmcnt(10)\n" \
  "v_swap_b32 v167, v189\n" \
  "v_mfma_f32_16x16x32_bf16 v[56:59], a[32:35], v[32:35], v[56:59]\n" \
  "s_waitcnt lgkmcnt(8)\n" \
  "ds_read_b128 v[48:51], v10 offset:10240\n" \
  "v_mfma_f32_16x16x32_bf16 v[68:71], a[32:35], v[36:39], v[68:71]\n" \
  "ds_read_b128 v[52:55], v10 offset:30720\n" \
  "v_mfma_f32_16x16x32_bf16 v[240:243], v[192:195], v[216:219], v[132:135]\n" \
  "v_mfma_f32_16x16x32_bf16 v[244:247], v[144:147], v[216:219], v[136:139]\n" \
  "v_mfma_f32_16x16x32_bf16 v[248:251], v[168:171], v[216:219], v[140:143]\n" \
  "v_mfma_f32_16x16x32_bf16 v[60:63], a[112:115], v[32:35], v[60:63]\n" \
  "buffer_load_dwordx4 v1, s[36:39], s70 offen offset:1024 sc1 lds\n" \
  "v_mfma_f32_16x16x32_bf16 v[240:243], v[196:199], v[220:223], v[240:243]\n" \
  "v_mfma_f32_16x16x32_bf16 v[244:247], v[148:151], v[220:223], v[244:247]\n" \
  "v_mfma_f32_16x16x32_bf16 v[248:251], v[172:175], v[220:223], v[248:251]\n" \
  "v_mfma_f32_16x16x32_bf16 v[72:75], a[112:115], v[36:39], v[72:75]\n" \
  "s_waitcnt lgkmcnt(9)\n" \
  "v_mfma_f32_16x16x32_bf16 v[240:243], v[200:203], v[224:227], v[240:243]\n" \
  "v_mfma_f32_16x16x32_bf16 v[244:247], v[152:155], v[224:227], v[244:247]\n" \
  "v_mfma_f32_16x16x32_bf16 v[248:251], v[176:179], v[224:227], v[248:251]\n" \
  "v_mfma_f32_16x16x32_bf16 v[64:67], a[192:195], v[32:35], v[64:67]\n" \
  "s_waitcnt lgkmcnt(8)\n" \
  "v_mfma_f32_16x16x32_bf16 v[240:243], v[204:207], v[228:231], v[240:243]\n" \
  "v_mfma_f32_16x16x32_bf16 v[244:247], v[156:159], v[228:231], v[244:247]\n" \
  "v_mfma_f32_16x16x32_bf16 v[248:251], v[180:183], v[228:231], v[248:251]\n" \
  "v_mfma_f32_16x16x32_bf16 v[76:79], a[192:195], v[36:39], v[76:79]\n" \
  "s_waitcnt lgkmcnt(6)\n" \
  "v_mfma_f32_16x16x32_bf16 v[240:243], v[208:211], v[232:235], v[240:243]\n" \
  "v_mfma_f32_16x16x32_bf16 v[244:247], v[160:163], v[232:235], v[244:247]\n" \
  "v_mfma_f32_16x16x32_bf16 v[248:251], v[184:187], v[232:235], v[248:251]\n" \
  "s_waitcnt lgkmcnt(3)\n" \
  "v_mfma_f32_16x16x32_bf16 v[56:59], a[36:39], v[40:43], v[56:59]\n" \
  "ds_read_b128 v[32:35], v10 offset:11264\n" \
  "v_mfma_f32_16x16x32_bf16 v[240:243], v[212:215], v[236:239], v[240:243]\n" \
  "v_mfma_f32_16x16x32_bf16 v[244:247], v[164:167], v[236:239], v[244:247]\n" \
  "v_mfma_f32_16x16x32_bf16 v[248:251], v[188:191], v[236:239], v[248:251]\n" \
  "s_waitcnt lgkmcnt(3)\n" \
  "v_mfma_f32_16x16x32_bf16 v[68:71], a[36:39], v[44:47], v[68:71]\n" \
  "ds_read_b128 v[36:39], v10 offset:31744\n" \
  "v_mfma_f32_16x16x32_bf16 v[60:63], a[116:119], v[40:43], v[60:63]\n" \
  "buffer_load_dwordx4 v1, s[36:39], s70 offen offset:2048 sc1 lds\n" \
  "v_mfma_f32_16x16x32_bf16 v[72:75], a[116:119], v[44:47], v[72:75]\n" \
  "v_cvt_pk_bf16_f32 v80, v240, v241\n" \
  "v_cvt_pk_bf16_f32 v81, v242, v243\n" \
  "v_mfma_f32_16x16x32_bf16 v[64:67], a[196:199], v[40:43], v[64:67]\n" \
  "v_cvt_pk_bf16_f32 v82, v244, v245\n" \
  "v_cvt_pk_bf16_f32 v83, v246, v247\n" \
  "v_mfma_f32_16x16x32_bf16 v[76:79], a[196:199], v[44:47], v[76:79]\n" \
  "s_waitcnt lgkmcnt(3)\n" \
  "v_cvt_pk_bf16_f32 v84, v248, v249\n" \
  "v_cvt_pk_bf16_f32 v85, v250, v251\n" \
  "v_mfma_f32_16x16x32_bf16 v[56:59], a[40:43], v[48:51], v[56:59]\n" \
  "s_waitcnt lgkmcnt(2)\n" \
  "ds_read_b128 v[40:43], v10 offset:12288\n" \
  "v_lshlrev_b32 v240, 16, v80\n" \
  "v_mfma_f32_16x16x32_bf16 v[68:71], a[40:43], v[52:55], v[68:71]\n" \
  "ds_read_b128 v[44:47], v10 offset:32768\n" \
  "v_and_b32 v241, 0xffff0000, v80\n" \
  "v_mfma_f32_16x16x32_bf16 v[60:63], a[120:123], v[48:51], v[60:63]\n" \
  "buffer_load_dwordx4 v1, s[36:39], s70 offen offset:3072 sc1 lds\n" \
  "v_mul_f32 v240, 0x3d93cd3a, v240\n" \
  "v_mfma_f32_16x16x32_bf16 v[72:75], a[120:123], v[52:55], v[72:75]\n" \
  "v_mul_f32 v241, 0x3d93cd3a, v241\n" \
  "v_cvt_pk_bf16_f32 v80, v240, v241\n" \
  "v_mfma_f32_16x16x32_bf16 v[64:67], a[200:203], v[48:51], v[64:67]\n" \
  "v_lshlrev_b32 v240, 16, v80\n" \
  "v_and_b32 v241, 0xffff0000, v80\n" \
  "v_mfma_f32_16x16x32_bf16 v[76:79], a[200:203], v[52:55], v[76:79]\n" \
  "s_waitcnt lgkmcnt(3)\n" \
  "v_lshlrev_b32 v242, 16, v81\n" \
  "v_and_b32 v243, 0xffff0000, v81\n" \
  "v_mfma_f32_16x16x32_bf16 v[56:59], a[44:47], v[32:35], v[56:59]\n" \
  "s_waitcnt lgkmcnt(2)\n" \
  "ds_read_b128 v[48:51], v10 offset:13312\n" \
  "v_mul_f32 v242, 0x3d93cd3a, v242\n" \
  "v_mfma_f32_16x16x32_bf16 v[68:71], a[44:47], v[36:39], v[68:71]\n" \
  "s_add_u32 m0, s53, 49152\n" \
  "ds_read_b128 v[52:55], v10 offset:33792\n" \
  "v_mul_f32 v243, 0x3d93cd3a, v243\n" \
  "v_mfma_f32_16x16x32_bf16 v[60:63], a[124:127], v[32:35], v[60:63]\n" \
  "buffer_load_dwordx4 v1, s[36:39], s71 offen sc1 lds\n" \
  "v_cvt_pk_bf16_f32 v81, v242, v243\n" \
  "v_mfma_f32_16x16x32_bf16 v[72:75], a[124:127], v[36:39], v[72:75]\n" \
  "v_lshlrev_b32 v242, 16, v81\n" \
  "v_and_b32 v243, 0xffff0000, v81\n" \
  "v_mfma_f32_16x16x32_bf16 v[64:67], a[204:207], v[32:35], v[64:67]\n" \
  "v_readlane_b32 s56, v124, s63\n" \
  "v_mfma_f32_16x16x32_bf16 v[76:79], a[204:207], v[36:39], v[76:79]\n" \
  "s_waitcnt lgkmcnt(3)\n" \
  "v_lshlrev_b32 v244, 16, v82\n" \
  "v_and_b32 v245, 0xffff0000, v82\n" \
  "v_mfma_f32_16x16x32_bf16 v[56:59], a[48:51], v[40:43], v[56:59]\n" \
  "s_waitcnt lgkmcnt(2)\n" \
  "ds_read_b128 v[32:35], v10 offset:14336\n" \
  "v_mul_f32 v244, 0x3d93cd3a, v244\n" \
  "v_mfma_f32_16x16x32_bf16 v[68:71], a[48:51], v[44:47], v[68:71]\n" \
  "ds_read_b128 v[36:39], v10 offset:34816\n" \
  "v_mul_f32 v245, 0x3d93cd3a, v245\n" \
  "v_mfma_f32_16x16x32_bf16 v[60:63], a[128:131], v[40:43], v[60:63]\n" \
  "buffer_load_dwordx4 v1, s[36:39], s71 offen offset:1024 sc1 lds\n" \
  "v_cvt_pk_bf16_f32 v82, v244, v245\n" \
  "v_mfma_f32_16x16x32_bf16 v[72:75], a[128:131], v[44:47], v[72:75]\n" \
  "s_add_u32 s57, s56, 16384\n" \
  "v_lshlrev_b32 v244, 16, v82\n" \
  "v_and_b32 v245, 0xffff0000, v82\n" \
  "v_mfma_f32_16x16x32_bf16 v[64:67], a[208:211], v[40:43], v[64:67]\n" \
  "s_add_u32 s58, s56, 32768\n" \
  "v_lshlrev_b32 v246, 16, v83\n" \
  "v_and_b32 v247, 0xffff0000, v83\n" \
  "v_mfma_f32_16x16x32_bf16 v[76:79], a[208:211], v[44:47], v[76:79]\n" \
  "s_add_u32 s68, s68, 40960\n" \
  "s_add_u32 s59, s56, 49152\n" \
  "s_waitcnt lgkmcnt(3)\n" \
  "v_mul_f32 v246, 0x3d93cd3a, v246\n" \
  "v_mfma_f32_16x16x32_bf16 v[56:59], a[52:55], v[48:51], v[56:59]\n" \
  "s_waitcnt lgkmcnt(2)\n" \
  "ds_read_b128 v[40:43], v10 offset:15360\n" \
  "v_mul_f32 v247, 0x3d93cd3a, v247\n" \
  "v_mfma_f32_16x16x32_bf16 v[68:71], a[52:55], v[52:55], v[68:71]\n" \
  "ds_read_b128 v[44:47], v10 offset:35840\n" \
  "v_cvt_pk_bf16_f32 v83, v246, v247\n" \
  "v_mfma_f32_16x16x32_bf16 v[60:63], a[132:135], v[48:51], v[60:63]\n" \
  "v_lshlrev_b32 v246, 16, v83\n" \
  "v_and_b32 v247, 0xffff0000, v83\n" \
  "v_mfma_f32_16x16x32_bf16 v[72:75], a[132:135], v[52:55], v[72:75]\n" \
  "v_lshlrev_b32 v248, 16, v84\n" \
  "v_and_b32 v249, 0xffff0000, v84\n" \
  "v_mfma_f32_16x16x32_bf16 v[64:67], a[212:215], v[48:51], v[64:67]\n" \
  "v_mul_f32 v248, 0x3d93cd3a, v248\n" \
  "v_mul_f32 v249, 0x3d93cd3a, v249\n" \
  "v_mfma_f32_16x16x32_bf16 v[76:79], a[212:215], v[52:55], v[76:79]\n" \
  "buffer_load_dwordx4 v[144:147], v5, s[40:43], s56 offen offset:0 sc1\n" \
  "v_cvt_pk_bf16_f32 v84, v248, v249\n" \
  "s_waitcnt lgkmcnt(3)\n" \
  "v_mfma_f32_16x16x32_bf16 v[56:59], a[56:59], v[32:35], v[56:59]\n" \
  "s_waitcnt lgkmcnt(2)\n" \
  "ds_read_b128 v[48:51], v10 offset:16384\n" \
  "v_lshlrev_b32 v248, 16, v84\n" \
  "v_mfma_f32_16x16x32_bf16 v[68:71], a[56:59], v[36:39], v[68:71]\n" \
  "ds_read_b128 v[52:55], v10 offset:36864\n" \
  "v_and_b32 v249, 0xffff0000, v84\n" \
  "v_mfma_f32_16x16x32_bf16 v[60:63], a[136:139], v[32:35], v[60:63]\n" \
  "v_lshlrev_b32 v250, 16, v85\n" \
  "v_and_b32 v251, 0xffff0000, v85\n" \
  "v_mfma_f32_16x16x32_bf16 v[72:75], a[136:139], v[36:39], v[72:75]\n" \
  "v_mul_f32 v250, 0x3d93cd3a, v250\n" \
  "v_mul_f32 v251, 0x3d93cd3a, v251\n" \
  "v_mfma_f32_16x16x32_bf16 v[64:67], a[216:219], v[32:35], v[64:67]\n" \
  "v_cvt_pk_bf16_f32 v85, v250, v251\n" \
  "v_lshlrev_b32 v250, 16, v85\n" \
  "v_mfma_f32_16x16x32_bf16 v[76:79], a[216:219], v[36:39], v[76:79]\n" \
  "buffer_load_dwordx4 v[168:171], v5, s[40:43], s56 offen offset:1024 sc1\n" \
  "v_and_b32 v251, 0xffff0000, v85\n" \
  "s_waitcnt lgkmcnt(3)\n" \
  "v_mfma_f32_16x16x32_bf16 v[56:59], a[60:63], v[40:43], v[56:59]\n" \
  "s_waitcnt lgkmcnt(2)\n" \
  "ds_read_b128 v[32:35], v10 offset:17408\n" \
  "v_max3_f32 v80, v240, v241, v242\n" \
  "v_mfma_f32_16x16x32_bf16 v[68:71], a[60:63], v[44:47], v[68:71]\n" \
  "ds_read_b128 v[36:39], v10 offset:37888\n" \
  "v_max3_f32 v80, v80, v243, v244\n" \
  "v_mfma_f32_16x16x32_bf16 v[60:63], a[140:143], v[40:43], v[60:63]\n" \
  "v_max3_f32 v80, v80, v245, v246\n" \
  "v_max3_f32 v80, v80, v247, v248\n" \
  "v_mfma_f32_16x16x32_bf16 v[72:75], a[140:143], v[44:47], v[72:75]\n" \
  "v_max3_f32 v80, v80, v249, v250\n" \
  "v_max_f32 v80, v80, v251\n" \
  "v_mfma_f32_16x16x32_bf16 v[64:67], a[220:223], v[40:43], v[64:67]\n" \
  "v_mov_b32 v81, v80\n" \
  "v_mfma_f32_16x16x32_bf16 v[76:79], a[220:223], v[44:47], v[76:79]\n" \
  "buffer_load_dwordx4 v[148:151], v5, s[40:43], s57 offen offset:0 sc1\n" \
  "s_waitcnt lgkmcnt(3)\n" \
  "v_mfma_f32_16x16x32_bf16 v[56:59], a[64:67], v[48:51], v[56:59]\n" \
  "s_waitcnt lgkmcnt(2)\n" \
  "ds_read_b128 v[40:43], v10 offset:18432\n" \
  "v_mfma_f32_16x16x32_bf16 v[68:71], a[64:67], v[52:55], v[68:71]\n" \
  "ds_read_b128 v[44:47], v10 offset:38912\n" \
  "v_mfma_f32_16x16x32_bf16 v[60:63], a[144:147], v[48:51], v[60:63]\n" \
  "v_permlane16_swap_b32 v80, v81\n" \
  "v_mfma_f32_16x16x32_bf16 v[72:75], a[144:147], v[52:55], v[72:75]\n" \
  "v_max_f32 v80, v80, v81\n" \
  "v_mov_b32 v81, v80\n" \
  "v_mfma_f32_16x16x32_bf16 v[64:67], a[224:227], v[48:51], v[64:67]\n" \
  "v_mfma_f32_16x16x32_bf16 v[76:79], a[224:227], v[52:55], v[76:79]\n" \
  "buffer_load_dwordx4 v[172:175], v5, s[40:43], s57 offen offset:1024 sc1\n" \
  "s_waitcnt lgkmcnt(3)\n" \
  "v_mfma_f32_16x16x32_bf16 v[56:59], a[68:71], v[32:35], v[56:59]\n" \
  "s_waitcnt lgkmcnt(2)\n" \
  "ds_read_b128 v[48:51], v10 offset:19456\n" \
  "v_mfma_f32_16x16x32_bf16 v[68:71], a[68:71], v[36:39], v[68:71]\n" \
  "ds_read_b128 v[52:55], v10 offset:39936\n" \
  "v_mfma_f32_16x16x32_bf16 v[60:63], a[148:151], v[32:35], v[60:63]\n" \
  "v_permlane32_swap_b32 v80, v81\n" \
  "v_mfma_f32_16x16x32_bf16 v[72:75], a[148:151], v[36:39], v[72:75]\n" \
  "v_max_f32 v80, v80, v81\n" \
  "v_mul_f32 v80, 0x3fb8aa3b, v80\n" \
  "v_mfma_f32_16x16x32_bf16 v[64:67], a[228:231], v[32:35], v[64:67]\n" \
  "v_mov_b32 v82, 0x3fb8aa3b\n" \
  "v_fma_f32 v240, v240, v82, -v80\n" \
  "v_mfma_f32_16x16x32_bf16 v[76:79], a[228:231], v[36:39], v[76:79]\n" \
  "buffer_load_dwordx4 v[152:155], v5, s[40:43], s58 offen offset:0 sc1\n" \
  "v_fma_f32 v241, v241, v82, -v80\n" \
  "s_waitcnt vmcnt(24)\n" \
  "s_barrier\n" \
  "s_waitcnt lgkmcnt(3)\n" \
  "v_mfma_f32_16x16x32_bf16 v[56:59], a[72:75], v[40:43], v[56:59]\n" \
  "s_waitcnt lgkmcnt(2)\n" \
  "ds_read_b128 v[32:35], v0 offset:0\n" \
  "v_fma_f32 v242, v242, v82, -v80\n" \
  "v_mfma_f32_16x16x32_bf16 v[68:71], a[72:75], v[44:47], v[68:71]\n" \
  "ds_read_b128 v[36:39], v0 offset:20480\n" \
  "v_fma_f32 v243, v243, v82, -v80\n" \
  "v_mfma_f32_16x16x32_bf16 v[60:63], a[152:155], v[40:43], v[60:63]\n" \
  "v_fma_f32 v244, v244, v82, -v80\n" \
  "v_fma_f32 v245, v245, v82, -v80\n" \
  "v_mfma_f32_16x16x32_bf16 v[72:75], a[152:155], v[44:47], v[72:75]\n" \
  "v_fma_f32 v246, v246, v82, -v80\n" \
  "v_fma_f32 v247, v247, v82, -v80\n" \
  "v_mfma_f32_16x16x32_bf16 v[64:67], a[232:235], v[40:43], v[64:67]\n" \
  "v_fma_f32 v248, v248, v82, -v80\n" \
  "v_fma_f32 v249, v249, v82, -v80\n" \
  "v_mfma_f32_16x16x32_bf16 v[76:79], a[232:235], v[44:47], v[76:79]\n" \
  "buffer_load_dwordx4 v[176:179], v5, s[40:43], s58 offen offset:1024 sc1\n" \
  "v_fma_f32 v250, v250, v82, -v80\n" \
  "s_waitcnt lgkmcnt(3)\n" \
  "v_mfma_f32_16x16x32_bf16 v[56:59], a[76:79], v[48:51], v[56:59]\n" \
  "s_waitcnt lgkmcnt(2)\n" \
  "ds_read_b128 v[40:43], v0 offset:1024\n" \
  "v_fma_f32 v251, v251, v82, -v80\n" \
  "v_mfma_f32_16x16x32_bf16 v[68:71], a[76:79], v[52:55], v[68:71]\n" \
  "ds_read_b128 v[44:47], v0 offset:21504\n" \
  "v_mfma_f32_16x16x32_bf16 v[60:63], a[156:159], v[48:51], v[60:63]\n" \
  "v_exp_f32 v240, v240\n" \
  "v_mfma_f32_16x16x32_bf16 v[72:75], a[156:159], v[52:55], v[72:75]\n" \
  "v_exp_f32 v241, v241\n" \
  "v_mfma_f32_16x16x32_bf16 v[64:67], a[236:239], v[48:51], v[64:67]\n" \
  "v_exp_f32 v242, v242\n" \
  "v_mfma_f32_16x16x32_bf16 v[76:79], a[236:239], v[52:55], v[76:79]\n" \
  "v_exp_f32 v243, v243\n" \
  "s_add_u32 s52, s52, 1\n" \
  "s_waitcnt lgkmcnt(3)\n" \
  "v_mfma_f32_16x16x32_bf16 v[100:103], a[0:3], v[32:35], 0\n" \
  "s_waitcnt lgkmcnt(2)\n" \
  "ds_read_b128 v[48:51], v0 offset:2048\n" \
  "v_mfma_f32_16x16x32_bf16 v[112:115], a[0:3], v[36:39], 0\n" \
  "ds_read_b128 v[52:55], v0 offset:22528\n" \
  "v_mfma_f32_16x16x32_bf16 v[104:107], a[80:83], v[32:35], 0\n" \
  "v_cvt_pk_bf16_f32 v16, v56, v57\n" \
  "v_cvt_pk_bf16_f32 v17, v58, v59\n" \
  "v_mfma_f32_16x16x32_bf16 v[116:119], a[80:83], v[36:39], 0\n" \
  "v_cvt_pk_bf16_f32 v18, v60, v61\n" \
  "v_cvt_pk_bf16_f32 v19, v62, v63\n" \
  "v_mfma_f32_16x16x32_bf16 v[108:111], a[160:163], v[32:35], 0\n" \
  "v_cvt_pk_bf16_f32 v20, v68, v69\n" \
  "v_cvt_pk_bf16_f32 v21, v70, v71\n" \
  "v_mfma_f32_16x16x32_bf16 v[120:123], a[160:163], v[36:39], 0\n" \
  "v_cvt_pk_bf16_f32 v22, v72, v73\n" \
  "v_cvt_pk_bf16_f32 v23, v74, v75\n" \
  "buffer_load_dwordx4 v[156:159], v5, s[40:43], s59 offen offset:0 sc1\n" \
  "s_waitcnt lgkmcnt(3)\n" \
  "v_mfma_f32_16x16x32_bf16 v[100:103], a[4:7], v[40:43], v[100:103]\n" \
  "s_waitcnt lgkmcnt(2)\n" \
  "ds_read_b128 v[32:35], v0 offset:3072\n" \
  "v_mfma_f32_16x16x32_bf16 v[112:115], a[4:7], v[44:47], v[112:115]\n" \
  "s_barrier\n" \
  "ds_read_b128 v[36:39], v0 offset:23552\n" \
  "v_mfma_f32_16x16x32_bf16 v[104:107], a[84:87], v[40:43], v[104:107]\n" \
  "v_cvt_pk_bf16_f32 v24, v64, v65\n" \
  "v_cvt_pk_bf16_f32 v25, v66, v67\n" \
  "v_mfma_f32_16x16x32_bf16 v[116:119], a[84:87], v[44:47], v[116:119]\n" \
  "v_cvt_pk_bf16_f32 v26, v76, v77\n" \
  "v_cvt_pk_bf16_f32 v27, v78, v79\n" \
  "v_mfma_f32_16x16x32_bf16 v[108:111], a[164:167], v[40:43], v[108:111]\n" \
  "ds_write_b128 v2, v[16:19] offset:0\n" \
  "v_mfma_f32_16x16x32_bf16 v[120:123], a[164:167], v[44:47], v[120:123]\n" \
  "buffer_load_dwordx4 v[180:183], v5, s[40:43], s59 offen offset:1024 sc1\n" \
  "s_waitcnt lgkmcnt(4)\n" \
  "v_mfma_f32_16x16x32_bf16 v[100:103], a[8:11], v[48:51], v[100:103]\n" \
  "s_waitcnt lgkmcnt(3)\n" \
  "ds_read_b128 v[40:43], v0 offset:4096\n" \
  "v_mfma_f32_16x16x32_bf16 v[112:115], a[8:11], v[52:55], v[112:115]\n" \
  "ds_read_b128 v[44:47], v0 offset:24576\n" \
  "v_mfma_f32_16x16x32_bf16 v[104:107], a[88:91], v[48:51], v[104:107]\n" \
  "ds_write_b128 v2, v[20:23] offset:1024\n" \
  "v_mfma_f32_16x16x32_bf16 v[116:119], a[88:91], v[52:55], v[116:119]\n" \
  "v_exp_f32 v244, v244\n" \
  "v_mfma_f32_16x16x32_bf16 v[108:111], a[168:171], v[48:51], v[108:111]\n" \
  "v_exp_f32 v245, v245\n" \
  "v_mfma_f32_16x16x32_bf16 v[120:123], a[168:171], v[52:55], v[120:123]\n" \
  "s_add_u32 s70, s68, 4096\n" \
  "s_add_u32 s71, s68, 8192\n" \
  "buffer_load_dwordx4 v[160:163], v5, s[40:43], s56 offen offset:2048 sc1\n" \
  "s_waitcnt lgkmcnt(5)\n" \
  "v_mfma_f32_16x16x32_bf16 v[100:103], a[12:15], v[32:35], v[100:103]\n" \
  "s_waitcnt lgkmcnt(4)\n" \
  "ds_write_b128 v2, v[24:27] offset:2048\n" \
  "ds_read_b128 v[48:51], v0 offset:5120\n" \
  "v_mfma_f32_16x16x32_bf16 v[112:115], a[12:15], v[36:39], v[112:115]\n" \
  "s_add_u32 m0, s53, 81920\n" \
  "ds_read_b128 v[52:55], v0 offset:25600\n" \
  "v_mfma_f32_16x16x32_bf16 v[104:107], a[92:95], v[32:35], v[104:107]\n" \
  "buffer_load_dwordx4 v1, s[36:39], s68 offen sc1 lds\n" \
  "v_mfma_f32_16x16x32_bf16 v[116:119], a[92:95], v[36:39], v[116:119]\n" \
  "v_exp_f32 v246, v246\n" \
  "v_mfma_f32_16x16x32_bf16 v[108:111], a[172:175], v[32:35], v[108:111]\n" \
  "v_exp_f32 v247, v247\n" \
  "v_mfma_f32_16x16x32_bf16 v[120:123], a[172:175], v[36:39], v[120:123]\n" \
  "buffer_load_dwordx4 v[184:187], v5, s[40:43], s57 offen offset:2048 sc1\n" \
  "s_waitcnt lgkmcnt(5)\n" \
  "v_mfma_f32_16x16x32_bf16 v[100:103], a[16:19], v[40:43], v[100:103]\n" \
  "s_waitcnt lgkmcnt(4)\n" \
  "ds_read_b128 v[32:35], v0 offset:6144\n" \
  "v_mfma_f32_16x16x32_bf16 v[112:115], a[16:19], v[44:47], v[112:115]\n" \
  "ds_read_b128 v[36:39], v0 offset:26624\n" \
  "v_mfma_f32_16x16x32_bf16 v[104:107], a[96:99], v[40:43], v[104:107]\n" \
  "buffer_load_dwordx4 v1, s[36:39], s68 offen offset:1024 sc1 lds\n" \
  "v_mfma_f32_16x16x32_bf16 v[116:119], a[96:99], v[44:47], v[116:119]\n" \
  "v_exp_f32 v248, v248\n" \
  "v_mfma_f32_16x16x32_bf16 v[108:111], a[176:179], v[40:43], v[108:111]\n" \
  "v_exp_f32 v249, v249\n" \
  "v_mfma_f32_16x16x32_bf16 v[120:123], a[176:179], v[44:47], v[120:123]\n" \
  "buffer_load_dwordx4 v[164:167], v5, s[40:43], s58 offen offset:2048 sc1\n" \
  "s_waitcnt lgkmcnt(3)\n" \
  "v_mfma_f32_16x16x32_bf16 v[100:103], a[20:23], v[48:51], v[100:103]\n" \
  "s_waitcnt lgkmcnt(2)\n" \
  "ds_read_b128 v[40:43], v0 offset:7168\n" \
  "v_mfma_f32_16x16x32_bf16 v[112:115], a[20:23], v[52:55], v[112:115]\n" \
  "ds_read_b128 v[44:47], v0 offset:27648\n" \
  "v_mfma_f32_16x16x32_bf16 v[104:107], a[100:103], v[48:51], v[104:107]\n" \
  "buffer_load_dwordx4 v1, s[36:39], s68 offen offset:2048 sc1 lds\n" \
  "v_mfma_f32_16x16x32_bf16 v[116:119], a[100:103], v[52:55], v[116:119]\n" \
  "v_exp_f32 v250, v250\n" \
  "v_mfma_f32_16x16x32_bf16 v[108:111], a[180:183], v[48:51], v[108:111]\n" \
  "v_exp_f32 v251, v251\n" \
  "v_mfma_f32_16x16x32_bf16 v[120:123], a[180:183], v[52:55], v[120:123]\n" \
  "buffer_load_dwordx4 v[188:191], v5, s[40:43], s59 offen offset:2048 sc1\n" \
  "v_add_f32 v80, v240, v241\n" \
  "s_waitcnt lgkmcnt(3)\n" \
  "v_mfma_f32_16x16x32_bf16 v[100:103], a[24:27], v[32:35], v[100:103]\n" \
  "s_waitcnt lgkmcnt(2)\n" \
  "ds_read_b128 v[48:51], v0 offset:8192\n" \
  "v_add_f32 v80, v80, v242\n" \
  "v_mfma_f32_16x16x32_bf16 v[112:115], a[24:27], v[36:39], v[112:115]\n" \
  "ds_read_b128 v[52:55], v0 offset:28672\n" \
  "v_add_f32 v80, v80, v243\n" \
  "v_mfma_f32_16x16x32_bf16 v[104:107], a[104:107], v[32:35], v[104:107]\n" \
  "buffer_load_dwordx4 v1, s[36:39], s68 offen offset:3072 sc1 lds\n" \
  "v_add_f32 v80, v80, v244\n" \
  "v_mfma_f32_16x16x32_bf16 v[116:119], a[104:107], v[36:39], v[116:119]\n" \
  "v_add_f32 v80, v80, v245\n" \
  "v_add_f32 v80, v80, v246\n" \
  "v_mfma_f32_16x16x32_bf16 v[108:111], a[184:187], v[32:35], v[108:111]\n" \
  "v_add_f32 v80, v80, v247\n" \
  "v_add_f32 v80, v80, v248\n" \
  "v_mfma_f32_16x16x32_bf16 v[120:123], a[184:187], v[36:39], v[120:123]\n" \
  "buffer_load_dword v11, v8, s[40:43], 0 offen sc1\n" \
  "v_add_f32 v80, v80, v249\n" \
  "s_waitcnt lgkmcnt(3)\n" \
  "v_mfma_f32_16x16x32_bf16 v[100:103], a[28:31], v[40:43], v[100:103]\n" \
  "s_waitcnt lgkmcnt(2)\n" \
  "ds_read_b128 v[32:35], v0 offset:9216\n" \
  "v_add_f32 v80, v80, v250\n" \
  "v_mfma_f32_16x16x32_bf16 v[112:115], a[28:31], v[44:47], v[112:115]\n" \
  "s_add_u32 m0, s53, 86016\n" \
  "ds_read_b128 v[36:39], v0 offset:29696\n" \
  "v_add_f32 v80, v80, v251\n" \
  "v_mfma_f32_16x16x32_bf16 v[104:107], a[108:111], v[40:43], v[104:107]\n" \
  "buffer_load_dwordx4 v1, s[36:39], s70 offen sc1 lds\n" \
  "v_mov_b32 v81, v80\n" \
  "v_mfma_f32_16x16x32_bf16 v[116:119], a[108:111], v[44:47], v[116:119]\n" \
  "v_mfma_f32_16x16x32_bf16 v[108:111], a[188:191], v[40:43], v[108:111]\n" \
  "v_readlane_b32 s60, v125, s63\n" \
  "v_mfma_f32_16x16x32_bf16 v[120:123], a[188:191], v[44:47], v[120:123]\n" \
  "s_waitcnt lgkmcnt(3)\n" \
  "v_permlane16_swap_b32 v80, v81\n" \
  "v_mfma_f32_16x16x32_bf16 v[100:103], a[32:35], v[48:51], v[100:103]\n" \
  "s_waitcnt lgkmcnt(2)\n" \
  "ds_read_b128 v[40:43], v0 offset:10240\n" \
  "v_add_f32 v80, v80, v81\n" \
  "v_mfma_f32_16x16x32_bf16 v[112:115], a[32:35], v[52:55], v[112:115]\n" \
  "ds_read_b128 v[44:47], v0 offset:30720\n" \
  "v_mov_b32 v81, v80\n" \
  "v_mfma_f32_16x16x32_bf16 v[104:107], a[112:115], v[48:51], v[104:107]\n" \
  "buffer_load_dwordx4 v1, s[36:39], s70 offen offset:1024 sc1 lds\n" \
  "v_mfma_f32_16x16x32_bf16 v[116:119], a[112:115], v[52:55], v[116:119]\n" \
  "v_permlane32_swap_b32 v80, v81\n" \
  "v_mfma_f32_16x16x32_bf16 v[108:111], a[192:195], v[48:51], v[108:111]\n" \
  "s_add_u32 s64, s52, 2\n" \
  "s_min_u32 s64, s64, 57\n" \
  "v_add_f32 v80, v80, v81\n" \
  "v_mfma_f32_16x16x32_bf16 v[120:123], a[192:195], v[52:55], v[120:123]\n" \
  "s_waitcnt lgkmcnt(3)\n" \
  "v_rcp_f32 v80, v80\n" \
  "v_mfma_f32_16x16x32_bf16 v[100:103], a[36:39], v[32:35], v[100:103]\n" \
  "s_waitcnt lgkmcnt(2)\n" \
  "ds_read_b128 v[48:51], v0 offset:11264\n" \
  "v_mul_f32 v244, v244, v80\n" \
  "v_mfma_f32_16x16x32_bf16 v[112:115], a[36:39], v[36:39], v[112:115]\n" \
  "ds_read_b128 v[52:55], v0 offset:31744\n" \
  "v_mul_f32 v245, v245, v80\n" \
  "v_mfma_f32_16x16x32_bf16 v[104:107], a[116:119], v[32:35], v[104:107]\n" \
  "buffer_load_dwordx4 v1, s[36:39], s70 offen offset:2048 sc1 lds\n" \
  "v_mul_f32 v246, v246, v80\n" \
  "v_mfma_f32_16x16x32_bf16 v[116:119], a[116:119], v[36:39], v[116:119]\n" \
  "v_mul_f32 v247, v247, v80\n" \
  "v_mul_f32 v248, v248, v80\n" \
  "v_mfma_f32_16x16x32_bf16 v[108:111], a[196:199], v[32:35], v[108:111]\n" \
  "v_mul_f32 v249, v249, v80\n" \
  "v_mul_f32 v250, v250, v80\n" \
  "v_mfma_f32_16x16x32_bf16 v[120:123], a[196:199], v[36:39], v[120:123]\n" \
  "s_waitcnt lgkmcnt(3)\n" \
  "v_mul_f32 v251, v251, v80\n" \
  "v_mul_f32 v240, v240, v80\n" \
  "v_mfma_f32_16x16x32_bf16 v[100:103], a[40:43], v[40:43], v[100:103]\n" \
  "s_waitcnt lgkmcnt(2)\n" \
  "ds_read_b128 v[32:35], v0 offset:12288\n" \
  "v_mul_f32 v241, v241, v80\n" \
  "v_mfma_f32_16x16x32_bf16 v[112:115], a[40:43], v[44:47], v[112:115]\n" \
  "ds_read_b128 v[36:39], v0 offset:32768\n" \
  "v_mul_f32 v242, v242, v80\n" \
  "v_mfma_f32_16x16x32_bf16 v[104:107], a[120:123], v[40:43], v[104:107]\n" \
  "buffer_load_dwordx4 v1, s[36:39], s70 offen offset:3072 sc1 lds\n" \
  "v_mul_f32 v243, v243, v80\n" \
  "v_mfma_f32_16x16x32_bf16 v[116:119], a[120:123], v[44:47], v[116:119]\n" \
  "v_cvt_pk_bf16_f32 v88, v244, v245\n" \
  "v_cvt_pk_bf16_f32 v89, v246, v247\n" \
  "v_mfma_f32_16x16x32_bf16 v[108:111], a[200:203], v[40:43], v[108:111]\n" \
  "v_cvt_pk_bf16_f32 v90, v248, v249\n" \
  "v_cvt_pk_bf16_f32 v91, v250, v251\n" \
  "v_mfma_f32_16x16x32_bf16 v[120:123], a[200:203], v[44:47], v[120:123]\n" \
  "s_waitcnt lgkmcnt(3)\n" \
  "v_cvt_pk_bf16_f32 v92, v240, v241\n" \
  "v_cvt_pk_bf16_f32 v93, v242, v243\n" \
  "v_mfma_f32_16x16x32_bf16 v[100:103], a[44:47], v[48:51], v[100:103]\n" \
  "ds_read_b128 v[40:43], v0 offset:13312\n" \
  "buffer_store_dwordx4 v[88:91], v127, s[40:43], s61 offen sc1\n" \
  "s_waitcnt lgkmcnt(3)\n" \
  "v_mfma_f32_16x16x32_bf16 v[112:115], a[44:47], v[52:55], v[112:115]\n" \
  "s_add_u32 m0, s53, 90112\n" \
  "ds_read_b128 v[44:47], v0 offset:33792\n" \
  "buffer_store_dwordx2 v[92:93], v128, s[40:43], s61 offen sc1\n" \
  "v_mfma_f32_16x16x32_bf16 v[104:107], a[124:127], v[48:51], v[104:107]\n" \
  "buffer_load_dwordx4 v1, s[36:39], s71 offen sc1 lds\n" \
  "v_mfma_f32_16x16x32_bf16 v[116:119], a[124:127], v[52:55], v[116:119]\n" \
  "v_mfma_f32_16x16x32_bf16 v[108:111], a[204:207], v[48:51], v[108:111]\n" \
  "v_mfma_f32_16x16x32_bf16 v[120:123], a[204:207], v[52:55], v[120:123]\n" \
  "s_waitcnt lgkmcnt(3)\n" \
  "v_mfma_f32_16x16x32_bf16 v[100:103], a[48:51], v[32:35], v[100:103]\n" \
  "s_waitcnt lgkmcnt(2)\n" \
  "ds_read_b128 v[48:51], v0 offset:14336\n" \
  "v_mfma_f32_16x16x32_bf16 v[112:115], a[48:51], v[36:39], v[112:115]\n" \
  "ds_read_b128 v[52:55], v0 offset:34816\n" \
  "v_mfma_f32_16x16x32_bf16 v[104:107], a[128:131], v[32:35], v[104:107]\n" \
  "buffer_load_dwordx4 v1, s[36:39], s71 offen offset:1024 sc1 lds\n" \
  "v_mfma_f32_16x16x32_bf16 v[116:119], a[128:131], v[36:39], v[116:119]\n" \
  "v_mfma_f32_16x16x32_bf16 v[108:111], a[208:211], v[32:35], v[108:111]\n" \
  "s_waitcnt vmcnt(8)\n" \
  "v_cmp_gt_u32 vcc, s64, v11\n" \
  "v_mfma_f32_16x16x32_bf16 v[120:123], a[208:211], v[36:39], v[120:123]\n" \
  "s_add_u32 s68, s68, 40960\n" \
  "s_waitcnt lgkmcnt(3)\n" \
  "v_mfma_f32_16x16x32_bf16 v[100:103], a[52:55], v[40:43], v[100:103]\n" \
  "s_waitcnt lgkmcnt(2)\n" \
  "ds_read_b128 v[32:35], v0 offset:15360\n" \
  "v_mfma_f32_16x16x32_bf16 v[112:115], a[52:55], v[44:47], v[112:115]\n" \
  "s_cbranch_vccnz LP3_%=\n" \
  "LK3_%=:\n" \
  "ds_read_b128 v[36:39], v0 offset:35840\n" \
  "v_mfma_f32_16x16x32_bf16 v[104:107], a[132:135], v[40:43], v[104:107]\n" \
  "v_mfma_f32_16x16x32_bf16 v[116:119], a[132:135], v[44:47], v[116:119]\n" \
  "v_mfma_f32_16x16x32_bf16 v[108:111], a[212:215], v[40:43], v[108:111]\n" \
  "v_mfma_f32_16x16x32_bf16 v[120:123], a[212:215], v[44:47], v[120:123]\n" \
  "buffer_load_dwordx4 v[192:195], v6, s[40:43], s60 offen offset:0 sc1\n" \
  "s_waitcnt lgkmcnt(3)\n" \
  "v_mfma_f32_16x16x32_bf16 v[100:103], a[56:59], v[48:51], v[100:103]\n" \
  "s_waitcnt lgkmcnt(2)\n" \
  "ds_read_b128 v[40:43], v0 offset:16384\n" \
  "v_mfma_f32_16x16x32_bf16 v[112:115], a[56:59], v[52:55], v[112:115]\n" \
  "ds_read_b128 v[44:47], v0 offset:36864\n" \
  "v_mfma_f32_16x16x32_bf16 v[104:107], a[136:139], v[48:51], v[104:107]\n" \
  "buffer_load_dwordx4 v[196:199], v6, s[40:43], s60 offen offset:1024 sc1\n" \
  "v_mfma_f32_16x16x32_bf16 v[116:119], a[136:139], v[52:55], v[116:119]\n" \
  "v_mfma_f32_16x16x32_bf16 v[108:111], a[216:219], v[48:51], v[108:111]\n" \
  "v_mfma_f32_16x16x32_bf16 v[120:123], a[216:219], v[52:55], v[120:123]\n" \
  "buffer_load_dwordx4 v[200:203], v6, s[40:43], s60 offen offset:2048 sc1\n" \
  "s_waitcnt lgkmcnt(3)\n" \
  "v_mfma_f32_16x16x32_bf16 v[100:103], a[60:63], v[32:35], v[100:103]\n" \
  "s_waitcnt lgkmcnt(2)\n" \
  "ds_read_b128 v[48:51], v0 offset:17408\n" \
  "v_mfma_f32_16x16x32_bf16 v[112:115], a[60:63], v[36:39], v[112:115]\n" \
  "ds_read_b128 v[52:55], v0 offset:37888\n" \
  "v_mfma_f32_16x16x32_bf16 v[104:107], a[140:143], v[32:35], v[104:107]\n" \
  "buffer_load_dwordx4 v[204:207], v6, s[40:43], s60 offen offset:3072 sc1\n" \
  "v_mfma_f32_16x16x32_bf16 v[116:119], a[140:143], v[36:39], v[116:119]\n" \
  "v_mfma_f32_16x16x32_bf16 v[108:111], a[220:223], v[32:35], v[108:111]\n" \
  "v_mfma_f32_16x16x32_bf16 v[120:123], a[220:223], v[36:39], v[120:123]\n" \
  "buffer_load_dwordx2 v[208:209], v7, s[40:43], s60 offen offset:0 sc1\n" \
  "s_waitcnt lgkmcnt(3)\n" \
  "v_mfma_f32_16x16x32_bf16 v[100:103], a[64:67], v[40:43], v[100:103]\n" \
  "s_waitcnt lgkmcnt(2)\n" \
  "ds_read_b128 v[32:35], v0 offset:18432\n" \
  "v_mfma_f32_16x16x32_bf16 v[112:115], a[64:67], v[44:47], v[112:115]\n" \
  "ds_read_b128 v[36:39], v0 offset:38912\n" \
  "v_mfma_f32_16x16x32_bf16 v[104:107], a[144:147], v[40:43], v[104:107]\n" \
  "buffer_load_dwordx2 v[210:211], v7, s[40:43], s60 offen offset:1024 sc1\n" \
  "v_mfma_f32_16x16x32_bf16 v[116:119], a[144:147], v[44:47], v[116:119]\n" \
  "v_mfma_f32_16x16x32_bf16 v[108:111], a[224:227], v[40:43], v[108:111]\n" \
  "v_mfma_f32_16x16x32_bf16 v[120:123], a[224:227], v[44:47], v[120:123]\n" \
  "buffer_load_dwordx2 v[212:213], v7, s[40:43], s60 offen offset:2048 sc1\n" \
  "s_waitcnt lgkmcnt(3)\n" \
  "v_mfma_f32_16x16x32_bf16 v[100:103], a[68:71], v[48:51], v[100:103]\n" \
  "s_waitcnt lgkmcnt(2)\n" \
  "ds_read_b128 v[40:43], v0 offset:19456\n" \
  "v_mfma_f32_16x16x32_bf16 v[112:115], a[68:71], v[52:55], v[112:115]\n" \
  "ds_read_b128 v[44:47], v0 offset:39936\n" \
  "v_mfma_f32_16x16x32_bf16 v[104:107], a[148:151], v[48:51], v[104:107]\n" \
  "buffer_load_dwordx2 v[214:215], v7, s[40:43], s60 offen offset:3072 sc1\n" \
  "v_mfma_f32_16x16x32_bf16 v[116:119], a[148:151], v[52:55], v[116:119]\n" \
  "v_mfma_f32_16x16x32_bf16 v[108:111], a[228:231], v[48:51], v[108:111]\n" \
  "v_mfma_f32_16x16x32_bf16 v[120:123], a[228:231], v[52:55], v[120:123]\n" \
  "s_waitcnt vmcnt(33)\n" \
  "s_barrier\n" \
  "s_waitcnt lgkmcnt(3)\n" \
  "v_mfma_f32_16x16x32_bf16 v[100:103], a[72:75], v[32:35], v[100:103]\n" \
  "s_waitcnt lgkmcnt(2)\n" \
  "ds_read_b128 v[48:51], v9 offset:0\n" \
  "v_mfma_f32_16x16x32_bf16 v[112:115], a[72:75], v[36:39], v[112:115]\n" \
  "ds_read_b128 v[52:55], v9 offset:20480\n" \
  "v_mfma_f32_16x16x32_bf16 v[104:107], a[152:155], v[32:35], v[104:107]\n" \
  "v_mfma_f32_16x16x32_bf16 v[116:119], a[152:155], v[36:39], v[116:119]\n" \
  "v_mfma_f32_16x16x32_bf16 v[108:111], a[232:235], v[32:35], v[108:111]\n" \
  "v_mfma_f32_16x16x32_bf16 v[120:123], a[232:235], v[36:39], v[120:123]\n" \
  "s_waitcnt lgkmcnt(3)\n" \
  "v_mfma_f32_16x16x32_bf16 v[100:103], a[76:79], v[40:43], v[100:103]\n" \
  "s_waitcnt lgkmcnt(2)\n" \
  "ds_read_b128 v[32:35], v9 offset:1024\n" \
  "v_mfma_f32_16x16x32_bf16 v[112:115], a[76:79], v[44:47], v[112:115]\n" \
  "ds_read_b128 v[36:39], v9 offset:21504\n" \
  "v_mfma_f32_16x16x32_bf16 v[104:107], a[156:159], v[40:43], v[104:107]\n" \
  "v_mfma_f32_16x16x32_bf16 v[116:119], a[156:159], v[44:47], v[116:119]\n" \
  "v_mfma_f32_16x16x32_bf16 v[108:111], a[236:239], v[40:43], v[108:111]\n" \
  "v_mfma_f32_16x16x32_bf16 v[120:123], a[236:239], v[44:47], v[120:123]\n" \
  "s_add_u32 s52, s52, 1\n" \
  "s_waitcnt lgkmcnt(3)\n" \
  "v_mfma_f32_16x16x32_bf16 v[56:59], a[0:3], v[48:51], 0\n" \
  "s_add_u32 s63, s52, s54\n" \
  "s_waitcnt lgkmcnt(2)\n" \
  "ds_read_b128 v[40:43], v9 offset:2048\n" \
  "v_mfma_f32_16x16x32_bf16 v[68:71], a[0:3], v[52:55], 0\n" \
  "s_sub_u32 s64, s63, 2\n" \
  "ds_read_b128 v[44:47], v9 offset:22528\n" \
  "v_mfma_f32_16x16x32_bf16 v[60:63], a[80:83], v[48:51], 0\n" \
  "v_cvt_pk_bf16_f32 v16, v100, v101\n" \
  "v_cvt_pk_bf16_f32 v17, v102, v103\n" \
  "v_mfma_f32_16x16x32_bf16 v[72:75], a[80:83], v[52:55], 0\n" \
  "v_cvt_pk_bf16_f32 v18, v104, v105\n" \
  "v_cvt_pk_bf16_f32 v19, v106, v107\n" \
  "v_mfma_f32_16x16x32_bf16 v[64:67], a[160:163], v[48:51], 0\n" \
  "v_cvt_pk_bf16_f32 v20, v112, v113\n" \
  "v_cvt_pk_bf16_f32 v21, v114, v115\n" \
  "v_mfma_f32_16x16x32_bf16 v[76:79], a[160:163], v[52:55], 0\n" \
  "s_waitcnt lgkmcnt(3)\n" \
  "v_cvt_pk_bf16_f32 v22, v116, v117\n" \
  "v_cvt_pk_bf16_f32 v23, v118, v119\n" \
  "v_mfma_f32_16x16x32_bf16 v[56:59], a[4:7], v[32:35], v[56:59]\n" \
  "s_waitcnt lgkmcnt(2)\n" \
  "ds_read_b128 v[48:51], v9 offset:3072\n" \
  "v_mfma_f32_16x16x32_bf16 v[68:71], a[4:7], v[36:39], v[68:71]\n" \
  "s_barrier\n" \
  "ds_read_b128 v[52:55], v9 offset:23552\n" \
  "v_mfma_f32_16x16x32_bf16 v[60:63], a[84:87], v[32:35], v[60:63]\n" \
  "v_cvt_pk_bf16_f32 v24, v108, v109\n" \
  "v_cvt_pk_bf16_f32 v25, v110, v111\n" \
  "v_mfma_f32_16x16x32_bf16 v[72:75], a[84:87], v[36:39], v[72:75]\n" \
  "v_cvt_pk_bf16_f32 v26, v120, v121\n" \
  "v_cvt_pk_bf16_f32 v27, v122, v123\n" \
  "v_mfma_f32_16x16x32_bf16 v[64:67], a[164:167], v[32:35], v[64:67]\n" \
  "ds_write_b128 v2, v[16:19] offset:12288\n" \
  "v_mfma_f32_16x16x32_bf16 v[76:79], a[164:167], v[36:39], v[76:79]\n" \
  "s_waitcnt lgkmcnt(4)\n" \
  "v_mfma_f32_16x16x32_bf16 v[56:59], a[8:11], v[40:43], v[56:59]\n" \
  "s_waitcnt lgkmcnt(3)\n" \
  "ds_read_b128 v[32:35], v9 offset:4096\n" \
  "v_mfma_f32_16x16x32_bf16 v[68:71], a[8:11], v[44:47], v[68:71]\n" \
  "ds_read_b128 v[36:39], v9 offset:24576\n" \
  "v_mfma_f32_16x16x32_bf16 v[60:63], a[88:91], v[40:43], v[60:63]\n" \
  "ds_write_b128 v2, v[20:23] offset:13312\n" \
  "v_mfma_f32_16x16x32_bf16 v[72:75], a[88:91], v[44:47], v[72:75]\n" \
  "v_mfma_f32_16x16x32_bf16 v[64:67], a[168:171], v[40:43], v[64:67]\n" \
  "v_mfma_f32_16x16x32_bf16 v[76:79], a[168:171], v[44:47], v[76:79]\n" \
  "s_add_u32 s70, s68, 4096\n" \
  "s_add_u32 s71, s68, 8192\n" \
  "s_waitcnt lgkmcnt(5)\n" \
  "v_mfma_f32_16x16x32_bf16 v[56:59], a[12:15], v[48:51], v[56:59]\n" \
  "s_waitcnt lgkmcnt(4)\n" \
  "ds_write_b128 v2, v[24:27] offset:14336\n" \
  "ds_read_b128 v[40:43], v9 offset:5120\n" \
  "v_mfma_f32_16x16x32_bf16 v[68:71], a[12:15], v[52:55], v[68:71]\n" \
  "s_add_u32 m0, s53, 0\n" \
  "ds_read_b128 v[44:47], v9 offset:25600\n" \
  "v_mfma_f32_16x16x32_bf16 v[60:63], a[92:95], v[48:51], v[60:63]\n" \
  "buffer_load_dwordx4 v1, s[36:39], s68 offen sc1 lds\n" \
  "v_mfma_f32_16x16x32_bf16 v[72:75], a[92:95], v[52:55], v[72:75]\n" \
  "v_mfma_f32_16x16x32_bf16 v[64:67], a[172:175], v[48:51], v[64:67]\n" \
  "v_mfma_f32_16x16x32_bf16 v[76:79], a[172:175], v[52:55], v[76:79]\n" \
  "s_waitcnt lgkmcnt(5)\n" \
  "v_mfma_f32_16x16x32_bf16 v[56:59], a[16:19], v[32:35], v[56:59]\n" \
  "s_waitcnt lgkmcnt(4)\n" \
  "ds_read_b128 v[48:51], v9 offset:6144\n" \
  "v_mfma_f32_16x16x32_bf16 v[68:71], a[16:19], v[36:39], v[68:71]\n" \
  "ds_read_b128 v[52:55], v9 offset:26624\n" \
  "v_mfma_f32_16x16x32_bf16 v[60:63], a[96:99], v[32:35], v[60:63]\n" \
  "buffer_load_dwordx4 v1, s[36:39], s68 offen offset:1024 sc1 lds\n" \
  "v_mfma_f32_16x16x32_bf16 v[72:75], a[96:99], v[36:39], v[72:75]\n" \
  "v_mfma_f32_16x16x32_bf16 v[64:67], a[176:179], v[32:35], v[64:67]\n" \
  "v_readlane_b32 s61, v126, s64\n" \
  "v_mfma_f32_16x16x32_bf16 v[76:79], a[176:179], v[36:39], v[76:79]\n" \
  "s_sub_u32 s64, s52, 2\n" \
  "s_cmp_lt_u32 s64, 56\n" \
  "s_waitcnt lgkmcnt(3)\n" \
  "v_mfma_f32_16x16x32_bf16 v[56:59], a[20:23], v[40:43], v[56:59]\n" \
  "s_cselect_b32 s61, s61, 1073741824\n" \
  "s_waitcnt lgkmcnt(2)\n" \
  "ds_read_b128 v[32:35], v9 offset:7168\n" \
  "v_mfma_f32_16x16x32_bf16 v[68:71], a[20:23], v[44:47], v[68:71]\n" \
  "ds_read_b128 v[36:39], v9 offset:27648\n" \
  "v_mfma_f32_16x16x32_bf16 v[60:63], a[100:103], v[40:43], v[60:63]\n" \
  "buffer_load_dwordx4 v1, s[36:39], s68 offen offset:2048 sc1 lds\n" \
  "v_mfma_f32_16x16x32_bf16 v[72:75], a[100:103], v[44:47], v[72:75]\n" \
  "s_cmp_ge_u32 s52, 4\n" \
  "s_cselect_b32 s62, 0, 1073741824\n" \
  "v_mfma_f32_16x16x32_bf16 v[64:67], a[180:183], v[40:43], v[64:67]\n" \
  "s_waitcnt vmcnt(3) lgkmcnt(4)\n" \
  "s_barrier\n" \
  "v_mfma_f32_16x16x32_bf16 v[76:79], a[180:183], v[44:47], v[76:79]\n" \
  "buffer_atomic_add v12, v129, s[40:43], s62 offen\n" \
  "ds_read_b128 v[216:219], v3 offset:0\n" \
  "s_waitcnt lgkmcnt(4)\n" \
  "v_mfma_f32_16x16x32_bf16 v[56:59], a[24:27], v[48:51], v[56:59]\n" \
  "s_waitcnt lgkmcnt(3)\n" \
  "ds_read_b128 v[40:43], v9 offset:8192\n" \
  "ds_read_b128 v[220:223], v3 offset:3072\n" \
  "v_mfma_f32_16x16x32_bf16 v[68:71], a[24:27], v[52:55], v[68:71]\n" \
  "ds_read_b128 v[44:47], v9 offset:28672\n" \
  "ds_read_b128 v[224:227], v3 offset:6144\n" \
  "v_mfma_f32_16x16x32_bf16 v[60:63], a[104:107], v[48:51], v[60:63]\n" \
  "buffer_load_dwordx4 v1, s[36:39], s68 offen offset:3072 sc1 lds\n" \
  "ds_read_b128 v[228:231], v3 offset:9216\n" \
  "v_mfma_f32_16x16x32_bf16 v[72:75], a[104:107], v[52:55], v[72:75]\n" \
  "ds_read_b64 v[232:233], v4 offset:0\n" \
  "ds_read_b64 v[234:235], v4 offset:3072\n" \
  "v_mfma_f32_16x16x32_bf16 v[64:67], a[184:187], v[48:51], v[64:67]\n" \
  "ds_read_b64 v[236:237], v4 offset:6144\n" \
  "ds_read_b64 v[238:239], v4 offset:9216\n" \
  "v_mfma_f32_16x16x32_bf16 v[76:79], a[184:187], v[52:55], v[76:79]\n" \
  "s_waitcnt lgkmcnt(11)\n" \
  "v_swap_b32 v162, v184\n" \
  "v_mfma_f32_16x16x32_bf16 v[56:59], a[28:31], v[32:35], v[56:59]\n" \
  "s_waitcnt lgkmcnt(10)\n" \
  "ds_read_b128 v[48:51], v9 offset:9216\n" \
  "v_mfma_f32_16x16x32_bf16 v[68:71], a[28:31], v[36:39], v[68:71]\n" \
  "s_add_u32 m0, s53, 4096\n" \
  "ds_read_b128 v[52:55], v9 offset:29696\n" \
  "v_mfma_f32_16x16x32_bf16 v[60:63], a[108:111], v[32:35], v[60:63]\n" \
  "buffer_load_dwordx4 v1, s[36:39], s70 offen sc1 lds\n" \
  "v_mfma_f32_16x16x32_bf16 v[72:75], a[108:111], v[36:39], v[72:75]\n" \
  "v_swap_b32 v163, v185\n" \
  "v_mfma_f32_16x16x32_bf16 v[64:67], a[188:191], v[32:35], v[64:67]\n" \
  "v_swap_b32 v166, v188\n" \
  "v_mfma_f32_16x16x32_bf16 v[76:79], a[188:191], v[36:39], v[76:79]\n" \
  "s_waitcnt lgkmcnt(10)\n" \
  "v_swap_b32 v167, v189\n" \
  "v_mfma_f32_16x16x32_bf16 v[56:59], a[32:35], v[40:43], v[56:59]\n" \
  "s_waitcnt lgkmcnt(8)\n" \
  "ds_read_b128 v[32:35], v9 offset:10240\n" \
  "v_mfma_f32_16x16x32_bf16 v[68:71], a[32:35], v[44:47], v[68:71]\n" \
  "ds_read_b128 v[36:39], v9 offset:30720\n" \
  "v_mfma_f32_16x16x32_bf16 v[240:243], v[192:195], v[216:219], v[132:135]\n" \
  "v_mfma_f32_16x16x32_bf16 v[244:247], v[144:147], v[216:219], v[136:139]\n" \
  "v_mfma_f32_16x16x32_bf16 v[248:251], v[168:171], v[216:219], v[140:143]\n" \
  "v_mfma_f32_16x16x32_bf16 v[60:63], a[112:115], v[40:43], v[60:63]\n" \
  "buffer_load_dwordx4 v1, s[36:39], s70 offen offset:1024 sc1 lds\n" \
  "v_mfma_f32_16x16x32_bf16 v[240:243], v[196:199], v[220:223], v[240:243]\n" \
  "v_mfma_f32_16x16x32_bf16 v[244:247], v[148:151], v[220:223], v[244:247]\n" \
  "v_mfma_f32_16x16x32_bf16 v[248:251], v[172:175], v[220:223], v[248:251]\n" \
  "v_mfma_f32_16x16x32_bf16 v[72:75], a[112:115], v[44:47], v[72:75]\n" \
  "s_waitcnt lgkmcnt(9)\n" \
  "v_mfma_f32_16x16x32_bf16 v[240:243], v[200:203], v[224:227], v[240:243]\n" \
  "v_mfma_f32_16x16x32_bf16 v[244:247], v[152:155], v[224:227], v[244:247]\n" \
  "v_mfma_f32_16x16x32_bf16 v[248:251], v[176:179], v[224:227], v[248:251]\n" \
  "v_mfma_f32_16x16x32_bf16 v[64:67], a[192:195], v[40:43], v[64:67]\n" \
  "s_waitcnt lgkmcnt(8)\n" \
  "v_mfma_f32_16x16x32_bf16 v[240:243], v[204:207], v[228:231], v[240:243]\n" \
  "v_mfma_f32_16x16x32_bf16 v[244:247], v[156:159], v[228:231], v[244:247]\n" \
  "v_mfma_f32_16x16x32_bf16 v[248:251], v[180:183], v[228:231], v[248:251]\n" \
  "v_mfma_f32_16x16x32_bf16 v[76:79], a[192:195], v[44:47], v[76:79]\n" \
  "s_waitcnt lgkmcnt(6)\n" \
  "v_mfma_f32_16x16x32_bf16 v[240:243], v[208:211], v[232:235], v[240:243]\n" \
  "v_mfma_f32_16x16x32_bf16 v[244:247], v[160:163], v[232:235], v[244:247]\n" \
  "v_mfma_f32_16x16x32_bf16 v[248:251], v[184:187], v[232:235], v[248:251]\n" \
  "s_waitcnt lgkmcnt(3)\n" \
  "v_mfma_f32_16x16x32_bf16 v[56:59], a[36:39], v[48:51], v[56:59]\n" \
  "ds_read_b128 v[40:43], v9 offset:11264\n" \
  "v_mfma_f32_16x16x32_bf16 v[240:243], v[212:215], v[236:239], v[240:243]\n" \
  "v_mfma_f32_16x16x32_bf16 v[244:247], v[164:167], v[236:239], v[244:247]\n" \
  "v_mfma_f32_16x16x32_bf16 v[248:251], v[188:191], v[236:239], v[248:251]\n" \
  "s_waitcnt lgkmcnt(3)\n" \
  "v_mfma_f32_16x16x32_bf16 v[68:71], a[36:39], v[52:55], v[68:71]\n" \
  "ds_read_b128 v[44:47], v9 offset:31744\n" \
  "v_mfma_f32_16x16x32_bf16 v[60:63], a[116:119], v[48:51], v[60:63]\n" \
  "buffer_load_dwordx4 v1, s[36:39], s70 offen offset:2048 sc1 lds\n" \
  "v_mfma_f32_16x16x32_bf16 v[72:75], a[116:119], v[52:55], v[72:75]\n" \
  "v_cvt_pk_bf16_f32 v80, v240, v241\n" \
  "v_cvt_pk_bf16_f32 v81, v242, v243\n" \
  "v_mfma_f32_16x16x32_bf16 v[64:67], a[196:199], v[48:51], v[64:67]\n" \
  "v_cvt_pk_bf16_f32 v82, v244, v245\n" \
  "v_cvt_pk_bf16_f32 v83, v246, v247\n" \
  "v_mfma_f32_16x16x32_bf16 v[76:79], a[196:199], v[52:55], v[76:79]\n" \
  "s_waitcnt lgkmcnt(3)\n" \
  "v_cvt_pk_bf16_f32 v84, v248, v249\n" \
  "v_cvt_pk_bf16_f32 v85, v250, v251\n" \
  "v_mfma_f32_16x16x32_bf16 v[56:59], a[40:43], v[32:35], v[56:59]\n" \
  "s_waitcnt lgkmcnt(2)\n" \
  "ds_read_b128 v[48:51], v9 offset:12288\n" \
  "v_lshlrev_b32 v240, 16, v80\n" \
  "v_mfma_f32_16x16x32_bf16 v[68:71], a[40:43], v[36:39], v[68:71]\n" \
  "ds_read_b128 v[52:55], v9 offset:32768\n" \
  "v_and_b32 v241, 0xffff0000, v80\n" \
  "v_mfma_f32_16x16x32_bf16 v[60:63], a[120:123], v[32:35], v[60:63]\n" \
  "buffer_load_dwordx4 v1, s[36:39], s70 offen offset:3072 sc1 lds\n" \
  "v_mul_f32 v240, 0x3d93cd3a, v240\n" \
  "v_mfma_f32_16x16x32_bf16 v[72:75], a[120:123], v[36:39], v[72:75]\n" \
  "v_mul_f32 v241, 0x3d93cd3a, v241\n" \
  "v_cvt_pk_bf16_f32 v80, v240, v241\n" \
  "v_mfma_f32_16x16x32_bf16 v[64:67], a[200:203], v[32:35], v[64:67]\n" \
  "v_lshlrev_b32 v240, 16, v80\n" \
  "v_and_b32 v241, 0xffff0000, v80\n" \
  "v_mfma_f32_16x16x32_bf16 v[76:79], a[200:203], v[36:39], v[76:79]\n" \
  "s_waitcnt lgkmcnt(3)\n" \
  "v_lshlrev_b32 v242, 16, v81\n" \
  "v_and_b32 v243, 0xffff0000, v81\n" \
  "v_mfma_f32_16x16x32_bf16 v[56:59], a[44:47], v[40:43], v[56:59]\n" \
  "s_waitcnt lgkmcnt(2)\n" \
  "ds_read_b128 v[32:35], v9 offset:13312\n" \
  "v_mul_f32 v242, 0x3d93cd3a, v242\n" \
  "v_mfma_f32_16x16x32_bf16 v[68:71], a[44:47], v[44:47], v[68:71]\n" \
  "s_add_u32 m0, s53, 8192\n" \
  "ds_read_b128 v[36:39], v9 offset:33792\n" \
  "v_mul_f32 v243, 0x3d93cd3a, v243\n" \
  "v_mfma_f32_16x16x32_bf16 v[60:63], a[124:127], v[40:43], v[60:63]\n" \
  "buffer_load_dwordx4 v1, s[36:39], s71 offen sc1 lds\n" \
  "v_cvt_pk_bf16_f32 v81, v242, v243\n" \
  "v_mfma_f32_16x16x32_bf16 v[72:75], a[124:127], v[44:47], v[72:75]\n" \
  "v_lshlrev_b32 v242, 16, v81\n" \
  "v_and_b32 v243, 0xffff0000, v81\n" \
  "v_mfma_f32_16x16x32_bf16 v[64:67], a[204:207], v[40:43], v[64:67]\n" \
  "v_readlane_b32 s56, v124, s63\n" \
  "v_mfma_f32_16x16x32_bf16 v[76:79], a[204:207], v[44:47], v[76:79]\n" \
  "s_waitcnt lgkmcnt(3)\n" \
  "v_lshlrev_b32 v244, 16, v82\n" \
  "v_and_b32 v245, 0xffff0000, v82\n" \
  "v_mfma_f32_16x16x32_bf16 v[56:59], a[48:51], v[48:51], v[56:59]\n" \
  "s_waitcnt lgkmcnt(2)\n" \
  "ds_read_b128 v[40:43], v9 offset:14336\n" \
  "v_mul_f32 v244, 0x3d93cd3a, v244\n" \
  "v_mfma_f32_16x16x32_bf16 v[68:71], a[48:51], v[52:55], v[68:71]\n" \
  "ds_read_b128 v[44:47], v9 offset:34816\n" \
  "v_mul_f32 v245, 0x3d93cd3a, v245\n" \
  "v_mfma_f32_16x16x32_bf16 v[60:63], a[128:131], v[48:51], v[60:63]\n" \
  "buffer_load_dwordx4 v1, s[36:39], s71 offen offset:1024 sc1 lds\n" \
  "v_cvt_pk_bf16_f32 v82, v244, v245\n" \
  "v_mfma_f32_16x16x32_bf16 v[72:75], a[128:131], v[52:55], v[72:75]\n" \
  "s_add_u32 s57, s56, 16384\n" \
  "v_lshlrev_b32 v244, 16, v82\n" \
  "v_and_b32 v245, 0xffff0000, v82\n" \
  "v_mfma_f32_16x16x32_bf16 v[64:67], a[208:211], v[48:51], v[64:67]\n" \
  "s_add_u32 s58, s56, 32768\n" \
  "v_lshlrev_b32 v246, 16, v83\n" \
  "v_and_b32 v247, 0xffff0000, v83\n" \
  "v_mfma_f32_16x16x32_bf16 v[76:79], a[208:211], v[52:55], v[76:79]\n" \
  "s_add_u32 s68, s68, 40960\n" \
  "s_add_u32 s59, s56, 49152\n" \
  "s_waitcnt lgkmcnt(3)\n" \
  "v_mul_f32 v246, 0x3d93cd3a, v246\n" \
  "v_mfma_f32_16x16x32_bf16 v[56:59], a[52:55], v[32:35], v[56:59]\n" \
  "s_waitcnt lgkmcnt(2)\n" \
  "ds_read_b128 v[48:51], v9 offset:15360\n" \
  "v_mul_f32 v247, 0x3d93cd3a, v247\n" \
  "v_mfma_f32_16x16x32_bf16 v[68:71], a[52:55], v[36:39], v[68:71]\n" \
  "ds_read_b128 v[52:55], v9 offset:35840\n" \
  "v_cvt_pk_bf16_f32 v83, v246, v247\n" \
  "v_mfma_f32_16x16x32_bf16 v[60:63], a[132:135], v[32:35], v[60:63]\n" \
  "v_lshlrev_b32 v246, 16, v83\n" \
  "v_and_b32 v247, 0xffff0000, v83\n" \
  "v_mfma_f32_16x16x32_bf16 v[72:75], a[132:135], v[36:39], v[72:75]\n" \
  "v_lshlrev_b32 v248, 16, v84\n" \
  "v_and_b32 v249, 0xffff0000, v84\n" \
  "v_mfma_f32_16x16x32_bf16 v[64:67], a[212:215], v[32:35], v[64:67]\n" \
  "v_mul_f32 v248, 0x3d93cd3a, v248\n" \
  "v_mul_f32 v249, 0x3d93cd3a, v249\n" \
  "v_mfma_f32_16x16x32_bf16 v[76:79], a[212:215], v[36:39], v[76:79]\n" \
  "buffer_load_dwordx4 v[144:147], v5, s[40:43], s56 offen offset:0 sc1\n" \
  "v_cvt_pk_bf16_f32 v84, v248, v249\n" \
  "s_waitcnt lgkmcnt(3)\n" \
  "v_mfma_f32_16x16x32_bf16 v[56:59], a[56:59], v[40:43], v[56:59]\n" \
  "s_waitcnt lgkmcnt(2)\n" \
  "ds_read_b128 v[32:35], v9 offset:16384\n" \
  "v_lshlrev_b32 v248, 16, v84\n" \
  "v_mfma_f32_16x16x32_bf16 v[68:71], a[56:59], v[44:47], v[68:71]\n" \
  "ds_read_b128 v[36:39], v9 offset:36864\n" \
  "v_and_b32 v249, 0xffff0000, v84\n" \
  "v_mfma_f32_16x16x32_bf16 v[60:63], a[136:139], v[40:43], v[60:63]\n" \
  "v_lshlrev_b32 v250, 16, v85\n" \
  "v_and_b32 v251, 0xffff0000, v85\n" \
  "v_mfma_f32_16x16x32_bf16 v[72:75], a[136:139], v[44:47], v[72:75]\n" \
  "v_mul_f32 v250, 0x3d93cd3a, v250\n" \
  "v_mul_f32 v251, 0x3d93cd3a, v251\n" \
  "v_mfma_f32_16x16x32_bf16 v[64:67], a[216:219], v[40:43], v[64:67]\n" \
  "v_cvt_pk_bf16_f32 v85, v250, v251\n" \
  "v_lshlrev_b32 v250, 16, v85\n" \
  "v_mfma_f32_16x16x32_bf16 v[76:79], a[216:219], v[44:47], v[76:79]\n" \
  "buffer_load_dwordx4 v[168:171], v5, s[40:43], s56 offen offset:1024 sc1\n" \
  "v_and_b32 v251, 0xffff0000, v85\n" \
  "s_waitcnt lgkmcnt(3)\n" \
  "v_mfma_f32_16x16x32_bf16 v[56:59], a[60:63], v[48:51], v[56:59]\n" \
  "s_waitcnt lgkmcnt(2)\n" \
  "ds_read_b128 v[40:43], v9 offset:17408\n" \
  "v_max3_f32 v80, v240, v241, v242\n" \
  "v_mfma_f32_16x16x32_bf16 v[68:71], a[60:63], v[52:55], v[68:71]\n" \
  "ds_read_b128 v[44:47], v9 offset:37888\n" \
  "v_max3_f32 v80, v80, v243, v244\n" \
  "v_mfma_f32_16x16x32_bf16 v[60:63], a[140:143], v[48:51], v[60:63]\n" \
  "v_max3_f32 v80, v80, v245, v246\n" \
  "v_max3_f32 v80, v80, v247, v248\n" \
  "v_mfma_f32_16x16x32_bf16 v[72:75], a[140:143], v[52:55], v[72:75]\n" \
  "v_max3_f32 v80, v80, v249, v250\n" \
  "v_max_f32 v80, v80, v251\n" \
  "v_mfma_f32_16x16x32_bf16 v[64:67], a[220:223], v[48:51], v[64:67]\n" \
  "v_mov_b32 v81, v80\n" \
  "v_mfma_f32_16x16x32_bf16 v[76:79], a[220:223], v[52:55], v[76:79]\n" \
  "buffer_load_dwordx4 v[148:151], v5, s[40:43], s57 offen offset:0 sc1\n" \
  "s_waitcnt lgkmcnt(3)\n" \
  "v_mfma_f32_16x16x32_bf16 v[56:59], a[64:67], v[32:35], v[56:59]\n" \
  "s_waitcnt lgkmcnt(2)\n" \
  "ds_read_b128 v[48:51], v9 offset:18432\n" \
  "v_mfma_f32_16x16x32_bf16 v[68:71], a[64:67], v[36:39], v[68:71]\n" \
  "ds_read_b128 v[52:55], v9 offset:38912\n" \
  "v_mfma_f32_16x16x32_bf16 v[60:63], a[144:147], v[32:35], v[60:63]\n" \
  "v_permlane16_swap_b32 v80, v81\n" \
  "v_mfma_f32_16x16x32_bf16 v[72:75], a[144:147], v[36:39], v[72:75]\n" \
  "v_max_f32 v80, v80, v81\n" \
  "v_mov_b32 v81, v80\n" \
  "v_mfma_f32_16x16x32_bf16 v[64:67], a[224:227], v[32:35], v[64:67]\n" \
  "v_mfma_f32_16x16x32_bf16 v[76:79], a[224:227], v[36:39], v[76:79]\n" \
  "buffer_load_dwordx4 v[172:175], v5, s[40:43], s57 offen offset:1024 sc1\n" \
  "s_waitcnt lgkmcnt(3)\n" \
  "v_mfma_f32_16x16x32_bf16 v[56:59], a[68:71], v[40:43], v[56:59]\n" \
  "s_waitcnt lgkmcnt(2)\n" \
  "ds_read_b128 v[32:35], v9 offset:19456\n" \
  "v_mfma_f32_16x16x32_bf16 v[68:71], a[68:71], v[44:47], v[68:71]\n" \
  "ds_read_b128 v[36:39], v9 offset:39936\n" \
  "v_mfma_f32_16x16x32_bf16 v[60:63], a[148:151], v[40:43], v[60:63]\n" \
  "v_permlane32_swap_b32 v80, v81\n" \
  "v_mfma_f32_16x16x32_bf16 v[72:75], a[148:151], v[44:47], v[72:75]\n" \
  "v_max_f32 v80, v80, v81\n" \
  "v_mul_f32 v80, 0x3fb8aa3b, v80\n" \
  "v_mfma_f32_16x16x32_bf16 v[64:67], a[228:231], v[40:43], v[64:67]\n" \
  "v_mov_b32 v82, 0x3fb8aa3b\n" \
  "v_fma_f32 v240, v240, v82, -v80\n" \
  "v_mfma_f32_16x16x32_bf16 v[76:79], a[228:231], v[44:47], v[76:79]\n" \
  "buffer_load_dwordx4 v[152:155], v5, s[40:43], s58 offen offset:0 sc1\n" \
  "v_fma_f32 v241, v241, v82, -v80\n" \
  "s_waitcnt vmcnt(24)\n" \
  "s_barrier\n" \
  "s_waitcnt lgkmcnt(3)\n" \
  "v_mfma_f32_16x16x32_bf16 v[56:59], a[72:75], v[48:51], v[56:59]\n" \
  "s_waitcnt lgkmcnt(2)\n" \
  "ds_read_b128 v[40:43], v10 offset:0\n" \
  "v_fma_f32 v242, v242, v82, -v80\n" \
  "v_mfma_f32_16x16x32_bf16 v[68:71], a[72:75], v[52:55], v[68:71]\n" \
  "ds_read_b128 v[44:47], v10 offset:20480\n" \
  "v_fma_f32 v243, v243, v82, -v80\n" \
  "v_mfma_f32_16x16x32_bf16 v[60:63], a[152:155], v[48:51], v[60:63]\n" \
  "v_fma_f32 v244, v244, v82, -v80\n" \
  "v_fma_f32 v245, v245, v82, -v80\n" \
  "v_mfma_f32_16x16x32_bf16 v[72:75], a[152:155], v[52:55], v[72:75]\n" \
  "v_fma_f32 v246, v246, v82, -v80\n" \
  "v_fma_f32 v247, v247, v82, -v80\n" \
  "v_mfma_f32_16x16x32_bf16 v[64:67], a[232:235], v[48:51], v[64:67]\n" \
  "v_fma_f32 v248, v248, v82, -v80\n" \
  "v_fma_f32 v249, v249, v82, -v80\n" \
  "v_mfma_f32_16x16x32_bf16 v[76:79], a[232:235], v[52:55], v[76:79]\n" \
  "buffer_load_dwordx4 v[176:179], v5, s[40:43], s58 offen offset:1024 sc1\n" \
  "v_fma_f32 v250, v250, v82, -v80\n" \
  "s_waitcnt lgkmcnt(3)\n" \
  "v_mfma_f32_16x16x32_bf16 v[56:59], a[76:79], v[32:35], v[56:59]\n" \
  "s_waitcnt lgkmcnt(2)\n" \
  "ds_read_b128 v[48:51], v10 offset:1024\n" \
  "v_fma_f32 v251, v251, v82, -v80\n" \
  "v_mfma_f32_16x16x32_bf16 v[68:71], a[76:79], v[36:39], v[68:71]\n" \
  "ds_read_b128 v[52:55], v10 offset:21504\n" \
  "v_mfma_f32_16x16x32_bf16 v[60:63], a[156:159], v[32:35], v[60:63]\n" \
  "v_exp_f32 v240, v240\n" \
  "v_mfma_f32_16x16x32_bf16 v[72:75], a[156:159], v[36:39], v[72:75]\n" \
  "v_exp_f32 v241, v241\n" \
  "v_mfma_f32_16x16x32_bf16 v[64:67], a[236:239], v[32:35], v[64:67]\n" \
  "v_exp_f32 v242, v242\n" \
  "v_mfma_f32_16x16x32_bf16 v[76:79], a[236:239], v[36:39], v[76:79]\n" \
  "v_exp_f32 v243, v243\n" \
  "s_add_u32 s52, s52, 1\n" \
  "s_waitcnt lgkmcnt(3)\n" \
  "v_mfma_f32_16x16x32_bf16 v[100:103], a[0:3], v[40:43], 0\n" \
  "s_waitcnt lgkmcnt(2)\n" \
  "ds_read_b128 v[32:35], v10 offset:2048\n" \
  "v_mfma_f32_16x16x32_bf16 v[112:115], a[0:3], v[44:47], 0\n" \
  "ds_read_b128 v[36:39], v10 offset:22528\n" \
  "v_mfma_f32_16x16x32_bf16 v[104:107], a[80:83], v[40:43], 0\n" \
  "v_cvt_pk_bf16_f32 v16, v56, v57\n" \
  "v_cvt_pk_bf16_f32 v17, v58, v59\n" \
  "v_mfma_f32_16x16x32_bf16 v[116:119], a[80:83], v[44:47], 0\n" \
  "v_cvt_pk_bf16_f32 v18, v60, v61\n" \
  "v_cvt_pk_bf16_f32 v19, v62, v63\n" \
  "v_mfma_f32_16x16x32_bf16 v[108:111], a[160:163], v[40:43], 0\n" \
  "v_cvt_pk_bf16_f32 v20, v68, v69\n" \
  "v_cvt_pk_bf16_f32 v21, v70, v71\n" \
  "v_mfma_f32_16x16x32_bf16 v[120:123], a[160:163], v[44:47], 0\n" \
  "v_cvt_pk_bf16_f32 v22, v72, v73\n" \
  "v_cvt_pk_bf16_f32 v23, v74, v75\n" \
  "buffer_load_dwordx4 v[156:159], v5, s[40:43], s59 offen offset:0 sc1\n" \
  "s_waitcnt lgkmcnt(3)\n" \
  "v_mfma_f32_16x16x32_bf16 v[100:103], a[4:7], v[48:51], v[100:103]\n" \
  "s_waitcnt lgkmcnt(2)\n" \
  "ds_read_b128 v[40:43], v10 offset:3072\n" \
  "v_mfma_f32_16x16x32_bf16 v[112:115], a[4:7], v[52:55], v[112:115]\n" \
  "s_barrier\n" \
  "ds_read_b128 v[44:47], v10 offset:23552\n" \
  "v_mfma_f32_16x16x32_bf16 v[104:107], a[84:87], v[48:51], v[104:107]\n" \
  "v_cvt_pk_bf16_f32 v24, v64, v65\n" \
  "v_cvt_pk_bf16_f32 v25, v66, v67\n" \
  "v_mfma_f32_16x16x32_bf16 v[116:119], a[84:87], v[52:55], v[116:119]\n" \
  "v_cvt_pk_bf16_f32 v26, v76, v77\n" \
  "v_cvt_pk_bf16_f32 v27, v78, v79\n" \
  "v_mfma_f32_16x16x32_bf16 v[108:111], a[164:167], v[48:51], v[108:111]\n" \
  "ds_write_b128 v2, v[16:19] offset:0\n" \
  "v_mfma_f32_16x16x32_bf16 v[120:123], a[164:167], v[52:55], v[120:123]\n" \
  "buffer_load_dwordx4 v[180:183], v5, s[40:43], s59 offen offset:1024 sc1\n" \
  "s_waitcnt lgkmcnt(4)\n" \
  "v_mfma_f32_16x16x32_bf16 v[100:103], a[8:11], v[32:35], v[100:103]\n" \
  "s_waitcnt lgkmcnt(3)\n" \
  "ds_read_b128 v[48:51], v10 offset:4096\n" \
  "v_mfma_f32_16x16x32_bf16 v[112:115], a[8:11], v[36:39], v[112:115]\n" \
  "ds_read_b128 v[52:55], v10 offset:24576\n" \
  "v_mfma_f32_16x16x32_bf16 v[104:107], a[88:91], v[32:35], v[104:107]\n" \
  "ds_write_b128 v2, v[20:23] offset:1024\n" \
  "v_mfma_f32_16x16x32_bf16 v[116:119], a[88:91], v[36:39], v[116:119]\n" \
  "v_exp_f32 v244, v244\n" \
  "v_mfma_f32_16x16x32_bf16 v[108:111], a[168:171], v[32:35], v[108:111]\n" \
  "v_exp_f32 v245, v245\n" \
  "v_mfma_f32_16x16x32_bf16 v[120:123], a[168:171], v[36:39], v[120:123]\n" \
  "s_add_u32 s70, s68, 4096\n" \
  "s_add_u32 s71, s68, 8192\n" \
  "buffer_load_dwordx4 v[160:163], v5, s[40:43], s56 offen offset:2048 sc1\n" \
  "s_waitcnt lgkmcnt(5)\n" \
  "v_mfma_f32_16x16x32_bf16 v[100:103], a[12:15], v[40:43], v[100:103]\n" \
  "s_waitcnt lgkmcnt(4)\n" \
  "ds_write_b128 v2, v[24:27] offset:2048\n" \
  "ds_read_b128 v[32:35], v10 offset:5120\n" \
  "v_mfma_f32_16x16x32_bf16 v[112:115], a[12:15], v[44:47], v[112:115]\n" \
  "s_add_u32 m0, s53, 40960\n" \
  "ds_read_b128 v[36:39], v10 offset:25600\n" \
  "v_mfma_f32_16x16x32_bf16 v[104:107], a[92:95], v[40:43], v[104:107]\n" \
  "buffer_load_dwordx4 v1, s[36:39], s68 offen sc1 lds\n" \
  "v_mfma_f32_16x16x32_bf16 v[116:119], a[92:95], v[44:47], v[116:119]\n" \
  "v_exp_f32 v246, v246\n" \
  "v_mfma_f32_16x16x32_bf16 v[108:111], a[172:175], v[40:43], v[108:111]\n" \
  "v_exp_f32 v247, v247\n" \
  "v_mfma_f32_16x16x32_bf16 v[120:123], a[172:175], v[44:47], v[120:123]\n" \
  "buffer_load_dwordx4 v[184:187], v5, s[40:43], s57 offen offset:2048 sc1\n" \
  "s_waitcnt lgkmcnt(5)\n" \
  "v_mfma_f32_16x16x32_bf16 v[100:103], a[16:19], v[48:51], v[100:103]\n" \
  "s_waitcnt lgkmcnt(4)\n" \
  "ds_read_b128 v[40:43], v10 offset:6144\n" \
  "v_mfma_f32_16x16x32_bf16 v[112:115], a[16:19], v[52:55], v[112:115]\n" \
  "ds_read_b128 v[44:47], v10 offset:26624\n" \
  "v_mfma_f32_16x16x32_bf16 v[104:107], a[96:99], v[48:51], v[104:107]\n" \
  "buffer_load_dwordx4 v1, s[36:39], s68 offen offset:1024 sc1 lds\n" \
  "v_mfma_f32_16x16x32_bf16 v[116:119], a[96:99], v[52:55], v[116:119]\n" \
  "v_exp_f32 v248, v248\n" \
  "v_mfma_f32_16x16x32_bf16 v[108:111], a[176:179], v[48:51], v[108:111]\n" \
  "v_exp_f32 v249, v249\n" \
  "v_mfma_f32_16x16x32_bf16 v[120:123], a[176:179], v[52:55], v[120:123]\n" \
  "buffer_load_dwordx4 v[164:167], v5, s[40:43], s58 offen offset:2048 sc1\n" \
  "s_waitcnt lgkmcnt(3)\n" \
  "v_mfma_f32_16x16x32_bf16 v[100:103], a[20:23], v[32:35], v[100:103]\n" \
  "s_waitcnt lgkmcnt(2)\n" \
  "ds_read_b128 v[48:51], v10 offset:7168\n" \
  "v_mfma_f32_16x16x32_bf16 v[112:115], a[20:23], v[36:39], v[112:115]\n" \
  "ds_read_b128 v[52:55], v10 offset:27648\n" \
  "v_mfma_f32_16x16x32_bf16 v[104:107], a[100:103], v[32:35], v[104:107]\n" \
  "buffer_load_dwordx4 v1, s[36:39], s68 offen offset:2048 sc1 lds\n" \
  "v_mfma_f32_16x16x32_bf16 v[116:119], a[100:103], v[36:39], v[116:119]\n" \
  "v_exp_f32 v250, v250\n" \
  "v_mfma_f32_16x16x32_bf16 v[108:111], a[180:183], v[32:35], v[108:111]\n" \
  "v_exp_f32 v251, v251\n" \
  "v_mfma_f32_16x16x32_bf16 v[120:123], a[180:183], v[36:39], v[120:123]\n" \
  "buffer_load_dwordx4 v[188:191], v5, s[40:43], s59 offen offset:2048 sc1\n" \
  "v_add_f32 v80, v240, v241\n" \
  "s_waitcnt lgkmcnt(3)\n" \
  "v_mfma_f32_16x16x32_bf16 v[100:103], a[24:27], v[40:43], v[100:103]\n" \
  "s_waitcnt lgkmcnt(2)\n" \
  "ds_read_b128 v[32:35], v10 offset:8192\n" \
  "v_add_f32 v80, v80, v242\n" \
  "v_mfma_f32_16x16x32_bf16 v[112:115], a[24:27], v[44:47], v[112:115]\n" \
  "ds_read_b128 v[36:39], v10 offset:28672\n" \
  "v_add_f32 v80, v80, v243\n" \
  "v_mfma_f32_16x16x32_bf16 v[104:107], a[104:107], v[40:43], v[104:107]\n" \
  "buffer_load_dwordx4 v1, s[36:39], s68 offen offset:3072 sc1 lds\n" \
  "v_add_f32 v80, v80, v244\n" \
  "v_mfma_f32_16x16x32_bf16 v[116:119], a[104:107], v[44:47], v[116:119]\n" \
  "v_add_f32 v80, v80, v245\n" \
  "v_add_f32 v80, v80, v246\n" \
  "v_mfma_f32_16x16x32_bf16 v[108:111], a[184:187], v[40:43], v[108:111]\n" \
  "v_add_f32 v80, v80, v247\n" \
  "v_add_f32 v80, v80, v248\n" \
  "v_mfma_f32_16x16x32_bf16 v[120:123], a[184:187], v[44:47], v[120:123]\n" \
  "buffer_load_dword v11, v8, s[40:43], 0 offen sc1\n" \
  "v_add_f32 v80, v80, v249\n" \
  "s_waitcnt lgkmcnt(3)\n" \
  "v_mfma_f32_16x16x32_bf16 v[100:103], a[28:31], v[48:51], v[100:103]\n" \
  "s_waitcnt lgkmcnt(2)\n" \
  "ds_read_b128 v[40:43], v10 offset:9216\n" \
  "v_add_f32 v80, v80, v250\n" \
  "v_mfma_f32_16x16x32_bf16 v[112:115], a[28:31], v[52:55], v[112:115]\n" \
  "s_add_u32 m0, s53, 45056\n" \
  "ds_read_b128 v[44:47], v10 offset:29696\n" \
  "v_add_f32 v80, v80, v251\n" \
  "v_mfma_f32_16x16x32_bf16 v[104:107], a[108:111], v[48:51], v[104:107]\n" \
  "buffer_load_dwordx4 v1, s[36:39], s70 offen sc1 lds\n" \
  "v_mov_b32 v81, v80\n" \
  "v_mfma_f32_16x16x32_bf16 v[116:119], a[108:111], v[52:55], v[116:119]\n" \
  "v_mfma_f32_16x16x32_bf16 v[108:111], a[188:191], v[48:51], v[108:111]\n" \
  "v_readlane_b32 s60, v125, s63\n" \
  "v_mfma_f32_16x16x32_bf16 v[120:123], a[188:191], v[52:55], v[120:123]\n" \
  "s_waitcnt lgkmcnt(3)\n" \
  "v_permlane16_swap_b32 v80, v81\n" \
  "v_mfma_f32_16x16x32_bf16 v[100:103], a[32:35], v[32:35], v[100:103]\n" \
  "s_waitcnt lgkmcnt(2)\n" \
  "ds_read_b128 v[48:51], v10 offset:10240\n" \
  "v_add_f32 v80, v80, v81\n" \
  "v_mfma_f32_16x16x32_bf16 v[112:115], a[32:35], v[36:39], v[112:115]\n" \
  "ds_read_b128 v[52:55], v10 offset:30720\n" \
  "v_mov_b32 v81, v80\n" \
  "v_mfma_f32_16x16x32_bf16 v[104:107], a[112:115], v[32:35], v[104:107]\n" \
  "buffer_load_dwordx4 v1, s[36:39], s70 offen offset:1024 sc1 lds\n" \
  "v_mfma_f32_16x16x32_bf16 v[116:119], a[112:115], v[36:39], v[116:119]\n" \
  "v_permlane32_swap_b32 v80, v81\n" \
  "v_mfma_f32_16x16x32_bf16 v[108:111], a[192:195], v[32:35], v[108:111]\n" \
  "s_add_u32 s64, s52, 2\n" \
  "s_min_u32 s64, s64, 57\n" \
  "v_add_f32 v80, v80, v81\n" \
  "v_mfma_f32_16x16x32_bf16 v[120:123], a[192:195], v[36:39], v[120:123]\n" \
  "s_waitcnt lgkmcnt(3)\n" \
  "v_rcp_f32 v80, v80\n" \
  "v_mfma_f32_16x16x32_bf16 v[100:103], a[36:39], v[40:43], v[100:103]\n" \
  "s_waitcnt lgkmcnt(2)\n" \
  "ds_read_b128 v[32:35], v10 offset:11264\n" \
  "v_mul_f32 v244, v244, v80\n" \
  "v_mfma_f32_16x16x32_bf16 v[112:115], a[36:39], v[44:47], v[112:115]\n" \
  "ds_read_b128 v[36:39], v10 offset:31744\n" \
  "v_mul_f32 v245, v245, v80\n" \
  "v_mfma_f32_16x16x32_bf16 v[104:107], a[116:119], v[40:43], v[104:107]\n" \
  "buffer_load_dwordx4 v1, s[36:39], s70 offen offset:2048 sc1 lds\n" \
  "v_mul_f32 v246, v246, v80\n" \
  "v_mfma_f32_16x16x32_bf16 v[116:119], a[116:119], v[44:47], v[116:119]\n" \
  "v_mul_f32 v247, v247, v80\n" \
  "v_mul_f32 v248, v248, v80\n" \
  "v_mfma_f32_16x16x32_bf16 v[108:111], a[196:199], v[40:43], v[108:111]\n" \
  "v_mul_f32 v249, v249, v80\n" \
  "v_mul_f32 v250, v250, v80\n" \
  "v_mfma_f32_16x16x32_bf16 v[120:123], a[196:199], v[44:47], v[120:123]\n" \
  "s_waitcnt lgkmcnt(3)\n" \
  "v_mul_f32 v251, v251, v80\n" \
  "v_mul_f32 v240, v240, v80\n" \
  "v_mfma_f32_16x16x32_bf16 v[100:103], a[40:43], v[48:51], v[100:103]\n" \
  "s_waitcnt lgkmcnt(2)\n" \
  "ds_read_b128 v[40:43], v10 offset:12288\n" \
  "v_mul_f32 v241, v241, v80\n" \
  "v_mfma_f32_16x16x32_bf16 v[112:115], a[40:43], v[52:55], v[112:115]\n" \
  "ds_read_b128 v[44:47], v10 offset:32768\n" \
  "v_mul_f32 v242, v242, v80\n" \
  "v_mfma_f32_16x16x32_bf16 v[104:107], a[120:123], v[48:51], v[104:107]\n" \
  "buffer_load_dwordx4 v1, s[36:39], s70 offen offset:3072 sc1 lds\n" \
  "v_mul_f32 v243, v243, v80\n" \
  "v_mfma_f32_16x16x32_bf16 v[116:119], a[120:123], v[52:55], v[116:119]\n" \
  "v_cvt_pk_bf16_f32 v88, v244, v245\n" \
  "v_cvt_pk_bf16_f32 v89, v246, v247\n" \
  "v_mfma_f32_16x16x32_bf16 v[108:111], a[200:203], v[48:51], v[108:111]\n" \
  "v_cvt_pk_bf16_f32 v90, v248, v249\n" \
  "v_cvt_pk_bf16_f32 v91, v250, v251\n" \
  "v_mfma_f32_16x16x32_bf16 v[120:123], a[200:203], v[52:55], v[120:123]\n" \
  "s_waitcnt lgkmcnt(3)\n" \
  "v_cvt_pk_bf16_f32 v92, v240, v241\n" \
  "v_cvt_pk_bf16_f32 v93, v242, v243\n" \
  "v_mfma_f32_16x16x32_bf16 v[100:103], a[44:47], v[32:35], v[100:103]\n" \
  "ds_read_b128 v[48:51], v10 offset:13312\n" \
  "buffer_store_dwordx4 v[88:91], v127, s[40:43], s61 offen sc1\n" \
  "s_waitcnt lgkmcnt(3)\n" \
  "v_mfma_f32_16x16x32_bf16 v[112:115], a[44:47], v[36:39], v[112:115]\n" \
  "s_add_u32 m0, s53, 49152\n" \
  "ds_read_b128 v[52:55], v10 offset:33792\n" \
  "buffer_store_dwordx2 v[92:93], v128, s[40:43], s61 offen sc1\n" \
  "v_mfma_f32_16x16x32_bf16 v[104:107], a[124:127], v[32:35], v[104:107]\n" \
  "buffer_load_dwordx4 v1, s[36:39], s71 offen sc1 lds\n" \
  "v_mfma_f32_16x16x32_bf16 v[116:119], a[124:127], v[36:39], v[116:119]\n" \
  "v_mfma_f32_16x16x32_bf16 v[108:111], a[204:207], v[32:35], v[108:111]\n" \
  "v_mfma_f32_16x16x32_bf16 v[120:123], a[204:207], v[36:39], v[120:123]\n" \
  "s_waitcnt lgkmcnt(3)\n" \
  "v_mfma_f32_16x16x32_bf16 v[100:103], a[48:51], v[40:43], v[100:103]\n" \
  "s_waitcnt lgkmcnt(2)\n" \
  "ds_read_b128 v[32:35], v10 offset:14336\n" \
  "v_mfma_f32_16x16x32_bf16 v[112:115], a[48:51], v[44:47], v[112:115]\n" \
  "ds_read_b128 v[36:39], v10 offset:34816\n" \
  "v_mfma_f32_16x16x32_bf16 v[104:107], a[128:131], v[40:43], v[104:107]\n" \
  "buffer_load_dwordx4 v1, s[36:39], s71 offen offset:1024 sc1 lds\n" \
  "v_mfma_f32_16x16x32_bf16 v[116:119], a[128:131], v[44:47], v[116:119]\n" \
  "v_mfma_f32_16x16x32_bf16 v[108:111], a[208:211], v[40:43], v[108:111]\n" \
  "s_waitcnt vmcnt(8)\n" \
  "v_cmp_gt_u32 vcc, s64, v11\n" \
  "v_mfma_f32_16x16x32_bf16 v[120:123], a[208:211], v[44:47], v[120:123]\n" \
  "s_add_u32 s68, s68, 40960\n" \
  "s_waitcnt lgkmcnt(3)\n" \
  "v_mfma_f32_16x16x32_bf16 v[100:103], a[52:55], v[48:51], v[100:103]\n" \
  "s_waitcnt lgkmcnt(2)\n" \
  "ds_read_b128 v[40:43], v10 offset:15360\n" \
  "v_mfma_f32_16x16x32_bf16 v[112:115], a[52:55], v[52:55], v[112:115]\n" \
  "s_cbranch_vccnz LP5_%=\n" \
  "LK5_%=:\n" \
  "ds_read_b128 v[44:47], v10 offset:35840\n" \
  "v_mfma_f32_16x16x32_bf16 v[104:107], a[132:135], v[48:51], v[104:107]\n" \
  "v_mfma_f32_16x16x32_bf16 v[116:119], a[132:135], v[52:55], v[116:119]\n" \
  "v_mfma_f32_16x16x32_bf16 v[108:111], a[212:215], v[48:51], v[108:111]\n" \
  "v_mfma_f32_16x16x32_bf16 v[120:123], a[212:215], v[52:55], v[120:123]\n" \
  "buffer_load_dwordx4 v[192:195], v6, s[40:43], s60 offen offset:0 sc1\n" \
  "s_waitcnt lgkmcnt(3)\n" \
  "v_mfma_f32_16x16x32_bf16 v[100:103], a[56:59], v[32:35], v[100:103]\n" \
  "s_waitcnt lgkmcnt(2)\n" \
  "ds_read_b128 v[48:51], v10 offset:16384\n" \
  "v_mfma_f32_16x16x32_bf16 v[112:115], a[56:59], v[36:39], v[112:115]\n" \
  "ds_read_b128 v[52:55], v10 offset:36864\n" \
  "v_mfma_f32_16x16x32_bf16 v[104:107], a[136:139], v[32:35], v[104:107]\n" \
  "buffer_load_dwordx4 v[196:199], v6, s[40:43], s60 offen offset:1024 sc1\n" \
  "v_mfma_f32_16x16x32_bf16 v[116:119], a[136:139], v[36:39], v[116:119]\n" \
  "v_mfma_f32_16x16x32_bf16 v[108:111], a[216:219], v[32:35], v[108:111]\n" \
  "v_mfma_f32_16x16x32_bf16 v[120:123], a[216:219], v[36:39], v[120:123]\n" \
  "buffer_load_dwordx4 v[200:203], v6, s[40:43], s60 offen offset:2048 sc1\n" \
  "s_waitcnt lgkmcnt(3)\n" \
  "v_mfma_f32_16x16x32_bf16 v[100:103], a[60:63], v[40:43], v[100:103]\n" \
  "s_waitcnt lgkmcnt(2)\n" \
  "ds_read_b128 v[32:35], v10 offset:17408\n" \
  "v_mfma_f32_16x16x32_bf16 v[112:115], a[60:63], v[44:47], v[112:115]\n" \
  "ds_read_b128 v[36:39], v10 offset:37888\n" \
  "v_mfma_f32_16x16x32_bf16 v[104:107], a[140:143], v[40:43], v[104:107]\n" \
  "buffer_load_dwordx4 v[204:207], v6, s[40:43], s60 offen offset:3072 sc1\n" \
  "v_mfma_f32_16x16x32_bf16 v[116:119], a[140:143], v[44:47], v[116:119]\n" \
  "v_mfma_f32_16x16x32_bf16 v[108:111], a[220:223], v[40:43], v[108:111]\n" \
  "v_mfma_f32_16x16x32_bf16 v[120:123], a[220:223], v[44:47], v[120:123]\n" \
  "buffer_load_dwordx2 v[208:209], v7, s[40:43], s60 offen offset:0 sc1\n" \
  "s_waitcnt lgkmcnt(3)\n" \
  "v_mfma_f32_16x16x32_bf16 v[100:103], a[64:67], v[48:51], v[100:103]\n" \
  "s_waitcnt lgkmcnt(2)\n" \
  "ds_read_b128 v[40:43], v10 offset:18432\n" \
  "v_mfma_f32_16x16x32_bf16 v[112:115], a[64:67], v[52:55], v[112:115]\n" \
  "ds_read_b128 v[44:47], v10 offset:38912\n" \
  "v_mfma_f32_16x16x32_bf16 v[104:107], a[144:147], v[48:51], v[104:107]\n" \
  "buffer_load_dwordx2 v[210:211], v7, s[40:43], s60 offen offset:1024 sc1\n" \
  "v_mfma_f32_16x16x32_bf16 v[116:119], a[144:147], v[52:55], v[116:119]\n" \
  "v_mfma_f32_16x16x32_bf16 v[108:111], a[224:227], v[48:51], v[108:111]\n" \
  "v_mfma_f32_16x16x32_bf16 v[120:123], a[224:227], v[52:55], v[120:123]\n" \
  "buffer_load_dwordx2 v[212:213], v7, s[40:43], s60 offen offset:2048 sc1\n" \
  "s_waitcnt lgkmcnt(3)\n" \
  "v_mfma_f32_16x16x32_bf16 v[100:103], a[68:71], v[32:35], v[100:103]\n" \
  "s_waitcnt lgkmcnt(2)\n" \
  "ds_read_b128 v[48:51], v10 offset:19456\n" \
  "v_mfma_f32_16x16x32_bf16 v[112:115], a[68:71], v[36:39], v[112:115]\n" \
  "ds_read_b128 v[52:55], v10 offset:39936\n" \
  "v_mfma_f32_16x16x32_bf16 v[104:107], a[148:151], v[32:35], v[104:107]\n" \
  "buffer_load_dwordx2 v[214:215], v7, s[40:43], s60 offen offset:3072 sc1\n" \
  "v_mfma_f32_16x16x32_bf16 v[116:119], a[148:151], v[36:39], v[116:119]\n" \
  "v_mfma_f32_16x16x32_bf16 v[108:111], a[228:231], v[32:35], v[108:111]\n" \
  "v_mfma_f32_16x16x32_bf16 v[120:123], a[228:231], v[36:39], v[120:123]\n" \
  "s_waitcnt vmcnt(33)\n" \
  "s_barrier\n" \
  "s_waitcnt lgkmcnt(3)\n" \
  "v_mfma_f32_16x16x32_bf16 v[100:103], a[72:75], v[40:43], v[100:103]\n" \
  "s_waitcnt lgkmcnt(2)\n" \
  "ds_read_b128 v[32:35], v0 offset:0\n" \
  "v_mfma_f32_16x16x32_bf16 v[112:115], a[72:75], v[44:47], v[112:115]\n" \
  "ds_read_b128 v[36:39], v0 offset:20480\n" \
  "v_mfma_f32_16x16x32_bf16 v[104:107], a[152:155], v[40:43], v[104:107]\n" \
  "v_mfma_f32_16x16x32_bf16 v[116:119], a[152:155], v[44:47], v[116:119]\n" \
  "v_mfma_f32_16x16x32_bf16 v[108:111], a[232:235], v[40:43], v[108:111]\n" \
  "v_mfma_f32_16x16x32_bf16 v[120:123], a[232:235], v[44:47], v[120:123]\n" \
  "s_waitcnt lgkmcnt(3)\n" \
  "v_mfma_f32_16x16x32_bf16 v[100:103], a[76:79], v[48:51], v[100:103]\n" \
  "s_waitcnt lgkmcnt(2)\n" \
  "ds_read_b128 v[40:43], v0 offset:1024\n" \
  "v_mfma_f32_16x16x32_bf16 v[112:115], a[76:79], v[52:55], v[112:115]\n" \
  "ds_read_b128 v[44:47], v0 offset:21504\n" \
  "v_mfma_f32_16x16x32_bf16 v[104:107], a[156:159], v[48:51], v[104:107]\n" \
  "v_mfma_f32_16x16x32_bf16 v[116:119], a[156:159], v[52:55], v[116:119]\n" \
  "v_mfma_f32_16x16x32_bf16 v[108:111], a[236:239], v[48:51], v[108:111]\n" \
  "v_mfma_f32_16x16x32_bf16 v[120:123], a[236:239], v[52:55], v[120:123]\n" \
  "s_add_u32 s52, s52, 1\n" \
  "s_cmp_lt_u32 s52, 60\n" \
  "s_cbranch_scc1 LL_%=\n" \
  "s_waitcnt vmcnt(0) lgkmcnt(0)\n" \
  "s_branch LX_%=\n" \
  "LP1_%=:\n" \
  "s_add_u32 s65, s65, 1\n" \
  "s_cmp_gt_u32 s65, 262144\n" \
  "s_cbranch_scc1 LK1_%=\n" \
  "s_sleep 6\n" \
  "buffer_load_dword v11, v8, s[40:43], 0 offen sc1\n" \
  "s_waitcnt vmcnt(0)\n" \
  "v_cmp_gt_u32 vcc, s64, v11\n" \
  "s_cbranch_vccnz LP1_%=\n" \
  "s_branch LK1_%=\n" \
  "LP3_%=:\n" \
  "s_add_u32 s65, s65, 1\n" \
  "s_cmp_gt_u32 s65, 262144\n" \
  "s_cbranch_scc1 LK3_%=\n" \
  "s_sleep 6\n" \
  "buffer_load_dword v11, v8, s[40:43], 0 offen sc1\n" \
  "s_waitcnt vmcnt(0)\n" \
  "v_cmp_gt_u32 vcc, s64, v11\n" \
  "s_cbranch_vccnz LP3_%=\n" \
  "s_branch LK3_%=\n" \
  "LP5_%=:\n" \
  "s_add_u32 s65, s65, 1\n" \
  "s_cmp_gt_u32 s65, 262144\n" \
  "s_cbranch_scc1 LK5_%=\n" \
  "s_sleep 6\n" \
  "buffer_load_dword v11, v8, s[40:43], 0 offen sc1\n" \
  "s_waitcnt vmcnt(0)\n" \
  "v_cmp_gt_u32 vcc, s64, v11\n" \
  "s_cbranch_vccnz LP5_%=\n" \
  "s_branch LK5_%=\n" \
  "LX_%=:\n" \
  "s_nop 7\n"
#define PROJK_CLOBBERS \
  "v9", "v10", "v11", "v12", "v13", "v14", "v15", "v16", "v17", "v18", "v19", "v20", "v21", "v22", "v23", "v24", "v25", "v26", "v27", "v28", "v29", \
  "v30", "v31", "v32", "v33", "v34", "v35", "v36", "v37", "v38", "v39", "v40", "v41", "v42", "v43", "v44", "v45", "v46", "v47", "v48", "v49", "v50", \
  "v51", "v52", "v53", "v54", "v55", "v56", "v57", "v58", "v59", "v60", "v61", "v62", "v63", "v64", "v65", "v66", "v67", "v68", "v69", "v70", "v71", \
  "v72", "v73", "v74", "v75", "v76", "v77", "v78", "v79", "v100", "v101", "v102", "v103", "v104", "v105", "v106", "v107", "v108", "v109", "v110", \
  "v111", "v112", "v113", "v114", "v115", "v116", "v117", "v118", "v119", "v120", "v121", "v122", "v123", "s68", "s69", "s70", "s71", "s72", "s73"
#define PROJK_ASM \
  "s_waitcnt vmcnt(10)\n" \
  "s_barrier\n" \
  "ds_read_b128 v[32:35], v0 offset:0\n" \
  "ds_read_b128 v[36:39], v0 offset:20480\n" \
  "ds_read_b128 v[40:43], v0 offset:1024\n" \
  "ds_read_b128 v[44:47], v0 offset:21504\n" \
  "v_mov_b32 v12, -1\n" \
  "v_mov_b32 v13, -1\n" \
  "v_mov_b32 v14, -1\n" \
  "v_mov_b32 v15, -1\n" \
  "v_mov_b32 v11, 1\n" \
  "v_add_u32 v9, 40960, v0\n" \
  "v_add_u32 v10, 81920, v0\n" \
  "s_mul_i32 s68, s52, 40960\n" \
  "s_add_u32 s68, s68, 81920\n" \
  "s_mov_b32 s72, s52\n" \
  "s_waitcnt lgkmcnt(2)\n" \
  "v_mfma_f32_16x16x32_bf16 v[56:59], a[0:3], v[32:35], 0\n" \
  "ds_read_b128 v[48:51], v0 offset:2048\n" \
  "v_mfma_f32_16x16x32_bf16 v[68:71], a[0:3], v[36:39], 0\n" \
  "ds_read_b128 v[52:55], v0 offset:22528\n" \
  "v_mfma_f32_16x16x32_bf16 v[60:63], a[80:83], v[32:35], 0\n" \
  "v_mfma_f32_16x16x32_bf16 v[72:75], a[80:83], v[36:39], 0\n" \
  "v_mfma_f32_16x16x32_bf16 v[64:67], a[160:163], v[32:35], 0\n" \
  "v_mfma_f32_16x16x32_bf16 v[76:79], a[160:163], v[36:39], 0\n" \
  "s_waitcnt lgkmcnt(2)\n" \
  "s_barrier\n" \
  "v_mfma_f32_16x16x32_bf16 v[56:59], a[4:7], v[40:43], v[56:59]\n" \
  "ds_read_b128 v[32:35], v0 offset:3072\n" \
  "v_mfma_f32_16x16x32_bf16 v[68:71], a[4:7], v[44:47], v[68:71]\n" \
  "ds_read_b128 v[36:39], v0 offset:23552\n" \
  "v_mfma_f32_16x16x32_bf16 v[60:63], a[84:87], v[40:43], v[60:63]\n" \
  "v_mfma_f32_16x16x32_bf16 v[72:75], a[84:87], v[44:47], v[72:75]\n" \
  "v_mfma_f32_16x16x32_bf16 v[64:67], a[164:167], v[40:43], v[64:67]\n" \
  "v_mfma_f32_16x16x32_bf16 v[76:79], a[164:167], v[44:47], v[76:79]\n" \
  "s_waitcnt lgkmcnt(2)\n" \
  "v_mfma_f32_16x16x32_bf16 v[56:59], a[8:11], v[48:51], v[56:59]\n" \
  "ds_read_b128 v[40:43], v0 offset:4096\n" \
  "v_mfma_f32_16x16x32_bf16 v[68:71], a[8:11], v[52:55], v[68:71]\n" \
  "ds_read_b128 v[44:47], v0 offset:24576\n" \
  "v_mfma_f32_16x16x32_bf16 v[60:63], a[88:91], v[48:51], v[60:63]\n" \
  "v_mfma_f32_16x16x32_bf16 v[72:75], a[88:91], v[52:55], v[72:75]\n" \
  "v_mfma_f32_16x16x32_bf16 v[64:67], a[168:171], v[48:51], v[64:67]\n" \
  "v_mfma_f32_16x16x32_bf16 v[76:79], a[168:171], v[52:55], v[76:79]\n" \
  "s_waitcnt lgkmcnt(2)\n" \
  "v_mfma_f32_16x16x32_bf16 v[56:59], a[12:15], v[32:35], v[56:59]\n" \
  "ds_read_b128 v[48:51], v0 offset:5120\n" \
  "v_mfma_f32_16x16x32_bf16 v[68:71], a[12:15], v[36:39], v[68:71]\n" \
  "ds_read_b128 v[52:55], v0 offset:25600\n" \
  "v_mfma_f32_16x16x32_bf16 v[60:63], a[92:95], v[32:35], v[60:63]\n" \
  "v_mfma_f32_16x16x32_bf16 v[72:75], a[92:95], v[36:39], v[72:75]\n" \
  "v_mfma_f32_16x16x32_bf16 v[64:67], a[172:175], v[32:35], v[64:67]\n" \
  "v_mfma_f32_16x16x32_bf16 v[76:79], a[172:175], v[36:39], v[76:79]\n" \
  "s_waitcnt lgkmcnt(2)\n" \
  "v_mfma_f32_16x16x32_bf16 v[56:59], a[16:19], v[40:43], v[56:59]\n" \
  "ds_read_b128 v[32:35], v0 offset:6144\n" \
  "v_mfma_f32_16x16x32_bf16 v[68:71], a[16:19], v[44:47], v[68:71]\n" \
  "ds_read_b128 v[36:39], v0 offset:26624\n" \
  "v_mfma_f32_16x16x32_bf16 v[60:63], a[96:99], v[40:43], v[60:63]\n" \
  "v_mfma_f32_16x16x32_bf16 v[72:75], a[96:99], v[44:47], v[72:75]\n" \
  "v_mfma_f32_16x16x32_bf16 v[64:67], a[176:179], v[40:43], v[64:67]\n" \
  "v_mfma_f32_16x16x32_bf16 v[76:79], a[176:179], v[44:47], v[76:79]\n" \
  "s_waitcnt lgkmcnt(2)\n" \
  "v_mfma_f32_16x16x32_bf16 v[56:59], a[20:23], v[48:51], v[56:59]\n" \
  "ds_read_b128 v[40:43], v0 offset:7168\n" \
  "v_mfma_f32_16x16x32_bf16 v[68:71], a[20:23], v[52:55], v[68:71]\n" \
  "ds_read_b128 v[44:47], v0 offset:27648\n" \
  "v_mfma_f32_16x16x32_bf16 v[60:63], a[100:103], v[48:51], v[60:63]\n" \
  "v_mfma_f32_16x16x32_bf16 v[72:75], a[100:103], v[52:55], v[72:75]\n" \
  "v_mfma_f32_16x16x32_bf16 v[64:67], a[180:183], v[48:51], v[64:67]\n" \
  "v_mfma_f32_16x16x32_bf16 v[76:79], a[180:183], v[52:55], v[76:79]\n" \
  "s_waitcnt lgkmcnt(2)\n" \
  "v_mfma_f32_16x16x32_bf16 v[56:59], a[24:27], v[32:35], v[56:59]\n" \
  "ds_read_b128 v[48:51], v0 offset:8192\n" \
  "v_mfma_f32_16x16x32_bf16 v[68:71], a[24:27], v[36:39], v[68:71]\n" \
  "ds_read_b128 v[52:55], v0 offset:28672\n" \
  "v_mfma_f32_16x16x32_bf16 v[60:63], a[104:107], v[32:35], v[60:63]\n" \
  "v_mfma_f32_16x16x32_bf16 v[72:75], a[104:107], v[36:39], v[72:75]\n" \
  "v_mfma_f32_16x16x32_bf16 v[64:67], a[184:187], v[32:35], v[64:67]\n" \
  "v_mfma_f32_16x16x32_bf16 v[76:79], a[184:187], v[36:39], v[76:79]\n" \
  "s_waitcnt lgkmcnt(2)\n" \
  "v_mfma_f32_16x16x32_bf16 v[56:59], a[28:31], v[40:43], v[56:59]\n" \
  "ds_read_b128 v[32:35], v0 offset:9216\n" \
  "v_mfma_f32_16x16x32_bf16 v[68:71], a[28:31], v[44:47], v[68:71]\n" \
  "ds_read_b128 v[36:39], v0 offset:29696\n" \
  "v_mfma_f32_16x16x32_bf16 v[60:63], a[108:111], v[40:43], v[60:63]\n" \
  "s_add_u32 s70, s68, 4096\n" \
  "v_mfma_f32_16x16x32_bf16 v[72:75], a[108:111], v[44:47], v[72:75]\n" \
  "s_add_u32 s71, s68, 8192\n" \
  "v_mfma_f32_16x16x32_bf16 v[64:67], a[188:191], v[40:43], v[64:67]\n" \
  "s_add_u32 m0, s53, 81920\n" \
  "v_mfma_f32_16x16x32_bf16 v[76:79], a[188:191], v[44:47], v[76:79]\n" \
  "buffer_load_dwordx4 v1, s[36:39], s68 offen sc1 lds\n" \
  "s_waitcnt lgkmcnt(2)\n" \
  "v_mfma_f32_16x16x32_bf16 v[56:59], a[32:35], v[48:51], v[56:59]\n" \
  "ds_read_b128 v[40:43], v0 offset:10240\n" \
  "v_mfma_f32_16x16x32_bf16 v[68:71], a[32:35], v[52:55], v[68:71]\n" \
  "ds_read_b128 v[44:47], v0 offset:30720\n" \
  "v_mfma_f32_16x16x32_bf16 v[60:63], a[112:115], v[48:51], v[60:63]\n" \
  "buffer_load_dwordx4 v1, s[36:39], s68 offen offset:1024 sc1 lds\n" \
  "v_mfma_f32_16x16x32_bf16 v[72:75], a[112:115], v[52:55], v[72:75]\n" \
  "v_mfma_f32_16x16x32_bf16 v[64:67], a[192:195], v[48:51], v[64:67]\n" \
  "v_mfma_f32_16x16x32_bf16 v[76:79], a[192:195], v[52:55], v[76:79]\n" \
  "s_waitcnt lgkmcnt(2)\n" \
  "v_mfma_f32_16x16x32_bf16 v[56:59], a[36:39], v[32:35], v[56:59]\n" \
  "ds_read_b128 v[48:51], v0 offset:11264\n" \
  "v_mfma_f32_16x16x32_bf16 v[68:71], a[36:39], v[36:39], v[68:71]\n" \
  "ds_read_b128 v[52:55], v0 offset:31744\n" \
  "v_mfma_f32_16x16x32_bf16 v[60:63], a[116:119], v[32:35], v[60:63]\n" \
  "buffer_load_dwordx4 v1, s[36:39], s68 offen offset:2048 sc1 lds\n" \
  "v_mfma_f32_16x16x32_bf16 v[72:75], a[116:119], v[36:39], v[72:75]\n" \
  "v_mfma_f32_16x16x32_bf16 v[64:67], a[196:199], v[32:35], v[64:67]\n" \
  "v_mfma_f32_16x16x32_bf16 v[76:79], a[196:199], v[36:39], v[76:79]\n" \
  "s_waitcnt lgkmcnt(2)\n" \
  "v_mfma_f32_16x16x32_bf16 v[56:59], a[40:43], v[40:43], v[56:59]\n" \
  "ds_read_b128 v[32:35], v0 offset:12288\n" \
  "v_mfma_f32_16x16x32_bf16 v[68:71], a[40:43], v[44:47], v[68:71]\n" \
  "ds_read_b128 v[36:39], v0 offset:32768\n" \
  "v_mfma_f32_16x16x32_bf16 v[60:63], a[120:123], v[40:43], v[60:63]\n" \
  "buffer_load_dwordx4 v1, s[36:39], s68 offen offset:3072 sc1 lds\n" \
  "v_mfma_f32_16x16x32_bf16 v[72:75], a[120:123], v[44:47], v[72:75]\n" \
  "v_mfma_f32_16x16x32_bf16 v[64:67], a[200:203], v[40:43], v[64:67]\n" \
  "v_mfma_f32_16x16x32_bf16 v[76:79], a[200:203], v[44:47], v[76:79]\n" \
  "s_waitcnt lgkmcnt(2)\n" \
  "v_mfma_f32_16x16x32_bf16 v[56:59], a[44:47], v[48:51], v[56:59]\n" \
  "ds_read_b128 v[40:43], v0 offset:13312\n" \
  "v_mfma_f32_16x16x32_bf16 v[68:71], a[44:47], v[52:55], v[68:71]\n" \
  "ds_read_b128 v[44:47], v0 offset:33792\n" \
  "v_mfma_f32_16x16x32_bf16 v[60:63], a[124:127], v[48:51], v[60:63]\n" \
  "s_add_u32 m0, s53, 86016\n" \
  "v_mfma_f32_16x16x32_bf16 v[72:75], a[124:127], v[52:55], v[72:75]\n" \
  "buffer_load_dwordx4 v1, s[36:39], s70 offen sc1 lds\n" \
  "v_mfma_f32_16x16x32_bf16 v[64:67], a[204:207], v[48:51], v[64:67]\n" \
  "v_mfma_f32_16x16x32_bf16 v[76:79], a[204:207], v[52:55], v[76:79]\n" \
  "s_waitcnt lgkmcnt(2)\n" \
  "v_mfma_f32_16x16x32_bf16 v[56:59], a[48:51], v[32:35], v[56:59]\n" \
  "ds_read_b128 v[48:51], v0 offset:14336\n" \
  "v_mfma_f32_16x16x32_bf16 v[68:71], a[48:51], v[36:39], v[68:71]\n" \
  "ds_read_b128 v[52:55], v0 offset:34816\n" \
  "v_mfma_f32_16x16x32_bf16 v[60:63], a[128:131], v[32:35], v[60:63]\n" \
  "buffer_load_dwordx4 v1, s[36:39], s70 offen offset:1024 sc1 lds\n" \
  "v_mfma_f32_16x16x32_bf16 v[72:75], a[128:131], v[36:39], v[72:75]\n" \
  "v_mfma_f32_16x16x32_bf16 v[64:67], a[208:211], v[32:35], v[64:67]\n" \
  "v_mfma_f32_16x16x32_bf16 v[76:79], a[208:211], v[36:39], v[76:79]\n" \
  "s_waitcnt lgkmcnt(2)\n" \
  "v_mfma_f32_16x16x32_bf16 v[56:59], a[52:55], v[40:43], v[56:59]\n" \
  "ds_read_b128 v[32:35], v0 offset:15360\n" \
  "v_mfma_f32_16x16x32_bf16 v[68:71], a[52:55], v[44:47], v[68:71]\n" \
  "ds_read_b128 v[36:39], v0 offset:35840\n" \
  "v_mfma_f32_16x16x32_bf16 v[60:63], a[132:135], v[40:43], v[60:63]\n" \
  "v_mfma_f32_16x16x32_bf16 v[72:75], a[132:135], v[44:47], v[72:75]\n" \
  "v_mfma_f32_16x16x32_bf16 v[64:67], a[212:215], v[40:43], v[64:67]\n" \
  "v_mfma_f32_16x16x32_bf16 v[76:79], a[212:215], v[44:47], v[76:79]\n" \
  "s_waitcnt lgkmcnt(2)\n" \
  "v_mfma_f32_16x16x32_bf16 v[56:59], a[56:59], v[48:51], v[56:59]\n" \
  "ds_read_b128 v[40:43], v0 offset:16384\n" \
  "v_mfma_f32_16x16x32_bf16 v[68:71], a[56:59], v[52:55], v[68:71]\n" \
  "ds_read_b128 v[44:47], v0 offset:36864\n" \
  "v_mfma_f32_16x16x32_bf16 v[60:63], a[136:139], v[48:51], v[60:63]\n" \
  "buffer_load_dwordx4 v1, s[36:39], s70 offen offset:2048 sc1 lds\n" \
  "v_mfma_f32_16x16x32_bf16 v[72:75], a[136:139], v[52:55], v[72:75]\n" \
  "v_readlane_b32 s69, v3, s72\n" \
  "v_mfma_f32_16x16x32_bf16 v[64:67], a[216:219], v[48:51], v[64:67]\n" \
  "s_add_u32 s72, s72, 1\n" \
  "v_mfma_f32_16x16x32_bf16 v[76:79], a[216:219], v[52:55], v[76:79]\n" \
  "s_waitcnt lgkmcnt(2)\n" \
  "v_mfma_f32_16x16x32_bf16 v[56:59], a[60:63], v[32:35], v[56:59]\n" \
  "ds_read_b128 v[48:51], v0 offset:17408\n" \
  "v_mfma_f32_16x16x32_bf16 v[68:71], a[60:63], v[36:39], v[68:71]\n" \
  "ds_read_b128 v[52:55], v0 offset:37888\n" \
  "v_mfma_f32_16x16x32_bf16 v[60:63], a[140:143], v[32:35], v[60:63]\n" \
  "buffer_load_dwordx4 v1, s[36:39], s70 offen offset:3072 sc1 lds\n" \
  "v_mfma_f32_16x16x32_bf16 v[72:75], a[140:143], v[36:39], v[72:75]\n" \
  "v_mfma_f32_16x16x32_bf16 v[64:67], a[220:223], v[32:35], v[64:67]\n" \
  "v_mfma_f32_16x16x32_bf16 v[76:79], a[220:223], v[36:39], v[76:79]\n" \
  "s_waitcnt lgkmcnt(2)\n" \
  "v_mfma_f32_16x16x32_bf16 v[56:59], a[64:67], v[40:43], v[56:59]\n" \
  "ds_read_b128 v[32:35], v0 offset:18432\n" \
  "v_mfma_f32_16x16x32_bf16 v[68:71], a[64:67], v[44:47], v[68:71]\n" \
  "ds_read_b128 v[36:39], v0 offset:38912\n" \
  "v_mfma_f32_16x16x32_bf16 v[60:63], a[144:147], v[40:43], v[60:63]\n" \
  "s_add_u32 m0, s53, 90112\n" \
  "v_mfma_f32_16x16x32_bf16 v[72:75], a[144:147], v[44:47], v[72:75]\n" \
  "buffer_load_dwordx4 v1, s[36:39], s71 offen sc1 lds\n" \
  "v_mfma_f32_16x16x32_bf16 v[64:67], a[224:227], v[40:43], v[64:67]\n" \
  "v_mfma_f32_16x16x32_bf16 v[76:79], a[224:227], v[44:47], v[76:79]\n" \
  "s_waitcnt lgkmcnt(2)\n" \
  "v_mfma_f32_16x16x32_bf16 v[56:59], a[68:71], v[48:51], v[56:59]\n" \
  "ds_read_b128 v[40:43], v0 offset:19456\n" \
  "v_mfma_f32_16x16x32_bf16 v[68:71], a[68:71], v[52:55], v[68:71]\n" \
  "ds_read_b128 v[44:47], v0 offset:39936\n" \
  "v_mfma_f32_16x16x32_bf16 v[60:63], a[148:151], v[48:51], v[60:63]\n" \
  "buffer_load_dwordx4 v1, s[36:39], s71 offen offset:1024 sc1 lds\n" \
  "v_mfma_f32_16x16x32_bf16 v[72:75], a[148:151], v[52:55], v[72:75]\n" \
  "s_add_u32 s68, s68, 40960\n" \
  "v_mfma_f32_16x16x32_bf16 v[64:67], a[228:231], v[48:51], v[64:67]\n" \
  "v_mfma_f32_16x16x32_bf16 v[76:79], a[228:231], v[52:55], v[76:79]\n" \
  "s_waitcnt vmcnt(10) lgkmcnt(2)\n" \
  "s_barrier\n" \
  "v_mfma_f32_16x16x32_bf16 v[56:59], a[72:75], v[32:35], v[56:59]\n" \
  "ds_read_b128 v[48:51], v9 offset:0\n" \
  "v_mfma_f32_16x16x32_bf16 v[68:71], a[72:75], v[36:39], v[68:71]\n" \
  "ds_read_b128 v[52:55], v9 offset:20480\n" \
  "v_mfma_f32_16x16x32_bf16 v[60:63], a[152:155], v[32:35], v[60:63]\n" \
  "v_mfma_f32_16x16x32_bf16 v[72:75], a[152:155], v[36:39], v[72:75]\n" \
  "v_mfma_f32_16x16x32_bf16 v[64:67], a[232:235], v[32:35], v[64:67]\n" \
  "v_mfma_f32_16x16x32_bf16 v[76:79], a[232:235], v[36:39], v[76:79]\n" \
  "s_waitcnt lgkmcnt(2)\n" \
  "v_mfma_f32_16x16x32_bf16 v[56:59], a[76:79], v[40:43], v[56:59]\n" \
  "ds_read_b128 v[32:35], v9 offset:1024\n" \
  "v_mfma_f32_16x16x32_bf16 v[68:71], a[76:79], v[44:47], v[68:71]\n" \
  "ds_read_b128 v[36:39], v9 offset:21504\n" \
  "v_mfma_f32_16x16x32_bf16 v[60:63], a[156:159], v[40:43], v[60:63]\n" \
  "s_cmp_eq_u32 s72, 64\n" \
  "v_mfma_f32_16x16x32_bf16 v[72:75], a[156:159], v[44:47], v[72:75]\n" \
  "v_mfma_f32_16x16x32_bf16 v[64:67], a[236:239], v[40:43], v[64:67]\n" \
  "v_mfma_f32_16x16x32_bf16 v[76:79], a[236:239], v[44:47], v[76:79]\n" \
  "s_waitcnt lgkmcnt(2)\n" \
  "v_mfma_f32_16x16x32_bf16 v[100:103], a[0:3], v[48:51], 0\n" \
  "ds_read_b128 v[40:43], v9 offset:2048\n" \
  "v_mfma_f32_16x16x32_bf16 v[112:115], a[0:3], v[52:55], 0\n" \
  "ds_read_b128 v[44:47], v9 offset:22528\n" \
  "v_mfma_f32_16x16x32_bf16 v[104:107], a[80:83], v[48:51], 0\n" \
  "v_cvt_pk_bf16_f32 v16, v56, v57\n" \
  "v_mfma_f32_16x16x32_bf16 v[116:119], a[80:83], v[52:55], 0\n" \
  "v_cvt_pk_bf16_f32 v17, v58, v59\n" \
  "v_mfma_f32_16x16x32_bf16 v[108:111], a[160:163], v[48:51], 0\n" \
  "v_cvt_pk_bf16_f32 v18, v60, v61\n" \
  "v_mfma_f32_16x16x32_bf16 v[120:123], a[160:163], v[52:55], 0\n" \
  "v_cvt_pk_bf16_f32 v19, v62, v63\n" \
  "s_waitcnt lgkmcnt(2)\n" \
  "s_barrier\n" \
  "v_mfma_f32_16x16x32_bf16 v[100:103], a[4:7], v[32:35], v[100:103]\n" \
  "ds_read_b128 v[48:51], v9 offset:3072\n" \
  "v_mfma_f32_16x16x32_bf16 v[112:115], a[4:7], v[36:39], v[112:115]\n" \
  "ds_read_b128 v[52:55], v9 offset:23552\n" \
  "v_mfma_f32_16x16x32_bf16 v[104:107], a[84:87], v[32:35], v[104:107]\n" \
  "buffer_store_dwordx4 v[16:19], v5, s[40:43], s69 offen offset:0 sc1\n" \
  "v_mfma_f32_16x16x32_bf16 v[116:119], a[84:87], v[36:39], v[116:119]\n" \
  "v_cvt_pk_bf16_f32 v20, v68, v69\n" \
  "v_mfma_f32_16x16x32_bf16 v[108:111], a[164:167], v[32:35], v[108:111]\n" \
  "v_cvt_pk_bf16_f32 v21, v70, v71\n" \
  "v_mfma_f32_16x16x32_bf16 v[120:123], a[164:167], v[36:39], v[120:123]\n" \
  "v_cvt_pk_bf16_f32 v22, v72, v73\n" \
  "s_waitcnt lgkmcnt(2)\n" \
  "v_mfma_f32_16x16x32_bf16 v[100:103], a[8:11], v[40:43], v[100:103]\n" \
  "ds_read_b128 v[32:35], v9 offset:4096\n" \
  "v_mfma_f32_16x16x32_bf16 v[112:115], a[8:11], v[44:47], v[112:115]\n" \
  "ds_read_b128 v[36:39], v9 offset:24576\n" \
  "v_mfma_f32_16x16x32_bf16 v[104:107], a[88:91], v[40:43], v[104:107]\n" \
  "v_cvt_pk_bf16_f32 v23, v74, v75\n" \
  "v_mfma_f32_16x16x32_bf16 v[116:119], a[88:91], v[44:47], v[116:119]\n" \
  "buffer_store_dwordx4 v[20:23], v5, s[40:43], s69 offen offset:1024 sc1\n" \
  "v_mfma_f32_16x16x32_bf16 v[108:111], a[168:171], v[40:43], v[108:111]\n" \
  "v_cvt_pk_bf16_f32 v24, v64, v65\n" \
  "v_mfma_f32_16x16x32_bf16 v[120:123], a[168:171], v[44:47], v[120:123]\n" \
  "v_cvt_pk_bf16_f32 v25, v66, v67\n" \
  "s_waitcnt lgkmcnt(2)\n" \
  "v_mfma_f32_16x16x32_bf16 v[100:103], a[12:15], v[48:51], v[100:103]\n" \
  "ds_read_b128 v[40:43], v9 offset:5120\n" \
  "v_mfma_f32_16x16x32_bf16 v[112:115], a[12:15], v[52:55], v[112:115]\n" \
  "ds_read_b128 v[44:47], v9 offset:25600\n" \
  "v_mfma_f32_16x16x32_bf16 v[104:107], a[92:95], v[48:51], v[104:107]\n" \
  "v_cvt_pk_bf16_f32 v26, v76, v77\n" \
  "v_mfma_f32_16x16x32_bf16 v[116:119], a[92:95], v[52:55], v[116:119]\n" \
  "v_cvt_pk_bf16_f32 v27, v78, v79\n" \
  "v_mfma_f32_16x16x32_bf16 v[108:111], a[172:175], v[48:51], v[108:111]\n" \
  "buffer_store_dwordx4 v[24:27], v5, s[40:43], s69 offen offset:2048 sc1\n" \
  "v_mfma_f32_16x16x32_bf16 v[120:123], a[172:175], v[52:55], v[120:123]\n" \
  "s_waitcnt lgkmcnt(2)\n" \
  "v_mfma_f32_16x16x32_bf16 v[100:103], a[16:19], v[32:35], v[100:103]\n" \
  "ds_read_b128 v[48:51], v9 offset:6144\n" \
  "v_mfma_f32_16x16x32_bf16 v[112:115], a[16:19], v[36:39], v[112:115]\n" \
  "ds_read_b128 v[52:55], v9 offset:26624\n" \
  "v_mfma_f32_16x16x32_bf16 v[104:107], a[96:99], v[32:35], v[104:107]\n" \
  "buffer_store_dwordx4 v[16:19], v2, s[40:43], s69 offen offset:0 sc1\n" \
  "v_mfma_f32_16x16x32_bf16 v[116:119], a[96:99], v[36:39], v[116:119]\n" \
  "v_mfma_f32_16x16x32_bf16 v[108:111], a[176:179], v[32:35], v[108:111]\n" \
  "v_mfma_f32_16x16x32_bf16 v[120:123], a[176:179], v[36:39], v[120:123]\n" \
  "s_waitcnt lgkmcnt(2)\n" \
  "v_mfma_f32_16x16x32_bf16 v[100:103], a[20:23], v[40:43], v[100:103]\n" \
  "ds_read_b128 v[32:35], v9 offset:7168\n" \
  "v_mfma_f32_16x16x32_bf16 v[112:115], a[20:23], v[44:47], v[112:115]\n" \
  "ds_read_b128 v[36:39], v9 offset:27648\n" \
  "v_mfma_f32_16x16x32_bf16 v[104:107], a[100:103], v[40:43], v[104:107]\n" \
  "buffer_store_dwordx4 v[20:23], v2, s[40:43], s69 offen offset:1024 sc1\n" \
  "v_mfma_f32_16x16x32_bf16 v[116:119], a[100:103], v[44:47], v[116:119]\n" \
  "v_mfma_f32_16x16x32_bf16 v[108:111], a[180:183], v[40:43], v[108:111]\n" \
  "v_mfma_f32_16x16x32_bf16 v[120:123], a[180:183], v[44:47], v[120:123]\n" \
  "s_waitcnt lgkmcnt(2)\n" \
  "v_mfma_f32_16x16x32_bf16 v[100:103], a[24:27], v[48:51], v[100:103]\n" \
  "ds_read_b128 v[40:43], v9 offset:8192\n" \
  "v_mfma_f32_16x16x32_bf16 v[112:115], a[24:27], v[52:55], v[112:115]\n" \
  "ds_read_b128 v[44:47], v9 offset:28672\n" \
  "v_mfma_f32_16x16x32_bf16 v[104:107], a[104:107], v[48:51], v[104:107]\n" \
  "buffer_store_dwordx4 v[24:27], v2, s[40:43], s69 offen offset:2048 sc1\n" \
  "v_mfma_f32_16x16x32_bf16 v[116:119], a[104:107], v[52:55], v[116:119]\n" \
  "s_nop 0\n" \
  "v_mfma_f32_16x16x32_bf16 v[108:111], a[184:187], v[48:51], v[108:111]\n" \
  "s_nop 0\n" \
  "v_mfma_f32_16x16x32_bf16 v[120:123], a[184:187], v[52:55], v[120:123]\n" \
  "s_waitcnt lgkmcnt(2)\n" \
  "v_mfma_f32_16x16x32_bf16 v[100:103], a[28:31], v[32:35], v[100:103]\n" \
  "ds_read_b128 v[48:51], v9 offset:9216\n" \
  "v_mfma_f32_16x16x32_bf16 v[112:115], a[28:31], v[36:39], v[112:115]\n" \
  "ds_read_b128 v[52:55], v9 offset:29696\n" \
  "v_mfma_f32_16x16x32_bf16 v[104:107], a[108:111], v[32:35], v[104:107]\n" \
  "s_add_u32 s70, s68, 4096\n" \
  "v_mfma_f32_16x16x32_bf16 v[116:119], a[108:111], v[36:39], v[116:119]\n" \
  "s_add_u32 s71, s68, 8192\n" \
  "v_mfma_f32_16x16x32_bf16 v[108:111], a[188:191], v[32:35], v[108:111]\n" \
  "s_add_u32 m0, s53, 0\n" \
  "v_mfma_f32_16x16x32_bf16 v[120:123], a[188:191], v[36:39], v[120:123]\n" \
  "buffer_load_dwordx4 v1, s[36:39], s68 offen sc1 lds\n" \
  "s_waitcnt lgkmcnt(2)\n" \
  "v_mfma_f32_16x16x32_bf16 v[100:103], a[32:35], v[40:43], v[100:103]\n" \
  "ds_read_b128 v[32:35], v9 offset:10240\n" \
  "v_mfma_f32_16x16x32_bf16 v[112:115], a[32:35], v[44:47], v[112:115]\n" \
  "ds_read_b128 v[36:39], v9 offset:30720\n" \
  "v_mfma_f32_16x16x32_bf16 v[104:107], a[112:115], v[40:43], v[104:107]\n" \
  "buffer_load_dwordx4 v1, s[36:39], s68 offen offset:1024 sc1 lds\n" \
  "v_mfma_f32_16x16x32_bf16 v[116:119], a[112:115], v[44:47], v[116:119]\n" \
  "v_mfma_f32_16x16x32_bf16 v[108:111], a[192:195], v[40:43], v[108:111]\n" \
  "v_mfma_f32_16x16x32_bf16 v[120:123], a[192:195], v[44:47], v[120:123]\n" \
  "s_waitcnt lgkmcnt(2)\n" \
  "v_mfma_f32_16x16x32_bf16 v[100:103], a[36:39], v[48:51], v[100:103]\n" \
  "ds_read_b128 v[40:43], v9 offset:11264\n" \
  "v_mfma_f32_16x16x32_bf16 v[112:115], a[36:39], v[52:55], v[112:115]\n" \
  "ds_read_b128 v[44:47], v9 offset:31744\n" \
  "v_mfma_f32_16x16x32_bf16 v[104:107], a[116:119], v[48:51], v[104:107]\n" \
  "buffer_load_dwordx4 v1, s[36:39], s68 offen offset:2048 sc1 lds\n" \
  "v_mfma_f32_16x16x32_bf16 v[116:119], a[116:119], v[52:55], v[116:119]\n" \
  "v_mfma_f32_16x16x32_bf16 v[108:111], a[196:199], v[48:51], v[108:111]\n" \
  "v_mfma_f32_16x16x32_bf16 v[120:123], a[196:199], v[52:55], v[120:123]\n" \
  "s_waitcnt lgkmcnt(2)\n" \
  "v_mfma_f32_16x16x32_bf16 v[100:103], a[40:43], v[32:35], v[100:103]\n" \
  "ds_read_b128 v[48:51], v9 offset:12288\n" \
  "v_mfma_f32_16x16x32_bf16 v[112:115], a[40:43], v[36:39], v[112:115]\n" \
  "ds_read_b128 v[52:55], v9 offset:32768\n" \
  "v_mfma_f32_16x16x32_bf16 v[104:107], a[120:123], v[32:35], v[104:107]\n" \
  "buffer_load_dwordx4 v1, s[36:39], s68 offen offset:3072 sc1 lds\n" \
  "v_mfma_f32_16x16x32_bf16 v[116:119], a[120:123], v[36:39], v[116:119]\n" \
  "v_mfma_f32_16x16x32_bf16 v[108:111], a[200:203], v[32:35], v[108:111]\n" \
  "v_mfma_f32_16x16x32_bf16 v[120:123], a[200:203], v[36:39], v[120:123]\n" \
  "s_waitcnt lgkmcnt(2)\n" \
  "v_mfma_f32_16x16x32_bf16 v[100:103], a[44:47], v[40:43], v[100:103]\n" \
  "ds_read_b128 v[32:35], v9 offset:13312\n" \
  "v_mfma_f32_16x16x32_bf16 v[112:115], a[44:47], v[44:47], v[112:115]\n" \
  "ds_read_b128 v[36:39], v9 offset:33792\n" \
  "v_mfma_f32_16x16x32_bf16 v[104:107], a[124:127], v[40:43], v[104:107]\n" \
  "s_add_u32 m0, s53, 4096\n" \
  "v_mfma_f32_16x16x32_bf16 v[116:119], a[124:127], v[44:47], v[116:119]\n" \
  "buffer_load_dwordx4 v1, s[36:39], s70 offen sc1 lds\n" \
  "v_mfma_f32_16x16x32_bf16 v[108:111], a[204:207], v[40:43], v[108:111]\n" \
  "v_mfma_f32_16x16x32_bf16 v[120:123], a[204:207], v[44:47], v[120:123]\n" \
  "s_waitcnt lgkmcnt(2)\n" \
  "v_mfma_f32_16x16x32_bf16 v[100:103], a[48:51], v[48:51], v[100:103]\n" \
  "ds_read_b128 v[40:43], v9 offset:14336\n" \
  "v_mfma_f32_16x16x32_bf16 v[112:115], a[48:51], v[52:55], v[112:115]\n" \
  "ds_read_b128 v[44:47], v9 offset:34816\n" \
  "v_mfma_f32_16x16x32_bf16 v[104:107], a[128:131], v[48:51], v[104:107]\n" \
  "buffer_load_dwordx4 v1, s[36:39], s70 offen offset:1024 sc1 lds\n" \
  "v_mfma_f32_16x16x32_bf16 v[116:119], a[128:131], v[52:55], v[116:119]\n" \
  "v_mfma_f32_16x16x32_bf16 v[108:111], a[208:211], v[48:51], v[108:111]\n" \
  "v_mfma_f32_16x16x32_bf16 v[120:123], a[208:211], v[52:55], v[120:123]\n" \
  "s_waitcnt vmcnt(6) lgkmcnt(2)\n" \
  "s_barrier\n" \
  "v_mfma_f32_16x16x32_bf16 v[100:103], a[52:55], v[32:35], v[100:103]\n" \
  "ds_read_b128 v[48:51], v9 offset:15360\n" \
  "v_mfma_f32_16x16x32_bf16 v[112:115], a[52:55], v[36:39], v[112:115]\n" \
  "ds_read_b128 v[52:55], v9 offset:35840\n" \
  "v_mfma_f32_16x16x32_bf16 v[104:107], a[132:135], v[32:35], v[104:107]\n" \
  "buffer_atomic_add v11, v4, s[40:43], s54 offen\n" \
  "v_mfma_f32_16x16x32_bf16 v[116:119], a[132:135], v[36:39], v[116:119]\n" \
  "s_add_u32 s54, s54, s55\n" \
  "v_mfma_f32_16x16x32_bf16 v[108:111], a[212:215], v[32:35], v[108:111]\n" \
  "s_and_b32 s54, s54, 0xffffdfff\n" \
  "v_mfma_f32_16x16x32_bf16 v[120:123], a[212:215], v[36:39], v[120:123]\n" \
  "s_waitcnt lgkmcnt(2)\n" \
  "v_mfma_f32_16x16x32_bf16 v[100:103], a[56:59], v[40:43], v[100:103]\n" \
  "ds_read_b128 v[32:35], v9 offset:16384\n" \
  "v_mfma_f32_16x16x32_bf16 v[112:115], a[56:59], v[44:47], v[112:115]\n" \
  "ds_read_b128 v[36:39], v9 offset:36864\n" \
  "v_mfma_f32_16x16x32_bf16 v[104:107], a[136:139], v[40:43], v[104:107]\n" \
  "buffer_load_dwordx4 v1, s[36:39], s70 offen offset:2048 sc1 lds\n" \
  "v_mfma_f32_16x16x32_bf16 v[116:119], a[136:139], v[44:47], v[116:119]\n" \
  "v_readlane_b32 s69, v3, s72\n" \
  "v_mfma_f32_16x16x32_bf16 v[108:111], a[216:219], v[40:43], v[108:111]\n" \
  "s_add_u32 s72, s72, 1\n" \
  "v_mfma_f32_16x16x32_bf16 v[120:123], a[216:219], v[44:47], v[120:123]\n" \
  "s_waitcnt lgkmcnt(2)\n" \
  "v_mfma_f32_16x16x32_bf16 v[100:103], a[60:63], v[48:51], v[100:103]\n" \
  "ds_read_b128 v[40:43], v9 offset:17408\n" \
  "v_mfma_f32_16x16x32_bf16 v[112:115], a[60:63], v[52:55], v[112:115]\n" \
  "ds_read_b128 v[44:47], v9 offset:37888\n" \
  "v_mfma_f32_16x16x32_bf16 v[104:107], a[140:143], v[48:51], v[104:107]\n" \
  "buffer_load_dwordx4 v1, s[36:39], s70 offen offset:3072 sc1 lds\n" \
  "v_mfma_f32_16x16x32_bf16 v[116:119], a[140:143], v[52:55], v[116:119]\n" \
  "v_mfma_f32_16x16x32_bf16 v[108:111], a[220:223], v[48:51], v[108:111]\n" \
  "v_mfma_f32_16x16x32_bf16 v[120:123], a[220:223], v[52:55], v[120:123]\n" \
  "s_waitcnt lgkmcnt(2)\n" \
  "v_mfma_f32_16x16x32_bf16 v[100:103], a[64:67], v[32:35], v[100:103]\n" \
  "ds_read_b128 v[48:51], v9 offset:18432\n" \
  "v_mfma_f32_16x16x32_bf16 v[112:115], a[64:67], v[36:39], v[112:115]\n" \
  "ds_read_b128 v[52:55], v9 offset:38912\n" \
  "v_mfma_f32_16x16x32_bf16 v[104:107], a[144:147], v[32:35], v[104:107]\n" \
  "s_add_u32 m0, s53, 8192\n" \
  "v_mfma_f32_16x16x32_bf16 v[116:119], a[144:147], v[36:39], v[116:119]\n" \
  "buffer_load_dwordx4 v1, s[36:39], s71 offen sc1 lds\n" \
  "v_mfma_f32_16x16x32_bf16 v[108:111], a[224:227], v[32:35], v[108:111]\n" \
  "v_mfma_f32_16x16x32_bf16 v[120:123], a[224:227], v[36:39], v[120:123]\n" \
  "s_waitcnt lgkmcnt(2)\n" \
  "v_mfma_f32_16x16x32_bf16 v[100:103], a[68:71], v[40:43], v[100:103]\n" \
  "ds_read_b128 v[32:35], v9 offset:19456\n" \
  "v_mfma_f32_16x16x32_bf16 v[112:115], a[68:71], v[44:47], v[112:115]\n" \
  "ds_read_b128 v[36:39], v9 offset:39936\n" \
  "v_mfma_f32_16x16x32_bf16 v[104:107], a[148:151], v[40:43], v[104:107]\n" \
  "buffer_load_dwordx4 v1, s[36:39], s71 offen offset:1024 sc1 lds\n" \
  "v_mfma_f32_16x16x32_bf16 v[116:119], a[148:151], v[44:47], v[116:119]\n" \
  "s_add_u32 s68, s68, 40960\n" \
  "v_mfma_f32_16x16x32_bf16 v[108:111], a[228:231], v[40:43], v[108:111]\n" \
  "v_mfma_f32_16x16x32_bf16 v[120:123], a[228:231], v[44:47], v[120:123]\n" \
  "s_waitcnt vmcnt(11) lgkmcnt(2)\n" \
  "s_barrier\n" \
  "v_mfma_f32_16x16x32_bf16 v[100:103], a[72:75], v[48:51], v[100:103]\n" \
  "ds_read_b128 v[40:43], v10 offset:0\n" \
  "v_mfma_f32_16x16x32_bf16 v[112:115], a[72:75], v[52:55], v[112:115]\n" \
  "ds_read_b128 v[44:47], v10 offset:20480\n" \
  "v_mfma_f32_16x16x32_bf16 v[104:107], a[152:155], v[48:51], v[104:107]\n" \
  "v_mfma_f32_16x16x32_bf16 v[116:119], a[152:155], v[52:55], v[116:119]\n" \
  "v_mfma_f32_16x16x32_bf16 v[108:111], a[232:235], v[48:51], v[108:111]\n" \
  "v_mfma_f32_16x16x32_bf16 v[120:123], a[232:235], v[52:55], v[120:123]\n" \
  "s_waitcnt lgkmcnt(2)\n" \
  "v_mfma_f32_16x16x32_bf16 v[100:103], a[76:79], v[32:35], v[100:103]\n" \
  "ds_read_b128 v[48:51], v10 offset:1024\n" \
  "v_mfma_f32_16x16x32_bf16 v[112:115], a[76:79], v[36:39], v[112:115]\n" \
  "ds_read_b128 v[52:55], v10 offset:21504\n" \
  "v_mfma_f32_16x16x32_bf16 v[104:107], a[156:159], v[32:35], v[104:107]\n" \
  "s_cmp_eq_u32 s72, 64\n" \
  "v_mfma_f32_16x16x32_bf16 v[116:119], a[156:159], v[36:39], v[116:119]\n" \
  "v_mfma_f32_16x16x32_bf16 v[108:111], a[236:239], v[32:35], v[108:111]\n" \
  "v_mfma_f32_16x16x32_bf16 v[120:123], a[236:239], v[36:39], v[120:123]\n" \
  "s_cbranch_scc1 LPE_%=\n" \
  "s_waitcnt lgkmcnt(2)\n" \
  "v_mfma_f32_16x16x32_bf16 v[56:59], a[0:3], v[40:43], 0\n" \
  "ds_read_b128 v[32:35], v10 offset:2048\n" \
  "v_mfma_f32_16x16x32_bf16 v[68:71], a[0:3], v[44:47], 0\n" \
  "ds_read_b128 v[36:39], v10 offset:22528\n" \
  "v_mfma_f32_16x16x32_bf16 v[60:63], a[80:83], v[40:43], 0\n" \
  "v_cvt_pk_bf16_f32 v16, v100, v101\n" \
  "v_mfma_f32_16x16x32_bf16 v[72:75], a[80:83], v[44:47], 0\n" \
  "v_cvt_pk_bf16_f32 v17, v102, v103\n" \
  "v_mfma_f32_16x16x32_bf16 v[64:67], a[160:163], v[40:43], 0\n" \
  "v_cvt_pk_bf16_f32 v18, v104, v105\n" \
  "v_mfma_f32_16x16x32_bf16 v[76:79], a[160:163], v[44:47], 0\n" \
  "v_cvt_pk_bf16_f32 v19, v106, v107\n" \
  "s_waitcnt lgkmcnt(2)\n" \
  "s_barrier\n" \
  "v_mfma_f32_16x16x32_bf16 v[56:59], a[4:7], v[48:51], v[56:59]\n" \
  "ds_read_b128 v[40:43], v10 offset:3072\n" \
  "v_mfma_f32_16x16x32_bf16 v[68:71], a[4:7], v[52:55], v[68:71]\n" \
  "ds_read_b128 v[44:47], v10 offset:23552\n" \
  "v_mfma_f32_16x16x32_bf16 v[60:63], a[84:87], v[48:51], v[60:63]\n" \
  "buffer_store_dwordx4 v[16:19], v5, s[40:43], s69 offen offset:0 sc1\n" \
  "v_mfma_f32_16x16x32_bf16 v[72:75], a[84:87], v[52:55], v[72:75]\n" \
  "v_cvt_pk_bf16_f32 v20, v112, v113\n" \
  "v_mfma_f32_16x16x32_bf16 v[64:67], a[164:167], v[48:51], v[64:67]\n" \
  "v_cvt_pk_bf16_f32 v21, v114, v115\n" \
  "v_mfma_f32_16x16x32_bf16 v[76:79], a[164:167], v[52:55], v[76:79]\n" \
  "v_cvt_pk_bf16_f32 v22, v116, v117\n" \
  "s_waitcnt lgkmcnt(2)\n" \
  "v_mfma_f32_16x16x32_bf16 v[56:59], a[8:11], v[32:35], v[56:59]\n" \
  "ds_read_b128 v[48:51], v10 offset:4096\n" \
  "v_mfma_f32_16x16x32_bf16 v[68:71], a[8:11], v[36:39], v[68:71]\n" \
  "ds_read_b128 v[52:55], v10 offset:24576\n" \
  "v_mfma_f32_16x16x32_bf16 v[60:63], a[88:91], v[32:35], v[60:63]\n" \
  "v_cvt_pk_bf16_f32 v23, v118, v119\n" \
  "v_mfma_f32_16x16x32_bf16 v[72:75], a[88:91], v[36:39], v[72:75]\n" \
  "buffer_store_dwordx4 v[20:23], v5, s[40:43], s69 offen offset:1024 sc1\n" \
  "v_mfma_f32_16x16x32_bf16 v[64:67], a[168:171], v[32:35], v[64:67]\n" \
  "v_cvt_pk_bf16_f32 v24, v108, v109\n" \
  "v_mfma_f32_16x16x32_bf16 v[76:79], a[168:171], v[36:39], v[76:79]\n" \
  "v_cvt_pk_bf16_f32 v25, v110, v111\n" \
  "s_waitcnt lgkmcnt(2)\n" \
  "v_mfma_f32_16x16x32_bf16 v[56:59], a[12:15], v[40:43], v[56:59]\n" \
  "ds_read_b128 v[32:35], v10 offset:5120\n" \
  "v_mfma_f32_16x16x32_bf16 v[68:71], a[12:15], v[44:47], v[68:71]\n" \
  "ds_read_b128 v[36:39], v10 offset:25600\n" \
  "v_mfma_f32_16x16x32_bf16 v[60:63], a[92:95], v[40:43], v[60:63]\n" \
  "v_cvt_pk_bf16_f32 v26, v120, v121\n" \
  "v_mfma_f32_16x16x32_bf16 v[72:75], a[92:95], v[44:47], v[72:75]\n" \
  "v_cvt_pk_bf16_f32 v27, v122, v123\n" \
  "v_mfma_f32_16x16x32_bf16 v[64:67], a[172:175], v[40:43], v[64:67]\n" \
  "buffer_store_dwordx4 v[24:27], v5, s[40:43], s69 offen offset:2048 sc1\n" \
  "v_mfma_f32_16x16x32_bf16 v[76:79], a[172:175], v[44:47], v[76:79]\n" \
  "s_waitcnt lgkmcnt(2)\n" \
  "v_mfma_f32_16x16x32_bf16 v[56:59], a[16:19], v[48:51], v[56:59]\n" \
  "ds_read_b128 v[40:43], v10 offset:6144\n" \
  "v_mfma_f32_16x16x32_bf16 v[68:71], a[16:19], v[52:55], v[68:71]\n" \
  "ds_read_b128 v[44:47], v10 offset:26624\n" \
  "v_mfma_f32_16x16x32_bf16 v[60:63], a[96:99], v[48:51], v[60:63]\n" \
  "buffer_store_dwordx4 v[16:19], v2, s[40:43], s69 offen offset:0 sc1\n" \
  "v_mfma_f32_16x16x32_bf16 v[72:75], a[96:99], v[52:55], v[72:75]\n" \
  "v_mfma_f32_16x16x32_bf16 v[64:67], a[176:179], v[48:51], v[64:67]\n" \
  "v_mfma_f32_16x16x32_bf16 v[76:79], a[176:179], v[52:55], v[76:79]\n" \
  "s_waitcnt lgkmcnt(2)\n" \
  "v_mfma_f32_16x16x32_bf16 v[56:59], a[20:23], v[32:35], v[56:59]\n" \
  "ds_read_b128 v[48:51], v10 offset:7168\n" \
  "v_mfma_f32_16x16x32_bf16 v[68:71], a[20:23], v[36:39], v[68:71]\n" \
  "ds_read_b128 v[52:55], v10 offset:27648\n" \
  "v_mfma_f32_16x16x32_bf16 v[60:63], a[100:103], v[32:35], v[60:63]\n" \
  "buffer_store_dwordx4 v[20:23], v2, s[40:43], s69 offen offset:1024 sc1\n" \
  "v_mfma_f32_16x16x32_bf16 v[72:75], a[100:103], v[36:39], v[72:75]\n" \
  "v_mfma_f32_16x16x32_bf16 v[64:67], a[180:183], v[32:35], v[64:67]\n" \
  "v_mfma_f32_16x16x32_bf16 v[76:79], a[180:183], v[36:39], v[76:79]\n" \
  "s_waitcnt lgkmcnt(2)\n" \
  "v_mfma_f32_16x16x32_bf16 v[56:59], a[24:27], v[40:43], v[56:59]\n" \
  "ds_read_b128 v[32:35], v10 offset:8192\n" \
  "v_mfma_f32_16x16x32_bf16 v[68:71], a[24:27], v[44:47], v[68:71]\n" \
  "ds_read_b128 v[36:39], v10 offset:28672\n" \
  "v_mfma_f32_16x16x32_bf16 v[60:63], a[104:107], v[40:43], v[60:63]\n" \
  "buffer_store_dwordx4 v[24:27], v2, s[40:43], s69 offen offset:2048 sc1\n" \
  "v_mfma_f32_16x16x32_bf16 v[72:75], a[104:107], v[44:47], v[72:75]\n" \
  "s_nop 0\n" \
  "v_mfma_f32_16x16x32_bf16 v[64:67], a[184:187], v[40:43], v[64:67]\n" \
  "s_nop 0\n" \
  "v_mfma_f32_16x16x32_bf16 v[76:79], a[184:187], v[44:47], v[76:79]\n" \
  "s_waitcnt lgkmcnt(2)\n" \
  "v_mfma_f32_16x16x32_bf16 v[56:59], a[28:31], v[48:51], v[56:59]\n" \
  "ds_read_b128 v[40:43], v10 offset:9216\n" \
  "v_mfma_f32_16x16x32_bf16 v[68:71], a[28:31], v[52:55], v[68:71]\n" \
  "ds_read_b128 v[44:47], v10 offset:29696\n" \
  "v_mfma_f32_16x16x32_bf16 v[60:63], a[108:111], v[48:51], v[60:63]\n" \
  "s_add_u32 s70, s68, 4096\n" \
  "v_mfma_f32_16x16x32_bf16 v[72:75], a[108:111], v[52:55], v[72:75]\n" \
  "s_add_u32 s71, s68, 8192\n" \
  "v_mfma_f32_16x16x32_bf16 v[64:67], a[188:191], v[48:51], v[64:67]\n" \
  "s_add_u32 m0, s53, 40960\n" \
  "v_mfma_f32_16x16x32_bf16 v[76:79], a[188:191], v[52:55], v[76:79]\n" \
  "buffer_load_dwordx4 v1, s[36:39], s68 offen sc1 lds\n" \
  "s_waitcnt lgkmcnt(2)\n" \
  "v_mfma_f32_16x16x32_bf16 v[56:59], a[32:35], v[32:35], v[56:59]\n" \
  "ds_read_b128 v[48:51], v10 offset:10240\n" \
  "v_mfma_f32_16x16x32_bf16 v[68:71], a[32:35], v[36:39], v[68:71]\n" \
  "ds_read_b128 v[52:55], v10 offset:30720\n" \
  "v_mfma_f32_16x16x32_bf16 v[60:63], a[112:115], v[32:35], v[60:63]\n" \
  "buffer_load_dwordx4 v1, s[36:39], s68 offen offset:1024 sc1 lds\n" \
  "v_mfma_f32_16x16x32_bf16 v[72:75], a[112:115], v[36:39], v[72:75]\n" \
  "v_mfma_f32_16x16x32_bf16 v[64:67], a[192:195], v[32:35], v[64:67]\n" \
  "v_mfma_f32_16x16x32_bf16 v[76:79], a[192:195], v[36:39], v[76:79]\n" \
  "s_waitcnt lgkmcnt(2)\n" \
  "v_mfma_f32_16x16x32_bf16 v[56:59], a[36:39], v[40:43], v[56:59]\n" \
  "ds_read_b128 v[32:35], v10 offset:11264\n" \
  "v_mfma_f32_16x16x32_bf16 v[68:71], a[36:39], v[44:47], v[68:71]\n" \
  "ds_read_b128 v[36:39], v10 offset:31744\n" \
  "v_mfma_f32_16x16x32_bf16 v[60:63], a[116:119], v[40:43], v[60:63]\n" \
  "buffer_load_dwordx4 v1, s[36:39], s68 offen offset:2048 sc1 lds\n" \
  "v_mfma_f32_16x16x32_bf16 v[72:75], a[116:119], v[44:47], v[72:75]\n" \
  "v_mfma_f32_16x16x32_bf16 v[64:67], a[196:199], v[40:43], v[64:67]\n" \
  "v_mfma_f32_16x16x32_bf16 v[76:79], a[196:199], v[44:47], v[76:79]\n" \
  "s_waitcnt lgkmcnt(2)\n" \
  "v_mfma_f32_16x16x32_bf16 v[56:59], a[40:43], v[48:51], v[56:59]\n" \
  "ds_read_b128 v[40:43], v10 offset:12288\n" \
  "v_mfma_f32_16x16x32_bf16 v[68:71], a[40:43], v[52:55], v[68:71]\n" \
  "ds_read_b128 v[44:47], v10 offset:32768\n" \
  "v_mfma_f32_16x16x32_bf16 v[60:63], a[120:123], v[48:51], v[60:63]\n" \
  "buffer_load_dwordx4 v1, s[36:39], s68 offen offset:3072 sc1 lds\n" \
  "v_mfma_f32_16x16x32_bf16 v[72:75], a[120:123], v[52:55], v[72:75]\n" \
  "v_mfma_f32_16x16x32_bf16 v[64:67], a[200:203], v[48:51], v[64:67]\n" \
  "v_mfma_f32_16x16x32_bf16 v[76:79], a[200:203], v[52:55], v[76:79]\n" \
  "s_waitcnt lgkmcnt(2)\n" \
  "v_mfma_f32_16x16x32_bf16 v[56:59], a[44:47], v[32:35], v[56:59]\n" \
  "ds_read_b128 v[48:51], v10 offset:13312\n" \
  "v_mfma_f32_16x16x32_bf16 v[68:71], a[44:47], v[36:39], v[68:71]\n" \
  "ds_read_b128 v[52:55], v10 offset:33792\n" \
  "v_mfma_f32_16x16x32_bf16 v[60:63], a[124:127], v[32:35], v[60:63]\n" \
  "s_add_u32 m0, s53, 45056\n" \
  "v_mfma_f32_16x16x32_bf16 v[72:75], a[124:127], v[36:39], v[72:75]\n" \
  "buffer_load_dwordx4 v1, s[36:39], s70 offen sc1 lds\n" \
  "v_mfma_f32_16x16x32_bf16 v[64:67], a[204:207], v[32:35], v[64:67]\n" \
  "v_mfma_f32_16x16x32_bf16 v[76:79], a[204:207], v[36:39], v[76:79]\n" \
  "s_waitcnt lgkmcnt(2)\n" \
  "v_mfma_f32_16x16x32_bf16 v[56:59], a[48:51], v[40:43], v[56:59]\n" \
  "ds_read_b128 v[32:35], v10 offset:14336\n" \
  "v_mfma_f32_16x16x32_bf16 v[68:71], a[48:51], v[44:47], v[68:71]\n" \
  "ds_read_b128 v[36:39], v10 offset:34816\n" \
  "v_mfma_f32_16x16x32_bf16 v[60:63], a[128:131], v[40:43], v[60:63]\n" \
  "buffer_load_dwordx4 v1, s[36:39], s70 offen offset:1024 sc1 lds\n" \
  "v_mfma_f32_16x16x32_bf16 v[72:75], a[128:131], v[44:47], v[72:75]\n" \
  "v_mfma_f32_16x16x32_bf16 v[64:67], a[208:211], v[40:43], v[64:67]\n" \
  "v_mfma_f32_16x16x32_bf16 v[76:79], a[208:211], v[44:47], v[76:79]\n" \
  "s_waitcnt vmcnt(6) lgkmcnt(2)\n" \
  "s_barrier\n" \
  "v_mfma_f32_16x16x32_bf16 v[56:59], a[52:55], v[48:51], v[56:59]\n" \
  "ds_read_b128 v[40:43], v10 offset:15360\n" \
  "v_mfma_f32_16x16x32_bf16 v[68:71], a[52:55], v[52:55], v[68:71]\n" \
  "ds_read_b128 v[44:47], v10 offset:35840\n" \
  "v_mfma_f32_16x16x32_bf16 v[60:63], a[132:135], v[48:51], v[60:63]\n" \
  "buffer_atomic_add v11, v4, s[40:43], s54 offen\n" \
  "v_mfma_f32_16x16x32_bf16 v[72:75], a[132:135], v[52:55], v[72:75]\n" \
  "s_add_u32 s54, s54, s55\n" \
  "v_mfma_f32_16x16x32_bf16 v[64:67], a[212:215], v[48:51], v[64:67]\n" \
  "s_and_b32 s54, s54, 0xffffdfff\n" \
  "v_mfma_f32_16x16x32_bf16 v[76:79], a[212:215], v[52:55], v[76:79]\n" \
  "s_waitcnt lgkmcnt(2)\n" \
  "v_mfma_f32_16x16x32_bf16 v[56:59], a[56:59], v[32:35], v[56:59]\n" \
  "ds_read_b128 v[48:51], v10 offset:16384\n" \
  "v_mfma_f32_16x16x32_bf16 v[68:71], a[56:59], v[36:39], v[68:71]\n" \
  "ds_read_b128 v[52:55], v10 offset:36864\n" \
  "v_mfma_f32_16x16x32_bf16 v[60:63], a[136:139], v[32:35], v[60:63]\n" \
  "buffer_load_dwordx4 v1, s[36:39], s70 offen offset:2048 sc1 lds\n" \
  "v_mfma_f32_16x16x32_bf16 v[72:75], a[136:139], v[36:39], v[72:75]\n" \
  "v_readlane_b32 s69, v3, s72\n" \
  "v_mfma_f32_16x16x32_bf16 v[64:67], a[216:219], v[32:35], v[64:67]\n" \
  "s_add_u32 s72, s72, 1\n" \
  "v_mfma_f32_16x16x32_bf16 v[76:79], a[216:219], v[36:39], v[76:79]\n" \
  "s_waitcnt lgkmcnt(2)\n" \
  "v_mfma_f32_16x16x32_bf16 v[56:59], a[60:63], v[40:43], v[56:59]\n" \
  "ds_read_b128 v[32:35], v10 offset:17408\n" \
  "v_mfma_f32_16x16x32_bf16 v[68:71], a[60:63], v[44:47], v[68:71]\n" \
  "ds_read_b128 v[36:39], v10 offset:37888\n" \
  "v_mfma_f32_16x16x32_bf16 v[60:63], a[140:143], v[40:43], v[60:63]\n" \
  "buffer_load_dwordx4 v1, s[36:39], s70 offen offset:3072 sc1 lds\n" \
  "v_mfma_f32_16x16x32_bf16 v[72:75], a[140:143], v[44:47], v[72:75]\n" \
  "v_mfma_f32_16x16x32_bf16 v[64:67], a[220:223], v[40:43], v[64:67]\n" \
  "v_mfma_f32_16x16x32_bf16 v[76:79], a[220:223], v[44:47], v[76:79]\n" \
  "s_waitcnt lgkmcnt(2)\n" \
  "v_mfma_f32_16x16x32_bf16 v[56:59], a[64:67], v[48:51], v[56:59]\n" \
  "ds_read_b128 v[40:43], v10 offset:18432\n" \
  "v_mfma_f32_16x16x32_bf16 v[68:71], a[64:67], v[52:55], v[68:71]\n" \
  "ds_read_b128 v[44:47], v10 offset:38912\n" \
  "v_mfma_f32_16x16x32_bf16 v[60:63], a[144:147], v[48:51], v[60:63]\n" \
  "s_add_u32 m0, s53, 49152\n" \
  "v_mfma_f32_16x16x32_bf16 v[72:75], a[144:147], v[52:55], v[72:75]\n" \
  "buffer_load_dwordx4 v1, s[36:39], s71 offen sc1 lds\n" \
  "v_mfma_f32_16x16x32_bf16 v[64:67], a[224:227], v[48:51], v[64:67]\n" \
  "v_mfma_f32_16x16x32_bf16 v[76:79], a[224:227], v[52:55], v[76:79]\n" \
  "s_waitcnt lgkmcnt(2)\n" \
  "v_mfma_f32_16x16x32_bf16 v[56:59], a[68:71], v[32:35], v[56:59]\n" \
  "ds_read_b128 v[48:51], v10 offset:19456\n" \
  "v_mfma_f32_16x16x32_bf16 v[68:71], a[68:71], v[36:39], v[68:71]\n" \
  "ds_read_b128 v[52:55], v10 offset:39936\n" \
  "v_mfma_f32_16x16x32_bf16 v[60:63], a[148:151], v[32:35], v[60:63]\n" \
  "buffer_load_dwordx4 v1, s[36:39], s71 offen offset:1024 sc1 lds\n" \
  "v_mfma_f32_16x16x32_bf16 v[72:75], a[148:151], v[36:39], v[72:75]\n" \
  "s_add_u32 s68, s68, 40960\n" \
  "v_mfma_f32_16x16x32_bf16 v[64:67], a[228:231], v[32:35], v[64:67]\n" \
  "v_mfma_f32_16x16x32_bf16 v[76:79], a[228:231], v[36:39], v[76:79]\n" \
  "s_waitcnt vmcnt(11) lgkmcnt(2)\n" \
  "s_barrier\n" \
  "v_mfma_f32_16x16x32_bf16 v[56:59], a[72:75], v[40:43], v[56:59]\n" \
  "ds_read_b128 v[32:35], v0 offset:0\n" \
  "v_mfma_f32_16x16x32_bf16 v[68:71], a[72:75], v[44:47], v[68:71]\n" \
  "ds_read_b128 v[36:39], v0 offset:20480\n" \
  "v_mfma_f32_16x16x32_bf16 v[60:63], a[152:155], v[40:43], v[60:63]\n" \
  "v_mfma_f32_16x16x32_bf16 v[72:75], a[152:155], v[44:47], v[72:75]\n" \
  "v_mfma_f32_16x16x32_bf16 v[64:67], a[232:235], v[40:43], v[64:67]\n" \
  "v_mfma_f32_16x16x32_bf16 v[76:79], a[232:235], v[44:47], v[76:79]\n" \
  "s_waitcnt lgkmcnt(2)\n" \
  "v_mfma_f32_16x16x32_bf16 v[56:59], a[76:79], v[48:51], v[56:59]\n" \
  "ds_read_b128 v[40:43], v0 offset:1024\n" \
  "v_mfma_f32_16x16x32_bf16 v[68:71], a[76:79], v[52:55], v[68:71]\n" \
  "ds_read_b128 v[44:47], v0 offset:21504\n" \
  "v_mfma_f32_16x16x32_bf16 v[60:63], a[156:159], v[48:51], v[60:63]\n" \
  "s_cmp_eq_u32 s72, 64\n" \
  "v_mfma_f32_16x16x32_bf16 v[72:75], a[156:159], v[52:55], v[72:75]\n" \
  "v_mfma_f32_16x16x32_bf16 v[64:67], a[236:239], v[48:51], v[64:67]\n" \
  "v_mfma_f32_16x16x32_bf16 v[76:79], a[236:239], v[52:55], v[76:79]\n" \
  "s_waitcnt lgkmcnt(2)\n" \
  "v_mfma_f32_16x16x32_bf16 v[100:103], a[0:3], v[32:35], 0\n" \
  "ds_read_b128 v[48:51], v0 offset:2048\n" \
  "v_mfma_f32_16x16x32_bf16 v[112:115], a[0:3], v[36:39], 0\n" \
  "ds_read_b128 v[52:55], v0 offset:22528\n" \
  "v_mfma_f32_16x16x32_bf16 v[104:107], a[80:83], v[32:35], 0\n" \
  "v_cvt_pk_bf16_f32 v16, v56, v57\n" \
  "v_mfma_f32_16x16x32_bf16 v[116:119], a[80:83], v[36:39], 0\n" \
  "v_cvt_pk_bf16_f32 v17, v58, v59\n" \
  "v_mfma_f32_16x16x32_bf16 v[108:111], a[160:163], v[32:35], 0\n" \
  "v_cvt_pk_bf16_f32 v18, v60, v61\n" \
  "v_mfma_f32_16x16x32_bf16 v[120:123], a[160:163], v[36:39], 0\n" \
  "v_cvt_pk_bf16_f32 v19, v62, v63\n" \
  "s_waitcnt lgkmcnt(2)\n" \
  "s_barrier\n" \
  "v_mfma_f32_16x16x32_bf16 v[100:103], a[4:7], v[40:43], v[100:103]\n" \
  "ds_read_b128 v[32:35], v0 offset:3072\n" \
  "v_mfma_f32_16x16x32_bf16 v[112:115], a[4:7], v[44:47], v[112:115]\n" \
  "ds_read_b128 v[36:39], v0 offset:23552\n" \
  "v_mfma_f32_16x16x32_bf16 v[104:107], a[84:87], v[40:43], v[104:107]\n" \
  "buffer_store_dwordx4 v[16:19], v5, s[40:43], s69 offen offset:0 sc1\n" \
  "v_mfma_f32_16x16x32_bf16 v[116:119], a[84:87], v[44:47], v[116:119]\n" \
  "v_cvt_pk_bf16_f32 v20, v68, v69\n" \
  "v_mfma_f32_16x16x32_bf16 v[108:111], a[164:167], v[40:43], v[108:111]\n" \
  "v_cvt_pk_bf16_f32 v21, v70, v71\n" \
  "v_mfma_f32_16x16x32_bf16 v[120:123], a[164:167], v[44:47], v[120:123]\n" \
  "v_cvt_pk_bf16_f32 v22, v72, v73\n" \
  "s_waitcnt lgkmcnt(2)\n" \
  "v_mfma_f32_16x16x32_bf16 v[100:103], a[8:11], v[48:51], v[100:103]\n" \
  "ds_read_b128 v[40:43], v0 offset:4096\n" \
  "v_mfma_f32_16x16x32_bf16 v[112:115], a[8:11], v[52:55], v[112:115]\n" \
  "ds_read_b128 v[44:47], v0 offset:24576\n" \
  "v_mfma_f32_16x16x32_bf16 v[104:107], a[88:91], v[48:51], v[104:107]\n" \
  "v_cvt_pk_bf16_f32 v23, v74, v75\n" \
  "v_mfma_f32_16x16x32_bf16 v[116:119], a[88:91], v[52:55], v[116:119]\n" \
  "buffer_store_dwordx4 v[20:23], v5, s[40:43], s69 offen offset:1024 sc1\n" \
  "v_mfma_f32_16x16x32_bf16 v[108:111], a[168:171], v[48:51], v[108:111]\n" \
  "v_cvt_pk_bf16_f32 v24, v64, v65\n" \
  "v_mfma_f32_16x16x32_bf16 v[120:123], a[168:171], v[52:55], v[120:123]\n" \
  "v_cvt_pk_bf16_f32 v25, v66, v67\n" \
  "s_waitcnt lgkmcnt(2)\n" \
  "v_mfma_f32_16x16x32_bf16 v[100:103], a[12:15], v[32:35], v[100:103]\n" \
  "ds_read_b128 v[48:51], v0 offset:5120\n" \
  "v_mfma_f32_16x16x32_bf16 v[112:115], a[12:15], v[36:39], v[112:115]\n" \
  "ds_read_b128 v[52:55], v0 offset:25600\n" \
  "v_mfma_f32_16x16x32_bf16 v[104:107], a[92:95], v[32:35], v[104:107]\n" \
  "v_cvt_pk_bf16_f32 v26, v76, v77\n" \
  "v_mfma_f32_16x16x32_bf16 v[116:119], a[92:95], v[36:39], v[116:119]\n" \
  "v_cvt_pk_bf16_f32 v27, v78, v79\n" \
  "v_mfma_f32_16x16x32_bf16 v[108:111], a[172:175], v[32:35], v[108:111]\n" \
  "buffer_store_dwordx4 v[24:27], v5, s[40:43], s69 offen offset:2048 sc1\n" \
  "v_mfma_f32_16x16x32_bf16 v[120:123], a[172:175], v[36:39], v[120:123]\n" \
  "s_waitcnt lgkmcnt(2)\n" \
  "v_mfma_f32_16x16x32_bf16 v[100:103], a[16:19], v[40:43], v[100:103]\n" \
  "ds_read_b128 v[32:35], v0 offset:6144\n" \
  "v_mfma_f32_16x16x32_bf16 v[112:115], a[16:19], v[44:47], v[112:115]\n" \
  "ds_read_b128 v[36:39], v0 offset:26624\n" \
  "v_mfma_f32_16x16x32_bf16 v[104:107], a[96:99], v[40:43], v[104:107]\n" \
  "buffer_store_dwordx4 v[16:19], v2, s[40:43], s69 offen offset:0 sc1\n" \
  "v_mfma_f32_16x16x32_bf16 v[116:119], a[96:99], v[44:47], v[116:119]\n" \
  "v_mfma_f32_16x16x32_bf16 v[108:111], a[176:179], v[40:43], v[108:111]\n" \
  "v_mfma_f32_16x16x32_bf16 v[120:123], a[176:179], v[44:47], v[120:123]\n" \
  "s_waitcnt lgkmcnt(2)\n" \
  "v_mfma_f32_16x16x32_bf16 v[100:103], a[20:23], v[48:51], v[100:103]\n" \
  "ds_read_b128 v[40:43], v0 offset:7168\n" \
  "v_mfma_f32_16x16x32_bf16 v[112:115], a[20:23], v[52:55], v[112:115]\n" \
  "ds_read_b128 v[44:47], v0 offset:27648\n" \
  "v_mfma_f32_16x16x32_bf16 v[104:107], a[100:103], v[48:51], v[104:107]\n" \
  "buffer_store_dwordx4 v[20:23], v2, s[40:43], s69 offen offset:1024 sc1\n" \
  "v_mfma_f32_16x16x32_bf16 v[116:119], a[100:103], v[52:55], v[116:119]\n" \
  "v_mfma_f32_16x16x32_bf16 v[108:111], a[180:183], v[48:51], v[108:111]\n" \
  "v_mfma_f32_16x16x32_bf16 v[120:123], a[180:183], v[52:55], v[120:123]\n" \
  "s_waitcnt lgkmcnt(2)\n" \
  "v_mfma_f32_16x16x32_bf16 v[100:103], a[24:27], v[32:35], v[100:103]\n" \
  "ds_read_b128 v[48:51], v0 offset:8192\n" \
  "v_mfma_f32_16x16x32_bf16 v[112:115], a[24:27], v[36:39], v[112:115]\n" \
  "ds_read_b128 v[52:55], v0 offset:28672\n" \
  "v_mfma_f32_16x16x32_bf16 v[104:107], a[104:107], v[32:35], v[104:107]\n" \
  "buffer_store_dwordx4 v[24:27], v2, s[40:43], s69 offen offset:2048 sc1\n" \
  "v_mfma_f32_16x16x32_bf16 v[116:119], a[104:107], v[36:39], v[116:119]\n" \
  "s_nop 0\n" \
  "v_mfma_f32_16x16x32_bf16 v[108:111], a[184:187], v[32:35], v[108:111]\n" \
  "s_nop 0\n" \
  "v_mfma_f32_16x16x32_bf16 v[120:123], a[184:187], v[36:39], v[120:123]\n" \
  "s_waitcnt lgkmcnt(2)\n" \
  "v_mfma_f32_16x16x32_bf16 v[100:103], a[28:31], v[40:43], v[100:103]\n" \
  "ds_read_b128 v[32:35], v0 offset:9216\n" \
  "v_mfma_f32_16x16x32_bf16 v[112:115], a[28:31], v[44:47], v[112:115]\n" \
  "ds_read_b128 v[36:39], v0 offset:29696\n" \
  "v_mfma_f32_16x16x32_bf16 v[104:107], a[108:111], v[40:43], v[104:107]\n" \
  "s_add_u32 s70, s68, 4096\n" \
  "v_mfma_f32_16x16x32_bf16 v[116:119], a[108:111], v[44:47], v[116:119]\n" \
  "s_add_u32 s71, s68, 8192\n" \
  "v_mfma_f32_16x16x32_bf16 v[108:111], a[188:191], v[40:43], v[108:111]\n" \
  "s_add_u32 m0, s53, 81920\n" \
  "v_mfma_f32_16x16x32_bf16 v[120:123], a[188:191], v[44:47], v[120:123]\n" \
  "buffer_load_dwordx4 v1, s[36:39], s68 offen sc1 lds\n" \
  "s_waitcnt lgkmcnt(2)\n" \
  "v_mfma_f32_16x16x32_bf16 v[100:103], a[32:35], v[48:51], v[100:103]\n" \
  "ds_read_b128 v[40:43], v0 offset:10240\n" \
  "v_mfma_f32_16x16x32_bf16 v[112:115], a[32:35], v[52:55], v[112:115]\n" \
  "ds_read_b128 v[44:47], v0 offset:30720\n" \
  "v_mfma_f32_16x16x32_bf16 v[104:107], a[112:115], v[48:51], v[104:107]\n" \
  "buffer_load_dwordx4 v1, s[36:39], s68 offen offset:1024 sc1 lds\n" \
  "v_mfma_f32_16x16x32_bf16 v[116:119], a[112:115], v[52:55], v[116:119]\n" \
  "v_mfma_f32_16x16x32_bf16 v[108:111], a[192:195], v[48:51], v[108:111]\n" \
  "v_mfma_f32_16x16x32_bf16 v[120:123], a[192:195], v[52:55], v[120:123]\n" \
  "s_waitcnt lgkmcnt(2)\n" \
  "v_mfma_f32_16x16x32_bf16 v[100:103], a[36:39], v[32:35], v[100:103]\n" \
  "ds_read_b128 v[48:51], v0 offset:11264\n" \
  "v_mfma_f32_16x16x32_bf16 v[112:115], a[36:39], v[36:39], v[112:115]\n" \
  "ds_read_b128 v[52:55], v0 offset:31744\n" \
  "v_mfma_f32_16x16x32_bf16 v[104:107], a[116:119], v[32:35], v[104:107]\n" \
  "buffer_load_dwordx4 v1, s[36:39], s68 offen offset:2048 sc1 lds\n" \
  "v_mfma_f32_16x16x32_bf16 v[116:119], a[116:119], v[36:39], v[116:119]\n" \
  "v_mfma_f32_16x16x32_bf16 v[108:111], a[196:199], v[32:35], v[108:111]\n" \
  "v_mfma_f32_16x16x32_bf16 v[120:123], a[196:199], v[36:39], v[120:123]\n" \
  "s_waitcnt lgkmcnt(2)\n" \
  "v_mfma_f32_16x16x32_bf16 v[100:103], a[40:43], v[40:43], v[100:103]\n" \
  "ds_read_b128 v[32:35], v0 offset:12288\n" \
  "v_mfma_f32_16x16x32_bf16 v[112:115], a[40:43], v[44:47], v[112:115]\n" \
  "ds_read_b128 v[36:39], v0 offset:32768\n" \
  "v_mfma_f32_16x16x32_bf16 v[104:107], a[120:123], v[40:43], v[104:107]\n" \
  "buffer_load_dwordx4 v1, s[36:39], s68 offen offset:3072 sc1 lds\n" \
  "v_mfma_f32_16x16x32_bf16 v[116:119], a[120:123], v[44:47], v[116:119]\n" \
  "v_mfma_f32_16x16x32_bf16 v[108:111], a[200:203], v[40:43], v[108:111]\n" \
  "v_mfma_f32_16x16x32_bf16 v[120:123], a[200:203], v[44:47], v[120:123]\n" \
  "s_waitcnt lgkmcnt(2)\n" \
  "v_mfma_f32_16x16x32_bf16 v[100:103], a[44:47], v[48:51], v[100:103]\n" \
  "ds_read_b128 v[40:43], v0 offset:13312\n" \
  "v_mfma_f32_16x16x32_bf16 v[112:115], a[44:47], v[52:55], v[112:115]\n" \
  "ds_read_b128 v[44:47], v0 offset:33792\n" \
  "v_mfma_f32_16x16x32_bf16 v[104:107], a[124:127], v[48:51], v[104:107]\n" \
  "s_add_u32 m0, s53, 86016\n" \
  "v_mfma_f32_16x16x32_bf16 v[116:119], a[124:127], v[52:55], v[116:119]\n" \
  "buffer_load_dwordx4 v1, s[36:39], s70 offen sc1 lds\n" \
  "v_mfma_f32_16x16x32_bf16 v[108:111], a[204:207], v[48:51], v[108:111]\n" \
  "v_mfma_f32_16x16x32_bf16 v[120:123], a[204:207], v[52:55], v[120:123]\n" \
  "s_waitcnt lgkmcnt(2)\n" \
  "v_mfma_f32_16x16x32_bf16 v[100:103], a[48:51], v[32:35], v[100:103]\n" \
  "ds_read_b128 v[48:51], v0 offset:14336\n" \
  "v_mfma_f32_16x16x32_bf16 v[112:115], a[48:51], v[36:39], v[112:115]\n" \
  "ds_read_b128 v[52:55], v0 offset:34816\n" \
  "v_mfma_f32_16x16x32_bf16 v[104:107], a[128:131], v[32:35], v[104:107]\n" \
  "buffer_load_dwordx4 v1, s[36:39], s70 offen offset:1024 sc1 lds\n" \
  "v_mfma_f32_16x16x32_bf16 v[116:119], a[128:131], v[36:39], v[116:119]\n" \
  "v_mfma_f32_16x16x32_bf16 v[108:111], a[208:211], v[32:35], v[108:111]\n" \
  "v_mfma_f32_16x16x32_bf16 v[120:123], a[208:211], v[36:39], v[120:123]\n" \
  "s_waitcnt vmcnt(6) lgkmcnt(2)\n" \
  "s_barrier\n" \
  "v_mfma_f32_16x16x32_bf16 v[100:103], a[52:55], v[40:43], v[100:103]\n" \
  "ds_read_b128 v[32:35], v0 offset:15360\n" \
  "v_mfma_f32_16x16x32_bf16 v[112:115], a[52:55], v[44:47], v[112:115]\n" \
  "ds_read_b128 v[36:39], v0 offset:35840\n" \
  "v_mfma_f32_16x16x32_bf16 v[104:107], a[132:135], v[40:43], v[104:107]\n" \
  "buffer_atomic_add v11, v4, s[40:43], s54 offen\n" \
  "v_mfma_f32_16x16x32_bf16 v[116:119], a[132:135], v[44:47], v[116:119]\n" \
  "s_add_u32 s54, s54, s55\n" \
  "v_mfma_f32_16x16x32_bf16 v[108:111], a[212:215], v[40:43], v[108:111]\n" \
  "s_and_b32 s54, s54, 0xffffdfff\n" \
  "v_mfma_f32_16x16x32_bf16 v[120:123], a[212:215], v[44:47], v[120:123]\n" \
  "s_waitcnt lgkmcnt(2)\n" \
  "v_mfma_f32_16x16x32_bf16 v[100:103], a[56:59], v[48:51], v[100:103]\n" \
  "ds_read_b128 v[40:43], v0 offset:16384\n" \
  "v_mfma_f32_16x16x32_bf16 v[112:115], a[56:59], v[52:55], v[112:115]\n" \
  "ds_read_b128 v[44:47], v0 offset:36864\n" \
  "v_mfma_f32_16x16x32_bf16 v[104:107], a[136:139], v[48:51], v[104:107]\n" \
  "buffer_load_dwordx4 v1, s[36:39], s70 offen offset:2048 sc1 lds\n" \
  "v_mfma_f32_16x16x32_bf16 v[116:119], a[136:139], v[52:55], v[116:119]\n" \
  "v_readlane_b32 s69, v3, s72\n" \
  "v_mfma_f32_16x16x32_bf16 v[108:111], a[216:219], v[48:51], v[108:111]\n" \
  "s_add_u32 s72, s72, 1\n" \
  "v_mfma_f32_16x16x32_bf16 v[120:123], a[216:219], v[52:55], v[120:123]\n" \
  "s_waitcnt lgkmcnt(2)\n" \
  "v_mfma_f32_16x16x32_bf16 v[100:103], a[60:63], v[32:35], v[100:103]\n" \
  "ds_read_b128 v[48:51], v0 offset:17408\n" \
  "v_mfma_f32_16x16x32_bf16 v[112:115], a[60:63], v[36:39], v[112:115]\n" \
  "ds_read_b128 v[52:55], v0 offset:37888\n" \
  "v_mfma_f32_16x16x32_bf16 v[104:107], a[140:143], v[32:35], v[104:107]\n" \
  "buffer_load_dwordx4 v1, s[36:39], s70 offen offset:3072 sc1 lds\n" \
  "v_mfma_f32_16x16x32_bf16 v[116:119], a[140:143], v[36:39], v[116:119]\n" \
  "v_mfma_f32_16x16x32_bf16 v[108:111], a[220:223], v[32:35], v[108:111]\n" \
  "v_mfma_f32_16x16x32_bf16 v[120:123], a[220:223], v[36:39], v[120:123]\n" \
  "s_waitcnt lgkmcnt(2)\n" \
  "v_mfma_f32_16x16x32_bf16 v[100:103], a[64:67], v[40:43], v[100:103]\n" \
  "ds_read_b128 v[32:35], v0 offset:18432\n" \
  "v_mfma_f32_16x16x32_bf16 v[112:115], a[64:67], v[44:47], v[112:115]\n" \
  "ds_read_b128 v[36:39], v0 offset:38912\n" \
  "v_mfma_f32_16x16x32_bf16 v[104:107], a[144:147], v[40:43], v[104:107]\n" \
  "s_add_u32 m0, s53, 90112\n" \
  "v_mfma_f32_16x16x32_bf16 v[116:119], a[144:147], v[44:47], v[116:119]\n" \
  "buffer_load_dwordx4 v1, s[36:39], s71 offen sc1 lds\n" \
  "v_mfma_f32_16x16x32_bf16 v[108:111], a[224:227], v[40:43], v[108:111]\n" \
  "v_mfma_f32_16x16x32_bf16 v[120:123], a[224:227], v[44:47], v[120:123]\n" \
  "s_waitcnt lgkmcnt(2)\n" \
  "v_mfma_f32_16x16x32_bf16 v[100:103], a[68:71], v[48:51], v[100:103]\n" \
  "ds_read_b128 v[40:43], v0 offset:19456\n" \
  "v_mfma_f32_16x16x32_bf16 v[112:115], a[68:71], v[52:55], v[112:115]\n" \
  "ds_read_b128 v[44:47], v0 offset:39936\n" \
  "v_mfma_f32_16x16x32_bf16 v[104:107], a[148:151], v[48:51], v[104:107]\n" \
  "buffer_load_dwordx4 v1, s[36:39], s71 offen offset:1024 sc1 lds\n" \
  "v_mfma_f32_16x16x32_bf16 v[116:119], a[148:151], v[52:55], v[116:119]\n" \
  "s_add_u32 s68, s68, 40960\n" \
  "v_mfma_f32_16x16x32_bf16 v[108:111], a[228:231], v[48:51], v[108:111]\n" \
  "v_mfma_f32_16x16x32_bf16 v[120:123], a[228:231], v[52:55], v[120:123]\n" \
  "s_waitcnt vmcnt(11) lgkmcnt(2)\n" \
  "s_barrier\n" \
  "v_mfma_f32_16x16x32_bf16 v[100:103], a[72:75], v[32:35], v[100:103]\n" \
  "ds_read_b128 v[48:51], v9 offset:0\n" \
  "v_mfma_f32_16x16x32_bf16 v[112:115], a[72:75], v[36:39], v[112:115]\n" \
  "ds_read_b128 v[52:55], v9 offset:20480\n" \
  "v_mfma_f32_16x16x32_bf16 v[104:107], a[152:155], v[32:35], v[104:107]\n" \
  "v_mfma_f32_16x16x32_bf16 v[116:119], a[152:155], v[36:39], v[116:119]\n" \
  "v_mfma_f32_16x16x32_bf16 v[108:111], a[232:235], v[32:35], v[108:111]\n" \
  "v_mfma_f32_16x16x32_bf16 v[120:123], a[232:235], v[36:39], v[120:123]\n" \
  "s_waitcnt lgkmcnt(2)\n" \
  "v_mfma_f32_16x16x32_bf16 v[100:103], a[76:79], v[40:43], v[100:103]\n" \
  "ds_read_b128 v[32:35], v9 offset:1024\n" \
  "v_mfma_f32_16x16x32_bf16 v[112:115], a[76:79], v[44:47], v[112:115]\n" \
  "ds_read_b128 v[36:39], v9 offset:21504\n" \
  "v_mfma_f32_16x16x32_bf16 v[104:107], a[156:159], v[40:43], v[104:107]\n" \
  "s_cmp_eq_u32 s72, 64\n" \
  "v_mfma_f32_16x16x32_bf16 v[116:119], a[156:159], v[44:47], v[116:119]\n" \
  "v_mfma_f32_16x16x32_bf16 v[108:111], a[236:239], v[40:43], v[108:111]\n" \
  "v_mfma_f32_16x16x32_bf16 v[120:123], a[236:239], v[44:47], v[120:123]\n" \
  "s_cbranch_scc1 LPE_%=\n" \
  "s_waitcnt lgkmcnt(2)\n" \
  "v_mfma_f32_16x16x32_bf16 v[56:59], a[0:3], v[48:51], 0\n" \
  "ds_read_b128 v[40:43], v9 offset:2048\n" \
  "v_mfma_f32_16x16x32_bf16 v[68:71], a[0:3], v[52:55], 0\n" \
  "ds_read_b128 v[44:47], v9 offset:22528\n" \
  "v_mfma_f32_16x16x32_bf16 v[60:63], a[80:83], v[48:51], 0\n" \
  "v_cvt_pk_bf16_f32 v16, v100, v101\n" \
  "v_mfma_f32_16x16x32_bf16 v[72:75], a[80:83], v[52:55], 0\n" \
  "v_cvt_pk_bf16_f32 v17, v102, v103\n" \
  "v_mfma_f32_16x16x32_bf16 v[64:67], a[160:163], v[48:51], 0\n" \
  "v_cvt_pk_bf16_f32 v18, v104, v105\n" \
  "v_mfma_f32_16x16x32_bf16 v[76:79], a[160:163], v[52:55], 0\n" \
  "v_cvt_pk_bf16_f32 v19, v106, v107\n" \
  "s_waitcnt lgkmcnt(2)\n" \
  "s_barrier\n" \
  "v_mfma_f32_16x16x32_bf16 v[56:59], a[4:7], v[32:35], v[56:59]\n" \
  "ds_read_b128 v[48:51], v9 offset:3072\n" \
  "v_mfma_f32_16x16x32_bf16 v[68:71], a[4:7], v[36:39], v[68:71]\n" \
  "ds_read_b128 v[52:55], v9 offset:23552\n" \
  "v_mfma_f32_16x16x32_bf16 v[60:63], a[84:87], v[32:35], v[60:63]\n" \
  "buffer_store_dwordx4 v[16:19], v5, s[40:43], s69 offen offset:0 sc1\n" \
  "v_mfma_f32_16x16x32_bf16 v[72:75], a[84:87], v[36:39], v[72:75]\n" \
  "v_cvt_pk_bf16_f32 v20, v112, v113\n" \
  "v_mfma_f32_16x16x32_bf16 v[64:67], a[164:167], v[32:35], v[64:67]\n" \
  "v_cvt_pk_bf16_f32 v21, v114, v115\n" \
  "v_mfma_f32_16x16x32_bf16 v[76:79], a[164:167], v[36:39], v[76:79]\n" \
  "v_cvt_pk_bf16_f32 v22, v116, v117\n" \
  "s_waitcnt lgkmcnt(2)\n" \
  "v_mfma_f32_16x16x32_bf16 v[56:59], a[8:11], v[40:43], v[56:59]\n" \
  "ds_read_b128 v[32:35], v9 offset:4096\n" \
  "v_mfma_f32_16x16x32_bf16 v[68:71], a[8:11], v[44:47], v[68:71]\n" \
  "ds_read_b128 v[36:39], v9 offset:24576\n" \
  "v_mfma_f32_16x16x32_bf16 v[60:63], a[88:91], v[40:43], v[60:63]\n" \
  "v_cvt_pk_bf16_f32 v23, v118, v119\n" \
  "v_mfma_f32_16x16x32_bf16 v[72:75], a[88:91], v[44:47], v[72:75]\n" \
  "buffer_store_dwordx4 v[20:23], v5, s[40:43], s69 offen offset:1024 sc1\n" \
  "v_mfma_f32_16x16x32_bf16 v[64:67], a[168:171], v[40:43], v[64:67]\n" \
  "v_cvt_pk_bf16_f32 v24, v108, v109\n" \
  "v_mfma_f32_16x16x32_bf16 v[76:79], a[168:171], v[44:47], v[76:79]\n" \
  "v_cvt_pk_bf16_f32 v25, v110, v111\n" \
  "s_waitcnt lgkmcnt(2)\n" \
  "v_mfma_f32_16x16x32_bf16 v[56:59], a[12:15], v[48:51], v[56:59]\n" \
  "ds_read_b128 v[40:43], v9 offset:5120\n" \
  "v_mfma_f32_16x16x32_bf16 v[68:71], a[12:15], v[52:55], v[68:71]\n" \
  "ds_read_b128 v[44:47], v9 offset:25600\n" \
  "v_mfma_f32_16x16x32_bf16 v[60:63], a[92:95], v[48:51], v[60:63]\n" \
  "v_cvt_pk_bf16_f32 v26, v120, v121\n" \
  "v_mfma_f32_16x16x32_bf16 v[72:75], a[92:95], v[52:55], v[72:75]\n" \
  "v_cvt_pk_bf16_f32 v27, v122, v123\n" \
  "v_mfma_f32_16x16x32_bf16 v[64:67], a[172:175], v[48:51], v[64:67]\n" \
  "buffer_store_dwordx4 v[24:27], v5, s[40:43], s69 offen offset:2048 sc1\n" \
  "v_mfma_f32_16x16x32_bf16 v[76:79], a[172:175], v[52:55], v[76:79]\n" \
  "s_waitcnt lgkmcnt(2)\n" \
  "v_mfma_f32_16x16x32_bf16 v[56:59], a[16:19], v[32:35], v[56:59]\n" \
  "ds_read_b128 v[48:51], v9 offset:6144\n" \
  "v_mfma_f32_16x16x32_bf16 v[68:71], a[16:19], v[36:39], v[68:71]\n" \
  "ds_read_b128 v[52:55], v9 offset:26624\n" \
  "v_mfma_f32_16x16x32_bf16 v[60:63], a[96:99], v[32:35], v[60:63]\n" \
  "buffer_store_dwordx4 v[16:19], v2, s[40:43], s69 offen offset:0 sc1\n" \
  "v_mfma_f32_16x16x32_bf16 v[72:75], a[96:99], v[36:39], v[72:75]\n" \
  "v_mfma_f32_16x16x32_bf16 v[64:67], a[176:179], v[32:35], v[64:67]\n" \
  "v_mfma_f32_16x16x32_bf16 v[76:79], a[176:179], v[36:39], v[76:79]\n" \
  "s_waitcnt lgkmcnt(2)\n" \
  "v_mfma_f32_16x16x32_bf16 v[56:59], a[20:23], v[40:43], v[56:59]\n" \
  "ds_read_b128 v[32:35], v9 offset:7168\n" \
  "v_mfma_f32_16x16x32_bf16 v[68:71], a[20:23], v[44:47], v[68:71]\n" \
  "ds_read_b128 v[36:39], v9 offset:27648\n" \
  "v_mfma_f32_16x16x32_bf16 v[60:63], a[100:103], v[40:43], v[60:63]\n" \
  "buffer_store_dwordx4 v[20:23], v2, s[40:43], s69 offen offset:1024 sc1\n" \
  "v_mfma_f32_16x16x32_bf16 v[72:75], a[100:103], v[44:47], v[72:75]\n" \
  "v_mfma_f32_16x16x32_bf16 v[64:67], a[180:183], v[40:43], v[64:67]\n" \
  "v_mfma_f32_16x16x32_bf16 v[76:79], a[180:183], v[44:47], v[76:79]\n" \
  "s_waitcnt lgkmcnt(2)\n" \
  "v_mfma_f32_16x16x32_bf16 v[56:59], a[24:27], v[48:51], v[56:59]\n" \
  "ds_read_b128 v[40:43], v9 offset:8192\n" \
  "v_mfma_f32_16x16x32_bf16 v[68:71], a[24:27], v[52:55], v[68:71]\n" \
  "ds_read_b128 v[44:47], v9 offset:28672\n" \
  "v_mfma_f32_16x16x32_bf16 v[60:63], a[104:107], v[48:51], v[60:63]\n" \
  "buffer_store_dwordx4 v[24:27], v2, s[40:43], s69 offen offset:2048 sc1\n" \
  "v_mfma_f32_16x16x32_bf16 v[72:75], a[104:107], v[52:55], v[72:75]\n" \
  "s_nop 0\n" \
  "v_mfma_f32_16x16x32_bf16 v[64:67], a[184:187], v[48:51], v[64:67]\n" \
  "s_nop 0\n" \
  "v_mfma_f32_16x16x32_bf16 v[76:79], a[184:187], v[52:55], v[76:79]\n" \
  "s_waitcnt lgkmcnt(2)\n" \
  "v_mfma_f32_16x16x32_bf16 v[56:59], a[28:31], v[32:35], v[56:59]\n" \
  "ds_read_b128 v[48:51], v9 offset:9216\n" \
  "v_mfma_f32_16x16x32_bf16 v[68:71], a[28:31], v[36:39], v[68:71]\n" \
  "ds_read_b128 v[52:55], v9 offset:29696\n" \
  "v_mfma_f32_16x16x32_bf16 v[60:63], a[108:111], v[32:35], v[60:63]\n" \
  "s_add_u32 s70, s68, 4096\n" \
  "v_mfma_f32_16x16x32_bf16 v[72:75], a[108:111], v[36:39], v[72:75]\n" \
  "s_add_u32 s71, s68, 8192\n" \
  "v_mfma_f32_16x16x32_bf16 v[64:67], a[188:191], v[32:35], v[64:67]\n" \
  "s_add_u32 m0, s53, 0\n" \
  "v_mfma_f32_16x16x32_bf16 v[76:79], a[188:191], v[36:39], v[76:79]\n" \
  "buffer_load_dwordx4 v1, s[36:39], s68 offen sc1 lds\n" \
  "s_waitcnt lgkmcnt(2)\n" \
  "v_mfma_f32_16x16x32_bf16 v[56:59], a[32:35], v[40:43], v[56:59]\n" \
  "ds_read_b128 v[32:35], v9 offset:10240\n" \
  "v_mfma_f32_16x16x32_bf16 v[68:71], a[32:35], v[44:47], v[68:71]\n" \
  "ds_read_b128 v[36:39], v9 offset:30720\n" \
  "v_mfma_f32_16x16x32_bf16 v[60:63], a[112:115], v[40:43], v[60:63]\n" \
  "buffer_load_dwordx4 v1, s[36:39], s68 offen offset:1024 sc1 lds\n" \
  "v_mfma_f32_16x16x32_bf16 v[72:75], a[112:115], v[44:47], v[72:75]\n" \
  "v_mfma_f32_16x16x32_bf16 v[64:67], a[192:195], v[40:43], v[64:67]\n" \
  "v_mfma_f32_16x16x32_bf16 v[76:79], a[192:195], v[44:47], v[76:79]\n" \
  "s_waitcnt lgkmcnt(2)\n" \
  "v_mfma_f32_16x16x32_bf16 v[56:59], a[36:39], v[48:51], v[56:59]\n" \
  "ds_read_b128 v[40:43], v9 offset:11264\n" \
  "v_mfma_f32_16x16x32_bf16 v[68:71], a[36:39], v[52:55], v[68:71]\n" \
  "ds_read_b128 v[44:47], v9 offset:31744\n" \
  "v_mfma_f32_16x16x32_bf16 v[60:63], a[116:119], v[48:51], v[60:63]\n" \
  "buffer_load_dwordx4 v1, s[36:39], s68 offen offset:2048 sc1 lds\n" \
  "v_mfma_f32_16x16x32_bf16 v[72:75], a[116:119], v[52:55], v[72:75]\n" \
  "v_mfma_f32_16x16x32_bf16 v[64:67], a[196:199], v[48:51], v[64:67]\n" \
  "v_mfma_f32_16x16x32_bf16 v[76:79], a[196:199], v[52:55], v[76:79]\n" \
  "s_waitcnt lgkmcnt(2)\n" \
  "v_mfma_f32_16x16x32_bf16 v[56:59], a[40:43], v[32:35], v[56:59]\n" \
  "ds_read_b128 v[48:51], v9 offset:12288\n" \
  "v_mfma_f32_16x16x32_bf16 v[68:71], a[40:43], v[36:39], v[68:71]\n" \
  "ds_read_b128 v[52:55], v9 offset:32768\n" \
  "v_mfma_f32_16x16x32_bf16 v[60:63], a[120:123], v[32:35], v[60:63]\n" \
  "buffer_load_dwordx4 v1, s[36:39], s68 offen offset:3072 sc1 lds\n" \
  "v_mfma_f32_16x16x32_bf16 v[72:75], a[120:123], v[36:39], v[72:75]\n" \
  "v_mfma_f32_16x16x32_bf16 v[64:67], a[200:203], v[32:35], v[64:67]\n" \
  "v_mfma_f32_16x16x32_bf16 v[76:79], a[200:203], v[36:39], v[76:79]\n" \
  "s_waitcnt lgkmcnt(2)\n" \
  "v_mfma_f32_16x16x32_bf16 v[56:59], a[44:47], v[40:43], v[56:59]\n" \
  "ds_read_b128 v[32:35], v9 offset:13312\n" \
  "v_mfma_f32_16x16x32_bf16 v[68:71], a[44:47], v[44:47], v[68:71]\n" \
  "ds_read_b128 v[36:39], v9 offset:33792\n" \
  "v_mfma_f32_16x16x32_bf16 v[60:63], a[124:127], v[40:43], v[60:63]\n" \
  "s_add_u32 m0, s53, 4096\n" \
  "v_mfma_f32_16x16x32_bf16 v[72:75], a[124:127], v[44:47], v[72:75]\n" \
  "buffer_load_dwordx4 v1, s[36:39], s70 offen sc1 lds\n" \
  "v_mfma_f32_16x16x32_bf16 v[64:67], a[204:207], v[40:43], v[64:67]\n" \
  "v_mfma_f32_16x16x32_bf16 v[76:79], a[204:207], v[44:47], v[76:79]\n" \
  "s_waitcnt lgkmcnt(2)\n" \
  "v_mfma_f32_16x16x32_bf16 v[56:59], a[48:51], v[48:51], v[56:59]\n" \
  "ds_read_b128 v[40:43], v9 offset:14336\n" \
  "v_mfma_f32_16x16x32_bf16 v[68:71], a[48:51], v[52:55], v[68:71]\n" \
  "ds_read_b128 v[44:47], v9 offset:34816\n" \
  "v_mfma_f32_16x16x32_bf16 v[60:63], a[128:131], v[48:51], v[60:63]\n" \
  "buffer_load_dwordx4 v1, s[36:39], s70 offen offset:1024 sc1 lds\n" \
  "v_mfma_f32_16x16x32_bf16 v[72:75], a[128:131], v[52:55], v[72:75]\n" \
  "v_mfma_f32_16x16x32_bf16 v[64:67], a[208:211], v[48:51], v[64:67]\n" \
  "v_mfma_f32_16x16x32_bf16 v[76:79], a[208:211], v[52:55], v[76:79]\n" \
  "s_waitcnt vmcnt(6) lgkmcnt(2)\n" \
  "s_barrier\n" \
  "v_mfma_f32_16x16x32_bf16 v[56:59], a[52:55], v[32:35], v[56:59]\n" \
  "ds_read_b128 v[48:51], v9 offset:15360\n" \
  "v_mfma_f32_16x16x32_bf16 v[68:71], a[52:55], v[36:39], v[68:71]\n" \
  "ds_read_b128 v[52:55], v9 offset:35840\n" \
  "v_mfma_f32_16x16x32_bf16 v[60:63], a[132:135], v[32:35], v[60:63]\n" \
  "buffer_atomic_add v11, v4, s[40:43], s54 offen\n" \
  "v_mfma_f32_16x16x32_bf16 v[72:75], a[132:135], v[36:39], v[72:75]\n" \
  "s_add_u32 s54, s54, s55\n" \
  "v_mfma_f32_16x16x32_bf16 v[64:67], a[212:215], v[32:35], v[64:67]\n" \
  "s_and_b32 s54, s54, 0xffffdfff\n" \
  "v_mfma_f32_16x16x32_bf16 v[76:79], a[212:215], v[36:39], v[76:79]\n" \
  "s_waitcnt lgkmcnt(2)\n" \
  "v_mfma_f32_16x16x32_bf16 v[56:59], a[56:59], v[40:43], v[56:59]\n" \
  "ds_read_b128 v[32:35], v9 offset:16384\n" \
  "v_mfma_f32_16x16x32_bf16 v[68:71], a[56:59], v[44:47], v[68:71]\n" \
  "ds_read_b128 v[36:39], v9 offset:36864\n" \
  "v_mfma_f32_16x16x32_bf16 v[60:63], a[136:139], v[40:43], v[60:63]\n" \
  "buffer_load_dwordx4 v1, s[36:39], s70 offen offset:2048 sc1 lds\n" \
  "v_mfma_f32_16x16x32_bf16 v[72:75], a[136:139], v[44:47], v[72:75]\n" \
  "v_readlane_b32 s69, v3, s72\n" \
  "v_mfma_f32_16x16x32_bf16 v[64:67], a[216:219], v[40:43], v[64:67]\n" \
  "s_add_u32 s72, s72, 1\n" \
  "v_mfma_f32_16x16x32_bf16 v[76:79], a[216:219], v[44:47], v[76:79]\n" \
  "s_waitcnt lgkmcnt(2)\n" \
  "v_mfma_f32_16x16x32_bf16 v[56:59], a[60:63], v[48:51], v[56:59]\n" \
  "ds_read_b128 v[40:43], v9 offset:17408\n" \
  "v_mfma_f32_16x16x32_bf16 v[68:71], a[60:63], v[52:55], v[68:71]\n" \
  "ds_read_b128 v[44:47], v9 offset:37888\n" \
  "v_mfma_f32_16x16x32_bf16 v[60:63], a[140:143], v[48:51], v[60:63]\n" \
  "buffer_load_dwordx4 v1, s[36:39], s70 offen offset:3072 sc1 lds\n" \
  "v_mfma_f32_16x16x32_bf16 v[72:75], a[140:143], v[52:55], v[72:75]\n" \
  "v_mfma_f32_16x16x32_bf16 v[64:67], a[220:223], v[48:51], v[64:67]\n" \
  "v_mfma_f32_16x16x32_bf16 v[76:79], a[220:223], v[52:55], v[76:79]\n" \
  "s_waitcnt lgkmcnt(2)\n" \
  "v_mfma_f32_16x16x32_bf16 v[56:59], a[64:67], v[32:35], v[56:59]\n" \
  "ds_read_b128 v[48:51], v9 offset:18432\n" \
  "v_mfma_f32_16x16x32_bf16 v[68:71], a[64:67], v[36:39], v[68:71]\n" \
  "ds_read_b128 v[52:55], v9 offset:38912\n" \
  "v_mfma_f32_16x16x32_bf16 v[60:63], a[144:147], v[32:35], v[60:63]\n" \
  "s_add_u32 m0, s53, 8192\n" \
  "v_mfma_f32_16x16x32_bf16 v[72:75], a[144:147], v[36:39], v[72:75]\n" \
  "buffer_load_dwordx4 v1, s[36:39], s71 offen sc1 lds\n" \
  "v_mfma_f32_16x16x32_bf16 v[64:67], a[224:227], v[32:35], v[64:67]\n" \
  "v_mfma_f32_16x16x32_bf16 v[76:79], a[224:227], v[36:39], v[76:79]\n" \
  "s_waitcnt lgkmcnt(2)\n" \
  "v_mfma_f32_16x16x32_bf16 v[56:59], a[68:71], v[40:43], v[56:59]\n" \
  "ds_read_b128 v[32:35], v9 offset:19456\n" \
  "v_mfma_f32_16x16x32_bf16 v[68:71], a[68:71], v[44:47], v[68:71]\n" \
  "ds_read_b128 v[36:39], v9 offset:39936\n" \
  "v_mfma_f32_16x16x32_bf16 v[60:63], a[148:151], v[40:43], v[60:63]\n" \
  "buffer_load_dwordx4 v1, s[36:39], s71 offen offset:1024 sc1 lds\n" \
  "v_mfma_f32_16x16x32_bf16 v[72:75], a[148:151], v[44:47], v[72:75]\n" \
  "s_add_u32 s68, s68, 40960\n" \
  "v_mfma_f32_16x16x32_bf16 v[64:67], a[228:231], v[40:43], v[64:67]\n" \
  "v_mfma_f32_16x16x32_bf16 v[76:79], a[228:231], v[44:47], v[76:79]\n" \
  "s_waitcnt vmcnt(11) lgkmcnt(2)\n" \
  "s_barrier\n" \
  "v_mfma_f32_16x16x32_bf16 v[56:59], a[72:75], v[48:51], v[56:59]\n" \
  "ds_read_b128 v[40:43], v10 offset:0\n" \
  "v_mfma_f32_16x16x32_bf16 v[68:71], a[72:75], v[52:55], v[68:71]\n" \
  "ds_read_b128 v[44:47], v10 offset:20480\n" \
  "v_mfma_f32_16x16x32_bf16 v[60:63], a[152:155], v[48:51], v[60:63]\n" \
  "v_mfma_f32_16x16x32_bf16 v[72:75], a[152:155], v[52:55], v[72:75]\n" \
  "v_mfma_f32_16x16x32_bf16 v[64:67], a[232:235], v[48:51], v[64:67]\n" \
  "v_mfma_f32_16x16x32_bf16 v[76:79], a[232:235], v[52:55], v[76:79]\n" \
  "s_waitcnt lgkmcnt(2)\n" \
  "v_mfma_f32_16x16x32_bf16 v[56:59], a[76:79], v[32:35], v[56:59]\n" \
  "ds_read_b128 v[48:51], v10 offset:1024\n" \
  "v_mfma_f32_16x16x32_bf16 v[68:71], a[76:79], v[36:39], v[68:71]\n" \
  "ds_read_b128 v[52:55], v10 offset:21504\n" \
  "v_mfma_f32_16x16x32_bf16 v[60:63], a[156:159], v[32:35], v[60:63]\n" \
  "s_cmp_eq_u32 s72, 64\n" \
  "v_mfma_f32_16x16x32_bf16 v[72:75], a[156:159], v[36:39], v[72:75]\n" \
  "v_mfma_f32_16x16x32_bf16 v[64:67], a[236:239], v[32:35], v[64:67]\n" \
  "v_mfma_f32_16x16x32_bf16 v[76:79], a[236:239], v[36:39], v[76:79]\n" \
  "s_waitcnt lgkmcnt(2)\n" \
  "v_mfma_f32_16x16x32_bf16 v[100:103], a[0:3], v[40:43], 0\n" \
  "ds_read_b128 v[32:35], v10 offset:2048\n" \
  "v_mfma_f32_16x16x32_bf16 v[112:115], a[0:3], v[44:47], 0\n" \
  "ds_read_b128 v[36:39], v10 offset:22528\n" \
  "v_mfma_f32_16x16x32_bf16 v[104:107], a[80:83], v[40:43], 0\n" \
  "v_cvt_pk_bf16_f32 v16, v56, v57\n" \
  "v_mfma_f32_16x16x32_bf16 v[116:119], a[80:83], v[44:47], 0\n" \
  "v_cvt_pk_bf16_f32 v17, v58, v59\n" \
  "v_mfma_f32_16x16x32_bf16 v[108:111], a[160:163], v[40:43], 0\n" \
  "v_cvt_pk_bf16_f32 v18, v60, v61\n" \
  "v_mfma_f32_16x16x32_bf16 v[120:123], a[160:163], v[44:47], 0\n" \
  "v_cvt_pk_bf16_f32 v19, v62, v63\n" \
  "s_waitcnt lgkmcnt(2)\n" \
  "s_barrier\n" \
  "v_mfma_f32_16x16x32_bf16 v[100:103], a[4:7], v[48:51], v[100:103]\n" \
  "ds_read_b128 v[40:43], v10 offset:3072\n" \
  "v_mfma_f32_16x16x32_bf16 v[112:115], a[4:7], v[52:55], v[112:115]\n" \
  "ds_read_b128 v[44:47], v10 offset:23552\n" \
  "v_mfma_f32_16x16x32_bf16 v[104:107], a[84:87], v[48:51], v[104:107]\n" \
  "buffer_store_dwordx4 v[16:19], v5, s[40:43], s69 offen offset:0 sc1\n" \
  "v_mfma_f32_16x16x32_bf16 v[116:119], a[84:87], v[52:55], v[116:119]\n" \
  "v_cvt_pk_bf16_f32 v20, v68, v69\n" \
  "v_mfma_f32_16x16x32_bf16 v[108:111], a[164:167], v[48:51], v[108:111]\n" \
  "v_cvt_pk_bf16_f32 v21, v70, v71\n" \
  "v_mfma_f32_16x16x32_bf16 v[120:123], a[164:167], v[52:55], v[120:123]\n" \
  "v_cvt_pk_bf16_f32 v22, v72, v73\n" \
  "s_waitcnt lgkmcnt(2)\n" \
  "v_mfma_f32_16x16x32_bf16 v[100:103], a[8:11], v[32:35], v[100:103]\n" \
  "ds_read_b128 v[48:51], v10 offset:4096\n" \
  "v_mfma_f32_16x16x32_bf16 v[112:115], a[8:11], v[36:39], v[112:115]\n" \
  "ds_read_b128 v[52:55], v10 offset:24576\n" \
  "v_mfma_f32_16x16x32_bf16 v[104:107], a[88:91], v[32:35], v[104:107]\n" \
  "v_cvt_pk_bf16_f32 v23, v74, v75\n" \
  "v_mfma_f32_16x16x32_bf16 v[116:119], a[88:91], v[36:39], v[116:119]\n" \
  "buffer_store_dwordx4 v[20:23], v5, s[40:43], s69 offen offset:1024 sc1\n" \
  "v_mfma_f32_16x16x32_bf16 v[108:111], a[168:171], v[32:35], v[108:111]\n" \
  "v_cvt_pk_bf16_f32 v24, v64, v65\n" \
  "v_mfma_f32_16x16x32_bf16 v[120:123], a[168:171], v[36:39], v[120:123]\n" \
  "v_cvt_pk_bf16_f32 v25, v66, v67\n" \
  "s_waitcnt lgkmcnt(2)\n" \
  "v_mfma_f32_16x16x32_bf16 v[100:103], a[12:15], v[40:43], v[100:103]\n" \
  "ds_read_b128 v[32:35], v10 offset:5120\n" \
  "v_mfma_f32_16x16x32_bf16 v[112:115], a[12:15], v[44:47], v[112:115]\n" \
  "ds_read_b128 v[36:39], v10 offset:25600\n" \
  "v_mfma_f32_16x16x32_bf16 v[104:107], a[92:95], v[40:43], v[104:107]\n" \
  "v_cvt_pk_bf16_f32 v26, v76, v77\n" \
  "v_mfma_f32_16x16x32_bf16 v[116:119], a[92:95], v[44:47], v[116:119]\n" \
  "v_cvt_pk_bf16_f32 v27, v78, v79\n" \
  "v_mfma_f32_16x16x32_bf16 v[108:111], a[172:175], v[40:43], v[108:111]\n" \
  "buffer_store_dwordx4 v[24:27], v5, s[40:43], s69 offen offset:2048 sc1\n" \
  "v_mfma_f32_16x16x32_bf16 v[120:123], a[172:175], v[44:47], v[120:123]\n" \
  "s_waitcnt lgkmcnt(2)\n" \
  "v_mfma_f32_16x16x32_bf16 v[100:103], a[16:19], v[48:51], v[100:103]\n" \
  "ds_read_b128 v[40:43], v10 offset:6144\n" \
  "v_mfma_f32_16x16x32_bf16 v[112:115], a[16:19], v[52:55], v[112:115]\n" \
  "ds_read_b128 v[44:47], v10 offset:26624\n" \
  "v_mfma_f32_16x16x32_bf16 v[104:107], a[96:99], v[48:51], v[104:107]\n" \
  "buffer_store_dwordx4 v[16:19], v2, s[40:43], s69 offen offset:0 sc1\n" \
  "v_mfma_f32_16x16x32_bf16 v[116:119], a[96:99], v[52:55], v[116:119]\n" \
  "v_mfma_f32_16x16x32_bf16 v[108:111], a[176:179], v[48:51], v[108:111]\n" \
  "v_mfma_f32_16x16x32_bf16 v[120:123], a[176:179], v[52:55], v[120:123]\n" \
  "s_waitcnt lgkmcnt(2)\n" \
  "v_mfma_f32_16x16x32_bf16 v[100:103], a[20:23], v[32:35], v[100:103]\n" \
  "ds_read_b128 v[48:51], v10 offset:7168\n" \
  "v_mfma_f32_16x16x32_bf16 v[112:115], a[20:23], v[36:39], v[112:115]\n" \
  "ds_read_b128 v[52:55], v10 offset:27648\n" \
  "v_mfma_f32_16x16x32_bf16 v[104:107], a[100:103], v[32:35], v[104:107]\n" \
  "buffer_store_dwordx4 v[20:23], v2, s[40:43], s69 offen offset:1024 sc1\n" \
  "v_mfma_f32_16x16x32_bf16 v[116:119], a[100:103], v[36:39], v[116:119]\n" \
  "v_mfma_f32_16x16x32_bf16 v[108:111], a[180:183], v[32:35], v[108:111]\n" \
  "v_mfma_f32_16x16x32_bf16 v[120:123], a[180:183], v[36:39], v[120:123]\n" \
  "s_waitcnt lgkmcnt(2)\n" \
  "v_mfma_f32_16x16x32_bf16 v[100:103], a[24:27], v[40:43], v[100:103]\n" \
  "ds_read_b128 v[32:35], v10 offset:8192\n" \
  "v_mfma_f32_16x16x32_bf16 v[112:115], a[24:27], v[44:47], v[112:115]\n" \
  "ds_read_b128 v[36:39], v10 offset:28672\n" \
  "v_mfma_f32_16x16x32_bf16 v[104:107], a[104:107], v[40:43], v[104:107]\n" \
  "buffer_store_dwordx4 v[24:27], v2, s[40:43], s69 offen offset:2048 sc1\n" \
  "v_mfma_f32_16x16x32_bf16 v[116:119], a[104:107], v[44:47], v[116:119]\n" \
  "s_nop 0\n" \
  "v_mfma_f32_16x16x32_bf16 v[108:111], a[184:187], v[40:43], v[108:111]\n" \
  "s_nop 0\n" \
  "v_mfma_f32_16x16x32_bf16 v[120:123], a[184:187], v[44:47], v[120:123]\n" \
  "s_waitcnt lgkmcnt(2)\n" \
  "v_mfma_f32_16x16x32_bf16 v[100:103], a[28:31], v[48:51], v[100:103]\n" \
  "ds_read_b128 v[40:43], v10 offset:9216\n" \
  "v_mfma_f32_16x16x32_bf16 v[112:115], a[28:31], v[52:55], v[112:115]\n" \
  "ds_read_b128 v[44:47], v10 offset:29696\n" \
  "v_mfma_f32_16x16x32_bf16 v[104:107], a[108:111], v[48:51], v[104:107]\n" \
  "s_add_u32 s70, s68, 4096\n" \
  "v_mfma_f32_16x16x32_bf16 v[116:119], a[108:111], v[52:55], v[116:119]\n" \
  "s_add_u32 s71, s68, 8192\n" \
  "v_mfma_f32_16x16x32_bf16 v[108:111], a[188:191], v[48:51], v[108:111]\n" \
  "s_add_u32 m0, s53, 40960\n" \
  "v_mfma_f32_16x16x32_bf16 v[120:123], a[188:191], v[52:55], v[120:123]\n" \
  "buffer_load_dwordx4 v1, s[36:39], s68 offen sc1 lds\n" \
  "s_waitcnt lgkmcnt(2)\n" \
  "v_mfma_f32_16x16x32_bf16 v[100:103], a[32:35], v[32:35], v[100:103]\n" \
  "ds_read_b128 v[48:51], v10 offset:10240\n" \
  "v_mfma_f32_16x16x32_bf16 v[112:115], a[32:35], v[36:39], v[112:115]\n" \
  "ds_read_b128 v[52:55], v10 offset:30720\n" \
  "v_mfma_f32_16x16x32_bf16 v[104:107], a[112:115], v[32:35], v[104:107]\n" \
  "buffer_load_dwordx4 v1, s[36:39], s68 offen offset:1024 sc1 lds\n" \
  "v_mfma_f32_16x16x32_bf16 v[116:119], a[112:115], v[36:39], v[116:119]\n" \
  "v_mfma_f32_16x16x32_bf16 v[108:111], a[192:195], v[32:35], v[108:111]\n" \
  "v_mfma_f32_16x16x32_bf16 v[120:123], a[192:195], v[36:39], v[120:123]\n" \
  "s_waitcnt lgkmcnt(2)\n" \
  "v_mfma_f32_16x16x32_bf16 v[100:103], a[36:39], v[40:43], v[100:103]\n" \
  "ds_read_b128 v[32:35], v10 offset:11264\n" \
  "v_mfma_f32_16x16x32_bf16 v[112:115], a[36:39], v[44:47], v[112:115]\n" \
  "ds_read_b128 v[36:39], v10 offset:31744\n" \
  "v_mfma_f32_16x16x32_bf16 v[104:107], a[116:119], v[40:43], v[104:107]\n" \
  "buffer_load_dwordx4 v1, s[36:39], s68 offen offset:2048 sc1 lds\n" \
  "v_mfma_f32_16x16x32_bf16 v[116:119], a[116:119], v[44:47], v[116:119]\n" \
  "v_mfma_f32_16x16x32_bf16 v[108:111], a[196:199], v[40:43], v[108:111]\n" \
  "v_mfma_f32_16x16x32_bf16 v[120:123], a[196:199], v[44:47], v[120:123]\n" \
  "s_waitcnt lgkmcnt(2)\n" \
  "v_mfma_f32_16x16x32_bf16 v[100:103], a[40:43], v[48:51], v[100:103]\n" \
  "ds_read_b128 v[40:43], v10 offset:12288\n" \
  "v_mfma_f32_16x16x32_bf16 v[112:115], a[40:43], v[52:55], v[112:115]\n" \
  "ds_read_b128 v[44:47], v10 offset:32768\n" \
  "v_mfma_f32_16x16x32_bf16 v[104:107], a[120:123], v[48:51], v[104:107]\n" \
  "buffer_load_dwordx4 v1, s[36:39], s68 offen offset:3072 sc1 lds\n" \
  "v_mfma_f32_16x16x32_bf16 v[116:119], a[120:123], v[52:55], v[116:119]\n" \
  "v_mfma_f32_16x16x32_bf16 v[108:111], a[200:203], v[48:51], v[108:111]\n" \
  "v_mfma_f32_16x16x32_bf16 v[120:123], a[200:203], v[52:55], v[120:123]\n" \
  "s_waitcnt lgkmcnt(2)\n" \
  "v_mfma_f32_16x16x32_bf16 v[100:103], a[44:47], v[32:35], v[100:103]\n" \
  "ds_read_b128 v[48:51], v10 offset:13312\n" \
  "v_mfma_f32_16x16x32_bf16 v[112:115], a[44:47], v[36:39], v[112:115]\n" \
  "ds_read_b128 v[52:55], v10 offset:33792\n" \
  "v_mfma_f32_16x16x32_bf16 v[104:107], a[124:127], v[32:35], v[104:107]\n" \
  "s_add_u32 m0, s53, 45056\n" \
  "v_mfma_f32_16x16x32_bf16 v[116:119], a[124:127], v[36:39], v[116:119]\n" \
  "buffer_load_dwordx4 v1, s[36:39], s70 offen sc1 lds\n" \
  "v_mfma_f32_16x16x32_bf16 v[108:111], a[204:207], v[32:35], v[108:111]\n" \
  "v_mfma_f32_16x16x32_bf16 v[120:123], a[204:207], v[36:39], v[120:123]\n" \
  "s_waitcnt lgkmcnt(2)\n" \
  "v_mfma_f32_16x16x32_bf16 v[100:103], a[48:51], v[40:43], v[100:103]\n" \
  "ds_read_b128 v[32:35], v10 offset:14336\n" \
  "v_mfma_f32_16x16x32_bf16 v[112:115], a[48:51], v[44:47], v[112:115]\n" \
  "ds_read_b128 v[36:39], v10 offset:34816\n" \
  "v_mfma_f32_16x16x32_bf16 v[104:107], a[128:131], v[40:43], v[104:107]\n" \
  "buffer_load_dwordx4 v1, s[36:39], s70 offen offset:1024 sc1 lds\n" \
  "v_mfma_f32_16x16x32_bf16 v[116:119], a[128:131], v[44:47], v[116:119]\n" \
  "v_mfma_f32_16x16x32_bf16 v[108:111], a[208:211], v[40:43], v[108:111]\n" \
  "v_mfma_f32_16x16x32_bf16 v[120:123], a[208:211], v[44:47], v[120:123]\n" \
  "s_waitcnt vmcnt(6) lgkmcnt(2)\n" \
  "s_barrier\n" \
  "v_mfma_f32_16x16x32_bf16 v[100:103], a[52:55], v[48:51], v[100:103]\n" \
  "ds_read_b128 v[40:43], v10 offset:15360\n" \
  "v_mfma_f32_16x16x32_bf16 v[112:115], a[52:55], v[52:55], v[112:115]\n" \
  "ds_read_b128 v[44:47], v10 offset:35840\n" \
  "v_mfma_f32_16x16x32_bf16 v[104:107], a[132:135], v[48:51], v[104:107]\n" \
  "buffer_atomic_add v11, v4, s[40:43], s54 offen\n" \
  "v_mfma_f32_16x16x32_bf16 v[116:119], a[132:135], v[52:55], v[116:119]\n" \
  "s_add_u32 s54, s54, s55\n" \
  "v_mfma_f32_16x16x32_bf16 v[108:111], a[212:215], v[48:51], v[108:111]\n" \
  "s_and_b32 s54, s54, 0xffffdfff\n" \
  "v_mfma_f32_16x16x32_bf16 v[120:123], a[212:215], v[52:55], v[120:123]\n" \
  "s_waitcnt lgkmcnt(2)\n" \
  "v_mfma_f32_16x16x32_bf16 v[100:103], a[56:59], v[32:35], v[100:103]\n" \
  "ds_read_b128 v[48:51], v10 offset:16384\n" \
  "v_mfma_f32_16x16x32_bf16 v[112:115], a[56:59], v[36:39], v[112:115]\n" \
  "ds_read_b128 v[52:55], v10 offset:36864\n" \
  "v_mfma_f32_16x16x32_bf16 v[104:107], a[136:139], v[32:35], v[104:107]\n" \
  "buffer_load_dwordx4 v1, s[36:39], s70 offen offset:2048 sc1 lds\n" \
  "v_mfma_f32_16x16x32_bf16 v[116:119], a[136:139], v[36:39], v[116:119]\n" \
  "v_readlane_b32 s69, v3, s72\n" \
  "v_mfma_f32_16x16x32_bf16 v[108:111], a[216:219], v[32:35], v[108:111]\n" \
  "s_add_u32 s72, s72, 1\n" \
  "v_mfma_f32_16x16x32_bf16 v[120:123], a[216:219], v[36:39], v[120:123]\n" \
  "s_waitcnt lgkmcnt(2)\n" \
  "v_mfma_f32_16x16x32_bf16 v[100:103], a[60:63], v[40:43], v[100:103]\n" \
  "ds_read_b128 v[32:35], v10 offset:17408\n" \
  "v_mfma_f32_16x16x32_bf16 v[112:115], a[60:63], v[44:47], v[112:115]\n" \
  "ds_read_b128 v[36:39], v10 offset:37888\n" \
  "v_mfma_f32_16x16x32_bf16 v[104:107], a[140:143], v[40:43], v[104:107]\n" \
  "buffer_load_dwordx4 v1, s[36:39], s70 offen offset:3072 sc1 lds\n" \
  "v_mfma_f32_16x16x32_bf16 v[116:119], a[140:143], v[44:47], v[116:119]\n" \
  "v_mfma_f32_16x16x32_bf16 v[108:111], a[220:223], v[40:43], v[108:111]\n" \
  "v_mfma_f32_16x16x32_bf16 v[120:123], a[220:223], v[44:47], v[120:123]\n" \
  "s_waitcnt lgkmcnt(2)\n" \
  "v_mfma_f32_16x16x32_bf16 v[100:103], a[64:67], v[48:51], v[100:103]\n" \
  "ds_read_b128 v[40:43], v10 offset:18432\n" \
  "v_mfma_f32_16x16x32_bf16 v[112:115], a[64:67], v[52:55], v[112:115]\n" \
  "ds_read_b128 v[44:47], v10 offset:38912\n" \
  "v_mfma_f32_16x16x32_bf16 v[104:107], a[144:147], v[48:51], v[104:107]\n" \
  "s_add_u32 m0, s53, 49152\n" \
  "v_mfma_f32_16x16x32_bf16 v[116:119], a[144:147], v[52:55], v[116:119]\n" \
  "buffer_load_dwordx4 v1, s[36:39], s71 offen sc1 lds\n" \
  "v_mfma_f32_16x16x32_bf16 v[108:111], a[224:227], v[48:51], v[108:111]\n" \
  "v_mfma_f32_16x16x32_bf16 v[120:123], a[224:227], v[52:55], v[120:123]\n" \
  "s_waitcnt lgkmcnt(2)\n" \
  "v_mfma_f32_16x16x32_bf16 v[100:103], a[68:71], v[32:35], v[100:103]\n" \
  "ds_read_b128 v[48:51], v10 offset:19456\n" \
  "v_mfma_f32_16x16x32_bf16 v[112:115], a[68:71], v[36:39], v[112:115]\n" \
  "ds_read_b128 v[52:55], v10 offset:39936\n" \
  "v_mfma_f32_16x16x32_bf16 v[104:107], a[148:151], v[32:35], v[104:107]\n" \
  "buffer_load_dwordx4 v1, s[36:39], s71 offen offset:1024 sc1 lds\n" \
  "v_mfma_f32_16x16x32_bf16 v[116:119], a[148:151], v[36:39], v[116:119]\n" \
  "s_add_u32 s68, s68, 40960\n" \
  "v_mfma_f32_16x16x32_bf16 v[108:111], a[228:231], v[32:35], v[108:111]\n" \
  "v_mfma_f32_16x16x32_bf16 v[120:123], a[228:231], v[36:39], v[120:123]\n" \
  "s_waitcnt vmcnt(11) lgkmcnt(2)\n" \
  "s_barrier\n" \
  "v_mfma_f32_16x16x32_bf16 v[100:103], a[72:75], v[40:43], v[100:103]\n" \
  "v_mfma_f32_16x16x32_bf16 v[112:115], a[72:75], v[44:47], v[112:115]\n" \
  "v_mfma_f32_16x16x32_bf16 v[104:107], a[152:155], v[40:43], v[104:107]\n" \
  "v_mfma_f32_16x16x32_bf16 v[116:119], a[152:155], v[44:47], v[116:119]\n" \
  "v_mfma_f32_16x16x32_bf16 v[108:111], a[232:235], v[40:43], v[108:111]\n" \
  "v_mfma_f32_16x16x32_bf16 v[120:123], a[232:235], v[44:47], v[120:123]\n" \
  "s_waitcnt lgkmcnt(0)\n" \
  "v_mfma_f32_16x16x32_bf16 v[100:103], a[76:79], v[48:51], v[100:103]\n" \
  "s_cmp_eq_u32 s72, 64\n" \
  "v_mfma_f32_16x16x32_bf16 v[112:115], a[76:79], v[52:55], v[112:115]\n" \
  "v_mfma_f32_16x16x32_bf16 v[104:107], a[156:159], v[48:51], v[104:107]\n" \
  "v_mfma_f32_16x16x32_bf16 v[116:119], a[156:159], v[52:55], v[116:119]\n" \
  "v_mfma_f32_16x16x32_bf16 v[108:111], a[236:239], v[48:51], v[108:111]\n" \
  "v_mfma_f32_16x16x32_bf16 v[120:123], a[236:239], v[52:55], v[120:123]\n" \
  "LPE_%=:\n" \
  "s_nop 6\n" \
  "v_cvt_pk_bf16_f32 v16, v100, v101\n" \
  "v_cvt_pk_bf16_f32 v17, v102, v103\n" \
  "v_cvt_pk_bf16_f32 v18, v104, v105\n" \
  "v_cvt_pk_bf16_f32 v19, v106, v107\n" \
  "buffer_store_dwordx4 v[16:19], v5, s[40:43], s69 offen offset:0 sc1\n" \
  "v_cvt_pk_bf16_f32 v20, v112, v113\n" \
  "v_cvt_pk_bf16_f32 v21, v114, v115\n" \
  "v_cvt_pk_bf16_f32 v22, v116, v117\n" \
  "v_cvt_pk_bf16_f32 v23, v118, v119\n" \
  "buffer_store_dwordx4 v[20:23], v5, s[40:43], s69 offen offset:1024 sc1\n" \
  "v_cvt_pk_bf16_f32 v24, v108, v109\n" \
  "v_cvt_pk_bf16_f32 v25, v110, v111\n" \
  "v_cvt_pk_bf16_f32 v26, v120, v121\n" \
  "v_cvt_pk_bf16_f32 v27, v122, v123\n" \
  "buffer_store_dwordx4 v[24:27], v5, s[40:43], s69 offen offset:2048 sc1\n" \
  "buffer_store_dwordx4 v[16:19], v2, s[40:43], s69 offen offset:0 sc1\n" \
  "buffer_store_dwordx4 v[20:23], v2, s[40:43], s69 offen offset:1024 sc1\n" \
  "buffer_store_dwordx4 v[24:27], v2, s[40:43], s69 offen offset:2048 sc1\n" \
  "s_nop 0\n" \
  "s_waitcnt vmcnt(0) lgkmcnt(0)\n" \
  "s_barrier\n" \
  "buffer_atomic_add v11, v4, s[40:43], s54 offen\n" \
  "s_add_u32 s54, s54, s55\n" \
  "s_and_b32 s54, s54, 0xffffdfff\n"
#define PROJV_CLOBBERS \
  "v9", "v10", "v11", "v12", "v13", "v14", "v15", "v16", "v17", "v18", "v19", "v20", "v21", "v22", "v23", "v24", "v25", "v26", "v27", "v28", "v29", \
  "v30", "v31", "v32", "v33", "v34", "v35", "v36", "v37", "v38", "v39", "v40", "v41", "v42", "v43", "v44", "v45", "v46", "v47", "v48", "v49", "v50", \
  "v51", "v52", "v53", "v54", "v55", "v56", "v57", "v58", "v59", "v60", "v61", "v62", "v63", "v64", "v65", "v66", "v67", "v68", "v69", "v70", "v71", \
  "v72", "v73", "v74", "v75", "v76", "v77", "v78", "v79", "v100", "v101", "v102", "v103", "v104", "v105", "v106", "v107", "v108", "v109", "v110", \
  "v111", "v112", "v113", "v114", "v115", "v116", "v117", "v118", "v119", "v120", "v121", "v122", "v123", "s68", "s69", "s70", "s71", "s72", "s73"
#define PROJV_ASM \
  "s_waitcnt vmcnt(10)\n" \
  "s_barrier\n" \
  "ds_read_b128 v[32:35], v0 offset:0\n" \
  "ds_read_b128 v[36:39], v0 offset:20480\n" \
  "ds_read_b128 v[40:43], v0 offset:1024\n" \
  "ds_read_b128 v[44:47], v0 offset:21504\n" \
  "v_mov_b32 v12, -1\n" \
  "v_mov_b32 v13, -1\n" \
  "v_mov_b32 v14, -1\n" \
  "v_mov_b32 v15, -1\n" \
  "v_mov_b32 v11, 1\n" \
  "v_add_u32 v9, 40960, v0\n" \
  "v_add_u32 v10, 81920, v0\n" \
  "s_mul_i32 s68, s52, 40960\n" \
  "s_add_u32 s68, s68, 81920\n" \
  "s_mov_b32 s72, s52\n" \
  "s_waitcnt lgkmcnt(2)\n" \
  "v_mfma_f32_16x16x32_bf16 v[56:59], a[0:3], v[32:35], 0\n" \
  "ds_read_b128 v[48:51], v0 offset:2048\n" \
  "v_mfma_f32_16x16x32_bf16 v[68:71], a[0:3], v[36:39], 0\n" \
  "ds_read_b128 v[52:55], v0 offset:22528\n" \
  "v_mfma_f32_16x16x32_bf16 v[60:63], a[80:83], v[32:35], 0\n" \
  "v_mfma_f32_16x16x32_bf16 v[72:75], a[80:83], v[36:39], 0\n" \
  "v_mfma_f32_16x16x32_bf16 v[64:67], a[160:163], v[32:35], 0\n" \
  "v_mfma_f32_16x16x32_bf16 v[76:79], a[160:163], v[36:39], 0\n" \
  "s_waitcnt lgkmcnt(2)\n" \
  "s_barrier\n" \
  "v_mfma_f32_16x16x32_bf16 v[56:59], a[4:7], v[40:43], v[56:59]\n" \
  "ds_read_b128 v[32:35], v0 offset:3072\n" \
  "v_mfma_f32_16x16x32_bf16 v[68:71], a[4:7], v[44:47], v[68:71]\n" \
  "ds_read_b128 v[36:39], v0 offset:23552\n" \
  "v_mfma_f32_16x16x32_bf16 v[60:63], a[84:87], v[40:43], v[60:63]\n" \
  "v_mfma_f32_16x16x32_bf16 v[72:75], a[84:87], v[44:47], v[72:75]\n" \
  "v_mfma_f32_16x16x32_bf16 v[64:67], a[164:167], v[40:43], v[64:67]\n" \
  "v_mfma_f32_16x16x32_bf16 v[76:79], a[164:167], v[44:47], v[76:79]\n" \
  "s_waitcnt lgkmcnt(2)\n" \
  "v_mfma_f32_16x16x32_bf16 v[56:59], a[8:11], v[48:51], v[56:59]\n" \
  "ds_read_b128 v[40:43], v0 offset:4096\n" \
  "v_mfma_f32_16x16x32_bf16 v[68:71], a[8:11], v[52:55], v[68:71]\n" \
  "ds_read_b128 v[44:47], v0 offset:24576\n" \
  "v_mfma_f32_16x16x32_bf16 v[60:63], a[88:91], v[48:51], v[60:63]\n" \
  "v_mfma_f32_16x16x32_bf16 v[72:75], a[88:91], v[52:55], v[72:75]\n" \
  "v_mfma_f32_16x16x32_bf16 v[64:67], a[168:171], v[48:51], v[64:67]\n" \
  "v_mfma_f32_16x16x32_bf16 v[76:79], a[168:171], v[52:55], v[76:79]\n" \
  "s_waitcnt lgkmcnt(2)\n" \
  "v_mfma_f32_16x16x32_bf16 v[56:59], a[12:15], v[32:35], v[56:59]\n" \
  "ds_read_b128 v[48:51], v0 offset:5120\n" \
  "v_mfma_f32_16x16x32_bf16 v[68:71], a[12:15], v[36:39], v[68:71]\n" \
  "ds_read_b128 v[52:55], v0 offset:25600\n" \
  "v_mfma_f32_16x16x32_bf16 v[60:63], a[92:95], v[32:35], v[60:63]\n" \
  "v_mfma_f32_16x16x32_bf16 v[72:75], a[92:95], v[36:39], v[72:75]\n" \
  "v_mfma_f32_16x16x32_bf16 v[64:67], a[172:175], v[32:35], v[64:67]\n" \
  "v_mfma_f32_16x16x32_bf16 v[76:79], a[172:175], v[36:39], v[76:79]\n" \
  "s_waitcnt lgkmcnt(2)\n" \
  "v_mfma_f32_16x16x32_bf16 v[56:59], a[16:19], v[40:43], v[56:59]\n" \
  "ds_read_b128 v[32:35], v0 offset:6144\n" \
  "v_mfma_f32_16x16x32_bf16 v[68:71], a[16:19], v[44:47], v[68:71]\n" \
  "ds_read_b128 v[36:39], v0 offset:26624\n" \
  "v_mfma_f32_16x16x32_bf16 v[60:63], a[96:99], v[40:43], v[60:63]\n" \
  "v_mfma_f32_16x16x32_bf16 v[72:75], a[96:99], v[44:47], v[72:75]\n" \
  "v_mfma_f32_16x16x32_bf16 v[64:67], a[176:179], v[40:43], v[64:67]\n" \
  "v_mfma_f32_16x16x32_bf16 v[76:79], a[176:179], v[44:47], v[76:79]\n" \
  "s_waitcnt lgkmcnt(2)\n" \
  "v_mfma_f32_16x16x32_bf16 v[56:59], a[20:23], v[48:51], v[56:59]\n" \
  "ds_read_b128 v[40:43], v0 offset:7168\n" \
  "v_mfma_f32_16x16x32_bf16 v[68:71], a[20:23], v[52:55], v[68:71]\n" \
  "ds_read_b128 v[44:47], v0 offset:27648\n" \
  "v_mfma_f32_16x16x32_bf16 v[60:63], a[100:103], v[48:51], v[60:63]\n" \
  "v_mfma_f32_16x16x32_bf16 v[72:75], a[100:103], v[52:55], v[72:75]\n" \
  "v_mfma_f32_16x16x32_bf16 v[64:67], a[180:183], v[48:51], v[64:67]\n" \
  "v_mfma_f32_16x16x32_bf16 v[76:79], a[180:183], v[52:55], v[76:79]\n" \
  "s_waitcnt lgkmcnt(2)\n" \
  "v_mfma_f32_16x16x32_bf16 v[56:59], a[24:27], v[32:35], v[56:59]\n" \
  "ds_read_b128 v[48:51], v0 offset:8192\n" \
  "v_mfma_f32_16x16x32_bf16 v[68:71], a[24:27], v[36:39], v[68:71]\n" \
  "ds_read_b128 v[52:55], v0 offset:28672\n" \
  "v_mfma_f32_16x16x32_bf16 v[60:63], a[104:107], v[32:35], v[60:63]\n" \
  "v_mfma_f32_16x16x32_bf16 v[72:75], a[104:107], v[36:39], v[72:75]\n" \
  "v_mfma_f32_16x16x32_bf16 v[64:67], a[184:187], v[32:35], v[64:67]\n" \
  "v_mfma_f32_16x16x32_bf16 v[76:79], a[184:187], v[36:39], v[76:79]\n" \
  "s_waitcnt lgkmcnt(2)\n" \
  "v_mfma_f32_16x16x32_bf16 v[56:59], a[28:31], v[40:43], v[56:59]\n" \
  "ds_read_b128 v[32:35], v0 offset:9216\n" \
  "v_mfma_f32_16x16x32_bf16 v[68:71], a[28:31], v[44:47], v[68:71]\n" \
  "ds_read_b128 v[36:39], v0 offset:29696\n" \
  "v_mfma_f32_16x16x32_bf16 v[60:63], a[108:111], v[40:43], v[60:63]\n" \
  "s_add_u32 s70, s68, 4096\n" \
  "v_mfma_f32_16x16x32_bf16 v[72:75], a[108:111], v[44:47], v[72:75]\n" \
  "s_add_u32 s71, s68, 8192\n" \
  "v_mfma_f32_16x16x32_bf16 v[64:67], a[188:191], v[40:43], v[64:67]\n" \
  "s_add_u32 m0, s53, 81920\n" \
  "v_mfma_f32_16x16x32_bf16 v[76:79], a[188:191], v[44:47], v[76:79]\n" \
  "buffer_load_dwordx4 v1, s[36:39], s68 offen sc1 lds\n" \
  "s_waitcnt lgkmcnt(2)\n" \
  "v_mfma_f32_16x16x32_bf16 v[56:59], a[32:35], v[48:51], v[56:59]\n" \
  "ds_read_b128 v[40:43], v0 offset:10240\n" \
  "v_mfma_f32_16x16x32_bf16 v[68:71], a[32:35], v[52:55], v[68:71]\n" \
  "ds_read_b128 v[44:47], v0 offset:30720\n" \
  "v_mfma_f32_16x16x32_bf16 v[60:63], a[112:115], v[48:51], v[60:63]\n" \
  "buffer_load_dwordx4 v1, s[36:39], s68 offen offset:1024 sc1 lds\n" \
  "v_mfma_f32_16x16x32_bf16 v[72:75], a[112:115], v[52:55], v[72:75]\n" \
  "v_mfma_f32_16x16x32_bf16 v[64:67], a[192:195], v[48:51], v[64:67]\n" \
  "v_mfma_f32_16x16x32_bf16 v[76:79], a[192:195], v[52:55], v[76:79]\n" \
  "s_waitcnt lgkmcnt(2)\n" \
  "v_mfma_f32_16x16x32_bf16 v[56:59], a[36:39], v[32:35], v[56:59]\n" \
  "ds_read_b128 v[48:51], v0 offset:11264\n" \
  "v_mfma_f32_16x16x32_bf16 v[68:71], a[36:39], v[36:39], v[68:71]\n" \
  "ds_read_b128 v[52:55], v0 offset:31744\n" \
  "v_mfma_f32_16x16x32_bf16 v[60:63], a[116:119], v[32:35], v[60:63]\n" \
  "buffer_load_dwordx4 v1, s[36:39], s68 offen offset:2048 sc1 lds\n" \
  "v_mfma_f32_16x16x32_bf16 v[72:75], a[116:119], v[36:39], v[72:75]\n" \
  "v_mfma_f32_16x16x32_bf16 v[64:67], a[196:199], v[32:35], v[64:67]\n" \
  "v_mfma_f32_16x16x32_bf16 v[76:79], a[196:199], v[36:39], v[76:79]\n" \
  "s_waitcnt lgkmcnt(2)\n" \
  "v_mfma_f32_16x16x32_bf16 v[56:59], a[40:43], v[40:43], v[56:59]\n" \
  "ds_read_b128 v[32:35], v0 offset:12288\n" \
  "v_mfma_f32_16x16x32_bf16 v[68:71], a[40:43], v[44:47], v[68:71]\n" \
  "ds_read_b128 v[36:39], v0 offset:32768\n" \
  "v_mfma_f32_16x16x32_bf16 v[60:63], a[120:123], v[40:43], v[60:63]\n" \
  "buffer_load_dwordx4 v1, s[36:39], s68 offen offset:3072 sc1 lds\n" \
  "v_mfma_f32_16x16x32_bf16 v[72:75], a[120:123], v[44:47], v[72:75]\n" \
  "v_mfma_f32_16x16x32_bf16 v[64:67], a[200:203], v[40:43], v[64:67]\n" \
  "v_mfma_f32_16x16x32_bf16 v[76:79], a[200:203], v[44:47], v[76:79]\n" \
  "s_waitcnt lgkmcnt(2)\n" \
  "v_mfma_f32_16x16x32_bf16 v[56:59], a[44:47], v[48:51], v[56:59]\n" \
  "ds_read_b128 v[40:43], v0 offset:13312\n" \
  "v_mfma_f32_16x16x32_bf16 v[68:71], a[44:47], v[52:55], v[68:71]\n" \
  "ds_read_b128 v[44:47], v0 offset:33792\n" \
  "v_mfma_f32_16x16x32_bf16 v[60:63], a[124:127], v[48:51], v[60:63]\n" \
  "s_add_u32 m0, s53, 86016\n" \
  "v_mfma_f32_16x16x32_bf16 v[72:75], a[124:127], v[52:55], v[72:75]\n" \
  "buffer_load_dwordx4 v1, s[36:39], s70 offen sc1 lds\n" \
  "v_mfma_f32_16x16x32_bf16 v[64:67], a[204:207], v[48:51], v[64:67]\n" \
  "v_mfma_f32_16x16x32_bf16 v[76:79], a[204:207], v[52:55], v[76:79]\n" \
  "s_waitcnt lgkmcnt(2)\n" \
  "v_mfma_f32_16x16x32_bf16 v[56:59], a[48:51], v[32:35], v[56:59]\n" \
  "ds_read_b128 v[48:51], v0 offset:14336\n" \
  "v_mfma_f32_16x16x32_bf16 v[68:71], a[48:51], v[36:39], v[68:71]\n" \
  "ds_read_b128 v[52:55], v0 offset:34816\n" \
  "v_mfma_f32_16x16x32_bf16 v[60:63], a[128:131], v[32:35], v[60:63]\n" \
  "buffer_load_dwordx4 v1, s[36:39], s70 offen offset:1024 sc1 lds\n" \
  "v_mfma_f32_16x16x32_bf16 v[72:75], a[128:131], v[36:39], v[72:75]\n" \
  "v_mfma_f32_16x16x32_bf16 v[64:67], a[208:211], v[32:35], v[64:67]\n" \
  "v_mfma_f32_16x16x32_bf16 v[76:79], a[208:211], v[36:39], v[76:79]\n" \
  "s_waitcnt lgkmcnt(2)\n" \
  "v_mfma_f32_16x16x32_bf16 v[56:59], a[52:55], v[40:43], v[56:59]\n" \
  "ds_read_b128 v[32:35], v0 offset:15360\n" \
  "v_mfma_f32_16x16x32_bf16 v[68:71], a[52:55], v[44:47], v[68:71]\n" \
  "ds_read_b128 v[36:39], v0 offset:35840\n" \
  "v_mfma_f32_16x16x32_bf16 v[60:63], a[132:135], v[40:43], v[60:63]\n" \
  "v_mfma_f32_16x16x32_bf16 v[72:75], a[132:135], v[44:47], v[72:75]\n" \
  "v_mfma_f32_16x16x32_bf16 v[64:67], a[212:215], v[40:43], v[64:67]\n" \
  "v_mfma_f32_16x16x32_bf16 v[76:79], a[212:215], v[44:47], v[76:79]\n" \
  "s_waitcnt lgkmcnt(2)\n" \
  "v_mfma_f32_16x16x32_bf16 v[56:59], a[56:59], v[48:51], v[56:59]\n" \
  "ds_read_b128 v[40:43], v0 offset:16384\n" \
  "v_mfma_f32_16x16x32_bf16 v[68:71], a[56:59], v[52:55], v[68:71]\n" \
  "ds_read_b128 v[44:47], v0 offset:36864\n" \
  "v_mfma_f32_16x16x32_bf16 v[60:63], a[136:139], v[48:51], v[60:63]\n" \
  "buffer_load_dwordx4 v1, s[36:39], s70 offen offset:2048 sc1 lds\n" \
  "v_mfma_f32_16x16x32_bf16 v[72:75], a[136:139], v[52:55], v[72:75]\n" \
  "v_readlane_b32 s69, v3, s72\n" \
  "v_mfma_f32_16x16x32_bf16 v[64:67], a[216:219], v[48:51], v[64:67]\n" \
  "v_readlane_b32 s73, v6, s72\n" \
  "v_mfma_f32_16x16x32_bf16 v[76:79], a[216:219], v[52:55], v[76:79]\n" \
  "s_add_u32 s72, s72, 1\n" \
  "s_waitcnt lgkmcnt(2)\n" \
  "v_mfma_f32_16x16x32_bf16 v[56:59], a[60:63], v[32:35], v[56:59]\n" \
  "ds_read_b128 v[48:51], v0 offset:17408\n" \
  "v_mfma_f32_16x16x32_bf16 v[68:71], a[60:63], v[36:39], v[68:71]\n" \
  "ds_read_b128 v[52:55], v0 offset:37888\n" \
  "v_mfma_f32_16x16x32_bf16 v[60:63], a[140:143], v[32:35], v[60:63]\n" \
  "buffer_load_dwordx4 v1, s[36:39], s70 offen offset:3072 sc1 lds\n" \
  "v_mfma_f32_16x16x32_bf16 v[72:75], a[140:143], v[36:39], v[72:75]\n" \
  "v_mfma_f32_16x16x32_bf16 v[64:67], a[220:223], v[32:35], v[64:67]\n" \
  "v_mfma_f32_16x16x32_bf16 v[76:79], a[220:223], v[36:39], v[76:79]\n" \
  "s_waitcnt lgkmcnt(2)\n" \
  "v_mfma_f32_16x16x32_bf16 v[56:59], a[64:67], v[40:43], v[56:59]\n" \
  "ds_read_b128 v[32:35], v0 offset:18432\n" \
  "v_mfma_f32_16x16x32_bf16 v[68:71], a[64:67], v[44:47], v[68:71]\n" \
  "ds_read_b128 v[36:39], v0 offset:38912\n" \
  "v_mfma_f32_16x16x32_bf16 v[60:63], a[144:147], v[40:43], v[60:63]\n" \
  "s_add_u32 m0, s53, 90112\n" \
  "v_mfma_f32_16x16x32_bf16 v[72:75], a[144:147], v[44:47], v[72:75]\n" \
  "buffer_load_dwordx4 v1, s[36:39], s71 offen sc1 lds\n" \
  "v_mfma_f32_16x16x32_bf16 v[64:67], a[224:227], v[40:43], v[64:67]\n" \
  "v_mfma_f32_16x16x32_bf16 v[76:79], a[224:227], v[44:47], v[76:79]\n" \
  "s_waitcnt lgkmcnt(2)\n" \
  "v_mfma_f32_16x16x32_bf16 v[56:59], a[68:71], v[48:51], v[56:59]\n" \
  "ds_read_b128 v[40:43], v0 offset:19456\n" \
  "v_mfma_f32_16x16x32_bf16 v[68:71], a[68:71], v[52:55], v[68:71]\n" \
  "ds_read_b128 v[44:47], v0 offset:39936\n" \
  "v_mfma_f32_16x16x32_bf16 v[60:63], a[148:151], v[48:51], v[60:63]\n" \
  "buffer_load_dwordx4 v1, s[36:39], s71 offen offset:1024 sc1 lds\n" \
  "v_mfma_f32_16x16x32_bf16 v[72:75], a[148:151], v[52:55], v[72:75]\n" \
  "s_add_u32 s68, s68, 40960\n" \
  "v_mfma_f32_16x16x32_bf16 v[64:67], a[228:231], v[48:51], v[64:67]\n" \
  "v_mfma_f32_16x16x32_bf16 v[76:79], a[228:231], v[52:55], v[76:79]\n" \
  "s_waitcnt vmcnt(10) lgkmcnt(2)\n" \
  "s_barrier\n" \
  "v_mfma_f32_16x16x32_bf16 v[56:59], a[72:75], v[32:35], v[56:59]\n" \
  "ds_read_b128 v[48:51], v9 offset:0\n" \
  "v_mfma_f32_16x16x32_bf16 v[68:71], a[72:75], v[36:39], v[68:71]\n" \
  "ds_read_b128 v[52:55], v9 offset:20480\n" \
  "v_mfma_f32_16x16x32_bf16 v[60:63], a[152:155], v[32:35], v[60:63]\n" \
  "v_mfma_f32_16x16x32_bf16 v[72:75], a[152:155], v[36:39], v[72:75]\n" \
  "v_mfma_f32_16x16x32_bf16 v[64:67], a[232:235], v[32:35], v[64:67]\n" \
  "v_mfma_f32_16x16x32_bf16 v[76:79], a[232:235], v[36:39], v[76:79]\n" \
  "s_waitcnt lgkmcnt(2)\n" \
  "v_mfma_f32_16x16x32_bf16 v[56:59], a[76:79], v[40:43], v[56:59]\n" \
  "ds_read_b128 v[32:35], v9 offset:1024\n" \
  "v_mfma_f32_16x16x32_bf16 v[68:71], a[76:79], v[44:47], v[68:71]\n" \
  "ds_read_b128 v[36:39], v9 offset:21504\n" \
  "v_mfma_f32_16x16x32_bf16 v[60:63], a[156:159], v[40:43], v[60:63]\n" \
  "s_cmp_eq_u32 s72, 64\n" \
  "v_mfma_f32_16x16x32_bf16 v[72:75], a[156:159], v[44:47], v[72:75]\n" \
  "v_mfma_f32_16x16x32_bf16 v[64:67], a[236:239], v[40:43], v[64:67]\n" \
  "v_mfma_f32_16x16x32_bf16 v[76:79], a[236:239], v[44:47], v[76:79]\n" \
  "s_waitcnt lgkmcnt(2)\n" \
  "v_mfma_f32_16x16x32_bf16 v[100:103], a[0:3], v[48:51], 0\n" \
  "ds_read_b128 v[40:43], v9 offset:2048\n" \
  "v_mfma_f32_16x16x32_bf16 v[112:115], a[0:3], v[52:55], 0\n" \
  "ds_read_b128 v[44:47], v9 offset:22528\n" \
  "v_mfma_f32_16x16x32_bf16 v[104:107], a[80:83], v[48:51], 0\n" \
  "v_cvt_pk_bf16_f32 v16, v56, v57\n" \
  "v_mfma_f32_16x16x32_bf16 v[116:119], a[80:83], v[52:55], 0\n" \
  "v_cvt_pk_bf16_f32 v17, v58, v59\n" \
  "v_mfma_f32_16x16x32_bf16 v[108:111], a[160:163], v[48:51], 0\n" \
  "v_cvt_pk_bf16_f32 v18, v60, v61\n" \
  "v_mfma_f32_16x16x32_bf16 v[120:123], a[160:163], v[52:55], 0\n" \
  "v_cvt_pk_bf16_f32 v19, v62, v63\n" \
  "s_waitcnt lgkmcnt(2)\n" \
  "s_barrier\n" \
  "v_mfma_f32_16x16x32_bf16 v[100:103], a[4:7], v[32:35], v[100:103]\n" \
  "ds_read_b128 v[48:51], v9 offset:3072\n" \
  "v_mfma_f32_16x16x32_bf16 v[112:115], a[4:7], v[36:39], v[112:115]\n" \
  "ds_read_b128 v[52:55], v9 offset:23552\n" \
  "v_mfma_f32_16x16x32_bf16 v[104:107], a[84:87], v[32:35], v[104:107]\n" \
  "buffer_store_dwordx4 v[16:19], v2, s[40:43], s69 offen offset:0 sc1\n" \
  "v_mfma_f32_16x16x32_bf16 v[116:119], a[84:87], v[36:39], v[116:119]\n" \
  "v_cvt_pk_bf16_f32 v20, v68, v69\n" \
  "v_mfma_f32_16x16x32_bf16 v[108:111], a[164:167], v[32:35], v[108:111]\n" \
  "v_cvt_pk_bf16_f32 v21, v70, v71\n" \
  "v_mfma_f32_16x16x32_bf16 v[120:123], a[164:167], v[36:39], v[120:123]\n" \
  "v_cvt_pk_bf16_f32 v22, v72, v73\n" \
  "s_waitcnt lgkmcnt(2)\n" \
  "v_mfma_f32_16x16x32_bf16 v[100:103], a[8:11], v[40:43], v[100:103]\n" \
  "ds_read_b128 v[32:35], v9 offset:4096\n" \
  "v_mfma_f32_16x16x32_bf16 v[112:115], a[8:11], v[44:47], v[112:115]\n" \
  "ds_read_b128 v[36:39], v9 offset:24576\n" \
  "v_mfma_f32_16x16x32_bf16 v[104:107], a[88:91], v[40:43], v[104:107]\n" \
  "v_cvt_pk_bf16_f32 v23, v74, v75\n" \
  "v_mfma_f32_16x16x32_bf16 v[116:119], a[88:91], v[44:47], v[116:119]\n" \
  "buffer_store_dwordx4 v[20:23], v2, s[40:43], s69 offen offset:1024 sc1\n" \
  "v_mfma_f32_16x16x32_bf16 v[108:111], a[168:171], v[40:43], v[108:111]\n" \
  "v_cvt_pk_bf16_f32 v24, v64, v65\n" \
  "v_mfma_f32_16x16x32_bf16 v[120:123], a[168:171], v[44:47], v[120:123]\n" \
  "v_cvt_pk_bf16_f32 v25, v66, v67\n" \
  "s_waitcnt lgkmcnt(2)\n" \
  "v_mfma_f32_16x16x32_bf16 v[100:103], a[12:15], v[48:51], v[100:103]\n" \
  "ds_read_b128 v[40:43], v9 offset:5120\n" \
  "v_mfma_f32_16x16x32_bf16 v[112:115], a[12:15], v[52:55], v[112:115]\n" \
  "ds_read_b128 v[44:47], v9 offset:25600\n" \
  "v_mfma_f32_16x16x32_bf16 v[104:107], a[92:95], v[48:51], v[104:107]\n" \
  "v_cvt_pk_bf16_f32 v26, v76, v77\n" \
  "v_mfma_f32_16x16x32_bf16 v[116:119], a[92:95], v[52:55], v[116:119]\n" \
  "v_cvt_pk_bf16_f32 v27, v78, v79\n" \
  "v_mfma_f32_16x16x32_bf16 v[108:111], a[172:175], v[48:51], v[108:111]\n" \
  "buffer_store_dwordx4 v[24:27], v2, s[40:43], s69 offen offset:2048 sc1\n" \
  "v_mfma_f32_16x16x32_bf16 v[120:123], a[172:175], v[52:55], v[120:123]\n" \
  "s_waitcnt lgkmcnt(2)\n" \
  "v_mfma_f32_16x16x32_bf16 v[100:103], a[16:19], v[32:35], v[100:103]\n" \
  "ds_read_b128 v[48:51], v9 offset:6144\n" \
  "v_mfma_f32_16x16x32_bf16 v[112:115], a[16:19], v[36:39], v[112:115]\n" \
  "ds_read_b128 v[52:55], v9 offset:26624\n" \
  "v_mfma_f32_16x16x32_bf16 v[104:107], a[96:99], v[32:35], v[104:107]\n" \
  "buffer_store_dwordx4 v[12:15], v5, s[40:43], s73 offen offset:0 sc1\n" \
  "v_mfma_f32_16x16x32_bf16 v[116:119], a[96:99], v[36:39], v[116:119]\n" \
  "v_mfma_f32_16x16x32_bf16 v[108:111], a[176:179], v[32:35], v[108:111]\n" \
  "v_mfma_f32_16x16x32_bf16 v[120:123], a[176:179], v[36:39], v[120:123]\n" \
  "s_waitcnt lgkmcnt(2)\n" \
  "v_mfma_f32_16x16x32_bf16 v[100:103], a[20:23], v[40:43], v[100:103]\n" \
  "ds_read_b128 v[32:35], v9 offset:7168\n" \
  "v_mfma_f32_16x16x32_bf16 v[112:115], a[20:23], v[44:47], v[112:115]\n" \
  "ds_read_b128 v[36:39], v9 offset:27648\n" \
  "v_mfma_f32_16x16x32_bf16 v[104:107], a[100:103], v[40:43], v[104:107]\n" \
  "buffer_store_dwordx4 v[12:15], v5, s[40:43], s73 offen offset:1024 sc1\n" \
  "v_mfma_f32_16x16x32_bf16 v[116:119], a[100:103], v[44:47], v[116:119]\n" \
  "v_mfma_f32_16x16x32_bf16 v[108:111], a[180:183], v[40:43], v[108:111]\n" \
  "v_mfma_f32_16x16x32_bf16 v[120:123], a[180:183], v[44:47], v[120:123]\n" \
  "s_waitcnt lgkmcnt(2)\n" \
  "v_mfma_f32_16x16x32_bf16 v[100:103], a[24:27], v[48:51], v[100:103]\n" \
  "ds_read_b128 v[40:43], v9 offset:8192\n" \
  "v_mfma_f32_16x16x32_bf16 v[112:115], a[24:27], v[52:55], v[112:115]\n" \
  "ds_read_b128 v[44:47], v9 offset:28672\n" \
  "v_mfma_f32_16x16x32_bf16 v[104:107], a[104:107], v[48:51], v[104:107]\n" \
  "buffer_store_dwordx4 v[12:15], v5, s[40:43], s73 offen offset:2048 sc1\n" \
  "v_mfma_f32_16x16x32_bf16 v[116:119], a[104:107], v[52:55], v[116:119]\n" \
  "s_nop 0\n" \
  "v_mfma_f32_16x16x32_bf16 v[108:111], a[184:187], v[48:51], v[108:111]\n" \
  "buffer_store_dwordx4 v[12:15], v5, s[40:43], s73 offen offset:3072 sc1\n" \
  "v_mfma_f32_16x16x32_bf16 v[120:123], a[184:187], v[52:55], v[120:123]\n" \
  "s_waitcnt lgkmcnt(2)\n" \
  "v_mfma_f32_16x16x32_bf16 v[100:103], a[28:31], v[32:35], v[100:103]\n" \
  "ds_read_b128 v[48:51], v9 offset:9216\n" \
  "v_mfma_f32_16x16x32_bf16 v[112:115], a[28:31], v[36:39], v[112:115]\n" \
  "ds_read_b128 v[52:55], v9 offset:29696\n" \
  "v_mfma_f32_16x16x32_bf16 v[104:107], a[108:111], v[32:35], v[104:107]\n" \
  "s_add_u32 s70, s68, 4096\n" \
  "v_mfma_f32_16x16x32_bf16 v[116:119], a[108:111], v[36:39], v[116:119]\n" \
  "s_add_u32 s71, s68, 8192\n" \
  "v_mfma_f32_16x16x32_bf16 v[108:111], a[188:191], v[32:35], v[108:111]\n" \
  "s_add_u32 m0, s53, 0\n" \
  "v_mfma_f32_16x16x32_bf16 v[120:123], a[188:191], v[36:39], v[120:123]\n" \
  "buffer_load_dwordx4 v1, s[36:39], s68 offen sc1 lds\n" \
  "s_waitcnt lgkmcnt(2)\n" \
  "v_mfma_f32_16x16x32_bf16 v[100:103], a[32:35], v[40:43], v[100:103]\n" \
  "ds_read_b128 v[32:35], v9 offset:10240\n" \
  "v_mfma_f32_16x16x32_bf16 v[112:115], a[32:35], v[44:47], v[112:115]\n" \
  "ds_read_b128 v[36:39], v9 offset:30720\n" \
  "v_mfma_f32_16x16x32_bf16 v[104:107], a[112:115], v[40:43], v[104:107]\n" \
  "buffer_load_dwordx4 v1, s[36:39], s68 offen offset:1024 sc1 lds\n" \
  "v_mfma_f32_16x16x32_bf16 v[116:119], a[112:115], v[44:47], v[116:119]\n" \
  "v_mfma_f32_16x16x32_bf16 v[108:111], a[192:195], v[40:43], v[108:111]\n" \
  "v_mfma_f32_16x16x32_bf16 v[120:123], a[192:195], v[44:47], v[120:123]\n" \
  "s_waitcnt lgkmcnt(2)\n" \
  "v_mfma_f32_16x16x32_bf16 v[100:103], a[36:39], v[48:51], v[100:103]\n" \
  "ds_read_b128 v[40:43], v9 offset:11264\n" \
  "v_mfma_f32_16x16x32_bf16 v[112:115], a[36:39], v[52:55], v[112:115]\n" \
  "ds_read_b128 v[44:47], v9 offset:31744\n" \
  "v_mfma_f32_16x16x32_bf16 v[104:107], a[116:119], v[48:51], v[104:107]\n" \
  "buffer_load_dwordx4 v1, s[36:39], s68 offen offset:2048 sc1 lds\n" \
  "v_mfma_f32_16x16x32_bf16 v[116:119], a[116:119], v[52:55], v[116:119]\n" \
  "v_mfma_f32_16x16x32_bf16 v[108:111], a[196:199], v[48:51], v[108:111]\n" \
  "v_mfma_f32_16x16x32_bf16 v[120:123], a[196:199], v[52:55], v[120:123]\n" \
  "s_waitcnt lgkmcnt(2)\n" \
  "v_mfma_f32_16x16x32_bf16 v[100:103], a[40:43], v[32:35], v[100:103]\n" \
  "ds_read_b128 v[48:51], v9 offset:12288\n" \
  "v_mfma_f32_16x16x32_bf16 v[112:115], a[40:43], v[36:39], v[112:115]\n" \
  "ds_read_b128 v[52:55], v9 offset:32768\n" \
  "v_mfma_f32_16x16x32_bf16 v[104:107], a[120:123], v[32:35], v[104:107]\n" \
  "buffer_load_dwordx4 v1, s[36:39], s68 offen offset:3072 sc1 lds\n" \
  "v_mfma_f32_16x16x32_bf16 v[116:119], a[120:123], v[36:39], v[116:119]\n" \
  "v_mfma_f32_16x16x32_bf16 v[108:111], a[200:203], v[32:35], v[108:111]\n" \
  "v_mfma_f32_16x16x32_bf16 v[120:123], a[200:203], v[36:39], v[120:123]\n" \
  "s_waitcnt lgkmcnt(2)\n" \
  "v_mfma_f32_16x16x32_bf16 v[100:103], a[44:47], v[40:43], v[100:103]\n" \
  "ds_read_b128 v[32:35], v9 offset:13312\n" \
  "v_mfma_f32_16x16x32_bf16 v[112:115], a[44:47], v[44:47], v[112:115]\n" \
  "ds_read_b128 v[36:39], v9 offset:33792\n" \
  "v_mfma_f32_16x16x32_bf16 v[104:107], a[124:127], v[40:43], v[104:107]\n" \
  "s_add_u32 m0, s53, 4096\n" \
  "v_mfma_f32_16x16x32_bf16 v[116:119], a[124:127], v[44:47], v[116:119]\n" \
  "buffer_load_dwordx4 v1, s[36:39], s70 offen sc1 lds\n" \
  "v_mfma_f32_16x16x32_bf16 v[108:111], a[204:207], v[40:43], v[108:111]\n" \
  "v_mfma_f32_16x16x32_bf16 v[120:123], a[204:207], v[44:47], v[120:123]\n" \
  "s_waitcnt lgkmcnt(2)\n" \
  "v_mfma_f32_16x16x32_bf16 v[100:103], a[48:51], v[48:51], v[100:103]\n" \
  "ds_read_b128 v[40:43], v9 offset:14336\n" \
  "v_mfma_f32_16x16x32_bf16 v[112:115], a[48:51], v[52:55], v[112:115]\n" \
  "ds_read_b128 v[44:47], v9 offset:34816\n" \
  "v_mfma_f32_16x16x32_bf16 v[104:107], a[128:131], v[48:51], v[104:107]\n" \
  "buffer_load_dwordx4 v1, s[36:39], s70 offen offset:1024 sc1 lds\n" \
  "v_mfma_f32_16x16x32_bf16 v[116:119], a[128:131], v[52:55], v[116:119]\n" \
  "v_mfma_f32_16x16x32_bf16 v[108:111], a[208:211], v[48:51], v[108:111]\n" \
  "v_mfma_f32_16x16x32_bf16 v[120:123], a[208:211], v[52:55], v[120:123]\n" \
  "s_waitcnt vmcnt(6) lgkmcnt(2)\n" \
  "s_barrier\n" \
  "v_mfma_f32_16x16x32_bf16 v[100:103], a[52:55], v[32:35], v[100:103]\n" \
  "ds_read_b128 v[48:51], v9 offset:15360\n" \
  "v_mfma_f32_16x16x32_bf16 v[112:115], a[52:55], v[36:39], v[112:115]\n" \
  "ds_read_b128 v[52:55], v9 offset:35840\n" \
  "v_mfma_f32_16x16x32_bf16 v[104:107], a[132:135], v[32:35], v[104:107]\n" \
  "buffer_atomic_add v11, v4, s[40:43], s54 offen\n" \
  "v_mfma_f32_16x16x32_bf16 v[116:119], a[132:135], v[36:39], v[116:119]\n" \
  "s_add_u32 s54, s54, s55\n" \
  "v_mfma_f32_16x16x32_bf16 v[108:111], a[212:215], v[32:35], v[108:111]\n" \
  "s_and_b32 s54, s54, 0xffffdfff\n" \
  "v_mfma_f32_16x16x32_bf16 v[120:123], a[212:215], v[36:39], v[120:123]\n" \
  "s_waitcnt lgkmcnt(2)\n" \
  "v_mfma_f32_16x16x32_bf16 v[100:103], a[56:59], v[40:43], v[100:103]\n" \
  "ds_read_b128 v[32:35], v9 offset:16384\n" \
  "v_mfma_f32_16x16x32_bf16 v[112:115], a[56:59], v[44:47], v[112:115]\n" \
  "ds_read_b128 v[36:39], v9 offset:36864\n" \
  "v_mfma_f32_16x16x32_bf16 v[104:107], a[136:139], v[40:43], v[104:107]\n" \
  "buffer_load_dwordx4 v1, s[36:39], s70 offen offset:2048 sc1 lds\n" \
  "v_mfma_f32_16x16x32_bf16 v[116:119], a[136:139], v[44:47], v[116:119]\n" \
  "v_readlane_b32 s69, v3, s72\n" \
  "v_mfma_f32_16x16x32_bf16 v[108:111], a[216:219], v[40:43], v[108:111]\n" \
  "v_readlane_b32 s73, v6, s72\n" \
  "v_mfma_f32_16x16x32_bf16 v[120:123], a[216:219], v[44:47], v[120:123]\n" \
  "s_add_u32 s72, s72, 1\n" \
  "s_waitcnt lgkmcnt(2)\n" \
  "v_mfma_f32_16x16x32_bf16 v[100:103], a[60:63], v[48:51], v[100:103]\n" \
  "ds_read_b128 v[40:43], v9 offset:17408\n" \
  "v_mfma_f32_16x16x32_bf16 v[112:115], a[60:63], v[52:55], v[112:115]\n" \
  "ds_read_b128 v[44:47], v9 offset:37888\n" \
  "v_mfma_f32_16x16x32_bf16 v[104:107], a[140:143], v[48:51], v[104:107]\n" \
  "buffer_load_dwordx4 v1, s[36:39], s70 offen offset:3072 sc1 lds\n" \
  "v_mfma_f32_16x16x32_bf16 v[116:119], a[140:143], v[52:55], v[116:119]\n" \
  "v_mfma_f32_16x16x32_bf16 v[108:111], a[220:223], v[48:51], v[108:111]\n" \
  "v_mfma_f32_16x16x32_bf16 v[120:123], a[220:223], v[52:55], v[120:123]\n" \
  "s_waitcnt lgkmcnt(2)\n" \
  "v_mfma_f32_16x16x32_bf16 v[100:103], a[64:67], v[32:35], v[100:103]\n" \
  "ds_read_b128 v[48:51], v9 offset:18432\n" \
  "v_mfma_f32_16x16x32_bf16 v[112:115], a[64:67], v[36:39], v[112:115]\n" \
  "ds_read_b128 v[52:55], v9 offset:38912\n" \
  "v_mfma_f32_16x16x32_bf16 v[104:107], a[144:147], v[32:35], v[104:107]\n" \
  "s_add_u32 m0, s53, 8192\n" \
  "v_mfma_f32_16x16x32_bf16 v[116:119], a[144:147], v[36:39], v[116:119]\n" \
  "buffer_load_dwordx4 v1, s[36:39], s71 offen sc1 lds\n" \
  "v_mfma_f32_16x16x32_bf16 v[108:111], a[224:227], v[32:35], v[108:111]\n" \
  "v_mfma_f32_16x16x32_bf16 v[120:123], a[224:227], v[36:39], v[120:123]\n" \
  "s_waitcnt lgkmcnt(2)\n" \
  "v_mfma_f32_16x16x32_bf16 v[100:103], a[68:71], v[40:43], v[100:103]\n" \
  "ds_read_b128 v[32:35], v9 offset:19456\n" \
  "v_mfma_f32_16x16x32_bf16 v[112:115], a[68:71], v[44:47], v[112:115]\n" \
  "ds_read_b128 v[36:39], v9 offset:39936\n" \
  "v_mfma_f32_16x16x32_bf16 v[104:107], a[148:151], v[40:43], v[104:107]\n" \
  "buffer_load_dwordx4 v1, s[36:39], s71 offen offset:1024 sc1 lds\n" \
  "v_mfma_f32_16x16x32_bf16 v[116:119], a[148:151], v[44:47], v[116:119]\n" \
  "s_add_u32 s68, s68, 40960\n" \
  "v_mfma_f32_16x16x32_bf16 v[108:111], a[228:231], v[40:43], v[108:111]\n" \
  "v_mfma_f32_16x16x32_bf16 v[120:123], a[228:231], v[44:47], v[120:123]\n" \
  "s_waitcnt vmcnt(11) lgkmcnt(2)\n" \
  "s_barrier\n" \
  "v_mfma_f32_16x16x32_bf16 v[100:103], a[72:75], v[48:51], v[100:103]\n" \
  "ds_read_b128 v[40:43], v10 offset:0\n" \
  "v_mfma_f32_16x16x32_bf16 v[112:115], a[72:75], v[52:55], v[112:115]\n" \
  "ds_read_b128 v[44:47], v10 offset:20480\n" \
  "v_mfma_f32_16x16x32_bf16 v[104:107], a[152:155], v[48:51], v[104:107]\n" \
  "v_mfma_f32_16x16x32_bf16 v[116:119], a[152:155], v[52:55], v[116:119]\n" \
  "v_mfma_f32_16x16x32_bf16 v[108:111], a[232:235], v[48:51], v[108:111]\n" \
  "v_mfma_f32_16x16x32_bf16 v[120:123], a[232:235], v[52:55], v[120:123]\n" \
  "s_waitcnt lgkmcnt(2)\n" \
  "v_mfma_f32_16x16x32_bf16 v[100:103], a[76:79], v[32:35], v[100:103]\n" \
  "ds_read_b128 v[48:51], v10 offset:1024\n" \
  "v_mfma_f32_16x16x32_bf16 v[112:115], a[76:79], v[36:39], v[112:115]\n" \
  "ds_read_b128 v[52:55], v10 offset:21504\n" \
  "v_mfma_f32_16x16x32_bf16 v[104:107], a[156:159], v[32:35], v[104:107]\n" \
  "s_cmp_eq_u32 s72, 64\n" \
  "v_mfma_f32_16x16x32_bf16 v[116:119], a[156:159], v[36:39], v[116:119]\n" \
  "v_mfma_f32_16x16x32_bf16 v[108:111], a[236:239], v[32:35], v[108:111]\n" \
  "v_mfma_f32_16x16x32_bf16 v[120:123], a[236:239], v[36:39], v[120:123]\n" \
  "s_cbranch_scc1 LPE_%=\n" \
  "s_waitcnt lgkmcnt(2)\n" \
  "v_mfma_f32_16x16x32_bf16 v[56:59], a[0:3], v[40:43], 0\n" \
  "ds_read_b128 v[32:35], v10 offset:2048\n" \
  "v_mfma_f32_16x16x32_bf16 v[68:71], a[0:3], v[44:47], 0\n" \
  "ds_read_b128 v[36:39], v10 offset:22528\n" \
  "v_mfma_f32_16x16x32_bf16 v[60:63], a[80:83], v[40:43], 0\n" \
  "v_cvt_pk_bf16_f32 v16, v100, v101\n" \
  "v_mfma_f32_16x16x32_bf16 v[72:75], a[80:83], v[44:47], 0\n" \
  "v_cvt_pk_bf16_f32 v17, v102, v103\n" \
  "v_mfma_f32_16x16x32_bf16 v[64:67], a[160:163], v[40:43], 0\n" \
  "v_cvt_pk_bf16_f32 v18, v104, v105\n" \
  "v_mfma_f32_16x16x32_bf16 v[76:79], a[160:163], v[44:47], 0\n" \
  "v_cvt_pk_bf16_f32 v19, v106, v107\n" \
  "s_waitcnt lgkmcnt(2)\n" \
  "s_barrier\n" \
  "v_mfma_f32_16x16x32_bf16 v[56:59], a[4:7], v[48:51], v[56:59]\n" \
  "ds_read_b128 v[40:43], v10 offset:3072\n" \
  "v_mfma_f32_16x16x32_bf16 v[68:71], a[4:7], v[52:55], v[68:71]\n" \
  "ds_read_b128 v[44:47], v10 offset:23552\n" \
  "v_mfma_f32_16x16x32_bf16 v[60:63], a[84:87], v[48:51], v[60:63]\n" \
  "buffer_store_dwordx4 v[16:19], v2, s[40:43], s69 offen offset:0 sc1\n" \
  "v_mfma_f32_16x16x32_bf16 v[72:75], a[84:87], v[52:55], v[72:75]\n" \
  "v_cvt_pk_bf16_f32 v20, v112, v113\n" \
  "v_mfma_f32_16x16x32_bf16 v[64:67], a[164:167], v[48:51], v[64:67]\n" \
  "v_cvt_pk_bf16_f32 v21, v114, v115\n" \
  "v_mfma_f32_16x16x32_bf16 v[76:79], a[164:167], v[52:55], v[76:79]\n" \
  "v_cvt_pk_bf16_f32 v22, v116, v117\n" \
  "s_waitcnt lgkmcnt(2)\n" \
  "v_mfma_f32_16x16x32_bf16 v[56:59], a[8:11], v[32:35], v[56:59]\n" \
  "ds_read_b128 v[48:51], v10 offset:4096\n" \
  "v_mfma_f32_16x16x32_bf16 v[68:71], a[8:11], v[36:39], v[68:71]\n" \
  "ds_read_b128 v[52:55], v10 offset:24576\n" \
  "v_mfma_f32_16x16x32_bf16 v[60:63], a[88:91], v[32:35], v[60:63]\n" \
  "v_cvt_pk_bf16_f32 v23, v118, v119\n" \
  "v_mfma_f32_16x16x32_bf16 v[72:75], a[88:91], v[36:39], v[72:75]\n" \
  "buffer_store_dwordx4 v[20:23], v2, s[40:43], s69 offen offset:1024 sc1\n" \
  "v_mfma_f32_16x16x32_bf16 v[64:67], a[168:171], v[32:35], v[64:67]\n" \
  "v_cvt_pk_bf16_f32 v24, v108, v109\n" \
  "v_mfma_f32_16x16x32_bf16 v[76:79], a[168:171], v[36:39], v[76:79]\n" \
  "v_cvt_pk_bf16_f32 v25, v110, v111\n" \
  "s_waitcnt lgkmcnt(2)\n" \
  "v_mfma_f32_16x16x32_bf16 v[56:59], a[12:15], v[40:43], v[56:59]\n" \
  "ds_read_b128 v[32:35], v10 offset:5120\n" \
  "v_mfma_f32_16x16x32_bf16 v[68:71], a[12:15], v[44:47], v[68:71]\n" \
  "ds_read_b128 v[36:39], v10 offset:25600\n" \
  "v_mfma_f32_16x16x32_bf16 v[60:63], a[92:95], v[40:43], v[60:63]\n" \
  "v_cvt_pk_bf16_f32 v26, v120, v121\n" \
  "v_mfma_f32_16x16x32_bf16 v[72:75], a[92:95], v[44:47], v[72:75]\n" \
  "v_cvt_pk_bf16_f32 v27, v122, v123\n" \
  "v_mfma_f32_16x16x32_bf16 v[64:67], a[172:175], v[40:43], v[64:67]\n" \
  "buffer_store_dwordx4 v[24:27], v2, s[40:43], s69 offen offset:2048 sc1\n" \
  "v_mfma_f32_16x16x32_bf16 v[76:79], a[172:175], v[44:47], v[76:79]\n" \
  "s_waitcnt lgkmcnt(2)\n" \
  "v_mfma_f32_16x16x32_bf16 v[56:59], a[16:19], v[48:51], v[56:59]\n" \
  "ds_read_b128 v[40:43], v10 offset:6144\n" \
  "v_mfma_f32_16x16x32_bf16 v[68:71], a[16:19], v[52:55], v[68:71]\n" \
  "ds_read_b128 v[44:47], v10 offset:26624\n" \
  "v_mfma_f32_16x16x32_bf16 v[60:63], a[96:99], v[48:51], v[60:63]\n" \
  "buffer_store_dwordx4 v[12:15], v5, s[40:43], s73 offen offset:0 sc1\n" \
  "v_mfma_f32_16x16x32_bf16 v[72:75], a[96:99], v[52:55], v[72:75]\n" \
  "v_mfma_f32_16x16x32_bf16 v[64:67], a[176:179], v[48:51], v[64:67]\n" \
  "v_mfma_f32_16x16x32_bf16 v[76:79], a[176:179], v[52:55], v[76:79]\n" \
  "s_waitcnt lgkmcnt(2)\n" \
  "v_mfma_f32_16x16x32_bf16 v[56:59], a[20:23], v[32:35], v[56:59]\n" \
  "ds_read_b128 v[48:51], v10 offset:7168\n" \
  "v_mfma_f32_16x16x32_bf16 v[68:71], a[20:23], v[36:39], v[68:71]\n" \
  "ds_read_b128 v[52:55], v10 offset:27648\n" \
  "v_mfma_f32_16x16x32_bf16 v[60:63], a[100:103], v[32:35], v[60:63]\n" \
  "buffer_store_dwordx4 v[12:15], v5, s[40:43], s73 offen offset:1024 sc1\n" \
  "v_mfma_f32_16x16x32_bf16 v[72:75], a[100:103], v[36:39], v[72:75]\n" \
  "v_mfma_f32_16x16x32_bf16 v[64:67], a[180:183], v[32:35], v[64:67]\n" \
  "v_mfma_f32_16x16x32_bf16 v[76:79], a[180:183], v[36:39], v[76:79]\n" \
  "s_waitcnt lgkmcnt(2)\n" \
  "v_mfma_f32_16x16x32_bf16 v[56:59], a[24:27], v[40:43], v[56:59]\n" \
  "ds_read_b128 v[32:35], v10 offset:8192\n" \
  "v_mfma_f32_16x16x32_bf16 v[68:71], a[24:27], v[44:47], v[68:71]\n" \
  "ds_read_b128 v[36:39], v10 offset:28672\n" \
  "v_mfma_f32_16x16x32_bf16 v[60:63], a[104:107], v[40:43], v[60:63]\n" \
  "buffer_store_dwordx4 v[12:15], v5, s[40:43], s73 offen offset:2048 sc1\n" \
  "v_mfma_f32_16x16x32_bf16 v[72:75], a[104:107], v[44:47], v[72:75]\n" \
  "s_nop 0\n" \
  "v_mfma_f32_16x16x32_bf16 v[64:67], a[184:187], v[40:43], v[64:67]\n" \
  "buffer_store_dwordx4 v[12:15], v5, s[40:43], s73 offen offset:3072 sc1\n" \
  "v_mfma_f32_16x16x32_bf16 v[76:79], a[184:187], v[44:47], v[76:79]\n" \
  "s_waitcnt lgkmcnt(2)\n" \
  "v_mfma_f32_16x16x32_bf16 v[56:59], a[28:31], v[48:51], v[56:59]\n" \
  "ds_read_b128 v[40:43], v10 offset:9216\n" \
  "v_mfma_f32_16x16x32_bf16 v[68:71], a[28:31], v[52:55], v[68:71]\n" \
  "ds_read_b128 v[44:47], v10 offset:29696\n" \
  "v_mfma_f32_16x16x32_bf16 v[60:63], a[108:111], v[48:51], v[60:63]\n" \
  "s_add_u32 s70, s68, 4096\n" \
  "v_mfma_f32_16x16x32_bf16 v[72:75], a[108:111], v[52:55], v[72:75]\n" \
  "s_add_u32 s71, s68, 8192\n" \
  "v_mfma_f32_16x16x32_bf16 v[64:67], a[188:191], v[48:51], v[64:67]\n" \
  "s_add_u32 m0, s53, 40960\n" \
  "v_mfma_f32_16x16x32_bf16 v[76:79], a[188:191], v[52:55], v[76:79]\n" \
  "buffer_load_dwordx4 v1, s[36:39], s68 offen sc1 lds\n" \
  "s_waitcnt lgkmcnt(2)\n" \
  "v_mfma_f32_16x16x32_bf16 v[56:59], a[32:35], v[32:35], v[56:59]\n" \
  "ds_read_b128 v[48:51], v10 offset:10240\n" \
  "v_mfma_f32_16x16x32_bf16 v[68:71], a[32:35], v[36:39], v[68:71]\n" \
  "ds_read_b128 v[52:55], v10 offset:30720\n" \
  "v_mfma_f32_16x16x32_bf16 v[60:63], a[112:115], v[32:35], v[60:63]\n" \
  "buffer_load_dwordx4 v1, s[36:39], s68 offen offset:1024 sc1 lds\n" \
  "v_mfma_f32_16x16x32_bf16 v[72:75], a[112:115], v[36:39], v[72:75]\n" \
  "v_mfma_f32_16x16x32_bf16 v[64:67], a[192:195], v[32:35], v[64:67]\n" \
  "v_mfma_f32_16x16x32_bf16 v[76:79], a[192:195], v[36:39], v[76:79]\n" \
  "s_waitcnt lgkmcnt(2)\n" \
  "v_mfma_f32_16x16x32_bf16 v[56:59], a[36:39], v[40:43], v[56:59]\n" \
  "ds_read_b128 v[32:35], v10 offset:11264\n" \
  "v_mfma_f32_16x16x32_bf16 v[68:71], a[36:39], v[44:47], v[68:71]\n" \
  "ds_read_b128 v[36:39], v10 offset:31744\n" \
  "v_mfma_f32_16x16x32_bf16 v[60:63], a[116:119], v[40:43], v[60:63]\n" \
  "buffer_load_dwordx4 v1, s[36:39], s68 offen offset:2048 sc1 lds\n" \
  "v_mfma_f32_16x16x32_bf16 v[72:75], a[116:119], v[44:47], v[72:75]\n" \
  "v_mfma_f32_16x16x32_bf16 v[64:67], a[196:199], v[40:43], v[64:67]\n" \
  "v_mfma_f32_16x16x32_bf16 v[76:79], a[196:199], v[44:47], v[76:79]\n" \
  "s_waitcnt lgkmcnt(2)\n" \
  "v_mfma_f32_16x16x32_bf16 v[56:59], a[40:43], v[48:51], v[56:59]\n" \
  "ds_read_b128 v[40:43], v10 offset:12288\n" \
  "v_mfma_f32_16x16x32_bf16 v[68:71], a[40:43], v[52:55], v[68:71]\n" \
  "ds_read_b128 v[44:47], v10 offset:32768\n" \
  "v_mfma_f32_16x16x32_bf16 v[60:63], a[120:123], v[48:51], v[60:63]\n" \
  "buffer_load_dwordx4 v1, s[36:39], s68 offen offset:3072 sc1 lds\n" \
  "v_mfma_f32_16x16x32_bf16 v[72:75], a[120:123], v[52:55], v[72:75]\n" \
  "v_mfma_f32_16x16x32_bf16 v[64:67], a[200:203], v[48:51], v[64:67]\n" \
  "v_mfma_f32_16x16x32_bf16 v[76:79], a[200:203], v[52:55], v[76:79]\n" \
  "s_waitcnt lgkmcnt(2)\n" \
  "v_mfma_f32_16x16x32_bf16 v[56:59], a[44:47], v[32:35], v[56:59]\n" \
  "ds_read_b128 v[48:51], v10 offset:13312\n" \
  "v_mfma_f32_16x16x32_bf16 v[68:71], a[44:47], v[36:39], v[68:71]\n" \
  "ds_read_b128 v[52:55], v10 offset:33792\n" \
  "v_mfma_f32_16x16x32_bf16 v[60:63], a[124:127], v[32:35], v[60:63]\n" \
  "s_add_u32 m0, s53, 45056\n" \
  "v_mfma_f32_16x16x32_bf16 v[72:75], a[124:127], v[36:39], v[72:75]\n" \
  "buffer_load_dwordx4 v1, s[36:39], s70 offen sc1 lds\n" \
  "v_mfma_f32_16x16x32_bf16 v[64:67], a[204:207], v[32:35], v[64:67]\n" \
  "v_mfma_f32_16x16x32_bf16 v[76:79], a[204:207], v[36:39], v[76:79]\n" \
  "s_waitcnt lgkmcnt(2)\n" \
  "v_mfma_f32_16x16x32_bf16 v[56:59], a[48:51], v[40:43], v[56:59]\n" \
  "ds_read_b128 v[32:35], v10 offset:14336\n" \
  "v_mfma_f32_16x16x32_bf16 v[68:71], a[48:51], v[44:47], v[68:71]\n" \
  "ds_read_b128 v[36:39], v10 offset:34816\n" \
  "v_mfma_f32_16x16x32_bf16 v[60:63], a[128:131], v[40:43], v[60:63]\n" \
  "buffer_load_dwordx4 v1, s[36:39], s70 offen offset:1024 sc1 lds\n" \
  "v_mfma_f32_16x16x32_bf16 v[72:75], a[128:131], v[44:47], v[72:75]\n" \
  "v_mfma_f32_16x16x32_bf16 v[64:67], a[208:211], v[40:43], v[64:67]\n" \
  "v_mfma_f32_16x16x32_bf16 v[76:79], a[208:211], v[44:47], v[76:79]\n" \
  "s_waitcnt vmcnt(6) lgkmcnt(2)\n" \
  "s_barrier\n" \
  "v_mfma_f32_16x16x32_bf16 v[56:59], a[52:55], v[48:51], v[56:59]\n" \
  "ds_read_b128 v[40:43], v10 offset:15360\n" \
  "v_mfma_f32_16x16x32_bf16 v[68:71], a[52:55], v[52:55], v[68:71]\n" \
  "ds_read_b128 v[44:47], v10 offset:35840\n" \
  "v_mfma_f32_16x16x32_bf16 v[60:63], a[132:135], v[48:51], v[60:63]\n" \
  "buffer_atomic_add v11, v4, s[40:43], s54 offen\n" \
  "v_mfma_f32_16x16x32_bf16 v[72:75], a[132:135], v[52:55], v[72:75]\n" \
  "s_add_u32 s54, s54, s55\n" \
  "v_mfma_f32_16x16x32_bf16 v[64:67], a[212:215], v[48:51], v[64:67]\n" \
  "s_and_b32 s54, s54, 0xffffdfff\n" \
  "v_mfma_f32_16x16x32_bf16 v[76:79], a[212:215], v[52:55], v[76:79]\n" \
  "s_waitcnt lgkmcnt(2)\n" \
  "v_mfma_f32_16x16x32_bf16 v[56:59], a[56:59], v[32:35], v[56:59]\n" \
  "ds_read_b128 v[48:51], v10 offset:16384\n" \
  "v_mfma_f32_16x16x32_bf16 v[68:71], a[56:59], v[36:39], v[68:71]\n" \
  "ds_read_b128 v[52:55], v10 offset:36864\n" \
  "v_mfma_f32_16x16x32_bf16 v[60:63], a[136:139], v[32:35], v[60:63]\n" \
  "buffer_load_dwordx4 v1, s[36:39], s70 offen offset:2048 sc1 lds\n" \
  "v_mfma_f32_16x16x32_bf16 v[72:75], a[136:139], v[36:39], v[72:75]\n" \
  "v_readlane_b32 s69, v3, s72\n" \
  "v_mfma_f32_16x16x32_bf16 v[64:67], a[216:219], v[32:35], v[64:67]\n" \
  "v_readlane_b32 s73, v6, s72\n" \
  "v_mfma_f32_16x16x32_bf16 v[76:79], a[216:219], v[36:39], v[76:79]\n" \
  "s_add_u32 s72, s72, 1\n" \
  "s_waitcnt lgkmcnt(2)\n" \
  "v_mfma_f32_16x16x32_bf16 v[56:59], a[60:63], v[40:43], v[56:59]\n" \
  "ds_read_b128 v[32:35], v10 offset:17408\n" \
  "v_mfma_f32_16x16x32_bf16 v[68:71], a[60:63], v[44:47], v[68:71]\n" \
  "ds_read_b128 v[36:39], v10 offset:37888\n" \
  "v_mfma_f32_16x16x32_bf16 v[60:63], a[140:143], v[40:43], v[60:63]\n" \
  "buffer_load_dwordx4 v1, s[36:39], s70 offen offset:3072 sc1 lds\n" \
  "v_mfma_f32_16x16x32_bf16 v[72:75], a[140:143], v[44:47], v[72:75]\n" \
  "v_mfma_f32_16x16x32_bf16 v[64:67], a[220:223], v[40:43], v[64:67]\n" \
  "v_mfma_f32_16x16x32_bf16 v[76:79], a[220:223], v[44:47], v[76:79]\n" \
  "s_waitcnt lgkmcnt(2)\n" \
  "v_mfma_f32_16x16x32_bf16 v[56:59], a[64:67], v[48:51], v[56:59]\n" \
  "ds_read_b128 v[40:43], v10 offset:18432\n" \
  "v_mfma_f32_16x16x32_bf16 v[68:71], a[64:67], v[52:55], v[68:71]\n" \
  "ds_read_b128 v[44:47], v10 offset:38912\n" \
  "v_mfma_f32_16x16x32_bf16 v[60:63], a[144:147], v[48:51], v[60:63]\n" \
  "s_add_u32 m0, s53, 49152\n" \
  "v_mfma_f32_16x16x32_bf16 v[72:75], a[144:147], v[52:55], v[72:75]\n" \
  "buffer_load_dwordx4 v1, s[36:39], s71 offen sc1 lds\n" \
  "v_mfma_f32_16x16x32_bf16 v[64:67], a[224:227], v[48:51], v[64:67]\n" \
  "v_mfma_f32_16x16x32_bf16 v[76:79], a[224:227], v[52:55], v[76:79]\n" \
  "s_waitcnt lgkmcnt(2)\n" \
  "v_mfma_f32_16x16x32_bf16 v[56:59], a[68:71], v[32:35], v[56:59]\n" \
  "ds_read_b128 v[48:51], v10 offset:19456\n" \
  "v_mfma_f32_16x16x32_bf16 v[68:71], a[68:71], v[36:39], v[68:71]\n" \
  "ds_read_b128 v[52:55], v10 offset:39936\n" \
  "v_mfma_f32_16x16x32_bf16 v[60:63], a[148:151], v[32:35], v[60:63]\n" \
  "buffer_load_dwordx4 v1, s[36:39], s71 offen offset:1024 sc1 lds\n" \
  "v_mfma_f32_16x16x32_bf16 v[72:75], a[148:151], v[36:39], v[72:75]\n" \
  "s_add_u32 s68, s68, 40960\n" \
  "v_mfma_f32_16x16x32_bf16 v[64:67], a[228:231], v[32:35], v[64:67]\n" \
  "v_mfma_f32_16x16x32_bf16 v[76:79], a[228:231], v[36:39], v[76:79]\n" \
  "s_waitcnt vmcnt(11) lgkmcnt(2)\n" \
  "s_barrier\n" \
  "v_mfma_f32_16x16x32_bf16 v[56:59], a[72:75], v[40:43], v[56:59]\n" \
  "ds_read_b128 v[32:35], v0 offset:0\n" \
  "v_mfma_f32_16x16x32_bf16 v[68:71], a[72:75], v[44:47], v[68:71]\n" \
  "ds_read_b128 v[36:39], v0 offset:20480\n" \
  "v_mfma_f32_16x16x32_bf16 v[60:63], a[152:155], v[40:43], v[60:63]\n" \
  "v_mfma_f32_16x16x32_bf16 v[72:75], a[152:155], v[44:47], v[72:75]\n" \
  "v_mfma_f32_16x16x32_bf16 v[64:67], a[232:235], v[40:43], v[64:67]\n" \
  "v_mfma_f32_16x16x32_bf16 v[76:79], a[232:235], v[44:47], v[76:79]\n" \
  "s_waitcnt lgkmcnt(2)\n" \
  "v_mfma_f32_16x16x32_bf16 v[56:59], a[76:79], v[48:51], v[56:59]\n" \
  "ds_read_b128 v[40:43], v0 offset:1024\n" \
  "v_mfma_f32_16x16x32_bf16 v[68:71], a[76:79], v[52:55], v[68:71]\n" \
  "ds_read_b128 v[44:47], v0 offset:21504\n" \
  "v_mfma_f32_16x16x32_bf16 v[60:63], a[156:159], v[48:51], v[60:63]\n" \
  "s_cmp_eq_u32 s72, 64\n" \
  "v_mfma_f32_16x16x32_bf16 v[72:75], a[156:159], v[52:55], v[72:75]\n" \
  "v_mfma_f32_16x16x32_bf16 v[64:67], a[236:239], v[48:51], v[64:67]\n" \
  "v_mfma_f32_16x16x32_bf16 v[76:79], a[236:239], v[52:55], v[76:79]\n" \
  "s_waitcnt lgkmcnt(2)\n" \
  "v_mfma_f32_16x16x32_bf16 v[100:103], a[0:3], v[32:35], 0\n" \
  "ds_read_b128 v[48:51], v0 offset:2048\n" \
  "v_mfma_f32_16x16x32_bf16 v[112:115], a[0:3], v[36:39], 0\n" \
  "ds_read_b128 v[52:55], v0 offset:22528\n" \
  "v_mfma_f32_16x16x32_bf16 v[104:107], a[80:83], v[32:35], 0\n" \
  "v_cvt_pk_bf16_f32 v16, v56, v57\n" \
  "v_mfma_f32_16x16x32_bf16 v[116:119], a[80:83], v[36:39], 0\n" \
  "v_cvt_pk_bf16_f32 v17, v58, v59\n" \
  "v_mfma_f32_16x16x32_bf16 v[108:111], a[160:163], v[32:35], 0\n" \
  "v_cvt_pk_bf16_f32 v18, v60, v61\n" \
  "v_mfma_f32_16x16x32_bf16 v[120:123], a[160:163], v[36:39], 0\n" \
  "v_cvt_pk_bf16_f32 v19, v62, v63\n" \
  "s_waitcnt lgkmcnt(2)\n" \
  "s_barrier\n" \
  "v_mfma_f32_16x16x32_bf16 v[100:103], a[4:7], v[40:43], v[100:103]\n" \
  "ds_read_b128 v[32:35], v0 offset:3072\n" \
  "v_mfma_f32_16x16x32_bf16 v[112:115], a[4:7], v[44:47], v[112:115]\n" \
  "ds_read_b128 v[36:39], v0 offset:23552\n" \
  "v_mfma_f32_16x16x32_bf16 v[104:107], a[84:87], v[40:43], v[104:107]\n" \
  "buffer_store_dwordx4 v[16:19], v2, s[40:43], s69 offen offset:0 sc1\n" \
  "v_mfma_f32_16x16x32_bf16 v[116:119], a[84:87], v[44:47], v[116:119]\n" \
  "v_cvt_pk_bf16_f32 v20, v68, v69\n" \
  "v_mfma_f32_16x16x32_bf16 v[108:111], a[164:167], v[40:43], v[108:111]\n" \
  "v_cvt_pk_bf16_f32 v21, v70, v71\n" \
  "v_mfma_f32_16x16x32_bf16 v[120:123], a[164:167], v[44:47], v[120:123]\n" \
  "v_cvt_pk_bf16_f32 v22, v72, v73\n" \
  "s_waitcnt lgkmcnt(2)\n" \
  "v_mfma_f32_16x16x32_bf16 v[100:103], a[8:11], v[48:51], v[100:103]\n" \
  "ds_read_b128 v[40:43], v0 offset:4096\n" \
  "v_mfma_f32_16x16x32_bf16 v[112:115], a[8:11], v[52:55], v[112:115]\n" \
  "ds_read_b128 v[44:47], v0 offset:24576\n" \
  "v_mfma_f32_16x16x32_bf16 v[104:107], a[88:91], v[48:51], v[104:107]\n" \
  "v_cvt_pk_bf16_f32 v23, v74, v75\n" \
  "v_mfma_f32_16x16x32_bf16 v[116:119], a[88:91], v[52:55], v[116:119]\n" \
  "buffer_store_dwordx4 v[20:23], v2, s[40:43], s69 offen offset:1024 sc1\n" \
  "v_mfma_f32_16x16x32_bf16 v[108:111], a[168:171], v[48:51], v[108:111]\n" \
  "v_cvt_pk_bf16_f32 v24, v64, v65\n" \
  "v_mfma_f32_16x16x32_bf16 v[120:123], a[168:171], v[52:55], v[120:123]\n" \
  "v_cvt_pk_bf16_f32 v25, v66, v67\n" \
  "s_waitcnt lgkmcnt(2)\n" \
  "v_mfma_f32_16x16x32_bf16 v[100:103], a[12:15], v[32:35], v[100:103]\n" \
  "ds_read_b128 v[48:51], v0 offset:5120\n" \
  "v_mfma_f32_16x16x32_bf16 v[112:115], a[12:15], v[36:39], v[112:115]\n" \
  "ds_read_b128 v[52:55], v0 offset:25600\n" \
  "v_mfma_f32_16x16x32_bf16 v[104:107], a[92:95], v[32:35], v[104:107]\n" \
  "v_cvt_pk_bf16_f32 v26, v76, v77\n" \
  "v_mfma_f32_16x16x32_bf16 v[116:119], a[92:95], v[36:39], v[116:119]\n" \
  "v_cvt_pk_bf16_f32 v27, v78, v79\n" \
  "v_mfma_f32_16x16x32_bf16 v[108:111], a[172:175], v[32:35], v[108:111]\n" \
  "buffer_store_dwordx4 v[24:27], v2, s[40:43], s69 offen offset:2048 sc1\n" \
  "v_mfma_f32_16x16x32_bf16 v[120:123], a[172:175], v[36:39], v[120:123]\n" \
  "s_waitcnt lgkmcnt(2)\n" \
  "v_mfma_f32_16x16x32_bf16 v[100:103], a[16:19], v[40:43], v[100:103]\n" \
  "ds_read_b128 v[32:35], v0 offset:6144\n" \
  "v_mfma_f32_16x16x32_bf16 v[112:115], a[16:19], v[44:47], v[112:115]\n" \
  "ds_read_b128 v[36:39], v0 offset:26624\n" \
  "v_mfma_f32_16x16x32_bf16 v[104:107], a[96:99], v[40:43], v[104:107]\n" \
  "buffer_store_dwordx4 v[12:15], v5, s[40:43], s73 offen offset:0 sc1\n" \
  "v_mfma_f32_16x16x32_bf16 v[116:119], a[96:99], v[44:47], v[116:119]\n" \
  "v_mfma_f32_16x16x32_bf16 v[108:111], a[176:179], v[40:43], v[108:111]\n" \
  "v_mfma_f32_16x16x32_bf16 v[120:123], a[176:179], v[44:47], v[120:123]\n" \
  "s_waitcnt lgkmcnt(2)\n" \
  "v_mfma_f32_16x16x32_bf16 v[100:103], a[20:23], v[48:51], v[100:103]\n" \
  "ds_read_b128 v[40:43], v0 offset:7168\n" \
  "v_mfma_f32_16x16x32_bf16 v[112:115], a[20:23], v[52:55], v[112:115]\n" \
  "ds_read_b128 v[44:47], v0 offset:27648\n" \
  "v_mfma_f32_16x16x32_bf16 v[104:107], a[100:103], v[48:51], v[104:107]\n" \
  "buffer_store_dwordx4 v[12:15], v5, s[40:43], s73 offen offset:1024 sc1\n" \
  "v_mfma_f32_16x16x32_bf16 v[116:119], a[100:103], v[52:55], v[116:119]\n" \
  "v_mfma_f32_16x16x32_bf16 v[108:111], a[180:183], v[48:51], v[108:111]\n" \
  "v_mfma_f32_16x16x32_bf16 v[120:123], a[180:183], v[52:55], v[120:123]\n" \
  "s_waitcnt lgkmcnt(2)\n" \
  "v_mfma_f32_16x16x32_bf16 v[100:103], a[24:27], v[32:35], v[100:103]\n" \
  "ds_read_b128 v[48:51], v0 offset:8192\n" \
  "v_mfma_f32_16x16x32_bf16 v[112:115], a[24:27], v[36:39], v[112:115]\n" \
  "ds_read_b128 v[52:55], v0 offset:28672\n" \
  "v_mfma_f32_16x16x32_bf16 v[104:107], a[104:107], v[32:35], v[104:107]\n" \
  "buffer_store_dwordx4 v[12:15], v5, s[40:43], s73 offen offset:2048 sc1\n" \
  "v_mfma_f32_16x16x32_bf16 v[116:119], a[104:107], v[36:39], v[116:119]\n" \
  "s_nop 0\n" \
  "v_mfma_f32_16x16x32_bf16 v[108:111], a[184:187], v[32:35], v[108:111]\n" \
  "buffer_store_dwordx4 v[12:15], v5, s[40:43], s73 offen offset:3072 sc1\n" \
  "v_mfma_f32_16x16x32_bf16 v[120:123], a[184:187], v[36:39], v[120:123]\n" \
  "s_waitcnt lgkmcnt(2)\n" \
  "v_mfma_f32_16x16x32_bf16 v[100:103], a[28:31], v[40:43], v[100:103]\n" \
  "ds_read_b128 v[32:35], v0 offset:9216\n" \
  "v_mfma_f32_16x16x32_bf16 v[112:115], a[28:31], v[44:47], v[112:115]\n" \
  "ds_read_b128 v[36:39], v0 offset:29696\n" \
  "v_mfma_f32_16x16x32_bf16 v[104:107], a[108:111], v[40:43], v[104:107]\n" \
  "s_add_u32 s70, s68, 4096\n" \
  "v_mfma_f32_16x16x32_bf16 v[116:119], a[108:111], v[44:47], v[116:119]\n" \
  "s_add_u32 s71, s68, 8192\n" \
  "v_mfma_f32_16x16x32_bf16 v[108:111], a[188:191], v[40:43], v[108:111]\n" \
  "s_add_u32 m0, s53, 81920\n" \
  "v_mfma_f32_16x16x32_bf16 v[120:123], a[188:191], v[44:47], v[120:123]\n" \
  "buffer_load_dwordx4 v1, s[36:39], s68 offen sc1 lds\n" \
  "s_waitcnt lgkmcnt(2)\n" \
  "v_mfma_f32_16x16x32_bf16 v[100:103], a[32:35], v[48:51], v[100:103]\n" \
  "ds_read_b128 v[40:43], v0 offset:10240\n" \
  "v_mfma_f32_16x16x32_bf16 v[112:115], a[32:35], v[52:55], v[112:115]\n" \
  "ds_read_b128 v[44:47], v0 offset:30720\n" \
  "v_mfma_f32_16x16x32_bf16 v[104:107], a[112:115], v[48:51], v[104:107]\n" \
  "buffer_load_dwordx4 v1, s[36:39], s68 offen offset:1024 sc1 lds\n" \
  "v_mfma_f32_16x16x32_bf16 v[116:119], a[112:115], v[52:55], v[116:119]\n" \
  "v_mfma_f32_16x16x32_bf16 v[108:111], a[192:195], v[48:51], v[108:111]\n" \
  "v_mfma_f32_16x16x32_bf16 v[120:123], a[192:195], v[52:55], v[120:123]\n" \
  "s_waitcnt lgkmcnt(2)\n" \
  "v_mfma_f32_16x16x32_bf16 v[100:103], a[36:39], v[32:35], v[100:103]\n" \
  "ds_read_b128 v[48:51], v0 offset:11264\n" \
  "v_mfma_f32_16x16x32_bf16 v[112:115], a[36:39], v[36:39], v[112:115]\n" \
  "ds_read_b128 v[52:55], v0 offset:31744\n" \
  "v_mfma_f32_16x16x32_bf16 v[104:107], a[116:119], v[32:35], v[104:107]\n" \
  "buffer_load_dwordx4 v1, s[36:39], s68 offen offset:2048 sc1 lds\n" \
  "v_mfma_f32_16x16x32_bf16 v[116:119], a[116:119], v[36:39], v[116:119]\n" \
  "v_mfma_f32_16x16x32_bf16 v[108:111], a[196:199], v[32:35], v[108:111]\n" \
  "v_mfma_f32_16x16x32_bf16 v[120:123], a[196:199], v[36:39], v[120:123]\n" \
  "s_waitcnt lgkmcnt(2)\n" \
  "v_mfma_f32_16x16x32_bf16 v[100:103], a[40:43], v[40:43], v[100:103]\n" \
  "ds_read_b128 v[32:35], v0 offset:12288\n" \
  "v_mfma_f32_16x16x32_bf16 v[112:115], a[40:43], v[44:47], v[112:115]\n" \
  "ds_read_b128 v[36:39], v0 offset:32768\n" \
  "v_mfma_f32_16x16x32_bf16 v[104:107], a[120:123], v[40:43], v[104:107]\n" \
  "buffer_load_dwordx4 v1, s[36:39], s68 offen offset:3072 sc1 lds\n" \
  "v_mfma_f32_16x16x32_bf16 v[116:119], a[120:123], v[44:47], v[116:119]\n" \
  "v_mfma_f32_16x16x32_bf16 v[108:111], a[200:203], v[40:43], v[108:111]\n" \
  "v_mfma_f32_16x16x32_bf16 v[120:123], a[200:203], v[44:47], v[120:123]\n" \
  "s_waitcnt lgkmcnt(2)\n" \
  "v_mfma_f32_16x16x32_bf16 v[100:103], a[44:47], v[48:51], v[100:103]\n" \
  "ds_read_b128 v[40:43], v0 offset:13312\n" \
  "v_mfma_f32_16x16x32_bf16 v[112:115], a[44:47], v[52:55], v[112:115]\n" \
  "ds_read_b128 v[44:47], v0 offset:33792\n" \
  "v_mfma_f32_16x16x32_bf16 v[104:107], a[124:127], v[48:51], v[104:107]\n" \
  "s_add_u32 m0, s53, 86016\n" \
  "v_mfma_f32_16x16x32_bf16 v[116:119], a[124:127], v[52:55], v[116:119]\n" \
  "buffer_load_dwordx4 v1, s[36:39], s70 offen sc1 lds\n" \
  "v_mfma_f32_16x16x32_bf16 v[108:111], a[204:207], v[48:51], v[108:111]\n" \
  "v_mfma_f32_16x16x32_bf16 v[120:123], a[204:207], v[52:55], v[120:123]\n" \
  "s_waitcnt lgkmcnt(2)\n" \
  "v_mfma_f32_16x16x32_bf16 v[100:103], a[48:51], v[32:35], v[100:103]\n" \
  "ds_read_b128 v[48:51], v0 offset:14336\n" \
  "v_mfma_f32_16x16x32_bf16 v[112:115], a[48:51], v[36:39], v[112:115]\n" \
  "ds_read_b128 v[52:55], v0 offset:34816\n" \
  "v_mfma_f32_16x16x32_bf16 v[104:107], a[128:131], v[32:35], v[104:107]\n" \
  "buffer_load_dwordx4 v1, s[36:39], s70 offen offset:1024 sc1 lds\n" \
  "v_mfma_f32_16x16x32_bf16 v[116:119], a[128:131], v[36:39], v[116:119]\n" \
  "v_mfma_f32_16x16x32_bf16 v[108:111], a[208:211], v[32:35], v[108:111]\n" \
  "v_mfma_f32_16x16x32_bf16 v[120:123], a[208:211], v[36:39], v[120:123]\n" \
  "s_waitcnt vmcnt(6) lgkmcnt(2)\n" \
  "s_barrier\n" \
  "v_mfma_f32_16x16x32_bf16 v[100:103], a[52:55], v[40:43], v[100:103]\n" \
  "ds_read_b128 v[32:35], v0 offset:15360\n" \
  "v_mfma_f32_16x16x32_bf16 v[112:115], a[52:55], v[44:47], v[112:115]\n" \
  "ds_read_b128 v[36:39], v0 offset:35840\n" \
  "v_mfma_f32_16x16x32_bf16 v[104:107], a[132:135], v[40:43], v[104:107]\n" \
  "buffer_atomic_add v11, v4, s[40:43], s54 offen\n" \
  "v_mfma_f32_16x16x32_bf16 v[116:119], a[132:135], v[44:47], v[116:119]\n" \
  "s_add_u32 s54, s54, s55\n" \
  "v_mfma_f32_16x16x32_bf16 v[108:111], a[212:215], v[40:43], v[108:111]\n" \
  "s_and_b32 s54, s54, 0xffffdfff\n" \
  "v_mfma_f32_16x16x32_bf16 v[120:123], a[212:215], v[44:47], v[120:123]\n" \
  "s_waitcnt lgkmcnt(2)\n" \
  "v_mfma_f32_16x16x32_bf16 v[100:103], a[56:59], v[48:51], v[100:103]\n" \
  "ds_read_b128 v[40:43], v0 offset:16384\n" \
  "v_mfma_f32_16x16x32_bf16 v[112:115], a[56:59], v[52:55], v[112:115]\n" \
  "ds_read_b128 v[44:47], v0 offset:36864\n" \
  "v_mfma_f32_16x16x32_bf16 v[104:107], a[136:139], v[48:51], v[104:107]\n" \
  "buffer_load_dwordx4 v1, s[36:39], s70 offen offset:2048 sc1 lds\n" \
  "v_mfma_f32_16x16x32_bf16 v[116:119], a[136:139], v[52:55], v[116:119]\n" \
  "v_readlane_b32 s69, v3, s72\n" \
  "v_mfma_f32_16x16x32_bf16 v[108:111], a[216:219], v[48:51], v[108:111]\n" \
  "v_readlane_b32 s73, v6, s72\n" \
  "v_mfma_f32_16x16x32_bf16 v[120:123], a[216:219], v[52:55], v[120:123]\n" \
  "s_add_u32 s72, s72, 1\n" \
  "s_waitcnt lgkmcnt(2)\n" \
  "v_mfma_f32_16x16x32_bf16 v[100:103], a[60:63], v[32:35], v[100:103]\n" \
  "ds_read_b128 v[48:51], v0 offset:17408\n" \
  "v_mfma_f32_16x16x32_bf16 v[112:115], a[60:63], v[36:39], v[112:115]\n" \
  "ds_read_b128 v[52:55], v0 offset:37888\n" \
  "v_mfma_f32_16x16x32_bf16 v[104:107], a[140:143], v[32:35], v[104:107]\n" \
  "buffer_load_dwordx4 v1, s[36:39], s70 offen offset:3072 sc1 lds\n" \
  "v_mfma_f32_16x16x32_bf16 v[116:119], a[140:143], v[36:39], v[116:119]\n" \
  "v_mfma_f32_16x16x32_bf16 v[108:111], a[220:223], v[32:35], v[108:111]\n" \
  "v_mfma_f32_16x16x32_bf16 v[120:123], a[220:223], v[36:39], v[120:123]\n" \
  "s_waitcnt lgkmcnt(2)\n" \
  "v_mfma_f32_16x16x32_bf16 v[100:103], a[64:67], v[40:43], v[100:103]\n" \
  "ds_read_b128 v[32:35], v0 offset:18432\n" \
  "v_mfma_f32_16x16x32_bf16 v[112:115], a[64:67], v[44:47], v[112:115]\n" \
  "ds_read_b128 v[36:39], v0 offset:38912\n" \
  "v_mfma_f32_16x16x32_bf16 v[104:107], a[144:147], v[40:43], v[104:107]\n" \
  "s_add_u32 m0, s53, 90112\n" \
  "v_mfma_f32_16x16x32_bf16 v[116:119], a[144:147], v[44:47], v[116:119]\n" \
  "buffer_load_dwordx4 v1, s[36:39], s71 offen sc1 lds\n" \
  "v_mfma_f32_16x16x32_bf16 v[108:111], a[224:227], v[40:43], v[108:111]\n" \
  "v_mfma_f32_16x16x32_bf16 v[120:123], a[224:227], v[44:47], v[120:123]\n" \
  "s_waitcnt lgkmcnt(2)\n" \
  "v_mfma_f32_16x16x32_bf16 v[100:103], a[68:71], v[48:51], v[100:103]\n" \
  "ds_read_b128 v[40:43], v0 offset:19456\n" \
  "v_mfma_f32_16x16x32_bf16 v[112:115], a[68:71], v[52:55], v[112:115]\n" \
  "ds_read_b128 v[44:47], v0 offset:39936\n" \
  "v_mfma_f32_16x16x32_bf16 v[104:107], a[148:151], v[48:51], v[104:107]\n" \
  "buffer_load_dwordx4 v1, s[36:39], s71 offen offset:1024 sc1 lds\n" \
  "v_mfma_f32_16x16x32_bf16 v[116:119], a[148:151], v[52:55], v[116:119]\n" \
  "s_add_u32 s68, s68, 40960\n" \
  "v_mfma_f32_16x16x32_bf16 v[108:111], a[228:231], v[48:51], v[108:111]\n" \
  "v_mfma_f32_16x16x32_bf16 v[120:123], a[228:231], v[52:55], v[120:123]\n" \
  "s_waitcnt vmcnt(11) lgkmcnt(2)\n" \
  "s_barrier\n" \
  "v_mfma_f32_16x16x32_bf16 v[100:103], a[72:75], v[32:35], v[100:103]\n" \
  "ds_read_b128 v[48:51], v9 offset:0\n" \
  "v_mfma_f32_16x16x32_bf16 v[112:115], a[72:75], v[36:39], v[112:115]\n" \
  "ds_read_b128 v[52:55], v9 offset:20480\n" \
  "v_mfma_f32_16x16x32_bf16 v[104:107], a[152:155], v[32:35], v[104:107]\n" \
  "v_mfma_f32_16x16x32_bf16 v[116:119], a[152:155], v[36:39], v[116:119]\n" \
  "v_mfma_f32_16x16x32_bf16 v[108:111], a[232:235], v[32:35], v[108:111]\n" \
  "v_mfma_f32_16x16x32_bf16 v[120:123], a[232:235], v[36:39], v[120:123]\n" \
  "s_waitcnt lgkmcnt(2)\n" \
  "v_mfma_f32_16x16x32_bf16 v[100:103], a[76:79], v[40:43], v[100:103]\n" \
  "ds_read_b128 v[32:35], v9 offset:1024\n" \
  "v_mfma_f32_16x16x32_bf16 v[112:115], a[76:79], v[44:47], v[112:115]\n" \
  "ds_read_b128 v[36:39], v9 offset:21504\n" \
  "v_mfma_f32_16x16x32_bf16 v[104:107], a[156:159], v[40:43], v[104:107]\n" \
  "s_cmp_eq_u32 s72, 64\n" \
  "v_mfma_f32_16x16x32_bf16 v[116:119], a[156:159], v[44:47], v[116:119]\n" \
  "v_mfma_f32_16x16x32_bf16 v[108:111], a[236:239], v[40:43], v[108:111]\n" \
  "v_mfma_f32_16x16x32_bf16 v[120:123], a[236:239], v[44:47], v[120:123]\n" \
  "s_cbranch_scc1 LPE_%=\n" \
  "s_waitcnt lgkmcnt(2)\n" \
  "v_mfma_f32_16x16x32_bf16 v[56:59], a[0:3], v[48:51], 0\n" \
  "ds_read_b128 v[40:43], v9 offset:2048\n" \
  "v_mfma_f32_16x16x32_bf16 v[68:71], a[0:3], v[52:55], 0\n" \
  "ds_read_b128 v[44:47], v9 offset:22528\n" \
  "v_mfma_f32_16x16x32_bf16 v[60:63], a[80:83], v[48:51], 0\n" \
  "v_cvt_pk_bf16_f32 v16, v100, v101\n" \
  "v_mfma_f32_16x16x32_bf16 v[72:75], a[80:83], v[52:55], 0\n" \
  "v_cvt_pk_bf16_f32 v17, v102, v103\n" \
  "v_mfma_f32_16x16x32_bf16 v[64:67], a[160:163], v[48:51], 0\n" \
  "v_cvt_pk_bf16_f32 v18, v104, v105\n" \
  "v_mfma_f32_16x16x32_bf16 v[76:79], a[160:163], v[52:55], 0\n" \
  "v_cvt_pk_bf16_f32 v19, v106, v107\n" \
  "s_waitcnt lgkmcnt(2)\n" \
  "s_barrier\n" \
  "v_mfma_f32_16x16x32_bf16 v[56:59], a[4:7], v[32:35], v[56:59]\n" \
  "ds_read_b128 v[48:51], v9 offset:3072\n" \
  "v_mfma_f32_16x16x32_bf16 v[68:71], a[4:7], v[36:39], v[68:71]\n" \
  "ds_read_b128 v[52:55], v9 offset:23552\n" \
  "v_mfma_f32_16x16x32_bf16 v[60:63], a[84:87], v[32:35], v[60:63]\n" \
  "buffer_store_dwordx4 v[16:19], v2, s[40:43], s69 offen offset:0 sc1\n" \
  "v_mfma_f32_16x16x32_bf16 v[72:75], a[84:87], v[36:39], v[72:75]\n" \
  "v_cvt_pk_bf16_f32 v20, v112, v113\n" \
  "v_mfma_f32_16x16x32_bf16 v[64:67], a[164:167], v[32:35], v[64:67]\n" \
  "v_cvt_pk_bf16_f32 v21, v114, v115\n" \
  "v_mfma_f32_16x16x32_bf16 v[76:79], a[164:167], v[36:39], v[76:79]\n" \
  "v_cvt_pk_bf16_f32 v22, v116, v117\n" \
  "s_waitcnt lgkmcnt(2)\n" \
  "v_mfma_f32_16x16x32_bf16 v[56:59], a[8:11], v[40:43], v[56:59]\n" \
  "ds_read_b128 v[32:35], v9 offset:4096\n" \
  "v_mfma_f32_16x16x32_bf16 v[68:71], a[8:11], v[44:47], v[68:71]\n" \
  "ds_read_b128 v[36:39], v9 offset:24576\n" \
  "v_mfma_f32_16x16x32_bf16 v[60:63], a[88:91], v[40:43], v[60:63]\n" \
  "v_cvt_pk_bf16_f32 v23, v118, v119\n" \
  "v_mfma_f32_16x16x32_bf16 v[72:75], a[88:91], v[44:47], v[72:75]\n" \
  "buffer_store_dwordx4 v[20:23], v2, s[40:43], s69 offen offset:1024 sc1\n" \
  "v_mfma_f32_16x16x32_bf16 v[64:67], a[168:171], v[40:43], v[64:67]\n" \
  "v_cvt_pk_bf16_f32 v24, v108, v109\n" \
  "v_mfma_f32_16x16x32_bf16 v[76:79], a[168:171], v[44:47], v[76:79]\n" \
  "v_cvt_pk_bf16_f32 v25, v110, v111\n" \
  "s_waitcnt lgkmcnt(2)\n" \
  "v_mfma_f32_16x16x32_bf16 v[56:59], a[12:15], v[48:51], v[56:59]\n" \
  "ds_read_b128 v[40:43], v9 offset:5120\n" \
  "v_mfma_f32_16x16x32_bf16 v[68:71], a[12:15], v[52:55], v[68:71]\n" \
  "ds_read_b128 v[44:47], v9 offset:25600\n" \
  "v_mfma_f32_16x16x32_bf16 v[60:63], a[92:95], v[48:51], v[60:63]\n" \
  "v_cvt_pk_bf16_f32 v26, v120, v121\n" \
  "v_mfma_f32_16x16x32_bf16 v[72:75], a[92:95], v[52:55], v[72:75]\n" \
  "v_cvt_pk_bf16_f32 v27, v122, v123\n" \
  "v_mfma_f32_16x16x32_bf16 v[64:67], a[172:175], v[48:51], v[64:67]\n" \
  "buffer_store_dwordx4 v[24:27], v2, s[40:43], s69 offen offset:2048 sc1\n" \
  "v_mfma_f32_16x16x32_bf16 v[76:79], a[172:175], v[52:55], v[76:79]\n" \
  "s_waitcnt lgkmcnt(2)\n" \
  "v_mfma_f32_16x16x32_bf16 v[56:59], a[16:19], v[32:35], v[56:59]\n" \
  "ds_read_b128 v[48:51], v9 offset:6144\n" \
  "v_mfma_f32_16x16x32_bf16 v[68:71], a[16:19], v[36:39], v[68:71]\n" \
  "ds_read_b128 v[52:55], v9 offset:26624\n" \
  "v_mfma_f32_16x16x32_bf16 v[60:63], a[96:99], v[32:35], v[60:63]\n" \
  "buffer_store_dwordx4 v[12:15], v5, s[40:43], s73 offen offset:0 sc1\n" \
  "v_mfma_f32_16x16x32_bf16 v[72:75], a[96:99], v[36:39], v[72:75]\n" \
  "v_mfma_f32_16x16x32_bf16 v[64:67], a[176:179], v[32:35], v[64:67]\n" \
  "v_mfma_f32_16x16x32_bf16 v[76:79], a[176:179], v[36:39], v[76:79]\n" \
  "s_waitcnt lgkmcnt(2)\n" \
  "v_mfma_f32_16x16x32_bf16 v[56:59], a[20:23], v[40:43], v[56:59]\n" \
  "ds_read_b128 v[32:35], v9 offset:7168\n" \
  "v_mfma_f32_16x16x32_bf16 v[68:71], a[20:23], v[44:47], v[68:71]\n" \
  "ds_read_b128 v[36:39], v9 offset:27648\n" \
  "v_mfma_f32_16x16x32_bf16 v[60:63], a[100:103], v[40:43], v[60:63]\n" \
  "buffer_store_dwordx4 v[12:15], v5, s[40:43], s73 offen offset:1024 sc1\n" \
  "v_mfma_f32_16x16x32_bf16 v[72:75], a[100:103], v[44:47], v[72:75]\n" \
  "v_mfma_f32_16x16x32_bf16 v[64:67], a[180:183], v[40:43], v[64:67]\n" \
  "v_mfma_f32_16x16x32_bf16 v[76:79], a[180:183], v[44:47], v[76:79]\n" \
  "s_waitcnt lgkmcnt(2)\n" \
  "v_mfma_f32_16x16x32_bf16 v[56:59], a[24:27], v[48:51], v[56:59]\n" \
  "ds_read_b128 v[40:43], v9 offset:8192\n" \
  "v_mfma_f32_16x16x32_bf16 v[68:71], a[24:27], v[52:55], v[68:71]\n" \
  "ds_read_b128 v[44:47], v9 offset:28672\n" \
  "v_mfma_f32_16x16x32_bf16 v[60:63], a[104:107], v[48:51], v[60:63]\n" \
  "buffer_store_dwordx4 v[12:15], v5, s[40:43], s73 offen offset:2048 sc1\n" \
  "v_mfma_f32_16x16x32_bf16 v[72:75], a[104:107], v[52:55], v[72:75]\n" \
  "s_nop 0\n" \
  "v_mfma_f32_16x16x32_bf16 v[64:67], a[184:187], v[48:51], v[64:67]\n" \
  "buffer_store_dwordx4 v[12:15], v5, s[40:43], s73 offen offset:3072 sc1\n" \
  "v_mfma_f32_16x16x32_bf16 v[76:79], a[184:187], v[52:55], v[76:79]\n" \
  "s_waitcnt lgkmcnt(2)\n" \
  "v_mfma_f32_16x16x32_bf16 v[56:59], a[28:31], v[32:35], v[56:59]\n" \
  "ds_read_b128 v[48:51], v9 offset:9216\n" \
  "v_mfma_f32_16x16x32_bf16 v[68:71], a[28:31], v[36:39], v[68:71]\n" \
  "ds_read_b128 v[52:55], v9 offset:29696\n" \
  "v_mfma_f32_16x16x32_bf16 v[60:63], a[108:111], v[32:35], v[60:63]\n" \
  "s_add_u32 s70, s68, 4096\n" \
  "v_mfma_f32_16x16x32_bf16 v[72:75], a[108:111], v[36:39], v[72:75]\n" \
  "s_add_u32 s71, s68, 8192\n" \
  "v_mfma_f32_16x16x32_bf16 v[64:67], a[188:191], v[32:35], v[64:67]\n" \
  "s_add_u32 m0, s53, 0\n" \
  "v_mfma_f32_16x16x32_bf16 v[76:79], a[188:191], v[36:39], v[76:79]\n" \
  "buffer_load_dwordx4 v1, s[36:39], s68 offen sc1 lds\n" \
  "s_waitcnt lgkmcnt(2)\n" \
  "v_mfma_f32_16x16x32_bf16 v[56:59], a[32:35], v[40:43], v[56:59]\n" \
  "ds_read_b128 v[32:35], v9 offset:10240\n" \
  "v_mfma_f32_16x16x32_bf16 v[68:71], a[32:35], v[44:47], v[68:71]\n" \
  "ds_read_b128 v[36:39], v9 offset:30720\n" \
  "v_mfma_f32_16x16x32_bf16 v[60:63], a[112:115], v[40:43], v[60:63]\n" \
  "buffer_load_dwordx4 v1, s[36:39], s68 offen offset:1024 sc1 lds\n" \
  "v_mfma_f32_16x16x32_bf16 v[72:75], a[112:115], v[44:47], v[72:75]\n" \
  "v_mfma_f32_16x16x32_bf16 v[64:67], a[192:195], v[40:43], v[64:67]\n" \
  "v_mfma_f32_16x16x32_bf16 v[76:79], a[192:195], v[44:47], v[76:79]\n" \
  "s_waitcnt lgkmcnt(2)\n" \
  "v_mfma_f32_16x16x32_bf16 v[56:59], a[36:39], v[48:51], v[56:59]\n" \
  "ds_read_b128 v[40:43], v9 offset:11264\n" \
  "v_mfma_f32_16x16x32_bf16 v[68:71], a[36:39], v[52:55], v[68:71]\n" \
  "ds_read_b128 v[44:47], v9 offset:31744\n" \
  "v_mfma_f32_16x16x32_bf16 v[60:63], a[116:119], v[48:51], v[60:63]\n" \
  "buffer_load_dwordx4 v1, s[36:39], s68 offen offset:2048 sc1 lds\n" \
  "v_mfma_f32_16x16x32_bf16 v[72:75], a[116:119], v[52:55], v[72:75]\n" \
  "v_mfma_f32_16x16x32_bf16 v[64:67], a[196:199], v[48:51], v[64:67]\n" \
  "v_mfma_f32_16x16x32_bf16 v[76:79], a[196:199], v[52:55], v[76:79]\n" \
  "s_waitcnt lgkmcnt(2)\n" \
  "v_mfma_f32_16x16x32_bf16 v[56:59], a[40:43], v[32:35], v[56:59]\n" \
  "ds_read_b128 v[48:51], v9 offset:12288\n" \
  "v_mfma_f32_16x16x32_bf16 v[68:71], a[40:43], v[36:39], v[68:71]\n" \
  "ds_read_b128 v[52:55], v9 offset:32768\n" \
  "v_mfma_f32_16x16x32_bf16 v[60:63], a[120:123], v[32:35], v[60:63]\n" \
  "buffer_load_dwordx4 v1, s[36:39], s68 offen offset:3072 sc1 lds\n" \
  "v_mfma_f32_16x16x32_bf16 v[72:75], a[120:123], v[36:39], v[72:75]\n" \
  "v_mfma_f32_16x16x32_bf16 v[64:67], a[200:203], v[32:35], v[64:67]\n" \
  "v_mfma_f32_16x16x32_bf16 v[76:79], a[200:203], v[36:39], v[76:79]\n" \
  "s_waitcnt lgkmcnt(2)\n" \
  "v_mfma_f32_16x16x32_bf16 v[56:59], a[44:47], v[40:43], v[56:59]\n" \
  "ds_read_b128 v[32:35], v9 offset:13312\n" \
  "v_mfma_f32_16x16x32_bf16 v[68:71], a[44:47], v[44:47], v[68:71]\n" \
  "ds_read_b128 v[36:39], v9 offset:33792\n" \
  "v_mfma_f32_16x16x32_bf16 v[60:63], a[124:127], v[40:43], v[60:63]\n" \
  "s_add_u32 m0, s53, 4096\n" \
  "v_mfma_f32_16x16x32_bf16 v[72:75], a[124:127], v[44:47], v[72:75]\n" \
  "buffer_load_dwordx4 v1, s[36:39], s70 offen sc1 lds\n" \
  "v_mfma_f32_16x16x32_bf16 v[64:67], a[204:207], v[40:43], v[64:67]\n" \
  "v_mfma_f32_16x16x32_bf16 v[76:79], a[204:207], v[44:47], v[76:79]\n" \
  "s_waitcnt lgkmcnt(2)\n" \
  "v_mfma_f32_16x16x32_bf16 v[56:59], a[48:51], v[48:51], v[56:59]\n" \
  "ds_read_b128 v[40:43], v9 offset:14336\n" \
  "v_mfma_f32_16x16x32_bf16 v[68:71], a[48:51], v[52:55], v[68:71]\n" \
  "ds_read_b128 v[44:47], v9 offset:34816\n" \
  "v_mfma_f32_16x16x32_bf16 v[60:63], a[128:131], v[48:51], v[60:63]\n" \
  "buffer_load_dwordx4 v1, s[36:39], s70 offen offset:1024 sc1 lds\n" \
  "v_mfma_f32_16x16x32_bf16 v[72:75], a[128:131], v[52:55], v[72:75]\n" \
  "v_mfma_f32_16x16x32_bf16 v[64:67], a[208:211], v[48:51], v[64:67]\n" \
  "v_mfma_f32_16x16x32_bf16 v[76:79], a[208:211], v[52:55], v[76:79]\n" \
  "s_waitcnt vmcnt(6) lgkmcnt(2)\n" \
  "s_barrier\n" \
  "v_mfma_f32_16x16x32_bf16 v[56:59], a[52:55], v[32:35], v[56:59]\n" \
  "ds_read_b128 v[48:51], v9 offset:15360\n" \
  "v_mfma_f32_16x16x32_bf16 v[68:71], a[52:55], v[36:39], v[68:71]\n" \
  "ds_read_b128 v[52:55], v9 offset:35840\n" \
  "v_mfma_f32_16x16x32_bf16 v[60:63], a[132:135], v[32:35], v[60:63]\n" \
  "buffer_atomic_add v11, v4, s[40:43], s54 offen\n" \
  "v_mfma_f32_16x16x32_bf16 v[72:75], a[132:135], v[36:39], v[72:75]\n" \
  "s_add_u32 s54, s54, s55\n" \
  "v_mfma_f32_16x16x32_bf16 v[64:67], a[212:215], v[32:35], v[64:67]\n" \
  "s_and_b32 s54, s54, 0xffffdfff\n" \
  "v_mfma_f32_16x16x32_bf16 v[76:79], a[212:215], v[36:39], v[76:79]\n" \
  "s_waitcnt lgkmcnt(2)\n" \
  "v_mfma_f32_16x16x32_bf16 v[56:59], a[56:59], v[40:43], v[56:59]\n" \
  "ds_read_b128 v[32:35], v9 offset:16384\n" \
  "v_mfma_f32_16x16x32_bf16 v[68:71], a[56:59], v[44:47], v[68:71]\n" \
  "ds_read_b128 v[36:39], v9 offset:36864\n" \
  "v_mfma_f32_16x16x32_bf16 v[60:63], a[136:139], v[40:43], v[60:63]\n" \
  "buffer_load_dwordx4 v1, s[36:39], s70 offen offset:2048 sc1 lds\n" \
  "v_mfma_f32_16x16x32_bf16 v[72:75], a[136:139], v[44:47], v[72:75]\n" \
  "v_readlane_b32 s69, v3, s72\n" \
  "v_mfma_f32_16x16x32_bf16 v[64:67], a[216:219], v[40:43], v[64:67]\n" \
  "v_readlane_b32 s73, v6, s72\n" \
  "v_mfma_f32_16x16x32_bf16 v[76:79], a[216:219], v[44:47], v[76:79]\n" \
  "s_add_u32 s72, s72, 1\n" \
  "s_waitcnt lgkmcnt(2)\n" \
  "v_mfma_f32_16x16x32_bf16 v[56:59], a[60:63], v[48:51], v[56:59]\n" \
  "ds_read_b128 v[40:43], v9 offset:17408\n" \
  "v_mfma_f32_16x16x32_bf16 v[68:71], a[60:63], v[52:55], v[68:71]\n" \
  "ds_read_b128 v[44:47], v9 offset:37888\n" \
  "v_mfma_f32_16x16x32_bf16 v[60:63], a[140:143], v[48:51], v[60:63]\n" \
  "buffer_load_dwordx4 v1, s[36:39], s70 offen offset:3072 sc1 lds\n" \
  "v_mfma_f32_16x16x32_bf16 v[72:75], a[140:143], v[52:55], v[72:75]\n" \
  "v_mfma_f32_16x16x32_bf16 v[64:67], a[220:223], v[48:51], v[64:67]\n" \
  "v_mfma_f32_16x16x32_bf16 v[76:79], a[220:223], v[52:55], v[76:79]\n" \
  "s_waitcnt lgkmcnt(2)\n" \
  "v_mfma_f32_16x16x32_bf16 v[56:59], a[64:67], v[32:35], v[56:59]\n" \
  "ds_read_b128 v[48:51], v9 offset:18432\n" \
  "v_mfma_f32_16x16x32_bf16 v[68:71], a[64:67], v[36:39], v[68:71]\n" \
  "ds_read_b128 v[52:55], v9 offset:38912\n" \
  "v_mfma_f32_16x16x32_bf16 v[60:63], a[144:147], v[32:35], v[60:63]\n" \
  "s_add_u32 m0, s53, 8192\n" \
  "v_mfma_f32_16x16x32_bf16 v[72:75], a[144:147], v[36:39], v[72:75]\n" \
  "buffer_load_dwordx4 v1, s[36:39], s71 offen sc1 lds\n" \
  "v_mfma_f32_16x16x32_bf16 v[64:67], a[224:227], v[32:35], v[64:67]\n" \
  "v_mfma_f32_16x16x32_bf16 v[76:79], a[224:227], v[36:39], v[76:79]\n" \
  "s_waitcnt lgkmcnt(2)\n" \
  "v_mfma_f32_16x16x32_bf16 v[56:59], a[68:71], v[40:43], v[56:59]\n" \
  "ds_read_b128 v[32:35], v9 offset:19456\n" \
  "v_mfma_f32_16x16x32_bf16 v[68:71], a[68:71], v[44:47], v[68:71]\n" \
  "ds_read_b128 v[36:39], v9 offset:39936\n" \
  "v_mfma_f32_16x16x32_bf16 v[60:63], a[148:151], v[40:43], v[60:63]\n" \
  "buffer_load_dwordx4 v1, s[36:39], s71 offen offset:1024 sc1 lds\n" \
  "v_mfma_f32_16x16x32_bf16 v[72:75], a[148:151], v[44:47], v[72:75]\n" \
  "s_add_u32 s68, s68, 40960\n" \
  "v_mfma_f32_16x16x32_bf16 v[64:67], a[228:231], v[40:43], v[64:67]\n" \
  "v_mfma_f32_16x16x32_bf16 v[76:79], a[228:231], v[44:47], v[76:79]\n" \
  "s_waitcnt vmcnt(11) lgkmcnt(2)\n" \
  "s_barrier\n" \
  "v_mfma_f32_16x16x32_bf16 v[56:59], a[72:75], v[48:51], v[56:59]\n" \
  "ds_read_b128 v[40:43], v10 offset:0\n" \
  "v_mfma_f32_16x16x32_bf16 v[68:71], a[72:75], v[52:55], v[68:71]\n" \
  "ds_read_b128 v[44:47], v10 offset:20480\n" \
  "v_mfma_f32_16x16x32_bf16 v[60:63], a[152:155], v[48:51], v[60:63]\n" \
  "v_mfma_f32_16x16x32_bf16 v[72:75], a[152:155], v[52:55], v[72:75]\n" \
  "v_mfma_f32_16x16x32_bf16 v[64:67], a[232:235], v[48:51], v[64:67]\n" \
  "v_mfma_f32_16x16x32_bf16 v[76:79], a[232:235], v[52:55], v[76:79]\n" \
  "s_waitcnt lgkmcnt(2)\n" \
  "v_mfma_f32_16x16x32_bf16 v[56:59], a[76:79], v[32:35], v[56:59]\n" \
  "ds_read_b128 v[48:51], v10 offset:1024\n" \
  "v_mfma_f32_16x16x32_bf16 v[68:71], a[76:79], v[36:39], v[68:71]\n" \
  "ds_read_b128 v[52:55], v10 offset:21504\n" \
  "v_mfma_f32_16x16x32_bf16 v[60:63], a[156:159], v[32:35], v[60:63]\n" \
  "s_cmp_eq_u32 s72, 64\n" \
  "v_mfma_f32_16x16x32_bf16 v[72:75], a[156:159], v[36:39], v[72:75]\n" \
  "v_mfma_f32_16x16x32_bf16 v[64:67], a[236:239], v[32:35], v[64:67]\n" \
  "v_mfma_f32_16x16x32_bf16 v[76:79], a[236:239], v[36:39], v[76:79]\n" \
  "s_waitcnt lgkmcnt(2)\n" \
  "v_mfma_f32_16x16x32_bf16 v[100:103], a[0:3], v[40:43], 0\n" \
  "ds_read_b128 v[32:35], v10 offset:2048\n" \
  "v_mfma_f32_16x16x32_bf16 v[112:115], a[0:3], v[44:47], 0\n" \
  "ds_read_b128 v[36:39], v10 offset:22528\n" \
  "v_mfma_f32_16x16x32_bf16 v[104:107], a[80:83], v[40:43], 0\n" \
  "v_cvt_pk_bf16_f32 v16, v56, v57\n" \
  "v_mfma_f32_16x16x32_bf16 v[116:119], a[80:83], v[44:47], 0\n" \
  "v_cvt_pk_bf16_f32 v17, v58, v59\n" \
  "v_mfma_f32_16x16x32_bf16 v[108:111], a[160:163], v[40:43], 0\n" \
  "v_cvt_pk_bf16_f32 v18, v60, v61\n" \
  "v_mfma_f32_16x16x32_bf16 v[120:123], a[160:163], v[44:47], 0\n" \
  "v_cvt_pk_bf16_f32 v19, v62, v63\n" \
  "s_waitcnt lgkmcnt(2)\n" \
  "s_barrier\n" \
  "v_mfma_f32_16x16x32_bf16 v[100:103], a[4:7], v[48:51], v[100:103]\n" \
  "ds_read_b128 v[40:43], v10 offset:3072\n" \
  "v_mfma_f32_16x16x32_bf16 v[112:115], a[4:7], v[52:55], v[112:115]\n" \
  "ds_read_b128 v[44:47], v10 offset:23552\n" \
  "v_mfma_f32_16x16x32_bf16 v[104:107], a[84:87], v[48:51], v[104:107]\n" \
  "buffer_store_dwordx4 v[16:19], v2, s[40:43], s69 offen offset:0 sc1\n" \
  "v_mfma_f32_16x16x32_bf16 v[116:119], a[84:87], v[52:55], v[116:119]\n" \
  "v_cvt_pk_bf16_f32 v20, v68, v69\n" \
  "v_mfma_f32_16x16x32_bf16 v[108:111], a[164:167], v[48:51], v[108:111]\n" \
  "v_cvt_pk_bf16_f32 v21, v70, v71\n" \
  "v_mfma_f32_16x16x32_bf16 v[120:123], a[164:167], v[52:55], v[120:123]\n" \
  "v_cvt_pk_bf16_f32 v22, v72, v73\n" \
  "s_waitcnt lgkmcnt(2)\n" \
  "v_mfma_f32_16x16x32_bf16 v[100:103], a[8:11], v[32:35], v[100:103]\n" \
  "ds_read_b128 v[48:51], v10 offset:4096\n" \
  "v_mfma_f32_16x16x32_bf16 v[112:115], a[8:11], v[36:39], v[112:115]\n" \
  "ds_read_b128 v[52:55], v10 offset:24576\n" \
  "v_mfma_f32_16x16x32_bf16 v[104:107], a[88:91], v[32:35], v[104:107]\n" \
  "v_cvt_pk_bf16_f32 v23, v74, v75\n" \
  "v_mfma_f32_16x16x32_bf16 v[116:119], a[88:91], v[36:39], v[116:119]\n" \
  "buffer_store_dwordx4 v[20:23], v2, s[40:43], s69 offen offset:1024 sc1\n" \
  "v_mfma_f32_16x16x32_bf16 v[108:111], a[168:171], v[32:35], v[108:111]\n" \
  "v_cvt_pk_bf16_f32 v24, v64, v65\n" \
  "v_mfma_f32_16x16x32_bf16 v[120:123], a[168:171], v[36:39], v[120:123]\n" \
  "v_cvt_pk_bf16_f32 v25, v66, v67\n" \
  "s_waitcnt lgkmcnt(2)\n" \
  "v_mfma_f32_16x16x32_bf16 v[100:103], a[12:15], v[40:43], v[100:103]\n" \
  "ds_read_b128 v[32:35], v10 offset:5120\n" \
  "v_mfma_f32_16x16x32_bf16 v[112:115], a[12:15], v[44:47], v[112:115]\n" \
  "ds_read_b128 v[36:39], v10 offset:25600\n" \
  "v_mfma_f32_16x16x32_bf16 v[104:107], a[92:95], v[40:43], v[104:107]\n" \
  "v_cvt_pk_bf16_f32 v26, v76, v77\n" \
  "v_mfma_f32_16x16x32_bf16 v[116:119], a[92:95], v[44:47], v[116:119]\n" \
  "v_cvt_pk_bf16_f32 v27, v78, v79\n" \
  "v_mfma_f32_16x16x32_bf16 v[108:111], a[172:175], v[40:43], v[108:111]\n" \
  "buffer_store_dwordx4 v[24:27], v2, s[40:43], s69 offen offset:2048 sc1\n" \
  "v_mfma_f32_16x16x32_bf16 v[120:123], a[172:175], v[44:47], v[120:123]\n" \
  "s_waitcnt lgkmcnt(2)\n" \
  "v_mfma_f32_16x16x32_bf16 v[100:103], a[16:19], v[48:51], v[100:103]\n" \
  "ds_read_b128 v[40:43], v10 offset:6144\n" \
  "v_mfma_f32_16x16x32_bf16 v[112:115], a[16:19], v[52:55], v[112:115]\n" \
  "ds_read_b128 v[44:47], v10 offset:26624\n" \
  "v_mfma_f32_16x16x32_bf16 v[104:107], a[96:99], v[48:51], v[104:107]\n" \
  "buffer_store_dwordx4 v[12:15], v5, s[40:43], s73 offen offset:0 sc1\n" \
  "v_mfma_f32_16x16x32_bf16 v[116:119], a[96:99], v[52:55], v[116:119]\n" \
  "v_mfma_f32_16x16x32_bf16 v[108:111], a[176:179], v[48:51], v[108:111]\n" \
  "v_mfma_f32_16x16x32_bf16 v[120:123], a[176:179], v[52:55], v[120:123]\n" \
  "s_waitcnt lgkmcnt(2)\n" \
  "v_mfma_f32_16x16x32_bf16 v[100:103], a[20:23], v[32:35], v[100:103]\n" \
  "ds_read_b128 v[48:51], v10 offset:7168\n" \
  "v_mfma_f32_16x16x32_bf16 v[112:115], a[20:23], v[36:39], v[112:115]\n" \
  "ds_read_b128 v[52:55], v10 offset:27648\n" \
  "v_mfma_f32_16x16x32_bf16 v[104:107], a[100:103], v[32:35], v[104:107]\n" \
  "buffer_store_dwordx4 v[12:15], v5, s[40:43], s73 offen offset:1024 sc1\n" \
  "v_mfma_f32_16x16x32_bf16 v[116:119], a[100:103], v[36:39], v[116:119]\n" \
  "v_mfma_f32_16x16x32_bf16 v[108:111], a[180:183], v[32:35], v[108:111]\n" \
  "v_mfma_f32_16x16x32_bf16 v[120:123], a[180:183], v[36:39], v[120:123]\n" \
  "s_waitcnt lgkmcnt(2)\n" \
  "v_mfma_f32_16x16x32_bf16 v[100:103], a[24:27], v[40:43], v[100:103]\n" \
  "ds_read_b128 v[32:35], v10 offset:8192\n" \
  "v_mfma_f32_16x16x32_bf16 v[112:115], a[24:27], v[44:47], v[112:115]\n" \
  "ds_read_b128 v[36:39], v10 offset:28672\n" \
  "v_mfma_f32_16x16x32_bf16 v[104:107], a[104:107], v[40:43], v[104:107]\n" \
  "buffer_store_dwordx4 v[12:15], v5, s[40:43], s73 offen offset:2048 sc1\n" \
  "v_mfma_f32_16x16x32_bf16 v[116:119], a[104:107], v[44:47], v[116:119]\n" \
  "s_nop 0\n" \
  "v_mfma_f32_16x16x32_bf16 v[108:111], a[184:187], v[40:43], v[108:111]\n" \
  "buffer_store_dwordx4 v[12:15], v5, s[40:43], s73 offen offset:3072 sc1\n" \
  "v_mfma_f32_16x16x32_bf16 v[120:123], a[184:187], v[44:47], v[120:123]\n" \
  "s_waitcnt lgkmcnt(2)\n" \
  "v_mfma_f32_16x16x32_bf16 v[100:103], a[28:31], v[48:51], v[100:103]\n" \
  "ds_read_b128 v[40:43], v10 offset:9216\n" \
  "v_mfma_f32_16x16x32_bf16 v[112:115], a[28:31], v[52:55], v[112:115]\n" \
  "ds_read_b128 v[44:47], v10 offset:29696\n" \
  "v_mfma_f32_16x16x32_bf16 v[104:107], a[108:111], v[48:51], v[104:107]\n" \
  "s_add_u32 s70, s68, 4096\n" \
  "v_mfma_f32_16x16x32_bf16 v[116:119], a[108:111], v[52:55], v[116:119]\n" \
  "s_add_u32 s71, s68, 8192\n" \
  "v_mfma_f32_16x16x32_bf16 v[108:111], a[188:191], v[48:51], v[108:111]\n" \
  "s_add_u32 m0, s53, 40960\n" \
  "v_mfma_f32_16x16x32_bf16 v[120:123], a[188:191], v[52:55], v[120:123]\n" \
  "buffer_load_dwordx4 v1, s[36:39], s68 offen sc1 lds\n" \
  "s_waitcnt lgkmcnt(2)\n" \
  "v_mfma_f32_16x16x32_bf16 v[100:103], a[32:35], v[32:35], v[100:103]\n" \
  "ds_read_b128 v[48:51], v10 offset:10240\n" \
  "v_mfma_f32_16x16x32_bf16 v[112:115], a[32:35], v[36:39], v[112:115]\n" \
  "ds_read_b128 v[52:55], v10 offset:30720\n" \
  "v_mfma_f32_16x16x32_bf16 v[104:107], a[112:115], v[32:35], v[104:107]\n" \
  "buffer_load_dwordx4 v1, s[36:39], s68 offen offset:1024 sc1 lds\n" \
  "v_mfma_f32_16x16x32_bf16 v[116:119], a[112:115], v[36:39], v[116:119]\n" \
  "v_mfma_f32_16x16x32_bf16 v[108:111], a[192:195], v[32:35], v[108:111]\n" \
  "v_mfma_f32_16x16x32_bf16 v[120:123], a[192:195], v[36:39], v[120:123]\n" \
  "s_waitcnt lgkmcnt(2)\n" \
  "v_mfma_f32_16x16x32_bf16 v[100:103], a[36:39], v[40:43], v[100:103]\n" \
  "ds_read_b128 v[32:35], v10 offset:11264\n" \
  "v_mfma_f32_16x16x32_bf16 v[112:115], a[36:39], v[44:47], v[112:115]\n" \
  "ds_read_b128 v[36:39], v10 offset:31744\n" \
  "v_mfma_f32_16x16x32_bf16 v[104:107], a[116:119], v[40:43], v[104:107]\n" \
  "buffer_load_dwordx4 v1, s[36:39], s68 offen offset:2048 sc1 lds\n" \
  "v_mfma_f32_16x16x32_bf16 v[116:119], a[116:119], v[44:47], v[116:119]\n" \
  "v_mfma_f32_16x16x32_bf16 v[108:111], a[196:199], v[40:43], v[108:111]\n" \
  "v_mfma_f32_16x16x32_bf16 v[120:123], a[196:199], v[44:47], v[120:123]\n" \
  "s_waitcnt lgkmcnt(2)\n" \
  "v_mfma_f32_16x16x32_bf16 v[100:103], a[40:43], v[48:51], v[100:103]\n" \
  "ds_read_b128 v[40:43], v10 offset:12288\n" \
  "v_mfma_f32_16x16x32_bf16 v[112:115], a[40:43], v[52:55], v[112:115]\n" \
  "ds_read_b128 v[44:47], v10 offset:32768\n" \
  "v_mfma_f32_16x16x32_bf16 v[104:107], a[120:123], v[48:51], v[104:107]\n" \
  "buffer_load_dwordx4 v1, s[36:39], s68 offen offset:3072 sc1 lds\n" \
  "v_mfma_f32_16x16x32_bf16 v[116:119], a[120:123], v[52:55], v[116:119]\n" \
  "v_mfma_f32_16x16x32_bf16 v[108:111], a[200:203], v[48:51], v[108:111]\n" \
  "v_mfma_f32_16x16x32_bf16 v[120:123], a[200:203], v[52:55], v[120:123]\n" \
  "s_waitcnt lgkmcnt(2)\n" \
  "v_mfma_f32_16x16x32_bf16 v[100:103], a[44:47], v[32:35], v[100:103]\n" \
  "ds_read_b128 v[48:51], v10 offset:13312\n" \
  "v_mfma_f32_16x16x32_bf16 v[112:115], a[44:47], v[36:39], v[112:115]\n" \
  "ds_read_b128 v[52:55], v10 offset:33792\n" \
  "v_mfma_f32_16x16x32_bf16 v[104:107], a[124:127], v[32:35], v[104:107]\n" \
  "s_add_u32 m0, s53, 45056\n" \
  "v_mfma_f32_16x16x32_bf16 v[116:119], a[124:127], v[36:39], v[116:119]\n" \
  "buffer_load_dwordx4 v1, s[36:39], s70 offen sc1 lds\n" \
  "v_mfma_f32_16x16x32_bf16 v[108:111], a[204:207], v[32:35], v[108:111]\n" \
  "v_mfma_f32_16x16x32_bf16 v[120:123], a[204:207], v[36:39], v[120:123]\n" \
  "s_waitcnt lgkmcnt(2)\n" \
  "v_mfma_f32_16x16x32_bf16 v[100:103], a[48:51], v[40:43], v[100:103]\n" \
  "ds_read_b128 v[32:35], v10 offset:14336\n" \
  "v_mfma_f32_16x16x32_bf16 v[112:115], a[48:51], v[44:47], v[112:115]\n" \
  "ds_read_b128 v[36:39], v10 offset:34816\n" \
  "v_mfma_f32_16x16x32_bf16 v[104:107], a[128:131], v[40:43], v[104:107]\n" \
  "buffer_load_dwordx4 v1, s[36:39], s70 offen offset:1024 sc1 lds\n" \
  "v_mfma_f32_16x16x32_bf16 v[116:119], a[128:131], v[44:47], v[116:119]\n" \
  "v_mfma_f32_16x16x32_bf16 v[108:111], a[208:211], v[40:43], v[108:111]\n" \
  "v_mfma_f32_16x16x32_bf16 v[120:123], a[208:211], v[44:47], v[120:123]\n" \
  "s_waitcnt vmcnt(6) lgkmcnt(2)\n" \
  "s_barrier\n" \
  "v_mfma_f32_16x16x32_bf16 v[100:103], a[52:55], v[48:51], v[100:103]\n" \
  "ds_read_b128 v[40:43], v10 offset:15360\n" \
  "v_mfma_f32_16x16x32_bf16 v[112:115], a[52:55], v[52:55], v[112:115]\n" \
  "ds_read_b128 v[44:47], v10 offset:35840\n" \
  "v_mfma_f32_16x16x32_bf16 v[104:107], a[132:135], v[48:51], v[104:107]\n" \
  "buffer_atomic_add v11, v4, s[40:43], s54 offen\n" \
  "v_mfma_f32_16x16x32_bf16 v[116:119], a[132:135], v[52:55], v[116:119]\n" \
  "s_add_u32 s54, s54, s55\n" \
  "v_mfma_f32_16x16x32_bf16 v[108:111], a[212:215], v[48:51], v[108:111]\n" \
  "s_and_b32 s54, s54, 0xffffdfff\n" \
  "v_mfma_f32_16x16x32_bf16 v[120:123], a[212:215], v[52:55], v[120:123]\n" \
  "s_waitcnt lgkmcnt(2)\n" \
  "v_mfma_f32_16x16x32_bf16 v[100:103], a[56:59], v[32:35], v[100:103]\n" \
  "ds_read_b128 v[48:51], v10 offset:16384\n" \
  "v_mfma_f32_16x16x32_bf16 v[112:115], a[56:59], v[36:39], v[112:115]\n" \
  "ds_read_b128 v[52:55], v10 offset:36864\n" \
  "v_mfma_f32_16x16x32_bf16 v[104:107], a[136:139], v[32:35], v[104:107]\n" \
  "buffer_load_dwordx4 v1, s[36:39], s70 offen offset:2048 sc1 lds\n" \
  "v_mfma_f32_16x16x32_bf16 v[116:119], a[136:139], v[36:39], v[116:119]\n" \
  "v_readlane_b32 s69, v3, s72\n" \
  "v_mfma_f32_16x16x32_bf16 v[108:111], a[216:219], v[32:35], v[108:111]\n" \
  "v_readlane_b32 s73, v6, s72\n" \
  "v_mfma_f32_16x16x32_bf16 v[120:123], a[216:219], v[36:39], v[120:123]\n" \
  "s_add_u32 s72, s72, 1\n" \
  "s_waitcnt lgkmcnt(2)\n" \
  "v_mfma_f32_16x16x32_bf16 v[100:103], a[60:63], v[40:43], v[100:103]\n" \
  "ds_read_b128 v[32:35], v10 offset:17408\n" \
  "v_mfma_f32_16x16x32_bf16 v[112:115], a[60:63], v[44:47], v[112:115]\n" \
  "ds_read_b128 v[36:39], v10 offset:37888\n" \
  "v_mfma_f32_16x16x32_bf16 v[104:107], a[140:143], v[40:43], v[104:107]\n" \
  "buffer_load_dwordx4 v1, s[36:39], s70 offen offset:3072 sc1 lds\n" \
  "v_mfma_f32_16x16x32_bf16 v[116:119], a[140:143], v[44:47], v[116:119]\n" \
  "v_mfma_f32_16x16x32_bf16 v[108:111], a[220:223], v[40:43], v[108:111]\n" \
  "v_mfma_f32_16x16x32_bf16 v[120:123], a[220:223], v[44:47], v[120:123]\n" \
  "s_waitcnt lgkmcnt(2)\n" \
  "v_mfma_f32_16x16x32_bf16 v[100:103], a[64:67], v[48:51], v[100:103]\n" \
  "ds_read_b128 v[40:43], v10 offset:18432\n" \
  "v_mfma_f32_16x16x32_bf16 v[112:115], a[64:67], v[52:55], v[112:115]\n" \
  "ds_read_b128 v[44:47], v10 offset:38912\n" \
  "v_mfma_f32_16x16x32_bf16 v[104:107], a[144:147], v[48:51], v[104:107]\n" \
  "s_add_u32 m0, s53, 49152\n" \
  "v_mfma_f32_16x16x32_bf16 v[116:119], a[144:147], v[52:55], v[116:119]\n" \
  "buffer_load_dwordx4 v1, s[36:39], s71 offen sc1 lds\n" \
  "v_mfma_f32_16x16x32_bf16 v[108:111], a[224:227], v[48:51], v[108:111]\n" \
  "v_mfma_f32_16x16x32_bf16 v[120:123], a[224:227], v[52:55], v[120:123]\n" \
  "s_waitcnt lgkmcnt(2)\n" \
  "v_mfma_f32_16x16x32_bf16 v[100:103], a[68:71], v[32:35], v[100:103]\n" \
  "ds_read_b128 v[48:51], v10 offset:19456\n" \
  "v_mfma_f32_16x16x32_bf16 v[112:115], a[68:71], v[36:39], v[112:115]\n" \
  "ds_read_b128 v[52:55], v10 offset:39936\n" \
  "v_mfma_f32_16x16x32_bf16 v[104:107], a[148:151], v[32:35], v[104:107]\n" \
  "buffer_load_dwordx4 v1, s[36:39], s71 offen offset:1024 sc1 lds\n" \
  "v_mfma_f32_16x16x32_bf16 v[116:119], a[148:151], v[36:39], v[116:119]\n" \
  "s_add_u32 s68, s68, 40960\n" \
  "v_mfma_f32_16x16x32_bf16 v[108:111], a[228:231], v[32:35], v[108:111]\n" \
  "v_mfma_f32_16x16x32_bf16 v[120:123], a[228:231], v[36:39], v[120:123]\n" \
  "s_waitcnt vmcnt(11) lgkmcnt(2)\n" \
  "s_barrier\n" \
  "v_mfma_f32_16x16x32_bf16 v[100:103], a[72:75], v[40:43], v[100:103]\n" \
  "v_mfma_f32_16x16x32_bf16 v[112:115], a[72:75], v[44:47], v[112:115]\n" \
  "v_mfma_f32_16x16x32_bf16 v[104:107], a[152:155], v[40:43], v[104:107]\n" \
  "v_mfma_f32_16x16x32_bf16 v[116:119], a[152:155], v[44:47], v[116:119]\n" \
  "v_mfma_f32_16x16x32_bf16 v[108:111], a[232:235], v[40:43], v[108:111]\n" \
  "v_mfma_f32_16x16x32_bf16 v[120:123], a[232:235], v[44:47], v[120:123]\n" \
  "s_waitcnt lgkmcnt(0)\n" \
  "v_mfma_f32_16x16x32_bf16 v[100:103], a[76:79], v[48:51], v[100:103]\n" \
  "s_cmp_eq_u32 s72, 64\n" \
  "v_mfma_f32_16x16x32_bf16 v[112:115], a[76:79], v[52:55], v[112:115]\n" \
  "v_mfma_f32_16x16x32_bf16 v[104:107], a[156:159], v[48:51], v[104:107]\n" \
  "v_mfma_f32_16x16x32_bf16 v[116:119], a[156:159], v[52:55], v[116:119]\n" \
  "v_mfma_f32_16x16x32_bf16 v[108:111], a[236:239], v[48:51], v[108:111]\n" \
  "v_mfma_f32_16x16x32_bf16 v[120:123], a[236:239], v[52:55], v[120:123]\n" \
  "LPE_%=:\n" \
  "s_nop 6\n" \
  "v_cvt_pk_bf16_f32 v16, v100, v101\n" \
  "v_cvt_pk_bf16_f32 v17, v102, v103\n" \
  "v_cvt_pk_bf16_f32 v18, v104, v105\n" \
  "v_cvt_pk_bf16_f32 v19, v106, v107\n" \
  "buffer_store_dwordx4 v[16:19], v2, s[40:43], s69 offen offset:0 sc1\n" \
  "v_cvt_pk_bf16_f32 v20, v112, v113\n" \
  "v_cvt_pk_bf16_f32 v21, v114, v115\n" \
  "v_cvt_pk_bf16_f32 v22, v116, v117\n" \
  "v_cvt_pk_bf16_f32 v23, v118, v119\n" \
  "buffer_store_dwordx4 v[20:23], v2, s[40:43], s69 offen offset:1024 sc1\n" \
  "v_cvt_pk_bf16_f32 v24, v108, v109\n" \
  "v_cvt_pk_bf16_f32 v25, v110, v111\n" \
  "v_cvt_pk_bf16_f32 v26, v120, v121\n" \
  "v_cvt_pk_bf16_f32 v27, v122, v123\n" \
  "buffer_store_dwordx4 v[24:27], v2, s[40:43], s69 offen offset:2048 sc1\n" \
  "buffer_store_dwordx4 v[12:15], v5, s[40:43], s73 offen offset:0 sc1\n" \
  "buffer_store_dwordx4 v[12:15], v5, s[40:43], s73 offen offset:1024 sc1\n" \
  "buffer_store_dwordx4 v[12:15], v5, s[40:43], s73 offen offset:2048 sc1\n" \
  "buffer_store_dwordx4 v[12:15], v5, s[40:43], s73 offen offset:3072 sc1\n" \
  "s_waitcnt vmcnt(0) lgkmcnt(0)\n" \
  "s_barrier\n" \
  "buffer_atomic_add v11, v4, s[40:43], s54 offen\n" \
  "s_add_u32 s54, s54, s55\n" \
  "s_and_b32 s54, s54, 0xffffdfff\n"
#define VO_CLOBBERS \
  "v16", "v17", "v18", "v19", "v20", "v21", "v22", "v23", "v24", "v25", "v26", "v27", "v28", "v29", "v30", "v31", "v32", "v33", "v34", "v35", "v36", \
  "v37", "v38", "v39", "v40", "v41", "v42", "v43", "v44", "v45", "v46", "v47", "v48", "v49", "v50", "v51", "v52", "v53", "v54", "v55", "v56", "v57", \
  "v58", "v59", "v60", "v61", "v62", "v63", "v64", "v65", "v66", "v67", "v68", "v69", "v70", "v71", "v72", "v73", "v74", "v75", "v76", "v77", "v78", \
  "v79", "v80", "v81", "v82", "v83", "v84", "v85", "v86", "v87", "v88", "v89", "v90", "v91", "v100", "v101", "v102", "v103", "v104", "v105", "v106", \
  "v107", "v108", "v109", "v110", "v111", "v112", "v113", "v114", "v115", "v116", "v117", "v118", "v119", "v120", "v121", "v122", "v123", "v124", \
  "v125", "v126", "v127", "v128", "v129", "v130", "v131", "v132", "v133", "v134", "v135", "v136", "v137", "v138", "v139", "v140", "v141", "v142", \
  "v143", "v144", "v145", "v146", "v147", "s41", "s42", "s43", "s44", "s45", "s46", "s47", "s48", "s49", "s50"
#define VO_ASM \
  "v_mov_b32 v91, 1\n" \
  "s_mov_b32 s41, 0\n" \
  "s_mov_b32 s48, 0\n" \
  "s_mov_b32 s47, 1073741824\n" \
  "s_mov_b32 s44, 1073741824\n" \
  "s_mov_b32 s49, 1073741824\n" \
  "v_readlane_b32 s42, v4, 0\n" \
  "s_add_u32 s46, s41, 0\n" \
  "s_min_u32 s46, s46, 63\n" \
  "v_mad_u32_u24 v88, v8, s46, v7\n" \
  "v_mad_u32_u24 v89, v9, s46, v10\n" \
  "buffer_load_dword v90, v88, s[36:39], 0 offen sc1\n" \
  "s_waitcnt vmcnt(0)\n" \
  "v_cmp_gt_u32 vcc, v89, v90\n" \
  "s_cbranch_vccnz LPS_%=\n" \
  "LKS_%=:\n" \
  "s_add_u32 m0, s40, 0\n" \
  "s_nop 0\n" \
  "buffer_load_dwordx4 v1, s[36:39], s42 offen offset:0 sc1 lds\n" \
  "buffer_load_dwordx4 v1, s[36:39], s42 offen offset:1024 sc1 lds\n" \
  "buffer_load_dwordx4 v1, s[36:39], s42 offen offset:2048 sc1 lds\n" \
  "s_waitcnt vmcnt(0)\n" \
  "s_barrier\n" \
  "ds_read_b128 v[40:43], v0 offset:0\n" \
  "ds_read_b128 v[44:47], v0 offset:3072\n" \
  "ds_read_b128 v[48:51], v0 offset:6144\n" \
  "ds_read_b128 v[52:55], v0 offset:9216\n" \
  "ds_read_b64 v[56:57], v0 offset:2048\n" \
  "ds_read_b64 v[58:59], v0 offset:5120\n" \
  "ds_read_b64 v[60:61], v0 offset:8192\n" \
  "ds_read_b64 v[62:63], v0 offset:11264\n" \
  "s_waitcnt lgkmcnt(0)\n" \
  "ds_read_b128 v[64:67], v0 offset:1024\n" \
  "ds_read_b128 v[68:71], v0 offset:4096\n" \
  "ds_read_b128 v[72:75], v0 offset:7168\n" \
  "ds_read_b128 v[76:79], v0 offset:10240\n" \
  "ds_read_b64 v[80:81], v0 offset:2056\n" \
  "ds_read_b64 v[82:83], v0 offset:5128\n" \
  "ds_read_b64 v[84:85], v0 offset:8200\n" \
  "ds_read_b64 v[86:87], v0 offset:11272\n" \
  "s_waitcnt lgkmcnt(0)\n" \
  "LL_%=:\n" \
  "v_mfma_f32_16x16x32_bf16 v[24:27], v[40:43], a[0:3], 0\n" \
  "s_add_u32 s46, s41, 1\n" \
  "s_min_u32 s46, s46, 63\n" \
  "v_mad_u32_u24 v88, v8, s46, v7\n" \
  "v_mfma_f32_16x16x32_bf16 v[28:31], v[64:67], a[0:3], 0\n" \
  "v_mad_u32_u24 v89, v9, s46, v10\n" \
  "v_mfma_f32_16x16x32_bf16 v[24:27], v[44:47], a[4:7], v[24:27]\n" \
  "v_cvt_pk_bf16_f32 v20, v32, v33\n" \
  "v_cvt_pk_bf16_f32 v21, v34, v35\n" \
  "v_mfma_f32_16x16x32_bf16 v[28:31], v[68:71], a[4:7], v[28:31]\n" \
  "v_cvt_pk_bf16_f32 v22, v36, v37\n" \
  "v_cvt_pk_bf16_f32 v23, v38, v39\n" \
  "v_mfma_f32_16x16x32_bf16 v[24:27], v[48:51], a[8:11], v[24:27]\n" \
  "buffer_store_dwordx4 v[20:23], v3, s[36:39], s44 offen sc1\n" \
  "v_mfma_f32_16x16x32_bf16 v[28:31], v[72:75], a[8:11], v[28:31]\n" \
  "s_xor_b32 s50, s44, 0x2000\n" \
  "s_add_u32 s44, s44, 0x40000\n" \
  "v_mfma_f32_16x16x32_bf16 v[24:27], v[52:55], a[12:15], v[24:27]\n" \
  "v_readlane_b32 s43, v5, s41\n" \
  "v_mfma_f32_16x16x32_bf16 v[28:31], v[76:79], a[12:15], v[28:31]\n" \
  "buffer_load_dword v90, v88, s[36:39], 0 offen sc1\n" \
  "v_mfma_f32_16x16x32_bf16 v[24:27], v[56:59], a[16:19], v[24:27]\n" \
  "v_readlane_b32 s44, v6, s41\n" \
  "v_mfma_f32_16x16x32_bf16 v[28:31], v[80:83], a[16:19], v[28:31]\n" \
  "v_readlane_b32 s42, v4, s46\n" \
  "v_mfma_f32_16x16x32_bf16 v[24:27], v[60:63], a[20:23], v[24:27]\n" \
  "buffer_store_dwordx4 v[20:23], v3, s[36:39], s50 offen sc1\n" \
  "v_mfma_f32_16x16x32_bf16 v[28:31], v[84:87], a[20:23], v[28:31]\n" \
  "v_mfma_f32_16x16x32_bf16 v[32:35], v[40:43], a[24:27], 0\n" \
  "v_mfma_f32_16x16x32_bf16 v[36:39], v[64:67], a[24:27], 0\n" \
  "v_mfma_f32_16x16x32_bf16 v[32:35], v[44:47], a[28:31], v[32:35]\n" \
  "v_cvt_pk_bf16_f32 v16, v24, v25\n" \
  "v_cvt_pk_bf16_f32 v17, v26, v27\n" \
  "v_mfma_f32_16x16x32_bf16 v[36:39], v[68:71], a[28:31], v[36:39]\n" \
  "v_cvt_pk_bf16_f32 v18, v28, v29\n" \
  "v_cvt_pk_bf16_f32 v19, v30, v31\n" \
  "v_mfma_f32_16x16x32_bf16 v[32:35], v[48:51], a[32:35], v[32:35]\n" \
  "buffer_store_dwordx4 v[16:19], v2, s[36:39], s43 offen sc1\n" \
  "v_mfma_f32_16x16x32_bf16 v[36:39], v[72:75], a[32:35], v[36:39]\n" \
  "s_xor_b32 s50, s43, 0x2000\n" \
  "s_add_u32 s43, s43, 0x40000\n" \
  "v_mfma_f32_16x16x32_bf16 v[32:35], v[52:55], a[36:39], v[32:35]\n" \
  "v_mfma_f32_16x16x32_bf16 v[36:39], v[76:79], a[36:39], v[36:39]\n" \
  "v_mfma_f32_16x16x32_bf16 v[32:35], v[56:59], a[40:43], v[32:35]\n" \
  "v_mfma_f32_16x16x32_bf16 v[36:39], v[80:83], a[40:43], v[36:39]\n" \
  "v_mfma_f32_16x16x32_bf16 v[32:35], v[60:63], a[44:47], v[32:35]\n" \
  "buffer_store_dwordx4 v[16:19], v2, s[36:39], s50 offen sc1\n" \
  "v_mfma_f32_16x16x32_bf16 v[36:39], v[84:87], a[44:47], v[36:39]\n" \
  "v_mfma_f32_16x16x32_bf16 v[24:27], v[40:43], a[48:51], 0\n" \
  "v_mfma_f32_16x16x32_bf16 v[28:31], v[64:67], a[48:51], 0\n" \
  "v_mfma_f32_16x16x32_bf16 v[24:27], v[44:47], a[52:55], v[24:27]\n" \
  "v_cvt_pk_bf16_f32 v20, v32, v33\n" \
  "v_cvt_pk_bf16_f32 v21, v34, v35\n" \
  "v_mfma_f32_16x16x32_bf16 v[28:31], v[68:71], a[52:55], v[28:31]\n" \
  "v_cvt_pk_bf16_f32 v22, v36, v37\n" \
  "v_cvt_pk_bf16_f32 v23, v38, v39\n" \
  "v_mfma_f32_16x16x32_bf16 v[24:27], v[48:51], a[56:59], v[24:27]\n" \
  "buffer_store_dwordx4 v[20:23], v2, s[36:39], s43 offen sc1\n" \
  "v_mfma_f32_16x16x32_bf16 v[28:31], v[72:75], a[56:59], v[28:31]\n" \
  "s_xor_b32 s50, s43, 0x2000\n" \
  "s_add_u32 s43, s43, 0x40000\n" \
  "v_mfma_f32_16x16x32_bf16 v[24:27], v[52:55], a[60:63], v[24:27]\n" \
  "v_mfma_f32_16x16x32_bf16 v[28:31], v[76:79], a[60:63], v[28:31]\n" \
  "v_mfma_f32_16x16x32_bf16 v[24:27], v[56:59], a[64:67], v[24:27]\n" \
  "v_mfma_f32_16x16x32_bf16 v[28:31], v[80:83], a[64:67], v[28:31]\n" \
  "v_mfma_f32_16x16x32_bf16 v[24:27], v[60:63], a[68:71], v[24:27]\n" \
  "buffer_store_dwordx4 v[20:23], v2, s[36:39], s50 offen sc1\n" \
  "v_mfma_f32_16x16x32_bf16 v[28:31], v[84:87], a[68:71], v[28:31]\n" \
  "v_mfma_f32_16x16x32_bf16 v[32:35], v[40:43], a[72:75], 0\n" \
  "v_mfma_f32_16x16x32_bf16 v[36:39], v[64:67], a[72:75], 0\n" \
  "v_mfma_f32_16x16x32_bf16 v[32:35], v[44:47], a[76:79], v[32:35]\n" \
  "v_cvt_pk_bf16_f32 v16, v24, v25\n" \
  "v_cvt_pk_bf16_f32 v17, v26, v27\n" \
  "v_mfma_f32_16x16x32_bf16 v[36:39], v[68:71], a[76:79], v[36:39]\n" \
  "v_cvt_pk_bf16_f32 v18, v28, v29\n" \
  "v_cvt_pk_bf16_f32 v19, v30, v31\n" \
  "v_mfma_f32_16x16x32_bf16 v[32:35], v[48:51], a[80:83], v[32:35]\n" \
  "buffer_store_dwordx4 v[16:19], v2, s[36:39], s43 offen sc1\n" \
  "v_mfma_f32_16x16x32_bf16 v[36:39], v[72:75], a[80:83], v[36:39]\n" \
  "s_xor_b32 s50, s43, 0x2000\n" \
  "s_add_u32 s43, s43, 0x40000\n" \
  "v_mfma_f32_16x16x32_bf16 v[32:35], v[52:55], a[84:87], v[32:35]\n" \
  "v_mfma_f32_16x16x32_bf16 v[36:39], v[76:79], a[84:87], v[36:39]\n" \
  "v_mfma_f32_16x16x32_bf16 v[32:35], v[56:59], a[88:91], v[32:35]\n" \
  "v_mfma_f32_16x16x32_bf16 v[36:39], v[80:83], a[88:91], v[36:39]\n" \
  "v_mfma_f32_16x16x32_bf16 v[32:35], v[60:63], a[92:95], v[32:35]\n" \
  "buffer_store_dwordx4 v[16:19], v2, s[36:39], s50 offen sc1\n" \
  "v_mfma_f32_16x16x32_bf16 v[36:39], v[84:87], a[92:95], v[36:39]\n" \
  "v_mfma_f32_16x16x32_bf16 v[24:27], v[40:43], a[96:99], 0\n" \
  "s_waitcnt vmcnt(6)\n" \
  "s_barrier\n" \
  "v_cmp_gt_u32 vcc, v89, v90\n" \
  "v_mfma_f32_16x16x32_bf16 v[28:31], v[64:67], a[96:99], 0\n" \
  "buffer_atomic_add v91, v11, s[36:39], s47 offen\n" \
  "v_mfma_f32_16x16x32_bf16 v[24:27], v[44:47], a[100:103], v[24:27]\n" \
  "v_cvt_pk_bf16_f32 v20, v32, v33\n" \
  "v_cvt_pk_bf16_f32 v21, v34, v35\n" \
  "v_mfma_f32_16x16x32_bf16 v[28:31], v[68:71], a[100:103], v[28:31]\n" \
  "v_cvt_pk_bf16_f32 v22, v36, v37\n" \
  "v_cvt_pk_bf16_f32 v23, v38, v39\n" \
  "v_mfma_f32_16x16x32_bf16 v[24:27], v[48:51], a[104:107], v[24:27]\n" \
  "buffer_store_dwordx4 v[20:23], v2, s[36:39], s43 offen sc1\n" \
  "v_mfma_f32_16x16x32_bf16 v[28:31], v[72:75], a[104:107], v[28:31]\n" \
  "s_xor_b32 s50, s43, 0x2000\n" \
  "s_add_u32 s43, s43, 0x40000\n" \
  "v_mfma_f32_16x16x32_bf16 v[24:27], v[52:55], a[108:111], v[24:27]\n" \
  "s_cbranch_vccnz LP0_%=\n" \
  "LK0_%=:\n" \
  "s_add_u32 m0, s40, 12288\n" \
  "v_mfma_f32_16x16x32_bf16 v[28:31], v[76:79], a[108:111], v[28:31]\n" \
  "buffer_load_dwordx4 v1, s[36:39], s42 offen offset:0 sc1 lds\n" \
  "v_mfma_f32_16x16x32_bf16 v[24:27], v[56:59], a[112:115], v[24:27]\n" \
  "v_mfma_f32_16x16x32_bf16 v[28:31], v[80:83], a[112:115], v[28:31]\n" \
  "buffer_load_dwordx4 v1, s[36:39], s42 offen offset:1024 sc1 lds\n" \
  "v_mfma_f32_16x16x32_bf16 v[24:27], v[60:63], a[116:119], v[24:27]\n" \
  "buffer_store_dwordx4 v[20:23], v2, s[36:39], s50 offen sc1\n" \
  "v_mfma_f32_16x16x32_bf16 v[28:31], v[84:87], a[116:119], v[28:31]\n" \
  "buffer_load_dwordx4 v1, s[36:39], s42 offen offset:2048 sc1 lds\n" \
  "v_mfma_f32_16x16x32_bf16 v[32:35], v[40:43], a[120:123], 0\n" \
  "v_mfma_f32_16x16x32_bf16 v[36:39], v[64:67], a[120:123], 0\n" \
  "buffer_atomic_add v91, v12, s[36:39], s49 offen\n" \
  "v_mfma_f32_16x16x32_bf16 v[32:35], v[44:47], a[124:127], v[32:35]\n" \
  "v_cvt_pk_bf16_f32 v16, v24, v25\n" \
  "v_cvt_pk_bf16_f32 v17, v26, v27\n" \
  "v_mfma_f32_16x16x32_bf16 v[36:39], v[68:71], a[124:127], v[36:39]\n" \
  "v_cvt_pk_bf16_f32 v18, v28, v29\n" \
  "v_cvt_pk_bf16_f32 v19, v30, v31\n" \
  "v_mfma_f32_16x16x32_bf16 v[32:35], v[48:51], a[128:131], v[32:35]\n" \
  "buffer_store_dwordx4 v[16:19], v2, s[36:39], s43 offen sc1\n" \
  "v_mfma_f32_16x16x32_bf16 v[36:39], v[72:75], a[128:131], v[36:39]\n" \
  "s_xor_b32 s50, s43, 0x2000\n" \
  "s_add_u32 s43, s43, 0x40000\n" \
  "v_mfma_f32_16x16x32_bf16 v[32:35], v[52:55], a[132:135], v[32:35]\n" \
  "s_mov_b32 s49, 0\n" \
  "s_lshr_b32 s45, s41, 4\n" \
  "s_lshl_b32 s45, s45, 13\n" \
  "v_mfma_f32_16x16x32_bf16 v[36:39], v[76:79], a[132:135], v[36:39]\n" \
  "s_lshl_b32 s47, s41, 9\n" \
  "s_add_u32 s47, s47, s45\n" \
  "v_mfma_f32_16x16x32_bf16 v[32:35], v[56:59], a[136:139], v[32:35]\n" \
  "v_mfma_f32_16x16x32_bf16 v[36:39], v[80:83], a[136:139], v[36:39]\n" \
  "v_mfma_f32_16x16x32_bf16 v[32:35], v[60:63], a[140:143], v[32:35]\n" \
  "buffer_store_dwordx4 v[16:19], v2, s[36:39], s50 offen sc1\n" \
  "v_mfma_f32_16x16x32_bf16 v[36:39], v[84:87], a[140:143], v[36:39]\n" \
  "v_mfma_f32_16x16x32_bf16 v[24:27], v[40:43], a[144:147], 0\n" \
  "v_mfma_f32_16x16x32_bf16 v[28:31], v[64:67], a[144:147], 0\n" \
  "v_mfma_f32_16x16x32_bf16 v[24:27], v[44:47], a[148:151], v[24:27]\n" \
  "v_cvt_pk_bf16_f32 v20, v32, v33\n" \
  "v_cvt_pk_bf16_f32 v21, v34, v35\n" \
  "v_mfma_f32_16x16x32_bf16 v[28:31], v[68:71], a[148:151], v[28:31]\n" \
  "v_cvt_pk_bf16_f32 v22, v36, v37\n" \
  "v_cvt_pk_bf16_f32 v23, v38, v39\n" \
  "v_mfma_f32_16x16x32_bf16 v[24:27], v[48:51], a[152:155], v[24:27]\n" \
  "buffer_store_dwordx4 v[20:23], v2, s[36:39], s43 offen sc1\n" \
  "v_mfma_f32_16x16x32_bf16 v[28:31], v[72:75], a[152:155], v[28:31]\n" \
  "s_xor_b32 s50, s43, 0x2000\n" \
  "s_add_u32 s43, s43, 0x40000\n" \
  "v_mfma_f32_16x16x32_bf16 v[24:27], v[52:55], a[156:159], v[24:27]\n" \
  "v_mfma_f32_16x16x32_bf16 v[28:31], v[76:79], a[156:159], v[28:31]\n" \
  "v_mfma_f32_16x16x32_bf16 v[24:27], v[56:59], a[160:163], v[24:27]\n" \
  "v_mfma_f32_16x16x32_bf16 v[28:31], v[80:83], a[160:163], v[28:31]\n" \
  "v_mfma_f32_16x16x32_bf16 v[24:27], v[60:63], a[164:167], v[24:27]\n" \
  "buffer_store_dwordx4 v[20:23], v2, s[36:39], s50 offen sc1\n" \
  "v_mfma_f32_16x16x32_bf16 v[28:31], v[84:87], a[164:167], v[28:31]\n" \
  "v_mfma_f32_16x16x32_bf16 v[32:35], v[40:43], a[168:171], 0\n" \
  "v_mfma_f32_16x16x32_bf16 v[36:39], v[64:67], a[168:171], 0\n" \
  "v_mfma_f32_16x16x32_bf16 v[32:35], v[44:47], a[172:175], v[32:35]\n" \
  "v_cvt_pk_bf16_f32 v16, v24, v25\n" \
  "v_cvt_pk_bf16_f32 v17, v26, v27\n" \
  "v_mfma_f32_16x16x32_bf16 v[36:39], v[68:71], a[172:175], v[36:39]\n" \
  "v_cvt_pk_bf16_f32 v18, v28, v29\n" \
  "v_cvt_pk_bf16_f32 v19, v30, v31\n" \
  "v_mfma_f32_16x16x32_bf16 v[32:35], v[48:51], a[176:179], v[32:35]\n" \
  "buffer_store_dwordx4 v[16:19], v2, s[36:39], s43 offen sc1\n" \
  "v_mfma_f32_16x16x32_bf16 v[36:39], v[72:75], a[176:179], v[36:39]\n" \
  "s_xor_b32 s50, s43, 0x2000\n" \
  "s_add_u32 s43, s43, 0x40000\n" \
  "v_mfma_f32_16x16x32_bf16 v[32:35], v[52:55], a[180:183], v[32:35]\n" \
  "v_mfma_f32_16x16x32_bf16 v[36:39], v[76:79], a[180:183], v[36:39]\n" \
  "v_mfma_f32_16x16x32_bf16 v[32:35], v[56:59], a[184:187], v[32:35]\n" \
  "v_mfma_f32_16x16x32_bf16 v[36:39], v[80:83], a[184:187], v[36:39]\n" \
  "v_mfma_f32_16x16x32_bf16 v[32:35], v[60:63], a[188:191], v[32:35]\n" \
  "buffer_store_dwordx4 v[16:19], v2, s[36:39], s50 offen sc1\n" \
  "v_mfma_f32_16x16x32_bf16 v[36:39], v[84:87], a[188:191], v[36:39]\n" \
  "v_mfma_f32_16x16x32_bf16 v[24:27], v[40:43], a[192:195], 0\n" \
  "v_mfma_f32_16x16x32_bf16 v[28:31], v[64:67], a[192:195], 0\n" \
  "v_mfma_f32_16x16x32_bf16 v[24:27], v[44:47], a[196:199], v[24:27]\n" \
  "v_cvt_pk_bf16_f32 v20, v32, v33\n" \
  "v_cvt_pk_bf16_f32 v21, v34, v35\n" \
  "v_mfma_f32_16x16x32_bf16 v[28:31], v[68:71], a[196:199], v[28:31]\n" \
  "v_cvt_pk_bf16_f32 v22, v36, v37\n" \
  "v_cvt_pk_bf16_f32 v23, v38, v39\n" \
  "v_mfma_f32_16x16x32_bf16 v[24:27], v[48:51], a[200:203], v[24:27]\n" \
  "buffer_store_dwordx4 v[20:23], v2, s[36:39], s43 offen sc1\n" \
  "v_mfma_f32_16x16x32_bf16 v[28:31], v[72:75], a[200:203], v[28:31]\n" \
  "s_xor_b32 s50, s43, 0x2000\n" \
  "s_add_u32 s43, s43, 0x40000\n" \
  "v_mfma_f32_16x16x32_bf16 v[24:27], v[52:55], a[204:207], v[24:27]\n" \
  "s_waitcnt vmcnt(8)\n" \
  "s_barrier\n" \
  "v_mfma_f32_16x16x32_bf16 v[28:31], v[76:79], a[204:207], v[28:31]\n" \
  "ds_read_b128 v[100:103], v0 offset:12288\n" \
  "ds_read_b128 v[104:107], v0 offset:15360\n" \
  "v_mfma_f32_16x16x32_bf16 v[24:27], v[56:59], a[208:211], v[24:27]\n" \
  "ds_read_b128 v[108:111], v0 offset:18432\n" \
  "ds_read_b128 v[112:115], v0 offset:21504\n" \
  "v_mfma_f32_16x16x32_bf16 v[28:31], v[80:83], a[208:211], v[28:31]\n" \
  "ds_read_b64 v[116:117], v0 offset:14336\n" \
  "ds_read_b64 v[118:119], v0 offset:17408\n" \
  "v_mfma_f32_16x16x32_bf16 v[24:27], v[60:63], a[212:215], v[24:27]\n" \
  "buffer_store_dwordx4 v[20:23], v2, s[36:39], s50 offen sc1\n" \
  "ds_read_b64 v[120:121], v0 offset:20480\n" \
  "v_mfma_f32_16x16x32_bf16 v[28:31], v[84:87], a[212:215], v[28:31]\n" \
  "ds_read_b64 v[122:123], v0 offset:23552\n" \
  "ds_read_b128 v[124:127], v0 offset:13312\n" \
  "v_mfma_f32_16x16x32_bf16 v[32:35], v[40:43], a[216:219], 0\n" \
  "ds_read_b128 v[128:131], v0 offset:16384\n" \
  "ds_read_b128 v[132:135], v0 offset:19456\n" \
  "v_mfma_f32_16x16x32_bf16 v[36:39], v[64:67], a[216:219], 0\n" \
  "ds_read_b128 v[136:139], v0 offset:22528\n" \
  "ds_read_b64 v[140:141], v0 offset:14344\n" \
  "v_mfma_f32_16x16x32_bf16 v[32:35], v[44:47], a[220:223], v[32:35]\n" \
  "v_cvt_pk_bf16_f32 v16, v24, v25\n" \
  "v_cvt_pk_bf16_f32 v17, v26, v27\n" \
  "v_mfma_f32_16x16x32_bf16 v[36:39], v[68:71], a[220:223], v[36:39]\n" \
  "v_cvt_pk_bf16_f32 v18, v28, v29\n" \
  "v_cvt_pk_bf16_f32 v19, v30, v31\n" \
  "v_mfma_f32_16x16x32_bf16 v[32:35], v[48:51], a[224:227], v[32:35]\n" \
  "buffer_store_dwordx4 v[16:19], v3, s[36:39], s44 offen sc1\n" \
  "ds_read_b64 v[142:143], v0 offset:17416\n" \
  "v_mfma_f32_16x16x32_bf16 v[36:39], v[72:75], a[224:227], v[36:39]\n" \
  "s_xor_b32 s50, s44, 0x2000\n" \
  "s_add_u32 s44, s44, 0x40000\n" \
  "ds_read_b64 v[144:145], v0 offset:20488\n" \
  "v_mfma_f32_16x16x32_bf16 v[32:35], v[52:55], a[228:231], v[32:35]\n" \
  "ds_read_b64 v[146:147], v0 offset:23560\n" \
  "v_mfma_f32_16x16x32_bf16 v[36:39], v[76:79], a[228:231], v[36:39]\n" \
  "v_mfma_f32_16x16x32_bf16 v[32:35], v[56:59], a[232:235], v[32:35]\n" \
  "v_mfma_f32_16x16x32_bf16 v[36:39], v[80:83], a[232:235], v[36:39]\n" \
  "v_mfma_f32_16x16x32_bf16 v[32:35], v[60:63], a[236:239], v[32:35]\n" \
  "buffer_store_dwordx4 v[16:19], v3, s[36:39], s50 offen sc1\n" \
  "v_mfma_f32_16x16x32_bf16 v[36:39], v[84:87], a[236:239], v[36:39]\n" \
  "s_add_u32 s41, s41, 1\n" \
  "s_waitcnt lgkmcnt(0)\n" \
  "v_mfma_f32_16x16x32_bf16 v[24:27], v[100:103], a[0:3], 0\n" \
  "s_add_u32 s46, s41, 1\n" \
  "s_min_u32 s46, s46, 63\n" \
  "v_mad_u32_u24 v88, v8, s46, v7\n" \
  "v_mfma_f32_16x16x32_bf16 v[28:31], v[124:127], a[0:3], 0\n" \
  "v_mad_u32_u24 v89, v9, s46, v10\n" \
  "v_mfma_f32_16x16x32_bf16 v[24:27], v[104:107], a[4:7], v[24:27]\n" \
  "v_cvt_pk_bf16_f32 v20, v32, v33\n" \
  "v_cvt_pk_bf16_f32 v21, v34, v35\n" \
  "v_mfma_f32_16x16x32_bf16 v[28:31], v[128:131], a[4:7], v[28:31]\n" \
  "v_cvt_pk_bf16_f32 v22, v36, v37\n" \
  "v_cvt_pk_bf16_f32 v23, v38, v39\n" \
  "v_mfma_f32_16x16x32_bf16 v[24:27], v[108:111], a[8:11], v[24:27]\n" \
  "buffer_store_dwordx4 v[20:23], v3, s[36:39], s44 offen sc1\n" \
  "v_mfma_f32_16x16x32_bf16 v[28:31], v[132:135], a[8:11], v[28:31]\n" \
  "s_xor_b32 s50, s44, 0x2000\n" \
  "s_add_u32 s44, s44, 0x40000\n" \
  "v_mfma_f32_16x16x32_bf16 v[24:27], v[112:115], a[12:15], v[24:27]\n" \
  "v_readlane_b32 s43, v5, s41\n" \
  "v_mfma_f32_16x16x32_bf16 v[28:31], v[136:139], a[12:15], v[28:31]\n" \
  "buffer_load_dword v90, v88, s[36:39], 0 offen sc1\n" \
  "v_mfma_f32_16x16x32_bf16 v[24:27], v[116:119], a[16:19], v[24:27]\n" \
  "v_readlane_b32 s44, v6, s41\n" \
  "v_mfma_f32_16x16x32_bf16 v[28:31], v[140:143], a[16:19], v[28:31]\n" \
  "v_readlane_b32 s42, v4, s46\n" \
  "v_mfma_f32_16x16x32_bf16 v[24:27], v[120:123], a[20:23], v[24:27]\n" \
  "buffer_store_dwordx4 v[20:23], v3, s[36:39], s50 offen sc1\n" \
  "v_mfma_f32_16x16x32_bf16 v[28:31], v[144:147], a[20:23], v[28:31]\n" \
  "v_mfma_f32_16x16x32_bf16 v[32:35], v[100:103], a[24:27], 0\n" \
  "v_mfma_f32_16x16x32_bf16 v[36:39], v[124:127], a[24:27], 0\n" \
  "v_mfma_f32_16x16x32_bf16 v[32:35], v[104:107], a[28:31], v[32:35]\n" \
  "v_cvt_pk_bf16_f32 v16, v24, v25\n" \
  "v_cvt_pk_bf16_f32 v17, v26, v27\n" \
  "v_mfma_f32_16x16x32_bf16 v[36:39], v[128:131], a[28:31], v[36:39]\n" \
  "v_cvt_pk_bf16_f32 v18, v28, v29\n" \
  "v_cvt_pk_bf16_f32 v19, v30, v31\n" \
  "v_mfma_f32_16x16x32_bf16 v[32:35], v[108:111], a[32:35], v[32:35]\n" \
  "buffer_store_dwordx4 v[16:19], v2, s[36:39], s43 offen sc1\n" \
  "v_mfma_f32_16x16x32_bf16 v[36:39], v[132:135], a[32:35], v[36:39]\n" \
  "s_xor_b32 s50, s43, 0x2000\n" \
  "s_add_u32 s43, s43, 0x40000\n" \
  "v_mfma_f32_16x16x32_bf16 v[32:35], v[112:115], a[36:39], v[32:35]\n" \
  "v_mfma_f32_16x16x32_bf16 v[36:39], v[136:139], a[36:39], v[36:39]\n" \
  "v_mfma_f32_16x16x32_bf16 v[32:35], v[116:119], a[40:43], v[32:35]\n" \
  "v_mfma_f32_16x16x32_bf16 v[36:39], v[140:143], a[40:43], v[36:39]\n" \
  "v_mfma_f32_16x16x32_bf16 v[32:35], v[120:123], a[44:47], v[32:35]\n" \
  "buffer_store_dwordx4 v[16:19], v2, s[36:39], s50 offen sc1\n" \
  "v_mfma_f32_16x16x32_bf16 v[36:39], v[144:147], a[44:47], v[36:39]\n" \
  "v_mfma_f32_16x16x32_bf16 v[24:27], v[100:103], a[48:51], 0\n" \
  "v_mfma_f32_16x16x32_bf16 v[28:31], v[124:127], a[48:51], 0\n" \
  "v_mfma_f32_16x16x32_bf16 v[24:27], v[104:107], a[52:55], v[24:27]\n" \
  "v_cvt_pk_bf16_f32 v20, v32, v33\n" \
  "v_cvt_pk_bf16_f32 v21, v34, v35\n" \
  "v_mfma_f32_16x16x32_bf16 v[28:31], v[128:131], a[52:55], v[28:31]\n" \
  "v_cvt_pk_bf16_f32 v22, v36, v37\n" \
  "v_cvt_pk_bf16_f32 v23, v38, v39\n" \
  "v_mfma_f32_16x16x32_bf16 v[24:27], v[108:111], a[56:59], v[24:27]\n" \
  "buffer_store_dwordx4 v[20:23], v2, s[36:39], s43 offen sc1\n" \
  "v_mfma_f32_16x16x32_bf16 v[28:31], v[132:135], a[56:59], v[28:31]\n" \
  "s_xor_b32 s50, s43, 0x2000\n" \
  "s_add_u32 s43, s43, 0x40000\n" \
  "v_mfma_f32_16x16x32_bf16 v[24:27], v[112:115], a[60:63], v[24:27]\n" \
  "v_mfma_f32_16x16x32_bf16 v[28:31], v[136:139], a[60:63], v[28:31]\n" \
  "v_mfma_f32_16x16x32_bf16 v[24:27], v[116:119], a[64:67], v[24:27]\n" \
  "v_mfma_f32_16x16x32_bf16 v[28:31], v[140:143], a[64:67], v[28:31]\n" \
  "v_mfma_f32_16x16x32_bf16 v[24:27], v[120:123], a[68:71], v[24:27]\n" \
  "buffer_store_dwordx4 v[20:23], v2, s[36:39], s50 offen sc1\n" \
  "v_mfma_f32_16x16x32_bf16 v[28:31], v[144:147], a[68:71], v[28:31]\n" \
  "v_mfma_f32_16x16x32_bf16 v[32:35], v[100:103], a[72:75], 0\n" \
  "v_mfma_f32_16x16x32_bf16 v[36:39], v[124:127], a[72:75], 0\n" \
  "v_mfma_f32_16x16x32_bf16 v[32:35], v[104:107], a[76:79], v[32:35]\n" \
  "v_cvt_pk_bf16_f32 v16, v24, v25\n" \
  "v_cvt_pk_bf16_f32 v17, v26, v27\n" \
  "v_mfma_f32_16x16x32_bf16 v[36:39], v[128:131], a[76:79], v[36:39]\n" \
  "v_cvt_pk_bf16_f32 v18, v28, v29\n" \
  "v_cvt_pk_bf16_f32 v19, v30, v31\n" \
  "v_mfma_f32_16x16x32_bf16 v[32:35], v[108:111], a[80:83], v[32:35]\n" \
  "buffer_store_dwordx4 v[16:19], v2, s[36:39], s43 offen sc1\n" \
  "v_mfma_f32_16x16x32_bf16 v[36:39], v[132:135], a[80:83], v[36:39]\n" \
  "s_xor_b32 s50, s43, 0x2000\n" \
  "s_add_u32 s43, s43, 0x40000\n" \
  "v_mfma_f32_16x16x32_bf16 v[32:35], v[112:115], a[84:87], v[32:35]\n" \
  "v_mfma_f32_16x16x32_bf16 v[36:39], v[136:139], a[84:87], v[36:39]\n" \
  "v_mfma_f32_16x16x32_bf16 v[32:35], v[116:119], a[88:91], v[32:35]\n" \
  "v_mfma_f32_16x16x32_bf16 v[36:39], v[140:143], a[88:91], v[36:39]\n" \
  "v_mfma_f32_16x16x32_bf16 v[32:35], v[120:123], a[92:95], v[32:35]\n" \
  "buffer_store_dwordx4 v[16:19], v2, s[36:39], s50 offen sc1\n" \
  "v_mfma_f32_16x16x32_bf16 v[36:39], v[144:147], a[92:95], v[36:39]\n" \
  "v_mfma_f32_16x16x32_bf16 v[24:27], v[100:103], a[96:99], 0\n" \
  "s_waitcnt vmcnt(6)\n" \
  "s_barrier\n" \
  "v_cmp_gt_u32 vcc, v89, v90\n" \
  "v_mfma_f32_16x16x32_bf16 v[28:31], v[124:127], a[96:99], 0\n" \
  "buffer_atomic_add v91, v11, s[36:39], s47 offen\n" \
  "v_mfma_f32_16x16x32_bf16 v[24:27], v[104:107], a[100:103], v[24:27]\n" \
  "v_cvt_pk_bf16_f32 v20, v32, v33\n" \
  "v_cvt_pk_bf16_f32 v21, v34, v35\n" \
  "v_mfma_f32_16x16x32_bf16 v[28:31], v[128:131], a[100:103], v[28:31]\n" \
  "v_cvt_pk_bf16_f32 v22, v36, v37\n" \
  "v_cvt_pk_bf16_f32 v23, v38, v39\n" \
  "v_mfma_f32_16x16x32_bf16 v[24:27], v[108:111], a[104:107], v[24:27]\n" \
  "buffer_store_dwordx4 v[20:23], v2, s[36:39], s43 offen sc1\n" \
  "v_mfma_f32_16x16x32_bf16 v[28:31], v[132:135], a[104:107], v[28:31]\n" \
  "s_xor_b32 s50, s43, 0x2000\n" \
  "s_add_u32 s43, s43, 0x40000\n" \
  "v_mfma_f32_16x16x32_bf16 v[24:27], v[112:115], a[108:111], v[24:27]\n" \
  "s_cbranch_vccnz LP1_%=\n" \
  "LK1_%=:\n" \
  "s_add_u32 m0, s40, 0\n" \
  "v_mfma_f32_16x16x32_bf16 v[28:31], v[136:139], a[108:111], v[28:31]\n" \
  "buffer_load_dwordx4 v1, s[36:39], s42 offen offset:0 sc1 lds\n" \
  "v_mfma_f32_16x16x32_bf16 v[24:27], v[116:119], a[112:115], v[24:27]\n" \
  "v_mfma_f32_16x16x32_bf16 v[28:31], v[140:143], a[112:115], v[28:31]\n" \
  "buffer_load_dwordx4 v1, s[36:39], s42 offen offset:1024 sc1 lds\n" \
  "v_mfma_f32_16x16x32_bf16 v[24:27], v[120:123], a[116:119], v[24:27]\n" \
  "buffer_store_dwordx4 v[20:23], v2, s[36:39], s50 offen sc1\n" \
  "v_mfma_f32_16x16x32_bf16 v[28:31], v[144:147], a[116:119], v[28:31]\n" \
  "buffer_load_dwordx4 v1, s[36:39], s42 offen offset:2048 sc1 lds\n" \
  "v_mfma_f32_16x16x32_bf16 v[32:35], v[100:103], a[120:123], 0\n" \
  "v_mfma_f32_16x16x32_bf16 v[36:39], v[124:127], a[120:123], 0\n" \
  "buffer_atomic_add v91, v12, s[36:39], s49 offen\n" \
  "v_mfma_f32_16x16x32_bf16 v[32:35], v[104:107], a[124:127], v[32:35]\n" \
  "v_cvt_pk_bf16_f32 v16, v24, v25\n" \
  "v_cvt_pk_bf16_f32 v17, v26, v27\n" \
  "v_mfma_f32_16x16x32_bf16 v[36:39], v[128:131], a[124:127], v[36:39]\n" \
  "v_cvt_pk_bf16_f32 v18, v28, v29\n" \
  "v_cvt_pk_bf16_f32 v19, v30, v31\n" \
  "v_mfma_f32_16x16x32_bf16 v[32:35], v[108:111], a[128:131], v[32:35]\n" \
  "buffer_store_dwordx4 v[16:19], v2, s[36:39], s43 offen sc1\n" \
  "v_mfma_f32_16x16x32_bf16 v[36:39], v[132:135], a[128:131], v[36:39]\n" \
  "s_xor_b32 s50, s43, 0x2000\n" \
  "s_add_u32 s43, s43, 0x40000\n" \
  "v_mfma_f32_16x16x32_bf16 v[32:35], v[112:115], a[132:135], v[32:35]\n" \
  "s_mov_b32 s49, 0\n" \
  "s_lshr_b32 s45, s41, 4\n" \
  "s_lshl_b32 s45, s45, 13\n" \
  "v_mfma_f32_16x16x32_bf16 v[36:39], v[136:139], a[132:135], v[36:39]\n" \
  "s_lshl_b32 s47, s41, 9\n" \
  "s_add_u32 s47, s47, s45\n" \
  "v_mfma_f32_16x16x32_bf16 v[32:35], v[116:119], a[136:139], v[32:35]\n" \
  "v_mfma_f32_16x16x32_bf16 v[36:39], v[140:143], a[136:139], v[36:39]\n" \
  "v_mfma_f32_16x16x32_bf16 v[32:35], v[120:123], a[140:143], v[32:35]\n" \
  "buffer_store_dwordx4 v[16:19], v2, s[36:39], s50 offen sc1\n" \
  "v_mfma_f32_16x16x32_bf16 v[36:39], v[144:147], a[140:143], v[36:39]\n" \
  "v_mfma_f32_16x16x32_bf16 v[24:27], v[100:103], a[144:147], 0\n" \
  "v_mfma_f32_16x16x32_bf16 v[28:31], v[124:127], a[144:147], 0\n" \
  "v_mfma_f32_16x16x32_bf16 v[24:27], v[104:107], a[148:151], v[24:27]\n" \
  "v_cvt_pk_bf16_f32 v20, v32, v33\n" \
  "v_cvt_pk_bf16_f32 v21, v34, v35\n" \
  "v_mfma_f32_16x16x32_bf16 v[28:31], v[128:131], a[148:151], v[28:31]\n" \
  "v_cvt_pk_bf16_f32 v22, v36, v37\n" \
  "v_cvt_pk_bf16_f32 v23, v38, v39\n" \
  "v_mfma_f32_16x16x32_bf16 v[24:27], v[108:111], a[152:155], v[24:27]\n" \
  "buffer_store_dwordx4 v[20:23], v2, s[36:39], s43 offen sc1\n" \
  "v_mfma_f32_16x16x32_bf16 v[28:31], v[132:135], a[152:155], v[28:31]\n" \
  "s_xor_b32 s50, s43, 0x2000\n" \
  "s_add_u32 s43, s43, 0x40000\n" \
  "v_mfma_f32_16x16x32_bf16 v[24:27], v[112:115], a[156:159], v[24:27]\n" \
  "v_mfma_f32_16x16x32_bf16 v[28:31], v[136:139], a[156:159], v[28:31]\n" \
  "v_mfma_f32_16x16x32_bf16 v[24:27], v[116:119], a[160:163], v[24:27]\n" \
  "v_mfma_f32_16x16x32_bf16 v[28:31], v[140:143], a[160:163], v[28:31]\n" \
  "v_mfma_f32_16x16x32_bf16 v[24:27], v[120:123], a[164:167], v[24:27]\n" \
  "buffer_store_dwordx4 v[20:23], v2, s[36:39], s50 offen sc1\n" \
  "v_mfma_f32_16x16x32_bf16 v[28:31], v[144:147], a[164:167], v[28:31]\n" \
  "v_mfma_f32_16x16x32_bf16 v[32:35], v[100:103], a[168:171], 0\n" \
  "v_mfma_f32_16x16x32_bf16 v[36:39], v[124:127], a[168:171], 0\n" \
  "v_mfma_f32_16x16x32_bf16 v[32:35], v[104:107], a[172:175], v[32:35]\n" \
  "v_cvt_pk_bf16_f32 v16, v24, v25\n" \
  "v_cvt_pk_bf16_f32 v17, v26, v27\n" \
  "v_mfma_f32_16x16x32_bf16 v[36:39], v[128:131], a[172:175], v[36:39]\n" \
  "v_cvt_pk_bf16_f32 v18, v28, v29\n" \
  "v_cvt_pk_bf16_f32 v19, v30, v31\n" \
  "v_mfma_f32_16x16x32_bf16 v[32:35], v[108:111], a[176:179], v[32:35]\n" \
  "buffer_store_dwordx4 v[16:19], v2, s[36:39], s43 offen sc1\n" \
  "v_mfma_f32_16x16x32_bf16 v[36:39], v[132:135], a[176:179], v[36:39]\n" \
  "s_xor_b32 s50, s43, 0x2000\n" \
  "s_add_u32 s43, s43, 0x40000\n" \
  "v_mfma_f32_16x16x32_bf16 v[32:35], v[112:115], a[180:183], v[32:35]\n" \
  "v_mfma_f32_16x16x32_bf16 v[36:39], v[136:139], a[180:183], v[36:39]\n" \
  "v_mfma_f32_16x16x32_bf16 v[32:35], v[116:119], a[184:187], v[32:35]\n" \
  "v_mfma_f32_16x16x32_bf16 v[36:39], v[140:143], a[184:187], v[36:39]\n" \
  "v_mfma_f32_16x16x32_bf16 v[32:35], v[120:123], a[188:191], v[32:35]\n" \
  "buffer_store_dwordx4 v[16:19], v2, s[36:39], s50 offen sc1\n" \
  "v_mfma_f32_16x16x32_bf16 v[36:39], v[144:147], a[188:191], v[36:39]\n" \
  "v_mfma_f32_16x16x32_bf16 v[24:27], v[100:103], a[192:195], 0\n" \
  "v_mfma_f32_16x16x32_bf16 v[28:31], v[124:127], a[192:195], 0\n" \
  "v_mfma_f32_16x16x32_bf16 v[24:27], v[104:107], a[196:199], v[24:27]\n" \
  "v_cvt_pk_bf16_f32 v20, v32, v33\n" \
  "v_cvt_pk_bf16_f32 v21, v34, v35\n" \
  "v_mfma_f32_16x16x32_bf16 v[28:31], v[128:131], a[196:199], v[28:31]\n" \
  "v_cvt_pk_bf16_f32 v22, v36, v37\n" \
  "v_cvt_pk_bf16_f32 v23, v38, v39\n" \
  "v_mfma_f32_16x16x32_bf16 v[24:27], v[108:111], a[200:203], v[24:27]\n" \
  "buffer_store_dwordx4 v[20:23], v2, s[36:39], s43 offen sc1\n" \
  "v_mfma_f32_16x16x32_bf16 v[28:31], v[132:135], a[200:203], v[28:31]\n" \
  "s_xor_b32 s50, s43, 0x2000\n" \
  "s_add_u32 s43, s43, 0x40000\n" \
  "v_mfma_f32_16x16x32_bf16 v[24:27], v[112:115], a[204:207], v[24:27]\n" \
  "s_waitcnt vmcnt(8)\n" \
  "s_barrier\n" \
  "v_mfma_f32_16x16x32_bf16 v[28:31], v[136:139], a[204:207], v[28:31]\n" \
  "ds_read_b128 v[40:43], v0 offset:0\n" \
  "ds_read_b128 v[44:47], v0 offset:3072\n" \
  "v_mfma_f32_16x16x32_bf16 v[24:27], v[116:119], a[208:211], v[24:27]\n" \
  "ds_read_b128 v[48:51], v0 offset:6144\n" \
  "ds_read_b128 v[52:55], v0 offset:9216\n" \
  "v_mfma_f32_16x16x32_bf16 v[28:31], v[140:143], a[208:211], v[28:31]\n" \
  "ds_read_b64 v[56:57], v0 offset:2048\n" \
  "ds_read_b64 v[58:59], v0 offset:5120\n" \
  "v_mfma_f32_16x16x32_bf16 v[24:27], v[120:123], a[212:215], v[24:27]\n" \
  "buffer_store_dwordx4 v[20:23], v2, s[36:39], s50 offen sc1\n" \
  "ds_read_b64 v[60:61], v0 offset:8192\n" \
  "v_mfma_f32_16x16x32_bf16 v[28:31], v[144:147], a[212:215], v[28:31]\n" \
  "ds_read_b64 v[62:63], v0 offset:11264\n" \
  "ds_read_b128 v[64:67], v0 offset:1024\n" \
  "v_mfma_f32_16x16x32_bf16 v[32:35], v[100:103], a[216:219], 0\n" \
  "ds_read_b128 v[68:71], v0 offset:4096\n" \
  "ds_read_b128 v[72:75], v0 offset:7168\n" \
  "v_mfma_f32_16x16x32_bf16 v[36:39], v[124:127], a[216:219], 0\n" \
  "ds_read_b128 v[76:79], v0 offset:10240\n" \
  "ds_read_b64 v[80:81], v0 offset:2056\n" \
  "v_mfma_f32_16x16x32_bf16 v[32:35], v[104:107], a[220:223], v[32:35]\n" \
  "v_cvt_pk_bf16_f32 v16, v24, v25\n" \
  "v_cvt_pk_bf16_f32 v17, v26, v27\n" \
  "v_mfma_f32_16x16x32_bf16 v[36:39], v[128:131], a[220:223], v[36:39]\n" \
  "v_cvt_pk_bf16_f32 v18, v28, v29\n" \
  "v_cvt_pk_bf16_f32 v19, v30, v31\n" \
  "v_mfma_f32_16x16x32_bf16 v[32:35], v[108:111], a[224:227], v[32:35]\n" \
  "buffer_store_dwordx4 v[16:19], v3, s[36:39], s44 offen sc1\n" \
  "ds_read_b64 v[82:83], v0 offset:5128\n" \
  "v_mfma_f32_16x16x32_bf16 v[36:39], v[132:135], a[224:227], v[36:39]\n" \
  "s_xor_b32 s50, s44, 0x2000\n" \
  "s_add_u32 s44, s44, 0x40000\n" \
  "ds_read_b64 v[84:85], v0 offset:8200\n" \
  "v_mfma_f32_16x16x32_bf16 v[32:35], v[112:115], a[228:231], v[32:35]\n" \
  "ds_read_b64 v[86:87], v0 offset:11272\n" \
  "v_mfma_f32_16x16x32_bf16 v[36:39], v[136:139], a[228:231], v[36:39]\n" \
  "v_mfma_f32_16x16x32_bf16 v[32:35], v[116:119], a[232:235], v[32:35]\n" \
  "v_mfma_f32_16x16x32_bf16 v[36:39], v[140:143], a[232:235], v[36:39]\n" \
  "v_mfma_f32_16x16x32_bf16 v[32:35], v[120:123], a[236:239], v[32:35]\n" \
  "buffer_store_dwordx4 v[16:19], v3, s[36:39], s50 offen sc1\n" \
  "v_mfma_f32_16x16x32_bf16 v[36:39], v[144:147], a[236:239], v[36:39]\n" \
  "s_add_u32 s41, s41, 1\n" \
  "s_waitcnt lgkmcnt(0)\n" \
  "s_cmp_lt_u32 s41, 64\n" \
  "s_cbranch_scc1 LL_%=\n" \
  "s_nop 7\n" \
  "v_cvt_pk_bf16_f32 v20, v32, v33\n" \
  "v_cvt_pk_bf16_f32 v21, v34, v35\n" \
  "v_cvt_pk_bf16_f32 v22, v36, v37\n" \
  "v_cvt_pk_bf16_f32 v23, v38, v39\n" \
  "buffer_store_dwordx4 v[20:23], v3, s[36:39], s44 offen sc1\n" \
  "s_xor_b32 s50, s44, 0x2000\n" \
  "s_add_u32 s44, s44, 0x40000\n" \
  "buffer_store_dwordx4 v[20:23], v3, s[36:39], s50 offen sc1\n" \
  "s_waitcnt vmcnt(0)\n" \
  "s_barrier\n" \
  "buffer_atomic_add v91, v11, s[36:39], s47 offen\n" \
  "buffer_atomic_add v91, v12, s[36:39], s49 offen\n" \
  "s_branch LX_%=\n" \
  "LPS_%=:\n" \
  "s_add_u32 s48, s48, 1\n" \
  "s_cmp_gt_u32 s48, 262144\n" \
  "s_cbranch_scc1 LKS_%=\n" \
  "s_sleep 4\n" \
  "buffer_load_dword v90, v88, s[36:39], 0 offen sc1\n" \
  "s_waitcnt vmcnt(0)\n" \
  "v_cmp_gt_u32 vcc, v89, v90\n" \
  "s_cbranch_vccnz LPS_%=\n" \
  "s_branch LKS_%=\n" \
  "LP0_%=:\n" \
  "s_add_u32 s48, s48, 1\n" \
  "s_cmp_gt_u32 s48, 262144\n" \
  "s_cbranch_scc1 LK0_%=\n" \
  "s_sleep 4\n" \
  "buffer_load_dword v90, v88, s[36:39], 0 offen sc1\n" \
  "s_waitcnt vmcnt(0)\n" \
  "v_cmp_gt_u32 vcc, v89, v90\n" \
  "s_cbranch_vccnz LP0_%=\n" \
  "s_branch LK0_%=\n" \
  "LP1_%=:\n" \
  "s_add_u32 s48, s48, 1\n" \
  "s_cmp_gt_u32 s48, 262144\n" \
  "s_cbranch_scc1 LK1_%=\n" \
  "s_sleep 4\n" \
  "buffer_load_dword v90, v88, s[36:39], 0 offen sc1\n" \
  "s_waitcnt vmcnt(0)\n" \
  "v_cmp_gt_u32 vcc, v89, v90\n" \
  "s_cbranch_vccnz LP1_%=\n" \
  "s_branch LK1_%=\n" \
  "LX_%=:\n"

__device__ __forceinline__ int fresh_lane() {
  int ln;
  asm volatile("v_mbcnt_lo_u32_b32 %0, -1, 0\n\tv_mbcnt_hi_u32_b32 %0, -1, %0" : "=v"(ln));
  __builtin_assume(ln >= 0 && ln < 64);
  return ln;
}
__device__ __forceinline__ unsigned now_ticks() { return (unsigned)__builtin_amdgcn_s_memrealtime(); }
template <int SLEEP>
__device__ __forceinline__ void wait_ge(gu32* ctrl, long word, unsigned need) {
#pragma unroll 1
  for (int spin = 0;; ++spin) {
    const unsigned x = __hip_atomic_load(ctrl + word, RLX_AGENT);
    if (__builtin_amdgcn_ballot_w64(x < need) == 0) return;
    if (spin > SPIN_CAP || ((spin & 1023) == 1023 && rfl(__hip_atomic_load(ctrl + C_ERR, RLX_AGENT)))) { __hip_atomic_store(ctrl + C_ERR, 1u, RLX_AGENT); return; }
    __builtin_amdgcn_s_sleep(SLEEP);
  }
}
__device__ __forceinline__ void wait_tag(gu32* ctrl, int word, unsigned tag) {
#pragma unroll 1
  for (int spin = 0; spin <= SPIN_CAP; ++spin) {
    if (__builtin_amdgcn_ballot_w64(__hip_atomic_load(ctrl + word, RLX_AGENT) != tag) == 0) return;
    __builtin_amdgcn_s_sleep(3);
  }
}
__device__ __forceinline__ unsigned long long wait_init(gu32* ctrl, unsigned tag, int lane) {
  unsigned x;
#pragma unroll 1
  for (int spin = 0;; ++spin) {
    x = __hip_atomic_load(ctrl + C_INIT + (lane & 31) * 32, RLX_AGENT);
    if (__builtin_amdgcn_ballot_w64((x & ~3u) != tag) == 0 || spin > SPIN_CAP) break;
    __builtin_amdgcn_s_sleep(4);
  }
  const unsigned long long m = __builtin_amdgcn_ballot_w64((x & (1u + (lane >> 5))) != 0), alt = 0x5555555555555555ull;
  const int d = __builtin_popcountll(m ^ alt);
  return d <= 26 ? alt : d >= 38 ? ~alt : m;
}
template <typename VT>
__device__ __forceinline__ void set_rsrc(VT& sr, int i, const void* ptr, unsigned bytes) {
  const unsigned long long a = uni64((unsigned long long)ptr);
  sr[4 * i] = (unsigned)a; sr[4 * i + 1] = (unsigned)(a >> 32) & 0xffffu; sr[4 * i + 2] = bytes; sr[4 * i + 3] = 0x00020000u;
}
__device__ __forceinline__ int so_near(unsigned long long par, int blk) { return (blk << 21) | ((int)(par >> (blk & 63) & 1) << 13); }
__device__ __forceinline__ int so_hand(unsigned long long par, int s) { return so_near(par, HB + (s >> 1)) + (s & 1) * 0x100000; }
__device__ __forceinline__ int so_far(unsigned long long par, int blk) { return so_near(par, blk) ^ 0x2000; }
__device__ __forceinline__ long nword(unsigned long long par, int off) { return ((long)so_near(par, XB) + lmap(off) - (long)CTRL_OFF) / 4; }
__device__ __forceinline__ v4u ld16s(rsrc_t r, int vo, int so) { return __builtin_amdgcn_raw_buffer_load_b128(r, vo, so, 16); }
__device__ __forceinline__ v4u ld16n(rsrc_t r, int vo, int so) { return __builtin_amdgcn_raw_buffer_load_b128(r, vo, so, TPOL); }
__device__ __forceinline__ v2u ld8s(rsrc_t r, int vo, int so) { return __builtin_amdgcn_raw_buffer_load_b64(r, vo, so, 16); }
__device__ __forceinline__ void st16s(v4u x, rsrc_t r, int vo, int so) { __builtin_amdgcn_raw_buffer_store_b128(x, r, vo, so, 16); }
#define TS(cond, i)
#define OPAQUE(v) asm volatile("" : "=v"(v))
#define W240_IN(w) "{a[0:31]}"(w[0]), "{a[32:63]}"(w[1]), "{a[64:95]}"(w[2]), "{a[96:127]}"(w[3]), "{a[128:159]}"(w[4]), "{a[160:191]}"(w[5]), \
                   "{a[192:223]}"(w[6]), "{a[224:239]}"(w##7)
#define W240_SET(w, i, k, val) do { if ((i) < 224) w[(i) >> 5][((i) & 31) + (k)] = (val); else w##7[(i) - 224 + (k)] = (val); } while (0)

__device__ __forceinline__ unsigned wave_max_u32(unsigned m) {
  m = max(m, (unsigned)__builtin_amdgcn_update_dpp(0, (int)m, 0xb1, 0xf, 0xf, true)); m = max(m, (unsigned)__builtin_amdgcn_update_dpp(0, (int)m, 0x4e, 0xf, 0xf, true));
  m = max(m, (unsigned)__builtin_amdgcn_update_dpp(0, (int)m, 0x124, 0xf, 0xf, true)); m = max(m, ror8u(m));
  auto a = __builtin_amdgcn_permlane16_swap(m, m, false, false); m = max(a[0], a[1]);
  auto b = __builtin_amdgcn_permlane32_swap(m, m, false, false); return max(b[0], b[1]);
}
__device__ __forceinline__ void select_rows(gu32* ctrl, rsrc_t rT, int lane) {
  unsigned key[32];
  v4u rr[8];
#pragma unroll
  for (int i = 0; i < 8; ++i) rr[i] = ld16s(rT, lane * 16 + i * 1024, 2 * (int)HF_BYTES);
  __builtin_amdgcn_sched_barrier(0);
#pragma unroll
  for (int i = 0; i < 8; ++i) {
    const v4u r = rr[i];
#pragma unroll
    for (int k = 0; k < 4; ++k) {
      unsigned u = f2u(u2f(r[k]) + 0.f);
      u = (u & 0x80000000u) ? ~u : (u | 0x80000000u);
      key[4 * i + k] = (u & 0xffff0000u) | (unsigned)(S - 1 - (i * 256 + lane * 4 + k));
    }
  }
  unsigned mine = 0, bound = 0;
#pragma unroll 1
  for (int it = 0; it < TOPK; ++it) {
    unsigned m = 0;
#pragma unroll
    for (int i = 0; i < 32; ++i) m = max(m, key[i] - bound);
    bound += wave_max_u32(m);
    if ((lane & 7) == it) mine = S - (bound & 0xffffu);
  }
#pragma unroll
  for (int i = 0; i < 2; ++i) __hip_atomic_store(ctrl + C_IDX + ((lane >> 3) + 8 * i) * 32 + (lane & 7), mine, RLX_AGENT);
}

__device__ __forceinline__ void ffn_role(const Params& p, int role, int lane, int wave) {
  gu32* ctrl = (gu32*)(p.ws + CTRL_OFF);
  const int g = lane >> 4, n = lane & 15, q = (lane >> 2) & 3, pp = lane & 3;
  const bool up = role < 96, down = role >= 88;
  const int c = role - 88;
  v4u wg[5], wu[5], wd[12];
  {
    const rsrc_t rg = mkrsrc(p.wg, DM * FF * 2), ru = mkrsrc(p.wu, DM * FF * 2), rd = mkrsrc(p.wd, FF * DM * 2);
#pragma unroll
    for (int i = 0; i < 5; ++i) {
      const int vo = up ? ((wave * 160 + i * 32 + (lane >> 1)) * FF + role * 16) * 2 + (lane & 1) * 16 : OOB;
      dma16(rg, vo, 0, wave * 22528 + i * 1024); dma16(ru, vo, 0, wave * 22528 + 5120 + i * 1024);
    }
#pragma unroll
    for (int i = 0; i < 12; ++i) dma16(rd, down ? ((wave * 384 + i * 32 + (lane >> 1)) * DM + c * 16) * 2 + (lane & 1) * 16 : OOB, 0, wave * 22528 + 10240 + i * 1024);
    VMCNT(0);
    const unsigned a = wave * 22528 + (8 * g + q) * 32 + pp * 8;
#pragma unroll
    for (int i = 0; i < 22; ++i) {
      const v2u lo = trread(a + i * 1024), hi = trread(a + i * 1024 + 128);
      const v4u x = {lo[0], lo[1], hi[0], hi[1]};
      if (i < 5) wg[i] = x; else if (i < 10) wu[i - 5] = x; else wd[i - 10] = x;
    }
  }
  const rsrc_t rT = mkrsrc(p.ws + X1_OFF, (unsigned)(WS_END - X1_OFF));
  constexpr int SO_H2 = DM * 2, SO_R = 2 * (int)HF_BYTES, SO_ACT = (int)(ACT_OFF - X1_OFF), RED = 98304;
  if (wave == 0) {
    wait_ge<3>(ctrl, C_IDX + (role & 15) * 32 + (lane & 7), 1u);
    if (lane < 8) *(LAS unsigned*)(L_MISC + lane * 4) = __hip_atomic_load(ctrl + C_IDX + (role & 15) * 32 + lane, RLX_AGENT);
  }
  __syncthreads();
  const unsigned t0 = now_ticks();
  const int row = (*(LAS int*)(L_MISC + (n & 7) * 4) - 1) & (S - 1);
  v2u x1, h2, gb; unsigned rv;
  if (down && wave == 0) { { const v4u t = ld16s(mkrsrc(p.ws + CTRL_OFF + C_MOD * 4, 15360), (5 * DM + c * 16 + 4 * g) * 4, 0); gb = v2u{t[0], t[2]}; } x1 = ld8s(rT, (row * 2 * DM + c * 16 + 4 * g) * 2, 0); h2 = ld8s(rT, (row * 2 * DM + c * 16 + 4 * g) * 2, SO_H2); rv = __builtin_amdgcn_raw_buffer_load_b32(rT, row * 4, SO_R, 16); }
  if (up) {
    v4u xb[5];
#pragma unroll
    for (int i = 0; i < 5; ++i) xb[i] = ld16s(rT, (row * 2 * DM + wave * 160 + i * 32 + 8 * g) * 2, SO_H2);
    __builtin_amdgcn_sched_barrier(0);
    f4 ag = {}, au = {};
#pragma unroll
    for (int i = 0; i < 5; ++i) { ag = mfma32(wg[i], xb[i], ag); au = mfma32(wu[i], xb[i], au); }
    *(LAS f4*)(RED + wave * 2048 + lane * 16) = ag; *(LAS f4*)(RED + wave * 2048 + 1024 + lane * 16) = au;
    __syncthreads();
    if (wave == 0) {
#pragma unroll
      for (int w = 1; w < 4; ++w) { ag += *(LAS f4*)(RED + w * 2048 + lane * 16); au += *(LAS f4*)(RED + w * 2048 + 1024 + lane * 16); }
      float a[4];
#pragma unroll
      for (int r = 0; r < 4; ++r) { const float gs = rbf(ag[r]); a[r] = rbf(gs / (1.f + __expf(-gs))) * rbf(au[r]); }
      __builtin_amdgcn_raw_buffer_store_b64(v2u{unsent(pack2(a[0], a[1])), unsent(pack2(a[2], a[3]))}, rT, n < 8 ? role * 256 + (g >> 1) * 128 + n * 16 + (g & 1) * 8 : OOB, SO_ACT, 16);
    }
  }
  if (!down) return;
  while ((int)(now_ticks() - t0) < FFN_WAIT) __builtin_amdgcn_s_sleep(1);
  v4u ab[12];
  for (int spin = 0;; ++spin) {
    asm volatile("" ::: "memory");
    unsigned m = 0;
#pragma unroll
    for (int i = 0; i < 12; ++i) ab[i] = ld16s(rT, (wave * 12 + i) * 512 + g * 128 + (n & 7) * 16, SO_ACT);
    __builtin_amdgcn_sched_barrier(0);
#pragma unroll
    for (int i = 0; i < 12; ++i) m = max(max(m, max(ab[i][0], ab[i][1])), max(ab[i][2], ab[i][3]));
    if (__builtin_amdgcn_ballot_w64(m == ~0u) == 0) break;
    if (spin > SPIN_CAP) { __hip_atomic_store(ctrl + C_ERR, 1u, RLX_AGENT); break; }
    __builtin_amdgcn_s_sleep(4);
  }
  f4 y = {};
#pragma unroll
  for (int i = 0; i < 12; ++i) y = mfma32(wd[i], ab[i], y);
  *(LAS f4*)(RED + 8192 + wave * 1024 + lane * 16) = y;
  __syncthreads();
  if (wave == 0 && n < 8) {
#pragma unroll
    for (int w = 1; w < 4; ++w) y += *(LAS f4*)(RED + 8192 + w * 1024 + lane * 16);
    const float rs = rbf(1.f / (1.f + __expf(-u2f(rv))));
    float o[4];
#pragma unroll
    for (int r = 0; r < 4; ++r) {
      const float hv = (r & 1) ? hi2f(h2[r >> 1]) : lo2f(h2[r >> 1]), xv = (r & 1) ? hi2f(x1[r >> 1]) : lo2f(x1[r >> 1]);
      o[r] = xv + rbf(((r & 1) ? hi2f(gb[r >> 1]) : lo2f(gb[r >> 1])) * rbf(hv + rbf(rbf(y[r]) * rs)));
    }
    *(v2u*)(p.out + (size_t)row * DM + c * 16 + 4 * g) = v2u{pack2(o[0], o[1]), pack2(o[2], o[3])};
  }
}

constexpr int LP = 0, L_PR = 2560, L_MOD = 16384;
__device__ __forceinline__ v4u ln1_piece(const Params& p, int row0, int i, int lane) {
  return __builtin_amdgcn_raw_buffer_load_b128(mkrsrc(p.x, S * DM * 2), (row0 + (lane & 7)) * 1280 + (lane >> 3) * 16, i * 128, 0);
}
__device__ __forceinline__ void ln1_stats(const v4u (&a)[10], float& mb, float& isd) {
  float s = 0.f, q = 0.f;
  const bf2 one = {(__bf16)1.f, (__bf16)1.f};
#pragma unroll
  for (int i = 0; i < 10; ++i)
#pragma unroll
    for (int k = 0; k < 4; ++k) { const unsigned w = a[i][k]; s = __builtin_amdgcn_fdot2_f32_bf16(__builtin_bit_cast(bf2, w), one, s, false); }
  s += ror8f(s); s = swapsum16(s, s); s = swapsum32(s, s);
  const float mean = s * (1.f / DM);
#pragma unroll
  for (int i = 0; i < 10; ++i)
#pragma unroll
    for (int k = 0; k < 4; ++k) { const float d0 = lo2f(a[i][k]) - mean, d1 = hi2f(a[i][k]) - mean; q = fmaf(d0, d0, fmaf(d1, d1, q)); }
  q += ror8f(q); q = swapsum16(q, q); q = swapsum32(q, q);
  mb = rbf(mean); isd = __builtin_amdgcn_rcpf(rbf(__builtin_amdgcn_sqrtf(rbf(rbf(q * (1.f / DM)) + 1e-5f))));
}
__device__ __forceinline__ unsigned ln_norm(unsigned a, float mb, float isd) { const unsigned d = pack2(lo2f(a) - mb, hi2f(a) - mb); return pack2(lo2f(d) * isd, hi2f(d) * isd); }
__device__ __forceinline__ unsigned ln_mod(unsigned n, unsigned sc, unsigned sh) { const unsigned m = pack2(lo2f(n) * lo2f(sc), hi2f(n) * hi2f(sc)); return pack2(lo2f(m) + lo2f(sh), hi2f(m) + hi2f(sh)); }
__device__ __forceinline__ void ln1_store(const Params& p, v4u o, int row0, int i, int lane) {
  st16s(o, mkrsrc(p.ws + HF_OFF, (unsigned)HF_BYTES), (row0 >> 4) * 20480 + (lane >> 3) * 256 + ((row0 & 8) + (lane & 7)) * 16, i * 2048);
}
template <typename B>
__device__ __forceinline__ void mod_unit(const Params& p, int u, int tid, unsigned tag, B between) {
  const rsrc_t rw = mkrsrc(p.wmod, DM * 3840 * 2), rc = mkrsrc(p.c, DM * 2);
  LAS float* sv = (LAS float*)L_MOD; LAS float* red = sv + 768;
  u16 cv[3]; v4u w[20];
#pragma unroll
  for (int i = 0; i < 3; ++i) cv[i] = __builtin_amdgcn_raw_buffer_load_b16(rc, tid * 2, i * 512, 0);
#pragma unroll
  for (int i = 0; i < 20; ++i) w[i] = __builtin_amdgcn_raw_buffer_load_b128(rw, u < 60 ? (tid >> 3) * 7680 + u * 128 + (tid & 7) * 16 : OOB, i * 32 * 7680, 0);
  between();
  __builtin_amdgcn_sched_barrier(0);
#pragma unroll
  for (int i = 0; i < 3; ++i) { const float c = bf2f(cv[i]); sv[i * 256 + tid] = rbf(c / (1.f + __expf(-c))); }
  LGKM0(); BAR();
  float acc[8] = {};
#pragma unroll
  for (int i = 0; i < 20; ++i) {
    const float x = sv[i * 32 + (tid >> 3)];
#pragma unroll
    for (int k = 0; k < 4; ++k) { acc[2 * k] += x * lo2f(w[i][k]); acc[2 * k + 1] += x * hi2f(w[i][k]); }
  }
#pragma unroll
  for (int k = 0; k < 8; ++k) red[(tid >> 3) * 65 + (tid & 7) * 8 + k] = acc[k];
  LGKM0(); BAR();
  float sum = 0.f;
#pragma unroll
  for (int i = 0; i < 8; ++i) sum += red[((tid & 3) * 8 + i) * 65 + (tid >> 2)];
  sum += u2f(__builtin_amdgcn_update_dpp(0, (int)f2u(sum), 0xb1, 0xf, 0xf, true)); sum += u2f(__builtin_amdgcn_update_dpp(0, (int)f2u(sum), 0x4e, 0xf, 0xf, true));
  const unsigned y = pack2((u / 10) % 3 == 1 ? 1.f + rbf(sum) : sum, 0.f), z = (unsigned)__builtin_amdgcn_update_dpp(0, (int)y, 0x104, 0xf, 0xf, true);
  __builtin_amdgcn_raw_buffer_store_b64(v2u{y | z << 16, tag}, mkrsrc(p.ws + CTRL_OFF + C_MOD * 4, 15360), (tid & 7) == 0 ? u * 256 + tid : OOB, 0, 16);
  LGKM0(); BAR();
}
template <typename I, typename T>
__device__ __forceinline__ void stage4(I issue, T take) {
  issue(0, 0); issue(1, 1);
  VMCNT(15); BAR(); take(0, 0); LGKM0(); BAR(); issue(2, 0);
  VMCNT(15); BAR(); take(1, 1); LGKM0(); BAR(); issue(3, 1);
  VMCNT(15); BAR(); take(2, 0);
  VMCNT(0); BAR(); take(3, 1); LGKM0();
}

__device__ __forceinline__ void chain_role(const Params& p, int role, int lane, int w, unsigned tag) {
  const int ty = role % 3, h = role / 3, r = 2 * h + ty;
  const int g = lane >> 4, n = lane & 15, q = (lane >> 2) & 3, pp = lane & 3, par = n >> 3, i = n & 7, sel = q >> 1;
  constexpr int soX = XB << 21;
  v32u vc, vi;
  OPAQUE(vc); OPAQUE(vi);
#pragma unroll
  for (int k = 0; k < 10; ++k) {
    int o;
    if (ty == 1) { const int u = ((w & 1) * 8 + k) * 64 + lane, row = u >> 4; o = row * 12288 + ((u & 15) ^ (((row & 1) | ((row >> 1) & 2) | ((w >> 1) << 2)) << 1)) * 16; }
    else if (ty == 0 && w == 3) { const int u = k * 64 + lane, row = u >> 3; o = row * 12288 + 256 + ((u & 7) ^ (row & 4) ^ 2) * 16; }
    else { const int u = ((ty ? 6 : 8) * w + k) * 64 + lane, row = u / 24; o = row * 12288 + ((u % 24) ^ (row & 4)) * 16; }
    vi[k] = o + h * 384;
    const int u = (10 * w + k) * 64 + lane, row = u / 80;
    vi[10 + k] = (h * DH + row) * 1280 + ((u % 80) ^ (((row & 1) | ((row >> 1) & 2)) << 2)) * 16;
  }
  const int r0 = 4 * g + (q & 1), sw = (g & 1) << 6;
  if (ty == 0) {
    vi[20] = r0 * 384 + ((64 * w + 32 * sel + 8 * pp) ^ sw); vi[22] = 384;
    vi[21] = sel ? 24576 + r0 * 128 + ((32 * w + 8 * pp) ^ sw ^ 32) : r0 * 384 + ((256 + 32 * w + 8 * pp) ^ sw); vi[23] = sel ? 128 : 384;
  } else if (ty == 1) {
#pragma unroll
    for (int k = 0; k < 2; ++k) { vi[20 + k] = sel * 16384 + r0 * 256 + ((64 * w + 32 * k + 8 * pp) ^ (((q & 1) | ((g & 1) << 1) | (sel << 2)) << 5)); vi[22 + k] = 256; }
  } else {
    const int rv = 16 * (g >> 1) + 8 * sel + 4 * (g & 1) + (q & 1);
    vi[20] = rv * 384 + ((64 * w + 8 * pp) ^ sw); vi[21] = rv * 384 + ((256 + 32 * w + 8 * pp) ^ sw);
    vi[22] = r0 * 1280 + ((64 * w + 32 * sel + 8 * pp) ^ (((q & 1) | ((g & 1) << 1)) << 6));
  }
  vi[24] = (int)CTRL_OFF + (C_ANCH + 128) * 4;
  vi[25] = (int)CTRL_OFF + (C_ANCH + (role >> 5) * 32) * 4;
  vi[26] = lane < 4 ? (int)CTRL_OFF + (C_ANCH + lane * 32) * 4 : OOB;
  vi[27] = soX + lmap(N_PP) + pp * 128;
  vi[28] = w == 0 && lane == 0 ? vi[24] : OOB;
  vi[29] = (int)CTRL_OFF + (C_INIT + (lane & 31) * 32) * 4; vi[30] = 1 + (lane >> 5);
  vi[31] = lane == 0 ? soX + lmap(N_CF) + h * 128 + ty * 4 : OOB;
  vc[0] = n < 8 ? 0x4444 : 0xEEEE;
  const int tok = 16 * ((n >> 2) & 1) + 4 * (2 * w + par) + (n & 3);
  const int ko = h * 65536 + 3072 + (g * 16 + (tok & 15)) * 16;
  const int own = (role >> 5) * CPY;
  vc[1] = w == 2 ? lmap(own + M_OFF) + lane * 16 : ko + (tok >> 4) * 1024;
  vc[2] = w == 2 ? lmap(own + M_OFF) + 4096 + lane * 16 : ko + 2048 + (tok >> 4) * 8;
  vc[3] = (XP & 8) && w != 2 ? OOB : lmap(w == 2 ? own + M_OFF + 8192 : w == 3 ? V2_OFF + r * 4096 : r * 16384 + w * 8192) + lane * 16;
  vc[4] = lmap(P2_OFF + (16 * w + 8 * (g >> 1) + (n >> 1)) * 1024 + h * 32 + (n & 1) * 16 + (g & 1) * 8);
  vc[5] = L_MEM + (ty < 2 ? g * 128 + i * 16 : lane * 16);
  vc[6] = L_MEM + 8192 + (ty < 2 ? g : 16 * (g >> 1) + 2 * (g & 1) + par) * 32 + i * 4; vc[7] = vc[6] + 1024;
  const int kmo = lmap(h * 4096 + w * 1024);
  auto im4 = [](int u, int d) { return u * 128 + ((d ^ ((u & 2) << 2)) * 8); };
  if (ty == 1) {
    vc[8] = L_Q + w * 1024 + im4(g, n); vc[9] = vc[8] + 512;
    vc[10] = L_Q + im4(pp, 4 * g + q); vc[11] = vc[10] + 512; vc[12] = L_Q + w * 1024 + (g >> 1) * 512 + im4(pp, 8 * (g & 1) + q);
    vc[14] = par ? kmo + (g * 8 + i) * 16 : OOB;
  } else {
    vc[8] = L_Q + w * 512 + g * 128 + n * 8;
    vc[9] = ty ? L_Q + 2048 + w * 256 + (g & 1) * 128 + n * 8 + (g >> 1) * 4 : L_Q2 + w * 512 + im4(g, n);
    vc[10] = L_Q + (g >> 1) * 256 + (pp & 1) * 128 + (8 * (g & 1) + q) * 8;
    vc[11] = ty ? L_WO + w * 10240 + lane * 16 : L_Q2 + im4(w < 2 ? pp & 1 : pp, 4 * g + q);
    vc[12] = L_Q2 + w * 512 + im4(pp, 4 * g + q);
    vc[14] = par ? kmo + 512 + (g * 8 + i) * 8 : OOB;
  }
  vc[13] = ty == 2 ? lmap(P_OFF + ((4 * w + 2 * (g >> 1) + par) * 32 + h) * 128 + i * 16 + (g & 1) * 8)
         : ty == 0 && w < 2 ? lmap(E_OFF + h * 512 + (2 * w + (g >> 1)) * 128 + i * 16 + (g & 1) * 8 + par * 4)
         : g >= 2 && !par ? lmap(S_OFF + h * 256 + ty * 128 + i * 16 + (g - 2) * 8) : OOB;
  const int h0 = 8 * w + 4 * par;
  vc[15] = lmap(own + S_OFF + (h0 + g) * 256 + i * 16);
  vc[16] = lmap(own + E_OFF + h0 * 512 + g * 128 + i * 16);
  vc[17] = lmap(P_OFF + (r * 32 + h0 + g) * 128 + i * 16);
  vc[18] = i < 2 ? lmap(P2_OFF + r * 1024 + (h0 + g) * 32 + i * 16) : OOB;
  vc[19] = L_VO + h0 * 512 + g * 128 + i * 16;
  vc[20] = L_VO + 16384 + h0 * 128 + g * 32 + (i & 1) * 16;
  vc[21] = L_RED + w * 768 + (g & 1) * 256 + i * 16 + par * 8;
  vc[22] = L_RED + w * 768 + 512 + (g & 1) * 128 + i * 16 + par * 8;
  vc[23] = L_RED + min(lane, 47) * 16;
  vc[24] = lane < 32 ? lmap(M_OFF + r * 128 + i * 16 + (lane >> 3) * 4) : lane < 40 ? lmap(M_OFF + 8192 + tslot(r) * 32 + i * 4) : OOB;
  vc[25] = so_hand(0, lane); vc[26] = (3 * (lane % RING) + 2) << 21; vc[27] = (3 * (lane % RING) + (w != 3)) << 21;
  vc[28] = soX + lmap(N_PP) + pp * 128;
  vc[29] = soX + lmap(N_REC) + pp * 32; vc[30] = lane == 0 && (role < 3 || role == 35 || role == 65) ? vc[29] + (role >> 5) * 32 : OOB;
  vc[31] = lane == 0 ? (role * 4 + w) * 16 : OOB;
  v4u sh, s0, s1; v16u sc;
  set_rsrc(sh, 0, p.ws, (unsigned)HAND_BYTES + CTRL_BYTES);
  set_rsrc(s0, 0, ty == 2 ? p.wv : w >= 3 - ty ? p.wk : p.wq, DM * HD * 2); set_rsrc(s1, 0, p.wo, DM * HD * 2);
  sc[0] = ty * 4 + w; sc[1] = role; sc[2] = p.dB; sc[3] = p.dM; sc[4] = p.dS; sc[5] = p.lead; sc[6] = soX; sc[7] = (unsigned)HAND_BYTES + CTRL_BYTES;
  sc[8] = w * (ty == 2 ? 6144 : 8192); sc[9] = w * 10240; sc[10] = tag;
#pragma unroll
  for (int k = 0; k < 16; ++k) sc[k] = k < 11 ? rfl(sc[k]) : 0;
  asm volatile(CHAIN_ASM : "+{s[48:63]}"(sc), "+{v[0:31]}"(vc), "+{v[192:223]}"(vi), "+{s[36:39]}"(sh), "+{s[40:43]}"(s0), "+{s[44:47]}"(s1) :: "memory", "vcc", "scc", CHAIN_CLOBBERS);
  lane = fresh_lane();
  gu32* ct = (gu32*)(((unsigned long long)sh[0] | (unsigned long long)sh[1] << 32) + CTRL_OFF);
  if (lane == 0) {
    if (sc[12] > 20000u || sc[10] > 4000u || sc[11] > 4000u) __hip_atomic_store(ct + C_ERR, 1u, RLX_AGENT);
  }
}

__device__ __forceinline__ void load_wo(const Params& p, int h, int col0, int cstep, int lane, int wave, v32u (&wa)[7], v16u& wa7) {
  const int g = lane >> 4, q = (lane >> 2) & 3, pp = lane & 3;
  const rsrc_t ro = mkrsrc(p.wo, DM * HD * 2);
  stage4(
    [&](int m, int slot) {
#pragma unroll 1
      for (int i = 0; i < 15; ++i) {
        const int c = wave * 15 + i;
        dma16(ro, lane * 16, (h * DH + (c < 40 ? 32 * m : 96 + 16 * m)) * DM * 2 + c * 1024, slot * 61440 + c * 1024);
      }
    },
    [&](int m, int slot) {
#pragma unroll
      for (int c = 0; c < 10; ++c) {
        const unsigned col = slot * 61440 + (col0 + cstep * c + 4 * pp) * 2;
        const v2u lo = trread((8 * g + q) * 1280 + col), hi = trread((8 * g + 4 + q) * 1280 + col), tl = trread((32 + 4 * g + q) * 1280 + col);
        const int r = (c * 6 + m) * 4, r2 = (c * 6 + 4 + (m >> 1)) * 4 + 2 * (m & 1);
        W240_SET(wa, r, 0, lo[0]); W240_SET(wa, r, 1, lo[1]); W240_SET(wa, r, 2, hi[0]); W240_SET(wa, r, 3, hi[1]);
        W240_SET(wa, r2, 0, tl[0]); W240_SET(wa, r2, 1, tl[1]);
      }
    });
  BAR();
}

__device__ __forceinline__ void vo_role(const Params& p, int h, int lane, int wave, unsigned tag) {
  v32u wa[7]; v16u wa7;
  load_wo(p, h, 16 * wave, 64, lane, wave, wa, wa7);
  TS(lane == 0 && wave == 0, 256 + 96 + h)
  const unsigned long long parmask = wait_init((gu32*)(p.ws + CTRL_OFF), tag, lane);
  const int g = lane >> 4, n = lane & 15, b = 3 * (lane % RING);
  v16u vc; v4u sh;
  OPAQUE(vc);
  vc[0] = lane * 16; vc[1] = h * 65536 + wave * 16384 + lane * 16;
  vc[2] = (XP & 4) ? OOB : lmap((2 * wave + (n >> 3)) * 16384 + h * 512 + g * 128 + (n & 7) * 16);
  vc[3] = lmap(V2_OFF + (8 * wave + (n >> 1)) * 4096 + h * 128 + g * 32 + (n & 1) * 16);
  vc[4] = so_far(parmask, b + 2); vc[5] = so_near(parmask, b + 1); vc[6] = so_near(parmask, b);
  const bool cl = lane & 1;
  vc[7] = (int)CTRL_OFF + (cl ? C_CL : C_VP + h * 32) * 4;
  vc[8] = cl ? 128 : 0; vc[9] = !cl; vc[10] = cl ? 8 : 1;
  vc[11] = wave == 0 && lane == 0 ? so_near(parmask, XB) + lmap(N_PP) + (h & 3) * 128 : OOB;
  vc[12] = wave == 0 && lane == 0 ? (int)CTRL_OFF + (C_WP + h * 32) * 4 : OOB;
  set_rsrc(sh, 0, p.ws, (unsigned)HAND_BYTES + CTRL_BYTES);
  asm volatile(VO_ASM :: "{v[0:15]}"(vc), "{s[36:39]}"(sh), "{s40}"(wave * 3072), W240_IN(wa) : "memory", "vcc", "scc", VO_CLOBBERS);
  wait_ge<32>((gu32*)(p.ws + CTRL_OFF), nword(parmask, N_CF + h * 128 + 8), NSEG - 1);
  TS(fresh_lane() == 0 && wave == 0, 992 + h)
}

__device__ __forceinline__ void set_up(const Params& p, int j, int blk, int lane, int wave, unsigned tag) {
  const rsrc_t rH = mkrsrc(p.ws, (unsigned)HAND_BYTES + CTRL_BYTES);
  const int tid = wave * 64 + lane;
  if (wave < 2) {
    unsigned best[2] = {1u << 30, 1u << 30};
#pragma unroll 1
    for (int rep = 0; rep < CALREP; ++rep) {
      const int off = lane < 8 ? ((j + 32 * wave) << 21) + 0x1FC000 + (rep & 1) * 8192 + lane * 16 : OOB;
      const unsigned long long t1 = __builtin_amdgcn_s_memtime();
      LGKM0();
      st16s(v4u{1u, 2u, 3u, 4u}, rH, off, 0);
      VMCNT(0);
      const unsigned d = (unsigned)(__builtin_amdgcn_s_memtime() - t1);
      if (rep & 1) best[1] = min(best[1], d); else best[0] = min(best[0], d);
    }
    const unsigned x = xcc_id();
    if (lane == 0) *(LAS unsigned*)(L_MISC + wave * 4) = ((best[1] < best[0] ? 1u : 0u) ^ (x >> 2) ^ ((x + 1 - blk) >> 2)) & 1u;
  } else {
    const int t = tid - 128, i = j * 128 + t;
    const v4u z = {0u, 0u, 0u, 0u};
    const int k = j * 80 + t, row = k / 320, cp = k % 320;
    const unsigned x = unsent(__builtin_amdgcn_raw_buffer_load_b32(mkrsrc(p.mem0, 8 * DM * 2), k * 4, 0, 0));
    st16s(v4u{(unsigned)(i * 4 + C_Z0 - C_CL) < RING * 32u ? 8u : 0u, 0u, 0u, 0u}, rH, i < (C_Z1 - C_Z0) / 4 ? (int)CTRL_OFF + C_Z0 * 4 + i * 16 : OOB, 0);
#pragma unroll
    for (int u = 0; u < 2; ++u) st16s(z, rH, i + u * 4096 < (N_END - N_PP) / 8 ? (XB << 21) + 2 * N_PP + (i + u * 4096) * 16 : OOB, 0);
    st16s(v4u{~0u, ~0u, ~0u, ~0u}, mkrsrc(p.ws + ACT_OFF, TOPK * FF * 2), i * 16, 0);
    const int mo = t < 80 ? lmap(cp < 256 ? (cp >> 2) * 128 + row * 16 + (cp & 3) * 4 : 8192 + tslot(cp - 256) * 32 + row * 4) : OOB;
#pragma unroll
    for (int u = 0; u < 6; ++u) __builtin_amdgcn_raw_buffer_store_b32(x, rH, mo, (XB << 21) + (u >> 1) * 2 * CPY + (u & 1) * 8192, 16);
  }
  VMCNT(0); LGKM0(); BAR();
  if (tid == 0) __hip_atomic_store((gu32*)(p.ws + CTRL_OFF) + C_INIT + j * 32, tag | *(LAS unsigned*)L_MISC | *(LAS unsigned*)(L_MISC + 4) << 1, RLX_AGENT);
  TS(tid == 0, 768 + j)
  mod_unit(p, 28 + j, tid, tag, [] {});
}

__device__ __forceinline__ void proj_role(const Params& p, int type, int head, int blk, int lane, int wave, unsigned tag) {
  gu32* ctrl = (gu32*)(p.ws + CTRL_OFF);
  const int g = lane >> 4, q = (lane >> 2) & 3, pp = lane & 3;
  v32u wa[7]; v16u wa7;
  {
    if (type == 0) { set_up(p, head, blk, lane, wave, tag); __builtin_amdgcn_s_sleep(QSLEEP); }
    const rsrc_t rw = mkrsrc(type == 0 ? p.wq : type == 1 ? p.wk : p.wv, DM * HD * 2);
    stage4(
      [&](int m, int slot) {
#pragma unroll 1
        for (int i = 0; i < 15; ++i) {
          const int c = (wave * 15 + i) * 64 + lane;
          dma16(rw, ((m * 160 + c / 24) * HD + head * DH) * 2 + (c % 24) * 16, 0, slot * 61440 + (wave * 15 + i) * 1024);
        }
      },
      [&](int m, int slot) {
#pragma unroll
        for (int ks = 0; ks < 5; ++ks)
#pragma unroll
          for (int t = 0; t < 3; ++t) {
            const unsigned a = slot * 61440 + (32 * ks + 8 * g + q) * 384 + (t < 2 ? 32 * wave + 8 * pp + 4 * t : 128 + 16 * wave + 4 * pp) * 2;
            const v2u lo = trread(a), hi = trread(a + 4 * 384);
            const int r = (t * 20 + m * 5 + ks) * 4;
            W240_SET(wa, r, 0, lo[0]); W240_SET(wa, r, 1, lo[1]); W240_SET(wa, r, 2, hi[0]); W240_SET(wa, r, 3, hi[1]);
          }
      });
  }
  TS(lane == 0 && wave == 0, 256 + 160 + type * 32 + head)
  const unsigned long long parmask = wait_init(ctrl, tag, lane);
  const int k0 = type ? 0 : HS;
  if (wave < 2) wait_tag(ctrl, C_LNF + (type ? wave * 64 + lane : (24 + HS) * 4 + (lane & 31)) * 8, tag);
  BAR();
  const rsrc_t rh = mkrsrc(p.ws + HF_OFF, (unsigned)HF_BYTES);
  auto first_rows = [&](int seg) {
#pragma unroll
    for (int k = 0; k < 20; ++k) dma16_sc1(rh, wave * 10240 + lane * 16, (seg + k / 10) * 40960 + (k % 10) * 1024, (k / 10) * 40960 + wave * 10240 + (k % 10) * 1024);
  };
  first_rows(k0);
  v8u vc, sr; v4u sc;
  vc[0] = lane * 16; vc[1] = wave * 10240 + lane * 16;
  vc[2] = (head * 4 + wave) * (type ? 16384 : 4096) + lane * 16;
  vc[3] = type ? so_near(parmask, 3 * (lane % RING) + 2) ^ (type == 1 ? 0 : 0x2000) : (QB << 21) + (lane & 7) * 524288;
  vc[4] = wave || lane ? OOB : type == 1 ? so_near(parmask, XB) + lmap(N_PP) + (head & 3) * 128 : (int)CTRL_OFF + ((type ? C_VP : C_QP) + head * 32) * 4;
  vc[5] = type == 1 ? vc[2] + 3072 : type && head < 30 ? lmap((head * 4 + wave) * 4096) + lane * 16 : OOB;
  vc[6] = so_hand(parmask, lane); vc[7] = 0;
  set_rsrc(sr, 0, p.ws + HF_OFF, (unsigned)HF_BYTES); set_rsrc(sr, 1, p.ws, (unsigned)HAND_BYTES + CTRL_BYTES);
  sc[1] = wave * 10240; sc[2] = 0; sc[3] = type == 1 ? 0x2200 : 0;
#pragma unroll 1
  for (int k = k0; k < NSEG; k += CHUNK) {
    if (wave == 3 && type && k + CHUNK > RING) wait_ge<8>(ctrl, C_OP + head * 32, max(k + CHUNK - RING - 2, 1) + STAG * (2 * (2 - type) + (head & 1)));
    if (wave == 2 && k) wait_tag(ctrl, C_LNF + (24 + min(k + 2 + (lane >> 2) % CHUNK, NSEG - 1)) * 32 + (lane & 3) * 8, tag);
    sc[0] = k;
    TS(lane == 0 && wave == 0 && head == 0, 3100 + type * 16 + k / CHUNK)
    if (type == 1) asm volatile(PROJK_ASM : "+{s[52:55]}"(sc) : "{v[0:7]}"(vc), "{s[36:43]}"(sr), W240_IN(wa) : "memory", "vcc", "scc", PROJK_CLOBBERS);
    else asm volatile(PROJV_ASM : "+{s[52:55]}"(sc) : "{v[0:7]}"(vc), "{s[36:43]}"(sr), W240_IN(wa) : "memory", "vcc", "scc", PROJV_CLOBBERS);
  }
  if (type) return;
  lane = fresh_lane();
  wait_tag(ctrl, C_LNF + (wave * 64 + lane) * 8, tag); wait_tag(ctrl, C_LNF + (256 + (wave & 1) * 32 + (lane & 31)) * 8, tag);
  if (wave == 0) wait_ge<8>(ctrl, nword(parmask, N_CF + head * 128 + 8), 1u);
  BAR();
  first_rows(0);
  TS(lane == 0 && wave == 0 && head == 0, 3100)
  const int g2 = lane >> 4, n = lane & 15, qt = wave & 1, lq = L_MISC + (wave >> 1) * 12288;
  v8u va, vb; v12u init;
  OPAQUE(va); OPAQUE(vb);
  va[0] = lane * 16; va[1] = wave * 10240 + lane * 16; va[2] = L_MISC + wave * 3072 + lane * 16;
  va[3] = lq + qt * 1024 + lane * 16; va[4] = lq + 2048 + lane * 16 + qt * 8; va[5] = head * 65536 + lane * 16;
  va[6] = lmap(head * 4096) + (g2 * 8 + (n & 7)) * 16; va[7] = lmap(head * 4096) + 512 + (g2 * 8 + (n & 7)) * 8;
  vb[0] = so_near(parmask, 3 * (lane % RING) + 2); vb[1] = so_hand(parmask, lane) ^ 0x2000; vb[2] = so_far(parmask, 3 * (lane % RING));
  vb[3] = lmap(PR_OFF) + (head >> 2) * 32768 + ((head & 3) * 2 + qt) * 1024 + lane * 16;
  vb[4] = g2 < 2 ? lmap(PR_OFF) + (head >> 2) * 32768 + 16384 + qt * 1024 + (head & 3) * 256 + n * 16 + g2 * 8 : OOB;
  vb[5] = wave || lane ? OOB : (int)CTRL_OFF + (C_OP + head * 32) * 4;
  const unsigned neg = f2u(-1e30f);
#pragma unroll
  for (int r = 0; r < 4; ++r) { init[r] = g2 < 2 ? 0u : neg; init[4 + r] = 4 * g2 + r <= n + 16 * qt ? 0u : neg; init[8 + r] = 4 * g2 + r + 16 <= n + 16 * qt ? 0u : neg; }
  asm volatile(PROJQ_ASM :: "{v[0:7]}"(va), "{v8}"(so_near(parmask, XB) + lmap(N_CF) + head * 128 + 8), "{v[124:131]}"(vb), "{v[132:143]}"(init), "{s[36:43]}"(sr),
               "{s53}"(wave * 10240), "{s54}"(wave >> 1), W240_IN(wa) : "memory", "vcc", "scc", PROJQ_CLOBBERS);
}

constexpr int L_P = 16384;
struct Row { v4u a; unsigned b; __device__ __forceinline__ unsigned operator[](int i) const { return i < 4 ? a[i] : b; } };
__device__ __forceinline__ Row row_ld(rsrc_t r, int o, int so, int lane) { return Row{ld16s(r, o + lane * 16, so), __builtin_amdgcn_raw_buffer_load_b32(r, o + 1024 + lane * 4, so, 16)}; }
__device__ __forceinline__ void row_st(const unsigned (&x)[5], rsrc_t r, int o, int lane) {
  st16s(v4u{x[0], x[1], x[2], x[3]}, r, o + lane * 16, 0); __builtin_amdgcn_raw_buffer_store_b32(x[4], r, o + 1024 + lane * 4, 0, 16);
}
__device__ __forceinline__ void row_load(const Params& p, rsrc_t rH, int so, int row, int lane, Row (&r)[9]) {
  r[8] = row_ld(mkrsrc(p.x, S * DM * 2), row * 1280, 0, lane);
#pragma unroll
  for (int k = 0; k < 8; ++k) r[k] = row_ld(rH, ((row & 31) / 6) * 16384 + ((row & 31) % 6) * 1280, so + k * 98304, lane);
}
__device__ __forceinline__ void row_finish(const Params& p, int row, int lane, const Row (&r)[9]) {
  const rsrc_t rT = mkrsrc(p.ws + X1_OFF, (unsigned)(WS_END - X1_OFF)), rO = mkrsrc(p.out, S * DM * 2);
  Row pr[5];
#pragma unroll
  for (int k = 0; k < 5; ++k) pr[k] = Row{lds128(L_PR + k * 1280 + lane * 16), *(LAS unsigned*)(L_PR + k * 1280 + 1024 + lane * 4)};
  unsigned ul = 0x3f80u, uh = 0x3f800000u;
  asm("" : "+s"(ul), "+s"(uh));
  const bf2 lo = __builtin_bit_cast(bf2, ul), hi = __builtin_bit_cast(bf2, uh);
  unsigned xs[5], oh[5], o[5];
  float x1[10], sum = 0.f;
#pragma unroll
  for (int i = 0; i < 5; ++i) {
    float a = 0.f, c = 0.f;
#pragma unroll
    for (int k = 0; k < 8; ++k) { const unsigned w = r[k][i]; a = __builtin_amdgcn_fdot2_f32_bf16(__builtin_bit_cast(bf2, w), lo, a, false); c = __builtin_amdgcn_fdot2_f32_bf16(__builtin_bit_cast(bf2, w), hi, c, false); }
    const unsigned yb = pack2(a, c), ga = pack2(lo2f(pr[0][i]) * lo2f(yb), hi2f(pr[0][i]) * hi2f(yb));
    xs[i] = pack2(lo2f(r[8][i]) + lo2f(ga), hi2f(r[8][i]) + hi2f(ga));
    x1[2 * i] = lo2f(xs[i]); x1[2 * i + 1] = hi2f(xs[i]);
    sum += x1[2 * i] + x1[2 * i + 1];
  }
  const float mean = wave_sum(sum) * (1.f / DM);
  float sq = 0.f;
#pragma unroll
  for (int i = 0; i < 10; ++i) sq = fmaf(x1[i] - mean, x1[i] - mean, sq);
  const float mb = rbf(mean), isd = __builtin_amdgcn_rcpf(rbf(__builtin_amdgcn_sqrtf(rbf(rbf(wave_sum(sq) * (1.f / DM)) + 1e-5f))));
  float rs = 0.f;
#pragma unroll
  for (int i = 0; i < 5; ++i) {
    const unsigned d = pack2(x1[2 * i] - mb, x1[2 * i + 1] - mb), nn = pack2(lo2f(d) * isd, hi2f(d) * isd);
    const unsigned m = pack2(lo2f(nn) * lo2f(pr[2][i]), hi2f(nn) * hi2f(pr[2][i]));
    oh[i] = pack2(lo2f(m) + lo2f(pr[1][i]), hi2f(m) + hi2f(pr[1][i]));
    const float h0 = lo2f(oh[i]), h1 = hi2f(oh[i]);
    rs = fmaf(h0, lo2f(pr[4][i]), fmaf(h1, hi2f(pr[4][i]), rs));
    const unsigned gh = pack2(lo2f(pr[3][i]) * h0, hi2f(pr[3][i]) * h1);
    o[i] = pack2(x1[2 * i] + lo2f(gh), x1[2 * i + 1] + hi2f(gh));
  }
  rs = wave_sum(rs);
  __builtin_amdgcn_raw_buffer_store_b32(f2u(rbf(rs)), rT, lane == 0 ? row * 4 : OOB, 2 * (int)HF_BYTES, 16);
  row_st(oh, rT, row * 2560 + 1280, lane); row_st(xs, rT, row * 2560, lane); row_st(o, rO, row * 1280, lane);
}
template <bool LAST>
__device__ __forceinline__ void tok_unit(const Params& p, unsigned long long parmask, int s, int hq, int wave, v4u (&pre)[4][4]) {
  gu32* ctrl = (gu32*)(p.ws + CTRL_OFF);
  const rsrc_t rH = mkrsrc(p.ws, (unsigned)HAND_BYTES + CTRL_BYTES);
  const int lane = fresh_lane(), g = lane >> 4, n = lane & 15, h = hq * 4 + wave, b = 3 * (s % RING);
  const int sH = so_near(parmask, b), sV = so_near(parmask, b + 1) ^ TFAR, sK = so_near(parmask, b + 2), sP = so_far(parmask, 3 * ((s + RING - 4) % RING));
  const int k4 = (lane >> 2) & 3;
  if (wave == 0)
    wait_ge<LAST ? 32 : 8>(ctrl, k4 >= 2 ? C_AF + max(s + 4 - RING, 0) * 32 : !LAST ? (k4 ? C_WP : C_OP) + (hq * 4 + g) * 32 : k4 ? C_QP + (hq * 4 + g) * 32 : nword(parmask, N_PP + s * 512 + (lane & 3) * 128),
                           k4 >= 2 ? (s + 4 < RING ? 0 : 8) : !LAST ? s + 1 + 4 * k4 : k4 ? 8 : 16);
  BAR();
  TS(lane == 0 && wave == 0 && hq == 0, 1024 + s * 8)
  v4u um[10], vo[10][4], pb[2][5];
  Row ri[9];
  const int row = s < 4 ? 0x80000 : (s - 4) * 32 + h;
  auto load_um = [&](int c) {
    um[c] = c < 8 ? UML(rH, lmap(P_OFF + (16 * wave + (n >> 3)) * 4096 + (hq * 4 + g) * 128 + (n & 7) * 16), so_hand(parmask, s) + c * 16384)
                  : UML(rH, lmap(P2_OFF + (16 * wave + (n >> 1)) * 1024 + (hq * 4 + g) * 32 + (n & 1) * 16), so_hand(parmask, s) + (c - 8) * 16384);
  };
  const int vm = (XP & 1) ? OOB : lmap((16 * wave + (n >> 3)) * 16384 + hq * 2048 + g * 128 + (n & 7) * 16), vt = lmap(V2_OFF + (16 * wave + (n >> 1)) * 4096 + hq * 512 + g * 32 + (n & 1) * 16);
  auto load_vo = [&](int c0, int c1) {
#pragma unroll
    for (int c = c0; c < c1; ++c) {
      if (LAST || c >= 4)
#pragma unroll
      for (int k = 0; k < 4; ++k) vo[c][k] = __builtin_amdgcn_raw_buffer_load_b128(rH, c < 8 ? vm + k * 512 : vt + k * 128, c < 8 ? sV + c * 65536 : (sH ^ TFAR) + (c - 8) * 65536, LAST ? LPOL : TPOL);
      if (!LAST) load_um(c);
    }
  };
  auto load_pre = [&](int so, int c0) {
#pragma unroll
    for (int i = 0; i < 8; ++i) pre[c0 + (i >> 2)][i & 3] = __builtin_amdgcn_raw_buffer_load_b128(rH, vm + (i & 3) * 512, so + (c0 + (i >> 2)) * 65536, TPOL);
  };
  if (LAST) {
    v4u qf[2][6], kf[2][6], qx[4], kx[4], km[6];
    const int sQ = (QB << 21) + (s - HS) * 524288;
#pragma unroll
    for (int w = 0; w < 4; ++w) {
#pragma unroll
      for (int t = 0; t < 2; ++t) { qf[t][w] = ld16s(rH, h * 16384 + w * 4096 + t * 1024 + lane * 16, sQ); kf[t][w] = ld16s(rH, h * 65536 + w * 16384 + t * 1024 + lane * 16, sK); }
      qx[w] = ld16s(rH, h * 16384 + w * 4096 + 2048 + lane * 16, sQ); kx[w] = ld16s(rH, h * 65536 + w * 16384 + 2048 + lane * 16, sK);
    }
    load_vo(0, 10);
    __builtin_amdgcn_sched_barrier(0);
#pragma unroll
    for (int w = 0; w < 4; ++w)
#pragma unroll
      for (int t = 0; t < 2; ++t)
#pragma unroll
        for (int k = 0; k < 2; ++k) { qf[t][4 + (w >> 1)][2 * (w & 1) + k] = qx[w][2 * t + k]; kf[t][4 + (w >> 1)][2 * (w & 1) + k] = kx[w][2 * t + k]; }
    f4 sc[2][3] = {};
#pragma unroll
    for (int f = 0; f < 6; ++f) { sc[0][1] = mfma32(kf[0][f], qf[0][f], sc[0][1]); sc[1][1] = mfma32(kf[0][f], qf[1][f], sc[1][1]); sc[1][2] = mfma32(kf[1][f], qf[1][f], sc[1][2]); }
    if (wave == 0) wait_ge<LPOLL>(ctrl, nword(parmask, N_CF + (hq * 4 + g) * 128 + (s >= NSEG - 2 ? min(lane & 3, 2) : 2) * 4), min(s + 2, NSEG));
    BAR();
#pragma unroll
    for (int w = 0; w < 4; ++w) {
      km[w] = ld16s(rH, lmap(h * 4096) + (g * 8 + (n & 7)) * 16 + w * 1024, so_hand(parmask, s) ^ 0x2000);
      const v2u a = ld8s(rH, lmap(h * 4096) + 512 + (g * 8 + (n & 7)) * 8 + w * 1024, so_hand(parmask, s) ^ 0x2000);
      km[4 + (w >> 1)][2 * (w & 1)] = a[0]; km[4 + (w >> 1)][2 * (w & 1) + 1] = a[1];
    }
#pragma unroll
    for (int c = 0; c < 10; ++c) load_um(c);
    __builtin_amdgcn_sched_barrier(0);
    TS(lane == 0 && wave == 0 && hq == 0, 1025 + s * 8)
#pragma unroll
    for (int f = 0; f < 6; ++f) { sc[0][0] = mfma32(km[f], qf[0][f], sc[0][0]); sc[1][0] = mfma32(km[f], qf[1][f], sc[1][0]); }
#pragma unroll
    for (int t = 0; t < 2; ++t) {
      float e[12], mx = -3e38f, sum = 0.f;
#pragma unroll
      for (int k = 0; k < 8 + 4 * t; k += 2) {
        const unsigned u = pack2(sc[t][k >> 2][k & 3], sc[t][k >> 2][(k & 3) + 1]), w = pack2(lo2f(u) * 0.07216878364870322f, hi2f(u) * 0.07216878364870322f);
#pragma unroll
        for (int i = 0; i < 2; ++i) {
          const bool on = k < 4 ? g < 2 : (k >> 2) - 1 < t || 4 * g + (k & 3) + i <= n;
          e[k + i] = on ? (i ? hi2f(w) : lo2f(w)) : -3e38f;
          mx = fmaxf(mx, e[k + i]);
        }
      }
      mx = max_x32(max_x16(mx));
#pragma unroll
      for (int k = 0; k < 8 + 4 * t; ++k) { e[k] = __builtin_amdgcn_exp2f((e[k] - mx) * 1.4426950408889634f); sum += e[k]; }
      sum = swapsum16(sum, sum); sum = swapsum32(sum, sum);
      const float r = __builtin_amdgcn_rcpf(sum);
      *(LAS v4u*)(L_P + (wave * 2 + t) * 1024 + lane * 16) = v4u{pack2(e[4] * r, e[5] * r), pack2(e[6] * r, e[7] * r), t ? pack2(e[8] * r, e[9] * r) : 0u, t ? pack2(e[10] * r, e[11] * r) : 0u};
      *(LAS v2u*)(L_P + 8192 + (g >> 1) * 2048 + t * 1024 + wave * 256 + n * 16 + (g & 1) * 8) = v2u{pack2(e[0] * r, e[1] * r), pack2(e[2] * r, e[3] * r)};
    }
    LGKM0();
  } else {
#pragma unroll
    for (int k = 0; k < 3; ++k) dma16_sc1(rH, lmap(PR_OFF) + hq * 32768 + lane * 16, (sH ^ 0x2000) + (k < 2 ? k * 4096 : 16384) + wave * 1024, L_P + k * 4096 + wave * 1024);
    if (s < 4) { load_pre(sV, 0); load_pre(sV, 2); VMCNT(0); }
    load_vo(0, 6);
    __builtin_amdgcn_sched_barrier(0);
    VMCNT(14);
#pragma unroll
    for (int i = 0; i < 16; ++i) vo[i >> 2][i & 3] = pre[i >> 2][i & 3];
  }
  BAR();
#pragma unroll
  for (int k = 0; k < 10; ++k) pb[k & 1][k >> 1] = lds128(L_P + k * 1024 + lane * 16);
  TS(lane == 0 && wave == 0 && hq == 0, 1026 + s * 8)
  int so[2];
#pragma unroll
  for (int t = 0; t < 2; ++t) so[t] = hq * 98304 + ((16 * t + n) / 6) * 16384 + ((16 * t + n) % 6) * 1280 + (g & 1) * 32 + (g >> 1) * 16;
  f4 d[2][2][2];
  unsigned af = 0;
#pragma unroll
  for (int c = 0; c < 12; c += 2) {
    if (!LAST) {
      if (c == 2) af = __hip_atomic_load(ctrl + C_AF + max(s - 4, 0) * 32, RLX_AGENT);
      if (c == 2 || c == 4) load_vo(c + 4, c + 6);
      if (c == 6) {
        if (s >= 4 && rfl(af) < 8) wait_ge<2>(ctrl, C_AF + (s - 4) * 32, 8u);
        row_load(p, rH, sP, row, lane, ri);
      }
      __builtin_amdgcn_sched_barrier(0);
    }
    if (c < 10) {
#pragma unroll
      for (int k = 0; k < 4; ++k) asm volatile("" : "+a"(vo[c][k]), "+a"(vo[c + 1][k]));
      asm volatile("" : "+a"(um[c]), "+a"(um[c + 1]));
#pragma unroll
      for (int t = 0; t < 2; ++t)
#pragma unroll
        for (int k = 0; k < 5; ++k)
#pragma unroll
          for (int u = 0; u < 2; ++u) d[(c >> 1) & 1][t][u] = mfma32(k < 4 ? vo[c + u][k] : um[c + u], pb[t][k], k ? d[(c >> 1) & 1][t][u] : f4{});
    }
    if (c > 0)
#pragma unroll
      for (int t = 0; t < 2; ++t) {
        const f4 x = d[~(c >> 1) & 1][t][0], y = d[~(c >> 1) & 1][t][1];
        auto lo = __builtin_amdgcn_permlane16_swap(pack2(x[0], x[1]), pack2(y[0], y[1]), false, false);
        auto hi = __builtin_amdgcn_permlane16_swap(pack2(x[2], x[3]), pack2(y[2], y[3]), false, false);
        st16s(v4u{lo[0], hi[0], lo[1], hi[1]}, rH, so[t] + (c < 10 ? 32 * (c - 2) : 1024), (sH ^ 0x2000) + (c < 10 ? 256 : 64) * wave);
      }
    __builtin_amdgcn_sched_barrier(0);
  }
  TS(lane == 0 && wave == 0 && hq == 0, 1027 + s * 8)
  const bool rel = !LAST && s + RING < NSEG;
  if (!LAST) {
    const int sN = s + 4 < HS ? so_near(parmask, 3 * ((s + 4) % RING) + 1) ^ TFAR : OOB;
    load_pre(sN, 0);
    __builtin_amdgcn_sched_barrier(0);
    row_finish(p, row, lane, ri);
    __builtin_amdgcn_sched_barrier(0);
    load_pre(sN, 2);
    __builtin_amdgcn_sched_barrier(0);
    VMCNT(23);
  } else VMCNT(0);
  BAR();
  if (wave == 0 && lane < (rel ? 2 : 1)) __hip_atomic_fetch_add(ctrl + (lane ? C_CL + (s + RING) * 32 : C_AF + s * 32), 1u, RLX_AGENT);
  TS(lane == 0 && wave == 0 && hq == 0, 1028 + s * 8)
}

__device__ __forceinline__ void mod_gather(const Params& p, int n0, int n1, int tid, unsigned tag) {
  const rsrc_t rm = mkrsrc(p.ws + CTRL_OFF + C_MOD * 4, 15360);
  if (tid < 80) *(LAS v4u*)(LP + 7680 + tid * 16) = ((const v4u*)p.wr)[tid];
#pragma unroll 1
  for (int n = n0; n < n1; ++n)
#pragma unroll 1
    for (int spin = 0; spin <= SPIN_CAP; ++spin) {
      const int o = min(n * 256 + tid, 959);
      const v4u a = ld16s(rm, o * 16, 0);
      *(LAS v2u*)(LP + o * 8) = v2u{a[0], a[2]};
      if (__builtin_amdgcn_ballot_w64(a[1] != tag || a[3] != tag) == 0) break;
      __builtin_amdgcn_s_sleep(8);
    }
  LGKM0(); BAR();
}
__device__ __forceinline__ void tok_role(const Params& p, int j, int lane, int wave, unsigned tag) {
  gu32* ctrl = (gu32*)(p.ws + CTRL_OFF);
  const rsrc_t rH = mkrsrc(p.ws, (unsigned)HAND_BYTES + CTRL_BYTES);
  const int tid = wave * 64 + lane, hq = j & 7, grp = j >> 3;
  const rsrc_t rm = mkrsrc(p.ws + CTRL_OFF + C_MOD * 4, 15360);
  {
    v4u a[10], own[3];
    float mb, isd;
    mod_unit(p, j < 28 ? j : 60, tid, tag, [&] {
#pragma unroll
      for (int i = 0; i < 10; ++i) a[i] = ln1_piece(p, j * 8, i, lane);
#pragma unroll
      for (int i = 0; i < 3; ++i) own[i] = ln1_piece(p, j * 8, min(wave + 4 * i, 9), lane);
    });
    TS(tid == 0, 896 + j)
    ln1_stats(a, mb, isd);
#pragma unroll
    for (int i = 0; i < 3; ++i)
#pragma unroll
      for (int k = 0; k < 4; ++k) own[i][k] = ln_norm(own[i][k], mb, isd);
    v4u g[3][4];
#pragma unroll 1
    for (int spin = 0; spin <= SPIN_CAP; ++spin) {
      bool bad = false;
#pragma unroll
      for (int i = 0; i < 3; ++i)
#pragma unroll
        for (int k = 0; k < 4; ++k) g[i][k] = ld16s(rm, (lane >> 3) * 32 + (k & 1) * 16 + (k >> 1) * 2560, min(wave + 4 * i, 9) * 256);
#pragma unroll
      for (int i = 0; i < 12; ++i) bad |= g[i >> 2][i & 3][1] != tag || g[i >> 2][i & 3][3] != tag;
      if (__builtin_amdgcn_ballot_w64(bad) == 0) break;
      __builtin_amdgcn_s_sleep(2);
    }
    TS(tid == 0, 928 + j)
#pragma unroll
    for (int i = 0; i < 3; ++i) {
#pragma unroll
      for (int k = 0; k < 4; ++k) own[i][k] = ln_mod(own[i][k], g[i][2 + (k >> 1)][2 * (k & 1)], g[i][k >> 1][2 * (k & 1)]);
      if (wave + 4 * i < 10) ln1_store(p, own[i], j * 8, wave + 4 * i, lane);
    }
    VMCNT(0);
    if (lane == 0) __hip_atomic_store(ctrl + C_LNF + j * 32 + wave * 8, tag, RLX_AGENT);
    TS(tid == 0, 800 + j)
  }
  mod_gather(p, 0, 2, tid, tag);
  const unsigned long long parmask = wait_init(ctrl, tag, lane);
#pragma unroll 1
  for (int seg = j < 24 ? 8 + j : 32 + j; seg < (j < 24 ? HS : NSEG); seg += 24) {
    const int row0 = seg * 32 + wave * 8, lane = fresh_lane();
    v4u a[10];
#pragma unroll
    for (int i = 0; i < 10; ++i) a[i] = ln1_piece(p, row0, i, lane);
    float mb, isd;
    ln1_stats(a, mb, isd);
#pragma unroll
    for (int i = 0; i < 10; ++i) {
      const v4u sh = lds128(LP + (lane >> 3) * 16 + i * 128), sc = lds128(LP + 1280 + (lane >> 3) * 16 + i * 128);
#pragma unroll
      for (int k = 0; k < 4; ++k) a[i][k] = ln_mod(ln_norm(a[i][k], mb, isd), sc[k], sh[k]);
      ln1_store(p, a[i], row0, i, lane);
    }
    VMCNT(0);
    if (lane == 0) __hip_atomic_store(ctrl + C_LNF + (24 + seg) * 32 + wave * 8, tag, RLX_AGENT);
  }
  mod_gather(p, 2, 4, tid, tag);
  TS(tid == 0, 832 + j)
  v4u pre[4][4] = {};
#pragma unroll 1
  for (int s = grp; s < HS; s += 4) tok_unit<false>(p, parmask, s, hq, wave, pre);
}
__device__ __forceinline__ void last_row(const Params& p, unsigned long long parmask, int s, int hq, int wave, unsigned tag) {
  gu32* ctrl = (gu32*)(p.ws + CTRL_OFF);
  const int lane = fresh_lane(), row = s * 32 + hq * 4 + wave;
  wait_ge<1>(ctrl, C_AF + s * 32, 8u);
  Row ri[9];
  row_load(p, mkrsrc(p.ws, (unsigned)HAND_BYTES), so_far(parmask, 3 * (s % RING)), row, lane, ri);
  row_finish(p, row, lane, ri);
  VMCNT(0);
  unsigned t = 0;
  if (lane == 0) t = __hip_atomic_fetch_add(ctrl + C_FIN, 1u, RLX_AGENT);
  if (rfl(t) == 3 * 128 - 1) {
    TS(lane == 0, 3074)
    select_rows(ctrl, mkrsrc(p.ws + X1_OFF, (unsigned)(WS_END - X1_OFF)), lane);
    TS(lane == 0, 3075)
    if (lane == 0) __hip_atomic_store(ctrl + C_EPOCH, tag, RLX_AGENT);
  }
}

__global__ void __launch_bounds__(256, 1) k_main(Params p) {
  extern __shared__ __attribute__((aligned(16))) unsigned char smem[];
  const int tid = threadIdx.x, wave = rfl(tid >> 6), blk = blockIdx.x;
  {
    unsigned long long pc;
    asm volatile("s_getpc_b64 s[40:41]\n\ts_add_u32 s40, s40, _Z6k_main6Params@rel32@lo+4\n\ts_addc_u32 s41, s41, _Z6k_main6Params@rel32@hi+12" : "={s[40:41]}"(pc) :: "scc");
    dma16(mkrsrc((const void*)pc, CODE_BYTES), (((blk >> 3) % 5) * 256 + tid) * 128, 0, ((blk & 7) < 3 ? 12288 : L_MISC + 1024) + wave * 1024);
  }
  const unsigned tag = (*(const __attribute__((address_space(4))) unsigned*)uni64((unsigned long long)(p.ws + CTRL_OFF)) + 4u) & ~3u;
  const int x = blk & 7, t = blk >> 3;
  const int idx = (x - 3) * 32 + t, role = x < 3 ? x * 32 + t : 96 + (idx % 5) * 32 + idx / 5;
  TS(tid == 0, role)
  if (role < 128) {
    if (role < 96) chain_role(p, role, fresh_lane(), wave, tag);
    else vo_role(p, role - 96, fresh_lane(), wave, tag);
    const __attribute__((address_space(4))) Params* pk = (const __attribute__((address_space(4))) Params*)__builtin_amdgcn_kernarg_segment_ptr();
    asm volatile("" : "+s"(pk) :: "memory");
    const int ln = fresh_lane();
    Params p2 = {};
    p2.wg = pk->wg; p2.wu = pk->wu; p2.wd = pk->wd; p2.out = pk->out; p2.ws = pk->ws;
    ffn_role(p2, role, ln, wave);
  }
  else {
    if (role < 160) tok_role(p, role - 128, fresh_lane(), wave, tag);
    else proj_role(p, (role - 160) >> 5, role & 31, blk, fresh_lane(), wave, tag);
    if (role < 160 || role >= 192) {
      const unsigned long long parmask = wait_init((gu32*)(p.ws + CTRL_OFF), tag, fresh_lane());
      const int s = (role < 160 ? HS - 4 : role < 224 ? HS + 4 : HS) + ((role >> 3) & 3);
      if (role >= 192) { mod_gather(p, 1, 4, wave * 64 + fresh_lane(), tag); v4u pre[4][4]; tok_unit<true>(p, parmask, s, role & 7, wave, pre); }
      last_row(p, parmask, s, role & 7, wave, tag);
    }
  }
  TS(wave == 0 && fresh_lane() == 0, 2048 + role)
}

#define SCHED_DB 84
#define SCHED_DM 74
#define SCHED_DS 115
#define SCHED_LEAD 250

extern "C" void kernel_launch(void* const* d_in, const int* in_sizes, int n_in, void* d_out, int out_size, void* d_ws, size_t ws_size,
                              hipStream_t stream) {
  Params p;
  p.x = (const u16*)d_in[0]; p.mem0 = (const u16*)d_in[1]; p.wq = (const u16*)d_in[2]; p.wk = (const u16*)d_in[3];
  p.wv = (const u16*)d_in[4]; p.wo = (const u16*)d_in[5]; p.wr = (const u16*)d_in[8]; p.wg = (const u16*)d_in[9];
  p.wu = (const u16*)d_in[10]; p.wd = (const u16*)d_in[11]; p.c = (const u16*)d_in[12]; p.wmod = (const u16*)d_in[13];
  p.out = (u16*)d_out;
  p.ws = (unsigned char*)(((uintptr_t)d_ws + 2 * MB - 1) & ~(uintptr_t)(2 * MB - 1));
  p.dB = SCHED_DB; p.dM = SCHED_DM; p.dS = SCHED_DS; p.lead = SCHED_LEAD;
  (void)hipFuncSetAttribute((const void*)k_main, hipFuncAttributeMaxDynamicSharedMemorySize, MAIN_LDS);
  k_main<<<256, 256, MAIN_LDS, stream>>>(p);
}
```

```cpp
#include <hip/hip_runtime.h>
#include <hip/hip_bf16.h>
#include <stdint.h>

#define GAS __attribute__((address_space(1)))
#define LAS __attribute__((address_space(3)))
#define RLX_AGENT __ATOMIC_RELAXED, __HIP_MEMORY_SCOPE_AGENT
typedef unsigned short u16;
typedef unsigned v4u __attribute__((ext_vector_type(4)));
typedef unsigned v2u __attribute__((ext_vector_type(2)));
typedef unsigned v8u __attribute__((ext_vector_type(8)));
typedef float f4 __attribute__((ext_vector_type(4)));
typedef __bf16 bf2 __attribute__((ext_vector_type(2)));
typedef __bf16 bf8 __attribute__((ext_vector_type(8)));
typedef __bf16 bf16v __attribute__((ext_vector_type(16)));
typedef short s4 __attribute__((ext_vector_type(4)));
typedef GAS unsigned gu32;
typedef __amdgpu_buffer_rsrc_t rsrc_t;

constexpr int S = 2048, DM = 640, NH = 32, DH = 192, HD = NH * DH, NSEG = 64, FF = 1536, TOPK = 8;

constexpr size_t MB = 1u << 20;
constexpr int RING = 20, CHUNK = 6, XB = 3 * RING, QB = 62, HB = 64;
constexpr int HS = 56;
constexpr size_t HAND_BYTES = 192 * MB;
constexpr size_t CTRL_OFF = HAND_BYTES;
constexpr unsigned CTRL_BYTES = 131072;
constexpr size_t HF_OFF = CTRL_OFF + CTRL_BYTES;
constexpr size_t HF_BYTES = (size_t)S * DM * 2;
constexpr size_t X1_OFF = HF_OFF + HF_BYTES;
constexpr size_t R_OFF = X1_OFF + 2 * HF_BYTES;
constexpr size_t ACT_OFF = R_OFF + S * 4;
constexpr size_t WS_END = ACT_OFF + TOPK * FF * 2;
static_assert(WS_END + 2 * MB <= 256 * MB, "workspace");

constexpr int C_EPOCH = 0;
constexpr int C_INIT = 64;
constexpr int C_LNF = 24576;
constexpr int C_MOD = 27648;
constexpr int C_Z0 = 2048;
constexpr int C_ANCH = 2048;
constexpr int C_ERR = 2304;
constexpr int C_FIN = 2336;
constexpr int C_IDX = 2560;
constexpr int C_AF = 4096;
constexpr int C_CL = 6144;
constexpr int C_VP = 8192;
constexpr int C_QP = 9216;
constexpr int C_OP = 10240;
constexpr int C_WP = 11264;
constexpr int C_Z1 = 12288;
constexpr int C_STAT = 12288;

#define QSLEEP 60
#define STAG 1
#define CALREP 10
#define MAP 0
#define XP 0
#define TFAR 0x2000
#define TPOL 16
#define UML ld16s
#define PACE 0
#define LPOL 16
#define LPOLL 3
#define FFN_WAIT 100
constexpr int M_OFF = 0, E_OFF = 16384, S_OFF = 32768, CPY = 40960, P_OFF = 4 * CPY, P2_OFF = P_OFF + 64 * 4096, V2_OFF = 393216, PR_OFF = 655360;
constexpr int N_PP = 0x80000, N_CF = N_PP + 32768, N_REC = N_CF + 8192, N_END = N_REC + 16384;
static_assert(P_OFF + 80 * 4096 == 120 * 4096 && P_OFF % 8192 == 0, "block");
__host__ __device__ constexpr int lmap(int off) { return ((off >> 13) << 14) | (off & 8191); }
constexpr int OOB = 0x40000000;
__host__ __device__ constexpr int tslot(int r) { return (r & ~15) | ((r & 3) << 2) | ((r >> 2) & 3); }

__device__ __forceinline__ float u2f(unsigned v) { return __builtin_bit_cast(float, v); }
__device__ __forceinline__ unsigned f2u(float v) { return __builtin_bit_cast(unsigned, v); }
__device__ __forceinline__ float bf2f(u16 v) { return __builtin_bit_cast(float, (unsigned)v << 16); }
__device__ __forceinline__ float lo2f(unsigned v) { return __builtin_bit_cast(float, v << 16); }
__device__ __forceinline__ float hi2f(unsigned v) { return __builtin_bit_cast(float, v & 0xffff0000u); }
__device__ __forceinline__ unsigned pack2(float a, float b) {
  bf2 v = {(__bf16)a, (__bf16)b};
  unsigned r = __builtin_bit_cast(unsigned, v);
  asm("" : "+v"(r));
  return r;
}
__device__ __forceinline__ float rbf(float a) { return (float)(__bf16)a; }
__device__ __forceinline__ unsigned long long uni64(unsigned long long a) {
  return (unsigned)__builtin_amdgcn_readfirstlane((unsigned)a) | (unsigned long long)(unsigned)__builtin_amdgcn_readfirstlane((unsigned)(a >> 32)) << 32;
}
__device__ __forceinline__ rsrc_t mkrsrc(const void* p, unsigned bytes) { return __builtin_amdgcn_make_buffer_rsrc((void*)uni64((unsigned long long)p), 0, bytes, 0x00020000); }
__device__ __forceinline__ unsigned xcc_id() { return (unsigned)__builtin_amdgcn_s_getreg(20 | (3 << 11)) & 0xfu; }
__device__ __forceinline__ int rfl(int v) { return __builtin_amdgcn_readfirstlane(v); }
__device__ __forceinline__ f4 mfma32(v4u a, v4u b, f4 c) { return __builtin_amdgcn_mfma_f32_16x16x32_bf16(__builtin_bit_cast(bf8, a), __builtin_bit_cast(bf8, b), c, 0, 0, 0); }
__device__ __forceinline__ f4 mfma16(v2u a, v2u b, f4 c) { return mfma32(v4u{a[0], a[1], 0u, 0u}, v4u{b[0], b[1], 0u, 0u}, c); }
__device__ __forceinline__ f4 smfma(v4u a, v8u b, f4 c, int idx) { return __builtin_amdgcn_smfmac_f32_16x16x64_bf16(__builtin_bit_cast(bf8, a), __builtin_bit_cast(bf16v, b), c, idx, 0, 0); }
__device__ __forceinline__ v2u trread(unsigned addr) { return __builtin_bit_cast(v2u, __builtin_amdgcn_ds_read_tr16_b64_v4i16((LAS s4*)addr)); }
__device__ __forceinline__ v4u lds128(unsigned addr) { return *(LAS v4u*)addr; }
__device__ __forceinline__ v2u lds64(unsigned addr) { return *(LAS v2u*)addr; }
__device__ __forceinline__ void dma16(rsrc_t r, int voff, int soff, unsigned ldsaddr) {
  asm volatile("s_mov_b32 m0, %0\n\ts_nop 3\n\tbuffer_load_dwordx4 %1, %2, %3 offen lds" ::"s"(ldsaddr), "v"(voff), "s"(r), "s"(soff) : "memory");
}
__device__ __forceinline__ void dma16_sc1(rsrc_t r, int voff, int soff, unsigned ldsaddr) {
  asm volatile("s_mov_b32 m0, %0\n\ts_nop 3\n\tbuffer_load_dwordx4 %1, %2, %3 offen sc1 lds" ::"s"(ldsaddr), "v"(voff), "s"(r), "s"(soff) : "memory");
}
#define VMCNT(n) asm volatile("s_waitcnt vmcnt(" #n ")" ::: "memory")
#define LGKM0() asm volatile("s_waitcnt lgkmcnt(0)" ::: "memory")
#define BAR() asm volatile("s_barrier" ::: "memory")
__device__ __forceinline__ float swapsum32(float a, float b) {
  auto r = __builtin_amdgcn_permlane32_swap(__builtin_bit_cast(unsigned, a), __builtin_bit_cast(unsigned, b), false, false);
  return u2f(r[0]) + u2f(r[1]);
}
__device__ __forceinline__ float swapsum16(float a, float b) {
  auto r = __builtin_amdgcn_permlane16_swap(__builtin_bit_cast(unsigned, a), __builtin_bit_cast(unsigned, b), false, false);
  return u2f(r[0]) + u2f(r[1]);
}
__device__ __forceinline__ float max_x32(float a) {
  auto r = __builtin_amdgcn_permlane32_swap(__builtin_bit_cast(unsigned, a), __builtin_bit_cast(unsigned, a), false, false);
  return fmaxf(u2f(r[0]), u2f(r[1]));
}
__device__ __forceinline__ float max_x16(float a) {
  auto r = __builtin_amdgcn_permlane16_swap(__builtin_bit_cast(unsigned, a), __builtin_bit_cast(unsigned, a), false, false);
  return fmaxf(u2f(r[0]), u2f(r[1]));
}
__device__ __forceinline__ unsigned ror8u(unsigned v) { return (unsigned)__builtin_amdgcn_update_dpp(0, (int)v, 0x128, 0xf, 0xf, true); }
__device__ __forceinline__ float ror8f(float v) { return __builtin_bit_cast(float, ror8u(__builtin_bit_cast(unsigned, v))); }
__device__ __forceinline__ float wave_sum(float v) {
  v += u2f(__builtin_amdgcn_update_dpp(0, (int)f2u(v), 0xb1, 0xf, 0xf, true)); v += u2f(__builtin_amdgcn_update_dpp(0, (int)f2u(v), 0x4e, 0xf, 0xf, true));
  v += u2f(__builtin_amdgcn_update_dpp(0, (int)f2u(v), 0x124, 0xf, 0xf, true)); v += ror8f(v);
  v = swapsum16(v, v);
  return swapsum32(v, v);
}
typedef unsigned short us2 __attribute__((ext_vector_type(2)));
__device__ __forceinline__ unsigned unsent(unsigned w) { return __builtin_bit_cast(unsigned, __builtin_elementwise_min(__builtin_bit_cast(us2, w), us2{0xfffe, 0xfffe})); }

constexpr unsigned CODE_BYTES = 141312;
struct Params {
  const u16 *x, *mem0, *wq, *wk, *wv, *wo, *wr, *wg, *wu, *wd, *c, *wmod;
  u16* out;
  unsigned char* ws;
  int dB, dM, dS, lead;
};

constexpr int L_MEM = 0, L_Q = 10240, L_RED = 16384, L_WO = 20480, L_VO = 20480, L_K = 40960, L_Q2 = L_Q + 2048;
constexpr int L_MISC = 122880;
constexpr int MAIN_LDS = 163840;
constexpr int SPIN_CAP = 1 << 18;
typedef unsigned v12u __attribute__((ext_vector_type(12)));
typedef unsigned v16u __attribute__((ext_vector_type(16)));
typedef unsigned v32u __attribute__((ext_vector_type(32)));

#define CHAIN_CLOBBERS \
  "v32", "v33", "v34", "v35", "v36", "v37", "v38", "v39", "v40", "v41", "v42", "v43", "v44", "v45", "v46", "v47", "v48", "v49", "v50", "v51", "v52", \
  "v53", "v54", "v55", "v56", "v57", "v58", "v59", "v60", "v61", "v62", "v63", "v64", "v65", "v66", "v67", "v68", "v69", "v70", "v71", "v72", "v73", \
  "v74", "v75", "v76", "v77", "v78", "v79", "v80", "v81", "v82", "v83", "v84", "v85", "v86", "v87", "v88", "v89", "v90", "v91", "v92", "v93", "v94", \
  "v95", "v100", "v101", "v102", "v103", "v104", "v105", "v106", "v107", "v108", "v109", "v110", "v111", "v112", "v113", "v114", "v115", "v116", "v117", \
  "v118", "v119", "v120", "v121", "v122", "v123", "v124", "v125", "v126", "v127", "v128", "v129", "v130", "v131", "v132", "v133", "v134", "v135", \
  "v136", "v137", "v138", "v139", "v140", "v141", "v142", "v143", "v144", "v145", "v146", "v147", "v148", "v149", "v150", "v151", "v152", "v153", \
  "v154", "v155", "v156", "v157", "v158", "v159", "v160", "v161", "v162", "v163", "v164", "v165", "v166", "v167", "v168", "v169", "v170", "v171", \
  "v172", "v173", "v174", "v175", "v176", "v177", "v178", "v179", "v180", "v181", "v182", "v183", "v184", "v185", "v186", "v187", "v188", "v189", \
  "v190", "v191", "v224", "v225", "v226", "v227", "v228", "v229", "v230", "v231", "v232", "v233", "v234", "v235", "v236", "v237", "v238", "v239", \
  "v240", "v241", "v242", "v243", "v244", "v245", "v246", "v247", "v248", "v249", "v250", "v251", "v252", "v253", "v254", "v255", "s18", "s19", "s20", \
  "s21", "s22", "s23", "s24", "s25", "s26", "s27", "s28", "s29", "s30", "s31", "s34", "s35", "s64", "s65", "s66", "s67", "s68", "s69", "s70", "s71", \
  "s72", "s73", "s74", "s75", "s76", "s77", "s78", "s79", "s80", "s81", "s82", "s83", "s84", "s85", "s86", "s87", "s88", "s89", "s90", "s91", "s92", \
  "s93", "s94", "s95", "s96", "s97", "s98", "s99", "a0", "a1", "a2", "a3", "a4", "a5", "a6", "a7", "a8", "a9", "a10", "a11", "a12", "a13", "a14", "a15", \
  "a16", "a17", "a18", "a19", "a20", "a21", "a22", "a23", "a24", "a25", "a26", "a27", "a28", "a29", "a30", "a31", "a32", "a33", "a34", "a35", "a36", \
  "a37", "a38", "a39", "a40", "a41", "a42", "a43", "a44", "a45", "a46", "a47", "a48", "a49", "a50", "a51", "a52", "a53", "a54", "a55", "a56", "a57", \
  "a58", "a59", "a60", "a61", "a62", "a63", "a64", "a65", "a66", "a67", "a68", "a69", "a70", "a71", "a72", "a73", "a74", "a75", "a76", "a77", "a78", \
  "a79", "a80", "a81", "a82", "a83", "a84", "a85", "a86", "a87", "a88", "a89", "a90", "a91", "a92", "a93", "a94", "a95", "a96", "a97", "a98", "a99", \
  "a100", "a101", "a102", "a103", "a104", "a105", "a106", "a107", "a108", "a109", "a110", "a111", "a112", "a113", "a114", "a115", "a116", "a117", \
  "a118", "a119", "a120", "a121", "a122", "a123", "a124", "a125", "a126", "a127", "a128", "a129", "a130", "a131", "a132", "a133", "a134", "a135", \
  "a136", "a137", "a138", "a139", "a140", "a141", "a142", "a143", "a144", "a145", "a146", "a147", "a148", "a149", "a150", "a151", "a152", "a153", \
  "a154", "a155", "a156", "a157", "a158", "a159", "a160", "a161", "a162", "a163", "a164", "a165", "a166", "a167", "a168", "a169", "a170", "a171", \
  "a172", "a173", "a174", "a175", "a176", "a177", "a178", "a179", "a180", "a181", "a182", "a183", "a184", "a185", "a186", "a187", "a188", "a189", \
  "a190", "a191", "a192", "a193", "a194", "a195", "a196", "a197", "a198", "a199", "a200", "a201", "a202", "a203", "a204", "a205", "a206", "a207", \
  "a208", "a209", "a210", "a211", "a212", "a213", "a214", "a215", "a216", "a217", "a218", "a219", "a220", "a221", "a222", "a223", "a224", "a225", \
  "a226", "a227", "a228", "a229", "a230", "a231", "a232", "a233", "a234", "a235", "a236", "a237", "a238", "a239", "a240", "a241", "a242", "a243", \
  "a244", "a245", "a246", "a247", "a248", "a249", "a250", "a251", "a252", "a253", "a254", "a255"
#define CHAIN_ASM \
  "s_and_b32 s71, s48, 3\n" \
  "s_cmp_eq_u32 s71, 2\n" \
  "s_cbranch_scc1 LF2_%=\n" \
  "s_cmp_ge_u32 s48, 8\n" \
  "s_cbranch_scc1 LF1_%=\n" \
  "s_mov_b32 s71, 0\n" \
  "s_add_u32 m0, s56, 57344\n" \
  "s_nop 0\n" \
  "buffer_load_dwordx4 v192, s[40:43], s71 offen lds\n" \
  "s_add_u32 m0, s56, 58368\n" \
  "s_nop 0\n" \
  "buffer_load_dwordx4 v193, s[40:43], s71 offen lds\n" \
  "s_add_u32 m0, s56, 59392\n" \
  "s_nop 0\n" \
  "buffer_load_dwordx4 v194, s[40:43], s71 offen lds\n" \
  "s_add_u32 m0, s56, 60416\n" \
  "s_nop 0\n" \
  "buffer_load_dwordx4 v195, s[40:43], s71 offen lds\n" \
  "s_add_u32 m0, s56, 61440\n" \
  "s_nop 0\n" \
  "buffer_load_dwordx4 v196, s[40:43], s71 offen lds\n" \
  "s_add_u32 m0, s56, 62464\n" \
  "s_nop 0\n" \
  "buffer_load_dwordx4 v197, s[40:43], s71 offen lds\n" \
  "s_add_u32 m0, s56, 63488\n" \
  "s_nop 0\n" \
  "buffer_load_dwordx4 v198, s[40:43], s71 offen lds\n" \
  "s_add_u32 m0, s56, 64512\n" \
  "s_nop 0\n" \
  "buffer_load_dwordx4 v199, s[40:43], s71 offen lds\n" \
  "s_mov_b32 s71, 786432\n" \
  "s_add_u32 m0, s56, 90112\n" \
  "s_nop 0\n" \
  "buffer_load_dwordx4 v192, s[40:43], s71 offen lds\n" \
  "s_add_u32 m0, s56, 91136\n" \
  "s_nop 0\n" \
  "buffer_load_dwordx4 v193, s[40:43], s71 offen lds\n" \
  "s_add_u32 m0, s56, 92160\n" \
  "s_nop 0\n" \
  "buffer_load_dwordx4 v194, s[40:43], s71 offen lds\n" \
  "s_add_u32 m0, s56, 93184\n" \
  "s_nop 0\n" \
  "buffer_load_dwordx4 v195, s[40:43], s71 offen lds\n" \
  "s_add_u32 m0, s56, 94208\n" \
  "s_nop 0\n" \
  "buffer_load_dwordx4 v196, s[40:43], s71 offen lds\n" \
  "s_add_u32 m0, s56, 95232\n" \
  "s_nop 0\n" \
  "buffer_load_dwordx4 v197, s[40:43], s71 offen lds\n" \
  "s_add_u32 m0, s56, 96256\n" \
  "s_nop 0\n" \
  "buffer_load_dwordx4 v198, s[40:43], s71 offen lds\n" \
  "s_add_u32 m0, s56, 97280\n" \
  "s_nop 0\n" \
  "buffer_load_dwordx4 v199, s[40:43], s71 offen lds\n" \
  "s_mov_b32 s71, 1572864\n" \
  "s_add_u32 m0, s56, 122880\n" \
  "s_nop 0\n" \
  "buffer_load_dwordx4 v192, s[40:43], s71 offen lds\n" \
  "s_add_u32 m0, s56, 123904\n" \
  "s_nop 0\n" \
  "buffer_load_dwordx4 v193, s[40:43], s71 offen lds\n" \
  "s_add_u32 m0, s56, 124928\n" \
  "s_nop 0\n" \
  "buffer_load_dwordx4 v194, s[40:43], s71 offen lds\n" \
  "s_add_u32 m0, s56, 125952\n" \
  "s_nop 0\n" \
  "buffer_load_dwordx4 v195, s[40:43], s71 offen lds\n" \
  "s_add_u32 m0, s56, 126976\n" \
  "s_nop 0\n" \
  "buffer_load_dwordx4 v196, s[40:43], s71 offen lds\n" \
  "s_add_u32 m0, s56, 128000\n" \
  "s_nop 0\n" \
  "buffer_load_dwordx4 v197, s[40:43], s71 offen lds\n" \
  "s_add_u32 m0, s56, 129024\n" \
  "s_nop 0\n" \
  "buffer_load_dwordx4 v198, s[40:43], s71 offen lds\n" \
  "s_add_u32 m0, s56, 130048\n" \
  "s_nop 0\n" \
  "buffer_load_dwordx4 v199, s[40:43], s71 offen lds\n" \
  "s_branch LF2_%=\n" \
  "LF1_%=:\n" \
  "s_mov_b32 s71, 0\n" \
  "s_add_u32 m0, s56, 16384\n" \
  "s_nop 0\n" \
  "buffer_load_dwordx4 v192, s[40:43], s71 offen lds\n" \
  "s_add_u32 m0, s56, 17408\n" \
  "s_nop 0\n" \
  "buffer_load_dwordx4 v193, s[40:43], s71 offen lds\n" \
  "s_add_u32 m0, s56, 18432\n" \
  "s_nop 0\n" \
  "buffer_load_dwordx4 v194, s[40:43], s71 offen lds\n" \
  "s_add_u32 m0, s56, 19456\n" \
  "s_nop 0\n" \
  "buffer_load_dwordx4 v195, s[40:43], s71 offen lds\n" \
  "s_add_u32 m0, s56, 20480\n" \
  "s_nop 0\n" \
  "buffer_load_dwordx4 v196, s[40:43], s71 offen lds\n" \
  "s_add_u32 m0, s56, 21504\n" \
  "s_nop 0\n" \
  "buffer_load_dwordx4 v197, s[40:43], s71 offen lds\n" \
  "s_mov_b32 s71, 786432\n" \
  "s_add_u32 m0, s56, 40960\n" \
  "s_nop 0\n" \
  "buffer_load_dwordx4 v192, s[40:43], s71 offen lds\n" \
  "s_add_u32 m0, s56, 41984\n" \
  "s_nop 0\n" \
  "buffer_load_dwordx4 v193, s[40:43], s71 offen lds\n" \
  "s_add_u32 m0, s56, 43008\n" \
  "s_nop 0\n" \
  "buffer_load_dwordx4 v194, s[40:43], s71 offen lds\n" \
  "s_add_u32 m0, s56, 44032\n" \
  "s_nop 0\n" \
  "buffer_load_dwordx4 v195, s[40:43], s71 offen lds\n" \
  "s_add_u32 m0, s56, 45056\n" \
  "s_nop 0\n" \
  "buffer_load_dwordx4 v196, s[40:43], s71 offen lds\n" \
  "s_add_u32 m0, s56, 46080\n" \
  "s_nop 0\n" \
  "buffer_load_dwordx4 v197, s[40:43], s71 offen lds\n" \
  "s_mov_b32 s71, 1572864\n" \
  "s_add_u32 m0, s56, 65536\n" \
  "s_nop 0\n" \
  "buffer_load_dwordx4 v192, s[40:43], s71 offen lds\n" \
  "s_add_u32 m0, s56, 66560\n" \
  "s_nop 0\n" \
  "buffer_load_dwordx4 v193, s[40:43], s71 offen lds\n" \
  "s_add_u32 m0, s56, 67584\n" \
  "s_nop 0\n" \
  "buffer_load_dwordx4 v194, s[40:43], s71 offen lds\n" \
  "s_add_u32 m0, s56, 68608\n" \
  "s_nop 0\n" \
  "buffer_load_dwordx4 v195, s[40:43], s71 offen lds\n" \
  "s_add_u32 m0, s56, 69632\n" \
  "s_nop 0\n" \
  "buffer_load_dwordx4 v196, s[40:43], s71 offen lds\n" \
  "s_add_u32 m0, s56, 70656\n" \
  "s_nop 0\n" \
  "buffer_load_dwordx4 v197, s[40:43], s71 offen lds\n" \
  "s_mov_b32 s71, 2359296\n" \
  "s_add_u32 m0, s56, 90112\n" \
  "s_nop 0\n" \
  "buffer_load_dwordx4 v192, s[40:43], s71 offen lds\n" \
  "s_add_u32 m0, s56, 91136\n" \
  "s_nop 0\n" \
  "buffer_load_dwordx4 v193, s[40:43], s71 offen lds\n" \
  "s_add_u32 m0, s56, 92160\n" \
  "s_nop 0\n" \
  "buffer_load_dwordx4 v194, s[40:43], s71 offen lds\n" \
  "s_add_u32 m0, s56, 93184\n" \
  "s_nop 0\n" \
  "buffer_load_dwordx4 v195, s[40:43], s71 offen lds\n" \
  "s_add_u32 m0, s56, 94208\n" \
  "s_nop 0\n" \
  "buffer_load_dwordx4 v196, s[40:43], s71 offen lds\n" \
  "s_add_u32 m0, s56, 95232\n" \
  "s_nop 0\n" \
  "buffer_load_dwordx4 v197, s[40:43], s71 offen lds\n" \
  "LF2_%=:\n" \
  "v_writelane_b32 v224, s50, 0\n" \
  "v_writelane_b32 v224, s51, 1\n" \
  "v_writelane_b32 v224, s52, 2\n" \
  "v_writelane_b32 v224, s58, 3\n" \
  "v_writelane_b32 v224, s40, 4\n" \
  "v_writelane_b32 v224, s41, 5\n" \
  "v_writelane_b32 v224, s42, 6\n" \
  "v_writelane_b32 v224, s43, 7\n" \
  "v_writelane_b32 v224, s44, 8\n" \
  "v_writelane_b32 v224, s45, 9\n" \
  "v_writelane_b32 v224, s46, 10\n" \
  "v_writelane_b32 v224, s47, 11\n" \
  "s_memrealtime s[74:75]\n" \
  "s_memtime s[76:77]\n" \
  "s_mov_b32 s73, 0\n" \
  "s_mov_b32 s38, 0\n" \
  "s_mov_b32 s64, 62\n" \
  "s_mov_b32 s60, 20001\n" \
  "s_mov_b32 s50, 0\n" \
  "s_mov_b32 s51, 0\n" \
  "s_mov_b32 s52, 4000\n" \
  "s_mov_b32 s90, 5900\n" \
  "s_waitcnt lgkmcnt(0)\n" \
  "s_mov_b32 s88, s74\n" \
  "v_mov_b32 v225, v25\n" \
  "v_mov_b32 v226, v26\n" \
  "s_cmp_ge_u32 s48, 8\n" \
  "s_cbranch_scc1 LDV_%=\n" \
  "LCN_%=:\n" \
  "s_mov_b32 s28, 0xffff0000\n" \
  "s_mov_b32 s29, 0x0\n" \
  "s_mov_b32 s26, 0x0\n" \
  "s_mov_b32 s27, 0xffff\n" \
  "s_mov_b32 s24, 0x0\n" \
  "s_mov_b32 s25, 0xffff0000\n" \
  "s_mov_b32 s46, 0x3030303\n" \
  "s_mov_b32 s47, 0x3030303\n" \
  "s_mov_b32 s30, 0xff00ff\n" \
  "s_mov_b32 s31, 0xff00ff00\n" \
  "s_mov_b32 s20, 0xff00ff\n" \
  "s_mov_b32 s21, 0xff00ff\n" \
  "s_mov_b32 s42, 0x3dd53b94\n" \
  "s_mov_b32 s43, 0x3dd53b94\n" \
  "s_mov_b32 s22, 0x3f80\n" \
  "s_mov_b32 s23, 0x3f800000\n" \
  "v_mov_b32 v56, 0x3f803f80\n" \
  "v_mov_b32 v72, 0\n" \
  "v_mov_b32 v57, 0x3f803f80\n" \
  "v_mov_b32 v73, 0\n" \
  "v_mov_b32 v58, 0x3f803f80\n" \
  "v_mov_b32 v74, 0\n" \
  "v_mov_b32 v59, 0x3f803f80\n" \
  "v_mov_b32 v75, 0\n" \
  "v_mov_b32 v60, 0x3f803f80\n" \
  "v_mov_b32 v76, 0\n" \
  "v_mov_b32 v61, 0x3f803f80\n" \
  "v_mov_b32 v77, 0\n" \
  "v_mov_b32 v62, 0x3f803f80\n" \
  "v_mov_b32 v78, 0\n" \
  "v_mov_b32 v63, 0x3f803f80\n" \
  "v_mov_b32 v79, 0\n" \
  "v_mov_b32 v64, 0x3f803f80\n" \
  "v_mov_b32 v80, 0\n" \
  "v_mov_b32 v65, 0x3f803f80\n" \
  "v_mov_b32 v81, 0\n" \
  "v_mov_b32 v66, 0x3f803f80\n" \
  "v_mov_b32 v82, 0\n" \
  "v_mov_b32 v67, 0x3f803f80\n" \
  "v_mov_b32 v83, 0\n" \
  "v_mov_b32 v68, 0x3f803f80\n" \
  "v_mov_b32 v84, 0\n" \
  "v_mov_b32 v69, 0x3f803f80\n" \
  "v_mov_b32 v85, 0\n" \
  "v_mov_b32 v70, 0x3f803f80\n" \
  "v_mov_b32 v86, 0\n" \
  "v_mov_b32 v71, 0x3f803f80\n" \
  "v_mov_b32 v87, 0\n" \
  "s_mov_b32 exec_lo, 0xfcfc\n" \
  "s_mov_b32 exec_hi, 0x0\n" \
  "v_mov_b32 v72, 0x3f803f80\n" \
  "v_mov_b32 v73, 0x3f803f80\n" \
  "v_mov_b32 v74, 0x3f803f80\n" \
  "v_mov_b32 v75, 0x3f803f80\n" \
  "s_mov_b32 exec_lo, 0xfcfc0000\n" \
  "s_mov_b32 exec_hi, 0x0\n" \
  "v_mov_b32 v76, 0x3f803f80\n" \
  "v_mov_b32 v77, 0x3f803f80\n" \
  "v_mov_b32 v78, 0x3f803f80\n" \
  "v_mov_b32 v79, 0x3f803f80\n" \
  "s_mov_b32 exec_lo, 0x0\n" \
  "s_mov_b32 exec_hi, 0xfcfc\n" \
  "v_mov_b32 v80, 0x3f803f80\n" \
  "v_mov_b32 v81, 0x3f803f80\n" \
  "v_mov_b32 v82, 0x3f803f80\n" \
  "v_mov_b32 v83, 0x3f803f80\n" \
  "s_mov_b32 exec_lo, 0x0\n" \
  "s_mov_b32 exec_hi, 0xfcfc0000\n" \
  "v_mov_b32 v84, 0x3f803f80\n" \
  "v_mov_b32 v85, 0x3f803f80\n" \
  "v_mov_b32 v86, 0x3f803f80\n" \
  "v_mov_b32 v87, 0x3f803f80\n" \
  "s_mov_b64 exec, -1\n" \
  "v_mov_b32 v33, 19456\n" \
  "v_mov_b32 v36, 0\n" \
  "ds_write_b32 v33, v36\n" \
  "s_cmp_eq_u32 s73, 1\n" \
  "s_cbranch_scc1 LSW_%=\n" \
  "s_branch LPASS_%=\n" \
  "LDV_%=:\n" \
  "s_mov_b32 s64, 63\n" \
  "LPASS_%=:\n" \
  "s_mul_i32 s93, s52, 24\n" \
  "s_cmp_eq_u32 s73, 0\n" \
  "s_cselect_b32 s94, -1073741824, -120\n" \
  "s_mov_b32 s97, 0\n" \
  "s_mov_b32 s98, 0\n" \
  "s_mov_b32 s19, 0\n" \
  "s_mov_b32 s18, 0\n" \
  "s_mov_b32 s92, 0\n" \
  "s_mov_b32 s34, 0\n" \
  "s_mov_b32 s35, 0\n" \
  "s_mul_i32 s58, s64, 100\n" \
  "s_mov_b32 s59, s58\n" \
  "s_mov_b32 s61, 64\n" \
  "s_mov_b32 s99, 8\n" \
  "s_mov_b32 s67, s54\n" \
  "s_cmp_ge_u32 s48, 8\n" \
  "s_cselect_b32 s22, s90, s22\n" \
  "s_sub_u32 s50, s50, 64\n" \
  "s_sub_u32 s51, s51, 64\n" \
  "s_memrealtime s[74:75]\n" \
  "s_memtime s[76:77]\n" \
  "s_waitcnt lgkmcnt(0)\n" \
  "s_add_u32 s89, s76, 0x1000000\n" \
  "v_readlane_b32 s66, v25, s64\n" \
  "v_readlane_b32 s68, v26, s64\n" \
  "s_nop 3\n" \
  "s_and_b32 s71, s48, 3\n" \
  "s_cmp_eq_u32 s71, 2\n" \
  "s_cbranch_scc0 LPA_%=\n" \
  "buffer_load_dword v34, v29, s[36:39], 0 offen sc1\n" \
  "s_waitcnt vmcnt(0)\n" \
  "LPA_%=:\n" \
  "s_cmp_lt_u32 s48, 2\n" \
  "s_cbranch_scc0 LPB_%=\n" \
  "s_add_u32 s91, s68, 16384\n" \
  "s_add_u32 s92, s68, 32768\n" \
  "s_add_u32 s93, s68, 49152\n" \
  "buffer_load_dwordx4 v[88:91], v1, s[36:39], s68 offen sc1\n" \
  "buffer_load_dwordx4 v[92:95], v1, s[36:39], s91 offen sc1\n" \
  "buffer_load_dwordx4 v[36:39], v1, s[36:39], s92 offen sc1\n" \
  "buffer_load_dwordx4 v[100:103], v1, s[36:39], s93 offen sc1\n" \
  "buffer_load_dwordx2 v[104:105], v2, s[36:39], s68 offen sc1\n" \
  "buffer_load_dwordx2 v[106:107], v2, s[36:39], s91 offen sc1\n" \
  "buffer_load_dwordx2 v[108:109], v2, s[36:39], s92 offen sc1\n" \
  "buffer_load_dwordx2 v[110:111], v2, s[36:39], s93 offen sc1\n" \
  "LPB_%=:\n" \
  "s_cmp_eq_u32 s48, 1\n" \
  "s_cbranch_scc1 LSTART01_%=\n" \
  "s_cmp_eq_u32 s48, 2\n" \
  "s_cbranch_scc1 LSTART02_%=\n" \
  "s_cmp_eq_u32 s48, 3\n" \
  "s_cbranch_scc1 LSTART03_%=\n" \
  "s_cmp_eq_u32 s48, 4\n" \
  "s_cbranch_scc1 LSTART10_%=\n" \
  "s_cmp_eq_u32 s48, 5\n" \
  "s_cbranch_scc1 LSTART11_%=\n" \
  "s_cmp_eq_u32 s48, 6\n" \
  "s_cbranch_scc1 LSTART12_%=\n" \
  "s_cmp_eq_u32 s48, 7\n" \
  "s_cbranch_scc1 LSTART13_%=\n" \
  "s_cmp_eq_u32 s48, 8\n" \
  "s_cbranch_scc1 LSTART20_%=\n" \
  "s_cmp_eq_u32 s48, 9\n" \
  "s_cbranch_scc1 LSTART21_%=\n" \
  "s_cmp_eq_u32 s48, 10\n" \
  "s_cbranch_scc1 LSTART22_%=\n" \
  "s_cmp_eq_u32 s48, 11\n" \
  "s_cbranch_scc1 LSTART23_%=\n" \
  "LSTART00_%=:\n" \
  "LOOP00_%=:\n" \
  "v_readlane_b32 s70, v27, s64\n" \
  "s_mov_b32 s65, s66\n" \
  "s_xor_b32 s69, s66, 0x2000\n" \
  "s_add_u32 s64, s64, 1\n" \
  "v_mov_b32 v176, 0\n" \
  "v_mov_b32 v177, 0\n" \
  "v_mov_b32 v178, 0\n" \
  "v_mov_b32 v179, 0\n" \
  "v_mov_b32 v180, 0\n" \
  "v_mov_b32 v181, 0\n" \
  "v_mov_b32 v182, 0\n" \
  "v_mov_b32 v183, 0\n" \
  "v_readlane_b32 s66, v25, s64\n" \
  "v_readlane_b32 s68, v26, s64\n" \
  "s_add_u32 s91, s65, 81920\n" \
  "s_add_u32 s92, s65, 163840\n" \
  "s_mov_b32 m0, 20480\n" \
  "s_add_u32 s72, s70, 4096\n" \
  "s_barrier\n" \
  "buffer_load_dwordx4 v3, s[36:39], s70 offen sc1 lds\n" \
  "buffer_load_dwordx4 v3, s[36:39], s70 offen offset:1024 sc1 lds\n" \
  "buffer_load_dwordx4 v3, s[36:39], s70 offen offset:2048 sc1 lds\n" \
  "buffer_load_dwordx4 v3, s[36:39], s70 offen offset:3072 sc1 lds\n" \
  "LM00_%=:\n" \
  "s_barrier\n" \
  "ds_read_b128 v[112:115], v5\n" \
  "ds_read_b128 v[116:119], v5 offset:512\n" \
  "s_barrier\n" \
  "ds_read_b128 v[120:123], v5 offset:1024\n" \
  "ds_read_b128 v[124:127], v5 offset:1536\n" \
  "s_waitcnt lgkmcnt(3)\n" \
  "v_smfmac_f32_16x16x64_bf16 v[176:179], v[112:115], a[0:7], v0\n" \
  "v_smfmac_f32_16x16x64_bf16 v[180:183], v[112:115], a[160:167], v0\n" \
  "s_waitcnt lgkmcnt(2)\n" \
  "v_smfmac_f32_16x16x64_bf16 v[176:179], v[116:119], a[8:15], v0\n" \
  "s_barrier\n" \
  "ds_read_b128 v[128:131], v5 offset:2048\n" \
  "v_smfmac_f32_16x16x64_bf16 v[180:183], v[116:119], a[168:175], v0\n" \
  "s_waitcnt lgkmcnt(2)\n" \
  "ds_read_b128 v[132:135], v5 offset:2560\n" \
  "v_smfmac_f32_16x16x64_bf16 v[176:179], v[120:123], a[16:23], v0\n" \
  "ds_read_b128 v[136:139], v5 offset:3072\n" \
  "v_smfmac_f32_16x16x64_bf16 v[180:183], v[120:123], a[176:183], v0\n" \
  "s_waitcnt lgkmcnt(3)\n" \
  "ds_read_b128 v[140:143], v5 offset:3584\n" \
  "v_smfmac_f32_16x16x64_bf16 v[176:179], v[124:127], a[24:31], v0\n" \
  "s_barrier\n" \
  "s_waitcnt vmcnt(4)\n" \
  "ds_read_b128 v[144:147], v5 offset:4096\n" \
  "v_smfmac_f32_16x16x64_bf16 v[180:183], v[124:127], a[184:191], v0\n" \
  "s_mov_b32 m0, 24576\n" \
  "s_waitcnt lgkmcnt(4)\n" \
  "ds_read_b128 v[148:151], v5 offset:4608\n" \
  "v_smfmac_f32_16x16x64_bf16 v[176:179], v[128:131], a[32:39], v0\n" \
  "ds_read_b128 v[152:155], v5 offset:5120\n" \
  "buffer_load_dwordx4 v3, s[36:39], s72 offen sc1 lds\n" \
  "v_smfmac_f32_16x16x64_bf16 v[180:183], v[128:131], a[192:199], v0\n" \
  "s_waitcnt lgkmcnt(5)\n" \
  "ds_read_b128 v[156:159], v5 offset:5632\n" \
  "v_smfmac_f32_16x16x64_bf16 v[176:179], v[132:135], a[40:47], v0\n" \
  "s_barrier\n" \
  "ds_read_b128 v[160:163], v5 offset:6144\n" \
  "buffer_load_dwordx4 v3, s[36:39], s72 offen offset:1024 sc1 lds\n" \
  "v_smfmac_f32_16x16x64_bf16 v[180:183], v[132:135], a[200:207], v0\n" \
  "s_waitcnt lgkmcnt(6)\n" \
  "ds_read_b128 v[164:167], v5 offset:6656\n" \
  "v_smfmac_f32_16x16x64_bf16 v[176:179], v[136:139], a[48:55], v0\n" \
  "ds_read_b128 v[168:171], v5 offset:7168\n" \
  "buffer_load_dwordx4 v3, s[36:39], s72 offen offset:2048 sc1 lds\n" \
  "v_smfmac_f32_16x16x64_bf16 v[180:183], v[136:139], a[208:215], v0\n" \
  "s_waitcnt lgkmcnt(7)\n" \
  "ds_read_b128 v[172:175], v5 offset:7680\n" \
  "v_smfmac_f32_16x16x64_bf16 v[176:179], v[140:143], a[56:63], v0\n" \
  "s_barrier\n" \
  "ds_read2_b32 v[112:113], v6 offset0:0 offset1:32\n" \
  "buffer_load_dwordx4 v3, s[36:39], s72 offen offset:3072 sc1 lds\n" \
  "v_smfmac_f32_16x16x64_bf16 v[180:183], v[140:143], a[216:223], v0\n" \
  "s_waitcnt lgkmcnt(8)\n" \
  "ds_read2_b32 v[114:115], v6 offset0:64 offset1:96\n" \
  "v_smfmac_f32_16x16x64_bf16 v[176:179], v[144:147], a[64:71], v0\n" \
  "s_waitcnt lgkmcnt(8)\n" \
  "ds_read2_b32 v[116:117], v6 offset0:128 offset1:160\n" \
  "v_smfmac_f32_16x16x64_bf16 v[176:179], v[148:151], a[72:79], v0\n" \
  "s_waitcnt lgkmcnt(8)\n" \
  "ds_read2_b32 v[118:119], v6 offset0:192 offset1:224\n" \
  "v_smfmac_f32_16x16x64_bf16 v[176:179], v[152:155], a[80:87], v0\n" \
  "s_waitcnt lgkmcnt(8)\n" \
  "ds_read2_b32 v[120:121], v7 offset0:0 offset1:32\n" \
  "v_smfmac_f32_16x16x64_bf16 v[176:179], v[156:159], a[88:95], v0\n" \
  "s_waitcnt lgkmcnt(8)\n" \
  "ds_read2_b32 v[122:123], v7 offset0:64 offset1:96\n" \
  "v_smfmac_f32_16x16x64_bf16 v[176:179], v[160:163], a[96:103], v0\n" \
  "s_waitcnt lgkmcnt(8)\n" \
  "ds_read2_b32 v[124:125], v7 offset0:128 offset1:160\n" \
  "v_smfmac_f32_16x16x64_bf16 v[176:179], v[164:167], a[104:111], v0\n" \
  "s_waitcnt lgkmcnt(8)\n" \
  "ds_read2_b32 v[126:127], v7 offset0:192 offset1:224\n" \
  "v_smfmac_f32_16x16x64_bf16 v[176:179], v[168:171], a[112:119], v0\n" \
  "s_waitcnt lgkmcnt(8)\n" \
  "v_smfmac_f32_16x16x64_bf16 v[176:179], v[172:175], a[120:127], v0\n" \
  "s_waitcnt lgkmcnt(6)\n" \
  "v_smfmac_f32_16x16x64_bf16 v[176:179], v[112:115], a[128:135], v0\n" \
  "s_waitcnt lgkmcnt(4)\n" \
  "v_smfmac_f32_16x16x64_bf16 v[176:179], v[116:119], a[136:143], v0\n" \
  "s_waitcnt lgkmcnt(2)\n" \
  "v_smfmac_f32_16x16x64_bf16 v[176:179], v[120:123], a[144:151], v0\n" \
  "s_waitcnt lgkmcnt(0)\n" \
  "v_smfmac_f32_16x16x64_bf16 v[176:179], v[124:127], a[152:159], v0\n" \
  "v_smfmac_f32_16x16x64_bf16 v[180:183], v[144:147], a[224:231], v0\n" \
  "v_smfmac_f32_16x16x64_bf16 v[180:183], v[148:151], a[232:239], v0\n" \
  "v_cvt_pk_bf16_f32 v184, v176, v177\n" \
  "v_smfmac_f32_16x16x64_bf16 v[180:183], v[152:155], a[240:247], v0\n" \
  "v_cvt_pk_bf16_f32 v185, v178, v179\n" \
  "v_smfmac_f32_16x16x64_bf16 v[180:183], v[156:159], a[248:255], v0\n" \
  "ds_write_b64 v8, v[184:185]\n" \
  "v_add_f32 v186, v176, v177\n" \
  "v_smfmac_f32_16x16x64_bf16 v[180:183], v[160:163], v[192:199], v0\n" \
  "v_add_f32 v187, v178, v179\n" \
  "v_smfmac_f32_16x16x64_bf16 v[180:183], v[164:167], v[200:207], v0\n" \
  "v_cmp_u_f32 vcc, v186, v187\n" \
  "v_smfmac_f32_16x16x64_bf16 v[180:183], v[168:171], v[208:215], v0\n" \
  "v_smfmac_f32_16x16x64_bf16 v[180:183], v[172:175], v[216:223], v0\n" \
  "s_cbranch_vccnz LMF00_%=\n" \
  "LMOK00_%=:\n" \
  "v_smfmac_f32_16x16x64_bf16 v[180:183], v[112:115], v[224:231], v0\n" \
  "s_waitcnt lgkmcnt(0)\n" \
  "s_barrier\n" \
  "ds_read_b64_tr_b16 v[128:129], v10\n" \
  "v_smfmac_f32_16x16x64_bf16 v[180:183], v[116:119], v[232:239], v0\n" \
  "ds_read_b64_tr_b16 v[130:131], v10 offset:32\n" \
  "ds_read_b64_tr_b16 v[132:133], v10 offset:512\n" \
  "v_smfmac_f32_16x16x64_bf16 v[180:183], v[120:123], v[240:247], v0\n" \
  "ds_read_b64_tr_b16 v[134:135], v10 offset:544\n" \
  "ds_read_b64_tr_b16 v[136:137], v10 offset:1024\n" \
  "v_smfmac_f32_16x16x64_bf16 v[180:183], v[124:127], v[248:255], v0\n" \
  "s_nop 0\n" \
  "ds_read_b64_tr_b16 v[138:139], v10 offset:1056\n" \
  "ds_read_b64_tr_b16 v[140:141], v10 offset:1536\n" \
  "ds_read_b64_tr_b16 v[142:143], v10 offset:1568\n" \
  "v_mov_b32 v31, v31\n" \
  "s_nop 2\n" \
  "v_cvt_pk_bf16_f32 v186, v180, v181\n" \
  "v_cvt_pk_bf16_f32 v187, v182, v183\n" \
  "ds_write_b64 v9, v[186:187]\n" \
  "s_waitcnt lgkmcnt(7)\n" \
  "v_mfma_f32_16x16x32_bf16 v[188:191], v[88:91], v[128:131], 0\n" \
  "s_waitcnt lgkmcnt(5)\n" \
  "v_mfma_f32_16x16x32_bf16 v[188:191], v[92:95], v[132:135], v[188:191]\n" \
  "s_waitcnt lgkmcnt(3)\n" \
  "v_mfma_f32_16x16x32_bf16 v[188:191], v[36:39], v[136:139], v[188:191]\n" \
  "s_waitcnt lgkmcnt(1)\n" \
  "v_mfma_f32_16x16x32_bf16 v[188:191], v[100:103], v[140:143], v[188:191]\n" \
  "s_waitcnt lgkmcnt(0)\n" \
  "s_barrier\n" \
  "ds_read_b64_tr_b16 v[144:145], v11\n" \
  "ds_read_b64_tr_b16 v[146:147], v11 offset:512\n" \
  "ds_read_b64_tr_b16 v[148:149], v11 offset:1024\n" \
  "ds_read_b64_tr_b16 v[150:151], v11 offset:1536\n" \
  "v_mov_b32 v31, v31\n" \
  "s_nop 2\n" \
  "v_mov_b32 v31, v31\n" \
  "s_nop 2\n" \
  "v_mov_b32 v31, v31\n" \
  "s_nop 0\n" \
  "s_waitcnt lgkmcnt(2)\n" \
  "v_mfma_f32_16x16x32_bf16 v[188:191], v[104:107], v[144:147], v[188:191]\n" \
  "s_waitcnt lgkmcnt(0)\n" \
  "v_mfma_f32_16x16x32_bf16 v[188:191], v[108:111], v[148:151], v[188:191]\n" \
  "s_nop 0\n" \
  "ds_read_b64_tr_b16 v[34:35], v12\n" \
  "s_nop 1\n" \
  "v_mov_b32 v31, v31\n" \
  "s_nop 2\n" \
  "v_cndmask_b32 v152, v190, v188, s[20:21]\n" \
  "v_cndmask_b32 v153, v191, v189, s[20:21]\n" \
  "v_mul_f32 v152, 0x3dd53b94, v152\n" \
  "v_mul_f32 v153, 0x3dd53b94, v153\n" \
  "v_max_f32 v156, |v152|, |v153|\n" \
  "v_cmp_lt_f32 vcc, 0x42800000, v156\n" \
  "v_exp_f32 v154, v152\n" \
  "v_exp_f32 v155, v153\n" \
  "s_cbranch_vccnz LMX00_%=\n" \
  "v_cvt_pk_bf16_f32 v154, v154, v155\n" \
  "LMY00_%=:\n" \
  "buffer_store_dword v154, v13, s[36:39], s65 offen sc1\n" \
  "buffer_store_dword v154, v13, s[36:39], s91 offen sc1\n" \
  "buffer_store_dword v154, v13, s[36:39], s92 offen sc1\n" \
  "s_mov_b32 s67, s66\n" \
  "s_cmp_eq_u32 s64, 64\n" \
  "s_cbranch_scc1 LKE00_%=\n" \
  "LB00_%=:\n" \
  "s_barrier\n" \
  "buffer_load_dwordx4 v[116:119], v15, s[36:39], s65 offen offset:128 sc1\n" \
  "s_barrier\n" \
  "buffer_load_dwordx4 v[112:115], v15, s[36:39], s65 offen sc1\n" \
  "s_barrier\n" \
  "buffer_load_dwordx4 v[120:123], v16, s[36:39], s65 offen sc1\n" \
  "s_barrier\n" \
  "buffer_load_dwordx4 v[124:127], v16, s[36:39], s65 offen offset:512 sc1\n" \
  "s_barrier\n" \
  "buffer_load_dwordx4 v[128:131], v16, s[36:39], s65 offen offset:1024 sc1\n" \
  "s_waitcnt vmcnt(8)\n" \
  "s_barrier\n" \
  "buffer_load_dwordx4 v[132:135], v16, s[36:39], s65 offen offset:1536 sc1\n" \
  "ds_read_b128 v[40:43], v19\n" \
  "ds_read_b128 v[44:47], v19 offset:512\n" \
  "ds_read_b128 v[48:51], v19 offset:1024\n" \
  "ds_read_b128 v[52:55], v19 offset:1536\n" \
  "s_mov_b64 exec, s[46:47]\n" \
  "ds_read_b128 v[56:59], v20\n" \
  "ds_read_b128 v[60:63], v20 offset:128\n" \
  "ds_read_b128 v[64:67], v20 offset:256\n" \
  "ds_read_b128 v[68:71], v20 offset:384\n" \
  "s_mov_b64 exec, -1\n" \
  "s_nop 15\n" \
  "s_nop 1\n" \
  "buffer_load_dwordx4 v[136:139], v17, s[36:39], s65 offen sc1\n" \
  "buffer_load_dwordx4 v[140:143], v18, s[36:39], s65 offen sc1\n" \
  "s_barrier\n" \
  "s_waitcnt lgkmcnt(0)\n" \
  "buffer_store_dwordx2 v[34:35], v14, s[36:39], s69 offen sc1\n" \
  "s_add_u32 s91, s68, 16384\n" \
  "s_add_u32 s92, s68, 32768\n" \
  "s_add_u32 s93, s68, 49152\n" \
  "buffer_load_dwordx4 v[88:91], v1, s[36:39], s68 offen sc1\n" \
  "buffer_load_dwordx4 v[92:95], v1, s[36:39], s91 offen sc1\n" \
  "buffer_load_dwordx4 v[36:39], v1, s[36:39], s92 offen sc1\n" \
  "buffer_load_dwordx4 v[100:103], v1, s[36:39], s93 offen sc1\n" \
  "s_waitcnt lgkmcnt(0)\n" \
  "s_getreg_b32 s71, hwreg(HW_REG_IB_STS, 0, 4)\n" \
  "s_cmp_le_u32 s71, 11\n" \
  "v_mfma_f32_16x16x32_bf16 v[144:147], v[72:75], v[72:75], 0\n" \
  "s_cbranch_scc1 LHXS00_%=\n" \
  "s_getreg_b32 s71, hwreg(HW_REG_IB_STS, 0, 4)\n" \
  "s_cmp_le_u32 s71, 11\n" \
  "v_mfma_f32_16x16x32_bf16 v[144:147], v[72:75], v[72:75], 0\n" \
  "s_cbranch_scc1 LHXS00_%=\n" \
  "s_getreg_b32 s71, hwreg(HW_REG_IB_STS, 0, 4)\n" \
  "s_cmp_le_u32 s71, 11\n" \
  "v_mfma_f32_16x16x32_bf16 v[144:147], v[72:75], v[72:75], 0\n" \
  "s_cbranch_scc1 LHXS00_%=\n" \
  "s_getreg_b32 s71, hwreg(HW_REG_IB_STS, 0, 4)\n" \
  "s_cmp_le_u32 s71, 11\n" \
  "v_mfma_f32_16x16x32_bf16 v[144:147], v[72:75], v[72:75], 0\n" \
  "s_cbranch_scc1 LHXS00_%=\n" \
  "s_getreg_b32 s71, hwreg(HW_REG_IB_STS, 0, 4)\n" \
  "s_cmp_le_u32 s71, 11\n" \
  "v_mfma_f32_16x16x32_bf16 v[144:147], v[72:75], v[72:75], 0\n" \
  "s_cbranch_scc1 LHXS00_%=\n" \
  "s_getreg_b32 s71, hwreg(HW_REG_IB_STS, 0, 4)\n" \
  "s_cmp_le_u32 s71, 11\n" \
  "v_mfma_f32_16x16x32_bf16 v[144:147], v[72:75], v[72:75], 0\n" \
  "s_cbranch_scc1 LHXS00_%=\n" \
  "s_getreg_b32 s71, hwreg(HW_REG_IB_STS, 0, 4)\n" \
  "s_cmp_le_u32 s71, 11\n" \
  "v_mfma_f32_16x16x32_bf16 v[144:147], v[72:75], v[72:75], 0\n" \
  "s_cbranch_scc1 LHXS00_%=\n" \
  "s_getreg_b32 s71, hwreg(HW_REG_IB_STS, 0, 4)\n" \
  "s_cmp_le_u32 s71, 11\n" \
  "v_mfma_f32_16x16x32_bf16 v[144:147], v[72:75], v[72:75], 0\n" \
  "s_cbranch_scc1 LHXS00_%=\n" \
  "s_getreg_b32 s71, hwreg(HW_REG_IB_STS, 0, 4)\n" \
  "s_cmp_le_u32 s71, 11\n" \
  "v_mfma_f32_16x16x32_bf16 v[144:147], v[72:75], v[72:75], 0\n" \
  "s_cbranch_scc1 LHXS00_%=\n" \
  "s_getreg_b32 s71, hwreg(HW_REG_IB_STS, 0, 4)\n" \
  "s_cmp_le_u32 s71, 11\n" \
  "v_mfma_f32_16x16x32_bf16 v[144:147], v[72:75], v[72:75], 0\n" \
  "s_cbranch_scc1 LHXS00_%=\n" \
  "s_getreg_b32 s71, hwreg(HW_REG_IB_STS, 0, 4)\n" \
  "s_cmp_le_u32 s71, 11\n" \
  "v_mfma_f32_16x16x32_bf16 v[144:147], v[72:75], v[72:75], 0\n" \
  "s_cbranch_scc1 LHXS00_%=\n" \
  "s_getreg_b32 s71, hwreg(HW_REG_IB_STS, 0, 4)\n" \
  "s_cmp_le_u32 s71, 11\n" \
  "v_mfma_f32_16x16x32_bf16 v[144:147], v[72:75], v[72:75], 0\n" \
  "s_cbranch_scc1 LHXS00_%=\n" \
  "s_getreg_b32 s71, hwreg(HW_REG_IB_STS, 0, 4)\n" \
  "s_cmp_le_u32 s71, 11\n" \
  "v_mfma_f32_16x16x32_bf16 v[144:147], v[72:75], v[72:75], 0\n" \
  "s_cbranch_scc1 LHXS00_%=\n" \
  "s_getreg_b32 s71, hwreg(HW_REG_IB_STS, 0, 4)\n" \
  "s_cmp_le_u32 s71, 11\n" \
  "v_mfma_f32_16x16x32_bf16 v[144:147], v[72:75], v[72:75], 0\n" \
  "s_cbranch_scc1 LHXS00_%=\n" \
  "s_getreg_b32 s71, hwreg(HW_REG_IB_STS, 0, 4)\n" \
  "s_cmp_le_u32 s71, 11\n" \
  "v_mfma_f32_16x16x32_bf16 v[144:147], v[72:75], v[72:75], 0\n" \
  "s_cbranch_scc1 LHXS00_%=\n" \
  "s_getreg_b32 s71, hwreg(HW_REG_IB_STS, 0, 4)\n" \
  "s_cmp_le_u32 s71, 11\n" \
  "v_mfma_f32_16x16x32_bf16 v[144:147], v[72:75], v[72:75], 0\n" \
  "s_cbranch_scc1 LHXS00_%=\n" \
  "s_getreg_b32 s71, hwreg(HW_REG_IB_STS, 0, 4)\n" \
  "s_cmp_le_u32 s71, 11\n" \
  "v_mfma_f32_16x16x32_bf16 v[144:147], v[72:75], v[72:75], 0\n" \
  "s_cbranch_scc1 LHXS00_%=\n" \
  "s_getreg_b32 s71, hwreg(HW_REG_IB_STS, 0, 4)\n" \
  "s_cmp_le_u32 s71, 11\n" \
  "v_mfma_f32_16x16x32_bf16 v[144:147], v[72:75], v[72:75], 0\n" \
  "s_cbranch_scc1 LHXS00_%=\n" \
  "s_getreg_b32 s71, hwreg(HW_REG_IB_STS, 0, 4)\n" \
  "s_cmp_le_u32 s71, 11\n" \
  "v_mfma_f32_16x16x32_bf16 v[144:147], v[72:75], v[72:75], 0\n" \
  "s_cbranch_scc1 LHXS00_%=\n" \
  "s_getreg_b32 s71, hwreg(HW_REG_IB_STS, 0, 4)\n" \
  "s_cmp_le_u32 s71, 11\n" \
  "v_mfma_f32_16x16x32_bf16 v[144:147], v[72:75], v[72:75], 0\n" \
  "s_cbranch_scc1 LHXS00_%=\n" \
  "LHXS00_%=:\n" \
  "s_waitcnt vmcnt(11)\n" \
  "LS200_%=:\n" \
  "v_dot2_f32_bf16 v176, v112, s22, 0\n" \
  "v_dot2_f32_bf16 v177, v112, s23, 0\n" \
  "v_dot2_f32_bf16 v178, v113, s22, 0\n" \
  "v_dot2_f32_bf16 v179, v113, s23, 0\n" \
  "v_dot2_f32_bf16 v180, v114, s22, 0\n" \
  "v_dot2_f32_bf16 v181, v114, s23, 0\n" \
  "v_dot2_f32_bf16 v182, v115, s22, 0\n" \
  "v_dot2_f32_bf16 v183, v115, s23, 0\n" \
  "v_dot2_f32_bf16 v176, v116, s22, v176\n" \
  "v_dot2_f32_bf16 v177, v116, s23, v177\n" \
  "v_dot2_f32_bf16 v178, v117, s22, v178\n" \
  "v_dot2_f32_bf16 v179, v117, s23, v179\n" \
  "v_dot2_f32_bf16 v180, v118, s22, v180\n" \
  "v_dot2_f32_bf16 v181, v118, s23, v181\n" \
  "v_dot2_f32_bf16 v182, v119, s22, v182\n" \
  "v_dot2_f32_bf16 v183, v119, s23, v183\n" \
  "v_exp_f32 v176, v176\n" \
  "v_exp_f32 v177, v177\n" \
  "v_exp_f32 v178, v178\n" \
  "v_exp_f32 v179, v179\n" \
  "s_waitcnt vmcnt(10)\n" \
  "v_mfma_f32_16x16x32_bf16 v[160:163], v[56:59], v[120:123], 0\n" \
  "v_exp_f32 v180, v180\n" \
  "v_mfma_f32_16x16x32_bf16 v[144:147], v[40:43], v[120:123], 0\n" \
  "v_exp_f32 v181, v181\n" \
  "v_exp_f32 v182, v182\n" \
  "v_exp_f32 v183, v183\n" \
  "v_cvt_pk_bf16_f32 v184, v176, v177\n" \
  "v_cvt_pk_bf16_f32 v185, v178, v179\n" \
  "v_cvt_pk_bf16_f32 v186, v180, v181\n" \
  "v_cvt_pk_bf16_f32 v187, v182, v183\n" \
  "s_nop 1\n" \
  "v_mfma_f32_16x16x32_bf16 v[160:163], v[72:75], v[184:187], v[160:163]\n" \
  "v_mfma_f32_16x16x32_bf16 v[164:167], v[76:79], v[184:187], 0\n" \
  "s_waitcnt vmcnt(9)\n" \
  "v_mfma_f32_16x16x32_bf16 v[164:167], v[60:63], v[124:127], v[164:167]\n" \
  "v_rcp_f32 v188, v162\n" \
  "v_mfma_f32_16x16x32_bf16 v[148:151], v[44:47], v[124:127], 0\n" \
  "v_mul_f32 v144, v188, v144\n" \
  "v_mul_f32 v145, v188, v145\n" \
  "v_mfma_f32_16x16x32_bf16 v[168:171], v[80:83], v[184:187], 0\n" \
  "s_waitcnt vmcnt(8)\n" \
  "v_rcp_f32 v189, v166\n" \
  "v_mfma_f32_16x16x32_bf16 v[168:171], v[64:67], v[128:131], v[168:171]\n" \
  "v_mul_f32 v146, v188, v146\n" \
  "v_mul_f32 v147, v188, v147\n" \
  "v_mfma_f32_16x16x32_bf16 v[152:155], v[48:51], v[128:131], 0\n" \
  "v_mul_f32 v160, v188, v160\n" \
  "v_mul_f32 v161, v188, v161\n" \
  "v_mfma_f32_16x16x32_bf16 v[172:175], v[84:87], v[184:187], 0\n" \
  "s_waitcnt vmcnt(7)\n" \
  "v_rcp_f32 v190, v170\n" \
  "v_mfma_f32_16x16x32_bf16 v[172:175], v[68:71], v[132:135], v[172:175]\n" \
  "v_cndmask_b32 v167, v188, v189, s[28:29]\n" \
  "v_cndmask_b32 v167, v167, v190, s[26:27]\n" \
  "v_mfma_f32_16x16x32_bf16 v[156:159], v[52:55], v[132:135], 0\n" \
  "v_fmac_f32 v144, v189, v148\n" \
  "v_fmac_f32 v145, v189, v149\n" \
  "v_fmac_f32 v146, v189, v150\n" \
  "v_fmac_f32 v147, v189, v151\n" \
  "v_rcp_f32 v191, v174\n" \
  "v_fmac_f32 v160, v189, v164\n" \
  "v_cndmask_b32 v167, v167, v191, s[24:25]\n" \
  "v_pk_mul_f32 v[176:177], v[176:177], v[166:167] op_sel:[0,1] op_sel_hi:[1,1]\n" \
  "v_pk_mul_f32 v[178:179], v[178:179], v[166:167] op_sel:[0,1] op_sel_hi:[1,1]\n" \
  "v_pk_mul_f32 v[180:181], v[180:181], v[166:167] op_sel:[0,1] op_sel_hi:[1,1]\n" \
  "v_pk_mul_f32 v[182:183], v[182:183], v[166:167] op_sel:[0,1] op_sel_hi:[1,1]\n" \
  "v_cmp_nlt_f32 vcc, 0x0d800000, v167\n" \
  "v_cvt_pk_bf16_f32 v184, v176, v177\n" \
  "v_cvt_pk_bf16_f32 v185, v178, v179\n" \
  "v_cvt_pk_bf16_f32 v186, v180, v181\n" \
  "v_cvt_pk_bf16_f32 v187, v182, v183\n" \
  "v_fmac_f32 v161, v189, v165\n" \
  "v_pk_fma_f32 v[160:161], v[168:169], v[190:191], v[160:161] op_sel:[0,0,0] op_sel_hi:[1,0,1]\n" \
  "v_pk_fma_f32 v[160:161], v[172:173], v[190:191], v[160:161] op_sel:[0,1,0] op_sel_hi:[1,1,1]\n" \
  "v_pk_fma_f32 v[144:145], v[152:153], v[190:191], v[144:145] op_sel:[0,0,0] op_sel_hi:[1,0,1]\n" \
  "v_pk_fma_f32 v[146:147], v[154:155], v[190:191], v[146:147] op_sel:[0,0,0] op_sel_hi:[1,0,1]\n" \
  "v_pk_fma_f32 v[144:145], v[156:157], v[190:191], v[144:145] op_sel:[0,1,0] op_sel_hi:[1,1,1]\n" \
  "v_pk_fma_f32 v[146:147], v[158:159], v[190:191], v[146:147] op_sel:[0,1,0] op_sel_hi:[1,1,1]\n" \
  "s_waitcnt vmcnt(5)\n" \
  "v_mfma_f32_16x16x32_bf16 v[160:163], v[140:143], v[184:187], v[160:163]\n" \
  "s_mov_b64 s[80:81], vcc\n" \
  "v_mfma_f32_16x16x32_bf16 v[144:147], v[136:139], v[184:187], v[144:147]\n" \
  "s_mov_b64 exec, s[30:31]\n" \
  "s_nop 2\n" \
  "ds_write_b64 v22, v[160:161]\n" \
  "v_cmp_u_f32 s[78:79], v160, v161\n" \
  "s_nop 1\n" \
  "ds_write2_b64 v21, v[144:145], v[146:147] offset1:16\n" \
  "v_add_f32 v188, v144, v145\n" \
  "v_add_f32 v189, v146, v147\n" \
  "v_cmp_u_f32 vcc, v188, v189\n" \
  "s_or_b64 s[80:81], s[80:81], s[78:79]\n" \
  "s_or_b64 vcc, vcc, s[80:81]\n" \
  "s_cbranch_vccnz LSF00_%=\n" \
  "LSOK00_%=:\n" \
  "s_mov_b64 exec, -1\n" \
  "s_waitcnt lgkmcnt(0)\n" \
  "s_barrier\n" \
  "ds_read_b32 v188, v33\n" \
  "s_waitcnt lgkmcnt(0)\n" \
  "v_cmp_ne_u32 vcc, 0, v188\n" \
  "s_cbranch_vccnz LSR00_%=\n" \
  "s_nop 15\n" \
  "s_nop 3\n" \
  "buffer_load_dwordx2 v[104:105], v2, s[36:39], s68 offen sc1\n" \
  "buffer_load_dwordx2 v[106:107], v2, s[36:39], s91 offen sc1\n" \
  "buffer_load_dwordx2 v[108:109], v2, s[36:39], s92 offen sc1\n" \
  "buffer_load_dwordx2 v[110:111], v2, s[36:39], s93 offen sc1\n" \
  "LNL00_%=:\n" \
  "s_waitcnt lgkmcnt(0)\n" \
  "s_cmp_eq_u32 s73, 1\n" \
  "s_cbranch_scc1 LOOP00_%=\n" \
  "s_branch LDONE_%=\n" \
  "LMF00_%=:\n" \
  "v_mbcnt_lo_u32_b32 v130, -1, 0\n" \
  "v_mbcnt_hi_u32_b32 v130, -1, v130\n" \
  "v_lshlrev_b32 v130, 4, v130\n" \
  "v_mov_b32 v128, 0\n" \
  "ds_read_b128 v[132:135], v130 offset:0\n" \
  "ds_read_b128 v[136:139], v130 offset:1024\n" \
  "ds_read_b128 v[140:143], v130 offset:2048\n" \
  "ds_read_b128 v[144:147], v130 offset:3072\n" \
  "ds_read_b128 v[148:151], v130 offset:4096\n" \
  "s_waitcnt lgkmcnt(0)\n" \
  "v_max3_u32 v128, v128, v132, v133\n" \
  "v_max3_u32 v128, v128, v134, v135\n" \
  "v_max3_u32 v128, v128, v136, v137\n" \
  "v_max3_u32 v128, v128, v138, v139\n" \
  "v_max3_u32 v128, v128, v140, v141\n" \
  "v_max3_u32 v128, v128, v142, v143\n" \
  "v_max3_u32 v128, v128, v144, v145\n" \
  "v_max3_u32 v128, v128, v146, v147\n" \
  "v_max3_u32 v128, v128, v148, v149\n" \
  "v_max3_u32 v128, v128, v150, v151\n" \
  "ds_read_b128 v[132:135], v130 offset:5120\n" \
  "ds_read_b128 v[136:139], v130 offset:6144\n" \
  "ds_read_b128 v[140:143], v130 offset:7168\n" \
  "ds_read_b128 v[144:147], v130 offset:8192\n" \
  "ds_read_b128 v[148:151], v130 offset:9216\n" \
  "s_waitcnt lgkmcnt(0)\n" \
  "v_max3_u32 v128, v128, v132, v133\n" \
  "v_max3_u32 v128, v128, v134, v135\n" \
  "v_max3_u32 v128, v128, v136, v137\n" \
  "v_max3_u32 v128, v128, v138, v139\n" \
  "v_max3_u32 v128, v128, v140, v141\n" \
  "v_max3_u32 v128, v128, v142, v143\n" \
  "v_max3_u32 v128, v128, v144, v145\n" \
  "v_max3_u32 v128, v128, v146, v147\n" \
  "v_max3_u32 v128, v128, v148, v149\n" \
  "v_max3_u32 v128, v128, v150, v151\n" \
  "v_cmp_eq_u32 vcc, -1, v128\n" \
  "s_cbranch_vccz LMOK00_%=\n" \
  "s_cmp_gt_u32 s58, 4000\n" \
  "s_cbranch_scc1 LMOK00_%=\n" \
  "s_add_u32 s58, s58, 1\n" \
  "s_sub_u32 s64, s64, 1\n" \
  "s_mov_b32 s66, s65\n" \
  "s_waitcnt vmcnt(0) lgkmcnt(0)\n" \
  "s_barrier\n" \
  "s_branch LOOP00_%=\n" \
  "LKE00_%=:\n" \
  "s_waitcnt lgkmcnt(0)\n" \
  "buffer_store_dwordx2 v[34:35], v14, s[36:39], s69 offen sc1\n" \
  "s_branch LDONE_%=\n" \
  "LMX00_%=:\n" \
  "v_cvt_pk_bf16_f32 v155, v152, v153\n" \
  "v_pk_min_u16 v155, v155, -2 op_sel_hi:[1,0]\n" \
  "s_add_u32 s71, s65, 245760\n" \
  "buffer_store_dword v155, v13, s[36:39], s71 offen sc1\n" \
  "v_mov_b32 v154, 0x7fd57fd5\n" \
  "s_branch LMY00_%=\n" \
  "LSF00_%=:\n" \
  "s_getpc_b64 s[78:79]\n" \
  "s_branch LSF_%=\n" \
  "s_branch LSOK00_%=\n" \
  "LSR00_%=:\n" \
  "v_mov_b32 v188, 0\n" \
  "s_add_u32 s59, s59, 1\n" \
  "s_barrier\n" \
  "ds_write_b32 v33, v188\n" \
  "s_waitcnt lgkmcnt(0)\n" \
  "s_branch LB00_%=\n" \
  "LSTART01_%=:\n" \
  "LOOP01_%=:\n" \
  "v_readlane_b32 s70, v27, s64\n" \
  "s_mov_b32 s65, s66\n" \
  "s_xor_b32 s69, s66, 0x2000\n" \
  "s_add_u32 s64, s64, 1\n" \
  "v_mov_b32 v176, 0\n" \
  "v_mov_b32 v177, 0\n" \
  "v_mov_b32 v178, 0\n" \
  "v_mov_b32 v179, 0\n" \
  "v_mov_b32 v180, 0\n" \
  "v_mov_b32 v181, 0\n" \
  "v_mov_b32 v182, 0\n" \
  "v_mov_b32 v183, 0\n" \
  "v_readlane_b32 s66, v25, s64\n" \
  "v_readlane_b32 s68, v26, s64\n" \
  "s_add_u32 s91, s65, 81920\n" \
  "s_add_u32 s92, s65, 163840\n" \
  "s_mov_b32 m0, 28672\n" \
  "s_add_u32 s72, s70, 4096\n" \
  "s_barrier\n" \
  "buffer_load_dwordx4 v3, s[36:39], s70 offen sc1 lds\n" \
  "buffer_load_dwordx4 v3, s[36:39], s70 offen offset:1024 sc1 lds\n" \
  "buffer_load_dwordx4 v3, s[36:39], s70 offen offset:2048 sc1 lds\n" \
  "buffer_load_dwordx4 v3, s[36:39], s70 offen offset:3072 sc1 lds\n" \
  "LM01_%=:\n" \
  "s_barrier\n" \
  "ds_read_b128 v[112:115], v5\n" \
  "ds_read_b128 v[116:119], v5 offset:512\n" \
  "s_barrier\n" \
  "ds_read_b128 v[120:123], v5 offset:1024\n" \
  "ds_read_b128 v[124:127], v5 offset:1536\n" \
  "s_waitcnt lgkmcnt(3)\n" \
  "v_smfmac_f32_16x16x64_bf16 v[176:179], v[112:115], a[0:7], v0\n" \
  "v_smfmac_f32_16x16x64_bf16 v[180:183], v[112:115], a[160:167], v0\n" \
  "s_waitcnt lgkmcnt(2)\n" \
  "v_smfmac_f32_16x16x64_bf16 v[176:179], v[116:119], a[8:15], v0\n" \
  "s_barrier\n" \
  "ds_read_b128 v[128:131], v5 offset:2048\n" \
  "v_smfmac_f32_16x16x64_bf16 v[180:183], v[116:119], a[168:175], v0\n" \
  "s_waitcnt lgkmcnt(2)\n" \
  "ds_read_b128 v[132:135], v5 offset:2560\n" \
  "v_smfmac_f32_16x16x64_bf16 v[176:179], v[120:123], a[16:23], v0\n" \
  "ds_read_b128 v[136:139], v5 offset:3072\n" \
  "v_smfmac_f32_16x16x64_bf16 v[180:183], v[120:123], a[176:183], v0\n" \
  "s_waitcnt lgkmcnt(3)\n" \
  "ds_read_b128 v[140:143], v5 offset:3584\n" \
  "v_smfmac_f32_16x16x64_bf16 v[176:179], v[124:127], a[24:31], v0\n" \
  "s_barrier\n" \
  "s_waitcnt vmcnt(4)\n" \
  "ds_read_b128 v[144:147], v5 offset:4096\n" \
  "v_smfmac_f32_16x16x64_bf16 v[180:183], v[124:127], a[184:191], v0\n" \
  "s_mov_b32 m0, 32768\n" \
  "s_waitcnt lgkmcnt(4)\n" \
  "ds_read_b128 v[148:151], v5 offset:4608\n" \
  "v_smfmac_f32_16x16x64_bf16 v[176:179], v[128:131], a[32:39], v0\n" \
  "ds_read_b128 v[152:155], v5 offset:5120\n" \
  "v_smfmac_f32_16x16x64_bf16 v[180:183], v[128:131], a[192:199], v0\n" \
  "ds_read_b128 v[156:159], v5 offset:5632\n" \
  "buffer_load_dwordx4 v3, s[36:39], s72 offen sc1 lds\n" \
  "s_waitcnt lgkmcnt(6)\n" \
  "v_smfmac_f32_16x16x64_bf16 v[176:179], v[132:135], a[40:47], v0\n" \
  "s_barrier\n" \
  "ds_read_b128 v[160:163], v5 offset:6144\n" \
  "v_smfmac_f32_16x16x64_bf16 v[180:183], v[132:135], a[200:207], v0\n" \
  "ds_read_b128 v[164:167], v5 offset:6656\n" \
  "buffer_load_dwordx4 v3, s[36:39], s72 offen offset:1024 sc1 lds\n" \
  "s_waitcnt lgkmcnt(7)\n" \
  "v_smfmac_f32_16x16x64_bf16 v[176:179], v[136:139], a[48:55], v0\n" \
  "ds_read_b128 v[168:171], v5 offset:7168\n" \
  "v_smfmac_f32_16x16x64_bf16 v[180:183], v[136:139], a[208:215], v0\n" \
  "ds_read_b128 v[172:175], v5 offset:7680\n" \
  "buffer_load_dwordx4 v3, s[36:39], s72 offen offset:2048 sc1 lds\n" \
  "s_waitcnt lgkmcnt(8)\n" \
  "v_smfmac_f32_16x16x64_bf16 v[176:179], v[140:143], a[56:63], v0\n" \
  "s_barrier\n" \
  "ds_read2_b32 v[112:113], v6 offset0:0 offset1:32\n" \
  "v_smfmac_f32_16x16x64_bf16 v[180:183], v[140:143], a[216:223], v0\n" \
  "ds_read2_b32 v[114:115], v6 offset0:64 offset1:96\n" \
  "buffer_load_dwordx4 v3, s[36:39], s72 offen offset:3072 sc1 lds\n" \
  "s_waitcnt lgkmcnt(9)\n" \
  "v_smfmac_f32_16x16x64_bf16 v[176:179], v[144:147], a[64:71], v0\n" \
  "s_waitcnt lgkmcnt(8)\n" \
  "ds_read2_b32 v[116:117], v6 offset0:128 offset1:160\n" \
  "v_smfmac_f32_16x16x64_bf16 v[176:179], v[148:151], a[72:79], v0\n" \
  "s_waitcnt lgkmcnt(8)\n" \
  "ds_read2_b32 v[118:119], v6 offset0:192 offset1:224\n" \
  "v_smfmac_f32_16x16x64_bf16 v[176:179], v[152:155], a[80:87], v0\n" \
  "s_waitcnt lgkmcnt(8)\n" \
  "ds_read2_b32 v[120:121], v7 offset0:0 offset1:32\n" \
  "v_smfmac_f32_16x16x64_bf16 v[176:179], v[156:159], a[88:95], v0\n" \
  "s_waitcnt lgkmcnt(8)\n" \
  "ds_read2_b32 v[122:123], v7 offset0:64 offset1:96\n" \
  "v_smfmac_f32_16x16x64_bf16 v[176:179], v[160:163], a[96:103], v0\n" \
  "s_waitcnt lgkmcnt(8)\n" \
  "ds_read2_b32 v[124:125], v7 offset0:128 offset1:160\n" \
  "v_smfmac_f32_16x16x64_bf16 v[176:179], v[164:167], a[104:111], v0\n" \
  "s_waitcnt lgkmcnt(8)\n" \
  "ds_read2_b32 v[126:127], v7 offset0:192 offset1:224\n" \
  "v_smfmac_f32_16x16x64_bf16 v[176:179], v[168:171], a[112:119], v0\n" \
  "s_waitcnt lgkmcnt(8)\n" \
  "v_smfmac_f32_16x16x64_bf16 v[176:179], v[172:175], a[120:127], v0\n" \
  "s_waitcnt lgkmcnt(6)\n" \
  "v_smfmac_f32_16x16x64_bf16 v[176:179], v[112:115], a[128:135], v0\n" \
  "s_waitcnt lgkmcnt(4)\n" \
  "v_smfmac_f32_16x16x64_bf16 v[176:179], v[116:119], a[136:143], v0\n" \
  "s_waitcnt lgkmcnt(2)\n" \
  "v_smfmac_f32_16x16x64_bf16 v[176:179], v[120:123], a[144:151], v0\n" \
  "s_waitcnt lgkmcnt(0)\n" \
  "v_smfmac_f32_16x16x64_bf16 v[176:179], v[124:127], a[152:159], v0\n" \
  "v_smfmac_f32_16x16x64_bf16 v[180:183], v[144:147], a[224:231], v0\n" \
  "v_smfmac_f32_16x16x64_bf16 v[180:183], v[148:151], a[232:239], v0\n" \
  "v_cvt_pk_bf16_f32 v184, v176, v177\n" \
  "v_smfmac_f32_16x16x64_bf16 v[180:183], v[152:155], a[240:247], v0\n" \
  "v_cvt_pk_bf16_f32 v185, v178, v179\n" \
  "v_smfmac_f32_16x16x64_bf16 v[180:183], v[156:159], a[248:255], v0\n" \
  "ds_write_b64 v8, v[184:185]\n" \
  "v_add_f32 v186, v176, v177\n" \
  "v_smfmac_f32_16x16x64_bf16 v[180:183], v[160:163], v[192:199], v0\n" \
  "v_add_f32 v187, v178, v179\n" \
  "v_smfmac_f32_16x16x64_bf16 v[180:183], v[164:167], v[200:207], v0\n" \
  "v_cmp_u_f32 vcc, v186, v187\n" \
  "v_smfmac_f32_16x16x64_bf16 v[180:183], v[168:171], v[208:215], v0\n" \
  "v_smfmac_f32_16x16x64_bf16 v[180:183], v[172:175], v[216:223], v0\n" \
  "s_cbranch_vccnz LMF01_%=\n" \
  "LMOK01_%=:\n" \
  "v_smfmac_f32_16x16x64_bf16 v[180:183], v[112:115], v[224:231], v0\n" \
  "s_waitcnt lgkmcnt(0)\n" \
  "s_barrier\n" \
  "ds_read_b64_tr_b16 v[128:129], v10\n" \
  "v_smfmac_f32_16x16x64_bf16 v[180:183], v[116:119], v[232:239], v0\n" \
  "ds_read_b64_tr_b16 v[130:131], v10 offset:32\n" \
  "ds_read_b64_tr_b16 v[132:133], v10 offset:512\n" \
  "v_smfmac_f32_16x16x64_bf16 v[180:183], v[120:123], v[240:247], v0\n" \
  "ds_read_b64_tr_b16 v[134:135], v10 offset:544\n" \
  "ds_read_b64_tr_b16 v[136:137], v10 offset:1024\n" \
  "v_smfmac_f32_16x16x64_bf16 v[180:183], v[124:127], v[248:255], v0\n" \
  "s_nop 0\n" \
  "ds_read_b64_tr_b16 v[138:139], v10 offset:1056\n" \
  "ds_read_b64_tr_b16 v[140:141], v10 offset:1536\n" \
  "ds_read_b64_tr_b16 v[142:143], v10 offset:1568\n" \
  "v_mov_b32 v31, v31\n" \
  "s_nop 2\n" \
  "v_cvt_pk_bf16_f32 v186, v180, v181\n" \
  "v_cvt_pk_bf16_f32 v187, v182, v183\n" \
  "ds_write_b64 v9, v[186:187]\n" \
  "s_waitcnt lgkmcnt(7)\n" \
  "v_mfma_f32_16x16x32_bf16 v[188:191], v[88:91], v[128:131], 0\n" \
  "s_waitcnt lgkmcnt(5)\n" \
  "v_mfma_f32_16x16x32_bf16 v[188:191], v[92:95], v[132:135], v[188:191]\n" \
  "s_waitcnt lgkmcnt(3)\n" \
  "v_mfma_f32_16x16x32_bf16 v[188:191], v[36:39], v[136:139], v[188:191]\n" \
  "s_waitcnt lgkmcnt(1)\n" \
  "v_mfma_f32_16x16x32_bf16 v[188:191], v[100:103], v[140:143], v[188:191]\n" \
  "s_waitcnt lgkmcnt(0)\n" \
  "s_barrier\n" \
  "ds_read_b64_tr_b16 v[144:145], v11\n" \
  "ds_read_b64_tr_b16 v[146:147], v11 offset:512\n" \
  "ds_read_b64_tr_b16 v[148:149], v11 offset:1024\n" \
  "ds_read_b64_tr_b16 v[150:151], v11 offset:1536\n" \
  "v_mov_b32 v31, v31\n" \
  "s_nop 2\n" \
  "v_mov_b32 v31, v31\n" \
  "s_nop 2\n" \
  "v_mov_b32 v31, v31\n" \
  "s_nop 0\n" \
  "s_waitcnt lgkmcnt(2)\n" \
  "v_mfma_f32_16x16x32_bf16 v[188:191], v[104:107], v[144:147], v[188:191]\n" \
  "s_waitcnt lgkmcnt(0)\n" \
  "v_mfma_f32_16x16x32_bf16 v[188:191], v[108:111], v[148:151], v[188:191]\n" \
  "s_nop 0\n" \
  "ds_read_b64_tr_b16 v[34:35], v12\n" \
  "s_nop 1\n" \
  "v_mov_b32 v31, v31\n" \
  "s_nop 2\n" \
  "v_cndmask_b32 v152, v190, v188, s[20:21]\n" \
  "v_cndmask_b32 v153, v191, v189, s[20:21]\n" \
  "v_mul_f32 v152, 0x3dd53b94, v152\n" \
  "v_mul_f32 v153, 0x3dd53b94, v153\n" \
  "v_max_f32 v156, |v152|, |v153|\n" \
  "v_cmp_lt_f32 vcc, 0x42800000, v156\n" \
  "v_exp_f32 v154, v152\n" \
  "v_exp_f32 v155, v153\n" \
  "s_cbranch_vccnz LMX01_%=\n" \
  "v_cvt_pk_bf16_f32 v154, v154, v155\n" \
  "LMY01_%=:\n" \
  "buffer_store_dword v154, v13, s[36:39], s65 offen sc1\n" \
  "buffer_store_dword v154, v13, s[36:39], s91 offen sc1\n" \
  "buffer_store_dword v154, v13, s[36:39], s92 offen sc1\n" \
  "s_mov_b32 s67, s66\n" \
  "s_cmp_eq_u32 s64, 64\n" \
  "s_cbranch_scc1 LKE01_%=\n" \
  "LB01_%=:\n" \
  "s_barrier\n" \
  "buffer_load_dwordx4 v[116:119], v15, s[36:39], s65 offen offset:128 sc1\n" \
  "s_barrier\n" \
  "buffer_load_dwordx4 v[112:115], v15, s[36:39], s65 offen sc1\n" \
  "s_barrier\n" \
  "buffer_load_dwordx4 v[120:123], v16, s[36:39], s65 offen sc1\n" \
  "s_barrier\n" \
  "buffer_load_dwordx4 v[124:127], v16, s[36:39], s65 offen offset:512 sc1\n" \
  "s_barrier\n" \
  "buffer_load_dwordx4 v[128:131], v16, s[36:39], s65 offen offset:1024 sc1\n" \
  "s_waitcnt vmcnt(8)\n" \
  "s_barrier\n" \
  "buffer_load_dwordx4 v[132:135], v16, s[36:39], s65 offen offset:1536 sc1\n" \
  "ds_read_b128 v[40:43], v19\n" \
  "ds_read_b128 v[44:47], v19 offset:512\n" \
  "ds_read_b128 v[48:51], v19 offset:1024\n" \
  "ds_read_b128 v[52:55], v19 offset:1536\n" \
  "s_mov_b64 exec, s[46:47]\n" \
  "ds_read_b128 v[56:59], v20\n" \
  "ds_read_b128 v[60:63], v20 offset:128\n" \
  "ds_read_b128 v[64:67], v20 offset:256\n" \
  "ds_read_b128 v[68:71], v20 offset:384\n" \
  "s_mov_b64 exec, -1\n" \
  "s_nop 15\n" \
  "s_nop 1\n" \
  "buffer_load_dwordx4 v[136:139], v17, s[36:39], s65 offen sc1\n" \
  "buffer_load_dwordx4 v[140:143], v18, s[36:39], s65 offen sc1\n" \
  "s_barrier\n" \
  "s_waitcnt lgkmcnt(0)\n" \
  "buffer_store_dwordx2 v[34:35], v14, s[36:39], s69 offen sc1\n" \
  "s_add_u32 s91, s68, 16384\n" \
  "s_add_u32 s92, s68, 32768\n" \
  "s_add_u32 s93, s68, 49152\n" \
  "buffer_load_dwordx4 v[88:91], v1, s[36:39], s68 offen sc1\n" \
  "buffer_load_dwordx4 v[92:95], v1, s[36:39], s91 offen sc1\n" \
  "buffer_load_dwordx4 v[36:39], v1, s[36:39], s92 offen sc1\n" \
  "buffer_load_dwordx4 v[100:103], v1, s[36:39], s93 offen sc1\n" \
  "s_waitcnt lgkmcnt(0)\n" \
  "s_getreg_b32 s71, hwreg(HW_REG_IB_STS, 0, 4)\n" \
  "s_cmp_le_u32 s71, 11\n" \
  "v_mfma_f32_16x16x32_bf16 v[144:147], v[72:75], v[72:75], 0\n" \
  "s_cbranch_scc1 LHXS01_%=\n" \
  "s_getreg_b32 s71, hwreg(HW_REG_IB_STS, 0, 4)\n" \
  "s_cmp_le_u32 s71, 11\n" \
  "v_mfma_f32_16x16x32_bf16 v[144:147], v[72:75], v[72:75], 0\n" \
  "s_cbranch_scc1 LHXS01_%=\n" \
  "s_getreg_b32 s71, hwreg(HW_REG_IB_STS, 0, 4)\n" \
  "s_cmp_le_u32 s71, 11\n" \
  "v_mfma_f32_16x16x32_bf16 v[144:147], v[72:75], v[72:75], 0\n" \
  "s_cbranch_scc1 LHXS01_%=\n" \
  "s_getreg_b32 s71, hwreg(HW_REG_IB_STS, 0, 4)\n" \
  "s_cmp_le_u32 s71, 11\n" \
  "v_mfma_f32_16x16x32_bf16 v[144:147], v[72:75], v[72:75], 0\n" \
  "s_cbranch_scc1 LHXS01_%=\n" \
  "s_getreg_b32 s71, hwreg(HW_REG_IB_STS, 0, 4)\n" \
  "s_cmp_le_u32 s71, 11\n" \
  "v_mfma_f32_16x16x32_bf16 v[144:147], v[72:75], v[72:75], 0\n" \
  "s_cbranch_scc1 LHXS01_%=\n" \
  "s_getreg_b32 s71, hwreg(HW_REG_IB_STS, 0, 4)\n" \
  "s_cmp_le_u32 s71, 11\n" \
  "v_mfma_f32_16x16x32_bf16 v[144:147], v[72:75], v[72:75], 0\n" \
  "s_cbranch_scc1 LHXS01_%=\n" \
  "s_getreg_b32 s71, hwreg(HW_REG_IB_STS, 0, 4)\n" \
  "s_cmp_le_u32 s71, 11\n" \
  "v_mfma_f32_16x16x32_bf16 v[144:147], v[72:75], v[72:75], 0\n" \
  "s_cbranch_scc1 LHXS01_%=\n" \
  "s_getreg_b32 s71, hwreg(HW_REG_IB_STS, 0, 4)\n" \
  "s_cmp_le_u32 s71, 11\n" \
  "v_mfma_f32_16x16x32_bf16 v[144:147], v[72:75], v[72:75], 0\n" \
  "s_cbranch_scc1 LHXS01_%=\n" \
  "s_getreg_b32 s71, hwreg(HW_REG_IB_STS, 0, 4)\n" \
  "s_cmp_le_u32 s71, 11\n" \
  "v_mfma_f32_16x16x32_bf16 v[144:147], v[72:75], v[72:75], 0\n" \
  "s_cbranch_scc1 LHXS01_%=\n" \
  "s_getreg_b32 s71, hwreg(HW_REG_IB_STS, 0, 4)\n" \
  "s_cmp_le_u32 s71, 11\n" \
  "v_mfma_f32_16x16x32_bf16 v[144:147], v[72:75], v[72:75], 0\n" \
  "s_cbranch_scc1 LHXS01_%=\n" \
  "s_getreg_b32 s71, hwreg(HW_REG_IB_STS, 0, 4)\n" \
  "s_cmp_le_u32 s71, 11\n" \
  "v_mfma_f32_16x16x32_bf16 v[144:147], v[72:75], v[72:75], 0\n" \
  "s_cbranch_scc1 LHXS01_%=\n" \
  "s_getreg_b32 s71, hwreg(HW_REG_IB_STS, 0, 4)\n" \
  "s_cmp_le_u32 s71, 11\n" \
  "v_mfma_f32_16x16x32_bf16 v[144:147], v[72:75], v[72:75], 0\n" \
  "s_cbranch_scc1 LHXS01_%=\n" \
  "s_getreg_b32 s71, hwreg(HW_REG_IB_STS, 0, 4)\n" \
  "s_cmp_le_u32 s71, 11\n" \
  "v_mfma_f32_16x16x32_bf16 v[144:147], v[72:75], v[72:75], 0\n" \
  "s_cbranch_scc1 LHXS01_%=\n" \
  "s_getreg_b32 s71, hwreg(HW_REG_IB_STS, 0, 4)\n" \
  "s_cmp_le_u32 s71, 11\n" \
  "v_mfma_f32_16x16x32_bf16 v[144:147], v[72:75], v[72:75], 0\n" \
  "s_cbranch_scc1 LHXS01_%=\n" \
  "s_getreg_b32 s71, hwreg(HW_REG_IB_STS, 0, 4)\n" \
  "s_cmp_le_u32 s71, 11\n" \
  "v_mfma_f32_16x16x32_bf16 v[144:147], v[72:75], v[72:75], 0\n" \
  "s_cbranch_scc1 LHXS01_%=\n" \
  "s_getreg_b32 s71, hwreg(HW_REG_IB_STS, 0, 4)\n" \
  "s_cmp_le_u32 s71, 11\n" \
  "v_mfma_f32_16x16x32_bf16 v[144:147], v[72:75], v[72:75], 0\n" \
  "s_cbranch_scc1 LHXS01_%=\n" \
  "s_getreg_b32 s71, hwreg(HW_REG_IB_STS, 0, 4)\n" \
  "s_cmp_le_u32 s71, 11\n" \
  "v_mfma_f32_16x16x32_bf16 v[144:147], v[72:75], v[72:75], 0\n" \
  "s_cbranch_scc1 LHXS01_%=\n" \
  "s_getreg_b32 s71, hwreg(HW_REG_IB_STS, 0, 4)\n" \
  "s_cmp_le_u32 s71, 11\n" \
  "v_mfma_f32_16x16x32_bf16 v[144:147], v[72:75], v[72:75], 0\n" \
  "s_cbranch_scc1 LHXS01_%=\n" \
  "s_getreg_b32 s71, hwreg(HW_REG_IB_STS, 0, 4)\n" \
  "s_cmp_le_u32 s71, 11\n" \
  "v_mfma_f32_16x16x32_bf16 v[144:147], v[72:75], v[72:75], 0\n" \
  "s_cbranch_scc1 LHXS01_%=\n" \
  "s_getreg_b32 s71, hwreg(HW_REG_IB_STS, 0, 4)\n" \
  "s_cmp_le_u32 s71, 11\n" \
  "v_mfma_f32_16x16x32_bf16 v[144:147], v[72:75], v[72:75], 0\n" \
  "s_cbranch_scc1 LHXS01_%=\n" \
  "LHXS01_%=:\n" \
  "s_waitcnt vmcnt(11)\n" \
  "LS201_%=:\n" \
  "v_dot2_f32_bf16 v176, v112, s22, 0\n" \
  "v_dot2_f32_bf16 v177, v112, s23, 0\n" \
  "v_dot2_f32_bf16 v178, v113, s22, 0\n" \
  "v_dot2_f32_bf16 v179, v113, s23, 0\n" \
  "v_dot2_f32_bf16 v180, v114, s22, 0\n" \
  "v_dot2_f32_bf16 v181, v114, s23, 0\n" \
  "v_dot2_f32_bf16 v182, v115, s22, 0\n" \
  "v_dot2_f32_bf16 v183, v115, s23, 0\n" \
  "v_dot2_f32_bf16 v176, v116, s22, v176\n" \
  "v_dot2_f32_bf16 v177, v116, s23, v177\n" \
  "v_dot2_f32_bf16 v178, v117, s22, v178\n" \
  "v_dot2_f32_bf16 v179, v117, s23, v179\n" \
  "v_dot2_f32_bf16 v180, v118, s22, v180\n" \
  "v_dot2_f32_bf16 v181, v118, s23, v181\n" \
  "v_dot2_f32_bf16 v182, v119, s22, v182\n" \
  "v_dot2_f32_bf16 v183, v119, s23, v183\n" \
  "v_exp_f32 v176, v176\n" \
  "v_exp_f32 v177, v177\n" \
  "v_exp_f32 v178, v178\n" \
  "v_exp_f32 v179, v179\n" \
  "s_waitcnt vmcnt(10)\n" \
  "v_mfma_f32_16x16x32_bf16 v[160:163], v[56:59], v[120:123], 0\n" \
  "v_exp_f32 v180, v180\n" \
  "v_mfma_f32_16x16x32_bf16 v[144:147], v[40:43], v[120:123], 0\n" \
  "v_exp_f32 v181, v181\n" \
  "v_exp_f32 v182, v182\n" \
  "v_exp_f32 v183, v183\n" \
  "v_cvt_pk_bf16_f32 v184, v176, v177\n" \
  "v_cvt_pk_bf16_f32 v185, v178, v179\n" \
  "v_cvt_pk_bf16_f32 v186, v180, v181\n" \
  "v_cvt_pk_bf16_f32 v187, v182, v183\n" \
  "s_nop 1\n" \
  "v_mfma_f32_16x16x32_bf16 v[160:163], v[72:75], v[184:187], v[160:163]\n" \
  "v_mfma_f32_16x16x32_bf16 v[164:167], v[76:79], v[184:187], 0\n" \
  "s_waitcnt vmcnt(9)\n" \
  "v_mfma_f32_16x16x32_bf16 v[164:167], v[60:63], v[124:127], v[164:167]\n" \
  "v_rcp_f32 v188, v162\n" \
  "v_mfma_f32_16x16x32_bf16 v[148:151], v[44:47], v[124:127], 0\n" \
  "v_mul_f32 v144, v188, v144\n" \
  "v_mul_f32 v145, v188, v145\n" \
  "v_mfma_f32_16x16x32_bf16 v[168:171], v[80:83], v[184:187], 0\n" \
  "s_waitcnt vmcnt(8)\n" \
  "v_rcp_f32 v189, v166\n" \
  "v_mfma_f32_16x16x32_bf16 v[168:171], v[64:67], v[128:131], v[168:171]\n" \
  "v_mul_f32 v146, v188, v146\n" \
  "v_mul_f32 v147, v188, v147\n" \
  "v_mfma_f32_16x16x32_bf16 v[152:155], v[48:51], v[128:131], 0\n" \
  "v_mul_f32 v160, v188, v160\n" \
  "v_mul_f32 v161, v188, v161\n" \
  "v_mfma_f32_16x16x32_bf16 v[172:175], v[84:87], v[184:187], 0\n" \
  "s_waitcnt vmcnt(7)\n" \
  "v_rcp_f32 v190, v170\n" \
  "v_mfma_f32_16x16x32_bf16 v[172:175], v[68:71], v[132:135], v[172:175]\n" \
  "v_cndmask_b32 v167, v188, v189, s[28:29]\n" \
  "v_cndmask_b32 v167, v167, v190, s[26:27]\n" \
  "v_mfma_f32_16x16x32_bf16 v[156:159], v[52:55], v[132:135], 0\n" \
  "v_fmac_f32 v144, v189, v148\n" \
  "v_fmac_f32 v145, v189, v149\n" \
  "v_fmac_f32 v146, v189, v150\n" \
  "v_fmac_f32 v147, v189, v151\n" \
  "v_rcp_f32 v191, v174\n" \
  "v_fmac_f32 v160, v189, v164\n" \
  "v_cndmask_b32 v167, v167, v191, s[24:25]\n" \
  "v_pk_mul_f32 v[176:177], v[176:177], v[166:167] op_sel:[0,1] op_sel_hi:[1,1]\n" \
  "v_pk_mul_f32 v[178:179], v[178:179], v[166:167] op_sel:[0,1] op_sel_hi:[1,1]\n" \
  "v_pk_mul_f32 v[180:181], v[180:181], v[166:167] op_sel:[0,1] op_sel_hi:[1,1]\n" \
  "v_pk_mul_f32 v[182:183], v[182:183], v[166:167] op_sel:[0,1] op_sel_hi:[1,1]\n" \
  "v_cmp_nlt_f32 vcc, 0x0d800000, v167\n" \
  "v_cvt_pk_bf16_f32 v184, v176, v177\n" \
  "v_cvt_pk_bf16_f32 v185, v178, v179\n" \
  "v_cvt_pk_bf16_f32 v186, v180, v181\n" \
  "v_cvt_pk_bf16_f32 v187, v182, v183\n" \
  "v_fmac_f32 v161, v189, v165\n" \
  "v_pk_fma_f32 v[160:161], v[168:169], v[190:191], v[160:161] op_sel:[0,0,0] op_sel_hi:[1,0,1]\n" \
  "v_pk_fma_f32 v[160:161], v[172:173], v[190:191], v[160:161] op_sel:[0,1,0] op_sel_hi:[1,1,1]\n" \
  "v_pk_fma_f32 v[144:145], v[152:153], v[190:191], v[144:145] op_sel:[0,0,0] op_sel_hi:[1,0,1]\n" \
  "v_pk_fma_f32 v[146:147], v[154:155], v[190:191], v[146:147] op_sel:[0,0,0] op_sel_hi:[1,0,1]\n" \
  "v_pk_fma_f32 v[144:145], v[156:157], v[190:191], v[144:145] op_sel:[0,1,0] op_sel_hi:[1,1,1]\n" \
  "v_pk_fma_f32 v[146:147], v[158:159], v[190:191], v[146:147] op_sel:[0,1,0] op_sel_hi:[1,1,1]\n" \
  "s_waitcnt vmcnt(5)\n" \
  "v_mfma_f32_16x16x32_bf16 v[160:163], v[140:143], v[184:187], v[160:163]\n" \
  "s_mov_b64 s[80:81], vcc\n" \
  "v_mfma_f32_16x16x32_bf16 v[144:147], v[136:139], v[184:187], v[144:147]\n" \
  "s_mov_b64 exec, s[30:31]\n" \
  "s_nop 2\n" \
  "ds_write_b64 v22, v[160:161]\n" \
  "v_cmp_u_f32 s[78:79], v160, v161\n" \
  "s_nop 1\n" \
  "ds_write2_b64 v21, v[144:145], v[146:147] offset1:16\n" \
  "v_add_f32 v188, v144, v145\n" \
  "v_add_f32 v189, v146, v147\n" \
  "v_cmp_u_f32 vcc, v188, v189\n" \
  "s_or_b64 s[80:81], s[80:81], s[78:79]\n" \
  "s_or_b64 vcc, vcc, s[80:81]\n" \
  "s_cbranch_vccnz LSF01_%=\n" \
  "LSOK01_%=:\n" \
  "s_mov_b64 exec, -1\n" \
  "s_waitcnt lgkmcnt(0)\n" \
  "s_barrier\n" \
  "ds_read_b32 v188, v33\n" \
  "s_waitcnt lgkmcnt(0)\n" \
  "v_cmp_ne_u32 vcc, 0, v188\n" \
  "s_cbranch_vccnz LSR01_%=\n" \
  "s_nop 15\n" \
  "s_nop 3\n" \
  "buffer_load_dwordx2 v[104:105], v2, s[36:39], s68 offen sc1\n" \
  "buffer_load_dwordx2 v[106:107], v2, s[36:39], s91 offen sc1\n" \
  "buffer_load_dwordx2 v[108:109], v2, s[36:39], s92 offen sc1\n" \
  "buffer_load_dwordx2 v[110:111], v2, s[36:39], s93 offen sc1\n" \
  "LNL01_%=:\n" \
  "s_waitcnt lgkmcnt(0)\n" \
  "s_cmp_eq_u32 s73, 1\n" \
  "s_cbranch_scc1 LOOP01_%=\n" \
  "s_branch LDONE_%=\n" \
  "LMF01_%=:\n" \
  "v_mbcnt_lo_u32_b32 v130, -1, 0\n" \
  "v_mbcnt_hi_u32_b32 v130, -1, v130\n" \
  "v_lshlrev_b32 v130, 4, v130\n" \
  "v_mov_b32 v128, 0\n" \
  "ds_read_b128 v[132:135], v130 offset:0\n" \
  "ds_read_b128 v[136:139], v130 offset:1024\n" \
  "ds_read_b128 v[140:143], v130 offset:2048\n" \
  "ds_read_b128 v[144:147], v130 offset:3072\n" \
  "ds_read_b128 v[148:151], v130 offset:4096\n" \
  "s_waitcnt lgkmcnt(0)\n" \
  "v_max3_u32 v128, v128, v132, v133\n" \
  "v_max3_u32 v128, v128, v134, v135\n" \
  "v_max3_u32 v128, v128, v136, v137\n" \
  "v_max3_u32 v128, v128, v138, v139\n" \
  "v_max3_u32 v128, v128, v140, v141\n" \
  "v_max3_u32 v128, v128, v142, v143\n" \
  "v_max3_u32 v128, v128, v144, v145\n" \
  "v_max3_u32 v128, v128, v146, v147\n" \
  "v_max3_u32 v128, v128, v148, v149\n" \
  "v_max3_u32 v128, v128, v150, v151\n" \
  "ds_read_b128 v[132:135], v130 offset:5120\n" \
  "ds_read_b128 v[136:139], v130 offset:6144\n" \
  "ds_read_b128 v[140:143], v130 offset:7168\n" \
  "ds_read_b128 v[144:147], v130 offset:8192\n" \
  "ds_read_b128 v[148:151], v130 offset:9216\n" \
  "s_waitcnt lgkmcnt(0)\n" \
  "v_max3_u32 v128, v128, v132, v133\n" \
  "v_max3_u32 v128, v128, v134, v135\n" \
  "v_max3_u32 v128, v128, v136, v137\n" \
  "v_max3_u32 v128, v128, v138, v139\n" \
  "v_max3_u32 v128, v128, v140, v141\n" \
  "v_max3_u32 v128, v128, v142, v143\n" \
  "v_max3_u32 v128, v128, v144, v145\n" \
  "v_max3_u32 v128, v128, v146, v147\n" \
  "v_max3_u32 v128, v128, v148, v149\n" \
  "v_max3_u32 v128, v128, v150, v151\n" \
  "v_cmp_eq_u32 vcc, -1, v128\n" \
  "s_cbranch_vccz LMOK01_%=\n" \
  "s_cmp_gt_u32 s58, 4000\n" \
  "s_cbranch_scc1 LMOK01_%=\n" \
  "s_add_u32 s58, s58, 1\n" \
  "s_sub_u32 s64, s64, 1\n" \
  "s_mov_b32 s66, s65\n" \
  "s_waitcnt vmcnt(0) lgkmcnt(0)\n" \
  "s_barrier\n" \
  "s_branch LOOP01_%=\n" \
  "LKE01_%=:\n" \
  "s_waitcnt lgkmcnt(0)\n" \
  "buffer_store_dwordx2 v[34:35], v14, s[36:39], s69 offen sc1\n" \
  "s_branch LDONE_%=\n" \
  "LMX01_%=:\n" \
  "v_cvt_pk_bf16_f32 v155, v152, v153\n" \
  "v_pk_min_u16 v155, v155, -2 op_sel_hi:[1,0]\n" \
  "s_add_u32 s71, s65, 245760\n" \
  "buffer_store_dword v155, v13, s[36:39], s71 offen sc1\n" \
  "v_mov_b32 v154, 0x7fd57fd5\n" \
  "s_branch LMY01_%=\n" \
  "LSF01_%=:\n" \
  "s_getpc_b64 s[78:79]\n" \
  "s_branch LSF_%=\n" \
  "s_branch LSOK01_%=\n" \
  "LSR01_%=:\n" \
  "v_mov_b32 v188, 0\n" \
  "s_add_u32 s59, s59, 1\n" \
  "s_barrier\n" \
  "ds_write_b32 v33, v188\n" \
  "s_waitcnt lgkmcnt(0)\n" \
  "s_branch LB01_%=\n" \
  "LSTART02_%=:\n" \
  "s_getpc_b64 s[80:81]\n" \
  "LPC02_%=:\n" \
  "s_add_u32 s82, s80, LEA02_%=-LPC02_%=+24\n" \
  "s_addc_u32 s83, s81, 0\n" \
  "s_add_u32 s84, s80, LEB02_%=-LPC02_%=+24\n" \
  "s_addc_u32 s85, s81, 0\n" \
  "LOOP02_%=:\n" \
  "v_readfirstlane_b32 s86, v34\n" \
  "v_readlane_b32 s43, v34, s53\n" \
  "s_add_u32 s71, s64, 1\n" \
  "s_min_u32 s71, s71, 63\n" \
  "s_lshr_b32 s72, s71, 4\n" \
  "s_lshl_b32 s72, s72, 13\n" \
  "s_lshl_b32 s71, s71, 9\n" \
  "s_add_u32 s71, s71, s72\n" \
  "s_mov_b32 s87, s71\n" \
  "s_lshl_b32 s40, s64, 7\n" \
  "s_mov_b32 m0, 0\n" \
  "LWA02_%=:\n" \
  "s_memrealtime s[74:75]\n" \
  "s_memtime s[76:77]\n" \
  "s_waitcnt lgkmcnt(0)\n" \
  "s_sub_i32 s72, s88, s74\n" \
  "s_mul_i32 s72, s72, s90\n" \
  "s_ashr_i32 s72, s72, 8\n" \
  "s_sub_i32 s71, s89, s76\n" \
  "s_min_i32 s71, s71, s72\n" \
  "s_sub_i32 s72, s72, 92\n" \
  "s_max_i32 s71, s71, s72\n" \
  "s_add_u32 s89, s71, s76\n" \
  "s_cmp_gt_i32 s71, 168\n" \
  "s_cbranch_scc1 LWA02_%=\n" \
  "s_cmp_lt_i32 s71, s94\n" \
  "s_cbranch_scc1 LSA02_%=\n" \
  "s_max_i32 s71, s71, 48\n" \
  "s_lshr_b32 s71, s71, 3\n" \
  "s_lshl_b32 s71, s71, 2\n" \
  "s_sub_u32 s78, s82, s71\n" \
  "s_subb_u32 s79, s83, 0\n" \
  "s_setpc_b64 s[78:79]\n" \
  "s_nop 1\n" \
  "s_nop 1\n" \
  "s_nop 1\n" \
  "s_nop 1\n" \
  "s_nop 1\n" \
  "s_nop 1\n" \
  "s_nop 1\n" \
  "s_nop 1\n" \
  "s_nop 1\n" \
  "s_nop 1\n" \
  "s_nop 1\n" \
  "s_nop 1\n" \
  "s_nop 1\n" \
  "s_nop 1\n" \
  "s_nop 1\n" \
  "s_nop 1\n" \
  "LEA02_%=:\n" \
  "ds_read_b32 v188, v33\n" \
  "buffer_load_dwordx4 v1, s[36:39], s67 offen sc1 lds\n" \
  "buffer_load_dwordx4 v1, s[36:39], s67 offen offset:1024 sc1 lds\n" \
  "buffer_load_dwordx4 v1, s[36:39], s67 offen offset:2048 sc1 lds\n" \
  "buffer_load_dwordx4 v1, s[36:39], s67 offen offset:3072 sc1 lds\n" \
  "s_mov_b32 m0, 4096\n" \
  "s_nop 0\n" \
  "buffer_load_dwordx4 v2, s[36:39], s67 offen sc1 lds\n" \
  "buffer_load_dwordx4 v2, s[36:39], s67 offen offset:1024 sc1 lds\n" \
  "buffer_load_dwordx4 v2, s[36:39], s67 offen offset:2048 sc1 lds\n" \
  "buffer_load_dwordx4 v2, s[36:39], s67 offen offset:3072 sc1 lds\n" \
  "s_mov_b32 m0, 8192\n" \
  "s_nop 0\n" \
  "buffer_load_dwordx4 v3, s[36:39], s67 offen sc1 lds\n" \
  "buffer_load_dwordx4 v3, s[36:39], s67 offen offset:1024 sc1 lds\n" \
  "s_waitcnt lgkmcnt(0)\n" \
  "v_cmp_ne_u32 vcc, 0, v188\n" \
  "s_cbranch_vccnz LSR02_%=\n" \
  "buffer_load_dword v35, v28, s[36:39], s87 offen sc1\n" \
  "buffer_load_dword v34, v29, s[36:39], s40 offen offset:128 sc1\n" \
  "v_readlane_b32 s70, v27, s64\n" \
  "s_mov_b32 s65, s66\n" \
  "s_xor_b32 s69, s66, 0x2000\n" \
  "s_add_u32 s64, s64, 1\n" \
  "s_barrier\n" \
  "v_mov_b32 v176, 0\n" \
  "v_mov_b32 v177, 0\n" \
  "v_mov_b32 v178, 0\n" \
  "v_mov_b32 v179, 0\n" \
  "v_mov_b32 v180, 0\n" \
  "v_mov_b32 v181, 0\n" \
  "v_mov_b32 v182, 0\n" \
  "v_mov_b32 v183, 0\n" \
  "v_readlane_b32 s66, v25, s64\n" \
  "v_readlane_b32 s68, v26, s64\n" \
  "s_cmp_eq_u32 s34, s64\n" \
  "s_cbranch_scc1 LRT02_%=\n" \
  "s_mov_b32 s34, s64\n" \
  "s_lshr_b32 s71, s86, 8\n" \
  "s_and_b32 s72, s86, 0xff\n" \
  "s_cselect_b32 s61, s72, s61\n" \
  "s_cselect_b32 s71, s71, s92\n" \
  "s_sub_i32 s72, s71, s92\n" \
  "s_add_u32 s92, s71, s61\n" \
  "s_lshl_b32 s72, s72, 1\n" \
  "s_add_u32 s88, s88, s72\n" \
  "s_mul_i32 s72, s72, 24\n" \
  "s_add_u32 s89, s89, s72\n" \
  "s_cmp_lg_u32 s43, 0\n" \
  "s_cselect_b32 s90, s43, s90\n" \
  "LRT02_%=:\n" \
  "LM02_%=:\n" \
  "s_waitcnt vmcnt(11)\n" \
  "s_barrier\n" \
  "ds_read_b128 v[112:115], v5\n" \
  "ds_read_b128 v[116:119], v5 offset:512\n" \
  "s_waitcnt vmcnt(10)\n" \
  "s_barrier\n" \
  "ds_read_b128 v[120:123], v5 offset:1024\n" \
  "ds_read_b128 v[124:127], v5 offset:1536\n" \
  "s_waitcnt lgkmcnt(3)\n" \
  "v_smfmac_f32_16x16x64_bf16 v[176:179], v[112:115], a[0:7], v0\n" \
  "v_smfmac_f32_16x16x64_bf16 v[180:183], v[112:115], a[160:167], v0\n" \
  "s_waitcnt vmcnt(8)\n" \
  "s_waitcnt lgkmcnt(2)\n" \
  "v_smfmac_f32_16x16x64_bf16 v[176:179], v[116:119], a[8:15], v0\n" \
  "s_barrier\n" \
  "ds_read_b128 v[128:131], v5 offset:2048\n" \
  "v_smfmac_f32_16x16x64_bf16 v[180:183], v[116:119], a[168:175], v0\n" \
  "s_waitcnt lgkmcnt(2)\n" \
  "ds_read_b128 v[132:135], v5 offset:2560\n" \
  "v_smfmac_f32_16x16x64_bf16 v[176:179], v[120:123], a[16:23], v0\n" \
  "ds_read_b128 v[136:139], v5 offset:3072\n" \
  "v_smfmac_f32_16x16x64_bf16 v[180:183], v[120:123], a[176:183], v0\n" \
  "s_waitcnt vmcnt(6)\n" \
  "s_waitcnt lgkmcnt(3)\n" \
  "ds_read_b128 v[140:143], v5 offset:3584\n" \
  "v_smfmac_f32_16x16x64_bf16 v[176:179], v[124:127], a[24:31], v0\n" \
  "s_barrier\n" \
  "ds_read_b128 v[144:147], v5 offset:4096\n" \
  "v_smfmac_f32_16x16x64_bf16 v[180:183], v[124:127], a[184:191], v0\n" \
  "s_waitcnt lgkmcnt(4)\n" \
  "ds_read_b128 v[148:151], v5 offset:4608\n" \
  "v_smfmac_f32_16x16x64_bf16 v[176:179], v[128:131], a[32:39], v0\n" \
  "ds_read_b128 v[152:155], v5 offset:5120\n" \
  "v_smfmac_f32_16x16x64_bf16 v[180:183], v[128:131], a[192:199], v0\n" \
  "s_waitcnt vmcnt(4)\n" \
  "s_waitcnt lgkmcnt(5)\n" \
  "ds_read_b128 v[156:159], v5 offset:5632\n" \
  "v_smfmac_f32_16x16x64_bf16 v[176:179], v[132:135], a[40:47], v0\n" \
  "s_barrier\n" \
  "ds_read_b128 v[160:163], v5 offset:6144\n" \
  "v_smfmac_f32_16x16x64_bf16 v[180:183], v[132:135], a[200:207], v0\n" \
  "s_waitcnt lgkmcnt(6)\n" \
  "ds_read_b128 v[164:167], v5 offset:6656\n" \
  "v_smfmac_f32_16x16x64_bf16 v[176:179], v[136:139], a[48:55], v0\n" \
  "ds_read_b128 v[168:171], v5 offset:7168\n" \
  "v_smfmac_f32_16x16x64_bf16 v[180:183], v[136:139], a[208:215], v0\n" \
  "s_waitcnt vmcnt(2)\n" \
  "s_waitcnt lgkmcnt(7)\n" \
  "ds_read_b128 v[172:175], v5 offset:7680\n" \
  "v_smfmac_f32_16x16x64_bf16 v[176:179], v[140:143], a[56:63], v0\n" \
  "s_barrier\n" \
  "ds_read2_b32 v[112:113], v6 offset0:0 offset1:32\n" \
  "v_smfmac_f32_16x16x64_bf16 v[180:183], v[140:143], a[216:223], v0\n" \
  "s_waitcnt lgkmcnt(8)\n" \
  "ds_read2_b32 v[114:115], v6 offset0:64 offset1:96\n" \
  "v_smfmac_f32_16x16x64_bf16 v[176:179], v[144:147], a[64:71], v0\n" \
  "s_waitcnt lgkmcnt(8)\n" \
  "ds_read2_b32 v[116:117], v6 offset0:128 offset1:160\n" \
  "v_smfmac_f32_16x16x64_bf16 v[176:179], v[148:151], a[72:79], v0\n" \
  "s_waitcnt lgkmcnt(8)\n" \
  "ds_read2_b32 v[118:119], v6 offset0:192 offset1:224\n" \
  "v_smfmac_f32_16x16x64_bf16 v[176:179], v[152:155], a[80:87], v0\n" \
  "s_waitcnt lgkmcnt(8)\n" \
  "ds_read2_b32 v[120:121], v7 offset0:0 offset1:32\n" \
  "v_smfmac_f32_16x16x64_bf16 v[176:179], v[156:159], a[88:95], v0\n" \
  "s_waitcnt lgkmcnt(8)\n" \
  "ds_read2_b32 v[122:123], v7 offset0:64 offset1:96\n" \
  "v_smfmac_f32_16x16x64_bf16 v[176:179], v[160:163], a[96:103], v0\n" \
  "s_waitcnt lgkmcnt(8)\n" \
  "ds_read2_b32 v[124:125], v7 offset0:128 offset1:160\n" \
  "v_smfmac_f32_16x16x64_bf16 v[176:179], v[164:167], a[104:111], v0\n" \
  "s_waitcnt lgkmcnt(8)\n" \
  "ds_read2_b32 v[126:127], v7 offset0:192 offset1:224\n" \
  "v_smfmac_f32_16x16x64_bf16 v[176:179], v[168:171], a[112:119], v0\n" \
  "s_waitcnt lgkmcnt(8)\n" \
  "v_smfmac_f32_16x16x64_bf16 v[176:179], v[172:175], a[120:127], v0\n" \
  "s_waitcnt lgkmcnt(6)\n" \
  "v_smfmac_f32_16x16x64_bf16 v[176:179], v[112:115], a[128:135], v0\n" \
  "s_waitcnt lgkmcnt(4)\n" \
  "v_smfmac_f32_16x16x64_bf16 v[176:179], v[116:119], a[136:143], v0\n" \
  "s_waitcnt lgkmcnt(2)\n" \
  "v_smfmac_f32_16x16x64_bf16 v[176:179], v[120:123], a[144:151], v0\n" \
  "s_waitcnt lgkmcnt(0)\n" \
  "v_smfmac_f32_16x16x64_bf16 v[176:179], v[124:127], a[152:159], v0\n" \
  "v_smfmac_f32_16x16x64_bf16 v[180:183], v[144:147], a[224:231], v0\n" \
  "v_smfmac_f32_16x16x64_bf16 v[180:183], v[148:151], a[232:239], v0\n" \
  "v_cvt_pk_bf16_f32 v184, v176, v177\n" \
  "v_smfmac_f32_16x16x64_bf16 v[180:183], v[152:155], a[240:247], v0\n" \
  "v_cvt_pk_bf16_f32 v185, v178, v179\n" \
  "v_smfmac_f32_16x16x64_bf16 v[180:183], v[156:159], a[248:255], v0\n" \
  "ds_write_b64 v8, v[184:185]\n" \
  "v_add_f32 v186, v176, v177\n" \
  "v_smfmac_f32_16x16x64_bf16 v[180:183], v[160:163], v[192:199], v0\n" \
  "v_add_f32 v187, v178, v179\n" \
  "v_smfmac_f32_16x16x64_bf16 v[180:183], v[164:167], v[200:207], v0\n" \
  "v_cmp_u_f32 vcc, v186, v187\n" \
  "v_smfmac_f32_16x16x64_bf16 v[180:183], v[168:171], v[208:215], v0\n" \
  "v_smfmac_f32_16x16x64_bf16 v[180:183], v[172:175], v[216:223], v0\n" \
  "s_cbranch_vccnz LMF02_%=\n" \
  "LMOK02_%=:\n" \
  "v_smfmac_f32_16x16x64_bf16 v[180:183], v[112:115], v[224:231], v0\n" \
  "s_waitcnt lgkmcnt(0)\n" \
  "s_barrier\n" \
  "v_smfmac_f32_16x16x64_bf16 v[180:183], v[116:119], v[232:239], v0\n" \
  "v_smfmac_f32_16x16x64_bf16 v[180:183], v[120:123], v[240:247], v0\n" \
  "v_smfmac_f32_16x16x64_bf16 v[180:183], v[124:127], v[248:255], v0\n" \
  "s_nop 3\n" \
  "v_mov_b32 v31, v31\n" \
  "s_nop 2\n" \
  "v_cvt_pk_bf16_f32 v186, v180, v181\n" \
  "v_cvt_pk_bf16_f32 v187, v182, v183\n" \
  "ds_write_b64 v9, v[186:187]\n" \
  "s_waitcnt lgkmcnt(0)\n" \
  "s_barrier\n" \
  "s_nop 0\n" \
  "ds_read_b64_tr_b16 v[88:89], v12\n" \
  "s_mov_b32 s67, s66\n" \
  "s_cmp_eq_u32 s64, 64\n" \
  "s_cbranch_scc1 LKE02_%=\n" \
  "s_add_u32 s72, s50, s61\n" \
  "s_add_u32 s88, s88, s72\n" \
  "s_mul_i32 s91, s72, s90\n" \
  "s_lshr_b32 s91, s91, 8\n" \
  "s_add_u32 s89, s89, s91\n" \
  "s_waitcnt vmcnt(0)\n" \
  "v_cmp_gt_u32 vcc, 16, v35\n" \
  "s_cbranch_vccnz LFF02_%=\n" \
  "LFOK02_%=:\n" \
  "LB02_%=:\n" \
  "LWB02_%=:\n" \
  "s_memrealtime s[74:75]\n" \
  "s_memtime s[76:77]\n" \
  "s_waitcnt lgkmcnt(0)\n" \
  "s_sub_i32 s72, s88, s74\n" \
  "s_mul_i32 s72, s72, s90\n" \
  "s_ashr_i32 s72, s72, 8\n" \
  "s_sub_i32 s71, s89, s76\n" \
  "s_min_i32 s71, s71, s72\n" \
  "s_sub_i32 s72, s72, 92\n" \
  "s_max_i32 s71, s71, s72\n" \
  "s_add_u32 s89, s71, s76\n" \
  "s_cmp_gt_i32 s71, 168\n" \
  "s_cbranch_scc1 LWB02_%=\n" \
  "s_cmp_lt_i32 s71, s94\n" \
  "s_cbranch_scc1 LSB02_%=\n" \
  "s_max_i32 s71, s71, 48\n" \
  "s_lshr_b32 s71, s71, 3\n" \
  "s_lshl_b32 s71, s71, 2\n" \
  "s_sub_u32 s78, s84, s71\n" \
  "s_subb_u32 s79, s85, 0\n" \
  "s_setpc_b64 s[78:79]\n" \
  "s_nop 1\n" \
  "s_nop 1\n" \
  "s_nop 1\n" \
  "s_nop 1\n" \
  "s_nop 1\n" \
  "s_nop 1\n" \
  "s_nop 1\n" \
  "s_nop 1\n" \
  "s_nop 1\n" \
  "s_nop 1\n" \
  "s_nop 1\n" \
  "s_nop 1\n" \
  "s_nop 1\n" \
  "s_nop 1\n" \
  "s_nop 1\n" \
  "s_nop 1\n" \
  "LEB02_%=:\n" \
  "s_barrier\n" \
  "buffer_load_dwordx4 v[116:119], v15, s[36:39], s65 offen offset:128 sc1\n" \
  "s_barrier\n" \
  "buffer_load_dwordx4 v[112:115], v15, s[36:39], s65 offen sc1\n" \
  "s_barrier\n" \
  "buffer_load_dwordx4 v[120:123], v16, s[36:39], s65 offen sc1\n" \
  "s_barrier\n" \
  "buffer_load_dwordx4 v[124:127], v16, s[36:39], s65 offen offset:512 sc1\n" \
  "s_barrier\n" \
  "buffer_load_dwordx4 v[128:131], v16, s[36:39], s65 offen offset:1024 sc1\n" \
  "s_barrier\n" \
  "buffer_load_dwordx4 v[132:135], v16, s[36:39], s65 offen offset:1536 sc1\n" \
  "ds_read_b128 v[40:43], v19\n" \
  "ds_read_b128 v[44:47], v19 offset:512\n" \
  "ds_read_b128 v[48:51], v19 offset:1024\n" \
  "ds_read_b128 v[52:55], v19 offset:1536\n" \
  "s_mov_b64 exec, s[46:47]\n" \
  "ds_read_b128 v[56:59], v20\n" \
  "ds_read_b128 v[60:63], v20 offset:128\n" \
  "ds_read_b128 v[64:67], v20 offset:256\n" \
  "ds_read_b128 v[68:71], v20 offset:384\n" \
  "s_mov_b64 exec, -1\n" \
  "s_add_u32 s72, s51, s61\n" \
  "s_add_u32 s88, s88, s72\n" \
  "s_mul_i32 s91, s72, s90\n" \
  "s_lshr_b32 s91, s91, 8\n" \
  "s_add_u32 s89, s89, s91\n" \
  "v_readfirstlane_b32 s86, v34\n" \
  "v_readlane_b32 s43, v34, s53\n" \
  "s_add_u32 s71, s64, 1\n" \
  "s_min_u32 s71, s71, 63\n" \
  "s_lshr_b32 s72, s71, 4\n" \
  "s_lshl_b32 s72, s72, 13\n" \
  "s_lshl_b32 s71, s71, 9\n" \
  "s_add_u32 s71, s71, s72\n" \
  "s_mov_b32 s87, s71\n" \
  "s_lshl_b32 s40, s64, 7\n" \
  "s_mov_b32 m0, 0\n" \
  "s_nop 1\n" \
  "buffer_load_dwordx4 v[136:139], v17, s[36:39], s65 offen sc1\n" \
  "buffer_load_dwordx4 v[140:143], v18, s[36:39], s65 offen sc1\n" \
  "s_barrier\n" \
  "s_waitcnt lgkmcnt(0)\n" \
  "buffer_store_dwordx2 v[88:89], v14, s[36:39], s69 offen sc1\n" \
  "s_waitcnt lgkmcnt(0)\n" \
  "s_getreg_b32 s71, hwreg(HW_REG_IB_STS, 0, 4)\n" \
  "s_cmp_le_u32 s71, 7\n" \
  "v_mfma_f32_16x16x32_bf16 v[144:147], v[72:75], v[72:75], 0\n" \
  "s_cbranch_scc1 LHXS02_%=\n" \
  "s_getreg_b32 s71, hwreg(HW_REG_IB_STS, 0, 4)\n" \
  "s_cmp_le_u32 s71, 7\n" \
  "v_mfma_f32_16x16x32_bf16 v[144:147], v[72:75], v[72:75], 0\n" \
  "s_cbranch_scc1 LHXS02_%=\n" \
  "s_getreg_b32 s71, hwreg(HW_REG_IB_STS, 0, 4)\n" \
  "s_cmp_le_u32 s71, 7\n" \
  "v_mfma_f32_16x16x32_bf16 v[144:147], v[72:75], v[72:75], 0\n" \
  "s_cbranch_scc1 LHXS02_%=\n" \
  "s_getreg_b32 s71, hwreg(HW_REG_IB_STS, 0, 4)\n" \
  "s_cmp_le_u32 s71, 7\n" \
  "v_mfma_f32_16x16x32_bf16 v[144:147], v[72:75], v[72:75], 0\n" \
  "s_cbranch_scc1 LHXS02_%=\n" \
  "s_getreg_b32 s71, hwreg(HW_REG_IB_STS, 0, 4)\n" \
  "s_cmp_le_u32 s71, 7\n" \
  "v_mfma_f32_16x16x32_bf16 v[144:147], v[72:75], v[72:75], 0\n" \
  "s_cbranch_scc1 LHXS02_%=\n" \
  "s_getreg_b32 s71, hwreg(HW_REG_IB_STS, 0, 4)\n" \
  "s_cmp_le_u32 s71, 7\n" \
  "v_mfma_f32_16x16x32_bf16 v[144:147], v[72:75], v[72:75], 0\n" \
  "s_cbranch_scc1 LHXS02_%=\n" \
  "s_getreg_b32 s71, hwreg(HW_REG_IB_STS, 0, 4)\n" \
  "s_cmp_le_u32 s71, 7\n" \
  "v_mfma_f32_16x16x32_bf16 v[144:147], v[72:75], v[72:75], 0\n" \
  "s_cbranch_scc1 LHXS02_%=\n" \
  "s_getreg_b32 s71, hwreg(HW_REG_IB_STS, 0, 4)\n" \
  "s_cmp_le_u32 s71, 7\n" \
  "v_mfma_f32_16x16x32_bf16 v[144:147], v[72:75], v[72:75], 0\n" \
  "s_cbranch_scc1 LHXS02_%=\n" \
  "s_getreg_b32 s71, hwreg(HW_REG_IB_STS, 0, 4)\n" \
  "s_cmp_le_u32 s71, 7\n" \
  "v_mfma_f32_16x16x32_bf16 v[144:147], v[72:75], v[72:75], 0\n" \
  "s_cbranch_scc1 LHXS02_%=\n" \
  "s_getreg_b32 s71, hwreg(HW_REG_IB_STS, 0, 4)\n" \
  "s_cmp_le_u32 s71, 7\n" \
  "v_mfma_f32_16x16x32_bf16 v[144:147], v[72:75], v[72:75], 0\n" \
  "s_cbranch_scc1 LHXS02_%=\n" \
  "s_getreg_b32 s71, hwreg(HW_REG_IB_STS, 0, 4)\n" \
  "s_cmp_le_u32 s71, 7\n" \
  "v_mfma_f32_16x16x32_bf16 v[144:147], v[72:75], v[72:75], 0\n" \
  "s_cbranch_scc1 LHXS02_%=\n" \
  "s_getreg_b32 s71, hwreg(HW_REG_IB_STS, 0, 4)\n" \
  "s_cmp_le_u32 s71, 7\n" \
  "v_mfma_f32_16x16x32_bf16 v[144:147], v[72:75], v[72:75], 0\n" \
  "s_cbranch_scc1 LHXS02_%=\n" \
  "s_getreg_b32 s71, hwreg(HW_REG_IB_STS, 0, 4)\n" \
  "s_cmp_le_u32 s71, 7\n" \
  "v_mfma_f32_16x16x32_bf16 v[144:147], v[72:75], v[72:75], 0\n" \
  "s_cbranch_scc1 LHXS02_%=\n" \
  "s_getreg_b32 s71, hwreg(HW_REG_IB_STS, 0, 4)\n" \
  "s_cmp_le_u32 s71, 7\n" \
  "v_mfma_f32_16x16x32_bf16 v[144:147], v[72:75], v[72:75], 0\n" \
  "s_cbranch_scc1 LHXS02_%=\n" \
  "s_getreg_b32 s71, hwreg(HW_REG_IB_STS, 0, 4)\n" \
  "s_cmp_le_u32 s71, 7\n" \
  "v_mfma_f32_16x16x32_bf16 v[144:147], v[72:75], v[72:75], 0\n" \
  "s_cbranch_scc1 LHXS02_%=\n" \
  "s_getreg_b32 s71, hwreg(HW_REG_IB_STS, 0, 4)\n" \
  "s_cmp_le_u32 s71, 7\n" \
  "v_mfma_f32_16x16x32_bf16 v[144:147], v[72:75], v[72:75], 0\n" \
  "s_cbranch_scc1 LHXS02_%=\n" \
  "s_getreg_b32 s71, hwreg(HW_REG_IB_STS, 0, 4)\n" \
  "s_cmp_le_u32 s71, 7\n" \
  "v_mfma_f32_16x16x32_bf16 v[144:147], v[72:75], v[72:75], 0\n" \
  "s_cbranch_scc1 LHXS02_%=\n" \
  "s_getreg_b32 s71, hwreg(HW_REG_IB_STS, 0, 4)\n" \
  "s_cmp_le_u32 s71, 7\n" \
  "v_mfma_f32_16x16x32_bf16 v[144:147], v[72:75], v[72:75], 0\n" \
  "s_cbranch_scc1 LHXS02_%=\n" \
  "s_getreg_b32 s71, hwreg(HW_REG_IB_STS, 0, 4)\n" \
  "s_cmp_le_u32 s71, 7\n" \
  "v_mfma_f32_16x16x32_bf16 v[144:147], v[72:75], v[72:75], 0\n" \
  "s_cbranch_scc1 LHXS02_%=\n" \
  "s_getreg_b32 s71, hwreg(HW_REG_IB_STS, 0, 4)\n" \
  "s_cmp_le_u32 s71, 7\n" \
  "v_mfma_f32_16x16x32_bf16 v[144:147], v[72:75], v[72:75], 0\n" \
  "s_cbranch_scc1 LHXS02_%=\n" \
  "LHXS02_%=:\n" \
  "s_waitcnt vmcnt(7)\n" \
  "LS202_%=:\n" \
  "v_dot2_f32_bf16 v176, v112, s22, 0\n" \
  "v_dot2_f32_bf16 v177, v112, s23, 0\n" \
  "v_dot2_f32_bf16 v178, v113, s22, 0\n" \
  "v_dot2_f32_bf16 v179, v113, s23, 0\n" \
  "v_dot2_f32_bf16 v180, v114, s22, 0\n" \
  "v_dot2_f32_bf16 v181, v114, s23, 0\n" \
  "v_dot2_f32_bf16 v182, v115, s22, 0\n" \
  "v_dot2_f32_bf16 v183, v115, s23, 0\n" \
  "v_dot2_f32_bf16 v176, v116, s22, v176\n" \
  "v_dot2_f32_bf16 v177, v116, s23, v177\n" \
  "v_dot2_f32_bf16 v178, v117, s22, v178\n" \
  "v_dot2_f32_bf16 v179, v117, s23, v179\n" \
  "v_dot2_f32_bf16 v180, v118, s22, v180\n" \
  "v_dot2_f32_bf16 v181, v118, s23, v181\n" \
  "v_dot2_f32_bf16 v182, v119, s22, v182\n" \
  "v_dot2_f32_bf16 v183, v119, s23, v183\n" \
  "v_exp_f32 v176, v176\n" \
  "v_exp_f32 v177, v177\n" \
  "v_exp_f32 v178, v178\n" \
  "v_exp_f32 v179, v179\n" \
  "s_waitcnt vmcnt(6)\n" \
  "v_mfma_f32_16x16x32_bf16 v[160:163], v[56:59], v[120:123], 0\n" \
  "v_exp_f32 v180, v180\n" \
  "v_mfma_f32_16x16x32_bf16 v[144:147], v[40:43], v[120:123], 0\n" \
  "v_exp_f32 v181, v181\n" \
  "v_exp_f32 v182, v182\n" \
  "v_exp_f32 v183, v183\n" \
  "v_cvt_pk_bf16_f32 v184, v176, v177\n" \
  "v_cvt_pk_bf16_f32 v185, v178, v179\n" \
  "v_cvt_pk_bf16_f32 v186, v180, v181\n" \
  "v_cvt_pk_bf16_f32 v187, v182, v183\n" \
  "s_nop 1\n" \
  "v_mfma_f32_16x16x32_bf16 v[160:163], v[72:75], v[184:187], v[160:163]\n" \
  "v_mfma_f32_16x16x32_bf16 v[164:167], v[76:79], v[184:187], 0\n" \
  "s_waitcnt vmcnt(5)\n" \
  "v_mfma_f32_16x16x32_bf16 v[164:167], v[60:63], v[124:127], v[164:167]\n" \
  "v_rcp_f32 v188, v162\n" \
  "v_mfma_f32_16x16x32_bf16 v[148:151], v[44:47], v[124:127], 0\n" \
  "v_mul_f32 v144, v188, v144\n" \
  "v_mul_f32 v145, v188, v145\n" \
  "v_mfma_f32_16x16x32_bf16 v[168:171], v[80:83], v[184:187], 0\n" \
  "s_waitcnt vmcnt(4)\n" \
  "v_rcp_f32 v189, v166\n" \
  "v_mfma_f32_16x16x32_bf16 v[168:171], v[64:67], v[128:131], v[168:171]\n" \
  "v_mul_f32 v146, v188, v146\n" \
  "v_mul_f32 v147, v188, v147\n" \
  "v_mfma_f32_16x16x32_bf16 v[152:155], v[48:51], v[128:131], 0\n" \
  "v_mul_f32 v160, v188, v160\n" \
  "v_mul_f32 v161, v188, v161\n" \
  "v_mfma_f32_16x16x32_bf16 v[172:175], v[84:87], v[184:187], 0\n" \
  "s_waitcnt vmcnt(3)\n" \
  "v_rcp_f32 v190, v170\n" \
  "v_mfma_f32_16x16x32_bf16 v[172:175], v[68:71], v[132:135], v[172:175]\n" \
  "v_cndmask_b32 v167, v188, v189, s[28:29]\n" \
  "v_cndmask_b32 v167, v167, v190, s[26:27]\n" \
  "v_mfma_f32_16x16x32_bf16 v[156:159], v[52:55], v[132:135], 0\n" \
  "v_fmac_f32 v144, v189, v148\n" \
  "v_fmac_f32 v145, v189, v149\n" \
  "v_fmac_f32 v146, v189, v150\n" \
  "v_fmac_f32 v147, v189, v151\n" \
  "v_rcp_f32 v191, v174\n" \
  "v_fmac_f32 v160, v189, v164\n" \
  "v_cndmask_b32 v167, v167, v191, s[24:25]\n" \
  "v_pk_mul_f32 v[176:177], v[176:177], v[166:167] op_sel:[0,1] op_sel_hi:[1,1]\n" \
  "v_pk_mul_f32 v[178:179], v[178:179], v[166:167] op_sel:[0,1] op_sel_hi:[1,1]\n" \
  "v_pk_mul_f32 v[180:181], v[180:181], v[166:167] op_sel:[0,1] op_sel_hi:[1,1]\n" \
  "v_pk_mul_f32 v[182:183], v[182:183], v[166:167] op_sel:[0,1] op_sel_hi:[1,1]\n" \
  "v_cmp_nlt_f32 vcc, 0x0d800000, v167\n" \
  "v_cvt_pk_bf16_f32 v184, v176, v177\n" \
  "v_cvt_pk_bf16_f32 v185, v178, v179\n" \
  "v_cvt_pk_bf16_f32 v186, v180, v181\n" \
  "v_cvt_pk_bf16_f32 v187, v182, v183\n" \
  "v_fmac_f32 v161, v189, v165\n" \
  "v_pk_fma_f32 v[160:161], v[168:169], v[190:191], v[160:161] op_sel:[0,0,0] op_sel_hi:[1,0,1]\n" \
  "v_pk_fma_f32 v[160:161], v[172:173], v[190:191], v[160:161] op_sel:[0,1,0] op_sel_hi:[1,1,1]\n" \
  "v_pk_fma_f32 v[144:145], v[152:153], v[190:191], v[144:145] op_sel:[0,0,0] op_sel_hi:[1,0,1]\n" \
  "v_pk_fma_f32 v[146:147], v[154:155], v[190:191], v[146:147] op_sel:[0,0,0] op_sel_hi:[1,0,1]\n" \
  "v_pk_fma_f32 v[144:145], v[156:157], v[190:191], v[144:145] op_sel:[0,1,0] op_sel_hi:[1,1,1]\n" \
  "v_pk_fma_f32 v[146:147], v[158:159], v[190:191], v[146:147] op_sel:[0,1,0] op_sel_hi:[1,1,1]\n" \
  "s_waitcnt vmcnt(1)\n" \
  "v_mfma_f32_16x16x32_bf16 v[160:163], v[140:143], v[184:187], v[160:163]\n" \
  "s_mov_b64 s[80:81], vcc\n" \
  "v_mfma_f32_16x16x32_bf16 v[144:147], v[136:139], v[184:187], v[144:147]\n" \
  "s_mov_b64 exec, s[30:31]\n" \
  "s_nop 2\n" \
  "ds_write_b64 v22, v[160:161]\n" \
  "v_cmp_u_f32 s[78:79], v160, v161\n" \
  "s_nop 1\n" \
  "ds_write2_b64 v21, v[144:145], v[146:147] offset1:16\n" \
  "v_add_f32 v188, v144, v145\n" \
  "v_add_f32 v189, v146, v147\n" \
  "v_cmp_u_f32 vcc, v188, v189\n" \
  "s_or_b64 s[80:81], s[80:81], s[78:79]\n" \
  "s_or_b64 vcc, vcc, s[80:81]\n" \
  "s_cbranch_vccnz LSF02_%=\n" \
  "LSOK02_%=:\n" \
  "s_mov_b64 exec, -1\n" \
  "s_waitcnt lgkmcnt(0)\n" \
  "s_barrier\n" \
  "s_cmp_eq_u32 s73, 1\n" \
  "s_cbranch_scc1 LWA02_%=\n" \
  "s_branch LDONE_%=\n" \
  "ds_read_b32 v188, v33\n" \
  "s_waitcnt lgkmcnt(0)\n" \
  "v_cmp_ne_u32 vcc, 0, v188\n" \
  "s_cbranch_vccnz LSR02_%=\n" \
  "LNL02_%=:\n" \
  "s_waitcnt lgkmcnt(0)\n" \
  "s_cmp_eq_u32 s73, 1\n" \
  "s_cbranch_scc1 LOOP02_%=\n" \
  "s_branch LDONE_%=\n" \
  "LSA02_%=:\n" \
  "ds_read_b32 v188, v33\n" \
  "s_waitcnt lgkmcnt(0)\n" \
  "v_cmp_ne_u32 vcc, 0, v188\n" \
  "s_cbranch_vccnz LSR02_%=\n" \
  "s_add_u32 s88, s88, s52\n" \
  "s_add_u32 s89, s89, s93\n" \
  "s_add_u32 s97, s97, 1\n" \
  "s_branch LWA02_%=\n" \
  "LSB02_%=:\n" \
  "s_add_u32 s88, s88, s52\n" \
  "s_add_u32 s89, s89, s93\n" \
  "s_add_u32 s97, s97, 1\n" \
  "s_branch LWB02_%=\n" \
  "LMF02_%=:\n" \
  "v_mbcnt_lo_u32_b32 v130, -1, 0\n" \
  "v_mbcnt_hi_u32_b32 v130, -1, v130\n" \
  "v_lshlrev_b32 v130, 4, v130\n" \
  "v_mov_b32 v128, 0\n" \
  "ds_read_b128 v[132:135], v130 offset:0\n" \
  "ds_read_b128 v[136:139], v130 offset:1024\n" \
  "ds_read_b128 v[140:143], v130 offset:2048\n" \
  "ds_read_b128 v[144:147], v130 offset:3072\n" \
  "ds_read_b128 v[148:151], v130 offset:4096\n" \
  "s_waitcnt lgkmcnt(0)\n" \
  "v_max3_u32 v128, v128, v132, v133\n" \
  "v_max3_u32 v128, v128, v134, v135\n" \
  "v_max3_u32 v128, v128, v136, v137\n" \
  "v_max3_u32 v128, v128, v138, v139\n" \
  "v_max3_u32 v128, v128, v140, v141\n" \
  "v_max3_u32 v128, v128, v142, v143\n" \
  "v_max3_u32 v128, v128, v144, v145\n" \
  "v_max3_u32 v128, v128, v146, v147\n" \
  "v_max3_u32 v128, v128, v148, v149\n" \
  "v_max3_u32 v128, v128, v150, v151\n" \
  "ds_read_b128 v[132:135], v130 offset:5120\n" \
  "ds_read_b128 v[136:139], v130 offset:6144\n" \
  "ds_read_b128 v[140:143], v130 offset:7168\n" \
  "ds_read_b128 v[144:147], v130 offset:8192\n" \
  "ds_read_b128 v[148:151], v130 offset:9216\n" \
  "s_waitcnt lgkmcnt(0)\n" \
  "v_max3_u32 v128, v128, v132, v133\n" \
  "v_max3_u32 v128, v128, v134, v135\n" \
  "v_max3_u32 v128, v128, v136, v137\n" \
  "v_max3_u32 v128, v128, v138, v139\n" \
  "v_max3_u32 v128, v128, v140, v141\n" \
  "v_max3_u32 v128, v128, v142, v143\n" \
  "v_max3_u32 v128, v128, v144, v145\n" \
  "v_max3_u32 v128, v128, v146, v147\n" \
  "v_max3_u32 v128, v128, v148, v149\n" \
  "v_max3_u32 v128, v128, v150, v151\n" \
  "v_cmp_eq_u32 vcc, -1, v128\n" \
  "s_cbranch_vccz LMOK02_%=\n" \
  "s_cmp_gt_u32 s58, 4000\n" \
  "s_cbranch_scc1 LMOK02_%=\n" \
  "s_add_u32 s58, s58, 1\n" \
  "s_sub_u32 s64, s64, 1\n" \
  "s_mov_b32 s66, s65\n" \
  "s_add_u32 s88, s88, s52\n" \
  "s_add_u32 s89, s89, s93\n" \
  "s_add_u32 s97, s97, 1\n" \
  "s_waitcnt vmcnt(0) lgkmcnt(0)\n" \
  "s_barrier\n" \
  "s_branch LOOP02_%=\n" \
  "LKE02_%=:\n" \
  "s_waitcnt lgkmcnt(0)\n" \
  "buffer_store_dwordx2 v[88:89], v14, s[36:39], s69 offen sc1\n" \
  "s_branch LDONE_%=\n" \
  "LFF02_%=:\n" \
  "s_add_u32 s71, s64, 0\n" \
  "s_min_u32 s71, s71, 63\n" \
  "s_lshr_b32 s72, s71, 4\n" \
  "s_lshl_b32 s72, s72, 13\n" \
  "s_lshl_b32 s71, s71, 9\n" \
  "s_add_u32 s71, s71, s72\n" \
  "LOF02_%=:\n" \
  "buffer_load_dword v35, v28, s[36:39], s71 offen sc1\n" \
  "s_waitcnt vmcnt(0)\n" \
  "v_cmp_gt_u32 vcc, 16, v35\n" \
  "s_cbranch_vccz LOXF02_%=\n" \
  "s_add_u32 s60, s60, 1\n" \
  "s_cmp_gt_u32 s60, 20000\n" \
  "s_cbranch_scc1 LOXF02_%=\n" \
  "s_sleep 16\n" \
  "s_branch LOF02_%=\n" \
  "LOXF02_%=:\n" \
  "s_branch LFOK02_%=\n" \
  "LSF02_%=:\n" \
  "s_getpc_b64 s[78:79]\n" \
  "s_branch LSF_%=\n" \
  "s_branch LSOK02_%=\n" \
  "LSR02_%=:\n" \
  "v_mov_b32 v188, 0\n" \
  "s_add_u32 s59, s59, 1\n" \
  "s_barrier\n" \
  "ds_write_b32 v33, v188\n" \
  "s_sub_u32 s88, s88, s51\n" \
  "s_sub_u32 s88, s88, s61\n" \
  "s_sub_u32 s89, s89, s91\n" \
  "s_add_u32 s88, s88, s52\n" \
  "s_add_u32 s89, s89, s93\n" \
  "s_add_u32 s97, s97, 1\n" \
  "s_waitcnt lgkmcnt(0)\n" \
  "s_branch LB02_%=\n" \
  "LSTART03_%=:\n" \
  "LOOP03_%=:\n" \
  "v_readlane_b32 s70, v27, s64\n" \
  "s_mov_b32 s65, s66\n" \
  "s_xor_b32 s69, s66, 0x2000\n" \
  "s_add_u32 s64, s64, 1\n" \
  "v_mov_b32 v176, 0\n" \
  "v_mov_b32 v177, 0\n" \
  "v_mov_b32 v178, 0\n" \
  "v_mov_b32 v179, 0\n" \
  "v_mov_b32 v180, 0\n" \
  "v_mov_b32 v181, 0\n" \
  "v_mov_b32 v182, 0\n" \
  "v_mov_b32 v183, 0\n" \
  "v_readlane_b32 s66, v25, s64\n" \
  "v_readlane_b32 s68, v26, s64\n" \
  "s_add_u32 s91, s65, 81920\n" \
  "s_add_u32 s92, s65, 163840\n" \
  "s_add_u32 s93, s66, 81920\n" \
  "s_add_u32 s94, s66, 163840\n" \
  "s_mov_b32 m0, 36864\n" \
  "s_nop 0\n" \
  "s_barrier\n" \
  "buffer_load_dwordx4 v3, s[36:39], s70 offen sc1 lds\n" \
  "buffer_load_dwordx4 v3, s[36:39], s70 offen offset:1024 sc1 lds\n" \
  "buffer_load_dwordx4 v3, s[36:39], s70 offen offset:2048 sc1 lds\n" \
  "buffer_load_dwordx4 v3, s[36:39], s70 offen offset:3072 sc1 lds\n" \
  "LM03_%=:\n" \
  "s_barrier\n" \
  "ds_read_b128 v[112:115], v5\n" \
  "ds_read_b128 v[116:119], v5 offset:512\n" \
  "s_barrier\n" \
  "ds_read_b128 v[120:123], v5 offset:1024\n" \
  "ds_read_b128 v[124:127], v5 offset:1536\n" \
  "s_waitcnt lgkmcnt(3)\n" \
  "v_smfmac_f32_16x16x64_bf16 v[176:179], v[112:115], a[0:7], v0\n" \
  "v_smfmac_f32_16x16x64_bf16 v[180:183], v[112:115], a[160:167], v0\n" \
  "s_waitcnt lgkmcnt(2)\n" \
  "v_smfmac_f32_16x16x64_bf16 v[176:179], v[116:119], a[8:15], v0\n" \
  "s_barrier\n" \
  "ds_read_b128 v[128:131], v5 offset:2048\n" \
  "v_smfmac_f32_16x16x64_bf16 v[180:183], v[116:119], a[168:175], v0\n" \
  "s_waitcnt lgkmcnt(2)\n" \
  "ds_read_b128 v[132:135], v5 offset:2560\n" \
  "v_smfmac_f32_16x16x64_bf16 v[176:179], v[120:123], a[16:23], v0\n" \
  "ds_read_b128 v[136:139], v5 offset:3072\n" \
  "v_smfmac_f32_16x16x64_bf16 v[180:183], v[120:123], a[176:183], v0\n" \
  "s_waitcnt lgkmcnt(3)\n" \
  "ds_read_b128 v[140:143], v5 offset:3584\n" \
  "v_smfmac_f32_16x16x64_bf16 v[176:179], v[124:127], a[24:31], v0\n" \
  "s_barrier\n" \
  "ds_read_b128 v[144:147], v5 offset:4096\n" \
  "v_smfmac_f32_16x16x64_bf16 v[180:183], v[124:127], a[184:191], v0\n" \
  "s_waitcnt lgkmcnt(4)\n" \
  "ds_read_b128 v[148:151], v5 offset:4608\n" \
  "v_smfmac_f32_16x16x64_bf16 v[176:179], v[128:131], a[32:39], v0\n" \
  "ds_read_b128 v[152:155], v5 offset:5120\n" \
  "v_smfmac_f32_16x16x64_bf16 v[180:183], v[128:131], a[192:199], v0\n" \
  "s_waitcnt lgkmcnt(5)\n" \
  "ds_read_b128 v[156:159], v5 offset:5632\n" \
  "v_smfmac_f32_16x16x64_bf16 v[176:179], v[132:135], a[40:47], v0\n" \
  "s_barrier\n" \
  "ds_read_b128 v[160:163], v5 offset:6144\n" \
  "v_smfmac_f32_16x16x64_bf16 v[180:183], v[132:135], a[200:207], v0\n" \
  "s_waitcnt lgkmcnt(6)\n" \
  "ds_read_b128 v[164:167], v5 offset:6656\n" \
  "v_smfmac_f32_16x16x64_bf16 v[176:179], v[136:139], a[48:55], v0\n" \
  "ds_read_b128 v[168:171], v5 offset:7168\n" \
  "v_smfmac_f32_16x16x64_bf16 v[180:183], v[136:139], a[208:215], v0\n" \
  "s_waitcnt lgkmcnt(7)\n" \
  "ds_read_b128 v[172:175], v5 offset:7680\n" \
  "v_smfmac_f32_16x16x64_bf16 v[176:179], v[140:143], a[56:63], v0\n" \
  "s_barrier\n" \
  "ds_read2_b32 v[112:113], v6 offset0:0 offset1:32\n" \
  "v_smfmac_f32_16x16x64_bf16 v[180:183], v[140:143], a[216:223], v0\n" \
  "s_waitcnt lgkmcnt(8)\n" \
  "ds_read2_b32 v[114:115], v6 offset0:64 offset1:96\n" \
  "v_smfmac_f32_16x16x64_bf16 v[176:179], v[144:147], a[64:71], v0\n" \
  "s_waitcnt lgkmcnt(8)\n" \
  "ds_read2_b32 v[116:117], v6 offset0:128 offset1:160\n" \
  "v_smfmac_f32_16x16x64_bf16 v[176:179], v[148:151], a[72:79], v0\n" \
  "s_waitcnt lgkmcnt(8)\n" \
  "ds_read2_b32 v[118:119], v6 offset0:192 offset1:224\n" \
  "v_smfmac_f32_16x16x64_bf16 v[176:179], v[152:155], a[80:87], v0\n" \
  "s_waitcnt lgkmcnt(8)\n" \
  "ds_read2_b32 v[120:121], v7 offset0:0 offset1:32\n" \
  "v_smfmac_f32_16x16x64_bf16 v[176:179], v[156:159], a[88:95], v0\n" \
  "s_waitcnt lgkmcnt(8)\n" \
  "ds_read2_b32 v[122:123], v7 offset0:64 offset1:96\n" \
  "v_smfmac_f32_16x16x64_bf16 v[176:179], v[160:163], a[96:103], v0\n" \
  "s_waitcnt lgkmcnt(8)\n" \
  "ds_read2_b32 v[124:125], v7 offset0:128 offset1:160\n" \
  "v_smfmac_f32_16x16x64_bf16 v[176:179], v[164:167], a[104:111], v0\n" \
  "s_waitcnt lgkmcnt(8)\n" \
  "ds_read2_b32 v[126:127], v7 offset0:192 offset1:224\n" \
  "v_smfmac_f32_16x16x64_bf16 v[176:179], v[168:171], a[112:119], v0\n" \
  "s_waitcnt lgkmcnt(8)\n" \
  "v_smfmac_f32_16x16x64_bf16 v[176:179], v[172:175], a[120:127], v0\n" \
  "s_waitcnt lgkmcnt(6)\n" \
  "v_smfmac_f32_16x16x64_bf16 v[176:179], v[112:115], a[128:135], v0\n" \
  "s_waitcnt lgkmcnt(4)\n" \
  "v_smfmac_f32_16x16x64_bf16 v[176:179], v[116:119], a[136:143], v0\n" \
  "s_waitcnt lgkmcnt(2)\n" \
  "v_smfmac_f32_16x16x64_bf16 v[176:179], v[120:123], a[144:151], v0\n" \
  "s_waitcnt lgkmcnt(0)\n" \
  "v_smfmac_f32_16x16x64_bf16 v[176:179], v[124:127], a[152:159], v0\n" \
  "v_smfmac_f32_16x16x64_bf16 v[180:183], v[144:147], a[224:231], v0\n" \
  "v_smfmac_f32_16x16x64_bf16 v[180:183], v[148:151], a[232:239], v0\n" \
  "v_cvt_pk_bf16_f32 v184, v176, v177\n" \
  "v_smfmac_f32_16x16x64_bf16 v[180:183], v[152:155], a[240:247], v0\n" \
  "v_cvt_pk_bf16_f32 v185, v178, v179\n" \
  "v_smfmac_f32_16x16x64_bf16 v[180:183], v[156:159], a[248:255], v0\n" \
  "ds_write_b64 v8, v[184:185]\n" \
  "v_add_f32 v186, v176, v177\n" \
  "v_smfmac_f32_16x16x64_bf16 v[180:183], v[160:163], v[192:199], v0\n" \
  "v_add_f32 v187, v178, v179\n" \
  "v_smfmac_f32_16x16x64_bf16 v[180:183], v[164:167], v[200:207], v0\n" \
  "v_cmp_u_f32 vcc, v186, v187\n" \
  "v_smfmac_f32_16x16x64_bf16 v[180:183], v[168:171], v[208:215], v0\n" \
  "v_smfmac_f32_16x16x64_bf16 v[180:183], v[172:175], v[216:223], v0\n" \
  "s_cbranch_vccnz LMF03_%=\n" \
  "LMOK03_%=:\n" \
  "v_smfmac_f32_16x16x64_bf16 v[180:183], v[112:115], v[224:231], v0\n" \
  "s_waitcnt lgkmcnt(0)\n" \
  "s_barrier\n" \
  "v_smfmac_f32_16x16x64_bf16 v[180:183], v[116:119], v[232:239], v0\n" \
  "v_smfmac_f32_16x16x64_bf16 v[180:183], v[120:123], v[240:247], v0\n" \
  "v_smfmac_f32_16x16x64_bf16 v[180:183], v[124:127], v[248:255], v0\n" \
  "s_nop 3\n" \
  "v_mov_b32 v31, v31\n" \
  "s_nop 2\n" \
  "v_cvt_pk_bf16_f32 v186, v180, v181\n" \
  "v_cvt_pk_bf16_f32 v187, v182, v183\n" \
  "ds_write_b64 v9, v[186:187]\n" \
  "v_mov_b32 v31, v31\n" \
  "s_nop 2\n" \
  "v_mov_b32 v31, v31\n" \
  "s_nop 2\n" \
  "v_mov_b32 v31, v31\n" \
  "s_nop 2\n" \
  "s_waitcnt lgkmcnt(0)\n" \
  "s_barrier\n" \
  "ds_read_b64_tr_b16 v[144:145], v11\n" \
  "ds_read_b64_tr_b16 v[146:147], v11 offset:512\n" \
  "ds_read_b64_tr_b16 v[148:149], v11 offset:1024\n" \
  "ds_read_b64_tr_b16 v[150:151], v11 offset:1536\n" \
  "v_mov_b32 v31, v31\n" \
  "s_nop 2\n" \
  "v_mov_b32 v31, v31\n" \
  "s_nop 2\n" \
  "v_mov_b32 v31, v31\n" \
  "s_nop 0\n" \
  "s_waitcnt lgkmcnt(2)\n" \
  "v_mfma_f32_16x16x32_bf16 v[188:191], v[144:147], v[144:147], 0\n" \
  "s_waitcnt lgkmcnt(0)\n" \
  "v_mfma_f32_16x16x32_bf16 v[188:191], v[148:151], v[148:151], v[188:191]\n" \
  "s_nop 0\n" \
  "ds_read_b64_tr_b16 v[88:89], v12\n" \
  "s_nop 1\n" \
  "v_mov_b32 v31, v31\n" \
  "s_nop 2\n" \
  "v_pk_mul_f32 v[188:189], v[188:189], s[42:43]\n" \
  "v_pk_mul_f32 v[190:191], v[190:191], s[42:43]\n" \
  "v_cvt_pk_bf16_f32 v152, v188, v189\n" \
  "v_cvt_pk_bf16_f32 v153, v190, v191\n" \
  "buffer_store_dwordx2 v[152:153], v13, s[36:39], s65 offen sc1\n" \
  "buffer_store_dwordx2 v[152:153], v13, s[36:39], s91 offen sc1\n" \
  "buffer_store_dwordx2 v[152:153], v13, s[36:39], s92 offen sc1\n" \
  "s_mov_b32 s67, s66\n" \
  "s_cmp_eq_u32 s64, 64\n" \
  "s_cbranch_scc1 LKE03_%=\n" \
  "LB03_%=:\n" \
  "s_barrier\n" \
  "buffer_load_dwordx4 v[116:119], v15, s[36:39], s65 offen offset:128 sc1\n" \
  "s_barrier\n" \
  "buffer_load_dwordx4 v[112:115], v15, s[36:39], s65 offen sc1\n" \
  "s_barrier\n" \
  "buffer_load_dwordx4 v[120:123], v16, s[36:39], s65 offen sc1\n" \
  "s_barrier\n" \
  "buffer_load_dwordx4 v[124:127], v16, s[36:39], s65 offen offset:512 sc1\n" \
  "s_barrier\n" \
  "buffer_load_dwordx4 v[128:131], v16, s[36:39], s65 offen offset:1024 sc1\n" \
  "s_waitcnt vmcnt(8)\n" \
  "s_barrier\n" \
  "buffer_load_dwordx4 v[132:135], v16, s[36:39], s65 offen offset:1536 sc1\n" \
  "ds_read_b128 v[40:43], v19\n" \
  "ds_read_b128 v[44:47], v19 offset:512\n" \
  "ds_read_b128 v[48:51], v19 offset:1024\n" \
  "ds_read_b128 v[52:55], v19 offset:1536\n" \
  "s_mov_b64 exec, s[46:47]\n" \
  "ds_read_b128 v[56:59], v20\n" \
  "ds_read_b128 v[60:63], v20 offset:128\n" \
  "ds_read_b128 v[64:67], v20 offset:256\n" \
  "ds_read_b128 v[68:71], v20 offset:384\n" \
  "s_mov_b64 exec, -1\n" \
  "s_nop 15\n" \
  "s_nop 1\n" \
  "buffer_load_dwordx4 v[136:139], v17, s[36:39], s65 offen sc1\n" \
  "buffer_load_dwordx4 v[140:143], v18, s[36:39], s65 offen sc1\n" \
  "s_barrier\n" \
  "s_waitcnt lgkmcnt(0)\n" \
  "buffer_store_dwordx2 v[88:89], v14, s[36:39], s69 offen sc1\n" \
  "s_waitcnt lgkmcnt(0)\n" \
  "s_getreg_b32 s71, hwreg(HW_REG_IB_STS, 0, 4)\n" \
  "s_cmp_le_u32 s71, 7\n" \
  "v_mfma_f32_16x16x32_bf16 v[144:147], v[72:75], v[72:75], 0\n" \
  "s_cbranch_scc1 LHXS03_%=\n" \
  "s_getreg_b32 s71, hwreg(HW_REG_IB_STS, 0, 4)\n" \
  "s_cmp_le_u32 s71, 7\n" \
  "v_mfma_f32_16x16x32_bf16 v[144:147], v[72:75], v[72:75], 0\n" \
  "s_cbranch_scc1 LHXS03_%=\n" \
  "s_getreg_b32 s71, hwreg(HW_REG_IB_STS, 0, 4)\n" \
  "s_cmp_le_u32 s71, 7\n" \
  "v_mfma_f32_16x16x32_bf16 v[144:147], v[72:75], v[72:75], 0\n" \
  "s_cbranch_scc1 LHXS03_%=\n" \
  "s_getreg_b32 s71, hwreg(HW_REG_IB_STS, 0, 4)\n" \
  "s_cmp_le_u32 s71, 7\n" \
  "v_mfma_f32_16x16x32_bf16 v[144:147], v[72:75], v[72:75], 0\n" \
  "s_cbranch_scc1 LHXS03_%=\n" \
  "s_getreg_b32 s71, hwreg(HW_REG_IB_STS, 0, 4)\n" \
  "s_cmp_le_u32 s71, 7\n" \
  "v_mfma_f32_16x16x32_bf16 v[144:147], v[72:75], v[72:75], 0\n" \
  "s_cbranch_scc1 LHXS03_%=\n" \
  "s_getreg_b32 s71, hwreg(HW_REG_IB_STS, 0, 4)\n" \
  "s_cmp_le_u32 s71, 7\n" \
  "v_mfma_f32_16x16x32_bf16 v[144:147], v[72:75], v[72:75], 0\n" \
  "s_cbranch_scc1 LHXS03_%=\n" \
  "s_getreg_b32 s71, hwreg(HW_REG_IB_STS, 0, 4)\n" \
  "s_cmp_le_u32 s71, 7\n" \
  "v_mfma_f32_16x16x32_bf16 v[144:147], v[72:75], v[72:75], 0\n" \
  "s_cbranch_scc1 LHXS03_%=\n" \
  "s_getreg_b32 s71, hwreg(HW_REG_IB_STS, 0, 4)\n" \
  "s_cmp_le_u32 s71, 7\n" \
  "v_mfma_f32_16x16x32_bf16 v[144:147], v[72:75], v[72:75], 0\n" \
  "s_cbranch_scc1 LHXS03_%=\n" \
  "s_getreg_b32 s71, hwreg(HW_REG_IB_STS, 0, 4)\n" \
  "s_cmp_le_u32 s71, 7\n" \
  "v_mfma_f32_16x16x32_bf16 v[144:147], v[72:75], v[72:75], 0\n" \
  "s_cbranch_scc1 LHXS03_%=\n" \
  "s_getreg_b32 s71, hwreg(HW_REG_IB_STS, 0, 4)\n" \
  "s_cmp_le_u32 s71, 7\n" \
  "v_mfma_f32_16x16x32_bf16 v[144:147], v[72:75], v[72:75], 0\n" \
  "s_cbranch_scc1 LHXS03_%=\n" \
  "s_getreg_b32 s71, hwreg(HW_REG_IB_STS, 0, 4)\n" \
  "s_cmp_le_u32 s71, 7\n" \
  "v_mfma_f32_16x16x32_bf16 v[144:147], v[72:75], v[72:75], 0\n" \
  "s_cbranch_scc1 LHXS03_%=\n" \
  "s_getreg_b32 s71, hwreg(HW_REG_IB_STS, 0, 4)\n" \
  "s_cmp_le_u32 s71, 7\n" \
  "v_mfma_f32_16x16x32_bf16 v[144:147], v[72:75], v[72:75], 0\n" \
  "s_cbranch_scc1 LHXS03_%=\n" \
  "s_getreg_b32 s71, hwreg(HW_REG_IB_STS, 0, 4)\n" \
  "s_cmp_le_u32 s71, 7\n" \
  "v_mfma_f32_16x16x32_bf16 v[144:147], v[72:75], v[72:75], 0\n" \
  "s_cbranch_scc1 LHXS03_%=\n" \
  "s_getreg_b32 s71, hwreg(HW_REG_IB_STS, 0, 4)\n" \
  "s_cmp_le_u32 s71, 7\n" \
  "v_mfma_f32_16x16x32_bf16 v[144:147], v[72:75], v[72:75], 0\n" \
  "s_cbranch_scc1 LHXS03_%=\n" \
  "s_getreg_b32 s71, hwreg(HW_REG_IB_STS, 0, 4)\n" \
  "s_cmp_le_u32 s71, 7\n" \
  "v_mfma_f32_16x16x32_bf16 v[144:147], v[72:75], v[72:75], 0\n" \
  "s_cbranch_scc1 LHXS03_%=\n" \
  "s_getreg_b32 s71, hwreg(HW_REG_IB_STS, 0, 4)\n" \
  "s_cmp_le_u32 s71, 7\n" \
  "v_mfma_f32_16x16x32_bf16 v[144:147], v[72:75], v[72:75], 0\n" \
  "s_cbranch_scc1 LHXS03_%=\n" \
  "s_getreg_b32 s71, hwreg(HW_REG_IB_STS, 0, 4)\n" \
  "s_cmp_le_u32 s71, 7\n" \
  "v_mfma_f32_16x16x32_bf16 v[144:147], v[72:75], v[72:75], 0\n" \
  "s_cbranch_scc1 LHXS03_%=\n" \
  "s_getreg_b32 s71, hwreg(HW_REG_IB_STS, 0, 4)\n" \
  "s_cmp_le_u32 s71, 7\n" \
  "v_mfma_f32_16x16x32_bf16 v[144:147], v[72:75], v[72:75], 0\n" \
  "s_cbranch_scc1 LHXS03_%=\n" \
  "s_getreg_b32 s71, hwreg(HW_REG_IB_STS, 0, 4)\n" \
  "s_cmp_le_u32 s71, 7\n" \
  "v_mfma_f32_16x16x32_bf16 v[144:147], v[72:75], v[72:75], 0\n" \
  "s_cbranch_scc1 LHXS03_%=\n" \
  "s_getreg_b32 s71, hwreg(HW_REG_IB_STS, 0, 4)\n" \
  "s_cmp_le_u32 s71, 7\n" \
  "v_mfma_f32_16x16x32_bf16 v[144:147], v[72:75], v[72:75], 0\n" \
  "s_cbranch_scc1 LHXS03_%=\n" \
  "LHXS03_%=:\n" \
  "s_waitcnt vmcnt(7)\n" \
  "LS203_%=:\n" \
  "v_dot2_f32_bf16 v176, v112, s22, 0\n" \
  "v_dot2_f32_bf16 v177, v112, s23, 0\n" \
  "v_dot2_f32_bf16 v178, v113, s22, 0\n" \
  "v_dot2_f32_bf16 v179, v113, s23, 0\n" \
  "v_dot2_f32_bf16 v180, v114, s22, 0\n" \
  "v_dot2_f32_bf16 v181, v114, s23, 0\n" \
  "v_dot2_f32_bf16 v182, v115, s22, 0\n" \
  "v_dot2_f32_bf16 v183, v115, s23, 0\n" \
  "v_dot2_f32_bf16 v176, v116, s22, v176\n" \
  "v_dot2_f32_bf16 v177, v116, s23, v177\n" \
  "v_dot2_f32_bf16 v178, v117, s22, v178\n" \
  "v_dot2_f32_bf16 v179, v117, s23, v179\n" \
  "v_dot2_f32_bf16 v180, v118, s22, v180\n" \
  "v_dot2_f32_bf16 v181, v118, s23, v181\n" \
  "v_dot2_f32_bf16 v182, v119, s22, v182\n" \
  "v_dot2_f32_bf16 v183, v119, s23, v183\n" \
  "v_exp_f32 v176, v176\n" \
  "v_exp_f32 v177, v177\n" \
  "v_exp_f32 v178, v178\n" \
  "v_exp_f32 v179, v179\n" \
  "s_waitcnt vmcnt(6)\n" \
  "v_mfma_f32_16x16x32_bf16 v[160:163], v[56:59], v[120:123], 0\n" \
  "v_exp_f32 v180, v180\n" \
  "v_mfma_f32_16x16x32_bf16 v[144:147], v[40:43], v[120:123], 0\n" \
  "v_exp_f32 v181, v181\n" \
  "v_exp_f32 v182, v182\n" \
  "v_exp_f32 v183, v183\n" \
  "v_cvt_pk_bf16_f32 v184, v176, v177\n" \
  "v_cvt_pk_bf16_f32 v185, v178, v179\n" \
  "v_cvt_pk_bf16_f32 v186, v180, v181\n" \
  "v_cvt_pk_bf16_f32 v187, v182, v183\n" \
  "s_nop 1\n" \
  "v_mfma_f32_16x16x32_bf16 v[160:163], v[72:75], v[184:187], v[160:163]\n" \
  "v_mfma_f32_16x16x32_bf16 v[164:167], v[76:79], v[184:187], 0\n" \
  "s_waitcnt vmcnt(5)\n" \
  "v_mfma_f32_16x16x32_bf16 v[164:167], v[60:63], v[124:127], v[164:167]\n" \
  "v_rcp_f32 v188, v162\n" \
  "v_mfma_f32_16x16x32_bf16 v[148:151], v[44:47], v[124:127], 0\n" \
  "v_mul_f32 v144, v188, v144\n" \
  "v_mul_f32 v145, v188, v145\n" \
  "v_mfma_f32_16x16x32_bf16 v[168:171], v[80:83], v[184:187], 0\n" \
  "s_waitcnt vmcnt(4)\n" \
  "v_rcp_f32 v189, v166\n" \
  "v_mfma_f32_16x16x32_bf16 v[168:171], v[64:67], v[128:131], v[168:171]\n" \
  "v_mul_f32 v146, v188, v146\n" \
  "v_mul_f32 v147, v188, v147\n" \
  "v_mfma_f32_16x16x32_bf16 v[152:155], v[48:51], v[128:131], 0\n" \
  "v_mul_f32 v160, v188, v160\n" \
  "v_mul_f32 v161, v188, v161\n" \
  "v_mfma_f32_16x16x32_bf16 v[172:175], v[84:87], v[184:187], 0\n" \
  "s_waitcnt vmcnt(3)\n" \
  "v_rcp_f32 v190, v170\n" \
  "v_mfma_f32_16x16x32_bf16 v[172:175], v[68:71], v[132:135], v[172:175]\n" \
  "v_cndmask_b32 v167, v188, v189, s[28:29]\n" \
  "v_cndmask_b32 v167, v167, v190, s[26:27]\n" \
  "v_mfma_f32_16x16x32_bf16 v[156:159], v[52:55], v[132:135], 0\n" \
  "v_fmac_f32 v144, v189, v148\n" \
  "v_fmac_f32 v145, v189, v149\n" \
  "v_fmac_f32 v146, v189, v150\n" \
  "v_fmac_f32 v147, v189, v151\n" \
  "v_rcp_f32 v191, v174\n" \
  "v_fmac_f32 v160, v189, v164\n" \
  "v_cndmask_b32 v167, v167, v191, s[24:25]\n" \
  "v_pk_mul_f32 v[176:177], v[176:177], v[166:167] op_sel:[0,1] op_sel_hi:[1,1]\n" \
  "v_pk_mul_f32 v[178:179], v[178:179], v[166:167] op_sel:[0,1] op_sel_hi:[1,1]\n" \
  "v_pk_mul_f32 v[180:181], v[180:181], v[166:167] op_sel:[0,1] op_sel_hi:[1,1]\n" \
  "v_pk_mul_f32 v[182:183], v[182:183], v[166:167] op_sel:[0,1] op_sel_hi:[1,1]\n" \
  "v_cmp_nlt_f32 vcc, 0x0d800000, v167\n" \
  "v_cvt_pk_bf16_f32 v184, v176, v177\n" \
  "v_cvt_pk_bf16_f32 v185, v178, v179\n" \
  "v_cvt_pk_bf16_f32 v186, v180, v181\n" \
  "v_cvt_pk_bf16_f32 v187, v182, v183\n" \
  "v_fmac_f32 v161, v189, v165\n" \
  "v_pk_fma_f32 v[160:161], v[168:169], v[190:191], v[160:161] op_sel:[0,0,0] op_sel_hi:[1,0,1]\n" \
  "v_pk_fma_f32 v[160:161], v[172:173], v[190:191], v[160:161] op_sel:[0,1,0] op_sel_hi:[1,1,1]\n" \
  "v_pk_fma_f32 v[144:145], v[152:153], v[190:191], v[144:145] op_sel:[0,0,0] op_sel_hi:[1,0,1]\n" \
  "v_pk_fma_f32 v[146:147], v[154:155], v[190:191], v[146:147] op_sel:[0,0,0] op_sel_hi:[1,0,1]\n" \
  "v_pk_fma_f32 v[144:145], v[156:157], v[190:191], v[144:145] op_sel:[0,1,0] op_sel_hi:[1,1,1]\n" \
  "v_pk_fma_f32 v[146:147], v[158:159], v[190:191], v[146:147] op_sel:[0,1,0] op_sel_hi:[1,1,1]\n" \
  "s_waitcnt vmcnt(1)\n" \
  "v_mfma_f32_16x16x32_bf16 v[160:163], v[140:143], v[184:187], v[160:163]\n" \
  "s_mov_b64 s[80:81], vcc\n" \
  "v_mfma_f32_16x16x32_bf16 v[144:147], v[136:139], v[184:187], v[144:147]\n" \
  "s_mov_b64 exec, s[30:31]\n" \
  "s_nop 2\n" \
  "ds_write_b64 v22, v[160:161]\n" \
  "v_cmp_u_f32 s[78:79], v160, v161\n" \
  "s_nop 1\n" \
  "ds_write2_b64 v21, v[144:145], v[146:147] offset1:16\n" \
  "v_add_f32 v188, v144, v145\n" \
  "v_add_f32 v189, v146, v147\n" \
  "v_cmp_u_f32 vcc, v188, v189\n" \
  "s_or_b64 s[80:81], s[80:81], s[78:79]\n" \
  "s_or_b64 vcc, vcc, s[80:81]\n" \
  "s_cbranch_vccnz LSF03_%=\n" \
  "LSOK03_%=:\n" \
  "s_mov_b64 exec, -1\n" \
  "s_waitcnt lgkmcnt(0)\n" \
  "s_barrier\n" \
  "ds_read_b32 v188, v33\n" \
  "ds_read_b128 v[120:123], v23\n" \
  "ds_read_b128 v[124:127], v23 offset:768\n" \
  "ds_read_b128 v[128:131], v23 offset:1536\n" \
  "ds_read_b128 v[132:135], v23 offset:2304\n" \
  "s_waitcnt lgkmcnt(4)\n" \
  "v_cmp_ne_u32 vcc, 0, v188\n" \
  "s_waitcnt lgkmcnt(2)\n" \
  "v_pk_add_f32 v[120:121], v[120:121], v[122:123]\n" \
  "v_pk_add_f32 v[124:125], v[124:125], v[126:127]\n" \
  "v_pk_add_f32 v[120:121], v[120:121], v[124:125]\n" \
  "s_waitcnt lgkmcnt(1)\n" \
  "v_pk_add_f32 v[128:129], v[128:129], v[130:131]\n" \
  "v_pk_add_f32 v[120:121], v[120:121], v[128:129]\n" \
  "s_waitcnt lgkmcnt(0)\n" \
  "v_pk_add_f32 v[132:133], v[132:133], v[134:135]\n" \
  "v_pk_add_f32 v[120:121], v[120:121], v[132:133]\n" \
  "v_cvt_pk_bf16_f32 v120, v120, v121\n" \
  "s_cbranch_vccnz LSR03_%=\n" \
  "buffer_store_dword v120, v24, s[36:39], s66 offen sc1\n" \
  "buffer_store_dword v120, v24, s[36:39], s93 offen sc1\n" \
  "buffer_store_dword v120, v24, s[36:39], s94 offen sc1\n" \
  "LNL03_%=:\n" \
  "s_waitcnt lgkmcnt(0)\n" \
  "s_cmp_eq_u32 s73, 1\n" \
  "s_cbranch_scc1 LOOP03_%=\n" \
  "s_branch LDONE_%=\n" \
  "LMF03_%=:\n" \
  "v_mbcnt_lo_u32_b32 v130, -1, 0\n" \
  "v_mbcnt_hi_u32_b32 v130, -1, v130\n" \
  "v_lshlrev_b32 v130, 4, v130\n" \
  "v_mov_b32 v128, 0\n" \
  "ds_read_b128 v[132:135], v130 offset:0\n" \
  "ds_read_b128 v[136:139], v130 offset:1024\n" \
  "ds_read_b128 v[140:143], v130 offset:2048\n" \
  "ds_read_b128 v[144:147], v130 offset:3072\n" \
  "ds_read_b128 v[148:151], v130 offset:4096\n" \
  "s_waitcnt lgkmcnt(0)\n" \
  "v_max3_u32 v128, v128, v132, v133\n" \
  "v_max3_u32 v128, v128, v134, v135\n" \
  "v_max3_u32 v128, v128, v136, v137\n" \
  "v_max3_u32 v128, v128, v138, v139\n" \
  "v_max3_u32 v128, v128, v140, v141\n" \
  "v_max3_u32 v128, v128, v142, v143\n" \
  "v_max3_u32 v128, v128, v144, v145\n" \
  "v_max3_u32 v128, v128, v146, v147\n" \
  "v_max3_u32 v128, v128, v148, v149\n" \
  "v_max3_u32 v128, v128, v150, v151\n" \
  "ds_read_b128 v[132:135], v130 offset:5120\n" \
  "ds_read_b128 v[136:139], v130 offset:6144\n" \
  "ds_read_b128 v[140:143], v130 offset:7168\n" \
  "ds_read_b128 v[144:147], v130 offset:8192\n" \
  "ds_read_b128 v[148:151], v130 offset:9216\n" \
  "s_waitcnt lgkmcnt(0)\n" \
  "v_max3_u32 v128, v128, v132, v133\n" \
  "v_max3_u32 v128, v128, v134, v135\n" \
  "v_max3_u32 v128, v128, v136, v137\n" \
  "v_max3_u32 v128, v128, v138, v139\n" \
  "v_max3_u32 v128, v128, v140, v141\n" \
  "v_max3_u32 v128, v128, v142, v143\n" \
  "v_max3_u32 v128, v128, v144, v145\n" \
  "v_max3_u32 v128, v128, v146, v147\n" \
  "v_max3_u32 v128, v128, v148, v149\n" \
  "v_max3_u32 v128, v128, v150, v151\n" \
  "v_cmp_eq_u32 vcc, -1, v128\n" \
  "s_cbranch_vccz LMOK03_%=\n" \
  "s_cmp_gt_u32 s58, 4000\n" \
  "s_cbranch_scc1 LMOK03_%=\n" \
  "s_add_u32 s58, s58, 1\n" \
  "s_sub_u32 s64, s64, 1\n" \
  "s_mov_b32 s66, s65\n" \
  "s_waitcnt vmcnt(0) lgkmcnt(0)\n" \
  "s_barrier\n" \
  "s_branch LOOP03_%=\n" \
  "LKE03_%=:\n" \
  "s_waitcnt lgkmcnt(0)\n" \
  "buffer_store_dwordx2 v[88:89], v14, s[36:39], s69 offen sc1\n" \
  "s_branch LDONE_%=\n" \
  "LSF03_%=:\n" \
  "s_getpc_b64 s[78:79]\n" \
  "s_branch LSF_%=\n" \
  "s_branch LSOK03_%=\n" \
  "LSR03_%=:\n" \
  "v_mov_b32 v188, 0\n" \
  "s_add_u32 s59, s59, 1\n" \
  "s_barrier\n" \
  "ds_write_b32 v33, v188\n" \
  "s_waitcnt lgkmcnt(0)\n" \
  "s_branch LB03_%=\n" \
  "LSTART10_%=:\n" \
  "LOOP10_%=:\n" \
  "v_readlane_b32 s70, v27, s64\n" \
  "s_mov_b32 s65, s66\n" \
  "s_xor_b32 s69, s66, 0x2000\n" \
  "s_add_u32 s64, s64, 1\n" \
  "v_mov_b32 v176, 0\n" \
  "v_mov_b32 v177, 0\n" \
  "v_mov_b32 v178, 0\n" \
  "v_mov_b32 v179, 0\n" \
  "v_mov_b32 v180, 0\n" \
  "v_mov_b32 v181, 0\n" \
  "v_mov_b32 v182, 0\n" \
  "v_mov_b32 v183, 0\n" \
  "v_readlane_b32 s66, v25, s64\n" \
  "v_readlane_b32 s68, v26, s64\n" \
  "s_add_u32 s91, s65, 81920\n" \
  "s_add_u32 s92, s65, 163840\n" \
  "s_mov_b32 m0, 20480\n" \
  "s_add_u32 s72, s70, 4096\n" \
  "s_barrier\n" \
  "buffer_load_dwordx4 v3, s[36:39], s70 offen sc1 lds\n" \
  "buffer_load_dwordx4 v3, s[36:39], s70 offen offset:1024 sc1 lds\n" \
  "buffer_load_dwordx4 v3, s[36:39], s70 offen offset:2048 sc1 lds\n" \
  "buffer_load_dwordx4 v3, s[36:39], s70 offen offset:3072 sc1 lds\n" \
  "LM10_%=:\n" \
  "s_barrier\n" \
  "ds_read_b128 v[112:115], v5\n" \
  "ds_read_b128 v[116:119], v5 offset:512\n" \
  "s_barrier\n" \
  "ds_read_b128 v[120:123], v5 offset:1024\n" \
  "ds_read_b128 v[124:127], v5 offset:1536\n" \
  "s_waitcnt lgkmcnt(3)\n" \
  "v_smfmac_f32_16x16x64_bf16 v[176:179], v[112:115], a[0:7], v0\n" \
  "v_smfmac_f32_16x16x64_bf16 v[180:183], v[112:115], a[160:167], v0\n" \
  "s_waitcnt lgkmcnt(2)\n" \
  "v_smfmac_f32_16x16x64_bf16 v[176:179], v[116:119], a[8:15], v0\n" \
  "s_barrier\n" \
  "ds_read_b128 v[128:131], v5 offset:2048\n" \
  "v_smfmac_f32_16x16x64_bf16 v[180:183], v[116:119], a[168:175], v0\n" \
  "s_waitcnt lgkmcnt(2)\n" \
  "ds_read_b128 v[132:135], v5 offset:2560\n" \
  "v_smfmac_f32_16x16x64_bf16 v[176:179], v[120:123], a[16:23], v0\n" \
  "ds_read_b128 v[136:139], v5 offset:3072\n" \
  "v_smfmac_f32_16x16x64_bf16 v[180:183], v[120:123], a[176:183], v0\n" \
  "s_waitcnt lgkmcnt(3)\n" \
  "ds_read_b128 v[140:143], v5 offset:3584\n" \
  "v_smfmac_f32_16x16x64_bf16 v[176:179], v[124:127], a[24:31], v0\n" \
  "s_barrier\n" \
  "ds_read_b128 v[144:147], v5 offset:4096\n" \
  "v_smfmac_f32_16x16x64_bf16 v[180:183], v[124:127], a[184:191], v0\n" \
  "s_mov_b32 m0, 24576\n" \
  "s_waitcnt lgkmcnt(4)\n" \
  "ds_read_b128 v[148:151], v5 offset:4608\n" \
  "v_smfmac_f32_16x16x64_bf16 v[176:179], v[128:131], a[32:39], v0\n" \
  "ds_read_b128 v[152:155], v5 offset:5120\n" \
  "buffer_load_dwordx4 v3, s[36:39], s72 offen sc1 lds\n" \
  "v_smfmac_f32_16x16x64_bf16 v[180:183], v[128:131], a[192:199], v0\n" \
  "s_waitcnt lgkmcnt(5)\n" \
  "ds_read_b128 v[156:159], v5 offset:5632\n" \
  "v_smfmac_f32_16x16x64_bf16 v[176:179], v[132:135], a[40:47], v0\n" \
  "s_barrier\n" \
  "ds_read_b128 v[160:163], v5 offset:6144\n" \
  "buffer_load_dwordx4 v3, s[36:39], s72 offen offset:1024 sc1 lds\n" \
  "v_smfmac_f32_16x16x64_bf16 v[180:183], v[132:135], a[200:207], v0\n" \
  "s_waitcnt lgkmcnt(6)\n" \
  "ds_read_b128 v[164:167], v5 offset:6656\n" \
  "v_smfmac_f32_16x16x64_bf16 v[176:179], v[136:139], a[48:55], v0\n" \
  "ds_read_b128 v[168:171], v5 offset:7168\n" \
  "buffer_load_dwordx4 v3, s[36:39], s72 offen offset:2048 sc1 lds\n" \
  "v_smfmac_f32_16x16x64_bf16 v[180:183], v[136:139], a[208:215], v0\n" \
  "s_waitcnt lgkmcnt(7)\n" \
  "ds_read_b128 v[172:175], v5 offset:7680\n" \
  "v_smfmac_f32_16x16x64_bf16 v[176:179], v[140:143], a[56:63], v0\n" \
  "s_barrier\n" \
  "ds_read2_b32 v[112:113], v6 offset0:0 offset1:32\n" \
  "buffer_load_dwordx4 v3, s[36:39], s72 offen offset:3072 sc1 lds\n" \
  "v_smfmac_f32_16x16x64_bf16 v[180:183], v[140:143], a[216:223], v0\n" \
  "s_waitcnt lgkmcnt(8)\n" \
  "ds_read2_b32 v[114:115], v6 offset0:64 offset1:96\n" \
  "v_smfmac_f32_16x16x64_bf16 v[176:179], v[144:147], a[64:71], v0\n" \
  "s_waitcnt lgkmcnt(8)\n" \
  "ds_read2_b32 v[116:117], v6 offset0:128 offset1:160\n" \
  "v_smfmac_f32_16x16x64_bf16 v[176:179], v[148:151], a[72:79], v0\n" \
  "s_waitcnt lgkmcnt(8)\n" \
  "ds_read2_b32 v[118:119], v6 offset0:192 offset1:224\n" \
  "v_smfmac_f32_16x16x64_bf16 v[176:179], v[152:155], a[80:87], v0\n" \
  "s_waitcnt lgkmcnt(8)\n" \
  "ds_read2_b32 v[120:121], v7 offset0:0 offset1:32\n" \
  "v_smfmac_f32_16x16x64_bf16 v[176:179], v[156:159], a[88:95], v0\n" \
  "s_waitcnt lgkmcnt(8)\n" \
  "ds_read2_b32 v[122:123], v7 offset0:64 offset1:96\n" \
  "v_smfmac_f32_16x16x64_bf16 v[176:179], v[160:163], a[96:103], v0\n" \
  "s_waitcnt lgkmcnt(8)\n" \
  "ds_read2_b32 v[124:125], v7 offset0:128 offset1:160\n" \
  "v_smfmac_f32_16x16x64_bf16 v[176:179], v[164:167], a[104:111], v0\n" \
  "s_waitcnt lgkmcnt(8)\n" \
  "ds_read2_b32 v[126:127], v7 offset0:192 offset1:224\n" \
  "v_smfmac_f32_16x16x64_bf16 v[176:179], v[168:171], a[112:119], v0\n" \
  "s_waitcnt lgkmcnt(8)\n" \
  "v_smfmac_f32_16x16x64_bf16 v[176:179], v[172:175], a[120:127], v0\n" \
  "s_waitcnt lgkmcnt(6)\n" \
  "v_smfmac_f32_16x16x64_bf16 v[176:179], v[112:115], a[128:135], v0\n" \
  "s_waitcnt lgkmcnt(4)\n" \
  "v_smfmac_f32_16x16x64_bf16 v[176:179], v[116:119], a[136:143], v0\n" \
  "s_waitcnt lgkmcnt(2)\n" \
  "v_smfmac_f32_16x16x64_bf16 v[176:179], v[120:123], a[144:151], v0\n" \
  "s_waitcnt lgkmcnt(0)\n" \
  "v_smfmac_f32_16x16x64_bf16 v[176:179], v[124:127], a[152:159], v0\n" \
  "v_smfmac_f32_16x16x64_bf16 v[180:183], v[144:147], a[224:231], v0\n" \
  "v_smfmac_f32_16x16x64_bf16 v[180:183], v[148:151], a[232:239], v0\n" \
  "v_cvt_pk_bf16_f32 v184, v176, v177\n" \
  "v_smfmac_f32_16x16x64_bf16 v[180:183], v[152:155], a[240:247], v0\n" \
  "v_cvt_pk_bf16_f32 v185, v178, v179\n" \
  "v_smfmac_f32_16x16x64_bf16 v[180:183], v[156:159], a[248:255], v0\n" \
  "ds_write_b64 v8, v[184:185]\n" \
  "v_add_f32 v186, v176, v177\n" \
  "v_smfmac_f32_16x16x64_bf16 v[180:183], v[160:163], v[192:199], v0\n" \
  "v_add_f32 v187, v178, v179\n" \
  "v_smfmac_f32_16x16x64_bf16 v[180:183], v[164:167], v[200:207], v0\n" \
  "v_cmp_u_f32 vcc, v186, v187\n" \
  "v_smfmac_f32_16x16x64_bf16 v[180:183], v[168:171], v[208:215], v0\n" \
  "v_smfmac_f32_16x16x64_bf16 v[180:183], v[172:175], v[216:223], v0\n" \
  "s_cbranch_vccnz LMF10_%=\n" \
  "LMOK10_%=:\n" \
  "v_smfmac_f32_16x16x64_bf16 v[180:183], v[112:115], v[224:231], v0\n" \
  "s_waitcnt lgkmcnt(0)\n" \
  "s_barrier\n" \
  "ds_read_b64_tr_b16 v[128:129], v10\n" \
  "v_smfmac_f32_16x16x64_bf16 v[180:183], v[116:119], v[232:239], v0\n" \
  "ds_read_b64_tr_b16 v[130:131], v10 offset:1024\n" \
  "ds_read_b64_tr_b16 v[132:133], v10 offset:2048\n" \
  "v_smfmac_f32_16x16x64_bf16 v[180:183], v[120:123], v[240:247], v0\n" \
  "ds_read_b64_tr_b16 v[134:135], v10 offset:3072\n" \
  "v_smfmac_f32_16x16x64_bf16 v[180:183], v[124:127], v[248:255], v0\n" \
  "s_nop 3\n" \
  "v_mov_b32 v31, v31\n" \
  "s_nop 2\n" \
  "v_cvt_pk_bf16_f32 v186, v180, v181\n" \
  "v_cvt_pk_bf16_f32 v187, v182, v183\n" \
  "ds_write_b64 v9, v[186:187]\n" \
  "s_waitcnt lgkmcnt(3)\n" \
  "v_mfma_f32_16x16x32_bf16 v[188:191], v[128:131], v[128:131], 0\n" \
  "s_waitcnt lgkmcnt(1)\n" \
  "v_mfma_f32_16x16x32_bf16 v[188:191], v[132:135], v[132:135], v[188:191]\n" \
  "s_waitcnt lgkmcnt(0)\n" \
  "s_barrier\n" \
  "ds_read_b64_tr_b16 v[144:145], v11\n" \
  "ds_read_b64_tr_b16 v[146:147], v11 offset:1024\n" \
  "ds_read_b64_tr_b16 v[148:149], v11 offset:2048\n" \
  "ds_read_b64_tr_b16 v[150:151], v11 offset:3072\n" \
  "s_waitcnt lgkmcnt(2)\n" \
  "v_mfma_f32_16x16x32_bf16 v[188:191], v[144:147], v[144:147], v[188:191]\n" \
  "s_waitcnt lgkmcnt(0)\n" \
  "v_mfma_f32_16x16x32_bf16 v[188:191], v[148:151], v[148:151], v[188:191]\n" \
  "s_nop 0\n" \
  "ds_read_b64_tr_b16 v[88:89], v12\n" \
  "ds_read_b64_tr_b16 v[90:91], v12 offset:32\n" \
  "s_nop 1\n" \
  "v_mov_b32 v31, v31\n" \
  "s_nop 1\n" \
  "v_pk_mul_f32 v[188:189], v[188:189], s[42:43]\n" \
  "v_pk_mul_f32 v[190:191], v[190:191], s[42:43]\n" \
  "v_cvt_pk_bf16_f32 v152, v188, v189\n" \
  "v_cvt_pk_bf16_f32 v153, v190, v191\n" \
  "buffer_store_dwordx2 v[152:153], v13, s[36:39], s65 offen sc1\n" \
  "buffer_store_dwordx2 v[152:153], v13, s[36:39], s91 offen sc1\n" \
  "buffer_store_dwordx2 v[152:153], v13, s[36:39], s92 offen sc1\n" \
  "s_mov_b32 s67, s66\n" \
  "s_cmp_eq_u32 s64, 64\n" \
  "s_cbranch_scc1 LKE10_%=\n" \
  "LB10_%=:\n" \
  "s_barrier\n" \
  "buffer_load_dwordx4 v[116:119], v15, s[36:39], s65 offen offset:128 sc1\n" \
  "s_barrier\n" \
  "buffer_load_dwordx4 v[112:115], v15, s[36:39], s65 offen sc1\n" \
  "s_barrier\n" \
  "buffer_load_dwordx4 v[120:123], v16, s[36:39], s65 offen sc1\n" \
  "s_barrier\n" \
  "buffer_load_dwordx4 v[124:127], v16, s[36:39], s65 offen offset:512 sc1\n" \
  "s_barrier\n" \
  "buffer_load_dwordx4 v[128:131], v16, s[36:39], s65 offen offset:1024 sc1\n" \
  "s_waitcnt vmcnt(8)\n" \
  "s_barrier\n" \
  "buffer_load_dwordx4 v[132:135], v16, s[36:39], s65 offen offset:1536 sc1\n" \
  "ds_read_b128 v[40:43], v19\n" \
  "ds_read_b128 v[44:47], v19 offset:512\n" \
  "ds_read_b128 v[48:51], v19 offset:1024\n" \
  "ds_read_b128 v[52:55], v19 offset:1536\n" \
  "s_mov_b64 exec, s[46:47]\n" \
  "ds_read_b128 v[56:59], v20\n" \
  "ds_read_b128 v[60:63], v20 offset:128\n" \
  "ds_read_b128 v[64:67], v20 offset:256\n" \
  "ds_read_b128 v[68:71], v20 offset:384\n" \
  "s_mov_b64 exec, -1\n" \
  "s_nop 15\n" \
  "s_nop 1\n" \
  "buffer_load_dwordx4 v[136:139], v17, s[36:39], s65 offen sc1\n" \
  "buffer_load_dwordx4 v[140:143], v18, s[36:39], s65 offen sc1\n" \
  "s_barrier\n" \
  "s_waitcnt lgkmcnt(0)\n" \
  "buffer_store_dwordx4 v[88:91], v14, s[36:39], s69 offen sc1\n" \
  "s_waitcnt lgkmcnt(0)\n" \
  "s_getreg_b32 s71, hwreg(HW_REG_IB_STS, 0, 4)\n" \
  "s_cmp_le_u32 s71, 7\n" \
  "v_mfma_f32_16x16x32_bf16 v[144:147], v[72:75], v[72:75], 0\n" \
  "s_cbranch_scc1 LHXS10_%=\n" \
  "s_getreg_b32 s71, hwreg(HW_REG_IB_STS, 0, 4)\n" \
  "s_cmp_le_u32 s71, 7\n" \
  "v_mfma_f32_16x16x32_bf16 v[144:147], v[72:75], v[72:75], 0\n" \
  "s_cbranch_scc1 LHXS10_%=\n" \
  "s_getreg_b32 s71, hwreg(HW_REG_IB_STS, 0, 4)\n" \
  "s_cmp_le_u32 s71, 7\n" \
  "v_mfma_f32_16x16x32_bf16 v[144:147], v[72:75], v[72:75], 0\n" \
  "s_cbranch_scc1 LHXS10_%=\n" \
  "s_getreg_b32 s71, hwreg(HW_REG_IB_STS, 0, 4)\n" \
  "s_cmp_le_u32 s71, 7\n" \
  "v_mfma_f32_16x16x32_bf16 v[144:147], v[72:75], v[72:75], 0\n" \
  "s_cbranch_scc1 LHXS10_%=\n" \
  "s_getreg_b32 s71, hwreg(HW_REG_IB_STS, 0, 4)\n" \
  "s_cmp_le_u32 s71, 7\n" \
  "v_mfma_f32_16x16x32_bf16 v[144:147], v[72:75], v[72:75], 0\n" \
  "s_cbranch_scc1 LHXS10_%=\n" \
  "s_getreg_b32 s71, hwreg(HW_REG_IB_STS, 0, 4)\n" \
  "s_cmp_le_u32 s71, 7\n" \
  "v_mfma_f32_16x16x32_bf16 v[144:147], v[72:75], v[72:75], 0\n" \
  "s_cbranch_scc1 LHXS10_%=\n" \
  "s_getreg_b32 s71, hwreg(HW_REG_IB_STS, 0, 4)\n" \
  "s_cmp_le_u32 s71, 7\n" \
  "v_mfma_f32_16x16x32_bf16 v[144:147], v[72:75], v[72:75], 0\n" \
  "s_cbranch_scc1 LHXS10_%=\n" \
  "s_getreg_b32 s71, hwreg(HW_REG_IB_STS, 0, 4)\n" \
  "s_cmp_le_u32 s71, 7\n" \
  "v_mfma_f32_16x16x32_bf16 v[144:147], v[72:75], v[72:75], 0\n" \
  "s_cbranch_scc1 LHXS10_%=\n" \
  "s_getreg_b32 s71, hwreg(HW_REG_IB_STS, 0, 4)\n" \
  "s_cmp_le_u32 s71, 7\n" \
  "v_mfma_f32_16x16x32_bf16 v[144:147], v[72:75], v[72:75], 0\n" \
  "s_cbranch_scc1 LHXS10_%=\n" \
  "s_getreg_b32 s71, hwreg(HW_REG_IB_STS, 0, 4)\n" \
  "s_cmp_le_u32 s71, 7\n" \
  "v_mfma_f32_16x16x32_bf16 v[144:147], v[72:75], v[72:75], 0\n" \
  "s_cbranch_scc1 LHXS10_%=\n" \
  "s_getreg_b32 s71, hwreg(HW_REG_IB_STS, 0, 4)\n" \
  "s_cmp_le_u32 s71, 7\n" \
  "v_mfma_f32_16x16x32_bf16 v[144:147], v[72:75], v[72:75], 0\n" \
  "s_cbranch_scc1 LHXS10_%=\n" \
  "s_getreg_b32 s71, hwreg(HW_REG_IB_STS, 0, 4)\n" \
  "s_cmp_le_u32 s71, 7\n" \
  "v_mfma_f32_16x16x32_bf16 v[144:147], v[72:75], v[72:75], 0\n" \
  "s_cbranch_scc1 LHXS10_%=\n" \
  "s_getreg_b32 s71, hwreg(HW_REG_IB_STS, 0, 4)\n" \
  "s_cmp_le_u32 s71, 7\n" \
  "v_mfma_f32_16x16x32_bf16 v[144:147], v[72:75], v[72:75], 0\n" \
  "s_cbranch_scc1 LHXS10_%=\n" \
  "s_getreg_b32 s71, hwreg(HW_REG_IB_STS, 0, 4)\n" \
  "s_cmp_le_u32 s71, 7\n" \
  "v_mfma_f32_16x16x32_bf16 v[144:147], v[72:75], v[72:75], 0\n" \
  "s_cbranch_scc1 LHXS10_%=\n" \
  "s_getreg_b32 s71, hwreg(HW_REG_IB_STS, 0, 4)\n" \
  "s_cmp_le_u32 s71, 7\n" \
  "v_mfma_f32_16x16x32_bf16 v[144:147], v[72:75], v[72:75], 0\n" \
  "s_cbranch_scc1 LHXS10_%=\n" \
  "s_getreg_b32 s71, hwreg(HW_REG_IB_STS, 0, 4)\n" \
  "s_cmp_le_u32 s71, 7\n" \
  "v_mfma_f32_16x16x32_bf16 v[144:147], v[72:75], v[72:75], 0\n" \
  "s_cbranch_scc1 LHXS10_%=\n" \
  "s_getreg_b32 s71, hwreg(HW_REG_IB_STS, 0, 4)\n" \
  "s_cmp_le_u32 s71, 7\n" \
  "v_mfma_f32_16x16x32_bf16 v[144:147], v[72:75], v[72:75], 0\n" \
  "s_cbranch_scc1 LHXS10_%=\n" \
  "s_getreg_b32 s71, hwreg(HW_REG_IB_STS, 0, 4)\n" \
  "s_cmp_le_u32 s71, 7\n" \
  "v_mfma_f32_16x16x32_bf16 v[144:147], v[72:75], v[72:75], 0\n" \
  "s_cbranch_scc1 LHXS10_%=\n" \
  "s_getreg_b32 s71, hwreg(HW_REG_IB_STS, 0, 4)\n" \
  "s_cmp_le_u32 s71, 7\n" \
  "v_mfma_f32_16x16x32_bf16 v[144:147], v[72:75], v[72:75], 0\n" \
  "s_cbranch_scc1 LHXS10_%=\n" \
  "s_getreg_b32 s71, hwreg(HW_REG_IB_STS, 0, 4)\n" \
  "s_cmp_le_u32 s71, 7\n" \
  "v_mfma_f32_16x16x32_bf16 v[144:147], v[72:75], v[72:75], 0\n" \
  "s_cbranch_scc1 LHXS10_%=\n" \
  "LHXS10_%=:\n" \
  "s_waitcnt vmcnt(7)\n" \
  "LS210_%=:\n" \
  "v_dot2_f32_bf16 v176, v112, s22, 0\n" \
  "v_dot2_f32_bf16 v177, v112, s23, 0\n" \
  "v_dot2_f32_bf16 v178, v113, s22, 0\n" \
  "v_dot2_f32_bf16 v179, v113, s23, 0\n" \
  "v_dot2_f32_bf16 v180, v114, s22, 0\n" \
  "v_dot2_f32_bf16 v181, v114, s23, 0\n" \
  "v_dot2_f32_bf16 v182, v115, s22, 0\n" \
  "v_dot2_f32_bf16 v183, v115, s23, 0\n" \
  "v_dot2_f32_bf16 v176, v116, s22, v176\n" \
  "v_dot2_f32_bf16 v177, v116, s23, v177\n" \
  "v_dot2_f32_bf16 v178, v117, s22, v178\n" \
  "v_dot2_f32_bf16 v179, v117, s23, v179\n" \
  "v_dot2_f32_bf16 v180, v118, s22, v180\n" \
  "v_dot2_f32_bf16 v181, v118, s23, v181\n" \
  "v_dot2_f32_bf16 v182, v119, s22, v182\n" \
  "v_dot2_f32_bf16 v183, v119, s23, v183\n" \
  "v_exp_f32 v176, v176\n" \
  "v_exp_f32 v177, v177\n" \
  "v_exp_f32 v178, v178\n" \
  "v_exp_f32 v179, v179\n" \
  "s_waitcnt vmcnt(6)\n" \
  "v_mfma_f32_16x16x32_bf16 v[160:163], v[56:59], v[120:123], 0\n" \
  "v_exp_f32 v180, v180\n" \
  "v_mfma_f32_16x16x32_bf16 v[144:147], v[40:43], v[120:123], 0\n" \
  "v_exp_f32 v181, v181\n" \
  "v_exp_f32 v182, v182\n" \
  "v_exp_f32 v183, v183\n" \
  "v_cvt_pk_bf16_f32 v184, v176, v177\n" \
  "v_cvt_pk_bf16_f32 v185, v178, v179\n" \
  "v_cvt_pk_bf16_f32 v186, v180, v181\n" \
  "v_cvt_pk_bf16_f32 v187, v182, v183\n" \
  "s_nop 1\n" \
  "v_mfma_f32_16x16x32_bf16 v[160:163], v[72:75], v[184:187], v[160:163]\n" \
  "v_mfma_f32_16x16x32_bf16 v[164:167], v[76:79], v[184:187], 0\n" \
  "s_waitcnt vmcnt(5)\n" \
  "v_mfma_f32_16x16x32_bf16 v[164:167], v[60:63], v[124:127], v[164:167]\n" \
  "v_rcp_f32 v188, v162\n" \
  "v_mfma_f32_16x16x32_bf16 v[148:151], v[44:47], v[124:127], 0\n" \
  "v_mul_f32 v144, v188, v144\n" \
  "v_mul_f32 v145, v188, v145\n" \
  "v_mfma_f32_16x16x32_bf16 v[168:171], v[80:83], v[184:187], 0\n" \
  "s_waitcnt vmcnt(4)\n" \
  "v_rcp_f32 v189, v166\n" \
  "v_mfma_f32_16x16x32_bf16 v[168:171], v[64:67], v[128:131], v[168:171]\n" \
  "v_mul_f32 v146, v188, v146\n" \
  "v_mul_f32 v147, v188, v147\n" \
  "v_mfma_f32_16x16x32_bf16 v[152:155], v[48:51], v[128:131], 0\n" \
  "v_mul_f32 v160, v188, v160\n" \
  "v_mul_f32 v161, v188, v161\n" \
  "v_mfma_f32_16x16x32_bf16 v[172:175], v[84:87], v[184:187], 0\n" \
  "s_waitcnt vmcnt(3)\n" \
  "v_rcp_f32 v190, v170\n" \
  "v_mfma_f32_16x16x32_bf16 v[172:175], v[68:71], v[132:135], v[172:175]\n" \
  "v_cndmask_b32 v167, v188, v189, s[28:29]\n" \
  "v_cndmask_b32 v167, v167, v190, s[26:27]\n" \
  "v_mfma_f32_16x16x32_bf16 v[156:159], v[52:55], v[132:135], 0\n" \
  "v_fmac_f32 v144, v189, v148\n" \
  "v_fmac_f32 v145, v189, v149\n" \
  "v_fmac_f32 v146, v189, v150\n" \
  "v_fmac_f32 v147, v189, v151\n" \
  "v_rcp_f32 v191, v174\n" \
  "v_fmac_f32 v160, v189, v164\n" \
  "v_cndmask_b32 v167, v167, v191, s[24:25]\n" \
  "v_pk_mul_f32 v[176:177], v[176:177], v[166:167] op_sel:[0,1] op_sel_hi:[1,1]\n" \
  "v_pk_mul_f32 v[178:179], v[178:179], v[166:167] op_sel:[0,1] op_sel_hi:[1,1]\n" \
  "v_pk_mul_f32 v[180:181], v[180:181], v[166:167] op_sel:[0,1] op_sel_hi:[1,1]\n" \
  "v_pk_mul_f32 v[182:183], v[182:183], v[166:167] op_sel:[0,1] op_sel_hi:[1,1]\n" \
  "v_cmp_nlt_f32 vcc, 0x0d800000, v167\n" \
  "v_cvt_pk_bf16_f32 v184, v176, v177\n" \
  "v_cvt_pk_bf16_f32 v185, v178, v179\n" \
  "v_cvt_pk_bf16_f32 v186, v180, v181\n" \
  "v_cvt_pk_bf16_f32 v187, v182, v183\n" \
  "v_fmac_f32 v161, v189, v165\n" \
  "v_pk_fma_f32 v[160:161], v[168:169], v[190:191], v[160:161] op_sel:[0,0,0] op_sel_hi:[1,0,1]\n" \
  "v_pk_fma_f32 v[160:161], v[172:173], v[190:191], v[160:161] op_sel:[0,1,0] op_sel_hi:[1,1,1]\n" \
  "v_pk_fma_f32 v[144:145], v[152:153], v[190:191], v[144:145] op_sel:[0,0,0] op_sel_hi:[1,0,1]\n" \
  "v_pk_fma_f32 v[146:147], v[154:155], v[190:191], v[146:147] op_sel:[0,0,0] op_sel_hi:[1,0,1]\n" \
  "v_pk_fma_f32 v[144:145], v[156:157], v[190:191], v[144:145] op_sel:[0,1,0] op_sel_hi:[1,1,1]\n" \
  "v_pk_fma_f32 v[146:147], v[158:159], v[190:191], v[146:147] op_sel:[0,1,0] op_sel_hi:[1,1,1]\n" \
  "s_waitcnt vmcnt(1)\n" \
  "v_mfma_f32_16x16x32_bf16 v[160:163], v[140:143], v[184:187], v[160:163]\n" \
  "s_mov_b64 s[80:81], vcc\n" \
  "v_mfma_f32_16x16x32_bf16 v[144:147], v[136:139], v[184:187], v[144:147]\n" \
  "s_mov_b64 exec, s[30:31]\n" \
  "s_nop 2\n" \
  "ds_write_b64 v22, v[160:161]\n" \
  "v_cmp_u_f32 s[78:79], v160, v161\n" \
  "s_nop 1\n" \
  "ds_write2_b64 v21, v[144:145], v[146:147] offset1:16\n" \
  "v_add_f32 v188, v144, v145\n" \
  "v_add_f32 v189, v146, v147\n" \
  "v_cmp_u_f32 vcc, v188, v189\n" \
  "s_or_b64 s[80:81], s[80:81], s[78:79]\n" \
  "s_or_b64 vcc, vcc, s[80:81]\n" \
  "s_cbranch_vccnz LSF10_%=\n" \
  "LSOK10_%=:\n" \
  "s_mov_b64 exec, -1\n" \
  "s_waitcnt lgkmcnt(0)\n" \
  "s_barrier\n" \
  "ds_read_b32 v188, v33\n" \
  "s_waitcnt lgkmcnt(0)\n" \
  "v_cmp_ne_u32 vcc, 0, v188\n" \
  "s_cbranch_vccnz LSR10_%=\n" \
  "LNL10_%=:\n" \
  "s_waitcnt lgkmcnt(0)\n" \
  "s_cmp_eq_u32 s73, 1\n" \
  "s_cbranch_scc1 LOOP10_%=\n" \
  "s_branch LDONE_%=\n" \
  "LMF10_%=:\n" \
  "v_mbcnt_lo_u32_b32 v130, -1, 0\n" \
  "v_mbcnt_hi_u32_b32 v130, -1, v130\n" \
  "v_lshlrev_b32 v130, 4, v130\n" \
  "v_mov_b32 v128, 0\n" \
  "ds_read_b128 v[132:135], v130 offset:0\n" \
  "ds_read_b128 v[136:139], v130 offset:1024\n" \
  "ds_read_b128 v[140:143], v130 offset:2048\n" \
  "ds_read_b128 v[144:147], v130 offset:3072\n" \
  "ds_read_b128 v[148:151], v130 offset:4096\n" \
  "s_waitcnt lgkmcnt(0)\n" \
  "v_max3_u32 v128, v128, v132, v133\n" \
  "v_max3_u32 v128, v128, v134, v135\n" \
  "v_max3_u32 v128, v128, v136, v137\n" \
  "v_max3_u32 v128, v128, v138, v139\n" \
  "v_max3_u32 v128, v128, v140, v141\n" \
  "v_max3_u32 v128, v128, v142, v143\n" \
  "v_max3_u32 v128, v128, v144, v145\n" \
  "v_max3_u32 v128, v128, v146, v147\n" \
  "v_max3_u32 v128, v128, v148, v149\n" \
  "v_max3_u32 v128, v128, v150, v151\n" \
  "ds_read_b128 v[132:135], v130 offset:5120\n" \
  "ds_read_b128 v[136:139], v130 offset:6144\n" \
  "ds_read_b128 v[140:143], v130 offset:7168\n" \
  "ds_read_b128 v[144:147], v130 offset:8192\n" \
  "ds_read_b128 v[148:151], v130 offset:9216\n" \
  "s_waitcnt lgkmcnt(0)\n" \
  "v_max3_u32 v128, v128, v132, v133\n" \
  "v_max3_u32 v128, v128, v134, v135\n" \
  "v_max3_u32 v128, v128, v136, v137\n" \
  "v_max3_u32 v128, v128, v138, v139\n" \
  "v_max3_u32 v128, v128, v140, v141\n" \
  "v_max3_u32 v128, v128, v142, v143\n" \
  "v_max3_u32 v128, v128, v144, v145\n" \
  "v_max3_u32 v128, v128, v146, v147\n" \
  "v_max3_u32 v128, v128, v148, v149\n" \
  "v_max3_u32 v128, v128, v150, v151\n" \
  "v_cmp_eq_u32 vcc, -1, v128\n" \
  "s_cbranch_vccz LMOK10_%=\n" \
  "s_cmp_gt_u32 s58, 4000\n" \
  "s_cbranch_scc1 LMOK10_%=\n" \
  "s_add_u32 s58, s58, 1\n" \
  "s_sub_u32 s64, s64, 1\n" \
  "s_mov_b32 s66, s65\n" \
  "s_waitcnt vmcnt(0) lgkmcnt(0)\n" \
  "s_barrier\n" \
  "s_branch LOOP10_%=\n" \
  "LKE10_%=:\n" \
  "s_waitcnt lgkmcnt(0)\n" \
  "buffer_store_dwordx4 v[88:91], v14, s[36:39], s69 offen sc1\n" \
  "s_branch LDONE_%=\n" \
  "LSF10_%=:\n" \
  "s_getpc_b64 s[78:79]\n" \
  "s_branch LSF_%=\n" \
  "s_branch LSOK10_%=\n" \
  "LSR10_%=:\n" \
  "v_mov_b32 v188, 0\n" \
  "s_add_u32 s59, s59, 1\n" \
  "s_barrier\n" \
  "ds_write_b32 v33, v188\n" \
  "s_waitcnt lgkmcnt(0)\n" \
  "s_branch LB10_%=\n" \
  "LSTART11_%=:\n" \
  "LOOP11_%=:\n" \
  "v_readlane_b32 s70, v27, s64\n" \
  "s_mov_b32 s65, s66\n" \
  "s_xor_b32 s69, s66, 0x2000\n" \
  "s_add_u32 s64, s64, 1\n" \
  "v_mov_b32 v176, 0\n" \
  "v_mov_b32 v177, 0\n" \
  "v_mov_b32 v178, 0\n" \
  "v_mov_b32 v179, 0\n" \
  "v_mov_b32 v180, 0\n" \
  "v_mov_b32 v181, 0\n" \
  "v_mov_b32 v182, 0\n" \
  "v_mov_b32 v183, 0\n" \
  "v_readlane_b32 s66, v25, s64\n" \
  "v_readlane_b32 s68, v26, s64\n" \
  "s_mov_b32 m0, 28672\n" \
  "s_add_u32 s72, s70, 4096\n" \
  "s_barrier\n" \
  "buffer_load_dwordx4 v3, s[36:39], s70 offen sc1 lds\n" \
  "buffer_load_dwordx4 v3, s[36:39], s70 offen offset:1024 sc1 lds\n" \
  "buffer_load_dwordx4 v3, s[36:39], s70 offen offset:2048 sc1 lds\n" \
  "buffer_load_dwordx4 v3, s[36:39], s70 offen offset:3072 sc1 lds\n" \
  "LM11_%=:\n" \
  "s_barrier\n" \
  "ds_read_b128 v[112:115], v5\n" \
  "ds_read_b128 v[116:119], v5 offset:512\n" \
  "s_barrier\n" \
  "ds_read_b128 v[120:123], v5 offset:1024\n" \
  "ds_read_b128 v[124:127], v5 offset:1536\n" \
  "s_waitcnt lgkmcnt(3)\n" \
  "v_smfmac_f32_16x16x64_bf16 v[176:179], v[112:115], a[0:7], v0\n" \
  "v_smfmac_f32_16x16x64_bf16 v[180:183], v[112:115], a[160:167], v0\n" \
  "s_waitcnt lgkmcnt(2)\n" \
  "v_smfmac_f32_16x16x64_bf16 v[176:179], v[116:119], a[8:15], v0\n" \
  "s_barrier\n" \
  "ds_read_b128 v[128:131], v5 offset:2048\n" \
  "v_smfmac_f32_16x16x64_bf16 v[180:183], v[116:119], a[168:175], v0\n" \
  "s_waitcnt lgkmcnt(2)\n" \
  "ds_read_b128 v[132:135], v5 offset:2560\n" \
  "v_smfmac_f32_16x16x64_bf16 v[176:179], v[120:123], a[16:23], v0\n" \
  "ds_read_b128 v[136:139], v5 offset:3072\n" \
  "v_smfmac_f32_16x16x64_bf16 v[180:183], v[120:123], a[176:183], v0\n" \
  "s_waitcnt lgkmcnt(3)\n" \
  "ds_read_b128 v[140:143], v5 offset:3584\n" \
  "v_smfmac_f32_16x16x64_bf16 v[176:179], v[124:127], a[24:31], v0\n" \
  "s_barrier\n" \
  "ds_read_b128 v[144:147], v5 offset:4096\n" \
  "v_smfmac_f32_16x16x64_bf16 v[180:183], v[124:127], a[184:191], v0\n" \
  "s_mov_b32 m0, 32768\n" \
  "s_waitcnt lgkmcnt(4)\n" \
  "ds_read_b128 v[148:151], v5 offset:4608\n" \
  "v_smfmac_f32_16x16x64_bf16 v[176:179], v[128:131], a[32:39], v0\n" \
  "ds_read_b128 v[152:155], v5 offset:5120\n" \
  "v_smfmac_f32_16x16x64_bf16 v[180:183], v[128:131], a[192:199], v0\n" \
  "ds_read_b128 v[156:159], v5 offset:5632\n" \
  "buffer_load_dwordx4 v3, s[36:39], s72 offen sc1 lds\n" \
  "s_waitcnt lgkmcnt(6)\n" \
  "v_smfmac_f32_16x16x64_bf16 v[176:179], v[132:135], a[40:47], v0\n" \
  "s_barrier\n" \
  "ds_read_b128 v[160:163], v5 offset:6144\n" \
  "v_smfmac_f32_16x16x64_bf16 v[180:183], v[132:135], a[200:207], v0\n" \
  "ds_read_b128 v[164:167], v5 offset:6656\n" \
  "buffer_load_dwordx4 v3, s[36:39], s72 offen offset:1024 sc1 lds\n" \
  "s_waitcnt lgkmcnt(7)\n" \
  "v_smfmac_f32_16x16x64_bf16 v[176:179], v[136:139], a[48:55], v0\n" \
  "ds_read_b128 v[168:171], v5 offset:7168\n" \
  "v_smfmac_f32_16x16x64_bf16 v[180:183], v[136:139], a[208:215], v0\n" \
  "ds_read_b128 v[172:175], v5 offset:7680\n" \
  "buffer_load_dwordx4 v3, s[36:39], s72 offen offset:2048 sc1 lds\n" \
  "s_waitcnt lgkmcnt(8)\n" \
  "v_smfmac_f32_16x16x64_bf16 v[176:179], v[140:143], a[56:63], v0\n" \
  "s_barrier\n" \
  "ds_read2_b32 v[112:113], v6 offset0:0 offset1:32\n" \
  "v_smfmac_f32_16x16x64_bf16 v[180:183], v[140:143], a[216:223], v0\n" \
  "ds_read2_b32 v[114:115], v6 offset0:64 offset1:96\n" \
  "buffer_load_dwordx4 v3, s[36:39], s72 offen offset:3072 sc1 lds\n" \
  "s_waitcnt lgkmcnt(9)\n" \
  "v_smfmac_f32_16x16x64_bf16 v[176:179], v[144:147], a[64:71], v0\n" \
  "s_waitcnt lgkmcnt(8)\n" \
  "ds_read2_b32 v[116:117], v6 offset0:128 offset1:160\n" \
  "v_smfmac_f32_16x16x64_bf16 v[176:179], v[148:151], a[72:79], v0\n" \
  "s_waitcnt lgkmcnt(8)\n" \
  "ds_read2_b32 v[118:119], v6 offset0:192 offset1:224\n" \
  "v_smfmac_f32_16x16x64_bf16 v[176:179], v[152:155], a[80:87], v0\n" \
  "s_waitcnt lgkmcnt(8)\n" \
  "ds_read2_b32 v[120:121], v7 offset0:0 offset1:32\n" \
  "v_smfmac_f32_16x16x64_bf16 v[176:179], v[156:159], a[88:95], v0\n" \
  "s_waitcnt lgkmcnt(8)\n" \
  "ds_read2_b32 v[122:123], v7 offset0:64 offset1:96\n" \
  "v_smfmac_f32_16x16x64_bf16 v[176:179], v[160:163], a[96:103], v0\n" \
  "s_waitcnt lgkmcnt(8)\n" \
  "ds_read2_b32 v[124:125], v7 offset0:128 offset1:160\n" \
  "v_smfmac_f32_16x16x64_bf16 v[176:179], v[164:167], a[104:111], v0\n" \
  "s_waitcnt lgkmcnt(8)\n" \
  "ds_read2_b32 v[126:127], v7 offset0:192 offset1:224\n" \
  "v_smfmac_f32_16x16x64_bf16 v[176:179], v[168:171], a[112:119], v0\n" \
  "s_waitcnt lgkmcnt(8)\n" \
  "v_smfmac_f32_16x16x64_bf16 v[176:179], v[172:175], a[120:127], v0\n" \
  "s_waitcnt lgkmcnt(6)\n" \
  "v_smfmac_f32_16x16x64_bf16 v[176:179], v[112:115], a[128:135], v0\n" \
  "s_waitcnt lgkmcnt(4)\n" \
  "v_smfmac_f32_16x16x64_bf16 v[176:179], v[116:119], a[136:143], v0\n" \
  "s_waitcnt lgkmcnt(2)\n" \
  "v_smfmac_f32_16x16x64_bf16 v[176:179], v[120:123], a[144:151], v0\n" \
  "s_waitcnt lgkmcnt(0)\n" \
  "v_smfmac_f32_16x16x64_bf16 v[176:179], v[124:127], a[152:159], v0\n" \
  "v_smfmac_f32_16x16x64_bf16 v[180:183], v[144:147], a[224:231], v0\n" \
  "v_smfmac_f32_16x16x64_bf16 v[180:183], v[148:151], a[232:239], v0\n" \
  "v_cvt_pk_bf16_f32 v184, v176, v177\n" \
  "v_smfmac_f32_16x16x64_bf16 v[180:183], v[152:155], a[240:247], v0\n" \
  "v_cvt_pk_bf16_f32 v185, v178, v179\n" \
  "v_smfmac_f32_16x16x64_bf16 v[180:183], v[156:159], a[248:255], v0\n" \
  "ds_write_b64 v8, v[184:185]\n" \
  "v_add_f32 v186, v176, v177\n" \
  "v_smfmac_f32_16x16x64_bf16 v[180:183], v[160:163], v[192:199], v0\n" \
  "v_add_f32 v187, v178, v179\n" \
  "v_smfmac_f32_16x16x64_bf16 v[180:183], v[164:167], v[200:207], v0\n" \
  "v_cmp_u_f32 vcc, v186, v187\n" \
  "v_smfmac_f32_16x16x64_bf16 v[180:183], v[168:171], v[208:215], v0\n" \
  "v_smfmac_f32_16x16x64_bf16 v[180:183], v[172:175], v[216:223], v0\n" \
  "s_cbranch_vccnz LMF11_%=\n" \
  "LMOK11_%=:\n" \
  "v_smfmac_f32_16x16x64_bf16 v[180:183], v[112:115], v[224:231], v0\n" \
  "s_waitcnt lgkmcnt(0)\n" \
  "s_barrier\n" \
  "v_smfmac_f32_16x16x64_bf16 v[180:183], v[116:119], v[232:239], v0\n" \
  "v_smfmac_f32_16x16x64_bf16 v[180:183], v[120:123], v[240:247], v0\n" \
  "v_smfmac_f32_16x16x64_bf16 v[180:183], v[124:127], v[248:255], v0\n" \
  "s_nop 3\n" \
  "v_mov_b32 v31, v31\n" \
  "s_nop 2\n" \
  "v_cvt_pk_bf16_f32 v186, v180, v181\n" \
  "v_cvt_pk_bf16_f32 v187, v182, v183\n" \
  "ds_write_b64 v9, v[186:187]\n" \
  "s_waitcnt lgkmcnt(0)\n" \
  "s_barrier\n" \
  "s_nop 15\n" \
  "s_nop 0\n" \
  "ds_read_b64_tr_b16 v[88:89], v12\n" \
  "ds_read_b64_tr_b16 v[90:91], v12 offset:32\n" \
  "s_mov_b32 s67, s66\n" \
  "s_cmp_eq_u32 s64, 64\n" \
  "s_cbranch_scc1 LKE11_%=\n" \
  "LB11_%=:\n" \
  "s_barrier\n" \
  "buffer_load_dwordx4 v[116:119], v15, s[36:39], s65 offen offset:128 sc1\n" \
  "s_barrier\n" \
  "buffer_load_dwordx4 v[112:115], v15, s[36:39], s65 offen sc1\n" \
  "s_barrier\n" \
  "buffer_load_dwordx4 v[120:123], v16, s[36:39], s65 offen sc1\n" \
  "s_barrier\n" \
  "buffer_load_dwordx4 v[124:127], v16, s[36:39], s65 offen offset:512 sc1\n" \
  "s_barrier\n" \
  "buffer_load_dwordx4 v[128:131], v16, s[36:39], s65 offen offset:1024 sc1\n" \
  "s_waitcnt vmcnt(5)\n" \
  "s_barrier\n" \
  "buffer_load_dwordx4 v[132:135], v16, s[36:39], s65 offen offset:1536 sc1\n" \
  "ds_read_b128 v[40:43], v19\n" \
  "ds_read_b128 v[44:47], v19 offset:512\n" \
  "ds_read_b128 v[48:51], v19 offset:1024\n" \
  "ds_read_b128 v[52:55], v19 offset:1536\n" \
  "s_mov_b64 exec, s[46:47]\n" \
  "ds_read_b128 v[56:59], v20\n" \
  "ds_read_b128 v[60:63], v20 offset:128\n" \
  "ds_read_b128 v[64:67], v20 offset:256\n" \
  "ds_read_b128 v[68:71], v20 offset:384\n" \
  "s_mov_b64 exec, -1\n" \
  "s_nop 15\n" \
  "s_nop 1\n" \
  "buffer_load_dwordx4 v[136:139], v17, s[36:39], s65 offen sc1\n" \
  "buffer_load_dwordx4 v[140:143], v18, s[36:39], s65 offen sc1\n" \
  "s_barrier\n" \
  "s_waitcnt lgkmcnt(0)\n" \
  "buffer_store_dwordx4 v[88:91], v14, s[36:39], s69 offen sc1\n" \
  "s_waitcnt lgkmcnt(0)\n" \
  "s_getreg_b32 s71, hwreg(HW_REG_IB_STS, 0, 4)\n" \
  "s_cmp_le_u32 s71, 7\n" \
  "v_mfma_f32_16x16x32_bf16 v[144:147], v[72:75], v[72:75], 0\n" \
  "s_cbranch_scc1 LHXS11_%=\n" \
  "s_getreg_b32 s71, hwreg(HW_REG_IB_STS, 0, 4)\n" \
  "s_cmp_le_u32 s71, 7\n" \
  "v_mfma_f32_16x16x32_bf16 v[144:147], v[72:75], v[72:75], 0\n" \
  "s_cbranch_scc1 LHXS11_%=\n" \
  "s_getreg_b32 s71, hwreg(HW_REG_IB_STS, 0, 4)\n" \
  "s_cmp_le_u32 s71, 7\n" \
  "v_mfma_f32_16x16x32_bf16 v[144:147], v[72:75], v[72:75], 0\n" \
  "s_cbranch_scc1 LHXS11_%=\n" \
  "s_getreg_b32 s71, hwreg(HW_REG_IB_STS, 0, 4)\n" \
  "s_cmp_le_u32 s71, 7\n" \
  "v_mfma_f32_16x16x32_bf16 v[144:147], v[72:75], v[72:75], 0\n" \
  "s_cbranch_scc1 LHXS11_%=\n" \
  "s_getreg_b32 s71, hwreg(HW_REG_IB_STS, 0, 4)\n" \
  "s_cmp_le_u32 s71, 7\n" \
  "v_mfma_f32_16x16x32_bf16 v[144:147], v[72:75], v[72:75], 0\n" \
  "s_cbranch_scc1 LHXS11_%=\n" \
  "s_getreg_b32 s71, hwreg(HW_REG_IB_STS, 0, 4)\n" \
  "s_cmp_le_u32 s71, 7\n" \
  "v_mfma_f32_16x16x32_bf16 v[144:147], v[72:75], v[72:75], 0\n" \
  "s_cbranch_scc1 LHXS11_%=\n" \
  "s_getreg_b32 s71, hwreg(HW_REG_IB_STS, 0, 4)\n" \
  "s_cmp_le_u32 s71, 7\n" \
  "v_mfma_f32_16x16x32_bf16 v[144:147], v[72:75], v[72:75], 0\n" \
  "s_cbranch_scc1 LHXS11_%=\n" \
  "s_getreg_b32 s71, hwreg(HW_REG_IB_STS, 0, 4)\n" \
  "s_cmp_le_u32 s71, 7\n" \
  "v_mfma_f32_16x16x32_bf16 v[144:147], v[72:75], v[72:75], 0\n" \
  "s_cbranch_scc1 LHXS11_%=\n" \
  "s_getreg_b32 s71, hwreg(HW_REG_IB_STS, 0, 4)\n" \
  "s_cmp_le_u32 s71, 7\n" \
  "v_mfma_f32_16x16x32_bf16 v[144:147], v[72:75], v[72:75], 0\n" \
  "s_cbranch_scc1 LHXS11_%=\n" \
  "s_getreg_b32 s71, hwreg(HW_REG_IB_STS, 0, 4)\n" \
  "s_cmp_le_u32 s71, 7\n" \
  "v_mfma_f32_16x16x32_bf16 v[144:147], v[72:75], v[72:75], 0\n" \
  "s_cbranch_scc1 LHXS11_%=\n" \
  "s_getreg_b32 s71, hwreg(HW_REG_IB_STS, 0, 4)\n" \
  "s_cmp_le_u32 s71, 7\n" \
  "v_mfma_f32_16x16x32_bf16 v[144:147], v[72:75], v[72:75], 0\n" \
  "s_cbranch_scc1 LHXS11_%=\n" \
  "s_getreg_b32 s71, hwreg(HW_REG_IB_STS, 0, 4)\n" \
  "s_cmp_le_u32 s71, 7\n" \
  "v_mfma_f32_16x16x32_bf16 v[144:147], v[72:75], v[72:75], 0\n" \
  "s_cbranch_scc1 LHXS11_%=\n" \
  "s_getreg_b32 s71, hwreg(HW_REG_IB_STS, 0, 4)\n" \
  "s_cmp_le_u32 s71, 7\n" \
  "v_mfma_f32_16x16x32_bf16 v[144:147], v[72:75], v[72:75], 0\n" \
  "s_cbranch_scc1 LHXS11_%=\n" \
  "s_getreg_b32 s71, hwreg(HW_REG_IB_STS, 0, 4)\n" \
  "s_cmp_le_u32 s71, 7\n" \
  "v_mfma_f32_16x16x32_bf16 v[144:147], v[72:75], v[72:75], 0\n" \
  "s_cbranch_scc1 LHXS11_%=\n" \
  "s_getreg_b32 s71, hwreg(HW_REG_IB_STS, 0, 4)\n" \
  "s_cmp_le_u32 s71, 7\n" \
  "v_mfma_f32_16x16x32_bf16 v[144:147], v[72:75], v[72:75], 0\n" \
  "s_cbranch_scc1 LHXS11_%=\n" \
  "s_getreg_b32 s71, hwreg(HW_REG_IB_STS, 0, 4)\n" \
  "s_cmp_le_u32 s71, 7\n" \
  "v_mfma_f32_16x16x32_bf16 v[144:147], v[72:75], v[72:75], 0\n" \
  "s_cbranch_scc1 LHXS11_%=\n" \
  "s_getreg_b32 s71, hwreg(HW_REG_IB_STS, 0, 4)\n" \
  "s_cmp_le_u32 s71, 7\n" \
  "v_mfma_f32_16x16x32_bf16 v[144:147], v[72:75], v[72:75], 0\n" \
  "s_cbranch_scc1 LHXS11_%=\n" \
  "s_getreg_b32 s71, hwreg(HW_REG_IB_STS, 0, 4)\n" \
  "s_cmp_le_u32 s71, 7\n" \
  "v_mfma_f32_16x16x32_bf16 v[144:147], v[72:75], v[72:75], 0\n" \
  "s_cbranch_scc1 LHXS11_%=\n" \
  "s_getreg_b32 s71, hwreg(HW_REG_IB_STS, 0, 4)\n" \
  "s_cmp_le_u32 s71, 7\n" \
  "v_mfma_f32_16x16x32_bf16 v[144:147], v[72:75], v[72:75], 0\n" \
  "s_cbranch_scc1 LHXS11_%=\n" \
  "s_getreg_b32 s71, hwreg(HW_REG_IB_STS, 0, 4)\n" \
  "s_cmp_le_u32 s71, 7\n" \
  "v_mfma_f32_16x16x32_bf16 v[144:147], v[72:75], v[72:75], 0\n" \
  "s_cbranch_scc1 LHXS11_%=\n" \
  "LHXS11_%=:\n" \
  "s_waitcnt vmcnt(7)\n" \
  "LS211_%=:\n" \
  "v_dot2_f32_bf16 v176, v112, s22, 0\n" \
  "v_dot2_f32_bf16 v177, v112, s23, 0\n" \
  "v_dot2_f32_bf16 v178, v113, s22, 0\n" \
  "v_dot2_f32_bf16 v179, v113, s23, 0\n" \
  "v_dot2_f32_bf16 v180, v114, s22, 0\n" \
  "v_dot2_f32_bf16 v181, v114, s23, 0\n" \
  "v_dot2_f32_bf16 v182, v115, s22, 0\n" \
  "v_dot2_f32_bf16 v183, v115, s23, 0\n" \
  "v_dot2_f32_bf16 v176, v116, s22, v176\n" \
  "v_dot2_f32_bf16 v177, v116, s23, v177\n" \
  "v_dot2_f32_bf16 v178, v117, s22, v178\n" \
  "v_dot2_f32_bf16 v179, v117, s23, v179\n" \
  "v_dot2_f32_bf16 v180, v118, s22, v180\n" \
  "v_dot2_f32_bf16 v181, v118, s23, v181\n" \
  "v_dot2_f32_bf16 v182, v119, s22, v182\n" \
  "v_dot2_f32_bf16 v183, v119, s23, v183\n" \
  "v_exp_f32 v176, v176\n" \
  "v_exp_f32 v177, v177\n" \
  "v_exp_f32 v178, v178\n" \
  "v_exp_f32 v179, v179\n" \
  "s_waitcnt vmcnt(6)\n" \
  "v_mfma_f32_16x16x32_bf16 v[160:163], v[56:59], v[120:123], 0\n" \
  "v_exp_f32 v180, v180\n" \
  "v_mfma_f32_16x16x32_bf16 v[144:147], v[40:43], v[120:123], 0\n" \
  "v_exp_f32 v181, v181\n" \
  "v_exp_f32 v182, v182\n" \
  "v_exp_f32 v183, v183\n" \
  "v_cvt_pk_bf16_f32 v184, v176, v177\n" \
  "v_cvt_pk_bf16_f32 v185, v178, v179\n" \
  "v_cvt_pk_bf16_f32 v186, v180, v181\n" \
  "v_cvt_pk_bf16_f32 v187, v182, v183\n" \
  "s_nop 1\n" \
  "v_mfma_f32_16x16x32_bf16 v[160:163], v[72:75], v[184:187], v[160:163]\n" \
  "v_mfma_f32_16x16x32_bf16 v[164:167], v[76:79], v[184:187], 0\n" \
  "s_waitcnt vmcnt(5)\n" \
  "v_mfma_f32_16x16x32_bf16 v[164:167], v[60:63], v[124:127], v[164:167]\n" \
  "v_rcp_f32 v188, v162\n" \
  "v_mfma_f32_16x16x32_bf16 v[148:151], v[44:47], v[124:127], 0\n" \
  "v_mul_f32 v144, v188, v144\n" \
  "v_mul_f32 v145, v188, v145\n" \
  "v_mfma_f32_16x16x32_bf16 v[168:171], v[80:83], v[184:187], 0\n" \
  "s_waitcnt vmcnt(4)\n" \
  "v_rcp_f32 v189, v166\n" \
  "v_mfma_f32_16x16x32_bf16 v[168:171], v[64:67], v[128:131], v[168:171]\n" \
  "v_mul_f32 v146, v188, v146\n" \
  "v_mul_f32 v147, v188, v147\n" \
  "v_mfma_f32_16x16x32_bf16 v[152:155], v[48:51], v[128:131], 0\n" \
  "v_mul_f32 v160, v188, v160\n" \
  "v_mul_f32 v161, v188, v161\n" \
  "v_mfma_f32_16x16x32_bf16 v[172:175], v[84:87], v[184:187], 0\n" \
  "s_waitcnt vmcnt(3)\n" \
  "v_rcp_f32 v190, v170\n" \
  "v_mfma_f32_16x16x32_bf16 v[172:175], v[68:71], v[132:135], v[172:175]\n" \
  "v_cndmask_b32 v167, v188, v189, s[28:29]\n" \
  "v_cndmask_b32 v167, v167, v190, s[26:27]\n" \
  "v_mfma_f32_16x16x32_bf16 v[156:159], v[52:55], v[132:135], 0\n" \
  "v_fmac_f32 v144, v189, v148\n" \
  "v_fmac_f32 v145, v189, v149\n" \
  "v_fmac_f32 v146, v189, v150\n" \
  "v_fmac_f32 v147, v189, v151\n" \
  "v_rcp_f32 v191, v174\n" \
  "v_fmac_f32 v160, v189, v164\n" \
  "v_cndmask_b32 v167, v167, v191, s[24:25]\n" \
  "v_pk_mul_f32 v[176:177], v[176:177], v[166:167] op_sel:[0,1] op_sel_hi:[1,1]\n" \
  "v_pk_mul_f32 v[178:179], v[178:179], v[166:167] op_sel:[0,1] op_sel_hi:[1,1]\n" \
  "v_pk_mul_f32 v[180:181], v[180:181], v[166:167] op_sel:[0,1] op_sel_hi:[1,1]\n" \
  "v_pk_mul_f32 v[182:183], v[182:183], v[166:167] op_sel:[0,1] op_sel_hi:[1,1]\n" \
  "v_cmp_nlt_f32 vcc, 0x0d800000, v167\n" \
  "v_cvt_pk_bf16_f32 v184, v176, v177\n" \
  "v_cvt_pk_bf16_f32 v185, v178, v179\n" \
  "v_cvt_pk_bf16_f32 v186, v180, v181\n" \
  "v_cvt_pk_bf16_f32 v187, v182, v183\n" \
  "v_fmac_f32 v161, v189, v165\n" \
  "v_pk_fma_f32 v[160:161], v[168:169], v[190:191], v[160:161] op_sel:[0,0,0] op_sel_hi:[1,0,1]\n" \
  "v_pk_fma_f32 v[160:161], v[172:173], v[190:191], v[160:161] op_sel:[0,1,0] op_sel_hi:[1,1,1]\n" \
  "v_pk_fma_f32 v[144:145], v[152:153], v[190:191], v[144:145] op_sel:[0,0,0] op_sel_hi:[1,0,1]\n" \
  "v_pk_fma_f32 v[146:147], v[154:155], v[190:191], v[146:147] op_sel:[0,0,0] op_sel_hi:[1,0,1]\n" \
  "v_pk_fma_f32 v[144:145], v[156:157], v[190:191], v[144:145] op_sel:[0,1,0] op_sel_hi:[1,1,1]\n" \
  "v_pk_fma_f32 v[146:147], v[158:159], v[190:191], v[146:147] op_sel:[0,1,0] op_sel_hi:[1,1,1]\n" \
  "s_waitcnt vmcnt(1)\n" \
  "v_mfma_f32_16x16x32_bf16 v[160:163], v[140:143], v[184:187], v[160:163]\n" \
  "s_mov_b64 s[80:81], vcc\n" \
  "v_mfma_f32_16x16x32_bf16 v[144:147], v[136:139], v[184:187], v[144:147]\n" \
  "s_mov_b64 exec, s[30:31]\n" \
  "s_nop 2\n" \
  "ds_write_b64 v22, v[160:161]\n" \
  "v_cmp_u_f32 s[78:79], v160, v161\n" \
  "s_nop 1\n" \
  "ds_write2_b64 v21, v[144:145], v[146:147] offset1:16\n" \
  "v_add_f32 v188, v144, v145\n" \
  "v_add_f32 v189, v146, v147\n" \
  "v_cmp_u_f32 vcc, v188, v189\n" \
  "s_or_b64 s[80:81], s[80:81], s[78:79]\n" \
  "s_or_b64 vcc, vcc, s[80:81]\n" \
  "s_cbranch_vccnz LSF11_%=\n" \
  "LSOK11_%=:\n" \
  "s_mov_b64 exec, -1\n" \
  "s_waitcnt lgkmcnt(0)\n" \
  "s_barrier\n" \
  "ds_read_b32 v188, v33\n" \
  "s_waitcnt lgkmcnt(0)\n" \
  "v_cmp_ne_u32 vcc, 0, v188\n" \
  "s_cbranch_vccnz LSR11_%=\n" \
  "LNL11_%=:\n" \
  "s_waitcnt lgkmcnt(0)\n" \
  "s_cmp_eq_u32 s73, 1\n" \
  "s_cbranch_scc1 LOOP11_%=\n" \
  "s_branch LDONE_%=\n" \
  "LMF11_%=:\n" \
  "v_mbcnt_lo_u32_b32 v130, -1, 0\n" \
  "v_mbcnt_hi_u32_b32 v130, -1, v130\n" \
  "v_lshlrev_b32 v130, 4, v130\n" \
  "v_mov_b32 v128, 0\n" \
  "ds_read_b128 v[132:135], v130 offset:0\n" \
  "ds_read_b128 v[136:139], v130 offset:1024\n" \
  "ds_read_b128 v[140:143], v130 offset:2048\n" \
  "ds_read_b128 v[144:147], v130 offset:3072\n" \
  "ds_read_b128 v[148:151], v130 offset:4096\n" \
  "s_waitcnt lgkmcnt(0)\n" \
  "v_max3_u32 v128, v128, v132, v133\n" \
  "v_max3_u32 v128, v128, v134, v135\n" \
  "v_max3_u32 v128, v128, v136, v137\n" \
  "v_max3_u32 v128, v128, v138, v139\n" \
  "v_max3_u32 v128, v128, v140, v141\n" \
  "v_max3_u32 v128, v128, v142, v143\n" \
  "v_max3_u32 v128, v128, v144, v145\n" \
  "v_max3_u32 v128, v128, v146, v147\n" \
  "v_max3_u32 v128, v128, v148, v149\n" \
  "v_max3_u32 v128, v128, v150, v151\n" \
  "ds_read_b128 v[132:135], v130 offset:5120\n" \
  "ds_read_b128 v[136:139], v130 offset:6144\n" \
  "ds_read_b128 v[140:143], v130 offset:7168\n" \
  "ds_read_b128 v[144:147], v130 offset:8192\n" \
  "ds_read_b128 v[148:151], v130 offset:9216\n" \
  "s_waitcnt lgkmcnt(0)\n" \
  "v_max3_u32 v128, v128, v132, v133\n" \
  "v_max3_u32 v128, v128, v134, v135\n" \
  "v_max3_u32 v128, v128, v136, v137\n" \
  "v_max3_u32 v128, v128, v138, v139\n" \
  "v_max3_u32 v128, v128, v140, v141\n" \
  "v_max3_u32 v128, v128, v142, v143\n" \
  "v_max3_u32 v128, v128, v144, v145\n" \
  "v_max3_u32 v128, v128, v146, v147\n" \
  "v_max3_u32 v128, v128, v148, v149\n" \
  "v_max3_u32 v128, v128, v150, v151\n" \
  "v_cmp_eq_u32 vcc, -1, v128\n" \
  "s_cbranch_vccz LMOK11_%=\n" \
  "s_cmp_gt_u32 s58, 4000\n" \
  "s_cbranch_scc1 LMOK11_%=\n" \
  "s_add_u32 s58, s58, 1\n" \
  "s_sub_u32 s64, s64, 1\n" \
  "s_mov_b32 s66, s65\n" \
  "s_waitcnt vmcnt(0) lgkmcnt(0)\n" \
  "s_barrier\n" \
  "s_branch LOOP11_%=\n" \
  "LKE11_%=:\n" \
  "s_waitcnt lgkmcnt(0)\n" \
  "buffer_store_dwordx4 v[88:91], v14, s[36:39], s69 offen sc1\n" \
  "s_branch LDONE_%=\n" \
  "LSF11_%=:\n" \
  "s_getpc_b64 s[78:79]\n" \
  "s_branch LSF_%=\n" \
  "s_branch LSOK11_%=\n" \
  "LSR11_%=:\n" \
  "v_mov_b32 v188, 0\n" \
  "s_add_u32 s59, s59, 1\n" \
  "s_barrier\n" \
  "ds_write_b32 v33, v188\n" \
  "s_waitcnt lgkmcnt(0)\n" \
  "s_branch LB11_%=\n" \
  "LSTART12_%=:\n" \
  "s_getpc_b64 s[80:81]\n" \
  "LPC12_%=:\n" \
  "s_add_u32 s82, s80, LEA12_%=-LPC12_%=+24\n" \
  "s_addc_u32 s83, s81, 0\n" \
  "s_add_u32 s84, s80, LEB12_%=-LPC12_%=+24\n" \
  "s_addc_u32 s85, s81, 0\n" \
  "LOOP12_%=:\n" \
  "v_readfirstlane_b32 s86, v34\n" \
  "v_readlane_b32 s43, v34, s53\n" \
  "s_add_u32 s71, s64, 1\n" \
  "s_min_u32 s71, s71, 63\n" \
  "s_lshr_b32 s72, s71, 4\n" \
  "s_lshl_b32 s72, s72, 13\n" \
  "s_lshl_b32 s71, s71, 9\n" \
  "s_add_u32 s71, s71, s72\n" \
  "s_mov_b32 s87, s71\n" \
  "s_lshl_b32 s40, s64, 7\n" \
  "s_mov_b32 m0, 0\n" \
  "LWA12_%=:\n" \
  "s_memrealtime s[74:75]\n" \
  "s_memtime s[76:77]\n" \
  "s_waitcnt lgkmcnt(0)\n" \
  "s_sub_i32 s72, s88, s74\n" \
  "s_mul_i32 s72, s72, s90\n" \
  "s_ashr_i32 s72, s72, 8\n" \
  "s_sub_i32 s71, s89, s76\n" \
  "s_min_i32 s71, s71, s72\n" \
  "s_sub_i32 s72, s72, 92\n" \
  "s_max_i32 s71, s71, s72\n" \
  "s_add_u32 s89, s71, s76\n" \
  "s_cmp_gt_i32 s71, 168\n" \
  "s_cbranch_scc1 LWA12_%=\n" \
  "s_cmp_lt_i32 s71, s94\n" \
  "s_cbranch_scc1 LSA12_%=\n" \
  "s_max_i32 s71, s71, 48\n" \
  "s_lshr_b32 s71, s71, 3\n" \
  "s_lshl_b32 s71, s71, 2\n" \
  "s_sub_u32 s78, s82, s71\n" \
  "s_subb_u32 s79, s83, 0\n" \
  "s_setpc_b64 s[78:79]\n" \
  "s_nop 1\n" \
  "s_nop 1\n" \
  "s_nop 1\n" \
  "s_nop 1\n" \
  "s_nop 1\n" \
  "s_nop 1\n" \
  "s_nop 1\n" \
  "s_nop 1\n" \
  "s_nop 1\n" \
  "s_nop 1\n" \
  "s_nop 1\n" \
  "s_nop 1\n" \
  "s_nop 1\n" \
  "s_nop 1\n" \
  "s_nop 1\n" \
  "s_nop 1\n" \
  "LEA12_%=:\n" \
  "ds_read_b32 v188, v33\n" \
  "buffer_load_dwordx4 v1, s[36:39], s67 offen sc1 lds\n" \
  "buffer_load_dwordx4 v1, s[36:39], s67 offen offset:1024 sc1 lds\n" \
  "buffer_load_dwordx4 v1, s[36:39], s67 offen offset:2048 sc1 lds\n" \
  "buffer_load_dwordx4 v1, s[36:39], s67 offen offset:3072 sc1 lds\n" \
  "s_mov_b32 m0, 4096\n" \
  "s_nop 0\n" \
  "buffer_load_dwordx4 v2, s[36:39], s67 offen sc1 lds\n" \
  "buffer_load_dwordx4 v2, s[36:39], s67 offen offset:1024 sc1 lds\n" \
  "buffer_load_dwordx4 v2, s[36:39], s67 offen offset:2048 sc1 lds\n" \
  "buffer_load_dwordx4 v2, s[36:39], s67 offen offset:3072 sc1 lds\n" \
  "s_mov_b32 m0, 8192\n" \
  "s_nop 0\n" \
  "buffer_load_dwordx4 v3, s[36:39], s67 offen sc1 lds\n" \
  "buffer_load_dwordx4 v3, s[36:39], s67 offen offset:1024 sc1 lds\n" \
  "s_waitcnt lgkmcnt(0)\n" \
  "v_cmp_ne_u32 vcc, 0, v188\n" \
  "s_cbranch_vccnz LSR12_%=\n" \
  "buffer_load_dword v35, v28, s[36:39], s87 offen sc1\n" \
  "buffer_load_dword v34, v29, s[36:39], s40 offen offset:128 sc1\n" \
  "v_readlane_b32 s70, v27, s64\n" \
  "s_mov_b32 s65, s66\n" \
  "s_xor_b32 s69, s66, 0x2000\n" \
  "s_add_u32 s64, s64, 1\n" \
  "s_barrier\n" \
  "v_mov_b32 v176, 0\n" \
  "v_mov_b32 v177, 0\n" \
  "v_mov_b32 v178, 0\n" \
  "v_mov_b32 v179, 0\n" \
  "v_mov_b32 v180, 0\n" \
  "v_mov_b32 v181, 0\n" \
  "v_mov_b32 v182, 0\n" \
  "v_mov_b32 v183, 0\n" \
  "v_readlane_b32 s66, v25, s64\n" \
  "v_readlane_b32 s68, v26, s64\n" \
  "s_cmp_eq_u32 s34, s64\n" \
  "s_cbranch_scc1 LRT12_%=\n" \
  "s_mov_b32 s34, s64\n" \
  "s_lshr_b32 s71, s86, 8\n" \
  "s_and_b32 s72, s86, 0xff\n" \
  "s_cselect_b32 s61, s72, s61\n" \
  "s_cselect_b32 s71, s71, s92\n" \
  "s_sub_i32 s72, s71, s92\n" \
  "s_add_u32 s92, s71, s61\n" \
  "s_lshl_b32 s72, s72, 1\n" \
  "s_add_u32 s88, s88, s72\n" \
  "s_mul_i32 s72, s72, 24\n" \
  "s_add_u32 s89, s89, s72\n" \
  "s_cmp_lg_u32 s43, 0\n" \
  "s_cselect_b32 s90, s43, s90\n" \
  "LRT12_%=:\n" \
  "LM12_%=:\n" \
  "s_waitcnt vmcnt(11)\n" \
  "s_barrier\n" \
  "ds_read_b128 v[112:115], v5\n" \
  "ds_read_b128 v[116:119], v5 offset:512\n" \
  "s_waitcnt vmcnt(10)\n" \
  "s_barrier\n" \
  "ds_read_b128 v[120:123], v5 offset:1024\n" \
  "ds_read_b128 v[124:127], v5 offset:1536\n" \
  "s_waitcnt lgkmcnt(3)\n" \
  "v_smfmac_f32_16x16x64_bf16 v[176:179], v[112:115], a[0:7], v0\n" \
  "v_smfmac_f32_16x16x64_bf16 v[180:183], v[112:115], a[160:167], v0\n" \
  "s_waitcnt vmcnt(8)\n" \
  "s_waitcnt lgkmcnt(2)\n" \
  "v_smfmac_f32_16x16x64_bf16 v[176:179], v[116:119], a[8:15], v0\n" \
  "s_barrier\n" \
  "ds_read_b128 v[128:131], v5 offset:2048\n" \
  "v_smfmac_f32_16x16x64_bf16 v[180:183], v[116:119], a[168:175], v0\n" \
  "s_waitcnt lgkmcnt(2)\n" \
  "ds_read_b128 v[132:135], v5 offset:2560\n" \
  "v_smfmac_f32_16x16x64_bf16 v[176:179], v[120:123], a[16:23], v0\n" \
  "ds_read_b128 v[136:139], v5 offset:3072\n" \
  "v_smfmac_f32_16x16x64_bf16 v[180:183], v[120:123], a[176:183], v0\n" \
  "s_waitcnt vmcnt(6)\n" \
  "s_waitcnt lgkmcnt(3)\n" \
  "ds_read_b128 v[140:143], v5 offset:3584\n" \
  "v_smfmac_f32_16x16x64_bf16 v[176:179], v[124:127], a[24:31], v0\n" \
  "s_barrier\n" \
  "ds_read_b128 v[144:147], v5 offset:4096\n" \
  "v_smfmac_f32_16x16x64_bf16 v[180:183], v[124:127], a[184:191], v0\n" \
  "s_waitcnt lgkmcnt(4)\n" \
  "ds_read_b128 v[148:151], v5 offset:4608\n" \
  "v_smfmac_f32_16x16x64_bf16 v[176:179], v[128:131], a[32:39], v0\n" \
  "ds_read_b128 v[152:155], v5 offset:5120\n" \
  "v_smfmac_f32_16x16x64_bf16 v[180:183], v[128:131], a[192:199], v0\n" \
  "s_waitcnt vmcnt(4)\n" \
  "s_waitcnt lgkmcnt(5)\n" \
  "ds_read_b128 v[156:159], v5 offset:5632\n" \
  "v_smfmac_f32_16x16x64_bf16 v[176:179], v[132:135], a[40:47], v0\n" \
  "s_barrier\n" \
  "ds_read_b128 v[160:163], v5 offset:6144\n" \
  "v_smfmac_f32_16x16x64_bf16 v[180:183], v[132:135], a[200:207], v0\n" \
  "s_waitcnt lgkmcnt(6)\n" \
  "ds_read_b128 v[164:167], v5 offset:6656\n" \
  "v_smfmac_f32_16x16x64_bf16 v[176:179], v[136:139], a[48:55], v0\n" \
  "ds_read_b128 v[168:171], v5 offset:7168\n" \
  "v_smfmac_f32_16x16x64_bf16 v[180:183], v[136:139], a[208:215], v0\n" \
  "s_waitcnt vmcnt(2)\n" \
  "s_waitcnt lgkmcnt(7)\n" \
  "ds_read_b128 v[172:175], v5 offset:7680\n" \
  "v_smfmac_f32_16x16x64_bf16 v[176:179], v[140:143], a[56:63], v0\n" \
  "s_barrier\n" \
  "ds_read2_b32 v[112:113], v6 offset0:0 offset1:32\n" \
  "v_smfmac_f32_16x16x64_bf16 v[180:183], v[140:143], a[216:223], v0\n" \
  "s_waitcnt lgkmcnt(8)\n" \
  "ds_read2_b32 v[114:115], v6 offset0:64 offset1:96\n" \
  "v_smfmac_f32_16x16x64_bf16 v[176:179], v[144:147], a[64:71], v0\n" \
  "s_waitcnt lgkmcnt(8)\n" \
  "ds_read2_b32 v[116:117], v6 offset0:128 offset1:160\n" \
  "v_smfmac_f32_16x16x64_bf16 v[176:179], v[148:151], a[72:79], v0\n" \
  "s_waitcnt lgkmcnt(8)\n" \
  "ds_read2_b32 v[118:119], v6 offset0:192 offset1:224\n" \
  "v_smfmac_f32_16x16x64_bf16 v[176:179], v[152:155], a[80:87], v0\n" \
  "s_waitcnt lgkmcnt(8)\n" \
  "ds_read2_b32 v[120:121], v7 offset0:0 offset1:32\n" \
  "v_smfmac_f32_16x16x64_bf16 v[176:179], v[156:159], a[88:95], v0\n" \
  "s_waitcnt lgkmcnt(8)\n" \
  "ds_read2_b32 v[122:123], v7 offset0:64 offset1:96\n" \
  "v_smfmac_f32_16x16x64_bf16 v[176:179], v[160:163], a[96:103], v0\n" \
  "s_waitcnt lgkmcnt(8)\n" \
  "ds_read2_b32 v[124:125], v7 offset0:128 offset1:160\n" \
  "v_smfmac_f32_16x16x64_bf16 v[176:179], v[164:167], a[104:111], v0\n" \
  "s_waitcnt lgkmcnt(8)\n" \
  "ds_read2_b32 v[126:127], v7 offset0:192 offset1:224\n" \
  "v_smfmac_f32_16x16x64_bf16 v[176:179], v[168:171], a[112:119], v0\n" \
  "s_waitcnt lgkmcnt(8)\n" \
  "v_smfmac_f32_16x16x64_bf16 v[176:179], v[172:175], a[120:127], v0\n" \
  "s_waitcnt lgkmcnt(6)\n" \
  "v_smfmac_f32_16x16x64_bf16 v[176:179], v[112:115], a[128:135], v0\n" \
  "s_waitcnt lgkmcnt(4)\n" \
  "v_smfmac_f32_16x16x64_bf16 v[176:179], v[116:119], a[136:143], v0\n" \
  "s_waitcnt lgkmcnt(2)\n" \
  "v_smfmac_f32_16x16x64_bf16 v[176:179], v[120:123], a[144:151], v0\n" \
  "s_waitcnt lgkmcnt(0)\n" \
  "v_smfmac_f32_16x16x64_bf16 v[176:179], v[124:127], a[152:159], v0\n" \
  "v_smfmac_f32_16x16x64_bf16 v[180:183], v[144:147], a[224:231], v0\n" \
  "v_smfmac_f32_16x16x64_bf16 v[180:183], v[148:151], a[232:239], v0\n" \
  "v_cvt_pk_bf16_f32 v184, v176, v177\n" \
  "v_smfmac_f32_16x16x64_bf16 v[180:183], v[152:155], a[240:247], v0\n" \
  "v_cvt_pk_bf16_f32 v185, v178, v179\n" \
  "v_smfmac_f32_16x16x64_bf16 v[180:183], v[156:159], a[248:255], v0\n" \
  "ds_write_b64 v8, v[184:185]\n" \
  "v_add_f32 v186, v176, v177\n" \
  "v_smfmac_f32_16x16x64_bf16 v[180:183], v[160:163], v[192:199], v0\n" \
  "v_add_f32 v187, v178, v179\n" \
  "v_smfmac_f32_16x16x64_bf16 v[180:183], v[164:167], v[200:207], v0\n" \
  "v_cmp_u_f32 vcc, v186, v187\n" \
  "v_smfmac_f32_16x16x64_bf16 v[180:183], v[168:171], v[208:215], v0\n" \
  "v_smfmac_f32_16x16x64_bf16 v[180:183], v[172:175], v[216:223], v0\n" \
  "s_cbranch_vccnz LMF12_%=\n" \
  "LMOK12_%=:\n" \
  "v_smfmac_f32_16x16x64_bf16 v[180:183], v[112:115], v[224:231], v0\n" \
  "s_waitcnt lgkmcnt(0)\n" \
  "s_barrier\n" \
  "v_smfmac_f32_16x16x64_bf16 v[180:183], v[116:119], v[232:239], v0\n" \
  "v_smfmac_f32_16x16x64_bf16 v[180:183], v[120:123], v[240:247], v0\n" \
  "v_smfmac_f32_16x16x64_bf16 v[180:183], v[124:127], v[248:255], v0\n" \
  "s_nop 3\n" \
  "v_mov_b32 v31, v31\n" \
  "s_nop 2\n" \
  "v_cvt_pk_bf16_f32 v186, v180, v181\n" \
  "v_cvt_pk_bf16_f32 v187, v182, v183\n" \
  "ds_write_b64 v9, v[186:187]\n" \
  "s_waitcnt lgkmcnt(0)\n" \
  "s_barrier\n" \
  "s_nop 0\n" \
  "ds_read_b64_tr_b16 v[88:89], v12\n" \
  "ds_read_b64_tr_b16 v[90:91], v12 offset:32\n" \
  "s_mov_b32 s67, s66\n" \
  "s_cmp_eq_u32 s64, 64\n" \
  "s_cbranch_scc1 LKE12_%=\n" \
  "s_add_u32 s72, s50, s61\n" \
  "s_add_u32 s88, s88, s72\n" \
  "s_mul_i32 s91, s72, s90\n" \
  "s_lshr_b32 s91, s91, 8\n" \
  "s_add_u32 s89, s89, s91\n" \
  "s_waitcnt vmcnt(0)\n" \
  "v_cmp_gt_u32 vcc, 16, v35\n" \
  "s_cbranch_vccnz LFF12_%=\n" \
  "LFOK12_%=:\n" \
  "LB12_%=:\n" \
  "LWB12_%=:\n" \
  "s_memrealtime s[74:75]\n" \
  "s_memtime s[76:77]\n" \
  "s_waitcnt lgkmcnt(0)\n" \
  "s_sub_i32 s72, s88, s74\n" \
  "s_mul_i32 s72, s72, s90\n" \
  "s_ashr_i32 s72, s72, 8\n" \
  "s_sub_i32 s71, s89, s76\n" \
  "s_min_i32 s71, s71, s72\n" \
  "s_sub_i32 s72, s72, 92\n" \
  "s_max_i32 s71, s71, s72\n" \
  "s_add_u32 s89, s71, s76\n" \
  "s_cmp_gt_i32 s71, 168\n" \
  "s_cbranch_scc1 LWB12_%=\n" \
  "s_cmp_lt_i32 s71, s94\n" \
  "s_cbranch_scc1 LSB12_%=\n" \
  "s_max_i32 s71, s71, 48\n" \
  "s_lshr_b32 s71, s71, 3\n" \
  "s_lshl_b32 s71, s71, 2\n" \
  "s_sub_u32 s78, s84, s71\n" \
  "s_subb_u32 s79, s85, 0\n" \
  "s_setpc_b64 s[78:79]\n" \
  "s_nop 1\n" \
  "s_nop 1\n" \
  "s_nop 1\n" \
  "s_nop 1\n" \
  "s_nop 1\n" \
  "s_nop 1\n" \
  "s_nop 1\n" \
  "s_nop 1\n" \
  "s_nop 1\n" \
  "s_nop 1\n" \
  "s_nop 1\n" \
  "s_nop 1\n" \
  "s_nop 1\n" \
  "s_nop 1\n" \
  "s_nop 1\n" \
  "s_nop 1\n" \
  "LEB12_%=:\n" \
  "s_barrier\n" \
  "buffer_load_dwordx4 v[116:119], v15, s[36:39], s65 offen offset:128 sc1\n" \
  "s_barrier\n" \
  "buffer_load_dwordx4 v[112:115], v15, s[36:39], s65 offen sc1\n" \
  "s_barrier\n" \
  "buffer_load_dwordx4 v[120:123], v16, s[36:39], s65 offen sc1\n" \
  "s_barrier\n" \
  "buffer_load_dwordx4 v[124:127], v16, s[36:39], s65 offen offset:512 sc1\n" \
  "s_barrier\n" \
  "buffer_load_dwordx4 v[128:131], v16, s[36:39], s65 offen offset:1024 sc1\n" \
  "s_barrier\n" \
  "buffer_load_dwordx4 v[132:135], v16, s[36:39], s65 offen offset:1536 sc1\n" \
  "ds_read_b128 v[40:43], v19\n" \
  "ds_read_b128 v[44:47], v19 offset:512\n" \
  "ds_read_b128 v[48:51], v19 offset:1024\n" \
  "ds_read_b128 v[52:55], v19 offset:1536\n" \
  "s_mov_b64 exec, s[46:47]\n" \
  "ds_read_b128 v[56:59], v20\n" \
  "ds_read_b128 v[60:63], v20 offset:128\n" \
  "ds_read_b128 v[64:67], v20 offset:256\n" \
  "ds_read_b128 v[68:71], v20 offset:384\n" \
  "s_mov_b64 exec, -1\n" \
  "s_add_u32 s72, s51, s61\n" \
  "s_add_u32 s88, s88, s72\n" \
  "s_mul_i32 s91, s72, s90\n" \
  "s_lshr_b32 s91, s91, 8\n" \
  "s_add_u32 s89, s89, s91\n" \
  "v_readfirstlane_b32 s86, v34\n" \
  "v_readlane_b32 s43, v34, s53\n" \
  "s_add_u32 s71, s64, 1\n" \
  "s_min_u32 s71, s71, 63\n" \
  "s_lshr_b32 s72, s71, 4\n" \
  "s_lshl_b32 s72, s72, 13\n" \
  "s_lshl_b32 s71, s71, 9\n" \
  "s_add_u32 s71, s71, s72\n" \
  "s_mov_b32 s87, s71\n" \
  "s_lshl_b32 s40, s64, 7\n" \
  "s_mov_b32 m0, 0\n" \
  "s_nop 1\n" \
  "buffer_load_dwordx4 v[136:139], v17, s[36:39], s65 offen sc1\n" \
  "buffer_load_dwordx4 v[140:143], v18, s[36:39], s65 offen sc1\n" \
  "s_barrier\n" \
  "s_waitcnt lgkmcnt(0)\n" \
  "buffer_store_dwordx4 v[88:91], v14, s[36:39], s69 offen sc1\n" \
  "s_waitcnt lgkmcnt(0)\n" \
  "s_getreg_b32 s71, hwreg(HW_REG_IB_STS, 0, 4)\n" \
  "s_cmp_le_u32 s71, 7\n" \
  "v_mfma_f32_16x16x32_bf16 v[144:147], v[72:75], v[72:75], 0\n" \
  "s_cbranch_scc1 LHXS12_%=\n" \
  "s_getreg_b32 s71, hwreg(HW_REG_IB_STS, 0, 4)\n" \
  "s_cmp_le_u32 s71, 7\n" \
  "v_mfma_f32_16x16x32_bf16 v[144:147], v[72:75], v[72:75], 0\n" \
  "s_cbranch_scc1 LHXS12_%=\n" \
  "s_getreg_b32 s71, hwreg(HW_REG_IB_STS, 0, 4)\n" \
  "s_cmp_le_u32 s71, 7\n" \
  "v_mfma_f32_16x16x32_bf16 v[144:147], v[72:75], v[72:75], 0\n" \
  "s_cbranch_scc1 LHXS12_%=\n" \
  "s_getreg_b32 s71, hwreg(HW_REG_IB_STS, 0, 4)\n" \
  "s_cmp_le_u32 s71, 7\n" \
  "v_mfma_f32_16x16x32_bf16 v[144:147], v[72:75], v[72:75], 0\n" \
  "s_cbranch_scc1 LHXS12_%=\n" \
  "s_getreg_b32 s71, hwreg(HW_REG_IB_STS, 0, 4)\n" \
  "s_cmp_le_u32 s71, 7\n" \
  "v_mfma_f32_16x16x32_bf16 v[144:147], v[72:75], v[72:75], 0\n" \
  "s_cbranch_scc1 LHXS12_%=\n" \
  "s_getreg_b32 s71, hwreg(HW_REG_IB_STS, 0, 4)\n" \
  "s_cmp_le_u32 s71, 7\n" \
  "v_mfma_f32_16x16x32_bf16 v[144:147], v[72:75], v[72:75], 0\n" \
  "s_cbranch_scc1 LHXS12_%=\n" \
  "s_getreg_b32 s71, hwreg(HW_REG_IB_STS, 0, 4)\n" \
  "s_cmp_le_u32 s71, 7\n" \
  "v_mfma_f32_16x16x32_bf16 v[144:147], v[72:75], v[72:75], 0\n" \
  "s_cbranch_scc1 LHXS12_%=\n" \
  "s_getreg_b32 s71, hwreg(HW_REG_IB_STS, 0, 4)\n" \
  "s_cmp_le_u32 s71, 7\n" \
  "v_mfma_f32_16x16x32_bf16 v[144:147], v[72:75], v[72:75], 0\n" \
  "s_cbranch_scc1 LHXS12_%=\n" \
  "s_getreg_b32 s71, hwreg(HW_REG_IB_STS, 0, 4)\n" \
  "s_cmp_le_u32 s71, 7\n" \
  "v_mfma_f32_16x16x32_bf16 v[144:147], v[72:75], v[72:75], 0\n" \
  "s_cbranch_scc1 LHXS12_%=\n" \
  "s_getreg_b32 s71, hwreg(HW_REG_IB_STS, 0, 4)\n" \
  "s_cmp_le_u32 s71, 7\n" \
  "v_mfma_f32_16x16x32_bf16 v[144:147], v[72:75], v[72:75], 0\n" \
  "s_cbranch_scc1 LHXS12_%=\n" \
  "s_getreg_b32 s71, hwreg(HW_REG_IB_STS, 0, 4)\n" \
  "s_cmp_le_u32 s71, 7\n" \
  "v_mfma_f32_16x16x32_bf16 v[144:147], v[72:75], v[72:75], 0\n" \
  "s_cbranch_scc1 LHXS12_%=\n" \
  "s_getreg_b32 s71, hwreg(HW_REG_IB_STS, 0, 4)\n" \
  "s_cmp_le_u32 s71, 7\n" \
  "v_mfma_f32_16x16x32_bf16 v[144:147], v[72:75], v[72:75], 0\n" \
  "s_cbranch_scc1 LHXS12_%=\n" \
  "s_getreg_b32 s71, hwreg(HW_REG_IB_STS, 0, 4)\n" \
  "s_cmp_le_u32 s71, 7\n" \
  "v_mfma_f32_16x16x32_bf16 v[144:147], v[72:75], v[72:75], 0\n" \
  "s_cbranch_scc1 LHXS12_%=\n" \
  "s_getreg_b32 s71, hwreg(HW_REG_IB_STS, 0, 4)\n" \
  "s_cmp_le_u32 s71, 7\n" \
  "v_mfma_f32_16x16x32_bf16 v[144:147], v[72:75], v[72:75], 0\n" \
  "s_cbranch_scc1 LHXS12_%=\n" \
  "s_getreg_b32 s71, hwreg(HW_REG_IB_STS, 0, 4)\n" \
  "s_cmp_le_u32 s71, 7\n" \
  "v_mfma_f32_16x16x32_bf16 v[144:147], v[72:75], v[72:75], 0\n" \
  "s_cbranch_scc1 LHXS12_%=\n" \
  "s_getreg_b32 s71, hwreg(HW_REG_IB_STS, 0, 4)\n" \
  "s_cmp_le_u32 s71, 7\n" \
  "v_mfma_f32_16x16x32_bf16 v[144:147], v[72:75], v[72:75], 0\n" \
  "s_cbranch_scc1 LHXS12_%=\n" \
  "s_getreg_b32 s71, hwreg(HW_REG_IB_STS, 0, 4)\n" \
  "s_cmp_le_u32 s71, 7\n" \
  "v_mfma_f32_16x16x32_bf16 v[144:147], v[72:75], v[72:75], 0\n" \
  "s_cbranch_scc1 LHXS12_%=\n" \
  "s_getreg_b32 s71, hwreg(HW_REG_IB_STS, 0, 4)\n" \
  "s_cmp_le_u32 s71, 7\n" \
  "v_mfma_f32_16x16x32_bf16 v[144:147], v[72:75], v[72:75], 0\n" \
  "s_cbranch_scc1 LHXS12_%=\n" \
  "s_getreg_b32 s71, hwreg(HW_REG_IB_STS, 0, 4)\n" \
  "s_cmp_le_u32 s71, 7\n" \
  "v_mfma_f32_16x16x32_bf16 v[144:147], v[72:75], v[72:75], 0\n" \
  "s_cbranch_scc1 LHXS12_%=\n" \
  "s_getreg_b32 s71, hwreg(HW_REG_IB_STS, 0, 4)\n" \
  "s_cmp_le_u32 s71, 7\n" \
  "v_mfma_f32_16x16x32_bf16 v[144:147], v[72:75], v[72:75], 0\n" \
  "s_cbranch_scc1 LHXS12_%=\n" \
  "LHXS12_%=:\n" \
  "s_waitcnt vmcnt(7)\n" \
  "LS212_%=:\n" \
  "v_dot2_f32_bf16 v176, v112, s22, 0\n" \
  "v_dot2_f32_bf16 v177, v112, s23, 0\n" \
  "v_dot2_f32_bf16 v178, v113, s22, 0\n" \
  "v_dot2_f32_bf16 v179, v113, s23, 0\n" \
  "v_dot2_f32_bf16 v180, v114, s22, 0\n" \
  "v_dot2_f32_bf16 v181, v114, s23, 0\n" \
  "v_dot2_f32_bf16 v182, v115, s22, 0\n" \
  "v_dot2_f32_bf16 v183, v115, s23, 0\n" \
  "v_dot2_f32_bf16 v176, v116, s22, v176\n" \
  "v_dot2_f32_bf16 v177, v116, s23, v177\n" \
  "v_dot2_f32_bf16 v178, v117, s22, v178\n" \
  "v_dot2_f32_bf16 v179, v117, s23, v179\n" \
  "v_dot2_f32_bf16 v180, v118, s22, v180\n" \
  "v_dot2_f32_bf16 v181, v118, s23, v181\n" \
  "v_dot2_f32_bf16 v182, v119, s22, v182\n" \
  "v_dot2_f32_bf16 v183, v119, s23, v183\n" \
  "v_exp_f32 v176, v176\n" \
  "v_exp_f32 v177, v177\n" \
  "v_exp_f32 v178, v178\n" \
  "v_exp_f32 v179, v179\n" \
  "s_waitcnt vmcnt(6)\n" \
  "v_mfma_f32_16x16x32_bf16 v[160:163], v[56:59], v[120:123], 0\n" \
  "v_exp_f32 v180, v180\n" \
  "v_mfma_f32_16x16x32_bf16 v[144:147], v[40:43], v[120:123], 0\n" \
  "v_exp_f32 v181, v181\n" \
  "v_exp_f32 v182, v182\n" \
  "v_exp_f32 v183, v183\n" \
  "v_cvt_pk_bf16_f32 v184, v176, v177\n" \
  "v_cvt_pk_bf16_f32 v185, v178, v179\n" \
  "v_cvt_pk_bf16_f32 v186, v180, v181\n" \
  "v_cvt_pk_bf16_f32 v187, v182, v183\n" \
  "s_nop 1\n" \
  "v_mfma_f32_16x16x32_bf16 v[160:163], v[72:75], v[184:187], v[160:163]\n" \
  "v_mfma_f32_16x16x32_bf16 v[164:167], v[76:79], v[184:187], 0\n" \
  "s_waitcnt vmcnt(5)\n" \
  "v_mfma_f32_16x16x32_bf16 v[164:167], v[60:63], v[124:127], v[164:167]\n" \
  "v_rcp_f32 v188, v162\n" \
  "v_mfma_f32_16x16x32_bf16 v[148:151], v[44:47], v[124:127], 0\n" \
  "v_mul_f32 v144, v188, v144\n" \
  "v_mul_f32 v145, v188, v145\n" \
  "v_mfma_f32_16x16x32_bf16 v[168:171], v[80:83], v[184:187], 0\n" \
  "s_waitcnt vmcnt(4)\n" \
  "v_rcp_f32 v189, v166\n" \
  "v_mfma_f32_16x16x32_bf16 v[168:171], v[64:67], v[128:131], v[168:171]\n" \
  "v_mul_f32 v146, v188, v146\n" \
  "v_mul_f32 v147, v188, v147\n" \
  "v_mfma_f32_16x16x32_bf16 v[152:155], v[48:51], v[128:131], 0\n" \
  "v_mul_f32 v160, v188, v160\n" \
  "v_mul_f32 v161, v188, v161\n" \
  "v_mfma_f32_16x16x32_bf16 v[172:175], v[84:87], v[184:187], 0\n" \
  "s_waitcnt vmcnt(3)\n" \
  "v_rcp_f32 v190, v170\n" \
  "v_mfma_f32_16x16x32_bf16 v[172:175], v[68:71], v[132:135], v[172:175]\n" \
  "v_cndmask_b32 v167, v188, v189, s[28:29]\n" \
  "v_cndmask_b32 v167, v167, v190, s[26:27]\n" \
  "v_mfma_f32_16x16x32_bf16 v[156:159], v[52:55], v[132:135], 0\n" \
  "v_fmac_f32 v144, v189, v148\n" \
  "v_fmac_f32 v145, v189, v149\n" \
  "v_fmac_f32 v146, v189, v150\n" \
  "v_fmac_f32 v147, v189, v151\n" \
  "v_rcp_f32 v191, v174\n" \
  "v_fmac_f32 v160, v189, v164\n" \
  "v_cndmask_b32 v167, v167, v191, s[24:25]\n" \
  "v_pk_mul_f32 v[176:177], v[176:177], v[166:167] op_sel:[0,1] op_sel_hi:[1,1]\n" \
  "v_pk_mul_f32 v[178:179], v[178:179], v[166:167] op_sel:[0,1] op_sel_hi:[1,1]\n" \
  "v_pk_mul_f32 v[180:181], v[180:181], v[166:167] op_sel:[0,1] op_sel_hi:[1,1]\n" \
  "v_pk_mul_f32 v[182:183], v[182:183], v[166:167] op_sel:[0,1] op_sel_hi:[1,1]\n" \
  "v_cmp_nlt_f32 vcc, 0x0d800000, v167\n" \
  "v_cvt_pk_bf16_f32 v184, v176, v177\n" \
  "v_cvt_pk_bf16_f32 v185, v178, v179\n" \
  "v_cvt_pk_bf16_f32 v186, v180, v181\n" \
  "v_cvt_pk_bf16_f32 v187, v182, v183\n" \
  "v_fmac_f32 v161, v189, v165\n" \
  "v_pk_fma_f32 v[160:161], v[168:169], v[190:191], v[160:161] op_sel:[0,0,0] op_sel_hi:[1,0,1]\n" \
  "v_pk_fma_f32 v[160:161], v[172:173], v[190:191], v[160:161] op_sel:[0,1,0] op_sel_hi:[1,1,1]\n" \
  "v_pk_fma_f32 v[144:145], v[152:153], v[190:191], v[144:145] op_sel:[0,0,0] op_sel_hi:[1,0,1]\n" \
  "v_pk_fma_f32 v[146:147], v[154:155], v[190:191], v[146:147] op_sel:[0,0,0] op_sel_hi:[1,0,1]\n" \
  "v_pk_fma_f32 v[144:145], v[156:157], v[190:191], v[144:145] op_sel:[0,1,0] op_sel_hi:[1,1,1]\n" \
  "v_pk_fma_f32 v[146:147], v[158:159], v[190:191], v[146:147] op_sel:[0,1,0] op_sel_hi:[1,1,1]\n" \
  "s_waitcnt vmcnt(1)\n" \
  "v_mfma_f32_16x16x32_bf16 v[160:163], v[140:143], v[184:187], v[160:163]\n" \
  "s_mov_b64 s[80:81], vcc\n" \
  "v_mfma_f32_16x16x32_bf16 v[144:147], v[136:139], v[184:187], v[144:147]\n" \
  "s_mov_b64 exec, s[30:31]\n" \
  "s_nop 2\n" \
  "ds_write_b64 v22, v[160:161]\n" \
  "v_cmp_u_f32 s[78:79], v160, v161\n" \
  "s_nop 1\n" \
  "ds_write2_b64 v21, v[144:145], v[146:147] offset1:16\n" \
  "v_add_f32 v188, v144, v145\n" \
  "v_add_f32 v189, v146, v147\n" \
  "v_cmp_u_f32 vcc, v188, v189\n" \
  "s_or_b64 s[80:81], s[80:81], s[78:79]\n" \
  "s_or_b64 vcc, vcc, s[80:81]\n" \
  "s_cbranch_vccnz LSF12_%=\n" \
  "LSOK12_%=:\n" \
  "s_mov_b64 exec, -1\n" \
  "s_waitcnt lgkmcnt(0)\n" \
  "s_barrier\n" \
  "s_cmp_eq_u32 s73, 1\n" \
  "s_cbranch_scc1 LWA12_%=\n" \
  "s_branch LDONE_%=\n" \
  "ds_read_b32 v188, v33\n" \
  "s_waitcnt lgkmcnt(0)\n" \
  "v_cmp_ne_u32 vcc, 0, v188\n" \
  "s_cbranch_vccnz LSR12_%=\n" \
  "LNL12_%=:\n" \
  "s_waitcnt lgkmcnt(0)\n" \
  "s_cmp_eq_u32 s73, 1\n" \
  "s_cbranch_scc1 LOOP12_%=\n" \
  "s_branch LDONE_%=\n" \
  "LSA12_%=:\n" \
  "ds_read_b32 v188, v33\n" \
  "s_waitcnt lgkmcnt(0)\n" \
  "v_cmp_ne_u32 vcc, 0, v188\n" \
  "s_cbranch_vccnz LSR12_%=\n" \
  "s_add_u32 s88, s88, s52\n" \
  "s_add_u32 s89, s89, s93\n" \
  "s_add_u32 s97, s97, 1\n" \
  "s_branch LWA12_%=\n" \
  "LSB12_%=:\n" \
  "s_add_u32 s88, s88, s52\n" \
  "s_add_u32 s89, s89, s93\n" \
  "s_add_u32 s97, s97, 1\n" \
  "s_branch LWB12_%=\n" \
  "LMF12_%=:\n" \
  "v_mbcnt_lo_u32_b32 v130, -1, 0\n" \
  "v_mbcnt_hi_u32_b32 v130, -1, v130\n" \
  "v_lshlrev_b32 v130, 4, v130\n" \
  "v_mov_b32 v128, 0\n" \
  "ds_read_b128 v[132:135], v130 offset:0\n" \
  "ds_read_b128 v[136:139], v130 offset:1024\n" \
  "ds_read_b128 v[140:143], v130 offset:2048\n" \
  "ds_read_b128 v[144:147], v130 offset:3072\n" \
  "ds_read_b128 v[148:151], v130 offset:4096\n" \
  "s_waitcnt lgkmcnt(0)\n" \
  "v_max3_u32 v128, v128, v132, v133\n" \
  "v_max3_u32 v128, v128, v134, v135\n" \
  "v_max3_u32 v128, v128, v136, v137\n" \
  "v_max3_u32 v128, v128, v138, v139\n" \
  "v_max3_u32 v128, v128, v140, v141\n" \
  "v_max3_u32 v128, v128, v142, v143\n" \
  "v_max3_u32 v128, v128, v144, v145\n" \
  "v_max3_u32 v128, v128, v146, v147\n" \
  "v_max3_u32 v128, v128, v148, v149\n" \
  "v_max3_u32 v128, v128, v150, v151\n" \
  "ds_read_b128 v[132:135], v130 offset:5120\n" \
  "ds_read_b128 v[136:139], v130 offset:6144\n" \
  "ds_read_b128 v[140:143], v130 offset:7168\n" \
  "ds_read_b128 v[144:147], v130 offset:8192\n" \
  "ds_read_b128 v[148:151], v130 offset:9216\n" \
  "s_waitcnt lgkmcnt(0)\n" \
  "v_max3_u32 v128, v128, v132, v133\n" \
  "v_max3_u32 v128, v128, v134, v135\n" \
  "v_max3_u32 v128, v128, v136, v137\n" \
  "v_max3_u32 v128, v128, v138, v139\n" \
  "v_max3_u32 v128, v128, v140, v141\n" \
  "v_max3_u32 v128, v128, v142, v143\n" \
  "v_max3_u32 v128, v128, v144, v145\n" \
  "v_max3_u32 v128, v128, v146, v147\n" \
  "v_max3_u32 v128, v128, v148, v149\n" \
  "v_max3_u32 v128, v128, v150, v151\n" \
  "v_cmp_eq_u32 vcc, -1, v128\n" \
  "s_cbranch_vccz LMOK12_%=\n" \
  "s_cmp_gt_u32 s58, 4000\n" \
  "s_cbranch_scc1 LMOK12_%=\n" \
  "s_add_u32 s58, s58, 1\n" \
  "s_sub_u32 s64, s64, 1\n" \
  "s_mov_b32 s66, s65\n" \
  "s_add_u32 s88, s88, s52\n" \
  "s_add_u32 s89, s89, s93\n" \
  "s_add_u32 s97, s97, 1\n" \
  "s_waitcnt vmcnt(0) lgkmcnt(0)\n" \
  "s_barrier\n" \
  "s_branch LOOP12_%=\n" \
  "LKE12_%=:\n" \
  "s_waitcnt lgkmcnt(0)\n" \
  "buffer_store_dwordx4 v[88:91], v14, s[36:39], s69 offen sc1\n" \
  "s_branch LDONE_%=\n" \
  "LFF12_%=:\n" \
  "s_add_u32 s71, s64, 0\n" \
  "s_min_u32 s71, s71, 63\n" \
  "s_lshr_b32 s72, s71, 4\n" \
  "s_lshl_b32 s72, s72, 13\n" \
  "s_lshl_b32 s71, s71, 9\n" \
  "s_add_u32 s71, s71, s72\n" \
  "LOF12_%=:\n" \
  "buffer_load_dword v35, v28, s[36:39], s71 offen sc1\n" \
  "s_waitcnt vmcnt(0)\n" \
  "v_cmp_gt_u32 vcc, 16, v35\n" \
  "s_cbranch_vccz LOXF12_%=\n" \
  "s_add_u32 s60, s60, 1\n" \
  "s_cmp_gt_u32 s60, 20000\n" \
  "s_cbranch_scc1 LOXF12_%=\n" \
  "s_sleep 16\n" \
  "s_branch LOF12_%=\n" \
  "LOXF12_%=:\n" \
  "s_branch LFOK12_%=\n" \
  "LSF12_%=:\n" \
  "s_getpc_b64 s[78:79]\n" \
  "s_branch LSF_%=\n" \
  "s_branch LSOK12_%=\n" \
  "LSR12_%=:\n" \
  "v_mov_b32 v188, 0\n" \
  "s_add_u32 s59, s59, 1\n" \
  "s_barrier\n" \
  "ds_write_b32 v33, v188\n" \
  "s_sub_u32 s88, s88, s51\n" \
  "s_sub_u32 s88, s88, s61\n" \
  "s_sub_u32 s89, s89, s91\n" \
  "s_add_u32 s88, s88, s52\n" \
  "s_add_u32 s89, s89, s93\n" \
  "s_add_u32 s97, s97, 1\n" \
  "s_waitcnt lgkmcnt(0)\n" \
  "s_branch LB12_%=\n" \
  "LSTART13_%=:\n" \
  "LOOP13_%=:\n" \
  "v_readlane_b32 s70, v27, s64\n" \
  "s_mov_b32 s65, s66\n" \
  "s_xor_b32 s69, s66, 0x2000\n" \
  "s_add_u32 s64, s64, 1\n" \
  "v_mov_b32 v176, 0\n" \
  "v_mov_b32 v177, 0\n" \
  "v_mov_b32 v178, 0\n" \
  "v_mov_b32 v179, 0\n" \
  "v_mov_b32 v180, 0\n" \
  "v_mov_b32 v181, 0\n" \
  "v_mov_b32 v182, 0\n" \
  "v_mov_b32 v183, 0\n" \
  "v_readlane_b32 s66, v25, s64\n" \
  "v_readlane_b32 s68, v26, s64\n" \
  "s_add_u32 s93, s66, 81920\n" \
  "s_add_u32 s94, s66, 163840\n" \
  "s_mov_b32 m0, 36864\n" \
  "s_nop 0\n" \
  "s_barrier\n" \
  "buffer_load_dwordx4 v3, s[36:39], s70 offen sc1 lds\n" \
  "buffer_load_dwordx4 v3, s[36:39], s70 offen offset:1024 sc1 lds\n" \
  "buffer_load_dwordx4 v3, s[36:39], s70 offen offset:2048 sc1 lds\n" \
  "buffer_load_dwordx4 v3, s[36:39], s70 offen offset:3072 sc1 lds\n" \
  "LM13_%=:\n" \
  "s_barrier\n" \
  "ds_read_b128 v[112:115], v5\n" \
  "ds_read_b128 v[116:119], v5 offset:512\n" \
  "s_barrier\n" \
  "ds_read_b128 v[120:123], v5 offset:1024\n" \
  "ds_read_b128 v[124:127], v5 offset:1536\n" \
  "s_waitcnt lgkmcnt(3)\n" \
  "v_smfmac_f32_16x16x64_bf16 v[176:179], v[112:115], a[0:7], v0\n" \
  "v_smfmac_f32_16x16x64_bf16 v[180:183], v[112:115], a[160:167], v0\n" \
  "s_waitcnt lgkmcnt(2)\n" \
  "v_smfmac_f32_16x16x64_bf16 v[176:179], v[116:119], a[8:15], v0\n" \
  "s_barrier\n" \
  "ds_read_b128 v[128:131], v5 offset:2048\n" \
  "v_smfmac_f32_16x16x64_bf16 v[180:183], v[116:119], a[168:175], v0\n" \
  "s_waitcnt lgkmcnt(2)\n" \
  "ds_read_b128 v[132:135], v5 offset:2560\n" \
  "v_smfmac_f32_16x16x64_bf16 v[176:179], v[120:123], a[16:23], v0\n" \
  "ds_read_b128 v[136:139], v5 offset:3072\n" \
  "v_smfmac_f32_16x16x64_bf16 v[180:183], v[120:123], a[176:183], v0\n" \
  "s_waitcnt lgkmcnt(3)\n" \
  "ds_read_b128 v[140:143], v5 offset:3584\n" \
  "v_smfmac_f32_16x16x64_bf16 v[176:179], v[124:127], a[24:31], v0\n" \
  "s_barrier\n" \
  "ds_read_b128 v[144:147], v5 offset:4096\n" \
  "v_smfmac_f32_16x16x64_bf16 v[180:183], v[124:127], a[184:191], v0\n" \
  "s_waitcnt lgkmcnt(4)\n" \
  "ds_read_b128 v[148:151], v5 offset:4608\n" \
  "v_smfmac_f32_16x16x64_bf16 v[176:179], v[128:131], a[32:39], v0\n" \
  "ds_read_b128 v[152:155], v5 offset:5120\n" \
  "v_smfmac_f32_16x16x64_bf16 v[180:183], v[128:131], a[192:199], v0\n" \
  "s_waitcnt lgkmcnt(5)\n" \
  "ds_read_b128 v[156:159], v5 offset:5632\n" \
  "v_smfmac_f32_16x16x64_bf16 v[176:179], v[132:135], a[40:47], v0\n" \
  "s_barrier\n" \
  "ds_read_b128 v[160:163], v5 offset:6144\n" \
  "v_smfmac_f32_16x16x64_bf16 v[180:183], v[132:135], a[200:207], v0\n" \
  "s_waitcnt lgkmcnt(6)\n" \
  "ds_read_b128 v[164:167], v5 offset:6656\n" \
  "v_smfmac_f32_16x16x64_bf16 v[176:179], v[136:139], a[48:55], v0\n" \
  "ds_read_b128 v[168:171], v5 offset:7168\n" \
  "v_smfmac_f32_16x16x64_bf16 v[180:183], v[136:139], a[208:215], v0\n" \
  "s_waitcnt lgkmcnt(7)\n" \
  "ds_read_b128 v[172:175], v5 offset:7680\n" \
  "v_smfmac_f32_16x16x64_bf16 v[176:179], v[140:143], a[56:63], v0\n" \
  "s_barrier\n" \
  "ds_read2_b32 v[112:113], v6 offset0:0 offset1:32\n" \
  "v_smfmac_f32_16x16x64_bf16 v[180:183], v[140:143], a[216:223], v0\n" \
  "s_waitcnt lgkmcnt(8)\n" \
  "ds_read2_b32 v[114:115], v6 offset0:64 offset1:96\n" \
  "v_smfmac_f32_16x16x64_bf16 v[176:179], v[144:147], a[64:71], v0\n" \
  "s_waitcnt lgkmcnt(8)\n" \
  "ds_read2_b32 v[116:117], v6 offset0:128 offset1:160\n" \
  "v_smfmac_f32_16x16x64_bf16 v[176:179], v[148:151], a[72:79], v0\n" \
  "s_waitcnt lgkmcnt(8)\n" \
  "ds_read2_b32 v[118:119], v6 offset0:192 offset1:224\n" \
  "v_smfmac_f32_16x16x64_bf16 v[176:179], v[152:155], a[80:87], v0\n" \
  "s_waitcnt lgkmcnt(8)\n" \
  "ds_read2_b32 v[120:121], v7 offset0:0 offset1:32\n" \
  "v_smfmac_f32_16x16x64_bf16 v[176:179], v[156:159], a[88:95], v0\n" \
  "s_waitcnt lgkmcnt(8)\n" \
  "ds_read2_b32 v[122:123], v7 offset0:64 offset1:96\n" \
  "v_smfmac_f32_16x16x64_bf16 v[176:179], v[160:163], a[96:103], v0\n" \
  "s_waitcnt lgkmcnt(8)\n" \
  "ds_read2_b32 v[124:125], v7 offset0:128 offset1:160\n" \
  "v_smfmac_f32_16x16x64_bf16 v[176:179], v[164:167], a[104:111], v0\n" \
  "s_waitcnt lgkmcnt(8)\n" \
  "ds_read2_b32 v[126:127], v7 offset0:192 offset1:224\n" \
  "v_smfmac_f32_16x16x64_bf16 v[176:179], v[168:171], a[112:119], v0\n" \
  "s_waitcnt lgkmcnt(8)\n" \
  "v_smfmac_f32_16x16x64_bf16 v[176:179], v[172:175], a[120:127], v0\n" \
  "s_waitcnt lgkmcnt(6)\n" \
  "v_smfmac_f32_16x16x64_bf16 v[176:179], v[112:115], a[128:135], v0\n" \
  "s_waitcnt lgkmcnt(4)\n" \
  "v_smfmac_f32_16x16x64_bf16 v[176:179], v[116:119], a[136:143], v0\n" \
  "s_waitcnt lgkmcnt(2)\n" \
  "v_smfmac_f32_16x16x64_bf16 v[176:179], v[120:123], a[144:151], v0\n" \
  "s_waitcnt lgkmcnt(0)\n" \
  "v_smfmac_f32_16x16x64_bf16 v[176:179], v[124:127], a[152:159], v0\n" \
  "v_smfmac_f32_16x16x64_bf16 v[180:183], v[144:147], a[224:231], v0\n" \
  "v_smfmac_f32_16x16x64_bf16 v[180:183], v[148:151], a[232:239], v0\n" \
  "v_cvt_pk_bf16_f32 v184, v176, v177\n" \
  "v_smfmac_f32_16x16x64_bf16 v[180:183], v[152:155], a[240:247], v0\n" \
  "v_cvt_pk_bf16_f32 v185, v178, v179\n" \
  "v_smfmac_f32_16x16x64_bf16 v[180:183], v[156:159], a[248:255], v0\n" \
  "ds_write_b64 v8, v[184:185]\n" \
  "v_add_f32 v186, v176, v177\n" \
  "v_smfmac_f32_16x16x64_bf16 v[180:183], v[160:163], v[192:199], v0\n" \
  "v_add_f32 v187, v178, v179\n" \
  "v_smfmac_f32_16x16x64_bf16 v[180:183], v[164:167], v[200:207], v0\n" \
  "v_cmp_u_f32 vcc, v186, v187\n" \
  "v_smfmac_f32_16x16x64_bf16 v[180:183], v[168:171], v[208:215], v0\n" \
  "v_smfmac_f32_16x16x64_bf16 v[180:183], v[172:175], v[216:223], v0\n" \
  "s_cbranch_vccnz LMF13_%=\n" \
  "LMOK13_%=:\n" \
  "v_smfmac_f32_16x16x64_bf16 v[180:183], v[112:115], v[224:231], v0\n" \
  "s_waitcnt lgkmcnt(0)\n" \
  "s_barrier\n" \
  "v_smfmac_f32_16x16x64_bf16 v[180:183], v[116:119], v[232:239], v0\n" \
  "v_smfmac_f32_16x16x64_bf16 v[180:183], v[120:123], v[240:247], v0\n" \
  "v_smfmac_f32_16x16x64_bf16 v[180:183], v[124:127], v[248:255], v0\n" \
  "s_nop 3\n" \
  "v_mov_b32 v31, v31\n" \
  "s_nop 2\n" \
  "v_cvt_pk_bf16_f32 v186, v180, v181\n" \
  "v_cvt_pk_bf16_f32 v187, v182, v183\n" \
  "ds_write_b64 v9, v[186:187]\n" \
  "s_waitcnt lgkmcnt(0)\n" \
  "s_barrier\n" \
  "s_nop 15\n" \
  "s_nop 0\n" \
  "ds_read_b64_tr_b16 v[88:89], v12\n" \
  "ds_read_b64_tr_b16 v[90:91], v12 offset:32\n" \
  "s_mov_b32 s67, s66\n" \
  "s_cmp_eq_u32 s64, 64\n" \
  "s_cbranch_scc1 LKE13_%=\n" \
  "LB13_%=:\n" \
  "s_barrier\n" \
  "buffer_load_dwordx4 v[116:119], v15, s[36:39], s65 offen offset:128 sc1\n" \
  "s_barrier\n" \
  "buffer_load_dwordx4 v[112:115], v15, s[36:39], s65 offen sc1\n" \
  "s_barrier\n" \
  "buffer_load_dwordx4 v[120:123], v16, s[36:39], s65 offen sc1\n" \
  "s_barrier\n" \
  "buffer_load_dwordx4 v[124:127], v16, s[36:39], s65 offen offset:512 sc1\n" \
  "s_barrier\n" \
  "buffer_load_dwordx4 v[128:131], v16, s[36:39], s65 offen offset:1024 sc1\n" \
  "s_waitcnt vmcnt(5)\n" \
  "s_barrier\n" \
  "buffer_load_dwordx4 v[132:135], v16, s[36:39], s65 offen offset:1536 sc1\n" \
  "ds_read_b128 v[40:43], v19\n" \
  "ds_read_b128 v[44:47], v19 offset:512\n" \
  "ds_read_b128 v[48:51], v19 offset:1024\n" \
  "ds_read_b128 v[52:55], v19 offset:1536\n" \
  "s_mov_b64 exec, s[46:47]\n" \
  "ds_read_b128 v[56:59], v20\n" \
  "ds_read_b128 v[60:63], v20 offset:128\n" \
  "ds_read_b128 v[64:67], v20 offset:256\n" \
  "ds_read_b128 v[68:71], v20 offset:384\n" \
  "s_mov_b64 exec, -1\n" \
  "s_nop 15\n" \
  "s_nop 1\n" \
  "buffer_load_dwordx4 v[136:139], v17, s[36:39], s65 offen sc1\n" \
  "buffer_load_dwordx4 v[140:143], v18, s[36:39], s65 offen sc1\n" \
  "s_barrier\n" \
  "s_waitcnt lgkmcnt(0)\n" \
  "buffer_store_dwordx4 v[88:91], v14, s[36:39], s69 offen sc1\n" \
  "s_waitcnt lgkmcnt(0)\n" \
  "s_getreg_b32 s71, hwreg(HW_REG_IB_STS, 0, 4)\n" \
  "s_cmp_le_u32 s71, 7\n" \
  "v_mfma_f32_16x16x32_bf16 v[144:147], v[72:75], v[72:75], 0\n" \
  "s_cbranch_scc1 LHXS13_%=\n" \
  "s_getreg_b32 s71, hwreg(HW_REG_IB_STS, 0, 4)\n" \
  "s_cmp_le_u32 s71, 7\n" \
  "v_mfma_f32_16x16x32_bf16 v[144:147], v[72:75], v[72:75], 0\n" \
  "s_cbranch_scc1 LHXS13_%=\n" \
  "s_getreg_b32 s71, hwreg(HW_REG_IB_STS, 0, 4)\n" \
  "s_cmp_le_u32 s71, 7\n" \
  "v_mfma_f32_16x16x32_bf16 v[144:147], v[72:75], v[72:75], 0\n" \
  "s_cbranch_scc1 LHXS13_%=\n" \
  "s_getreg_b32 s71, hwreg(HW_REG_IB_STS, 0, 4)\n" \
  "s_cmp_le_u32 s71, 7\n" \
  "v_mfma_f32_16x16x32_bf16 v[144:147], v[72:75], v[72:75], 0\n" \
  "s_cbranch_scc1 LHXS13_%=\n" \
  "s_getreg_b32 s71, hwreg(HW_REG_IB_STS, 0, 4)\n" \
  "s_cmp_le_u32 s71, 7\n" \
  "v_mfma_f32_16x16x32_bf16 v[144:147], v[72:75], v[72:75], 0\n" \
  "s_cbranch_scc1 LHXS13_%=\n" \
  "s_getreg_b32 s71, hwreg(HW_REG_IB_STS, 0, 4)\n" \
  "s_cmp_le_u32 s71, 7\n" \
  "v_mfma_f32_16x16x32_bf16 v[144:147], v[72:75], v[72:75], 0\n" \
  "s_cbranch_scc1 LHXS13_%=\n" \
  "s_getreg_b32 s71, hwreg(HW_REG_IB_STS, 0, 4)\n" \
  "s_cmp_le_u32 s71, 7\n" \
  "v_mfma_f32_16x16x32_bf16 v[144:147], v[72:75], v[72:75], 0\n" \
  "s_cbranch_scc1 LHXS13_%=\n" \
  "s_getreg_b32 s71, hwreg(HW_REG_IB_STS, 0, 4)\n" \
  "s_cmp_le_u32 s71, 7\n" \
  "v_mfma_f32_16x16x32_bf16 v[144:147], v[72:75], v[72:75], 0\n" \
  "s_cbranch_scc1 LHXS13_%=\n" \
  "s_getreg_b32 s71, hwreg(HW_REG_IB_STS, 0, 4)\n" \
  "s_cmp_le_u32 s71, 7\n" \
  "v_mfma_f32_16x16x32_bf16 v[144:147], v[72:75], v[72:75], 0\n" \
  "s_cbranch_scc1 LHXS13_%=\n" \
  "s_getreg_b32 s71, hwreg(HW_REG_IB_STS, 0, 4)\n" \
  "s_cmp_le_u32 s71, 7\n" \
  "v_mfma_f32_16x16x32_bf16 v[144:147], v[72:75], v[72:75], 0\n" \
  "s_cbranch_scc1 LHXS13_%=\n" \
  "s_getreg_b32 s71, hwreg(HW_REG_IB_STS, 0, 4)\n" \
  "s_cmp_le_u32 s71, 7\n" \
  "v_mfma_f32_16x16x32_bf16 v[144:147], v[72:75], v[72:75], 0\n" \
  "s_cbranch_scc1 LHXS13_%=\n" \
  "s_getreg_b32 s71, hwreg(HW_REG_IB_STS, 0, 4)\n" \
  "s_cmp_le_u32 s71, 7\n" \
  "v_mfma_f32_16x16x32_bf16 v[144:147], v[72:75], v[72:75], 0\n" \
  "s_cbranch_scc1 LHXS13_%=\n" \
  "s_getreg_b32 s71, hwreg(HW_REG_IB_STS, 0, 4)\n" \
  "s_cmp_le_u32 s71, 7\n" \
  "v_mfma_f32_16x16x32_bf16 v[144:147], v[72:75], v[72:75], 0\n" \
  "s_cbranch_scc1 LHXS13_%=\n" \
  "s_getreg_b32 s71, hwreg(HW_REG_IB_STS, 0, 4)\n" \
  "s_cmp_le_u32 s71, 7\n" \
  "v_mfma_f32_16x16x32_bf16 v[144:147], v[72:75], v[72:75], 0\n" \
  "s_cbranch_scc1 LHXS13_%=\n" \
  "s_getreg_b32 s71, hwreg(HW_REG_IB_STS, 0, 4)\n" \
  "s_cmp_le_u32 s71, 7\n" \
  "v_mfma_f32_16x16x32_bf16 v[144:147], v[72:75], v[72:75], 0\n" \
  "s_cbranch_scc1 LHXS13_%=\n" \
  "s_getreg_b32 s71, hwreg(HW_REG_IB_STS, 0, 4)\n" \
  "s_cmp_le_u32 s71, 7\n" \
  "v_mfma_f32_16x16x32_bf16 v[144:147], v[72:75], v[72:75], 0\n" \
  "s_cbranch_scc1 LHXS13_%=\n" \
  "s_getreg_b32 s71, hwreg(HW_REG_IB_STS, 0, 4)\n" \
  "s_cmp_le_u32 s71, 7\n" \
  "v_mfma_f32_16x16x32_bf16 v[144:147], v[72:75], v[72:75], 0\n" \
  "s_cbranch_scc1 LHXS13_%=\n" \
  "s_getreg_b32 s71, hwreg(HW_REG_IB_STS, 0, 4)\n" \
  "s_cmp_le_u32 s71, 7\n" \
  "v_mfma_f32_16x16x32_bf16 v[144:147], v[72:75], v[72:75], 0\n" \
  "s_cbranch_scc1 LHXS13_%=\n" \
  "s_getreg_b32 s71, hwreg(HW_REG_IB_STS, 0, 4)\n" \
  "s_cmp_le_u32 s71, 7\n" \
  "v_mfma_f32_16x16x32_bf16 v[144:147], v[72:75], v[72:75], 0\n" \
  "s_cbranch_scc1 LHXS13_%=\n" \
  "s_getreg_b32 s71, hwreg(HW_REG_IB_STS, 0, 4)\n" \
  "s_cmp_le_u32 s71, 7\n" \
  "v_mfma_f32_16x16x32_bf16 v[144:147], v[72:75], v[72:75], 0\n" \
  "s_cbranch_scc1 LHXS13_%=\n" \
  "LHXS13_%=:\n" \
  "s_waitcnt vmcnt(7)\n" \
  "LS213_%=:\n" \
  "v_dot2_f32_bf16 v176, v112, s22, 0\n" \
  "v_dot2_f32_bf16 v177, v112, s23, 0\n" \
  "v_dot2_f32_bf16 v178, v113, s22, 0\n" \
  "v_dot2_f32_bf16 v179, v113, s23, 0\n" \
  "v_dot2_f32_bf16 v180, v114, s22, 0\n" \
  "v_dot2_f32_bf16 v181, v114, s23, 0\n" \
  "v_dot2_f32_bf16 v182, v115, s22, 0\n" \
  "v_dot2_f32_bf16 v183, v115, s23, 0\n" \
  "v_dot2_f32_bf16 v176, v116, s22, v176\n" \
  "v_dot2_f32_bf16 v177, v116, s23, v177\n" \
  "v_dot2_f32_bf16 v178, v117, s22, v178\n" \
  "v_dot2_f32_bf16 v179, v117, s23, v179\n" \
  "v_dot2_f32_bf16 v180, v118, s22, v180\n" \
  "v_dot2_f32_bf16 v181, v118, s23, v181\n" \
  "v_dot2_f32_bf16 v182, v119, s22, v182\n" \
  "v_dot2_f32_bf16 v183, v119, s23, v183\n" \
  "v_exp_f32 v176, v176\n" \
  "v_exp_f32 v177, v177\n" \
  "v_exp_f32 v178, v178\n" \
  "v_exp_f32 v179, v179\n" \
  "s_waitcnt vmcnt(6)\n" \
  "v_mfma_f32_16x16x32_bf16 v[160:163], v[56:59], v[120:123], 0\n" \
  "v_exp_f32 v180, v180\n" \
  "v_mfma_f32_16x16x32_bf16 v[144:147], v[40:43], v[120:123], 0\n" \
  "v_exp_f32 v181, v181\n" \
  "v_exp_f32 v182, v182\n" \
  "v_exp_f32 v183, v183\n" \
  "v_cvt_pk_bf16_f32 v184, v176, v177\n" \
  "v_cvt_pk_bf16_f32 v185, v178, v179\n" \
  "v_cvt_pk_bf16_f32 v186, v180, v181\n" \
  "v_cvt_pk_bf16_f32 v187, v182, v183\n" \
  "s_nop 1\n" \
  "v_mfma_f32_16x16x32_bf16 v[160:163], v[72:75], v[184:187], v[160:163]\n" \
  "v_mfma_f32_16x16x32_bf16 v[164:167], v[76:79], v[184:187], 0\n" \
  "s_waitcnt vmcnt(5)\n" \
  "v_mfma_f32_16x16x32_bf16 v[164:167], v[60:63], v[124:127], v[164:167]\n" \
  "v_rcp_f32 v188, v162\n" \
  "v_mfma_f32_16x16x32_bf16 v[148:151], v[44:47], v[124:127], 0\n" \
  "v_mul_f32 v144, v188, v144\n" \
  "v_mul_f32 v145, v188, v145\n" \
  "v_mfma_f32_16x16x32_bf16 v[168:171], v[80:83], v[184:187], 0\n" \
  "s_waitcnt vmcnt(4)\n" \
  "v_rcp_f32 v189, v166\n" \
  "v_mfma_f32_16x16x32_bf16 v[168:171], v[64:67], v[128:131], v[168:171]\n" \
  "v_mul_f32 v146, v188, v146\n" \
  "v_mul_f32 v147, v188, v147\n" \
  "v_mfma_f32_16x16x32_bf16 v[152:155], v[48:51], v[128:131], 0\n" \
  "v_mul_f32 v160, v188, v160\n" \
  "v_mul_f32 v161, v188, v161\n" \
  "v_mfma_f32_16x16x32_bf16 v[172:175], v[84:87], v[184:187], 0\n" \
  "s_waitcnt vmcnt(3)\n" \
  "v_rcp_f32 v190, v170\n" \
  "v_mfma_f32_16x16x32_bf16 v[172:175], v[68:71], v[132:135], v[172:175]\n" \
  "v_cndmask_b32 v167, v188, v189, s[28:29]\n" \
  "v_cndmask_b32 v167, v167, v190, s[26:27]\n" \
  "v_mfma_f32_16x16x32_bf16 v[156:159], v[52:55], v[132:135], 0\n" \
  "v_fmac_f32 v144, v189, v148\n" \
  "v_fmac_f32 v145, v189, v149\n" \
  "v_fmac_f32 v146, v189, v150\n" \
  "v_fmac_f32 v147, v189, v151\n" \
  "v_rcp_f32 v191, v174\n" \
  "v_fmac_f32 v160, v189, v164\n" \
  "v_cndmask_b32 v167, v167, v191, s[24:25]\n" \
  "v_pk_mul_f32 v[176:177], v[176:177], v[166:167] op_sel:[0,1] op_sel_hi:[1,1]\n" \
  "v_pk_mul_f32 v[178:179], v[178:179], v[166:167] op_sel:[0,1] op_sel_hi:[1,1]\n" \
  "v_pk_mul_f32 v[180:181], v[180:181], v[166:167] op_sel:[0,1] op_sel_hi:[1,1]\n" \
  "v_pk_mul_f32 v[182:183], v[182:183], v[166:167] op_sel:[0,1] op_sel_hi:[1,1]\n" \
  "v_cmp_nlt_f32 vcc, 0x0d800000, v167\n" \
  "v_cvt_pk_bf16_f32 v184, v176, v177\n" \
  "v_cvt_pk_bf16_f32 v185, v178, v179\n" \
  "v_cvt_pk_bf16_f32 v186, v180, v181\n" \
  "v_cvt_pk_bf16_f32 v187, v182, v183\n" \
  "v_fmac_f32 v161, v189, v165\n" \
  "v_pk_fma_f32 v[160:161], v[168:169], v[190:191], v[160:161] op_sel:[0,0,0] op_sel_hi:[1,0,1]\n" \
  "v_pk_fma_f32 v[160:161], v[172:173], v[190:191], v[160:161] op_sel:[0,1,0] op_sel_hi:[1,1,1]\n" \
  "v_pk_fma_f32 v[144:145], v[152:153], v[190:191], v[144:145] op_sel:[0,0,0] op_sel_hi:[1,0,1]\n" \
  "v_pk_fma_f32 v[146:147], v[154:155], v[190:191], v[146:147] op_sel:[0,0,0] op_sel_hi:[1,0,1]\n" \
  "v_pk_fma_f32 v[144:145], v[156:157], v[190:191], v[144:145] op_sel:[0,1,0] op_sel_hi:[1,1,1]\n" \
  "v_pk_fma_f32 v[146:147], v[158:159], v[190:191], v[146:147] op_sel:[0,1,0] op_sel_hi:[1,1,1]\n" \
  "s_waitcnt vmcnt(1)\n" \
  "v_mfma_f32_16x16x32_bf16 v[160:163], v[140:143], v[184:187], v[160:163]\n" \
  "s_mov_b64 s[80:81], vcc\n" \
  "v_mfma_f32_16x16x32_bf16 v[144:147], v[136:139], v[184:187], v[144:147]\n" \
  "s_mov_b64 exec, s[30:31]\n" \
  "s_nop 2\n" \
  "ds_write_b64 v22, v[160:161]\n" \
  "v_cmp_u_f32 s[78:79], v160, v161\n" \
  "s_nop 1\n" \
  "ds_write2_b64 v21, v[144:145], v[146:147] offset1:16\n" \
  "v_add_f32 v188, v144, v145\n" \
  "v_add_f32 v189, v146, v147\n" \
  "v_cmp_u_f32 vcc, v188, v189\n" \
  "s_or_b64 s[80:81], s[80:81], s[78:79]\n" \
  "s_or_b64 vcc, vcc, s[80:81]\n" \
  "s_cbranch_vccnz LSF13_%=\n" \
  "LSOK13_%=:\n" \
  "s_mov_b64 exec, -1\n" \
  "s_waitcnt lgkmcnt(0)\n" \
  "s_barrier\n" \
  "ds_read_b32 v188, v33\n" \
  "ds_read_b128 v[120:123], v23\n" \
  "ds_read_b128 v[124:127], v23 offset:768\n" \
  "ds_read_b128 v[128:131], v23 offset:1536\n" \
  "ds_read_b128 v[132:135], v23 offset:2304\n" \
  "s_waitcnt lgkmcnt(4)\n" \
  "v_cmp_ne_u32 vcc, 0, v188\n" \
  "s_waitcnt lgkmcnt(2)\n" \
  "v_pk_add_f32 v[120:121], v[120:121], v[122:123]\n" \
  "v_pk_add_f32 v[124:125], v[124:125], v[126:127]\n" \
  "v_pk_add_f32 v[120:121], v[120:121], v[124:125]\n" \
  "s_waitcnt lgkmcnt(1)\n" \
  "v_pk_add_f32 v[128:129], v[128:129], v[130:131]\n" \
  "v_pk_add_f32 v[120:121], v[120:121], v[128:129]\n" \
  "s_waitcnt lgkmcnt(0)\n" \
  "v_pk_add_f32 v[132:133], v[132:133], v[134:135]\n" \
  "v_pk_add_f32 v[120:121], v[120:121], v[132:133]\n" \
  "v_cvt_pk_bf16_f32 v120, v120, v121\n" \
  "s_cbranch_vccnz LSR13_%=\n" \
  "buffer_store_dword v120, v24, s[36:39], s66 offen sc1\n" \
  "buffer_store_dword v120, v24, s[36:39], s93 offen sc1\n" \
  "buffer_store_dword v120, v24, s[36:39], s94 offen sc1\n" \
  "LNL13_%=:\n" \
  "s_waitcnt lgkmcnt(0)\n" \
  "s_cmp_eq_u32 s73, 1\n" \
  "s_cbranch_scc1 LOOP13_%=\n" \
  "s_branch LDONE_%=\n" \
  "LMF13_%=:\n" \
  "v_mbcnt_lo_u32_b32 v130, -1, 0\n" \
  "v_mbcnt_hi_u32_b32 v130, -1, v130\n" \
  "v_lshlrev_b32 v130, 4, v130\n" \
  "v_mov_b32 v128, 0\n" \
  "ds_read_b128 v[132:135], v130 offset:0\n" \
  "ds_read_b128 v[136:139], v130 offset:1024\n" \
  "ds_read_b128 v[140:143], v130 offset:2048\n" \
  "ds_read_b128 v[144:147], v130 offset:3072\n" \
  "ds_read_b128 v[148:151], v130 offset:4096\n" \
  "s_waitcnt lgkmcnt(0)\n" \
  "v_max3_u32 v128, v128, v132, v133\n" \
  "v_max3_u32 v128, v128, v134, v135\n" \
  "v_max3_u32 v128, v128, v136, v137\n" \
  "v_max3_u32 v128, v128, v138, v139\n" \
  "v_max3_u32 v128, v128, v140, v141\n" \
  "v_max3_u32 v128, v128, v142, v143\n" \
  "v_max3_u32 v128, v128, v144, v145\n" \
  "v_max3_u32 v128, v128, v146, v147\n" \
  "v_max3_u32 v128, v128, v148, v149\n" \
  "v_max3_u32 v128, v128, v150, v151\n" \
  "ds_read_b128 v[132:135], v130 offset:5120\n" \
  "ds_read_b128 v[136:139], v130 offset:6144\n" \
  "ds_read_b128 v[140:143], v130 offset:7168\n" \
  "ds_read_b128 v[144:147], v130 offset:8192\n" \
  "ds_read_b128 v[148:151], v130 offset:9216\n" \
  "s_waitcnt lgkmcnt(0)\n" \
  "v_max3_u32 v128, v128, v132, v133\n" \
  "v_max3_u32 v128, v128, v134, v135\n" \
  "v_max3_u32 v128, v128, v136, v137\n" \
  "v_max3_u32 v128, v128, v138, v139\n" \
  "v_max3_u32 v128, v128, v140, v141\n" \
  "v_max3_u32 v128, v128, v142, v143\n" \
  "v_max3_u32 v128, v128, v144, v145\n" \
  "v_max3_u32 v128, v128, v146, v147\n" \
  "v_max3_u32 v128, v128, v148, v149\n" \
  "v_max3_u32 v128, v128, v150, v151\n" \
  "v_cmp_eq_u32 vcc, -1, v128\n" \
  "s_cbranch_vccz LMOK13_%=\n" \
  "s_cmp_gt_u32 s58, 4000\n" \
  "s_cbranch_scc1 LMOK13_%=\n" \
  "s_add_u32 s58, s58, 1\n" \
  "s_sub_u32 s64, s64, 1\n" \
  "s_mov_b32 s66, s65\n" \
  "s_waitcnt vmcnt(0) lgkmcnt(0)\n" \
  "s_barrier\n" \
  "s_branch LOOP13_%=\n" \
  "LKE13_%=:\n" \
  "s_waitcnt lgkmcnt(0)\n" \
  "buffer_store_dwordx4 v[88:91], v14, s[36:39], s69 offen sc1\n" \
  "s_branch LDONE_%=\n" \
  "LSF13_%=:\n" \
  "s_getpc_b64 s[78:79]\n" \
  "s_branch LSF_%=\n" \
  "s_branch LSOK13_%=\n" \
  "LSR13_%=:\n" \
  "v_mov_b32 v188, 0\n" \
  "s_add_u32 s59, s59, 1\n" \
  "s_barrier\n" \
  "ds_write_b32 v33, v188\n" \
  "s_waitcnt lgkmcnt(0)\n" \
  "s_branch LB13_%=\n" \
  "LSF_%=:\n" \
  "s_mov_b64 exec, -1\n" \
  "v_mov_b32 v188, 0\n" \
  "v_max3_u32 v188, v188, v112, v113\n" \
  "v_max3_u32 v188, v188, v114, v115\n" \
  "v_max3_u32 v188, v188, v116, v117\n" \
  "v_max3_u32 v188, v188, v118, v119\n" \
  "v_max3_u32 v188, v188, v120, v121\n" \
  "v_max3_u32 v188, v188, v122, v123\n" \
  "v_max3_u32 v188, v188, v124, v125\n" \
  "v_max3_u32 v188, v188, v126, v127\n" \
  "v_max3_u32 v188, v188, v128, v129\n" \
  "v_max3_u32 v188, v188, v130, v131\n" \
  "v_max3_u32 v188, v188, v132, v133\n" \
  "v_max3_u32 v188, v188, v134, v135\n" \
  "v_max3_u32 v188, v188, v136, v137\n" \
  "v_max3_u32 v188, v188, v138, v139\n" \
  "v_max3_u32 v188, v188, v140, v141\n" \
  "v_max3_u32 v188, v188, v142, v143\n" \
  "v_cmp_eq_u32 vcc, -1, v188\n" \
  "s_cbranch_vccz LSG_%=\n" \
  "s_cmp_gt_u32 s59, 4000\n" \
  "s_cbranch_scc1 LSG_%=\n" \
  "ds_write_b32 v33, v32\n" \
  "s_add_u32 s78, s78, 4\n" \
  "s_addc_u32 s79, s79, 0\n" \
  "s_setpc_b64 s[78:79]\n" \
  "LSG_%=:\n" \
  "v_dot2_f32_bf16 v176, v112, s22, 0\n" \
  "v_dot2_f32_bf16 v176, v116, s22, v176\n" \
  "v_dot2_f32_bf16 v177, v112, s23, 0\n" \
  "v_dot2_f32_bf16 v177, v116, s23, v177\n" \
  "v_dot2_f32_bf16 v178, v113, s22, 0\n" \
  "v_dot2_f32_bf16 v178, v117, s22, v178\n" \
  "v_dot2_f32_bf16 v179, v113, s23, 0\n" \
  "v_dot2_f32_bf16 v179, v117, s23, v179\n" \
  "v_dot2_f32_bf16 v180, v114, s22, 0\n" \
  "v_dot2_f32_bf16 v180, v118, s22, v180\n" \
  "v_dot2_f32_bf16 v181, v114, s23, 0\n" \
  "v_dot2_f32_bf16 v181, v118, s23, v181\n" \
  "v_dot2_f32_bf16 v182, v115, s22, 0\n" \
  "v_dot2_f32_bf16 v182, v119, s22, v182\n" \
  "v_dot2_f32_bf16 v183, v115, s23, 0\n" \
  "v_dot2_f32_bf16 v183, v119, s23, v183\n" \
  "s_nop 2\n" \
  "v_max3_f32 v158, v176, v177, v178\n" \
  "v_max3_f32 v158, v158, v179, v180\n" \
  "v_max3_f32 v158, v158, v181, v182\n" \
  "v_max_f32 v158, v158, v183\n" \
  "s_lshr_b32 s72, s49, 5\n" \
  "s_sub_u32 s72, 3, s72\n" \
  "s_mul_i32 s72, s72, 81920\n" \
  "s_add_u32 s72, s65, s72\n" \
  "v_mov_b32 v157, 0x7fd57fd5\n" \
  "v_cmp_eq_u32 s[80:81], v120, v157\n" \
  "LSP0_%=:\n" \
  "buffer_load_dwordx4 v[152:155], v16, s[36:39], s72 offen sc1\n" \
  "s_waitcnt vmcnt(0)\n" \
  "v_max3_u32 v156, v152, v153, v154\n" \
  "v_max_u32 v156, v156, v155\n" \
  "v_cmp_eq_u32 vcc, -1, v156\n" \
  "s_and_b64 vcc, vcc, s[80:81]\n" \
  "s_cbranch_vccz LSQ0_%=\n" \
  "s_add_u32 s60, s60, 1\n" \
  "s_cmp_gt_u32 s60, 20000\n" \
  "s_cbranch_scc1 LSQ0_%=\n" \
  "s_sleep 8\n" \
  "s_branch LSP0_%=\n" \
  "LSQ0_%=:\n" \
  "v_lshlrev_b32 v144, 16, v120\n" \
  "v_log_f32 v144, v144\n" \
  "v_lshlrev_b32 v157, 16, v152\n" \
  "v_cndmask_b32 v144, v144, v157, s[80:81]\n" \
  "v_and_b32 v145, 0xffff0000, v120\n" \
  "v_log_f32 v145, v145\n" \
  "v_and_b32 v157, 0xffff0000, v152\n" \
  "v_cndmask_b32 v145, v145, v157, s[80:81]\n" \
  "v_lshlrev_b32 v146, 16, v121\n" \
  "v_log_f32 v146, v146\n" \
  "v_lshlrev_b32 v157, 16, v153\n" \
  "v_cndmask_b32 v146, v146, v157, s[80:81]\n" \
  "v_and_b32 v147, 0xffff0000, v121\n" \
  "v_log_f32 v147, v147\n" \
  "v_and_b32 v157, 0xffff0000, v153\n" \
  "v_cndmask_b32 v147, v147, v157, s[80:81]\n" \
  "v_lshlrev_b32 v148, 16, v122\n" \
  "v_log_f32 v148, v148\n" \
  "v_lshlrev_b32 v157, 16, v154\n" \
  "v_cndmask_b32 v148, v148, v157, s[80:81]\n" \
  "v_and_b32 v149, 0xffff0000, v122\n" \
  "v_log_f32 v149, v149\n" \
  "v_and_b32 v157, 0xffff0000, v154\n" \
  "v_cndmask_b32 v149, v149, v157, s[80:81]\n" \
  "v_lshlrev_b32 v150, 16, v123\n" \
  "v_log_f32 v150, v150\n" \
  "v_lshlrev_b32 v157, 16, v155\n" \
  "v_cndmask_b32 v150, v150, v157, s[80:81]\n" \
  "v_and_b32 v151, 0xffff0000, v123\n" \
  "v_log_f32 v151, v151\n" \
  "v_and_b32 v157, 0xffff0000, v155\n" \
  "v_cndmask_b32 v151, v151, v157, s[80:81]\n" \
  "v_max3_f32 v156, v144, v145, v146\n" \
  "v_max3_f32 v156, v156, v147, v148\n" \
  "v_max3_f32 v156, v156, v149, v150\n" \
  "v_max_f32 v156, v156, v151\n" \
  "s_mov_b64 exec, 0xffff\n" \
  "v_max_f32 v156, v156, v158\n" \
  "s_mov_b64 exec, -1\n" \
  "v_mov_b32 v157, v156\n" \
  "s_nop 1\n" \
  "v_permlane16_swap_b32 v156, v157\n" \
  "v_max_f32 v156, v156, v157\n" \
  "v_mov_b32 v157, v156\n" \
  "s_nop 1\n" \
  "v_permlane32_swap_b32 v156, v157\n" \
  "v_max_f32 v156, v156, v157\n" \
  "s_mov_b64 exec, 0xffff\n" \
  "v_mov_b32 v188, v156\n" \
  "s_mov_b64 exec, -1\n" \
  "v_sub_f32 v144, v144, v156\n" \
  "v_exp_f32 v144, v144\n" \
  "v_sub_f32 v145, v145, v156\n" \
  "v_exp_f32 v145, v145\n" \
  "v_sub_f32 v146, v146, v156\n" \
  "v_exp_f32 v146, v146\n" \
  "v_sub_f32 v147, v147, v156\n" \
  "v_exp_f32 v147, v147\n" \
  "v_sub_f32 v148, v148, v156\n" \
  "v_exp_f32 v148, v148\n" \
  "v_sub_f32 v149, v149, v156\n" \
  "v_exp_f32 v149, v149\n" \
  "v_sub_f32 v150, v150, v156\n" \
  "v_exp_f32 v150, v150\n" \
  "v_sub_f32 v151, v151, v156\n" \
  "v_exp_f32 v151, v151\n" \
  "v_cvt_pk_bf16_f32 v120, v144, v145\n" \
  "v_cvt_pk_bf16_f32 v121, v146, v147\n" \
  "v_cvt_pk_bf16_f32 v122, v148, v149\n" \
  "v_cvt_pk_bf16_f32 v123, v150, v151\n" \
  "v_mov_b32 v157, 0x7fd57fd5\n" \
  "v_cmp_eq_u32 s[80:81], v124, v157\n" \
  "LSP1_%=:\n" \
  "buffer_load_dwordx4 v[152:155], v16, s[36:39], s72 offen offset:512 sc1\n" \
  "s_waitcnt vmcnt(0)\n" \
  "v_max3_u32 v156, v152, v153, v154\n" \
  "v_max_u32 v156, v156, v155\n" \
  "v_cmp_eq_u32 vcc, -1, v156\n" \
  "s_and_b64 vcc, vcc, s[80:81]\n" \
  "s_cbranch_vccz LSQ1_%=\n" \
  "s_add_u32 s60, s60, 1\n" \
  "s_cmp_gt_u32 s60, 20000\n" \
  "s_cbranch_scc1 LSQ1_%=\n" \
  "s_sleep 8\n" \
  "s_branch LSP1_%=\n" \
  "LSQ1_%=:\n" \
  "v_lshlrev_b32 v144, 16, v124\n" \
  "v_log_f32 v144, v144\n" \
  "v_lshlrev_b32 v157, 16, v152\n" \
  "v_cndmask_b32 v144, v144, v157, s[80:81]\n" \
  "v_and_b32 v145, 0xffff0000, v124\n" \
  "v_log_f32 v145, v145\n" \
  "v_and_b32 v157, 0xffff0000, v152\n" \
  "v_cndmask_b32 v145, v145, v157, s[80:81]\n" \
  "v_lshlrev_b32 v146, 16, v125\n" \
  "v_log_f32 v146, v146\n" \
  "v_lshlrev_b32 v157, 16, v153\n" \
  "v_cndmask_b32 v146, v146, v157, s[80:81]\n" \
  "v_and_b32 v147, 0xffff0000, v125\n" \
  "v_log_f32 v147, v147\n" \
  "v_and_b32 v157, 0xffff0000, v153\n" \
  "v_cndmask_b32 v147, v147, v157, s[80:81]\n" \
  "v_lshlrev_b32 v148, 16, v126\n" \
  "v_log_f32 v148, v148\n" \
  "v_lshlrev_b32 v157, 16, v154\n" \
  "v_cndmask_b32 v148, v148, v157, s[80:81]\n" \
  "v_and_b32 v149, 0xffff0000, v126\n" \
  "v_log_f32 v149, v149\n" \
  "v_and_b32 v157, 0xffff0000, v154\n" \
  "v_cndmask_b32 v149, v149, v157, s[80:81]\n" \
  "v_lshlrev_b32 v150, 16, v127\n" \
  "v_log_f32 v150, v150\n" \
  "v_lshlrev_b32 v157, 16, v155\n" \
  "v_cndmask_b32 v150, v150, v157, s[80:81]\n" \
  "v_and_b32 v151, 0xffff0000, v127\n" \
  "v_log_f32 v151, v151\n" \
  "v_and_b32 v157, 0xffff0000, v155\n" \
  "v_cndmask_b32 v151, v151, v157, s[80:81]\n" \
  "v_max3_f32 v156, v144, v145, v146\n" \
  "v_max3_f32 v156, v156, v147, v148\n" \
  "v_max3_f32 v156, v156, v149, v150\n" \
  "v_max_f32 v156, v156, v151\n" \
  "s_mov_b64 exec, s[28:29]\n" \
  "v_max_f32 v156, v156, v158\n" \
  "s_mov_b64 exec, -1\n" \
  "v_mov_b32 v157, v156\n" \
  "s_nop 1\n" \
  "v_permlane16_swap_b32 v156, v157\n" \
  "v_max_f32 v156, v156, v157\n" \
  "v_mov_b32 v157, v156\n" \
  "s_nop 1\n" \
  "v_permlane32_swap_b32 v156, v157\n" \
  "v_max_f32 v156, v156, v157\n" \
  "s_mov_b64 exec, s[28:29]\n" \
  "v_mov_b32 v188, v156\n" \
  "s_mov_b64 exec, -1\n" \
  "v_sub_f32 v144, v144, v156\n" \
  "v_exp_f32 v144, v144\n" \
  "v_sub_f32 v145, v145, v156\n" \
  "v_exp_f32 v145, v145\n" \
  "v_sub_f32 v146, v146, v156\n" \
  "v_exp_f32 v146, v146\n" \
  "v_sub_f32 v147, v147, v156\n" \
  "v_exp_f32 v147, v147\n" \
  "v_sub_f32 v148, v148, v156\n" \
  "v_exp_f32 v148, v148\n" \
  "v_sub_f32 v149, v149, v156\n" \
  "v_exp_f32 v149, v149\n" \
  "v_sub_f32 v150, v150, v156\n" \
  "v_exp_f32 v150, v150\n" \
  "v_sub_f32 v151, v151, v156\n" \
  "v_exp_f32 v151, v151\n" \
  "v_cvt_pk_bf16_f32 v124, v144, v145\n" \
  "v_cvt_pk_bf16_f32 v125, v146, v147\n" \
  "v_cvt_pk_bf16_f32 v126, v148, v149\n" \
  "v_cvt_pk_bf16_f32 v127, v150, v151\n" \
  "v_mov_b32 v157, 0x7fd57fd5\n" \
  "v_cmp_eq_u32 s[80:81], v128, v157\n" \
  "LSP2_%=:\n" \
  "buffer_load_dwordx4 v[152:155], v16, s[36:39], s72 offen offset:1024 sc1\n" \
  "s_waitcnt vmcnt(0)\n" \
  "v_max3_u32 v156, v152, v153, v154\n" \
  "v_max_u32 v156, v156, v155\n" \
  "v_cmp_eq_u32 vcc, -1, v156\n" \
  "s_and_b64 vcc, vcc, s[80:81]\n" \
  "s_cbranch_vccz LSQ2_%=\n" \
  "s_add_u32 s60, s60, 1\n" \
  "s_cmp_gt_u32 s60, 20000\n" \
  "s_cbranch_scc1 LSQ2_%=\n" \
  "s_sleep 8\n" \
  "s_branch LSP2_%=\n" \
  "LSQ2_%=:\n" \
  "v_lshlrev_b32 v144, 16, v128\n" \
  "v_log_f32 v144, v144\n" \
  "v_lshlrev_b32 v157, 16, v152\n" \
  "v_cndmask_b32 v144, v144, v157, s[80:81]\n" \
  "v_and_b32 v145, 0xffff0000, v128\n" \
  "v_log_f32 v145, v145\n" \
  "v_and_b32 v157, 0xffff0000, v152\n" \
  "v_cndmask_b32 v145, v145, v157, s[80:81]\n" \
  "v_lshlrev_b32 v146, 16, v129\n" \
  "v_log_f32 v146, v146\n" \
  "v_lshlrev_b32 v157, 16, v153\n" \
  "v_cndmask_b32 v146, v146, v157, s[80:81]\n" \
  "v_and_b32 v147, 0xffff0000, v129\n" \
  "v_log_f32 v147, v147\n" \
  "v_and_b32 v157, 0xffff0000, v153\n" \
  "v_cndmask_b32 v147, v147, v157, s[80:81]\n" \
  "v_lshlrev_b32 v148, 16, v130\n" \
  "v_log_f32 v148, v148\n" \
  "v_lshlrev_b32 v157, 16, v154\n" \
  "v_cndmask_b32 v148, v148, v157, s[80:81]\n" \
  "v_and_b32 v149, 0xffff0000, v130\n" \
  "v_log_f32 v149, v149\n" \
  "v_and_b32 v157, 0xffff0000, v154\n" \
  "v_cndmask_b32 v149, v149, v157, s[80:81]\n" \
  "v_lshlrev_b32 v150, 16, v131\n" \
  "v_log_f32 v150, v150\n" \
  "v_lshlrev_b32 v157, 16, v155\n" \
  "v_cndmask_b32 v150, v150, v157, s[80:81]\n" \
  "v_and_b32 v151, 0xffff0000, v131\n" \
  "v_log_f32 v151, v151\n" \
  "v_and_b32 v157, 0xffff0000, v155\n" \
  "v_cndmask_b32 v151, v151, v157, s[80:81]\n" \
  "v_max3_f32 v156, v144, v145, v146\n" \
  "v_max3_f32 v156, v156, v147, v148\n" \
  "v_max3_f32 v156, v156, v149, v150\n" \
  "v_max_f32 v156, v156, v151\n" \
  "s_mov_b64 exec, s[26:27]\n" \
  "v_max_f32 v156, v156, v158\n" \
  "s_mov_b64 exec, -1\n" \
  "v_mov_b32 v157, v156\n" \
  "s_nop 1\n" \
  "v_permlane16_swap_b32 v156, v157\n" \
  "v_max_f32 v156, v156, v157\n" \
  "v_mov_b32 v157, v156\n" \
  "s_nop 1\n" \
  "v_permlane32_swap_b32 v156, v157\n" \
  "v_max_f32 v156, v156, v157\n" \
  "s_mov_b64 exec, s[26:27]\n" \
  "v_mov_b32 v188, v156\n" \
  "s_mov_b64 exec, -1\n" \
  "v_sub_f32 v144, v144, v156\n" \
  "v_exp_f32 v144, v144\n" \
  "v_sub_f32 v145, v145, v156\n" \
  "v_exp_f32 v145, v145\n" \
  "v_sub_f32 v146, v146, v156\n" \
  "v_exp_f32 v146, v146\n" \
  "v_sub_f32 v147, v147, v156\n" \
  "v_exp_f32 v147, v147\n" \
  "v_sub_f32 v148, v148, v156\n" \
  "v_exp_f32 v148, v148\n" \
  "v_sub_f32 v149, v149, v156\n" \
  "v_exp_f32 v149, v149\n" \
  "v_sub_f32 v150, v150, v156\n" \
  "v_exp_f32 v150, v150\n" \
  "v_sub_f32 v151, v151, v156\n" \
  "v_exp_f32 v151, v151\n" \
  "v_cvt_pk_bf16_f32 v128, v144, v145\n" \
  "v_cvt_pk_bf16_f32 v129, v146, v147\n" \
  "v_cvt_pk_bf16_f32 v130, v148, v149\n" \
  "v_cvt_pk_bf16_f32 v131, v150, v151\n" \
  "v_mov_b32 v157, 0x7fd57fd5\n" \
  "v_cmp_eq_u32 s[80:81], v132, v157\n" \
  "LSP3_%=:\n" \
  "buffer_load_dwordx4 v[152:155], v16, s[36:39], s72 offen offset:1536 sc1\n" \
  "s_waitcnt vmcnt(0)\n" \
  "v_max3_u32 v156, v152, v153, v154\n" \
  "v_max_u32 v156, v156, v155\n" \
  "v_cmp_eq_u32 vcc, -1, v156\n" \
  "s_and_b64 vcc, vcc, s[80:81]\n" \
  "s_cbranch_vccz LSQ3_%=\n" \
  "s_add_u32 s60, s60, 1\n" \
  "s_cmp_gt_u32 s60, 20000\n" \
  "s_cbranch_scc1 LSQ3_%=\n" \
  "s_sleep 8\n" \
  "s_branch LSP3_%=\n" \
  "LSQ3_%=:\n" \
  "v_lshlrev_b32 v144, 16, v132\n" \
  "v_log_f32 v144, v144\n" \
  "v_lshlrev_b32 v157, 16, v152\n" \
  "v_cndmask_b32 v144, v144, v157, s[80:81]\n" \
  "v_and_b32 v145, 0xffff0000, v132\n" \
  "v_log_f32 v145, v145\n" \
  "v_and_b32 v157, 0xffff0000, v152\n" \
  "v_cndmask_b32 v145, v145, v157, s[80:81]\n" \
  "v_lshlrev_b32 v146, 16, v133\n" \
  "v_log_f32 v146, v146\n" \
  "v_lshlrev_b32 v157, 16, v153\n" \
  "v_cndmask_b32 v146, v146, v157, s[80:81]\n" \
  "v_and_b32 v147, 0xffff0000, v133\n" \
  "v_log_f32 v147, v147\n" \
  "v_and_b32 v157, 0xffff0000, v153\n" \
  "v_cndmask_b32 v147, v147, v157, s[80:81]\n" \
  "v_lshlrev_b32 v148, 16, v134\n" \
  "v_log_f32 v148, v148\n" \
  "v_lshlrev_b32 v157, 16, v154\n" \
  "v_cndmask_b32 v148, v148, v157, s[80:81]\n" \
  "v_and_b32 v149, 0xffff0000, v134\n" \
  "v_log_f32 v149, v149\n" \
  "v_and_b32 v157, 0xffff0000, v154\n" \
  "v_cndmask_b32 v149, v149, v157, s[80:81]\n" \
  "v_lshlrev_b32 v150, 16, v135\n" \
  "v_log_f32 v150, v150\n" \
  "v_lshlrev_b32 v157, 16, v155\n" \
  "v_cndmask_b32 v150, v150, v157, s[80:81]\n" \
  "v_and_b32 v151, 0xffff0000, v135\n" \
  "v_log_f32 v151, v151\n" \
  "v_and_b32 v157, 0xffff0000, v155\n" \
  "v_cndmask_b32 v151, v151, v157, s[80:81]\n" \
  "v_max3_f32 v156, v144, v145, v146\n" \
  "v_max3_f32 v156, v156, v147, v148\n" \
  "v_max3_f32 v156, v156, v149, v150\n" \
  "v_max_f32 v156, v156, v151\n" \
  "s_mov_b64 exec, s[24:25]\n" \
  "v_max_f32 v156, v156, v158\n" \
  "s_mov_b64 exec, -1\n" \
  "v_mov_b32 v157, v156\n" \
  "s_nop 1\n" \
  "v_permlane16_swap_b32 v156, v157\n" \
  "v_max_f32 v156, v156, v157\n" \
  "v_mov_b32 v157, v156\n" \
  "s_nop 1\n" \
  "v_permlane32_swap_b32 v156, v157\n" \
  "v_max_f32 v156, v156, v157\n" \
  "s_mov_b64 exec, s[24:25]\n" \
  "v_mov_b32 v188, v156\n" \
  "s_mov_b64 exec, -1\n" \
  "v_sub_f32 v144, v144, v156\n" \
  "v_exp_f32 v144, v144\n" \
  "v_sub_f32 v145, v145, v156\n" \
  "v_exp_f32 v145, v145\n" \
  "v_sub_f32 v146, v146, v156\n" \
  "v_exp_f32 v146, v146\n" \
  "v_sub_f32 v147, v147, v156\n" \
  "v_exp_f32 v147, v147\n" \
  "v_sub_f32 v148, v148, v156\n" \
  "v_exp_f32 v148, v148\n" \
  "v_sub_f32 v149, v149, v156\n" \
  "v_exp_f32 v149, v149\n" \
  "v_sub_f32 v150, v150, v156\n" \
  "v_exp_f32 v150, v150\n" \
  "v_sub_f32 v151, v151, v156\n" \
  "v_exp_f32 v151, v151\n" \
  "v_cvt_pk_bf16_f32 v132, v144, v145\n" \
  "v_cvt_pk_bf16_f32 v133, v146, v147\n" \
  "v_cvt_pk_bf16_f32 v134, v148, v149\n" \
  "v_cvt_pk_bf16_f32 v135, v150, v151\n" \
  "v_sub_f32 v176, v176, v188\n" \
  "v_exp_f32 v176, v176\n" \
  "v_sub_f32 v177, v177, v188\n" \
  "v_exp_f32 v177, v177\n" \
  "v_sub_f32 v178, v178, v188\n" \
  "v_exp_f32 v178, v178\n" \
  "v_sub_f32 v179, v179, v188\n" \
  "v_exp_f32 v179, v179\n" \
  "v_sub_f32 v180, v180, v188\n" \
  "v_exp_f32 v180, v180\n" \
  "v_sub_f32 v181, v181, v188\n" \
  "v_exp_f32 v181, v181\n" \
  "v_sub_f32 v182, v182, v188\n" \
  "v_exp_f32 v182, v182\n" \
  "v_sub_f32 v183, v183, v188\n" \
  "v_exp_f32 v183, v183\n" \
  "v_cvt_pk_bf16_f32 v184, v176, v177\n" \
  "v_cvt_pk_bf16_f32 v185, v178, v179\n" \
  "v_cvt_pk_bf16_f32 v186, v180, v181\n" \
  "v_cvt_pk_bf16_f32 v187, v182, v183\n" \
  "v_mfma_f32_16x16x32_bf16 v[144:147], v[40:43], v[120:123], 0\n" \
  "v_mfma_f32_16x16x32_bf16 v[160:163], v[56:59], v[120:123], 0\n" \
  "v_mfma_f32_16x16x32_bf16 v[160:163], v[72:75], v[184:187], v[160:163]\n" \
  "v_mfma_f32_16x16x32_bf16 v[148:151], v[44:47], v[124:127], 0\n" \
  "v_mfma_f32_16x16x32_bf16 v[164:167], v[60:63], v[124:127], 0\n" \
  "v_mfma_f32_16x16x32_bf16 v[164:167], v[76:79], v[184:187], v[164:167]\n" \
  "v_mfma_f32_16x16x32_bf16 v[152:155], v[48:51], v[128:131], 0\n" \
  "v_mfma_f32_16x16x32_bf16 v[168:171], v[64:67], v[128:131], 0\n" \
  "v_mfma_f32_16x16x32_bf16 v[168:171], v[80:83], v[184:187], v[168:171]\n" \
  "v_mfma_f32_16x16x32_bf16 v[156:159], v[52:55], v[132:135], 0\n" \
  "v_mfma_f32_16x16x32_bf16 v[172:175], v[68:71], v[132:135], 0\n" \
  "v_mfma_f32_16x16x32_bf16 v[172:175], v[84:87], v[184:187], v[172:175]\n" \
  "v_rcp_f32 v188, v162\n" \
  "s_nop 0\n" \
  "v_pk_mul_f32 v[144:145], v[144:145], v[188:189] op_sel_hi:[1,0]\n" \
  "v_pk_mul_f32 v[146:147], v[146:147], v[188:189] op_sel_hi:[1,0]\n" \
  "v_pk_mul_f32 v[160:161], v[160:161], v[188:189] op_sel_hi:[1,0]\n" \
  "v_rcp_f32 v189, v166\n" \
  "s_nop 0\n" \
  "v_cndmask_b32 v167, v188, v189, s[28:29]\n" \
  "v_pk_fma_f32 v[144:145], v[148:149], v[188:189], v[144:145] op_sel:[0,1,0] op_sel_hi:[1,1,1]\n" \
  "v_pk_fma_f32 v[146:147], v[150:151], v[188:189], v[146:147] op_sel:[0,1,0] op_sel_hi:[1,1,1]\n" \
  "v_pk_fma_f32 v[160:161], v[164:165], v[188:189], v[160:161] op_sel:[0,1,0] op_sel_hi:[1,1,1]\n" \
  "v_rcp_f32 v190, v170\n" \
  "s_nop 0\n" \
  "v_cndmask_b32 v167, v167, v190, s[26:27]\n" \
  "v_pk_fma_f32 v[144:145], v[152:153], v[190:191], v[144:145] op_sel:[0,0,0] op_sel_hi:[1,0,1]\n" \
  "v_pk_fma_f32 v[146:147], v[154:155], v[190:191], v[146:147] op_sel:[0,0,0] op_sel_hi:[1,0,1]\n" \
  "v_pk_fma_f32 v[160:161], v[168:169], v[190:191], v[160:161] op_sel:[0,0,0] op_sel_hi:[1,0,1]\n" \
  "v_rcp_f32 v191, v174\n" \
  "s_nop 0\n" \
  "v_cndmask_b32 v167, v167, v191, s[24:25]\n" \
  "v_pk_fma_f32 v[144:145], v[156:157], v[190:191], v[144:145] op_sel:[0,1,0] op_sel_hi:[1,1,1]\n" \
  "v_pk_fma_f32 v[146:147], v[158:159], v[190:191], v[146:147] op_sel:[0,1,0] op_sel_hi:[1,1,1]\n" \
  "v_pk_fma_f32 v[160:161], v[172:173], v[190:191], v[160:161] op_sel:[0,1,0] op_sel_hi:[1,1,1]\n" \
  "v_pk_mul_f32 v[176:177], v[176:177], v[166:167] op_sel:[0,1] op_sel_hi:[1,1]\n" \
  "v_pk_mul_f32 v[178:179], v[178:179], v[166:167] op_sel:[0,1] op_sel_hi:[1,1]\n" \
  "v_pk_mul_f32 v[180:181], v[180:181], v[166:167] op_sel:[0,1] op_sel_hi:[1,1]\n" \
  "v_pk_mul_f32 v[182:183], v[182:183], v[166:167] op_sel:[0,1] op_sel_hi:[1,1]\n" \
  "v_cvt_pk_bf16_f32 v184, v176, v177\n" \
  "v_cvt_pk_bf16_f32 v185, v178, v179\n" \
  "v_cvt_pk_bf16_f32 v186, v180, v181\n" \
  "v_cvt_pk_bf16_f32 v187, v182, v183\n" \
  "s_nop 1\n" \
  "v_mfma_f32_16x16x32_bf16 v[144:147], v[136:139], v[184:187], v[144:147]\n" \
  "v_mfma_f32_16x16x32_bf16 v[160:163], v[140:143], v[184:187], v[160:163]\n" \
  "s_mov_b64 exec, s[30:31]\n" \
  "s_nop 6\n" \
  "ds_write_b64 v22, v[160:161]\n" \
  "ds_write2_b64 v21, v[144:145], v[146:147] offset1:16\n" \
  "s_add_u32 s78, s78, 4\n" \
  "s_addc_u32 s79, s79, 0\n" \
  "s_setpc_b64 s[78:79]\n" \
  "LSTART20_%=:\n" \
  "LOOP20_%=:\n" \
  "v_mov_b32 v80, 0\n" \
  "v_mov_b32 v81, 0\n" \
  "v_mov_b32 v82, 0\n" \
  "v_mov_b32 v83, 0\n" \
  "v_mov_b32 v84, 0\n" \
  "v_mov_b32 v85, 0\n" \
  "v_mov_b32 v86, 0\n" \
  "v_mov_b32 v87, 0\n" \
  "v_mov_b32 v88, 0\n" \
  "v_mov_b32 v89, 0\n" \
  "v_mov_b32 v90, 0\n" \
  "v_mov_b32 v91, 0\n" \
  "v_mov_b32 v16, 0\n" \
  "v_mov_b32 v17, 0\n" \
  "v_mov_b32 v18, 0\n" \
  "v_mov_b32 v19, 0\n" \
  "v_mov_b32 v20, 0\n" \
  "v_mov_b32 v21, 0\n" \
  "v_mov_b32 v22, 0\n" \
  "v_mov_b32 v23, 0\n" \
  "v_mov_b32 v24, 0\n" \
  "v_mov_b32 v25, 0\n" \
  "v_mov_b32 v26, 0\n" \
  "v_mov_b32 v27, 0\n" \
  "v_mov_b32 v104, 0\n" \
  "v_mov_b32 v105, 0\n" \
  "v_mov_b32 v106, 0\n" \
  "v_mov_b32 v107, 0\n" \
  "s_mov_b32 s65, s66\n" \
  "s_xor_b32 s69, s66, 0x2000\n" \
  "s_add_u32 s64, s64, 1\n" \
  "v_readlane_b32 s66, v28, s64\n" \
  "s_add_u32 s40, s65, 131072\n" \
  "s_add_u32 s41, s65, 262144\n" \
  "s_add_u32 s42, s65, 393216\n" \
  "s_barrier\n" \
  "LM20_%=:\n" \
  "s_barrier\n" \
  "ds_read_b128 v[40:43], v5\n" \
  "s_barrier\n" \
  "ds_read_b128 v[44:47], v5 offset:1024\n" \
  "s_waitcnt lgkmcnt(1)\n" \
  "v_smfmac_f32_16x16x64_bf16 v[80:83], v[40:43], a[0:7], v0\n" \
  "v_smfmac_f32_16x16x64_bf16 v[84:87], v[40:43], a[80:87], v0\n" \
  "s_barrier\n" \
  "ds_read_b128 v[48:51], v5 offset:2048\n" \
  "v_smfmac_f32_16x16x64_bf16 v[88:91], v[40:43], a[160:167], v0\n" \
  "s_waitcnt lgkmcnt(1)\n" \
  "ds_read_b128 v[52:55], v5 offset:3072\n" \
  "v_smfmac_f32_16x16x64_bf16 v[80:83], v[44:47], a[8:15], v0\n" \
  "v_smfmac_f32_16x16x64_bf16 v[84:87], v[44:47], a[88:95], v0\n" \
  "v_smfmac_f32_16x16x64_bf16 v[88:91], v[44:47], a[168:175], v0\n" \
  "s_barrier\n" \
  "s_waitcnt lgkmcnt(1)\n" \
  "ds_read_b128 v[56:59], v5 offset:4096\n" \
  "v_smfmac_f32_16x16x64_bf16 v[80:83], v[48:51], a[16:23], v0\n" \
  "ds_read_b128 v[60:63], v5 offset:5120\n" \
  "v_smfmac_f32_16x16x64_bf16 v[84:87], v[48:51], a[96:103], v0\n" \
  "v_smfmac_f32_16x16x64_bf16 v[88:91], v[48:51], a[176:183], v0\n" \
  "s_waitcnt lgkmcnt(2)\n" \
  "v_smfmac_f32_16x16x64_bf16 v[80:83], v[52:55], a[24:31], v0\n" \
  "s_barrier\n" \
  "ds_read_b128 v[64:67], v5 offset:6144\n" \
  "v_smfmac_f32_16x16x64_bf16 v[84:87], v[52:55], a[104:111], v0\n" \
  "s_waitcnt lgkmcnt(2)\n" \
  "ds_read_b128 v[68:71], v5 offset:7168\n" \
  "v_smfmac_f32_16x16x64_bf16 v[80:83], v[56:59], a[32:39], v0\n" \
  "v_smfmac_f32_16x16x64_bf16 v[84:87], v[56:59], a[112:119], v0\n" \
  "s_waitcnt lgkmcnt(2)\n" \
  "v_smfmac_f32_16x16x64_bf16 v[80:83], v[60:63], a[40:47], v0\n" \
  "s_barrier\n" \
  "ds_read2_b32 v[72:73], v6 offset0:0 offset1:32\n" \
  "ds_read2_b32 v[74:75], v6 offset0:64 offset1:96\n" \
  "v_smfmac_f32_16x16x64_bf16 v[84:87], v[60:63], a[120:127], v0\n" \
  "s_waitcnt lgkmcnt(3)\n" \
  "ds_read2_b32 v[76:77], v7 offset0:0 offset1:32\n" \
  "ds_read2_b32 v[78:79], v7 offset0:64 offset1:96\n" \
  "v_smfmac_f32_16x16x64_bf16 v[80:83], v[64:67], a[48:55], v0\n" \
  "v_smfmac_f32_16x16x64_bf16 v[84:87], v[64:67], a[128:135], v0\n" \
  "s_waitcnt lgkmcnt(4)\n" \
  "v_smfmac_f32_16x16x64_bf16 v[80:83], v[68:71], a[56:63], v0\n" \
  "v_smfmac_f32_16x16x64_bf16 v[84:87], v[68:71], a[136:143], v0\n" \
  "s_waitcnt lgkmcnt(2)\n" \
  "v_smfmac_f32_16x16x64_bf16 v[80:83], v[72:75], a[64:71], v0\n" \
  "v_smfmac_f32_16x16x64_bf16 v[84:87], v[72:75], a[144:151], v0\n" \
  "s_waitcnt lgkmcnt(0)\n" \
  "ds_read_b128 v[40:43], v11 offset:8192\n" \
  "v_smfmac_f32_16x16x64_bf16 v[80:83], v[76:79], a[72:79], v0\n" \
  "ds_read_b128 v[44:47], v11 offset:9216\n" \
  "v_smfmac_f32_16x16x64_bf16 v[84:87], v[76:79], a[152:159], v0\n" \
  "v_smfmac_f32_16x16x64_bf16 v[88:91], v[52:55], a[184:191], v0\n" \
  "ds_read_b128 v[48:51], v11 offset:49152\n" \
  "v_smfmac_f32_16x16x64_bf16 v[88:91], v[56:59], a[192:199], v0\n" \
  "ds_read_b128 v[52:55], v11 offset:50176\n" \
  "v_permlane32_swap_b32 v80, v84\n" \
  "v_smfmac_f32_16x16x64_bf16 v[88:91], v[60:63], a[200:207], v0\n" \
  "ds_read_b128 v[56:59], v11 offset:0\n" \
  "v_permlane32_swap_b32 v81, v85\n" \
  "v_smfmac_f32_16x16x64_bf16 v[88:91], v[64:67], a[208:215], v0\n" \
  "ds_read_b128 v[60:63], v11 offset:1024\n" \
  "v_permlane32_swap_b32 v82, v86\n" \
  "v_smfmac_f32_16x16x64_bf16 v[88:91], v[68:71], a[216:223], v0\n" \
  "ds_read_b128 v[64:67], v11 offset:40960\n" \
  "v_permlane32_swap_b32 v83, v87\n" \
  "v_smfmac_f32_16x16x64_bf16 v[88:91], v[72:75], a[224:231], v0\n" \
  "ds_read_b128 v[68:71], v11 offset:41984\n" \
  "v_add_f32 v108, v80, v84\n" \
  "v_smfmac_f32_16x16x64_bf16 v[88:91], v[76:79], a[232:239], v0\n" \
  "s_nop 1\n" \
  "v_add_f32 v109, v81, v85\n" \
  "v_add_f32 v110, v82, v86\n" \
  "v_add_f32 v111, v83, v87\n" \
  "v_cvt_pk_bf16_f32 v32, v108, v109\n" \
  "v_cvt_pk_bf16_f32 v33, v110, v111\n" \
  "s_nop 0\n" \
  "v_permlane32_swap_b32 v88, v90\n" \
  "v_permlane32_swap_b32 v89, v91\n" \
  "v_pk_add_f32 v[80:81], v[88:89], v[90:91]\n" \
  "v_cvt_pk_bf16_f32 v12, v80, v81\n" \
  "v_cmp_u_f32 vcc, v80, v81\n" \
  "ds_write_b64 v8, v[32:33]\n" \
  "ds_write_b32 v9, v12\n" \
  "s_cbranch_vccnz LMF20_%=\n" \
  "LMOK20_%=:\n" \
  "s_waitcnt lgkmcnt(0)\n" \
  "s_barrier\n" \
  "ds_read_b64_tr_b16 v[80:81], v10\n" \
  "ds_read_b64_tr_b16 v[82:83], v10 offset:32\n" \
  "ds_read_b64_tr_b16 v[84:85], v10 offset:512\n" \
  "ds_read_b64_tr_b16 v[86:87], v10 offset:544\n" \
  "ds_read_b64_tr_b16 v[88:89], v10 offset:1024\n" \
  "ds_read_b64_tr_b16 v[90:91], v10 offset:1056\n" \
  "ds_read_b64_tr_b16 v[92:93], v10 offset:1536\n" \
  "ds_read_b64_tr_b16 v[94:95], v10 offset:1568\n" \
  "ds_read_b64_tr_b16 v[36:37], v10 offset:2048\n" \
  "ds_read_b64_tr_b16 v[38:39], v10 offset:2080\n" \
  "ds_read_b64_tr_b16 v[100:101], v10 offset:2560\n" \
  "ds_read_b64_tr_b16 v[102:103], v10 offset:2592\n" \
  "s_waitcnt lgkmcnt(10)\n" \
  "v_smfmac_f32_16x16x64_bf16 v[16:19], v[80:83], v[240:247], v0\n" \
  "s_waitcnt lgkmcnt(8)\n" \
  "v_smfmac_f32_16x16x64_bf16 v[16:19], v[84:87], v[248:255], v0\n" \
  "s_waitcnt lgkmcnt(6)\n" \
  "v_smfmac_f32_16x16x64_bf16 v[16:19], v[88:91], a[240:247], v0\n" \
  "s_waitcnt lgkmcnt(4)\n" \
  "ds_read_b128 v[72:75], v11 offset:2048\n" \
  "v_smfmac_f32_16x16x64_bf16 v[16:19], v[92:95], a[248:255], v0\n" \
  "s_waitcnt lgkmcnt(3)\n" \
  "ds_read_b128 v[76:79], v11 offset:3072\n" \
  "v_smfmac_f32_16x16x64_bf16 v[16:19], v[36:39], v[40:47], v0\n" \
  "s_waitcnt lgkmcnt(2)\n" \
  "ds_read_b128 v[40:43], v11 offset:43008\n" \
  "v_smfmac_f32_16x16x64_bf16 v[16:19], v[100:103], v[48:55], v0\n" \
  "ds_read_b128 v[44:47], v11 offset:44032\n" \
  "v_smfmac_f32_16x16x64_bf16 v[20:23], v[80:83], v[112:119], v0\n" \
  "ds_read_b128 v[48:51], v11 offset:4096\n" \
  "v_smfmac_f32_16x16x64_bf16 v[20:23], v[84:87], v[120:127], v0\n" \
  "ds_read_b128 v[52:55], v11 offset:5120\n" \
  "v_cvt_pk_bf16_f32 v108, v16, v17\n" \
  "v_smfmac_f32_16x16x64_bf16 v[20:23], v[88:91], v[128:135], v0\n" \
  "v_cvt_pk_bf16_f32 v109, v18, v19\n" \
  "v_smfmac_f32_16x16x64_bf16 v[20:23], v[92:95], v[136:143], v0\n" \
  "buffer_store_dwordx2 v[108:109], v4, s[36:39], s65 offen sc1\n" \
  "v_smfmac_f32_16x16x64_bf16 v[20:23], v[36:39], v[56:63], v0\n" \
  "ds_read_b128 v[56:59], v11 offset:45056\n" \
  "v_smfmac_f32_16x16x64_bf16 v[20:23], v[100:103], v[64:71], v0\n" \
  "ds_read_b128 v[60:63], v11 offset:46080\n" \
  "v_smfmac_f32_16x16x64_bf16 v[104:107], v[80:83], v[144:151], v0\n" \
  "ds_read_b128 v[64:67], v11 offset:6144\n" \
  "v_smfmac_f32_16x16x64_bf16 v[104:107], v[84:87], v[152:159], v0\n" \
  "ds_read_b128 v[68:71], v11 offset:7168\n" \
  "v_cvt_pk_bf16_f32 v110, v20, v21\n" \
  "v_smfmac_f32_16x16x64_bf16 v[104:107], v[88:91], v[160:167], v0\n" \
  "v_cvt_pk_bf16_f32 v111, v22, v23\n" \
  "v_smfmac_f32_16x16x64_bf16 v[104:107], v[92:95], v[168:175], v0\n" \
  "buffer_store_dwordx2 v[110:111], v13, s[36:39], s65 offen sc1\n" \
  "v_mov_b32 v16, 0\n" \
  "s_waitcnt lgkmcnt(8)\n" \
  "v_smfmac_f32_16x16x64_bf16 v[104:107], v[36:39], v[72:79], v0\n" \
  "s_waitcnt lgkmcnt(6)\n" \
  "ds_read_b128 v[72:75], v11 offset:47104\n" \
  "v_mov_b32 v17, 0\n" \
  "v_smfmac_f32_16x16x64_bf16 v[104:107], v[100:103], v[40:47], v0\n" \
  "ds_read_b128 v[76:79], v11 offset:48128\n" \
  "v_mov_b32 v18, 0\n" \
  "v_smfmac_f32_16x16x64_bf16 v[24:27], v[80:83], v[176:183], v0\n" \
  "v_mov_b32 v19, 0\n" \
  "v_smfmac_f32_16x16x64_bf16 v[24:27], v[84:87], v[184:191], v0\n" \
  "v_cvt_pk_bf16_f32 v108, v104, v105\n" \
  "v_smfmac_f32_16x16x64_bf16 v[24:27], v[88:91], v[192:199], v0\n" \
  "v_cvt_pk_bf16_f32 v109, v106, v107\n" \
  "v_smfmac_f32_16x16x64_bf16 v[24:27], v[92:95], v[200:207], v0\n" \
  "buffer_store_dwordx2 v[108:109], v13, s[36:39], s40 offen sc1\n" \
  "s_waitcnt lgkmcnt(6)\n" \
  "v_smfmac_f32_16x16x64_bf16 v[24:27], v[36:39], v[48:55], v0\n" \
  "s_waitcnt lgkmcnt(4)\n" \
  "v_smfmac_f32_16x16x64_bf16 v[24:27], v[100:103], v[56:63], v0\n" \
  "v_smfmac_f32_16x16x64_bf16 v[16:19], v[80:83], v[208:215], v0\n" \
  "v_smfmac_f32_16x16x64_bf16 v[16:19], v[84:87], v[216:223], v0\n" \
  "v_cvt_pk_bf16_f32 v110, v24, v25\n" \
  "v_smfmac_f32_16x16x64_bf16 v[16:19], v[88:91], v[224:231], v0\n" \
  "v_cvt_pk_bf16_f32 v111, v26, v27\n" \
  "v_smfmac_f32_16x16x64_bf16 v[16:19], v[92:95], v[232:239], v0\n" \
  "buffer_store_dwordx2 v[110:111], v13, s[36:39], s41 offen sc1\n" \
  "s_waitcnt lgkmcnt(2)\n" \
  "v_smfmac_f32_16x16x64_bf16 v[16:19], v[36:39], v[64:71], v0\n" \
  "s_waitcnt lgkmcnt(0)\n" \
  "v_smfmac_f32_16x16x64_bf16 v[16:19], v[100:103], v[72:79], v0\n" \
  "s_nop 3\n" \
  "v_mov_b32 v31, v31\n" \
  "s_nop 2\n" \
  "v_cvt_pk_bf16_f32 v108, v16, v17\n" \
  "v_cvt_pk_bf16_f32 v109, v18, v19\n" \
  "buffer_store_dwordx2 v[108:109], v13, s[36:39], s42 offen sc1\n" \
  "s_mov_b32 s67, s66\n" \
  "s_cmp_eq_u32 s64, 64\n" \
  "s_cbranch_scc1 LDONE_%=\n" \
  "s_waitcnt vmcnt(0)\n" \
  "s_branch LOOP20_%=\n" \
  "LMF20_%=:\n" \
  "v_mbcnt_lo_u32_b32 v94, -1, 0\n" \
  "v_mbcnt_hi_u32_b32 v94, -1, v94\n" \
  "v_lshlrev_b32 v94, 4, v94\n" \
  "v_mov_b32 v92, 0\n" \
  "ds_read_b128 v[72:75], v94 offset:0\n" \
  "ds_read_b128 v[76:79], v94 offset:1024\n" \
  "ds_read_b128 v[80:83], v94 offset:2048\n" \
  "ds_read_b128 v[84:87], v94 offset:3072\n" \
  "ds_read_b128 v[88:91], v94 offset:4096\n" \
  "s_waitcnt lgkmcnt(0)\n" \
  "v_max3_u32 v92, v92, v72, v73\n" \
  "v_max3_u32 v92, v92, v74, v75\n" \
  "v_max3_u32 v92, v92, v76, v77\n" \
  "v_max3_u32 v92, v92, v78, v79\n" \
  "v_max3_u32 v92, v92, v80, v81\n" \
  "v_max3_u32 v92, v92, v82, v83\n" \
  "v_max3_u32 v92, v92, v84, v85\n" \
  "v_max3_u32 v92, v92, v86, v87\n" \
  "v_max3_u32 v92, v92, v88, v89\n" \
  "v_max3_u32 v92, v92, v90, v91\n" \
  "ds_read_b128 v[72:75], v94 offset:5120\n" \
  "ds_read_b128 v[76:79], v94 offset:6144\n" \
  "ds_read_b128 v[80:83], v94 offset:7168\n" \
  "ds_read_b128 v[84:87], v94 offset:8192\n" \
  "ds_read_b128 v[88:91], v94 offset:9216\n" \
  "s_waitcnt lgkmcnt(0)\n" \
  "v_max3_u32 v92, v92, v72, v73\n" \
  "v_max3_u32 v92, v92, v74, v75\n" \
  "v_max3_u32 v92, v92, v76, v77\n" \
  "v_max3_u32 v92, v92, v78, v79\n" \
  "v_max3_u32 v92, v92, v80, v81\n" \
  "v_max3_u32 v92, v92, v82, v83\n" \
  "v_max3_u32 v92, v92, v84, v85\n" \
  "v_max3_u32 v92, v92, v86, v87\n" \
  "v_max3_u32 v92, v92, v88, v89\n" \
  "v_max3_u32 v92, v92, v90, v91\n" \
  "v_cmp_eq_u32 vcc, -1, v92\n" \
  "s_cbranch_vccz LMOK20_%=\n" \
  "s_cmp_gt_u32 s58, 4000\n" \
  "s_cbranch_scc1 LMOK20_%=\n" \
  "s_add_u32 s58, s58, 1\n" \
  "s_sub_u32 s64, s64, 1\n" \
  "s_mov_b32 s66, s65\n" \
  "s_waitcnt vmcnt(0) lgkmcnt(0)\n" \
  "s_barrier\n" \
  "s_branch LOOP20_%=\n" \
  "LSTART21_%=:\n" \
  "LOOP21_%=:\n" \
  "v_mov_b32 v80, 0\n" \
  "v_mov_b32 v81, 0\n" \
  "v_mov_b32 v82, 0\n" \
  "v_mov_b32 v83, 0\n" \
  "v_mov_b32 v84, 0\n" \
  "v_mov_b32 v85, 0\n" \
  "v_mov_b32 v86, 0\n" \
  "v_mov_b32 v87, 0\n" \
  "v_mov_b32 v88, 0\n" \
  "v_mov_b32 v89, 0\n" \
  "v_mov_b32 v90, 0\n" \
  "v_mov_b32 v91, 0\n" \
  "v_mov_b32 v16, 0\n" \
  "v_mov_b32 v17, 0\n" \
  "v_mov_b32 v18, 0\n" \
  "v_mov_b32 v19, 0\n" \
  "v_mov_b32 v20, 0\n" \
  "v_mov_b32 v21, 0\n" \
  "v_mov_b32 v22, 0\n" \
  "v_mov_b32 v23, 0\n" \
  "v_mov_b32 v24, 0\n" \
  "v_mov_b32 v25, 0\n" \
  "v_mov_b32 v26, 0\n" \
  "v_mov_b32 v27, 0\n" \
  "v_mov_b32 v104, 0\n" \
  "v_mov_b32 v105, 0\n" \
  "v_mov_b32 v106, 0\n" \
  "v_mov_b32 v107, 0\n" \
  "s_mov_b32 s65, s66\n" \
  "s_xor_b32 s69, s66, 0x2000\n" \
  "s_add_u32 s64, s64, 1\n" \
  "v_readlane_b32 s66, v28, s64\n" \
  "s_add_u32 s40, s65, 131072\n" \
  "s_add_u32 s41, s65, 262144\n" \
  "s_add_u32 s42, s65, 393216\n" \
  "s_barrier\n" \
  "LM21_%=:\n" \
  "s_barrier\n" \
  "ds_read_b128 v[40:43], v5\n" \
  "s_barrier\n" \
  "ds_read_b128 v[44:47], v5 offset:1024\n" \
  "s_waitcnt lgkmcnt(1)\n" \
  "v_smfmac_f32_16x16x64_bf16 v[80:83], v[40:43], a[0:7], v0\n" \
  "v_smfmac_f32_16x16x64_bf16 v[84:87], v[40:43], a[80:87], v0\n" \
  "s_barrier\n" \
  "ds_read_b128 v[48:51], v5 offset:2048\n" \
  "v_smfmac_f32_16x16x64_bf16 v[88:91], v[40:43], a[160:167], v0\n" \
  "s_waitcnt lgkmcnt(1)\n" \
  "ds_read_b128 v[52:55], v5 offset:3072\n" \
  "v_smfmac_f32_16x16x64_bf16 v[80:83], v[44:47], a[8:15], v0\n" \
  "v_smfmac_f32_16x16x64_bf16 v[84:87], v[44:47], a[88:95], v0\n" \
  "v_smfmac_f32_16x16x64_bf16 v[88:91], v[44:47], a[168:175], v0\n" \
  "s_barrier\n" \
  "s_waitcnt lgkmcnt(1)\n" \
  "ds_read_b128 v[56:59], v5 offset:4096\n" \
  "v_smfmac_f32_16x16x64_bf16 v[80:83], v[48:51], a[16:23], v0\n" \
  "ds_read_b128 v[60:63], v5 offset:5120\n" \
  "v_smfmac_f32_16x16x64_bf16 v[84:87], v[48:51], a[96:103], v0\n" \
  "v_smfmac_f32_16x16x64_bf16 v[88:91], v[48:51], a[176:183], v0\n" \
  "s_waitcnt lgkmcnt(2)\n" \
  "v_smfmac_f32_16x16x64_bf16 v[80:83], v[52:55], a[24:31], v0\n" \
  "s_barrier\n" \
  "ds_read_b128 v[64:67], v5 offset:6144\n" \
  "v_smfmac_f32_16x16x64_bf16 v[84:87], v[52:55], a[104:111], v0\n" \
  "s_waitcnt lgkmcnt(2)\n" \
  "ds_read_b128 v[68:71], v5 offset:7168\n" \
  "v_smfmac_f32_16x16x64_bf16 v[80:83], v[56:59], a[32:39], v0\n" \
  "v_smfmac_f32_16x16x64_bf16 v[84:87], v[56:59], a[112:119], v0\n" \
  "s_waitcnt lgkmcnt(2)\n" \
  "v_smfmac_f32_16x16x64_bf16 v[80:83], v[60:63], a[40:47], v0\n" \
  "s_barrier\n" \
  "ds_read2_b32 v[72:73], v6 offset0:0 offset1:32\n" \
  "ds_read2_b32 v[74:75], v6 offset0:64 offset1:96\n" \
  "v_smfmac_f32_16x16x64_bf16 v[84:87], v[60:63], a[120:127], v0\n" \
  "s_waitcnt lgkmcnt(3)\n" \
  "ds_read2_b32 v[76:77], v7 offset0:0 offset1:32\n" \
  "ds_read2_b32 v[78:79], v7 offset0:64 offset1:96\n" \
  "v_smfmac_f32_16x16x64_bf16 v[80:83], v[64:67], a[48:55], v0\n" \
  "v_smfmac_f32_16x16x64_bf16 v[84:87], v[64:67], a[128:135], v0\n" \
  "s_waitcnt lgkmcnt(4)\n" \
  "v_smfmac_f32_16x16x64_bf16 v[80:83], v[68:71], a[56:63], v0\n" \
  "v_smfmac_f32_16x16x64_bf16 v[84:87], v[68:71], a[136:143], v0\n" \
  "s_waitcnt lgkmcnt(2)\n" \
  "v_smfmac_f32_16x16x64_bf16 v[80:83], v[72:75], a[64:71], v0\n" \
  "v_smfmac_f32_16x16x64_bf16 v[84:87], v[72:75], a[144:151], v0\n" \
  "s_waitcnt lgkmcnt(0)\n" \
  "ds_read_b128 v[40:43], v11 offset:8192\n" \
  "v_smfmac_f32_16x16x64_bf16 v[80:83], v[76:79], a[72:79], v0\n" \
  "ds_read_b128 v[44:47], v11 offset:9216\n" \
  "v_smfmac_f32_16x16x64_bf16 v[84:87], v[76:79], a[152:159], v0\n" \
  "v_smfmac_f32_16x16x64_bf16 v[88:91], v[52:55], a[184:191], v0\n" \
  "ds_read_b128 v[48:51], v11 offset:49152\n" \
  "v_smfmac_f32_16x16x64_bf16 v[88:91], v[56:59], a[192:199], v0\n" \
  "ds_read_b128 v[52:55], v11 offset:50176\n" \
  "v_permlane32_swap_b32 v80, v84\n" \
  "v_smfmac_f32_16x16x64_bf16 v[88:91], v[60:63], a[200:207], v0\n" \
  "ds_read_b128 v[56:59], v11 offset:0\n" \
  "v_permlane32_swap_b32 v81, v85\n" \
  "v_smfmac_f32_16x16x64_bf16 v[88:91], v[64:67], a[208:215], v0\n" \
  "ds_read_b128 v[60:63], v11 offset:1024\n" \
  "v_permlane32_swap_b32 v82, v86\n" \
  "v_smfmac_f32_16x16x64_bf16 v[88:91], v[68:71], a[216:223], v0\n" \
  "ds_read_b128 v[64:67], v11 offset:40960\n" \
  "v_permlane32_swap_b32 v83, v87\n" \
  "v_smfmac_f32_16x16x64_bf16 v[88:91], v[72:75], a[224:231], v0\n" \
  "ds_read_b128 v[68:71], v11 offset:41984\n" \
  "v_add_f32 v108, v80, v84\n" \
  "v_smfmac_f32_16x16x64_bf16 v[88:91], v[76:79], a[232:239], v0\n" \
  "s_nop 1\n" \
  "v_add_f32 v109, v81, v85\n" \
  "v_add_f32 v110, v82, v86\n" \
  "v_add_f32 v111, v83, v87\n" \
  "v_cvt_pk_bf16_f32 v32, v108, v109\n" \
  "v_cvt_pk_bf16_f32 v33, v110, v111\n" \
  "s_nop 0\n" \
  "v_permlane32_swap_b32 v88, v90\n" \
  "v_permlane32_swap_b32 v89, v91\n" \
  "v_pk_add_f32 v[80:81], v[88:89], v[90:91]\n" \
  "v_cvt_pk_bf16_f32 v12, v80, v81\n" \
  "v_cmp_u_f32 vcc, v80, v81\n" \
  "ds_write_b64 v8, v[32:33]\n" \
  "ds_write_b32 v9, v12\n" \
  "s_cbranch_vccnz LMF21_%=\n" \
  "LMOK21_%=:\n" \
  "s_waitcnt lgkmcnt(0)\n" \
  "s_barrier\n" \
  "ds_read_b64_tr_b16 v[80:81], v10\n" \
  "ds_read_b64_tr_b16 v[82:83], v10 offset:32\n" \
  "ds_read_b64_tr_b16 v[84:85], v10 offset:512\n" \
  "ds_read_b64_tr_b16 v[86:87], v10 offset:544\n" \
  "ds_read_b64_tr_b16 v[88:89], v10 offset:1024\n" \
  "ds_read_b64_tr_b16 v[90:91], v10 offset:1056\n" \
  "ds_read_b64_tr_b16 v[92:93], v10 offset:1536\n" \
  "ds_read_b64_tr_b16 v[94:95], v10 offset:1568\n" \
  "ds_read_b64_tr_b16 v[36:37], v10 offset:2048\n" \
  "ds_read_b64_tr_b16 v[38:39], v10 offset:2080\n" \
  "ds_read_b64_tr_b16 v[100:101], v10 offset:2560\n" \
  "ds_read_b64_tr_b16 v[102:103], v10 offset:2592\n" \
  "s_waitcnt lgkmcnt(10)\n" \
  "v_smfmac_f32_16x16x64_bf16 v[16:19], v[80:83], v[240:247], v0\n" \
  "s_waitcnt lgkmcnt(8)\n" \
  "v_smfmac_f32_16x16x64_bf16 v[16:19], v[84:87], v[248:255], v0\n" \
  "s_waitcnt lgkmcnt(6)\n" \
  "v_smfmac_f32_16x16x64_bf16 v[16:19], v[88:91], a[240:247], v0\n" \
  "s_waitcnt lgkmcnt(4)\n" \
  "ds_read_b128 v[72:75], v11 offset:2048\n" \
  "v_smfmac_f32_16x16x64_bf16 v[16:19], v[92:95], a[248:255], v0\n" \
  "s_waitcnt lgkmcnt(3)\n" \
  "ds_read_b128 v[76:79], v11 offset:3072\n" \
  "v_smfmac_f32_16x16x64_bf16 v[16:19], v[36:39], v[40:47], v0\n" \
  "s_waitcnt lgkmcnt(2)\n" \
  "ds_read_b128 v[40:43], v11 offset:43008\n" \
  "v_smfmac_f32_16x16x64_bf16 v[16:19], v[100:103], v[48:55], v0\n" \
  "ds_read_b128 v[44:47], v11 offset:44032\n" \
  "v_smfmac_f32_16x16x64_bf16 v[20:23], v[80:83], v[112:119], v0\n" \
  "ds_read_b128 v[48:51], v11 offset:4096\n" \
  "v_smfmac_f32_16x16x64_bf16 v[20:23], v[84:87], v[120:127], v0\n" \
  "ds_read_b128 v[52:55], v11 offset:5120\n" \
  "v_cvt_pk_bf16_f32 v108, v16, v17\n" \
  "v_smfmac_f32_16x16x64_bf16 v[20:23], v[88:91], v[128:135], v0\n" \
  "v_cvt_pk_bf16_f32 v109, v18, v19\n" \
  "v_smfmac_f32_16x16x64_bf16 v[20:23], v[92:95], v[136:143], v0\n" \
  "buffer_store_dwordx2 v[108:109], v4, s[36:39], s65 offen sc1\n" \
  "v_smfmac_f32_16x16x64_bf16 v[20:23], v[36:39], v[56:63], v0\n" \
  "ds_read_b128 v[56:59], v11 offset:45056\n" \
  "v_smfmac_f32_16x16x64_bf16 v[20:23], v[100:103], v[64:71], v0\n" \
  "ds_read_b128 v[60:63], v11 offset:46080\n" \
  "v_smfmac_f32_16x16x64_bf16 v[104:107], v[80:83], v[144:151], v0\n" \
  "ds_read_b128 v[64:67], v11 offset:6144\n" \
  "v_smfmac_f32_16x16x64_bf16 v[104:107], v[84:87], v[152:159], v0\n" \
  "ds_read_b128 v[68:71], v11 offset:7168\n" \
  "v_cvt_pk_bf16_f32 v110, v20, v21\n" \
  "v_smfmac_f32_16x16x64_bf16 v[104:107], v[88:91], v[160:167], v0\n" \
  "v_cvt_pk_bf16_f32 v111, v22, v23\n" \
  "v_smfmac_f32_16x16x64_bf16 v[104:107], v[92:95], v[168:175], v0\n" \
  "buffer_store_dwordx2 v[110:111], v13, s[36:39], s65 offen sc1\n" \
  "v_mov_b32 v16, 0\n" \
  "s_waitcnt lgkmcnt(8)\n" \
  "v_smfmac_f32_16x16x64_bf16 v[104:107], v[36:39], v[72:79], v0\n" \
  "s_waitcnt lgkmcnt(6)\n" \
  "ds_read_b128 v[72:75], v11 offset:47104\n" \
  "v_mov_b32 v17, 0\n" \
  "v_smfmac_f32_16x16x64_bf16 v[104:107], v[100:103], v[40:47], v0\n" \
  "ds_read_b128 v[76:79], v11 offset:48128\n" \
  "v_mov_b32 v18, 0\n" \
  "v_smfmac_f32_16x16x64_bf16 v[24:27], v[80:83], v[176:183], v0\n" \
  "v_mov_b32 v19, 0\n" \
  "v_smfmac_f32_16x16x64_bf16 v[24:27], v[84:87], v[184:191], v0\n" \
  "v_cvt_pk_bf16_f32 v108, v104, v105\n" \
  "v_smfmac_f32_16x16x64_bf16 v[24:27], v[88:91], v[192:199], v0\n" \
  "v_cvt_pk_bf16_f32 v109, v106, v107\n" \
  "v_smfmac_f32_16x16x64_bf16 v[24:27], v[92:95], v[200:207], v0\n" \
  "buffer_store_dwordx2 v[108:109], v13, s[36:39], s40 offen sc1\n" \
  "s_waitcnt lgkmcnt(6)\n" \
  "v_smfmac_f32_16x16x64_bf16 v[24:27], v[36:39], v[48:55], v0\n" \
  "s_waitcnt lgkmcnt(4)\n" \
  "v_smfmac_f32_16x16x64_bf16 v[24:27], v[100:103], v[56:63], v0\n" \
  "v_smfmac_f32_16x16x64_bf16 v[16:19], v[80:83], v[208:215], v0\n" \
  "v_smfmac_f32_16x16x64_bf16 v[16:19], v[84:87], v[216:223], v0\n" \
  "v_cvt_pk_bf16_f32 v110, v24, v25\n" \
  "v_smfmac_f32_16x16x64_bf16 v[16:19], v[88:91], v[224:231], v0\n" \
  "v_cvt_pk_bf16_f32 v111, v26, v27\n" \
  "v_smfmac_f32_16x16x64_bf16 v[16:19], v[92:95], v[232:239], v0\n" \
  "buffer_store_dwordx2 v[110:111], v13, s[36:39], s41 offen sc1\n" \
  "s_waitcnt lgkmcnt(2)\n" \
  "v_smfmac_f32_16x16x64_bf16 v[16:19], v[36:39], v[64:71], v0\n" \
  "s_waitcnt lgkmcnt(0)\n" \
  "v_smfmac_f32_16x16x64_bf16 v[16:19], v[100:103], v[72:79], v0\n" \
  "s_nop 3\n" \
  "v_mov_b32 v31, v31\n" \
  "s_nop 2\n" \
  "v_cvt_pk_bf16_f32 v108, v16, v17\n" \
  "v_cvt_pk_bf16_f32 v109, v18, v19\n" \
  "buffer_store_dwordx2 v[108:109], v13, s[36:39], s42 offen sc1\n" \
  "s_mov_b32 s67, s66\n" \
  "s_cmp_eq_u32 s64, 64\n" \
  "s_cbranch_scc1 LDONE_%=\n" \
  "s_waitcnt vmcnt(0)\n" \
  "s_branch LOOP21_%=\n" \
  "LMF21_%=:\n" \
  "v_mbcnt_lo_u32_b32 v94, -1, 0\n" \
  "v_mbcnt_hi_u32_b32 v94, -1, v94\n" \
  "v_lshlrev_b32 v94, 4, v94\n" \
  "v_mov_b32 v92, 0\n" \
  "ds_read_b128 v[72:75], v94 offset:0\n" \
  "ds_read_b128 v[76:79], v94 offset:1024\n" \
  "ds_read_b128 v[80:83], v94 offset:2048\n" \
  "ds_read_b128 v[84:87], v94 offset:3072\n" \
  "ds_read_b128 v[88:91], v94 offset:4096\n" \
  "s_waitcnt lgkmcnt(0)\n" \
  "v_max3_u32 v92, v92, v72, v73\n" \
  "v_max3_u32 v92, v92, v74, v75\n" \
  "v_max3_u32 v92, v92, v76, v77\n" \
  "v_max3_u32 v92, v92, v78, v79\n" \
  "v_max3_u32 v92, v92, v80, v81\n" \
  "v_max3_u32 v92, v92, v82, v83\n" \
  "v_max3_u32 v92, v92, v84, v85\n" \
  "v_max3_u32 v92, v92, v86, v87\n" \
  "v_max3_u32 v92, v92, v88, v89\n" \
  "v_max3_u32 v92, v92, v90, v91\n" \
  "ds_read_b128 v[72:75], v94 offset:5120\n" \
  "ds_read_b128 v[76:79], v94 offset:6144\n" \
  "ds_read_b128 v[80:83], v94 offset:7168\n" \
  "ds_read_b128 v[84:87], v94 offset:8192\n" \
  "ds_read_b128 v[88:91], v94 offset:9216\n" \
  "s_waitcnt lgkmcnt(0)\n" \
  "v_max3_u32 v92, v92, v72, v73\n" \
  "v_max3_u32 v92, v92, v74, v75\n" \
  "v_max3_u32 v92, v92, v76, v77\n" \
  "v_max3_u32 v92, v92, v78, v79\n" \
  "v_max3_u32 v92, v92, v80, v81\n" \
  "v_max3_u32 v92, v92, v82, v83\n" \
  "v_max3_u32 v92, v92, v84, v85\n" \
  "v_max3_u32 v92, v92, v86, v87\n" \
  "v_max3_u32 v92, v92, v88, v89\n" \
  "v_max3_u32 v92, v92, v90, v91\n" \
  "v_cmp_eq_u32 vcc, -1, v92\n" \
  "s_cbranch_vccz LMOK21_%=\n" \
  "s_cmp_gt_u32 s58, 4000\n" \
  "s_cbranch_scc1 LMOK21_%=\n" \
  "s_add_u32 s58, s58, 1\n" \
  "s_sub_u32 s64, s64, 1\n" \
  "s_mov_b32 s66, s65\n" \
  "s_waitcnt vmcnt(0) lgkmcnt(0)\n" \
  "s_barrier\n" \
  "s_branch LOOP21_%=\n" \
  "LSTART22_%=:\n" \
  "s_getpc_b64 s[80:81]\n" \
  "LPC22_%=:\n" \
  "s_add_u32 s82, s80, LEA22_%=-LPC22_%=+24\n" \
  "s_addc_u32 s83, s81, 0\n" \
  "LOOP22_%=:\n" \
  "v_mov_b32 v80, 0\n" \
  "v_mov_b32 v81, 0\n" \
  "v_mov_b32 v82, 0\n" \
  "v_mov_b32 v83, 0\n" \
  "v_mov_b32 v84, 0\n" \
  "v_mov_b32 v85, 0\n" \
  "v_mov_b32 v86, 0\n" \
  "v_mov_b32 v87, 0\n" \
  "v_mov_b32 v88, 0\n" \
  "v_mov_b32 v89, 0\n" \
  "v_mov_b32 v90, 0\n" \
  "v_mov_b32 v91, 0\n" \
  "v_mov_b32 v16, 0\n" \
  "v_mov_b32 v17, 0\n" \
  "v_mov_b32 v18, 0\n" \
  "v_mov_b32 v19, 0\n" \
  "v_mov_b32 v20, 0\n" \
  "v_mov_b32 v21, 0\n" \
  "v_mov_b32 v22, 0\n" \
  "v_mov_b32 v23, 0\n" \
  "v_mov_b32 v24, 0\n" \
  "v_mov_b32 v25, 0\n" \
  "v_mov_b32 v26, 0\n" \
  "v_mov_b32 v27, 0\n" \
  "v_mov_b32 v104, 0\n" \
  "v_mov_b32 v105, 0\n" \
  "v_mov_b32 v106, 0\n" \
  "v_mov_b32 v107, 0\n" \
  "v_readfirstlane_b32 s86, v34\n" \
  "v_readlane_b32 s43, v34, s53\n" \
  "s_mov_b32 m0, 0\n" \
  "LWA22_%=:\n" \
  "s_memrealtime s[74:75]\n" \
  "s_memtime s[76:77]\n" \
  "s_waitcnt lgkmcnt(0)\n" \
  "s_sub_i32 s72, s88, s74\n" \
  "s_mul_i32 s72, s72, s90\n" \
  "s_ashr_i32 s72, s72, 8\n" \
  "s_sub_i32 s71, s89, s76\n" \
  "s_min_i32 s71, s71, s72\n" \
  "s_sub_i32 s72, s72, 92\n" \
  "s_max_i32 s71, s71, s72\n" \
  "s_add_u32 s89, s71, s76\n" \
  "s_cmp_gt_i32 s71, 168\n" \
  "s_cbranch_scc1 LWA22_%=\n" \
  "s_cmp_lt_i32 s71, s94\n" \
  "s_cbranch_scc1 LSA22_%=\n" \
  "s_max_i32 s71, s71, 48\n" \
  "s_lshr_b32 s71, s71, 3\n" \
  "s_lshl_b32 s71, s71, 2\n" \
  "s_sub_u32 s78, s82, s71\n" \
  "s_subb_u32 s79, s83, 0\n" \
  "s_setpc_b64 s[78:79]\n" \
  "s_nop 1\n" \
  "s_nop 1\n" \
  "s_nop 1\n" \
  "s_nop 1\n" \
  "s_nop 1\n" \
  "s_nop 1\n" \
  "s_nop 1\n" \
  "s_nop 1\n" \
  "s_nop 1\n" \
  "s_nop 1\n" \
  "s_nop 1\n" \
  "s_nop 1\n" \
  "s_nop 1\n" \
  "s_nop 1\n" \
  "s_nop 1\n" \
  "s_nop 1\n" \
  "LEA22_%=:\n" \
  "buffer_load_dwordx4 v1, s[36:39], s67 offen sc1 lds\n" \
  "buffer_load_dwordx4 v1, s[36:39], s67 offen offset:1024 sc1 lds\n" \
  "buffer_load_dwordx4 v1, s[36:39], s67 offen offset:2048 sc1 lds\n" \
  "buffer_load_dwordx4 v1, s[36:39], s67 offen offset:3072 sc1 lds\n" \
  "s_mov_b32 m0, 4096\n" \
  "s_nop 0\n" \
  "buffer_load_dwordx4 v2, s[36:39], s67 offen sc1 lds\n" \
  "buffer_load_dwordx4 v2, s[36:39], s67 offen offset:1024 sc1 lds\n" \
  "buffer_load_dwordx4 v2, s[36:39], s67 offen offset:2048 sc1 lds\n" \
  "buffer_load_dwordx4 v2, s[36:39], s67 offen offset:3072 sc1 lds\n" \
  "s_mov_b32 m0, 8192\n" \
  "s_nop 0\n" \
  "buffer_load_dwordx4 v3, s[36:39], s67 offen sc1 lds\n" \
  "buffer_load_dwordx4 v3, s[36:39], s67 offen offset:1024 sc1 lds\n" \
  "s_mov_b32 s65, s66\n" \
  "s_xor_b32 s69, s66, 0x2000\n" \
  "s_add_u32 s64, s64, 1\n" \
  "s_barrier\n" \
  "v_readlane_b32 s66, v28, s64\n" \
  "s_add_u32 s40, s65, 131072\n" \
  "s_add_u32 s41, s65, 262144\n" \
  "s_add_u32 s42, s65, 393216\n" \
  "s_cmp_eq_u32 s34, s64\n" \
  "s_cbranch_scc1 LRT22_%=\n" \
  "s_mov_b32 s34, s64\n" \
  "s_lshr_b32 s71, s86, 8\n" \
  "s_and_b32 s72, s86, 0xff\n" \
  "s_cselect_b32 s61, s72, s61\n" \
  "s_cselect_b32 s71, s71, s92\n" \
  "s_sub_i32 s72, s71, s92\n" \
  "s_add_u32 s92, s71, s61\n" \
  "s_lshl_b32 s72, s72, 1\n" \
  "s_add_u32 s88, s88, s72\n" \
  "s_mul_i32 s72, s72, 24\n" \
  "s_add_u32 s89, s89, s72\n" \
  "s_cmp_lg_u32 s43, 0\n" \
  "s_cselect_b32 s90, s43, s90\n" \
  "LRT22_%=:\n" \
  "LM22_%=:\n" \
  "s_waitcnt vmcnt(9)\n" \
  "s_barrier\n" \
  "ds_read_b128 v[40:43], v5\n" \
  "s_waitcnt vmcnt(8)\n" \
  "s_barrier\n" \
  "ds_read_b128 v[44:47], v5 offset:1024\n" \
  "s_waitcnt lgkmcnt(1)\n" \
  "v_smfmac_f32_16x16x64_bf16 v[80:83], v[40:43], a[0:7], v0\n" \
  "s_waitcnt vmcnt(6)\n" \
  "v_smfmac_f32_16x16x64_bf16 v[84:87], v[40:43], a[80:87], v0\n" \
  "s_barrier\n" \
  "ds_read_b128 v[48:51], v5 offset:2048\n" \
  "v_smfmac_f32_16x16x64_bf16 v[88:91], v[40:43], a[160:167], v0\n" \
  "s_waitcnt lgkmcnt(1)\n" \
  "ds_read_b128 v[52:55], v5 offset:3072\n" \
  "v_smfmac_f32_16x16x64_bf16 v[80:83], v[44:47], a[8:15], v0\n" \
  "v_smfmac_f32_16x16x64_bf16 v[84:87], v[44:47], a[88:95], v0\n" \
  "s_waitcnt vmcnt(4)\n" \
  "v_smfmac_f32_16x16x64_bf16 v[88:91], v[44:47], a[168:175], v0\n" \
  "s_barrier\n" \
  "s_waitcnt lgkmcnt(1)\n" \
  "ds_read_b128 v[56:59], v5 offset:4096\n" \
  "v_smfmac_f32_16x16x64_bf16 v[80:83], v[48:51], a[16:23], v0\n" \
  "ds_read_b128 v[60:63], v5 offset:5120\n" \
  "v_smfmac_f32_16x16x64_bf16 v[84:87], v[48:51], a[96:103], v0\n" \
  "v_smfmac_f32_16x16x64_bf16 v[88:91], v[48:51], a[176:183], v0\n" \
  "s_waitcnt vmcnt(2)\n" \
  "s_waitcnt lgkmcnt(2)\n" \
  "v_smfmac_f32_16x16x64_bf16 v[80:83], v[52:55], a[24:31], v0\n" \
  "s_barrier\n" \
  "ds_read_b128 v[64:67], v5 offset:6144\n" \
  "v_smfmac_f32_16x16x64_bf16 v[84:87], v[52:55], a[104:111], v0\n" \
  "s_waitcnt lgkmcnt(2)\n" \
  "ds_read_b128 v[68:71], v5 offset:7168\n" \
  "v_smfmac_f32_16x16x64_bf16 v[80:83], v[56:59], a[32:39], v0\n" \
  "v_smfmac_f32_16x16x64_bf16 v[84:87], v[56:59], a[112:119], v0\n" \
  "s_waitcnt vmcnt(0)\n" \
  "s_waitcnt lgkmcnt(2)\n" \
  "v_smfmac_f32_16x16x64_bf16 v[80:83], v[60:63], a[40:47], v0\n" \
  "s_barrier\n" \
  "ds_read2_b32 v[72:73], v6 offset0:0 offset1:32\n" \
  "ds_read2_b32 v[74:75], v6 offset0:64 offset1:96\n" \
  "v_smfmac_f32_16x16x64_bf16 v[84:87], v[60:63], a[120:127], v0\n" \
  "s_waitcnt lgkmcnt(3)\n" \
  "ds_read2_b32 v[76:77], v7 offset0:0 offset1:32\n" \
  "ds_read2_b32 v[78:79], v7 offset0:64 offset1:96\n" \
  "v_smfmac_f32_16x16x64_bf16 v[80:83], v[64:67], a[48:55], v0\n" \
  "v_smfmac_f32_16x16x64_bf16 v[84:87], v[64:67], a[128:135], v0\n" \
  "s_waitcnt lgkmcnt(4)\n" \
  "v_smfmac_f32_16x16x64_bf16 v[80:83], v[68:71], a[56:63], v0\n" \
  "v_smfmac_f32_16x16x64_bf16 v[84:87], v[68:71], a[136:143], v0\n" \
  "s_waitcnt lgkmcnt(2)\n" \
  "v_smfmac_f32_16x16x64_bf16 v[80:83], v[72:75], a[64:71], v0\n" \
  "v_smfmac_f32_16x16x64_bf16 v[84:87], v[72:75], a[144:151], v0\n" \
  "s_waitcnt lgkmcnt(0)\n" \
  "ds_read_b128 v[40:43], v11 offset:8192\n" \
  "v_smfmac_f32_16x16x64_bf16 v[80:83], v[76:79], a[72:79], v0\n" \
  "ds_read_b128 v[44:47], v11 offset:9216\n" \
  "v_smfmac_f32_16x16x64_bf16 v[84:87], v[76:79], a[152:159], v0\n" \
  "v_smfmac_f32_16x16x64_bf16 v[88:91], v[52:55], a[184:191], v0\n" \
  "ds_read_b128 v[48:51], v11 offset:49152\n" \
  "v_smfmac_f32_16x16x64_bf16 v[88:91], v[56:59], a[192:199], v0\n" \
  "ds_read_b128 v[52:55], v11 offset:50176\n" \
  "v_permlane32_swap_b32 v80, v84\n" \
  "v_smfmac_f32_16x16x64_bf16 v[88:91], v[60:63], a[200:207], v0\n" \
  "ds_read_b128 v[56:59], v11 offset:0\n" \
  "v_permlane32_swap_b32 v81, v85\n" \
  "v_smfmac_f32_16x16x64_bf16 v[88:91], v[64:67], a[208:215], v0\n" \
  "ds_read_b128 v[60:63], v11 offset:1024\n" \
  "v_permlane32_swap_b32 v82, v86\n" \
  "v_smfmac_f32_16x16x64_bf16 v[88:91], v[68:71], a[216:223], v0\n" \
  "ds_read_b128 v[64:67], v11 offset:40960\n" \
  "v_permlane32_swap_b32 v83, v87\n" \
  "v_smfmac_f32_16x16x64_bf16 v[88:91], v[72:75], a[224:231], v0\n" \
  "ds_read_b128 v[68:71], v11 offset:41984\n" \
  "v_add_f32 v108, v80, v84\n" \
  "v_smfmac_f32_16x16x64_bf16 v[88:91], v[76:79], a[232:239], v0\n" \
  "s_nop 1\n" \
  "v_add_f32 v109, v81, v85\n" \
  "v_add_f32 v110, v82, v86\n" \
  "v_add_f32 v111, v83, v87\n" \
  "v_cvt_pk_bf16_f32 v32, v108, v109\n" \
  "v_cvt_pk_bf16_f32 v33, v110, v111\n" \
  "s_nop 0\n" \
  "v_permlane32_swap_b32 v88, v90\n" \
  "v_permlane32_swap_b32 v89, v91\n" \
  "v_pk_add_f32 v[80:81], v[88:89], v[90:91]\n" \
  "v_cvt_pk_bf16_f32 v12, v80, v81\n" \
  "v_cmp_u_f32 vcc, v80, v81\n" \
  "ds_write_b64 v8, v[32:33]\n" \
  "ds_write_b32 v9, v12\n" \
  "s_cbranch_vccnz LMF22_%=\n" \
  "LMOK22_%=:\n" \
  "s_waitcnt lgkmcnt(0)\n" \
  "s_barrier\n" \
  "ds_read_b64_tr_b16 v[80:81], v10\n" \
  "ds_read_b64_tr_b16 v[82:83], v10 offset:32\n" \
  "ds_read_b64_tr_b16 v[84:85], v10 offset:512\n" \
  "ds_read_b64_tr_b16 v[86:87], v10 offset:544\n" \
  "ds_read_b64_tr_b16 v[88:89], v10 offset:1024\n" \
  "ds_read_b64_tr_b16 v[90:91], v10 offset:1056\n" \
  "ds_read_b64_tr_b16 v[92:93], v10 offset:1536\n" \
  "ds_read_b64_tr_b16 v[94:95], v10 offset:1568\n" \
  "ds_read_b64_tr_b16 v[36:37], v10 offset:2048\n" \
  "ds_read_b64_tr_b16 v[38:39], v10 offset:2080\n" \
  "ds_read_b64_tr_b16 v[100:101], v10 offset:2560\n" \
  "ds_read_b64_tr_b16 v[102:103], v10 offset:2592\n" \
  "s_waitcnt lgkmcnt(10)\n" \
  "v_smfmac_f32_16x16x64_bf16 v[16:19], v[80:83], v[240:247], v0\n" \
  "s_waitcnt lgkmcnt(8)\n" \
  "v_smfmac_f32_16x16x64_bf16 v[16:19], v[84:87], v[248:255], v0\n" \
  "s_waitcnt lgkmcnt(6)\n" \
  "v_smfmac_f32_16x16x64_bf16 v[16:19], v[88:91], a[240:247], v0\n" \
  "s_waitcnt lgkmcnt(4)\n" \
  "ds_read_b128 v[72:75], v11 offset:2048\n" \
  "v_smfmac_f32_16x16x64_bf16 v[16:19], v[92:95], a[248:255], v0\n" \
  "s_waitcnt lgkmcnt(3)\n" \
  "ds_read_b128 v[76:79], v11 offset:3072\n" \
  "v_smfmac_f32_16x16x64_bf16 v[16:19], v[36:39], v[40:47], v0\n" \
  "s_waitcnt lgkmcnt(2)\n" \
  "ds_read_b128 v[40:43], v11 offset:43008\n" \
  "v_smfmac_f32_16x16x64_bf16 v[16:19], v[100:103], v[48:55], v0\n" \
  "ds_read_b128 v[44:47], v11 offset:44032\n" \
  "v_smfmac_f32_16x16x64_bf16 v[20:23], v[80:83], v[112:119], v0\n" \
  "ds_read_b128 v[48:51], v11 offset:4096\n" \
  "v_smfmac_f32_16x16x64_bf16 v[20:23], v[84:87], v[120:127], v0\n" \
  "ds_read_b128 v[52:55], v11 offset:5120\n" \
  "v_cvt_pk_bf16_f32 v108, v16, v17\n" \
  "v_smfmac_f32_16x16x64_bf16 v[20:23], v[88:91], v[128:135], v0\n" \
  "v_cvt_pk_bf16_f32 v109, v18, v19\n" \
  "v_smfmac_f32_16x16x64_bf16 v[20:23], v[92:95], v[136:143], v0\n" \
  "buffer_store_dwordx2 v[108:109], v4, s[36:39], s65 offen sc1\n" \
  "v_smfmac_f32_16x16x64_bf16 v[20:23], v[36:39], v[56:63], v0\n" \
  "ds_read_b128 v[56:59], v11 offset:45056\n" \
  "v_smfmac_f32_16x16x64_bf16 v[20:23], v[100:103], v[64:71], v0\n" \
  "ds_read_b128 v[60:63], v11 offset:46080\n" \
  "v_smfmac_f32_16x16x64_bf16 v[104:107], v[80:83], v[144:151], v0\n" \
  "ds_read_b128 v[64:67], v11 offset:6144\n" \
  "v_smfmac_f32_16x16x64_bf16 v[104:107], v[84:87], v[152:159], v0\n" \
  "ds_read_b128 v[68:71], v11 offset:7168\n" \
  "v_cvt_pk_bf16_f32 v110, v20, v21\n" \
  "v_smfmac_f32_16x16x64_bf16 v[104:107], v[88:91], v[160:167], v0\n" \
  "v_cvt_pk_bf16_f32 v111, v22, v23\n" \
  "v_smfmac_f32_16x16x64_bf16 v[104:107], v[92:95], v[168:175], v0\n" \
  "buffer_store_dwordx2 v[110:111], v13, s[36:39], s65 offen sc1\n" \
  "v_mov_b32 v16, 0\n" \
  "s_waitcnt lgkmcnt(8)\n" \
  "v_smfmac_f32_16x16x64_bf16 v[104:107], v[36:39], v[72:79], v0\n" \
  "s_waitcnt lgkmcnt(6)\n" \
  "ds_read_b128 v[72:75], v11 offset:47104\n" \
  "v_mov_b32 v17, 0\n" \
  "v_smfmac_f32_16x16x64_bf16 v[104:107], v[100:103], v[40:47], v0\n" \
  "ds_read_b128 v[76:79], v11 offset:48128\n" \
  "v_mov_b32 v18, 0\n" \
  "v_smfmac_f32_16x16x64_bf16 v[24:27], v[80:83], v[176:183], v0\n" \
  "v_mov_b32 v19, 0\n" \
  "v_smfmac_f32_16x16x64_bf16 v[24:27], v[84:87], v[184:191], v0\n" \
  "v_cvt_pk_bf16_f32 v108, v104, v105\n" \
  "v_smfmac_f32_16x16x64_bf16 v[24:27], v[88:91], v[192:199], v0\n" \
  "v_cvt_pk_bf16_f32 v109, v106, v107\n" \
  "v_smfmac_f32_16x16x64_bf16 v[24:27], v[92:95], v[200:207], v0\n" \
  "buffer_store_dwordx2 v[108:109], v13, s[36:39], s40 offen sc1\n" \
  "s_waitcnt lgkmcnt(6)\n" \
  "v_smfmac_f32_16x16x64_bf16 v[24:27], v[36:39], v[48:55], v0\n" \
  "s_waitcnt lgkmcnt(4)\n" \
  "v_smfmac_f32_16x16x64_bf16 v[24:27], v[100:103], v[56:63], v0\n" \
  "v_smfmac_f32_16x16x64_bf16 v[16:19], v[80:83], v[208:215], v0\n" \
  "v_smfmac_f32_16x16x64_bf16 v[16:19], v[84:87], v[216:223], v0\n" \
  "v_cvt_pk_bf16_f32 v110, v24, v25\n" \
  "v_smfmac_f32_16x16x64_bf16 v[16:19], v[88:91], v[224:231], v0\n" \
  "v_cvt_pk_bf16_f32 v111, v26, v27\n" \
  "v_smfmac_f32_16x16x64_bf16 v[16:19], v[92:95], v[232:239], v0\n" \
  "buffer_store_dwordx2 v[110:111], v13, s[36:39], s41 offen sc1\n" \
  "s_waitcnt lgkmcnt(2)\n" \
  "v_smfmac_f32_16x16x64_bf16 v[16:19], v[36:39], v[64:71], v0\n" \
  "s_waitcnt lgkmcnt(0)\n" \
  "v_smfmac_f32_16x16x64_bf16 v[16:19], v[100:103], v[72:79], v0\n" \
  "s_nop 3\n" \
  "v_mov_b32 v31, v31\n" \
  "s_nop 2\n" \
  "v_cvt_pk_bf16_f32 v108, v16, v17\n" \
  "v_cvt_pk_bf16_f32 v109, v18, v19\n" \
  "buffer_store_dwordx2 v[108:109], v13, s[36:39], s42 offen sc1\n" \
  "s_mov_b32 s67, s66\n" \
  "s_cmp_eq_u32 s64, 64\n" \
  "s_cbranch_scc1 LDONE_%=\n" \
  "s_add_u32 s72, s50, s61\n" \
  "s_add_u32 s88, s88, s72\n" \
  "s_mul_i32 s91, s72, s90\n" \
  "s_lshr_b32 s91, s91, 8\n" \
  "s_add_u32 s89, s89, s91\n" \
  "s_add_u32 s72, s51, s61\n" \
  "s_add_u32 s88, s88, s72\n" \
  "s_mul_i32 s91, s72, s90\n" \
  "s_lshr_b32 s91, s91, 8\n" \
  "s_add_u32 s89, s89, s91\n" \
  "s_memtime s[76:77]\n" \
  "s_memrealtime s[74:75]\n" \
  "s_mov_b32 s71, 0\n" \
  "s_nop 1\n" \
  "s_memrealtime s[80:81]\n" \
  "s_nop 2\n" \
  "s_memrealtime s[78:79]\n" \
  "s_nop 2\n" \
  "s_memrealtime s[44:45]\n" \
  "s_nop 2\n" \
  "s_memrealtime s[46:47]\n" \
  "s_waitcnt lgkmcnt(3)\n" \
  "s_cmp_eq_u32 s80, s74\n" \
  "s_addc_u32 s71, s71, 0\n" \
  "s_memrealtime s[80:81]\n" \
  "s_waitcnt lgkmcnt(3)\n" \
  "s_cmp_eq_u32 s78, s74\n" \
  "s_addc_u32 s71, s71, 0\n" \
  "s_memrealtime s[78:79]\n" \
  "s_waitcnt lgkmcnt(3)\n" \
  "s_cmp_eq_u32 s44, s74\n" \
  "s_addc_u32 s71, s71, 0\n" \
  "s_waitcnt lgkmcnt(0)\n" \
  "s_cmp_eq_u32 s46, s74\n" \
  "s_addc_u32 s71, s71, 0\n" \
  "s_cmp_eq_u32 s80, s74\n" \
  "s_addc_u32 s71, s71, 0\n" \
  "s_cmp_eq_u32 s78, s74\n" \
  "s_addc_u32 s71, s71, 0\n" \
  "s_lshl_b32 s71, s71, 4\n" \
  "s_add_u32 s76, s76, s71\n" \
  "s_sub_u32 s71, s76, s96\n" \
  "s_sub_u32 s72, s74, s95\n" \
  "s_mov_b32 s95, s74\n" \
  "s_mov_b32 s96, s76\n" \
  "v_cvt_f32_u32 v110, s71\n" \
  "v_cvt_f32_u32 v111, s72\n" \
  "v_rcp_f32 v111, v111\n" \
  "v_mul_f32 v110, 0x43810000, v110\n" \
  "v_mul_f32 v110, v110, v111\n" \
  "v_cvt_u32_f32 v110, v110\n" \
  "v_min_u32 v110, 6400, v110\n" \
  "v_max_u32 v110, 3072, v110\n" \
  "s_nop 0\n" \
  "v_readfirstlane_b32 s23, v110\n" \
  "s_add_u32 s22, s22, 20\n" \
  "s_min_u32 s22, s22, s23\n" \
  "v_readlane_b32 s71, v34, 2\n" \
  "s_sub_u32 s71, s71, 1\n" \
  "s_min_u32 s22, s22, s71\n" \
  "v_readlane_b32 s71, v34, 3\n" \
  "s_sub_u32 s71, s71, 1\n" \
  "s_min_u32 s22, s22, s71\n" \
  "v_cvt_f32_u32 v110, s22\n" \
  "v_rcp_f32 v110, v110\n" \
  "v_mov_b32 v111, 0xc0b00000\n" \
  "v_fmac_f32 v111, 0x48cad280, v110\n" \
  "v_cvt_u32_f32 v111, v111\n" \
  "s_cmp_lt_u32 s64, s99\n" \
  "s_cselect_b32 s98, s97, s98\n" \
  "s_add_u32 s72, s64, 3\n" \
  "s_cmp_lg_u32 s98, s97\n" \
  "s_cselect_b32 s99, s72, s99\n" \
  "s_addc_u32 s18, s18, 0\n" \
  "s_mov_b32 s98, s97\n" \
  "s_sub_i32 s72, 14, s64\n" \
  "s_max_i32 s72, s72, 0\n" \
  "s_lshr_b32 s72, s72, 1\n" \
  "s_add_u32 s72, s72, s18\n" \
  "v_readfirstlane_b32 s71, v111\n" \
  "s_add_u32 s71, s71, s72\n" \
  "s_cmp_eq_u32 s19, 0\n" \
  "s_cselect_b32 s19, s61, s19\n" \
  "s_add_u32 s72, s92, s19\n" \
  "s_lshl_b32 s72, s72, 8\n" \
  "s_or_b32 s72, s72, s71\n" \
  "s_mov_b32 s19, s71\n" \
  "v_mov_b32 v110, s23\n" \
  "v_writelane_b32 v110, s72, 0\n" \
  "s_lshl_b32 s72, s64, 7\n" \
  "buffer_store_dword v110, v30, s[36:39], s72 offen offset:128 sc1\n" \
  "buffer_load_dword v34, v29, s[36:39], s72 offen sc1\n" \
  "s_waitcnt vmcnt(0)\n" \
  "s_branch LOOP22_%=\n" \
  "LSA22_%=:\n" \
  "s_add_u32 s88, s88, s52\n" \
  "s_add_u32 s89, s89, s93\n" \
  "s_add_u32 s97, s97, 1\n" \
  "s_branch LWA22_%=\n" \
  "LMF22_%=:\n" \
  "v_mbcnt_lo_u32_b32 v94, -1, 0\n" \
  "v_mbcnt_hi_u32_b32 v94, -1, v94\n" \
  "v_lshlrev_b32 v94, 4, v94\n" \
  "v_mov_b32 v92, 0\n" \
  "ds_read_b128 v[72:75], v94 offset:0\n" \
  "ds_read_b128 v[76:79], v94 offset:1024\n" \
  "ds_read_b128 v[80:83], v94 offset:2048\n" \
  "ds_read_b128 v[84:87], v94 offset:3072\n" \
  "ds_read_b128 v[88:91], v94 offset:4096\n" \
  "s_waitcnt lgkmcnt(0)\n" \
  "v_max3_u32 v92, v92, v72, v73\n" \
  "v_max3_u32 v92, v92, v74, v75\n" \
  "v_max3_u32 v92, v92, v76, v77\n" \
  "v_max3_u32 v92, v92, v78, v79\n" \
  "v_max3_u32 v92, v92, v80, v81\n" \
  "v_max3_u32 v92, v92, v82, v83\n" \
  "v_max3_u32 v92, v92, v84, v85\n" \
  "v_max3_u32 v92, v92, v86, v87\n" \
  "v_max3_u32 v92, v92, v88, v89\n" \
  "v_max3_u32 v92, v92, v90, v91\n" \
  "ds_read_b128 v[72:75], v94 offset:5120\n" \
  "ds_read_b128 v[76:79], v94 offset:6144\n" \
  "ds_read_b128 v[80:83], v94 offset:7168\n" \
  "ds_read_b128 v[84:87], v94 offset:8192\n" \
  "ds_read_b128 v[88:91], v94 offset:9216\n" \
  "s_waitcnt lgkmcnt(0)\n" \
  "v_max3_u32 v92, v92, v72, v73\n" \
  "v_max3_u32 v92, v92, v74, v75\n" \
  "v_max3_u32 v92, v92, v76, v77\n" \
  "v_max3_u32 v92, v92, v78, v79\n" \
  "v_max3_u32 v92, v92, v80, v81\n" \
  "v_max3_u32 v92, v92, v82, v83\n" \
  "v_max3_u32 v92, v92, v84, v85\n" \
  "v_max3_u32 v92, v92, v86, v87\n" \
  "v_max3_u32 v92, v92, v88, v89\n" \
  "v_max3_u32 v92, v92, v90, v91\n" \
  "v_cmp_eq_u32 vcc, -1, v92\n" \
  "s_cbranch_vccz LMOK22_%=\n" \
  "s_cmp_gt_u32 s58, 4000\n" \
  "s_cbranch_scc1 LMOK22_%=\n" \
  "s_add_u32 s58, s58, 1\n" \
  "s_sub_u32 s64, s64, 1\n" \
  "s_mov_b32 s66, s65\n" \
  "s_add_u32 s88, s88, s52\n" \
  "s_add_u32 s89, s89, s93\n" \
  "s_add_u32 s97, s97, 1\n" \
  "s_waitcnt vmcnt(0) lgkmcnt(0)\n" \
  "s_barrier\n" \
  "s_branch LOOP22_%=\n" \
  "LSTART23_%=:\n" \
  "LOOP23_%=:\n" \
  "v_mov_b32 v80, 0\n" \
  "v_mov_b32 v81, 0\n" \
  "v_mov_b32 v82, 0\n" \
  "v_mov_b32 v83, 0\n" \
  "v_mov_b32 v84, 0\n" \
  "v_mov_b32 v85, 0\n" \
  "v_mov_b32 v86, 0\n" \
  "v_mov_b32 v87, 0\n" \
  "v_mov_b32 v88, 0\n" \
  "v_mov_b32 v89, 0\n" \
  "v_mov_b32 v90, 0\n" \
  "v_mov_b32 v91, 0\n" \
  "v_mov_b32 v16, 0\n" \
  "v_mov_b32 v17, 0\n" \
  "v_mov_b32 v18, 0\n" \
  "v_mov_b32 v19, 0\n" \
  "v_mov_b32 v20, 0\n" \
  "v_mov_b32 v21, 0\n" \
  "v_mov_b32 v22, 0\n" \
  "v_mov_b32 v23, 0\n" \
  "v_mov_b32 v24, 0\n" \
  "v_mov_b32 v25, 0\n" \
  "v_mov_b32 v26, 0\n" \
  "v_mov_b32 v27, 0\n" \
  "v_mov_b32 v104, 0\n" \
  "v_mov_b32 v105, 0\n" \
  "v_mov_b32 v106, 0\n" \
  "v_mov_b32 v107, 0\n" \
  "s_mov_b32 s65, s66\n" \
  "s_xor_b32 s69, s66, 0x2000\n" \
  "v_mov_b32 v34, s64\n" \
  "s_add_u32 s64, s64, 1\n" \
  "v_readlane_b32 s66, v28, s64\n" \
  "s_add_u32 s40, s65, 131072\n" \
  "s_add_u32 s41, s65, 262144\n" \
  "s_add_u32 s42, s65, 393216\n" \
  "s_barrier\n" \
  "LM23_%=:\n" \
  "s_barrier\n" \
  "ds_read_b128 v[40:43], v5\n" \
  "s_barrier\n" \
  "ds_read_b128 v[44:47], v5 offset:1024\n" \
  "s_waitcnt lgkmcnt(1)\n" \
  "v_smfmac_f32_16x16x64_bf16 v[80:83], v[40:43], a[0:7], v0\n" \
  "v_smfmac_f32_16x16x64_bf16 v[84:87], v[40:43], a[80:87], v0\n" \
  "s_barrier\n" \
  "ds_read_b128 v[48:51], v5 offset:2048\n" \
  "v_smfmac_f32_16x16x64_bf16 v[88:91], v[40:43], a[160:167], v0\n" \
  "s_waitcnt lgkmcnt(1)\n" \
  "ds_read_b128 v[52:55], v5 offset:3072\n" \
  "v_smfmac_f32_16x16x64_bf16 v[80:83], v[44:47], a[8:15], v0\n" \
  "v_smfmac_f32_16x16x64_bf16 v[84:87], v[44:47], a[88:95], v0\n" \
  "v_smfmac_f32_16x16x64_bf16 v[88:91], v[44:47], a[168:175], v0\n" \
  "s_barrier\n" \
  "s_waitcnt lgkmcnt(1)\n" \
  "ds_read_b128 v[56:59], v5 offset:4096\n" \
  "v_smfmac_f32_16x16x64_bf16 v[80:83], v[48:51], a[16:23], v0\n" \
  "ds_read_b128 v[60:63], v5 offset:5120\n" \
  "v_smfmac_f32_16x16x64_bf16 v[84:87], v[48:51], a[96:103], v0\n" \
  "v_smfmac_f32_16x16x64_bf16 v[88:91], v[48:51], a[176:183], v0\n" \
  "s_waitcnt lgkmcnt(2)\n" \
  "v_smfmac_f32_16x16x64_bf16 v[80:83], v[52:55], a[24:31], v0\n" \
  "s_barrier\n" \
  "ds_read_b128 v[64:67], v5 offset:6144\n" \
  "v_smfmac_f32_16x16x64_bf16 v[84:87], v[52:55], a[104:111], v0\n" \
  "s_waitcnt lgkmcnt(2)\n" \
  "ds_read_b128 v[68:71], v5 offset:7168\n" \
  "v_smfmac_f32_16x16x64_bf16 v[80:83], v[56:59], a[32:39], v0\n" \
  "v_smfmac_f32_16x16x64_bf16 v[84:87], v[56:59], a[112:119], v0\n" \
  "s_waitcnt lgkmcnt(2)\n" \
  "v_smfmac_f32_16x16x64_bf16 v[80:83], v[60:63], a[40:47], v0\n" \
  "s_barrier\n" \
  "ds_read2_b32 v[72:73], v6 offset0:0 offset1:32\n" \
  "ds_read2_b32 v[74:75], v6 offset0:64 offset1:96\n" \
  "v_smfmac_f32_16x16x64_bf16 v[84:87], v[60:63], a[120:127], v0\n" \
  "s_waitcnt lgkmcnt(3)\n" \
  "ds_read2_b32 v[76:77], v7 offset0:0 offset1:32\n" \
  "ds_read2_b32 v[78:79], v7 offset0:64 offset1:96\n" \
  "v_smfmac_f32_16x16x64_bf16 v[80:83], v[64:67], a[48:55], v0\n" \
  "v_smfmac_f32_16x16x64_bf16 v[84:87], v[64:67], a[128:135], v0\n" \
  "s_waitcnt lgkmcnt(4)\n" \
  "v_smfmac_f32_16x16x64_bf16 v[80:83], v[68:71], a[56:63], v0\n" \
  "v_smfmac_f32_16x16x64_bf16 v[84:87], v[68:71], a[136:143], v0\n" \
  "s_waitcnt lgkmcnt(2)\n" \
  "v_smfmac_f32_16x16x64_bf16 v[80:83], v[72:75], a[64:71], v0\n" \
  "v_smfmac_f32_16x16x64_bf16 v[84:87], v[72:75], a[144:151], v0\n" \
  "s_waitcnt lgkmcnt(0)\n" \
  "ds_read_b128 v[40:43], v11 offset:8192\n" \
  "v_smfmac_f32_16x16x64_bf16 v[80:83], v[76:79], a[72:79], v0\n" \
  "ds_read_b128 v[44:47], v11 offset:9216\n" \
  "v_smfmac_f32_16x16x64_bf16 v[84:87], v[76:79], a[152:159], v0\n" \
  "v_smfmac_f32_16x16x64_bf16 v[88:91], v[52:55], a[184:191], v0\n" \
  "ds_read_b128 v[48:51], v11 offset:49152\n" \
  "v_smfmac_f32_16x16x64_bf16 v[88:91], v[56:59], a[192:199], v0\n" \
  "ds_read_b128 v[52:55], v11 offset:50176\n" \
  "v_permlane32_swap_b32 v80, v84\n" \
  "v_smfmac_f32_16x16x64_bf16 v[88:91], v[60:63], a[200:207], v0\n" \
  "ds_read_b128 v[56:59], v11 offset:0\n" \
  "v_permlane32_swap_b32 v81, v85\n" \
  "v_smfmac_f32_16x16x64_bf16 v[88:91], v[64:67], a[208:215], v0\n" \
  "ds_read_b128 v[60:63], v11 offset:1024\n" \
  "v_permlane32_swap_b32 v82, v86\n" \
  "v_smfmac_f32_16x16x64_bf16 v[88:91], v[68:71], a[216:223], v0\n" \
  "ds_read_b128 v[64:67], v11 offset:40960\n" \
  "v_permlane32_swap_b32 v83, v87\n" \
  "v_smfmac_f32_16x16x64_bf16 v[88:91], v[72:75], a[224:231], v0\n" \
  "ds_read_b128 v[68:71], v11 offset:41984\n" \
  "v_add_f32 v108, v80, v84\n" \
  "v_smfmac_f32_16x16x64_bf16 v[88:91], v[76:79], a[232:239], v0\n" \
  "s_nop 1\n" \
  "v_add_f32 v109, v81, v85\n" \
  "v_add_f32 v110, v82, v86\n" \
  "v_add_f32 v111, v83, v87\n" \
  "v_cvt_pk_bf16_f32 v32, v108, v109\n" \
  "v_cvt_pk_bf16_f32 v33, v110, v111\n" \
  "s_nop 0\n" \
  "v_permlane32_swap_b32 v88, v90\n" \
  "v_permlane32_swap_b32 v89, v91\n" \
  "v_pk_add_f32 v[80:81], v[88:89], v[90:91]\n" \
  "v_cvt_pk_bf16_f32 v12, v80, v81\n" \
  "v_cmp_u_f32 vcc, v80, v81\n" \
  "ds_write_b64 v8, v[32:33]\n" \
  "ds_write_b32 v9, v12\n" \
  "s_cbranch_vccnz LMF23_%=\n" \
  "LMOK23_%=:\n" \
  "s_waitcnt lgkmcnt(0)\n" \
  "s_barrier\n" \
  "ds_read_b64_tr_b16 v[80:81], v10\n" \
  "ds_read_b64_tr_b16 v[82:83], v10 offset:32\n" \
  "ds_read_b64_tr_b16 v[84:85], v10 offset:512\n" \
  "ds_read_b64_tr_b16 v[86:87], v10 offset:544\n" \
  "ds_read_b64_tr_b16 v[88:89], v10 offset:1024\n" \
  "ds_read_b64_tr_b16 v[90:91], v10 offset:1056\n" \
  "ds_read_b64_tr_b16 v[92:93], v10 offset:1536\n" \
  "ds_read_b64_tr_b16 v[94:95], v10 offset:1568\n" \
  "ds_read_b64_tr_b16 v[36:37], v10 offset:2048\n" \
  "ds_read_b64_tr_b16 v[38:39], v10 offset:2080\n" \
  "ds_read_b64_tr_b16 v[100:101], v10 offset:2560\n" \
  "ds_read_b64_tr_b16 v[102:103], v10 offset:2592\n" \
  "s_waitcnt lgkmcnt(10)\n" \
  "v_smfmac_f32_16x16x64_bf16 v[16:19], v[80:83], v[240:247], v0\n" \
  "s_waitcnt lgkmcnt(8)\n" \
  "v_smfmac_f32_16x16x64_bf16 v[16:19], v[84:87], v[248:255], v0\n" \
  "s_waitcnt lgkmcnt(6)\n" \
  "v_smfmac_f32_16x16x64_bf16 v[16:19], v[88:91], a[240:247], v0\n" \
  "s_waitcnt lgkmcnt(4)\n" \
  "ds_read_b128 v[72:75], v11 offset:2048\n" \
  "v_smfmac_f32_16x16x64_bf16 v[16:19], v[92:95], a[248:255], v0\n" \
  "s_waitcnt lgkmcnt(3)\n" \
  "ds_read_b128 v[76:79], v11 offset:3072\n" \
  "v_smfmac_f32_16x16x64_bf16 v[16:19], v[36:39], v[40:47], v0\n" \
  "s_waitcnt lgkmcnt(2)\n" \
  "ds_read_b128 v[40:43], v11 offset:43008\n" \
  "v_smfmac_f32_16x16x64_bf16 v[16:19], v[100:103], v[48:55], v0\n" \
  "ds_read_b128 v[44:47], v11 offset:44032\n" \
  "v_smfmac_f32_16x16x64_bf16 v[20:23], v[80:83], v[112:119], v0\n" \
  "ds_read_b128 v[48:51], v11 offset:4096\n" \
  "v_smfmac_f32_16x16x64_bf16 v[20:23], v[84:87], v[120:127], v0\n" \
  "ds_read_b128 v[52:55], v11 offset:5120\n" \
  "v_cvt_pk_bf16_f32 v108, v16, v17\n" \
  "v_smfmac_f32_16x16x64_bf16 v[20:23], v[88:91], v[128:135], v0\n" \
  "v_cvt_pk_bf16_f32 v109, v18, v19\n" \
  "v_smfmac_f32_16x16x64_bf16 v[20:23], v[92:95], v[136:143], v0\n" \
  "buffer_store_dwordx2 v[108:109], v4, s[36:39], s65 offen sc1\n" \
  "v_smfmac_f32_16x16x64_bf16 v[20:23], v[36:39], v[56:63], v0\n" \
  "ds_read_b128 v[56:59], v11 offset:45056\n" \
  "v_smfmac_f32_16x16x64_bf16 v[20:23], v[100:103], v[64:71], v0\n" \
  "ds_read_b128 v[60:63], v11 offset:46080\n" \
  "v_smfmac_f32_16x16x64_bf16 v[104:107], v[80:83], v[144:151], v0\n" \
  "ds_read_b128 v[64:67], v11 offset:6144\n" \
  "v_smfmac_f32_16x16x64_bf16 v[104:107], v[84:87], v[152:159], v0\n" \
  "ds_read_b128 v[68:71], v11 offset:7168\n" \
  "v_cvt_pk_bf16_f32 v110, v20, v21\n" \
  "v_smfmac_f32_16x16x64_bf16 v[104:107], v[88:91], v[160:167], v0\n" \
  "v_cvt_pk_bf16_f32 v111, v22, v23\n" \
  "v_smfmac_f32_16x16x64_bf16 v[104:107], v[92:95], v[168:175], v0\n" \
  "buffer_store_dwordx2 v[110:111], v13, s[36:39], s65 offen sc1\n" \
  "v_mov_b32 v16, 0\n" \
  "s_waitcnt lgkmcnt(8)\n" \
  "v_smfmac_f32_16x16x64_bf16 v[104:107], v[36:39], v[72:79], v0\n" \
  "s_waitcnt lgkmcnt(6)\n" \
  "ds_read_b128 v[72:75], v11 offset:47104\n" \
  "v_mov_b32 v17, 0\n" \
  "v_smfmac_f32_16x16x64_bf16 v[104:107], v[100:103], v[40:47], v0\n" \
  "ds_read_b128 v[76:79], v11 offset:48128\n" \
  "v_mov_b32 v18, 0\n" \
  "v_smfmac_f32_16x16x64_bf16 v[24:27], v[80:83], v[176:183], v0\n" \
  "v_mov_b32 v19, 0\n" \
  "v_smfmac_f32_16x16x64_bf16 v[24:27], v[84:87], v[184:191], v0\n" \
  "v_cvt_pk_bf16_f32 v108, v104, v105\n" \
  "v_smfmac_f32_16x16x64_bf16 v[24:27], v[88:91], v[192:199], v0\n" \
  "v_cvt_pk_bf16_f32 v109, v106, v107\n" \
  "v_smfmac_f32_16x16x64_bf16 v[24:27], v[92:95], v[200:207], v0\n" \
  "buffer_store_dwordx2 v[108:109], v13, s[36:39], s40 offen sc1\n" \
  "s_waitcnt lgkmcnt(6)\n" \
  "v_smfmac_f32_16x16x64_bf16 v[24:27], v[36:39], v[48:55], v0\n" \
  "s_waitcnt lgkmcnt(4)\n" \
  "v_smfmac_f32_16x16x64_bf16 v[24:27], v[100:103], v[56:63], v0\n" \
  "v_smfmac_f32_16x16x64_bf16 v[16:19], v[80:83], v[208:215], v0\n" \
  "v_smfmac_f32_16x16x64_bf16 v[16:19], v[84:87], v[216:223], v0\n" \
  "v_cvt_pk_bf16_f32 v110, v24, v25\n" \
  "v_smfmac_f32_16x16x64_bf16 v[16:19], v[88:91], v[224:231], v0\n" \
  "v_cvt_pk_bf16_f32 v111, v26, v27\n" \
  "v_smfmac_f32_16x16x64_bf16 v[16:19], v[92:95], v[232:239], v0\n" \
  "buffer_store_dwordx2 v[110:111], v13, s[36:39], s41 offen sc1\n" \
  "s_waitcnt lgkmcnt(2)\n" \
  "v_smfmac_f32_16x16x64_bf16 v[16:19], v[36:39], v[64:71], v0\n" \
  "s_waitcnt lgkmcnt(0)\n" \
  "v_smfmac_f32_16x16x64_bf16 v[16:19], v[100:103], v[72:79], v0\n" \
  "s_nop 3\n" \
  "v_mov_b32 v31, v31\n" \
  "s_nop 2\n" \
  "v_cvt_pk_bf16_f32 v108, v16, v17\n" \
  "v_cvt_pk_bf16_f32 v109, v18, v19\n" \
  "buffer_store_dwordx2 v[108:109], v13, s[36:39], s42 offen sc1\n" \
  "buffer_store_dword v34, v1, s[36:39], 0 offen sc1\n" \
  "s_mov_b32 s67, s66\n" \
  "s_cmp_eq_u32 s64, 64\n" \
  "s_cbranch_scc1 LDONE_%=\n" \
  "s_waitcnt vmcnt(0)\n" \
  "s_branch LOOP23_%=\n" \
  "LMF23_%=:\n" \
  "v_mbcnt_lo_u32_b32 v94, -1, 0\n" \
  "v_mbcnt_hi_u32_b32 v94, -1, v94\n" \
  "v_lshlrev_b32 v94, 4, v94\n" \
  "v_mov_b32 v92, 0\n" \
  "ds_read_b128 v[72:75], v94 offset:0\n" \
  "ds_read_b128 v[76:79], v94 offset:1024\n" \
  "ds_read_b128 v[80:83], v94 offset:2048\n" \
  "ds_read_b128 v[84:87], v94 offset:3072\n" \
  "ds_read_b128 v[88:91], v94 offset:4096\n" \
  "s_waitcnt lgkmcnt(0)\n" \
  "v_max3_u32 v92, v92, v72, v73\n" \
  "v_max3_u32 v92, v92, v74, v75\n" \
  "v_max3_u32 v92, v92, v76, v77\n" \
  "v_max3_u32 v92, v92, v78, v79\n" \
  "v_max3_u32 v92, v92, v80, v81\n" \
  "v_max3_u32 v92, v92, v82, v83\n" \
  "v_max3_u32 v92, v92, v84, v85\n" \
  "v_max3_u32 v92, v92, v86, v87\n" \
  "v_max3_u32 v92, v92, v88, v89\n" \
  "v_max3_u32 v92, v92, v90, v91\n" \
  "ds_read_b128 v[72:75], v94 offset:5120\n" \
  "ds_read_b128 v[76:79], v94 offset:6144\n" \
  "ds_read_b128 v[80:83], v94 offset:7168\n" \
  "ds_read_b128 v[84:87], v94 offset:8192\n" \
  "ds_read_b128 v[88:91], v94 offset:9216\n" \
  "s_waitcnt lgkmcnt(0)\n" \
  "v_max3_u32 v92, v92, v72, v73\n" \
  "v_max3_u32 v92, v92, v74, v75\n" \
  "v_max3_u32 v92, v92, v76, v77\n" \
  "v_max3_u32 v92, v92, v78, v79\n" \
  "v_max3_u32 v92, v92, v80, v81\n" \
  "v_max3_u32 v92, v92, v82, v83\n" \
  "v_max3_u32 v92, v92, v84, v85\n" \
  "v_max3_u32 v92, v92, v86, v87\n" \
  "v_max3_u32 v92, v92, v88, v89\n" \
  "v_max3_u32 v92, v92, v90, v91\n" \
  "v_cmp_eq_u32 vcc, -1, v92\n" \
  "s_cbranch_vccz LMOK23_%=\n" \
  "s_cmp_gt_u32 s58, 4000\n" \
  "s_cbranch_scc1 LMOK23_%=\n" \
  "s_add_u32 s58, s58, 1\n" \
  "s_sub_u32 s64, s64, 1\n" \
  "s_mov_b32 s66, s65\n" \
  "s_waitcnt vmcnt(0) lgkmcnt(0)\n" \
  "s_barrier\n" \
  "s_branch LOOP23_%=\n" \
  "LDONE_%=:\n" \
  "s_cmp_eq_u32 s73, 1\n" \
  "s_cbranch_scc1 LFIN_%=\n" \
  "s_mov_b32 s73, 1\n" \
  "s_mov_b32 s38, s55\n" \
  "s_mov_b32 s64, 0\n" \
  "s_mov_b32 s60, 0\n" \
  "s_memrealtime s[74:75]\n" \
  "s_memtime s[76:77]\n" \
  "v_readlane_b32 s50, v224, 0\n" \
  "v_readlane_b32 s51, v224, 1\n" \
  "v_readlane_b32 s52, v224, 2\n" \
  "v_readlane_b32 s58, v224, 3\n" \
  "v_readlane_b32 s40, v224, 4\n" \
  "v_readlane_b32 s41, v224, 5\n" \
  "v_readlane_b32 s42, v224, 6\n" \
  "v_readlane_b32 s43, v224, 7\n" \
  "v_readlane_b32 s44, v224, 8\n" \
  "v_readlane_b32 s45, v224, 9\n" \
  "v_readlane_b32 s46, v224, 10\n" \
  "v_readlane_b32 s47, v224, 11\n" \
  "v_mov_b32 v40, v192\n" \
  "v_mov_b32 v41, v193\n" \
  "v_mov_b32 v42, v194\n" \
  "v_mov_b32 v43, v195\n" \
  "v_mov_b32 v44, v196\n" \
  "v_mov_b32 v45, v197\n" \
  "v_mov_b32 v46, v198\n" \
  "v_mov_b32 v47, v199\n" \
  "v_mov_b32 v48, v200\n" \
  "v_mov_b32 v49, v201\n" \
  "v_mov_b32 v50, v202\n" \
  "v_mov_b32 v51, v203\n" \
  "v_mov_b32 v52, v204\n" \
  "v_mov_b32 v53, v205\n" \
  "v_mov_b32 v54, v206\n" \
  "v_mov_b32 v55, v207\n" \
  "v_mov_b32 v56, v208\n" \
  "v_mov_b32 v57, v209\n" \
  "v_mov_b32 v58, v210\n" \
  "v_mov_b32 v59, v211\n" \
  "v_mov_b32 v60, v212\n" \
  "v_mov_b32 v61, v213\n" \
  "v_mov_b32 v62, v214\n" \
  "v_mov_b32 v63, v215\n" \
  "v_mov_b32 v64, v216\n" \
  "v_mov_b32 v65, v217\n" \
  "v_mov_b32 v66, v218\n" \
  "v_mov_b32 v67, v219\n" \
  "v_mov_b32 v68, v220\n" \
  "v_mov_b32 v69, v221\n" \
  "v_mov_b32 v70, v222\n" \
  "v_mov_b32 v71, v223\n" \
  "v_mov_b32 v25, v225\n" \
  "v_mov_b32 v26, v226\n" \
  "v_mov_b32 v32, 1\n" \
  "s_waitcnt lgkmcnt(0)\n" \
  "s_mov_b32 s55, s74\n" \
  "s_mov_b32 s62, s74\n" \
  "s_mov_b32 s86, s76\n" \
  "s_cmp_ge_u32 s48, 8\n" \
  "s_cbranch_scc1 LSV_%=\n" \
  "v_mad_u32_u24 v112, v62, 0, v60\n" \
  "v_add_u32 v128, 90112, v112\n" \
  "v_mad_u32_u24 v113, v62, 2, v60\n" \
  "v_add_u32 v129, 90112, v113\n" \
  "v_mad_u32_u24 v114, v62, 16, v60\n" \
  "v_add_u32 v130, 90112, v114\n" \
  "v_mad_u32_u24 v115, v62, 18, v60\n" \
  "v_add_u32 v131, 90112, v115\n" \
  "v_mad_u32_u24 v116, v62, 32, v60\n" \
  "v_add_u32 v132, 90112, v116\n" \
  "v_mad_u32_u24 v117, v62, 34, v60\n" \
  "v_add_u32 v133, 90112, v117\n" \
  "v_mad_u32_u24 v118, v62, 48, v60\n" \
  "v_add_u32 v134, 90112, v118\n" \
  "v_mad_u32_u24 v119, v62, 50, v60\n" \
  "v_add_u32 v135, 90112, v119\n" \
  "v_mad_u32_u24 v120, v63, 0, v61\n" \
  "v_add_u32 v136, 90112, v120\n" \
  "v_mad_u32_u24 v121, v63, 2, v61\n" \
  "v_add_u32 v137, 90112, v121\n" \
  "v_mad_u32_u24 v122, v63, 16, v61\n" \
  "v_add_u32 v138, 90112, v122\n" \
  "v_mad_u32_u24 v123, v63, 18, v61\n" \
  "v_add_u32 v139, 90112, v123\n" \
  "v_mad_u32_u24 v124, v63, 32, v61\n" \
  "v_add_u32 v140, 90112, v124\n" \
  "v_mad_u32_u24 v125, v63, 34, v61\n" \
  "v_add_u32 v141, 90112, v125\n" \
  "v_mad_u32_u24 v126, v63, 48, v61\n" \
  "v_add_u32 v142, 90112, v126\n" \
  "v_mad_u32_u24 v127, v63, 50, v61\n" \
  "v_add_u32 v143, 90112, v127\n" \
  "s_and_b32 s71, s48, 3\n" \
  "s_cmp_eq_u32 s71, 2\n" \
  "s_cbranch_scc0 LGQ_%=\n" \
  "s_mov_b32 s71, 0\n" \
  "s_add_u32 m0, s56, 57344\n" \
  "s_nop 0\n" \
  "buffer_load_dwordx4 v40, s[40:43], s71 offen lds\n" \
  "s_add_u32 m0, s56, 58368\n" \
  "s_nop 0\n" \
  "buffer_load_dwordx4 v41, s[40:43], s71 offen lds\n" \
  "s_add_u32 m0, s56, 59392\n" \
  "s_nop 0\n" \
  "buffer_load_dwordx4 v42, s[40:43], s71 offen lds\n" \
  "s_add_u32 m0, s56, 60416\n" \
  "s_nop 0\n" \
  "buffer_load_dwordx4 v43, s[40:43], s71 offen lds\n" \
  "s_add_u32 m0, s56, 61440\n" \
  "s_nop 0\n" \
  "buffer_load_dwordx4 v44, s[40:43], s71 offen lds\n" \
  "s_add_u32 m0, s56, 62464\n" \
  "s_nop 0\n" \
  "buffer_load_dwordx4 v45, s[40:43], s71 offen lds\n" \
  "s_add_u32 m0, s56, 63488\n" \
  "s_nop 0\n" \
  "buffer_load_dwordx4 v46, s[40:43], s71 offen lds\n" \
  "s_add_u32 m0, s56, 64512\n" \
  "s_nop 0\n" \
  "buffer_load_dwordx4 v47, s[40:43], s71 offen lds\n" \
  "s_mov_b32 s71, 786432\n" \
  "s_add_u32 m0, s56, 90112\n" \
  "s_nop 0\n" \
  "buffer_load_dwordx4 v40, s[40:43], s71 offen lds\n" \
  "s_add_u32 m0, s56, 91136\n" \
  "s_nop 0\n" \
  "buffer_load_dwordx4 v41, s[40:43], s71 offen lds\n" \
  "s_add_u32 m0, s56, 92160\n" \
  "s_nop 0\n" \
  "buffer_load_dwordx4 v42, s[40:43], s71 offen lds\n" \
  "s_add_u32 m0, s56, 93184\n" \
  "s_nop 0\n" \
  "buffer_load_dwordx4 v43, s[40:43], s71 offen lds\n" \
  "s_add_u32 m0, s56, 94208\n" \
  "s_nop 0\n" \
  "buffer_load_dwordx4 v44, s[40:43], s71 offen lds\n" \
  "s_add_u32 m0, s56, 95232\n" \
  "s_nop 0\n" \
  "buffer_load_dwordx4 v45, s[40:43], s71 offen lds\n" \
  "s_add_u32 m0, s56, 96256\n" \
  "s_nop 0\n" \
  "buffer_load_dwordx4 v46, s[40:43], s71 offen lds\n" \
  "s_add_u32 m0, s56, 97280\n" \
  "s_nop 0\n" \
  "buffer_load_dwordx4 v47, s[40:43], s71 offen lds\n" \
  "s_mov_b32 s71, 1572864\n" \
  "s_add_u32 m0, s56, 122880\n" \
  "s_nop 0\n" \
  "buffer_load_dwordx4 v40, s[40:43], s71 offen lds\n" \
  "s_add_u32 m0, s56, 123904\n" \
  "s_nop 0\n" \
  "buffer_load_dwordx4 v41, s[40:43], s71 offen lds\n" \
  "s_add_u32 m0, s56, 124928\n" \
  "s_nop 0\n" \
  "buffer_load_dwordx4 v42, s[40:43], s71 offen lds\n" \
  "s_add_u32 m0, s56, 125952\n" \
  "s_nop 0\n" \
  "buffer_load_dwordx4 v43, s[40:43], s71 offen lds\n" \
  "s_add_u32 m0, s56, 126976\n" \
  "s_nop 0\n" \
  "buffer_load_dwordx4 v44, s[40:43], s71 offen lds\n" \
  "s_add_u32 m0, s56, 128000\n" \
  "s_nop 0\n" \
  "buffer_load_dwordx4 v45, s[40:43], s71 offen lds\n" \
  "s_add_u32 m0, s56, 129024\n" \
  "s_nop 0\n" \
  "buffer_load_dwordx4 v46, s[40:43], s71 offen lds\n" \
  "s_add_u32 m0, s56, 130048\n" \
  "s_nop 0\n" \
  "buffer_load_dwordx4 v47, s[40:43], s71 offen lds\n" \
  "LGQ_%=:\n" \
  "s_waitcnt vmcnt(16) lgkmcnt(0)\n" \
  "s_barrier\n" \
  "s_mov_b32 s71, 2359296\n" \
  "s_add_u32 m0, s56, 0\n" \
  "s_nop 0\n" \
  "buffer_load_dwordx4 v40, s[40:43], s71 offen lds\n" \
  "s_add_u32 m0, s56, 1024\n" \
  "s_nop 0\n" \
  "buffer_load_dwordx4 v41, s[40:43], s71 offen lds\n" \
  "s_add_u32 m0, s56, 2048\n" \
  "s_nop 0\n" \
  "buffer_load_dwordx4 v42, s[40:43], s71 offen lds\n" \
  "s_add_u32 m0, s56, 3072\n" \
  "s_nop 0\n" \
  "buffer_load_dwordx4 v43, s[40:43], s71 offen lds\n" \
  "s_add_u32 m0, s56, 4096\n" \
  "s_nop 0\n" \
  "buffer_load_dwordx4 v44, s[40:43], s71 offen lds\n" \
  "s_add_u32 m0, s56, 5120\n" \
  "s_nop 0\n" \
  "buffer_load_dwordx4 v45, s[40:43], s71 offen lds\n" \
  "s_add_u32 m0, s56, 6144\n" \
  "s_nop 0\n" \
  "buffer_load_dwordx4 v46, s[40:43], s71 offen lds\n" \
  "s_add_u32 m0, s56, 7168\n" \
  "s_nop 0\n" \
  "buffer_load_dwordx4 v47, s[40:43], s71 offen lds\n" \
  "ds_read_b64_tr_b16 a[0:1], v112 offset:57344\n" \
  "ds_read_b64_tr_b16 a[2:3], v113 offset:57344\n" \
  "ds_read_b64_tr_b16 a[4:5], v114 offset:57344\n" \
  "ds_read_b64_tr_b16 a[6:7], v115 offset:57344\n" \
  "ds_read_b64_tr_b16 a[8:9], v116 offset:57344\n" \
  "ds_read_b64_tr_b16 a[10:11], v117 offset:57344\n" \
  "ds_read_b64_tr_b16 a[12:13], v118 offset:57344\n" \
  "ds_read_b64_tr_b16 a[14:15], v119 offset:57344\n" \
  "s_waitcnt lgkmcnt(0)\n" \
  "ds_read_b64_tr_b16 a[160:161], v120 offset:57344\n" \
  "ds_read_b64_tr_b16 a[162:163], v121 offset:57344\n" \
  "ds_read_b64_tr_b16 a[164:165], v122 offset:57344\n" \
  "ds_read_b64_tr_b16 a[166:167], v123 offset:57344\n" \
  "ds_read_b64_tr_b16 a[168:169], v124 offset:57344\n" \
  "ds_read_b64_tr_b16 a[170:171], v125 offset:57344\n" \
  "ds_read_b64_tr_b16 a[172:173], v126 offset:57344\n" \
  "ds_read_b64_tr_b16 a[174:175], v127 offset:57344\n" \
  "s_waitcnt lgkmcnt(0)\n" \
  "s_waitcnt vmcnt(16) lgkmcnt(0)\n" \
  "s_barrier\n" \
  "s_mov_b32 s71, 3145728\n" \
  "s_add_u32 m0, s56, 57344\n" \
  "s_nop 0\n" \
  "buffer_load_dwordx4 v40, s[40:43], s71 offen lds\n" \
  "s_add_u32 m0, s56, 58368\n" \
  "s_nop 0\n" \
  "buffer_load_dwordx4 v41, s[40:43], s71 offen lds\n" \
  "s_add_u32 m0, s56, 59392\n" \
  "s_nop 0\n" \
  "buffer_load_dwordx4 v42, s[40:43], s71 offen lds\n" \
  "s_add_u32 m0, s56, 60416\n" \
  "s_nop 0\n" \
  "buffer_load_dwordx4 v43, s[40:43], s71 offen lds\n" \
  "s_add_u32 m0, s56, 61440\n" \
  "s_nop 0\n" \
  "buffer_load_dwordx4 v44, s[40:43], s71 offen lds\n" \
  "s_add_u32 m0, s56, 62464\n" \
  "s_nop 0\n" \
  "buffer_load_dwordx4 v45, s[40:43], s71 offen lds\n" \
  "s_add_u32 m0, s56, 63488\n" \
  "s_nop 0\n" \
  "buffer_load_dwordx4 v46, s[40:43], s71 offen lds\n" \
  "s_add_u32 m0, s56, 64512\n" \
  "s_nop 0\n" \
  "buffer_load_dwordx4 v47, s[40:43], s71 offen lds\n" \
  "ds_read_b64_tr_b16 a[16:17], v128\n" \
  "ds_read_b64_tr_b16 a[18:19], v129\n" \
  "ds_read_b64_tr_b16 a[20:21], v130\n" \
  "ds_read_b64_tr_b16 a[22:23], v131\n" \
  "ds_read_b64_tr_b16 a[24:25], v132\n" \
  "ds_read_b64_tr_b16 a[26:27], v133\n" \
  "ds_read_b64_tr_b16 a[28:29], v134\n" \
  "ds_read_b64_tr_b16 a[30:31], v135\n" \
  "s_waitcnt lgkmcnt(0)\n" \
  "ds_read_b64_tr_b16 a[176:177], v136\n" \
  "ds_read_b64_tr_b16 a[178:179], v137\n" \
  "ds_read_b64_tr_b16 a[180:181], v138\n" \
  "ds_read_b64_tr_b16 a[182:183], v139\n" \
  "ds_read_b64_tr_b16 a[184:185], v140\n" \
  "ds_read_b64_tr_b16 a[186:187], v141\n" \
  "ds_read_b64_tr_b16 a[188:189], v142\n" \
  "ds_read_b64_tr_b16 a[190:191], v143\n" \
  "s_waitcnt lgkmcnt(0)\n" \
  "s_waitcnt vmcnt(16) lgkmcnt(0)\n" \
  "s_barrier\n" \
  "s_mov_b32 s71, 3932160\n" \
  "s_add_u32 m0, s56, 90112\n" \
  "s_nop 0\n" \
  "buffer_load_dwordx4 v40, s[40:43], s71 offen lds\n" \
  "s_add_u32 m0, s56, 91136\n" \
  "s_nop 0\n" \
  "buffer_load_dwordx4 v41, s[40:43], s71 offen lds\n" \
  "s_add_u32 m0, s56, 92160\n" \
  "s_nop 0\n" \
  "buffer_load_dwordx4 v42, s[40:43], s71 offen lds\n" \
  "s_add_u32 m0, s56, 93184\n" \
  "s_nop 0\n" \
  "buffer_load_dwordx4 v43, s[40:43], s71 offen lds\n" \
  "s_add_u32 m0, s56, 94208\n" \
  "s_nop 0\n" \
  "buffer_load_dwordx4 v44, s[40:43], s71 offen lds\n" \
  "s_add_u32 m0, s56, 95232\n" \
  "s_nop 0\n" \
  "buffer_load_dwordx4 v45, s[40:43], s71 offen lds\n" \
  "s_add_u32 m0, s56, 96256\n" \
  "s_nop 0\n" \
  "buffer_load_dwordx4 v46, s[40:43], s71 offen lds\n" \
  "s_add_u32 m0, s56, 97280\n" \
  "s_nop 0\n" \
  "buffer_load_dwordx4 v47, s[40:43], s71 offen lds\n" \
  "ds_read_b64_tr_b16 a[32:33], v128 offset:32768\n" \
  "ds_read_b64_tr_b16 a[34:35], v129 offset:32768\n" \
  "ds_read_b64_tr_b16 a[36:37], v130 offset:32768\n" \
  "ds_read_b64_tr_b16 a[38:39], v131 offset:32768\n" \
  "ds_read_b64_tr_b16 a[40:41], v132 offset:32768\n" \
  "ds_read_b64_tr_b16 a[42:43], v133 offset:32768\n" \
  "ds_read_b64_tr_b16 a[44:45], v134 offset:32768\n" \
  "ds_read_b64_tr_b16 a[46:47], v135 offset:32768\n" \
  "s_waitcnt lgkmcnt(0)\n" \
  "ds_read_b64_tr_b16 a[192:193], v136 offset:32768\n" \
  "ds_read_b64_tr_b16 a[194:195], v137 offset:32768\n" \
  "ds_read_b64_tr_b16 a[196:197], v138 offset:32768\n" \
  "ds_read_b64_tr_b16 a[198:199], v139 offset:32768\n" \
  "ds_read_b64_tr_b16 a[200:201], v140 offset:32768\n" \
  "ds_read_b64_tr_b16 a[202:203], v141 offset:32768\n" \
  "ds_read_b64_tr_b16 a[204:205], v142 offset:32768\n" \
  "ds_read_b64_tr_b16 a[206:207], v143 offset:32768\n" \
  "s_waitcnt lgkmcnt(0)\n" \
  "s_waitcnt vmcnt(16) lgkmcnt(0)\n" \
  "s_barrier\n" \
  "s_mov_b32 s71, 4718592\n" \
  "s_add_u32 m0, s56, 122880\n" \
  "s_nop 0\n" \
  "buffer_load_dwordx4 v40, s[40:43], s71 offen lds\n" \
  "s_add_u32 m0, s56, 123904\n" \
  "s_nop 0\n" \
  "buffer_load_dwordx4 v41, s[40:43], s71 offen lds\n" \
  "s_add_u32 m0, s56, 124928\n" \
  "s_nop 0\n" \
  "buffer_load_dwordx4 v42, s[40:43], s71 offen lds\n" \
  "s_add_u32 m0, s56, 125952\n" \
  "s_nop 0\n" \
  "buffer_load_dwordx4 v43, s[40:43], s71 offen lds\n" \
  "s_add_u32 m0, s56, 126976\n" \
  "s_nop 0\n" \
  "buffer_load_dwordx4 v44, s[40:43], s71 offen lds\n" \
  "s_add_u32 m0, s56, 128000\n" \
  "s_nop 0\n" \
  "buffer_load_dwordx4 v45, s[40:43], s71 offen lds\n" \
  "s_add_u32 m0, s56, 129024\n" \
  "s_nop 0\n" \
  "buffer_load_dwordx4 v46, s[40:43], s71 offen lds\n" \
  "s_add_u32 m0, s56, 130048\n" \
  "s_nop 0\n" \
  "buffer_load_dwordx4 v47, s[40:43], s71 offen lds\n" \
  "ds_read_b64_tr_b16 a[48:49], v112\n" \
  "ds_read_b64_tr_b16 a[50:51], v113\n" \
  "ds_read_b64_tr_b16 a[52:53], v114\n" \
  "ds_read_b64_tr_b16 a[54:55], v115\n" \
  "ds_read_b64_tr_b16 a[56:57], v116\n" \
  "ds_read_b64_tr_b16 a[58:59], v117\n" \
  "ds_read_b64_tr_b16 a[60:61], v118\n" \
  "ds_read_b64_tr_b16 a[62:63], v119\n" \
  "s_waitcnt lgkmcnt(0)\n" \
  "ds_read_b64_tr_b16 a[208:209], v120\n" \
  "ds_read_b64_tr_b16 a[210:211], v121\n" \
  "ds_read_b64_tr_b16 a[212:213], v122\n" \
  "ds_read_b64_tr_b16 a[214:215], v123\n" \
  "ds_read_b64_tr_b16 a[216:217], v124\n" \
  "ds_read_b64_tr_b16 a[218:219], v125\n" \
  "ds_read_b64_tr_b16 a[220:221], v126\n" \
  "ds_read_b64_tr_b16 a[222:223], v127\n" \
  "s_waitcnt lgkmcnt(0)\n" \
  "s_waitcnt vmcnt(16) lgkmcnt(0)\n" \
  "s_barrier\n" \
  "s_mov_b32 s71, 5505024\n" \
  "s_add_u32 m0, s56, 0\n" \
  "s_nop 0\n" \
  "buffer_load_dwordx4 v40, s[40:43], s71 offen lds\n" \
  "s_add_u32 m0, s56, 1024\n" \
  "s_nop 0\n" \
  "buffer_load_dwordx4 v41, s[40:43], s71 offen lds\n" \
  "s_add_u32 m0, s56, 2048\n" \
  "s_nop 0\n" \
  "buffer_load_dwordx4 v42, s[40:43], s71 offen lds\n" \
  "s_add_u32 m0, s56, 3072\n" \
  "s_nop 0\n" \
  "buffer_load_dwordx4 v43, s[40:43], s71 offen lds\n" \
  "s_add_u32 m0, s56, 4096\n" \
  "s_nop 0\n" \
  "buffer_load_dwordx4 v44, s[40:43], s71 offen lds\n" \
  "s_add_u32 m0, s56, 5120\n" \
  "s_nop 0\n" \
  "buffer_load_dwordx4 v45, s[40:43], s71 offen lds\n" \
  "s_add_u32 m0, s56, 6144\n" \
  "s_nop 0\n" \
  "buffer_load_dwordx4 v46, s[40:43], s71 offen lds\n" \
  "s_add_u32 m0, s56, 7168\n" \
  "s_nop 0\n" \
  "buffer_load_dwordx4 v47, s[40:43], s71 offen lds\n" \
  "ds_read_b64_tr_b16 a[64:65], v112 offset:57344\n" \
  "ds_read_b64_tr_b16 a[66:67], v113 offset:57344\n" \
  "ds_read_b64_tr_b16 a[68:69], v114 offset:57344\n" \
  "ds_read_b64_tr_b16 a[70:71], v115 offset:57344\n" \
  "ds_read_b64_tr_b16 a[72:73], v116 offset:57344\n" \
  "ds_read_b64_tr_b16 a[74:75], v117 offset:57344\n" \
  "ds_read_b64_tr_b16 a[76:77], v118 offset:57344\n" \
  "ds_read_b64_tr_b16 a[78:79], v119 offset:57344\n" \
  "s_waitcnt lgkmcnt(0)\n" \
  "ds_read_b64_tr_b16 a[224:225], v120 offset:57344\n" \
  "ds_read_b64_tr_b16 a[226:227], v121 offset:57344\n" \
  "ds_read_b64_tr_b16 a[228:229], v122 offset:57344\n" \
  "ds_read_b64_tr_b16 a[230:231], v123 offset:57344\n" \
  "ds_read_b64_tr_b16 a[232:233], v124 offset:57344\n" \
  "ds_read_b64_tr_b16 a[234:235], v125 offset:57344\n" \
  "ds_read_b64_tr_b16 a[236:237], v126 offset:57344\n" \
  "ds_read_b64_tr_b16 a[238:239], v127 offset:57344\n" \
  "s_waitcnt lgkmcnt(0)\n" \
  "s_waitcnt vmcnt(16) lgkmcnt(0)\n" \
  "s_barrier\n" \
  "s_mov_b32 s71, 6291456\n" \
  "s_add_u32 m0, s56, 57344\n" \
  "s_nop 0\n" \
  "buffer_load_dwordx4 v40, s[40:43], s71 offen lds\n" \
  "s_add_u32 m0, s56, 58368\n" \
  "s_nop 0\n" \
  "buffer_load_dwordx4 v41, s[40:43], s71 offen lds\n" \
  "s_add_u32 m0, s56, 59392\n" \
  "s_nop 0\n" \
  "buffer_load_dwordx4 v42, s[40:43], s71 offen lds\n" \
  "s_add_u32 m0, s56, 60416\n" \
  "s_nop 0\n" \
  "buffer_load_dwordx4 v43, s[40:43], s71 offen lds\n" \
  "s_add_u32 m0, s56, 61440\n" \
  "s_nop 0\n" \
  "buffer_load_dwordx4 v44, s[40:43], s71 offen lds\n" \
  "s_add_u32 m0, s56, 62464\n" \
  "s_nop 0\n" \
  "buffer_load_dwordx4 v45, s[40:43], s71 offen lds\n" \
  "s_add_u32 m0, s56, 63488\n" \
  "s_nop 0\n" \
  "buffer_load_dwordx4 v46, s[40:43], s71 offen lds\n" \
  "s_add_u32 m0, s56, 64512\n" \
  "s_nop 0\n" \
  "buffer_load_dwordx4 v47, s[40:43], s71 offen lds\n" \
  "ds_read_b64_tr_b16 a[80:81], v128\n" \
  "ds_read_b64_tr_b16 a[82:83], v129\n" \
  "ds_read_b64_tr_b16 a[84:85], v130\n" \
  "ds_read_b64_tr_b16 a[86:87], v131\n" \
  "ds_read_b64_tr_b16 a[88:89], v132\n" \
  "ds_read_b64_tr_b16 a[90:91], v133\n" \
  "ds_read_b64_tr_b16 a[92:93], v134\n" \
  "ds_read_b64_tr_b16 a[94:95], v135\n" \
  "s_waitcnt lgkmcnt(0)\n" \
  "ds_read_b64_tr_b16 a[240:241], v136\n" \
  "ds_read_b64_tr_b16 a[242:243], v137\n" \
  "ds_read_b64_tr_b16 a[244:245], v138\n" \
  "ds_read_b64_tr_b16 a[246:247], v139\n" \
  "ds_read_b64_tr_b16 a[248:249], v140\n" \
  "ds_read_b64_tr_b16 a[250:251], v141\n" \
  "ds_read_b64_tr_b16 a[252:253], v142\n" \
  "ds_read_b64_tr_b16 a[254:255], v143\n" \
  "s_waitcnt lgkmcnt(0)\n" \
  "s_waitcnt vmcnt(16) lgkmcnt(0)\n" \
  "s_barrier\n" \
  "s_mov_b32 s71, 7077888\n" \
  "s_add_u32 m0, s56, 90112\n" \
  "s_nop 0\n" \
  "buffer_load_dwordx4 v40, s[40:43], s71 offen lds\n" \
  "s_add_u32 m0, s56, 91136\n" \
  "s_nop 0\n" \
  "buffer_load_dwordx4 v41, s[40:43], s71 offen lds\n" \
  "s_add_u32 m0, s56, 92160\n" \
  "s_nop 0\n" \
  "buffer_load_dwordx4 v42, s[40:43], s71 offen lds\n" \
  "s_add_u32 m0, s56, 93184\n" \
  "s_nop 0\n" \
  "buffer_load_dwordx4 v43, s[40:43], s71 offen lds\n" \
  "s_add_u32 m0, s56, 94208\n" \
  "s_nop 0\n" \
  "buffer_load_dwordx4 v44, s[40:43], s71 offen lds\n" \
  "s_add_u32 m0, s56, 95232\n" \
  "s_nop 0\n" \
  "buffer_load_dwordx4 v45, s[40:43], s71 offen lds\n" \
  "s_add_u32 m0, s56, 96256\n" \
  "s_nop 0\n" \
  "buffer_load_dwordx4 v46, s[40:43], s71 offen lds\n" \
  "s_add_u32 m0, s56, 97280\n" \
  "s_nop 0\n" \
  "buffer_load_dwordx4 v47, s[40:43], s71 offen lds\n" \
  "ds_read_b64_tr_b16 a[96:97], v128 offset:32768\n" \
  "ds_read_b64_tr_b16 a[98:99], v129 offset:32768\n" \
  "ds_read_b64_tr_b16 a[100:101], v130 offset:32768\n" \
  "ds_read_b64_tr_b16 a[102:103], v131 offset:32768\n" \
  "ds_read_b64_tr_b16 a[104:105], v132 offset:32768\n" \
  "ds_read_b64_tr_b16 a[106:107], v133 offset:32768\n" \
  "ds_read_b64_tr_b16 a[108:109], v134 offset:32768\n" \
  "ds_read_b64_tr_b16 a[110:111], v135 offset:32768\n" \
  "s_waitcnt lgkmcnt(0)\n" \
  "ds_read_b64_tr_b16 v[192:193], v136 offset:32768\n" \
  "ds_read_b64_tr_b16 v[194:195], v137 offset:32768\n" \
  "ds_read_b64_tr_b16 v[196:197], v138 offset:32768\n" \
  "ds_read_b64_tr_b16 v[198:199], v139 offset:32768\n" \
  "ds_read_b64_tr_b16 v[200:201], v140 offset:32768\n" \
  "ds_read_b64_tr_b16 v[202:203], v141 offset:32768\n" \
  "ds_read_b64_tr_b16 v[204:205], v142 offset:32768\n" \
  "ds_read_b64_tr_b16 v[206:207], v143 offset:32768\n" \
  "s_waitcnt lgkmcnt(0)\n" \
  "s_waitcnt vmcnt(16) lgkmcnt(0)\n" \
  "s_barrier\n" \
  "ds_read_b64_tr_b16 a[112:113], v112\n" \
  "ds_read_b64_tr_b16 a[114:115], v113\n" \
  "ds_read_b64_tr_b16 a[116:117], v114\n" \
  "ds_read_b64_tr_b16 a[118:119], v115\n" \
  "ds_read_b64_tr_b16 a[120:121], v116\n" \
  "ds_read_b64_tr_b16 a[122:123], v117\n" \
  "ds_read_b64_tr_b16 a[124:125], v118\n" \
  "ds_read_b64_tr_b16 a[126:127], v119\n" \
  "s_waitcnt lgkmcnt(0)\n" \
  "ds_read_b64_tr_b16 v[208:209], v120\n" \
  "ds_read_b64_tr_b16 v[210:211], v121\n" \
  "ds_read_b64_tr_b16 v[212:213], v122\n" \
  "ds_read_b64_tr_b16 v[214:215], v123\n" \
  "ds_read_b64_tr_b16 v[216:217], v124\n" \
  "ds_read_b64_tr_b16 v[218:219], v125\n" \
  "ds_read_b64_tr_b16 v[220:221], v126\n" \
  "ds_read_b64_tr_b16 v[222:223], v127\n" \
  "s_waitcnt lgkmcnt(0)\n" \
  "s_waitcnt vmcnt(8) lgkmcnt(0)\n" \
  "s_barrier\n" \
  "ds_read_b64_tr_b16 a[128:129], v112 offset:57344\n" \
  "ds_read_b64_tr_b16 a[130:131], v113 offset:57344\n" \
  "ds_read_b64_tr_b16 a[132:133], v114 offset:57344\n" \
  "ds_read_b64_tr_b16 a[134:135], v115 offset:57344\n" \
  "ds_read_b64_tr_b16 a[136:137], v116 offset:57344\n" \
  "ds_read_b64_tr_b16 a[138:139], v117 offset:57344\n" \
  "ds_read_b64_tr_b16 a[140:141], v118 offset:57344\n" \
  "ds_read_b64_tr_b16 a[142:143], v119 offset:57344\n" \
  "s_waitcnt lgkmcnt(0)\n" \
  "ds_read_b64_tr_b16 v[224:225], v120 offset:57344\n" \
  "ds_read_b64_tr_b16 v[226:227], v121 offset:57344\n" \
  "ds_read_b64_tr_b16 v[228:229], v122 offset:57344\n" \
  "ds_read_b64_tr_b16 v[230:231], v123 offset:57344\n" \
  "ds_read_b64_tr_b16 v[232:233], v124 offset:57344\n" \
  "ds_read_b64_tr_b16 v[234:235], v125 offset:57344\n" \
  "ds_read_b64_tr_b16 v[236:237], v126 offset:57344\n" \
  "ds_read_b64_tr_b16 v[238:239], v127 offset:57344\n" \
  "s_waitcnt lgkmcnt(0)\n" \
  "s_waitcnt vmcnt(0) lgkmcnt(0)\n" \
  "s_barrier\n" \
  "ds_read_b64_tr_b16 a[144:145], v128\n" \
  "ds_read_b64_tr_b16 a[146:147], v129\n" \
  "ds_read_b64_tr_b16 a[148:149], v130\n" \
  "ds_read_b64_tr_b16 a[150:151], v131\n" \
  "ds_read_b64_tr_b16 a[152:153], v132\n" \
  "ds_read_b64_tr_b16 a[154:155], v133\n" \
  "ds_read_b64_tr_b16 a[156:157], v134\n" \
  "ds_read_b64_tr_b16 a[158:159], v135\n" \
  "s_waitcnt lgkmcnt(0)\n" \
  "ds_read_b64_tr_b16 v[240:241], v136\n" \
  "ds_read_b64_tr_b16 v[242:243], v137\n" \
  "ds_read_b64_tr_b16 v[244:245], v138\n" \
  "ds_read_b64_tr_b16 v[246:247], v139\n" \
  "ds_read_b64_tr_b16 v[248:249], v140\n" \
  "ds_read_b64_tr_b16 v[250:251], v141\n" \
  "ds_read_b64_tr_b16 v[252:253], v142\n" \
  "ds_read_b64_tr_b16 v[254:255], v143\n" \
  "s_waitcnt lgkmcnt(0)\n" \
  "s_waitcnt lgkmcnt(0)\n" \
  "s_barrier\n" \
  "LIQ_%=:\n" \
  "buffer_load_dword v92, v69, s[36:39], 0 offen sc1\n" \
  "s_waitcnt vmcnt(0)\n" \
  "v_and_b32 v93, -4, v92\n" \
  "v_cmp_ne_u32 vcc, s58, v93\n" \
  "s_cbranch_vccz LIXQ_%=\n" \
  "s_add_u32 s60, s60, 1\n" \
  "s_cmp_gt_u32 s60, 20000\n" \
  "s_cbranch_scc1 LIXQ_%=\n" \
  "s_sleep 8\n" \
  "s_branch LIQ_%=\n" \
  "LIXQ_%=:\n" \
  "v_and_b32 v92, v70, v92\n" \
  "v_cmp_ne_u32 s[80:81], 0, v92\n" \
  "s_mov_b32 s78, 0x55555555\n" \
  "s_mov_b32 s79, 0x55555555\n" \
  "s_xor_b64 s[82:83], s[80:81], s[78:79]\n" \
  "s_bcnt1_i32_b64 s71, s[82:83]\n" \
  "s_cmp_le_u32 s71, 26\n" \
  "s_cselect_b64 s[80:81], s[78:79], s[80:81]\n" \
  "s_not_b64 s[78:79], s[78:79]\n" \
  "s_cmp_ge_u32 s71, 38\n" \
  "s_cselect_b64 s[80:81], s[78:79], s[80:81]\n" \
  "v_lshrrev_b32 v92, 21, v25\n" \
  "v_lshrrev_b64 v[92:93], v92, s[80:81]\n" \
  "v_and_b32 v92, 1, v92\n" \
  "v_lshlrev_b32 v92, 13, v92\n" \
  "v_xor_b32 v25, v92, v25\n" \
  "v_lshrrev_b32 v92, 21, v26\n" \
  "v_lshrrev_b64 v[92:93], v92, s[80:81]\n" \
  "v_and_b32 v92, 1, v92\n" \
  "v_lshlrev_b32 v92, 13, v92\n" \
  "v_xor_b32 v26, v92, v26\n" \
  "v_lshrrev_b32 v92, 21, v27\n" \
  "v_lshrrev_b64 v[92:93], v92, s[80:81]\n" \
  "v_and_b32 v92, 1, v92\n" \
  "v_lshlrev_b32 v92, 13, v92\n" \
  "v_xor_b32 v27, v92, v27\n" \
  "v_lshrrev_b32 v92, 21, v28\n" \
  "v_lshrrev_b64 v[92:93], v92, s[80:81]\n" \
  "v_and_b32 v92, 1, v92\n" \
  "v_lshlrev_b32 v92, 13, v92\n" \
  "v_xor_b32 v28, v92, v28\n" \
  "v_lshrrev_b32 v92, 21, v29\n" \
  "v_lshrrev_b64 v[92:93], v92, s[80:81]\n" \
  "v_and_b32 v92, 1, v92\n" \
  "v_lshlrev_b32 v92, 13, v92\n" \
  "v_xor_b32 v29, v92, v29\n" \
  "v_lshrrev_b32 v92, 21, v30\n" \
  "v_lshrrev_b64 v[92:93], v92, s[80:81]\n" \
  "v_and_b32 v92, 1, v92\n" \
  "v_lshlrev_b32 v92, 13, v92\n" \
  "v_xor_b32 v30, v92, v30\n" \
  "v_lshrrev_b32 v92, 21, v67\n" \
  "v_lshrrev_b64 v[92:93], v92, s[80:81]\n" \
  "v_and_b32 v92, 1, v92\n" \
  "v_lshlrev_b32 v92, 13, v92\n" \
  "v_xor_b32 v67, v92, v67\n" \
  "v_lshrrev_b32 v92, 21, v71\n" \
  "v_lshrrev_b64 v[92:93], v92, s[80:81]\n" \
  "v_and_b32 v92, 1, v92\n" \
  "v_lshlrev_b32 v92, 13, v92\n" \
  "v_xor_b32 v71, v92, v71\n" \
  "s_lshr_b32 s71, s54, 21\n" \
  "s_lshr_b64 s[80:81], s[80:81], s71\n" \
  "s_and_b32 s71, s80, 1\n" \
  "s_lshl_b32 s71, s71, 13\n" \
  "s_or_b32 s54, s54, s71\n" \
  "s_and_b32 s71, s48, 3\n" \
  "s_cmp_eq_u32 s71, 3\n" \
  "s_cbranch_scc0 LIYQ_%=\n" \
  "v_mov_b32 v1, v71\n" \
  "LIYQ_%=:\n" \
  "buffer_atomic_add v32, v68, s[36:39], 0 offen\n" \
  "v_mov_b32 v104, v64\n" \
  "v_mov_b32 v105, v65\n" \
  "v_mov_b32 v106, v66\n" \
  "v_mov_b32 v107, v67\n" \
  "s_branch LCN_%=\n" \
  "LSV_%=:\n" \
  "v_add_u32 v72, 16384, v60\n" \
  "v_add_u32 v73, 16384, v61\n" \
  "v_add_u32 v74, 16384, v62\n" \
  "v_add_u32 v76, 65536, v60\n" \
  "v_add_u32 v77, 65536, v61\n" \
  "v_add_u32 v78, 65536, v62\n" \
  "v_add_u32 v80, 114688, v60\n" \
  "v_add_u32 v81, 114688, v61\n" \
  "v_add_u32 v82, 114688, v62\n" \
  "s_and_b32 s71, s48, 3\n" \
  "s_cmp_eq_u32 s71, 2\n" \
  "s_cbranch_scc0 LGV_%=\n" \
  "s_mov_b32 s71, 0\n" \
  "s_add_u32 m0, s56, 16384\n" \
  "s_nop 0\n" \
  "buffer_load_dwordx4 v40, s[40:43], s71 offen lds\n" \
  "s_add_u32 m0, s56, 17408\n" \
  "s_nop 0\n" \
  "buffer_load_dwordx4 v41, s[40:43], s71 offen lds\n" \
  "s_add_u32 m0, s56, 18432\n" \
  "s_nop 0\n" \
  "buffer_load_dwordx4 v42, s[40:43], s71 offen lds\n" \
  "s_add_u32 m0, s56, 19456\n" \
  "s_nop 0\n" \
  "buffer_load_dwordx4 v43, s[40:43], s71 offen lds\n" \
  "s_add_u32 m0, s56, 20480\n" \
  "s_nop 0\n" \
  "buffer_load_dwordx4 v44, s[40:43], s71 offen lds\n" \
  "s_add_u32 m0, s56, 21504\n" \
  "s_nop 0\n" \
  "buffer_load_dwordx4 v45, s[40:43], s71 offen lds\n" \
  "s_mov_b32 s71, 786432\n" \
  "s_add_u32 m0, s56, 40960\n" \
  "s_nop 0\n" \
  "buffer_load_dwordx4 v40, s[40:43], s71 offen lds\n" \
  "s_add_u32 m0, s56, 41984\n" \
  "s_nop 0\n" \
  "buffer_load_dwordx4 v41, s[40:43], s71 offen lds\n" \
  "s_add_u32 m0, s56, 43008\n" \
  "s_nop 0\n" \
  "buffer_load_dwordx4 v42, s[40:43], s71 offen lds\n" \
  "s_add_u32 m0, s56, 44032\n" \
  "s_nop 0\n" \
  "buffer_load_dwordx4 v43, s[40:43], s71 offen lds\n" \
  "s_add_u32 m0, s56, 45056\n" \
  "s_nop 0\n" \
  "buffer_load_dwordx4 v44, s[40:43], s71 offen lds\n" \
  "s_add_u32 m0, s56, 46080\n" \
  "s_nop 0\n" \
  "buffer_load_dwordx4 v45, s[40:43], s71 offen lds\n" \
  "s_mov_b32 s71, 1572864\n" \
  "s_add_u32 m0, s56, 65536\n" \
  "s_nop 0\n" \
  "buffer_load_dwordx4 v40, s[40:43], s71 offen lds\n" \
  "s_add_u32 m0, s56, 66560\n" \
  "s_nop 0\n" \
  "buffer_load_dwordx4 v41, s[40:43], s71 offen lds\n" \
  "s_add_u32 m0, s56, 67584\n" \
  "s_nop 0\n" \
  "buffer_load_dwordx4 v42, s[40:43], s71 offen lds\n" \
  "s_add_u32 m0, s56, 68608\n" \
  "s_nop 0\n" \
  "buffer_load_dwordx4 v43, s[40:43], s71 offen lds\n" \
  "s_add_u32 m0, s56, 69632\n" \
  "s_nop 0\n" \
  "buffer_load_dwordx4 v44, s[40:43], s71 offen lds\n" \
  "s_add_u32 m0, s56, 70656\n" \
  "s_nop 0\n" \
  "buffer_load_dwordx4 v45, s[40:43], s71 offen lds\n" \
  "s_mov_b32 s71, 2359296\n" \
  "s_add_u32 m0, s56, 90112\n" \
  "s_nop 0\n" \
  "buffer_load_dwordx4 v40, s[40:43], s71 offen lds\n" \
  "s_add_u32 m0, s56, 91136\n" \
  "s_nop 0\n" \
  "buffer_load_dwordx4 v41, s[40:43], s71 offen lds\n" \
  "s_add_u32 m0, s56, 92160\n" \
  "s_nop 0\n" \
  "buffer_load_dwordx4 v42, s[40:43], s71 offen lds\n" \
  "s_add_u32 m0, s56, 93184\n" \
  "s_nop 0\n" \
  "buffer_load_dwordx4 v43, s[40:43], s71 offen lds\n" \
  "s_add_u32 m0, s56, 94208\n" \
  "s_nop 0\n" \
  "buffer_load_dwordx4 v44, s[40:43], s71 offen lds\n" \
  "s_add_u32 m0, s56, 95232\n" \
  "s_nop 0\n" \
  "buffer_load_dwordx4 v45, s[40:43], s71 offen lds\n" \
  "LGV_%=:\n" \
  "s_waitcnt vmcnt(12) lgkmcnt(0)\n" \
  "s_barrier\n" \
  "s_mov_b32 s71, 3145728\n" \
  "s_add_u32 m0, s56, 114688\n" \
  "s_nop 0\n" \
  "buffer_load_dwordx4 v40, s[40:43], s71 offen lds\n" \
  "s_add_u32 m0, s56, 115712\n" \
  "s_nop 0\n" \
  "buffer_load_dwordx4 v41, s[40:43], s71 offen lds\n" \
  "s_add_u32 m0, s56, 116736\n" \
  "s_nop 0\n" \
  "buffer_load_dwordx4 v42, s[40:43], s71 offen lds\n" \
  "s_add_u32 m0, s56, 117760\n" \
  "s_nop 0\n" \
  "buffer_load_dwordx4 v43, s[40:43], s71 offen lds\n" \
  "s_add_u32 m0, s56, 118784\n" \
  "s_nop 0\n" \
  "buffer_load_dwordx4 v44, s[40:43], s71 offen lds\n" \
  "s_add_u32 m0, s56, 119808\n" \
  "s_nop 0\n" \
  "buffer_load_dwordx4 v45, s[40:43], s71 offen lds\n" \
  "s_mov_b32 s71, 3932160\n" \
  "s_add_u32 m0, s56, 139264\n" \
  "s_nop 0\n" \
  "buffer_load_dwordx4 v40, s[40:43], s71 offen lds\n" \
  "s_add_u32 m0, s56, 140288\n" \
  "s_nop 0\n" \
  "buffer_load_dwordx4 v41, s[40:43], s71 offen lds\n" \
  "s_add_u32 m0, s56, 141312\n" \
  "s_nop 0\n" \
  "buffer_load_dwordx4 v42, s[40:43], s71 offen lds\n" \
  "s_add_u32 m0, s56, 142336\n" \
  "s_nop 0\n" \
  "buffer_load_dwordx4 v43, s[40:43], s71 offen lds\n" \
  "s_add_u32 m0, s56, 143360\n" \
  "s_nop 0\n" \
  "buffer_load_dwordx4 v44, s[40:43], s71 offen lds\n" \
  "s_add_u32 m0, s56, 144384\n" \
  "s_nop 0\n" \
  "buffer_load_dwordx4 v45, s[40:43], s71 offen lds\n" \
  "ds_read_b64_tr_b16 a[0:1], v72\n" \
  "ds_read_b64_tr_b16 a[2:3], v72 offset:768\n" \
  "ds_read_b64_tr_b16 a[4:5], v72 offset:12288\n" \
  "ds_read_b64_tr_b16 a[6:7], v72 offset:13056\n" \
  "ds_read_b64_tr_b16 a[80:81], v72 offset:32\n" \
  "ds_read_b64_tr_b16 a[82:83], v72 offset:800\n" \
  "ds_read_b64_tr_b16 a[84:85], v72 offset:12320\n" \
  "ds_read_b64_tr_b16 a[86:87], v72 offset:13088\n" \
  "ds_read_b64_tr_b16 a[160:161], v73\n" \
  "ds_read_b64_tr_b16 a[162:163], v73 offset:768\n" \
  "ds_read_b64_tr_b16 a[164:165], v73 offset:12288\n" \
  "ds_read_b64_tr_b16 a[166:167], v73 offset:13056\n" \
  "s_waitcnt lgkmcnt(0)\n" \
  "ds_read_b64_tr_b16 a[8:9], v72 offset:24576\n" \
  "ds_read_b64_tr_b16 a[10:11], v72 offset:25344\n" \
  "ds_read_b64_tr_b16 a[12:13], v72 offset:36864\n" \
  "ds_read_b64_tr_b16 a[14:15], v72 offset:37632\n" \
  "ds_read_b64_tr_b16 a[88:89], v72 offset:24608\n" \
  "ds_read_b64_tr_b16 a[90:91], v72 offset:25376\n" \
  "ds_read_b64_tr_b16 a[92:93], v72 offset:36896\n" \
  "ds_read_b64_tr_b16 a[94:95], v72 offset:37664\n" \
  "ds_read_b64_tr_b16 a[168:169], v73 offset:24576\n" \
  "ds_read_b64_tr_b16 a[170:171], v73 offset:25344\n" \
  "ds_read_b64_tr_b16 a[172:173], v73 offset:36864\n" \
  "ds_read_b64_tr_b16 a[174:175], v73 offset:37632\n" \
  "s_waitcnt lgkmcnt(0)\n" \
  "s_waitcnt vmcnt(12) lgkmcnt(0)\n" \
  "s_barrier\n" \
  "s_mov_b32 s71, 4718592\n" \
  "s_add_u32 m0, s56, 16384\n" \
  "s_nop 0\n" \
  "buffer_load_dwordx4 v40, s[40:43], s71 offen lds\n" \
  "s_add_u32 m0, s56, 17408\n" \
  "s_nop 0\n" \
  "buffer_load_dwordx4 v41, s[40:43], s71 offen lds\n" \
  "s_add_u32 m0, s56, 18432\n" \
  "s_nop 0\n" \
  "buffer_load_dwordx4 v42, s[40:43], s71 offen lds\n" \
  "s_add_u32 m0, s56, 19456\n" \
  "s_nop 0\n" \
  "buffer_load_dwordx4 v43, s[40:43], s71 offen lds\n" \
  "s_add_u32 m0, s56, 20480\n" \
  "s_nop 0\n" \
  "buffer_load_dwordx4 v44, s[40:43], s71 offen lds\n" \
  "s_add_u32 m0, s56, 21504\n" \
  "s_nop 0\n" \
  "buffer_load_dwordx4 v45, s[40:43], s71 offen lds\n" \
  "s_mov_b32 s71, 5505024\n" \
  "s_add_u32 m0, s56, 40960\n" \
  "s_nop 0\n" \
  "buffer_load_dwordx4 v40, s[40:43], s71 offen lds\n" \
  "s_add_u32 m0, s56, 41984\n" \
  "s_nop 0\n" \
  "buffer_load_dwordx4 v41, s[40:43], s71 offen lds\n" \
  "s_add_u32 m0, s56, 43008\n" \
  "s_nop 0\n" \
  "buffer_load_dwordx4 v42, s[40:43], s71 offen lds\n" \
  "s_add_u32 m0, s56, 44032\n" \
  "s_nop 0\n" \
  "buffer_load_dwordx4 v43, s[40:43], s71 offen lds\n" \
  "s_add_u32 m0, s56, 45056\n" \
  "s_nop 0\n" \
  "buffer_load_dwordx4 v44, s[40:43], s71 offen lds\n" \
  "s_add_u32 m0, s56, 46080\n" \
  "s_nop 0\n" \
  "buffer_load_dwordx4 v45, s[40:43], s71 offen lds\n" \
  "ds_read_b64_tr_b16 a[16:17], v76\n" \
  "ds_read_b64_tr_b16 a[18:19], v76 offset:768\n" \
  "ds_read_b64_tr_b16 a[20:21], v76 offset:12288\n" \
  "ds_read_b64_tr_b16 a[22:23], v76 offset:13056\n" \
  "ds_read_b64_tr_b16 a[96:97], v76 offset:32\n" \
  "ds_read_b64_tr_b16 a[98:99], v76 offset:800\n" \
  "ds_read_b64_tr_b16 a[100:101], v76 offset:12320\n" \
  "ds_read_b64_tr_b16 a[102:103], v76 offset:13088\n" \
  "ds_read_b64_tr_b16 a[176:177], v77\n" \
  "ds_read_b64_tr_b16 a[178:179], v77 offset:768\n" \
  "ds_read_b64_tr_b16 a[180:181], v77 offset:12288\n" \
  "ds_read_b64_tr_b16 a[182:183], v77 offset:13056\n" \
  "s_waitcnt lgkmcnt(0)\n" \
  "ds_read_b64_tr_b16 a[24:25], v76 offset:24576\n" \
  "ds_read_b64_tr_b16 a[26:27], v76 offset:25344\n" \
  "ds_read_b64_tr_b16 a[28:29], v76 offset:36864\n" \
  "ds_read_b64_tr_b16 a[30:31], v76 offset:37632\n" \
  "ds_read_b64_tr_b16 a[104:105], v76 offset:24608\n" \
  "ds_read_b64_tr_b16 a[106:107], v76 offset:25376\n" \
  "ds_read_b64_tr_b16 a[108:109], v76 offset:36896\n" \
  "ds_read_b64_tr_b16 a[110:111], v76 offset:37664\n" \
  "ds_read_b64_tr_b16 a[184:185], v77 offset:24576\n" \
  "ds_read_b64_tr_b16 a[186:187], v77 offset:25344\n" \
  "ds_read_b64_tr_b16 a[188:189], v77 offset:36864\n" \
  "ds_read_b64_tr_b16 a[190:191], v77 offset:37632\n" \
  "s_waitcnt lgkmcnt(0)\n" \
  "s_waitcnt vmcnt(12) lgkmcnt(0)\n" \
  "s_barrier\n" \
  "s_mov_b32 s71, 6291456\n" \
  "s_add_u32 m0, s56, 65536\n" \
  "s_nop 0\n" \
  "buffer_load_dwordx4 v40, s[40:43], s71 offen lds\n" \
  "s_add_u32 m0, s56, 66560\n" \
  "s_nop 0\n" \
  "buffer_load_dwordx4 v41, s[40:43], s71 offen lds\n" \
  "s_add_u32 m0, s56, 67584\n" \
  "s_nop 0\n" \
  "buffer_load_dwordx4 v42, s[40:43], s71 offen lds\n" \
  "s_add_u32 m0, s56, 68608\n" \
  "s_nop 0\n" \
  "buffer_load_dwordx4 v43, s[40:43], s71 offen lds\n" \
  "s_add_u32 m0, s56, 69632\n" \
  "s_nop 0\n" \
  "buffer_load_dwordx4 v44, s[40:43], s71 offen lds\n" \
  "s_add_u32 m0, s56, 70656\n" \
  "s_nop 0\n" \
  "buffer_load_dwordx4 v45, s[40:43], s71 offen lds\n" \
  "s_mov_b32 s71, 7077888\n" \
  "s_add_u32 m0, s56, 90112\n" \
  "s_nop 0\n" \
  "buffer_load_dwordx4 v40, s[40:43], s71 offen lds\n" \
  "s_add_u32 m0, s56, 91136\n" \
  "s_nop 0\n" \
  "buffer_load_dwordx4 v41, s[40:43], s71 offen lds\n" \
  "s_add_u32 m0, s56, 92160\n" \
  "s_nop 0\n" \
  "buffer_load_dwordx4 v42, s[40:43], s71 offen lds\n" \
  "s_add_u32 m0, s56, 93184\n" \
  "s_nop 0\n" \
  "buffer_load_dwordx4 v43, s[40:43], s71 offen lds\n" \
  "s_add_u32 m0, s56, 94208\n" \
  "s_nop 0\n" \
  "buffer_load_dwordx4 v44, s[40:43], s71 offen lds\n" \
  "s_add_u32 m0, s56, 95232\n" \
  "s_nop 0\n" \
  "buffer_load_dwordx4 v45, s[40:43], s71 offen lds\n" \
  "ds_read_b64_tr_b16 a[32:33], v80\n" \
  "ds_read_b64_tr_b16 a[34:35], v80 offset:768\n" \
  "ds_read_b64_tr_b16 a[36:37], v80 offset:12288\n" \
  "ds_read_b64_tr_b16 a[38:39], v80 offset:13056\n" \
  "ds_read_b64_tr_b16 a[112:113], v80 offset:32\n" \
  "ds_read_b64_tr_b16 a[114:115], v80 offset:800\n" \
  "ds_read_b64_tr_b16 a[116:117], v80 offset:12320\n" \
  "ds_read_b64_tr_b16 a[118:119], v80 offset:13088\n" \
  "ds_read_b64_tr_b16 a[192:193], v81\n" \
  "ds_read_b64_tr_b16 a[194:195], v81 offset:768\n" \
  "ds_read_b64_tr_b16 a[196:197], v81 offset:12288\n" \
  "ds_read_b64_tr_b16 a[198:199], v81 offset:13056\n" \
  "s_waitcnt lgkmcnt(0)\n" \
  "ds_read_b64_tr_b16 a[40:41], v80 offset:24576\n" \
  "ds_read_b64_tr_b16 a[42:43], v80 offset:25344\n" \
  "ds_read_b64_tr_b16 a[44:45], v80 offset:36864\n" \
  "ds_read_b64_tr_b16 a[46:47], v80 offset:37632\n" \
  "ds_read_b64_tr_b16 a[120:121], v80 offset:24608\n" \
  "ds_read_b64_tr_b16 a[122:123], v80 offset:25376\n" \
  "ds_read_b64_tr_b16 a[124:125], v80 offset:36896\n" \
  "ds_read_b64_tr_b16 a[126:127], v80 offset:37664\n" \
  "ds_read_b64_tr_b16 a[200:201], v81 offset:24576\n" \
  "ds_read_b64_tr_b16 a[202:203], v81 offset:25344\n" \
  "ds_read_b64_tr_b16 a[204:205], v81 offset:36864\n" \
  "ds_read_b64_tr_b16 a[206:207], v81 offset:37632\n" \
  "s_waitcnt lgkmcnt(0)\n" \
  "s_waitcnt vmcnt(12) lgkmcnt(0)\n" \
  "s_barrier\n" \
  "s_mov_b32 s71, 0\n" \
  "s_add_u32 m0, s57, 114688\n" \
  "s_nop 0\n" \
  "buffer_load_dwordx4 v50, s[44:47], s71 offen lds\n" \
  "s_add_u32 m0, s57, 115712\n" \
  "s_nop 0\n" \
  "buffer_load_dwordx4 v51, s[44:47], s71 offen lds\n" \
  "s_add_u32 m0, s57, 116736\n" \
  "s_nop 0\n" \
  "buffer_load_dwordx4 v52, s[44:47], s71 offen lds\n" \
  "s_add_u32 m0, s57, 117760\n" \
  "s_nop 0\n" \
  "buffer_load_dwordx4 v53, s[44:47], s71 offen lds\n" \
  "s_add_u32 m0, s57, 118784\n" \
  "s_nop 0\n" \
  "buffer_load_dwordx4 v54, s[44:47], s71 offen lds\n" \
  "s_add_u32 m0, s57, 119808\n" \
  "s_nop 0\n" \
  "buffer_load_dwordx4 v55, s[44:47], s71 offen lds\n" \
  "s_add_u32 m0, s57, 120832\n" \
  "s_nop 0\n" \
  "buffer_load_dwordx4 v56, s[44:47], s71 offen lds\n" \
  "s_add_u32 m0, s57, 121856\n" \
  "s_nop 0\n" \
  "buffer_load_dwordx4 v57, s[44:47], s71 offen lds\n" \
  "s_add_u32 m0, s57, 122880\n" \
  "s_nop 0\n" \
  "buffer_load_dwordx4 v58, s[44:47], s71 offen lds\n" \
  "s_add_u32 m0, s57, 123904\n" \
  "s_nop 0\n" \
  "buffer_load_dwordx4 v59, s[44:47], s71 offen lds\n" \
  "ds_read_b64_tr_b16 a[48:49], v72\n" \
  "ds_read_b64_tr_b16 a[50:51], v72 offset:768\n" \
  "ds_read_b64_tr_b16 a[52:53], v72 offset:12288\n" \
  "ds_read_b64_tr_b16 a[54:55], v72 offset:13056\n" \
  "ds_read_b64_tr_b16 a[128:129], v72 offset:32\n" \
  "ds_read_b64_tr_b16 a[130:131], v72 offset:800\n" \
  "ds_read_b64_tr_b16 a[132:133], v72 offset:12320\n" \
  "ds_read_b64_tr_b16 a[134:135], v72 offset:13088\n" \
  "ds_read_b64_tr_b16 a[208:209], v73\n" \
  "ds_read_b64_tr_b16 a[210:211], v73 offset:768\n" \
  "ds_read_b64_tr_b16 a[212:213], v73 offset:12288\n" \
  "ds_read_b64_tr_b16 a[214:215], v73 offset:13056\n" \
  "s_waitcnt lgkmcnt(0)\n" \
  "ds_read_b64_tr_b16 a[56:57], v72 offset:24576\n" \
  "ds_read_b64_tr_b16 a[58:59], v72 offset:25344\n" \
  "ds_read_b64_tr_b16 a[60:61], v72 offset:36864\n" \
  "ds_read_b64_tr_b16 a[62:63], v72 offset:37632\n" \
  "ds_read_b64_tr_b16 a[136:137], v72 offset:24608\n" \
  "ds_read_b64_tr_b16 a[138:139], v72 offset:25376\n" \
  "ds_read_b64_tr_b16 a[140:141], v72 offset:36896\n" \
  "ds_read_b64_tr_b16 a[142:143], v72 offset:37664\n" \
  "ds_read_b64_tr_b16 a[216:217], v73 offset:24576\n" \
  "ds_read_b64_tr_b16 a[218:219], v73 offset:25344\n" \
  "ds_read_b64_tr_b16 a[220:221], v73 offset:36864\n" \
  "ds_read_b64_tr_b16 a[222:223], v73 offset:37632\n" \
  "s_waitcnt lgkmcnt(0)\n" \
  "s_waitcnt vmcnt(10) lgkmcnt(0)\n" \
  "s_barrier\n" \
  "s_mov_b32 s71, 40960\n" \
  "s_add_u32 m0, s57, 16384\n" \
  "s_nop 0\n" \
  "buffer_load_dwordx4 v50, s[44:47], s71 offen lds\n" \
  "s_add_u32 m0, s57, 17408\n" \
  "s_nop 0\n" \
  "buffer_load_dwordx4 v51, s[44:47], s71 offen lds\n" \
  "s_add_u32 m0, s57, 18432\n" \
  "s_nop 0\n" \
  "buffer_load_dwordx4 v52, s[44:47], s71 offen lds\n" \
  "s_add_u32 m0, s57, 19456\n" \
  "s_nop 0\n" \
  "buffer_load_dwordx4 v53, s[44:47], s71 offen lds\n" \
  "s_add_u32 m0, s57, 20480\n" \
  "s_nop 0\n" \
  "buffer_load_dwordx4 v54, s[44:47], s71 offen lds\n" \
  "s_add_u32 m0, s57, 21504\n" \
  "s_nop 0\n" \
  "buffer_load_dwordx4 v55, s[44:47], s71 offen lds\n" \
  "s_add_u32 m0, s57, 22528\n" \
  "s_nop 0\n" \
  "buffer_load_dwordx4 v56, s[44:47], s71 offen lds\n" \
  "s_add_u32 m0, s57, 23552\n" \
  "s_nop 0\n" \
  "buffer_load_dwordx4 v57, s[44:47], s71 offen lds\n" \
  "s_add_u32 m0, s57, 24576\n" \
  "s_nop 0\n" \
  "buffer_load_dwordx4 v58, s[44:47], s71 offen lds\n" \
  "s_add_u32 m0, s57, 25600\n" \
  "s_nop 0\n" \
  "buffer_load_dwordx4 v59, s[44:47], s71 offen lds\n" \
  "ds_read_b64_tr_b16 a[64:65], v76\n" \
  "ds_read_b64_tr_b16 a[66:67], v76 offset:768\n" \
  "ds_read_b64_tr_b16 a[68:69], v76 offset:12288\n" \
  "ds_read_b64_tr_b16 a[70:71], v76 offset:13056\n" \
  "ds_read_b64_tr_b16 a[144:145], v76 offset:32\n" \
  "ds_read_b64_tr_b16 a[146:147], v76 offset:800\n" \
  "ds_read_b64_tr_b16 a[148:149], v76 offset:12320\n" \
  "ds_read_b64_tr_b16 a[150:151], v76 offset:13088\n" \
  "ds_read_b64_tr_b16 a[224:225], v77\n" \
  "ds_read_b64_tr_b16 a[226:227], v77 offset:768\n" \
  "ds_read_b64_tr_b16 a[228:229], v77 offset:12288\n" \
  "ds_read_b64_tr_b16 a[230:231], v77 offset:13056\n" \
  "s_waitcnt lgkmcnt(0)\n" \
  "ds_read_b64_tr_b16 a[72:73], v76 offset:24576\n" \
  "ds_read_b64_tr_b16 a[74:75], v76 offset:25344\n" \
  "ds_read_b64_tr_b16 a[76:77], v76 offset:36864\n" \
  "ds_read_b64_tr_b16 a[78:79], v76 offset:37632\n" \
  "ds_read_b64_tr_b16 a[152:153], v76 offset:24608\n" \
  "ds_read_b64_tr_b16 a[154:155], v76 offset:25376\n" \
  "ds_read_b64_tr_b16 a[156:157], v76 offset:36896\n" \
  "ds_read_b64_tr_b16 a[158:159], v76 offset:37664\n" \
  "ds_read_b64_tr_b16 a[232:233], v77 offset:24576\n" \
  "ds_read_b64_tr_b16 a[234:235], v77 offset:25344\n" \
  "ds_read_b64_tr_b16 a[236:237], v77 offset:36864\n" \
  "ds_read_b64_tr_b16 a[238:239], v77 offset:37632\n" \
  "s_waitcnt lgkmcnt(0)\n" \
  "s_waitcnt vmcnt(10) lgkmcnt(0)\n" \
  "s_barrier\n" \
  "s_mov_b32 s71, 81920\n" \
  "s_add_u32 m0, s57, 65536\n" \
  "s_nop 0\n" \
  "buffer_load_dwordx4 v50, s[44:47], s71 offen lds\n" \
  "s_add_u32 m0, s57, 66560\n" \
  "s_nop 0\n" \
  "buffer_load_dwordx4 v51, s[44:47], s71 offen lds\n" \
  "s_add_u32 m0, s57, 67584\n" \
  "s_nop 0\n" \
  "buffer_load_dwordx4 v52, s[44:47], s71 offen lds\n" \
  "s_add_u32 m0, s57, 68608\n" \
  "s_nop 0\n" \
  "buffer_load_dwordx4 v53, s[44:47], s71 offen lds\n" \
  "s_add_u32 m0, s57, 69632\n" \
  "s_nop 0\n" \
  "buffer_load_dwordx4 v54, s[44:47], s71 offen lds\n" \
  "s_add_u32 m0, s57, 70656\n" \
  "s_nop 0\n" \
  "buffer_load_dwordx4 v55, s[44:47], s71 offen lds\n" \
  "s_add_u32 m0, s57, 71680\n" \
  "s_nop 0\n" \
  "buffer_load_dwordx4 v56, s[44:47], s71 offen lds\n" \
  "s_add_u32 m0, s57, 72704\n" \
  "s_nop 0\n" \
  "buffer_load_dwordx4 v57, s[44:47], s71 offen lds\n" \
  "s_add_u32 m0, s57, 73728\n" \
  "s_nop 0\n" \
  "buffer_load_dwordx4 v58, s[44:47], s71 offen lds\n" \
  "s_add_u32 m0, s57, 74752\n" \
  "s_nop 0\n" \
  "buffer_load_dwordx4 v59, s[44:47], s71 offen lds\n" \
  "ds_read_b64_tr_b16 v[112:113], v82\n" \
  "ds_read_b64_tr_b16 v[114:115], v82 offset:2560\n" \
  "ds_read_b64_tr_b16 v[116:117], v82 offset:20480\n" \
  "ds_read_b64_tr_b16 v[118:119], v82 offset:23040\n" \
  "ds_read_b64_tr_b16 v[144:145], v82 offset:256\n" \
  "ds_read_b64_tr_b16 v[146:147], v82 offset:2816\n" \
  "ds_read_b64_tr_b16 v[148:149], v82 offset:20736\n" \
  "ds_read_b64_tr_b16 v[150:151], v82 offset:23296\n" \
  "s_waitcnt lgkmcnt(0)\n" \
  "ds_read_b64_tr_b16 v[176:177], v82 offset:512\n" \
  "ds_read_b64_tr_b16 v[178:179], v82 offset:3072\n" \
  "ds_read_b64_tr_b16 v[180:181], v82 offset:20992\n" \
  "ds_read_b64_tr_b16 v[182:183], v82 offset:23552\n" \
  "ds_read_b64_tr_b16 v[208:209], v82 offset:768\n" \
  "ds_read_b64_tr_b16 v[210:211], v82 offset:3328\n" \
  "ds_read_b64_tr_b16 v[212:213], v82 offset:21248\n" \
  "ds_read_b64_tr_b16 v[214:215], v82 offset:23808\n" \
  "s_waitcnt lgkmcnt(0)\n" \
  "ds_read_b64_tr_b16 v[240:241], v82 offset:1024\n" \
  "ds_read_b64_tr_b16 v[242:243], v82 offset:3584\n" \
  "ds_read_b64_tr_b16 v[244:245], v82 offset:21504\n" \
  "ds_read_b64_tr_b16 v[246:247], v82 offset:24064\n" \
  "s_waitcnt vmcnt(10) lgkmcnt(0)\n" \
  "s_barrier\n" \
  "s_mov_b32 s71, 122880\n" \
  "s_add_u32 m0, s57, 114688\n" \
  "s_nop 0\n" \
  "buffer_load_dwordx4 v50, s[44:47], s71 offen lds\n" \
  "s_add_u32 m0, s57, 115712\n" \
  "s_nop 0\n" \
  "buffer_load_dwordx4 v51, s[44:47], s71 offen lds\n" \
  "s_add_u32 m0, s57, 116736\n" \
  "s_nop 0\n" \
  "buffer_load_dwordx4 v52, s[44:47], s71 offen lds\n" \
  "s_add_u32 m0, s57, 117760\n" \
  "s_nop 0\n" \
  "buffer_load_dwordx4 v53, s[44:47], s71 offen lds\n" \
  "s_add_u32 m0, s57, 118784\n" \
  "s_nop 0\n" \
  "buffer_load_dwordx4 v54, s[44:47], s71 offen lds\n" \
  "s_add_u32 m0, s57, 119808\n" \
  "s_nop 0\n" \
  "buffer_load_dwordx4 v55, s[44:47], s71 offen lds\n" \
  "s_add_u32 m0, s57, 120832\n" \
  "s_nop 0\n" \
  "buffer_load_dwordx4 v56, s[44:47], s71 offen lds\n" \
  "s_add_u32 m0, s57, 121856\n" \
  "s_nop 0\n" \
  "buffer_load_dwordx4 v57, s[44:47], s71 offen lds\n" \
  "s_add_u32 m0, s57, 122880\n" \
  "s_nop 0\n" \
  "buffer_load_dwordx4 v58, s[44:47], s71 offen lds\n" \
  "s_add_u32 m0, s57, 123904\n" \
  "s_nop 0\n" \
  "buffer_load_dwordx4 v59, s[44:47], s71 offen lds\n" \
  "ds_read_b64_tr_b16 v[120:121], v74\n" \
  "ds_read_b64_tr_b16 v[122:123], v74 offset:2560\n" \
  "ds_read_b64_tr_b16 v[124:125], v74 offset:20480\n" \
  "ds_read_b64_tr_b16 v[126:127], v74 offset:23040\n" \
  "ds_read_b64_tr_b16 v[152:153], v74 offset:256\n" \
  "ds_read_b64_tr_b16 v[154:155], v74 offset:2816\n" \
  "ds_read_b64_tr_b16 v[156:157], v74 offset:20736\n" \
  "ds_read_b64_tr_b16 v[158:159], v74 offset:23296\n" \
  "s_waitcnt lgkmcnt(0)\n" \
  "ds_read_b64_tr_b16 v[184:185], v74 offset:512\n" \
  "ds_read_b64_tr_b16 v[186:187], v74 offset:3072\n" \
  "ds_read_b64_tr_b16 v[188:189], v74 offset:20992\n" \
  "ds_read_b64_tr_b16 v[190:191], v74 offset:23552\n" \
  "ds_read_b64_tr_b16 v[216:217], v74 offset:768\n" \
  "ds_read_b64_tr_b16 v[218:219], v74 offset:3328\n" \
  "ds_read_b64_tr_b16 v[220:221], v74 offset:21248\n" \
  "ds_read_b64_tr_b16 v[222:223], v74 offset:23808\n" \
  "s_waitcnt lgkmcnt(0)\n" \
  "ds_read_b64_tr_b16 v[248:249], v74 offset:1024\n" \
  "ds_read_b64_tr_b16 v[250:251], v74 offset:3584\n" \
  "ds_read_b64_tr_b16 v[252:253], v74 offset:21504\n" \
  "ds_read_b64_tr_b16 v[254:255], v74 offset:24064\n" \
  "s_waitcnt vmcnt(10) lgkmcnt(0)\n" \
  "s_barrier\n" \
  "s_mov_b32 s71, 163840\n" \
  "s_add_u32 m0, s57, 16384\n" \
  "s_nop 0\n" \
  "buffer_load_dwordx4 v50, s[44:47], s71 offen lds\n" \
  "s_add_u32 m0, s57, 17408\n" \
  "s_nop 0\n" \
  "buffer_load_dwordx4 v51, s[44:47], s71 offen lds\n" \
  "s_add_u32 m0, s57, 18432\n" \
  "s_nop 0\n" \
  "buffer_load_dwordx4 v52, s[44:47], s71 offen lds\n" \
  "s_add_u32 m0, s57, 19456\n" \
  "s_nop 0\n" \
  "buffer_load_dwordx4 v53, s[44:47], s71 offen lds\n" \
  "s_add_u32 m0, s57, 20480\n" \
  "s_nop 0\n" \
  "buffer_load_dwordx4 v54, s[44:47], s71 offen lds\n" \
  "s_add_u32 m0, s57, 21504\n" \
  "s_nop 0\n" \
  "buffer_load_dwordx4 v55, s[44:47], s71 offen lds\n" \
  "s_add_u32 m0, s57, 22528\n" \
  "s_nop 0\n" \
  "buffer_load_dwordx4 v56, s[44:47], s71 offen lds\n" \
  "s_add_u32 m0, s57, 23552\n" \
  "s_nop 0\n" \
  "buffer_load_dwordx4 v57, s[44:47], s71 offen lds\n" \
  "s_add_u32 m0, s57, 24576\n" \
  "s_nop 0\n" \
  "buffer_load_dwordx4 v58, s[44:47], s71 offen lds\n" \
  "s_add_u32 m0, s57, 25600\n" \
  "s_nop 0\n" \
  "buffer_load_dwordx4 v59, s[44:47], s71 offen lds\n" \
  "ds_read_b64_tr_b16 v[128:129], v78\n" \
  "ds_read_b64_tr_b16 v[130:131], v78 offset:2560\n" \
  "ds_read_b64_tr_b16 v[132:133], v78 offset:20480\n" \
  "ds_read_b64_tr_b16 v[134:135], v78 offset:23040\n" \
  "ds_read_b64_tr_b16 v[160:161], v78 offset:256\n" \
  "ds_read_b64_tr_b16 v[162:163], v78 offset:2816\n" \
  "ds_read_b64_tr_b16 v[164:165], v78 offset:20736\n" \
  "ds_read_b64_tr_b16 v[166:167], v78 offset:23296\n" \
  "s_waitcnt lgkmcnt(0)\n" \
  "ds_read_b64_tr_b16 v[192:193], v78 offset:512\n" \
  "ds_read_b64_tr_b16 v[194:195], v78 offset:3072\n" \
  "ds_read_b64_tr_b16 v[196:197], v78 offset:20992\n" \
  "ds_read_b64_tr_b16 v[198:199], v78 offset:23552\n" \
  "ds_read_b64_tr_b16 v[224:225], v78 offset:768\n" \
  "ds_read_b64_tr_b16 v[226:227], v78 offset:3328\n" \
  "ds_read_b64_tr_b16 v[228:229], v78 offset:21248\n" \
  "ds_read_b64_tr_b16 v[230:231], v78 offset:23808\n" \
  "s_waitcnt lgkmcnt(0)\n" \
  "ds_read_b64_tr_b16 a[240:241], v78 offset:1024\n" \
  "ds_read_b64_tr_b16 a[242:243], v78 offset:3584\n" \
  "ds_read_b64_tr_b16 a[244:245], v78 offset:21504\n" \
  "ds_read_b64_tr_b16 a[246:247], v78 offset:24064\n" \
  "s_waitcnt vmcnt(10) lgkmcnt(0)\n" \
  "s_barrier\n" \
  "s_mov_b32 s71, 204800\n" \
  "s_add_u32 m0, s57, 65536\n" \
  "s_nop 0\n" \
  "buffer_load_dwordx4 v50, s[44:47], s71 offen lds\n" \
  "s_add_u32 m0, s57, 66560\n" \
  "s_nop 0\n" \
  "buffer_load_dwordx4 v51, s[44:47], s71 offen lds\n" \
  "s_add_u32 m0, s57, 67584\n" \
  "s_nop 0\n" \
  "buffer_load_dwordx4 v52, s[44:47], s71 offen lds\n" \
  "s_add_u32 m0, s57, 68608\n" \
  "s_nop 0\n" \
  "buffer_load_dwordx4 v53, s[44:47], s71 offen lds\n" \
  "s_add_u32 m0, s57, 69632\n" \
  "s_nop 0\n" \
  "buffer_load_dwordx4 v54, s[44:47], s71 offen lds\n" \
  "s_add_u32 m0, s57, 70656\n" \
  "s_nop 0\n" \
  "buffer_load_dwordx4 v55, s[44:47], s71 offen lds\n" \
  "s_add_u32 m0, s57, 71680\n" \
  "s_nop 0\n" \
  "buffer_load_dwordx4 v56, s[44:47], s71 offen lds\n" \
  "s_add_u32 m0, s57, 72704\n" \
  "s_nop 0\n" \
  "buffer_load_dwordx4 v57, s[44:47], s71 offen lds\n" \
  "s_add_u32 m0, s57, 73728\n" \
  "s_nop 0\n" \
  "buffer_load_dwordx4 v58, s[44:47], s71 offen lds\n" \
  "s_add_u32 m0, s57, 74752\n" \
  "s_nop 0\n" \
  "buffer_load_dwordx4 v59, s[44:47], s71 offen lds\n" \
  "ds_read_b64_tr_b16 v[136:137], v82\n" \
  "ds_read_b64_tr_b16 v[138:139], v82 offset:2560\n" \
  "ds_read_b64_tr_b16 v[140:141], v82 offset:20480\n" \
  "ds_read_b64_tr_b16 v[142:143], v82 offset:23040\n" \
  "ds_read_b64_tr_b16 v[168:169], v82 offset:256\n" \
  "ds_read_b64_tr_b16 v[170:171], v82 offset:2816\n" \
  "ds_read_b64_tr_b16 v[172:173], v82 offset:20736\n" \
  "ds_read_b64_tr_b16 v[174:175], v82 offset:23296\n" \
  "s_waitcnt lgkmcnt(0)\n" \
  "ds_read_b64_tr_b16 v[200:201], v82 offset:512\n" \
  "ds_read_b64_tr_b16 v[202:203], v82 offset:3072\n" \
  "ds_read_b64_tr_b16 v[204:205], v82 offset:20992\n" \
  "ds_read_b64_tr_b16 v[206:207], v82 offset:23552\n" \
  "ds_read_b64_tr_b16 v[232:233], v82 offset:768\n" \
  "ds_read_b64_tr_b16 v[234:235], v82 offset:3328\n" \
  "ds_read_b64_tr_b16 v[236:237], v82 offset:21248\n" \
  "ds_read_b64_tr_b16 v[238:239], v82 offset:23808\n" \
  "s_waitcnt lgkmcnt(0)\n" \
  "ds_read_b64_tr_b16 a[248:249], v82 offset:1024\n" \
  "ds_read_b64_tr_b16 a[250:251], v82 offset:3584\n" \
  "ds_read_b64_tr_b16 a[252:253], v82 offset:21504\n" \
  "ds_read_b64_tr_b16 a[254:255], v82 offset:24064\n" \
  "s_waitcnt vmcnt(10) lgkmcnt(0)\n" \
  "s_barrier\n" \
  "s_waitcnt vmcnt(0) lgkmcnt(0)\n" \
  "s_barrier\n" \
  "s_waitcnt lgkmcnt(0)\n" \
  "s_barrier\n" \
  "LIV_%=:\n" \
  "buffer_load_dword v92, v69, s[36:39], 0 offen sc1\n" \
  "s_waitcnt vmcnt(0)\n" \
  "v_and_b32 v93, -4, v92\n" \
  "v_cmp_ne_u32 vcc, s58, v93\n" \
  "s_cbranch_vccz LIXV_%=\n" \
  "s_add_u32 s60, s60, 1\n" \
  "s_cmp_gt_u32 s60, 20000\n" \
  "s_cbranch_scc1 LIXV_%=\n" \
  "s_sleep 8\n" \
  "s_branch LIV_%=\n" \
  "LIXV_%=:\n" \
  "v_and_b32 v92, v70, v92\n" \
  "v_cmp_ne_u32 s[80:81], 0, v92\n" \
  "s_mov_b32 s78, 0x55555555\n" \
  "s_mov_b32 s79, 0x55555555\n" \
  "s_xor_b64 s[82:83], s[80:81], s[78:79]\n" \
  "s_bcnt1_i32_b64 s71, s[82:83]\n" \
  "s_cmp_le_u32 s71, 26\n" \
  "s_cselect_b64 s[80:81], s[78:79], s[80:81]\n" \
  "s_not_b64 s[78:79], s[78:79]\n" \
  "s_cmp_ge_u32 s71, 38\n" \
  "s_cselect_b64 s[80:81], s[78:79], s[80:81]\n" \
  "v_lshrrev_b32 v92, 21, v25\n" \
  "v_lshrrev_b64 v[92:93], v92, s[80:81]\n" \
  "v_and_b32 v92, 1, v92\n" \
  "v_lshlrev_b32 v92, 13, v92\n" \
  "v_xor_b32 v25, v92, v25\n" \
  "v_lshrrev_b32 v92, 21, v26\n" \
  "v_lshrrev_b64 v[92:93], v92, s[80:81]\n" \
  "v_and_b32 v92, 1, v92\n" \
  "v_lshlrev_b32 v92, 13, v92\n" \
  "v_xor_b32 v26, v92, v26\n" \
  "v_lshrrev_b32 v92, 21, v27\n" \
  "v_lshrrev_b64 v[92:93], v92, s[80:81]\n" \
  "v_and_b32 v92, 1, v92\n" \
  "v_lshlrev_b32 v92, 13, v92\n" \
  "v_xor_b32 v27, v92, v27\n" \
  "v_lshrrev_b32 v92, 21, v28\n" \
  "v_lshrrev_b64 v[92:93], v92, s[80:81]\n" \
  "v_and_b32 v92, 1, v92\n" \
  "v_lshlrev_b32 v92, 13, v92\n" \
  "v_xor_b32 v28, v92, v28\n" \
  "v_lshrrev_b32 v92, 21, v29\n" \
  "v_lshrrev_b64 v[92:93], v92, s[80:81]\n" \
  "v_and_b32 v92, 1, v92\n" \
  "v_lshlrev_b32 v92, 13, v92\n" \
  "v_xor_b32 v29, v92, v29\n" \
  "v_lshrrev_b32 v92, 21, v30\n" \
  "v_lshrrev_b64 v[92:93], v92, s[80:81]\n" \
  "v_and_b32 v92, 1, v92\n" \
  "v_lshlrev_b32 v92, 13, v92\n" \
  "v_xor_b32 v30, v92, v30\n" \
  "v_lshrrev_b32 v92, 21, v67\n" \
  "v_lshrrev_b64 v[92:93], v92, s[80:81]\n" \
  "v_and_b32 v92, 1, v92\n" \
  "v_lshlrev_b32 v92, 13, v92\n" \
  "v_xor_b32 v67, v92, v67\n" \
  "v_lshrrev_b32 v92, 21, v71\n" \
  "v_lshrrev_b64 v[92:93], v92, s[80:81]\n" \
  "v_and_b32 v92, 1, v92\n" \
  "v_lshlrev_b32 v92, 13, v92\n" \
  "v_xor_b32 v71, v92, v71\n" \
  "s_lshr_b32 s71, s54, 21\n" \
  "s_lshr_b64 s[80:81], s[80:81], s71\n" \
  "s_and_b32 s71, s80, 1\n" \
  "s_lshl_b32 s71, s71, 13\n" \
  "s_or_b32 s54, s54, s71\n" \
  "s_and_b32 s71, s48, 3\n" \
  "s_cmp_eq_u32 s71, 3\n" \
  "s_cbranch_scc0 LIYV_%=\n" \
  "v_mov_b32 v1, v71\n" \
  "LIYV_%=:\n" \
  "buffer_atomic_add v32, v68, s[36:39], 0 offen\n" \
  "v_mov_b32 v104, v64\n" \
  "v_mov_b32 v105, v65\n" \
  "v_mov_b32 v106, v66\n" \
  "v_mov_b32 v107, v67\n" \
  "v_mov_b32 v28, v25\n" \
  "v_mov_b32 v88, v74\n" \
  "v_mov_b32 v89, v78\n" \
  "ds_read_b64_tr_b16 v[40:41], v88\n" \
  "ds_read_b64_tr_b16 v[42:43], v88 offset:2560\n" \
  "ds_read_b64_tr_b16 v[44:45], v88 offset:20480\n" \
  "ds_read_b64_tr_b16 v[46:47], v88 offset:23040\n" \
  "ds_read_b64_tr_b16 v[48:49], v88 offset:256\n" \
  "ds_read_b64_tr_b16 v[50:51], v88 offset:2816\n" \
  "ds_read_b64_tr_b16 v[52:53], v88 offset:20736\n" \
  "ds_read_b64_tr_b16 v[54:55], v88 offset:23296\n" \
  "s_waitcnt lgkmcnt(0)\n" \
  "ds_read_b64_tr_b16 v[56:57], v88 offset:512\n" \
  "ds_read_b64_tr_b16 v[58:59], v88 offset:3072\n" \
  "ds_read_b64_tr_b16 v[60:61], v88 offset:20992\n" \
  "ds_read_b64_tr_b16 v[62:63], v88 offset:23552\n" \
  "ds_read_b64_tr_b16 v[64:65], v88 offset:768\n" \
  "ds_read_b64_tr_b16 v[66:67], v88 offset:3328\n" \
  "ds_read_b64_tr_b16 v[68:69], v88 offset:21248\n" \
  "ds_read_b64_tr_b16 v[70:71], v88 offset:23808\n" \
  "s_waitcnt lgkmcnt(0)\n" \
  "ds_read_b64_tr_b16 v[72:73], v88 offset:1024\n" \
  "ds_read_b64_tr_b16 v[74:75], v88 offset:3584\n" \
  "ds_read_b64_tr_b16 v[76:77], v88 offset:21504\n" \
  "ds_read_b64_tr_b16 v[78:79], v88 offset:24064\n" \
  "s_waitcnt lgkmcnt(0)\n" \
  "s_barrier\n" \
  "ds_write_b128 v11, v[40:43] offset:0\n" \
  "ds_write_b128 v11, v[44:47] offset:1024\n" \
  "ds_write_b128 v11, v[48:51] offset:2048\n" \
  "ds_write_b128 v11, v[52:55] offset:3072\n" \
  "ds_write_b128 v11, v[56:59] offset:4096\n" \
  "ds_write_b128 v11, v[60:63] offset:5120\n" \
  "ds_write_b128 v11, v[64:67] offset:6144\n" \
  "ds_write_b128 v11, v[68:71] offset:7168\n" \
  "ds_write_b128 v11, v[72:75] offset:8192\n" \
  "ds_write_b128 v11, v[76:79] offset:9216\n" \
  "s_waitcnt lgkmcnt(0)\n" \
  "ds_read_b64_tr_b16 v[40:41], v89\n" \
  "ds_read_b64_tr_b16 v[42:43], v89 offset:2560\n" \
  "ds_read_b64_tr_b16 v[44:45], v89 offset:20480\n" \
  "ds_read_b64_tr_b16 v[46:47], v89 offset:23040\n" \
  "ds_read_b64_tr_b16 v[48:49], v89 offset:256\n" \
  "ds_read_b64_tr_b16 v[50:51], v89 offset:2816\n" \
  "ds_read_b64_tr_b16 v[52:53], v89 offset:20736\n" \
  "ds_read_b64_tr_b16 v[54:55], v89 offset:23296\n" \
  "s_waitcnt lgkmcnt(0)\n" \
  "ds_read_b64_tr_b16 v[56:57], v89 offset:512\n" \
  "ds_read_b64_tr_b16 v[58:59], v89 offset:3072\n" \
  "ds_read_b64_tr_b16 v[60:61], v89 offset:20992\n" \
  "ds_read_b64_tr_b16 v[62:63], v89 offset:23552\n" \
  "ds_read_b64_tr_b16 v[64:65], v89 offset:768\n" \
  "ds_read_b64_tr_b16 v[66:67], v89 offset:3328\n" \
  "ds_read_b64_tr_b16 v[68:69], v89 offset:21248\n" \
  "ds_read_b64_tr_b16 v[70:71], v89 offset:23808\n" \
  "s_waitcnt lgkmcnt(0)\n" \
  "ds_read_b64_tr_b16 v[72:73], v89 offset:1024\n" \
  "ds_read_b64_tr_b16 v[74:75], v89 offset:3584\n" \
  "ds_read_b64_tr_b16 v[76:77], v89 offset:21504\n" \
  "ds_read_b64_tr_b16 v[78:79], v89 offset:24064\n" \
  "s_waitcnt lgkmcnt(0)\n" \
  "s_barrier\n" \
  "ds_write_b128 v11, v[40:43] offset:40960\n" \
  "ds_write_b128 v11, v[44:47] offset:41984\n" \
  "ds_write_b128 v11, v[48:51] offset:43008\n" \
  "ds_write_b128 v11, v[52:55] offset:44032\n" \
  "ds_write_b128 v11, v[56:59] offset:45056\n" \
  "ds_write_b128 v11, v[60:63] offset:46080\n" \
  "ds_write_b128 v11, v[64:67] offset:47104\n" \
  "ds_write_b128 v11, v[68:71] offset:48128\n" \
  "ds_write_b128 v11, v[72:75] offset:49152\n" \
  "ds_write_b128 v11, v[76:79] offset:50176\n" \
  "s_waitcnt lgkmcnt(0)\n" \
  "s_barrier\n" \
  "LSW_%=:\n" \
  "s_memrealtime s[74:75]\n" \
  "s_waitcnt vmcnt(0) lgkmcnt(0)\n" \
  "s_mov_b32 s63, s74\n" \
  "s_and_b32 s71, s48, 3\n" \
  "s_cmp_eq_u32 s71, 2\n" \
  "s_cbranch_scc0 LHB_%=\n" \
  "s_cmp_eq_u32 s49, 0\n" \
  "s_cbranch_scc0 LHA_%=\n" \
  "LOA_%=:\n" \
  "buffer_load_dword v36, v104, s[36:39], 0 offen sc1\n" \
  "s_waitcnt vmcnt(0)\n" \
  "v_cmp_gt_u32 vcc, 96, v36\n" \
  "s_cbranch_vccz LOXA_%=\n" \
  "s_add_u32 s60, s60, 1\n" \
  "s_cmp_gt_u32 s60, 20000\n" \
  "s_cbranch_scc1 LOXA_%=\n" \
  "s_sleep 4\n" \
  "s_branch LOA_%=\n" \
  "LOXA_%=:\n" \
  "LOB_%=:\n" \
  "buffer_load_dword v36, v107, s[36:39], 0 offen sc1\n" \
  "s_waitcnt vmcnt(0)\n" \
  "v_cmp_gt_u32 vcc, 8, v36\n" \
  "s_cbranch_vccz LOXB_%=\n" \
  "s_add_u32 s60, s60, 1\n" \
  "s_cmp_gt_u32 s60, 20000\n" \
  "s_cbranch_scc1 LOXB_%=\n" \
  "s_sleep 2\n" \
  "s_branch LOB_%=\n" \
  "LOXB_%=:\n" \
  "v_cmp_gt_u32 vcc, 16, v36\n" \
  "s_cmp_eq_u64 vcc, 0\n" \
  "s_cselect_b32 s53, 135, s53\n" \
  "s_mov_b32 s95, s62\n" \
  "s_mov_b32 s96, s86\n" \
  "s_memrealtime s[74:75]\n" \
  "s_memtime s[76:77]\n" \
  "s_waitcnt lgkmcnt(0)\n" \
  "s_sub_u32 s71, s76, s96\n" \
  "s_sub_u32 s72, s74, s95\n" \
  "s_mov_b32 s95, s74\n" \
  "s_mov_b32 s96, s76\n" \
  "v_cvt_f32_u32 v36, s71\n" \
  "v_cvt_f32_u32 v37, s72\n" \
  "v_rcp_f32 v37, v37\n" \
  "v_mul_f32 v36, 0x43810000, v36\n" \
  "v_mul_f32 v36, v36, v37\n" \
  "v_cvt_u32_f32 v36, v36\n" \
  "v_min_u32 v36, 6400, v36\n" \
  "v_max_u32 v36, 3072, v36\n" \
  "s_nop 0\n" \
  "v_readfirstlane_b32 s90, v36\n" \
  "s_nop 1\n" \
  "v_cvt_f32_u32 v36, s90\n" \
  "v_rcp_f32 v36, v36\n" \
  "v_mov_b32 v37, 0xc0b00000\n" \
  "v_fmac_f32 v37, 0x48cad280, v36\n" \
  "v_cvt_u32_f32 v37, v37\n" \
  "v_add_u32 v37, 12, v37\n" \
  "buffer_store_dword v37, v30, s[36:39], 0 offen sc1\n" \
  "s_add_u32 s71, s74, s53\n" \
  "s_or_b32 s71, s71, 1\n" \
  "v_mov_b32 v36, s71\n" \
  "buffer_store_dword v36, v106, s[36:39], 0 offen sc1\n" \
  "s_nop 1\n" \
  "LHA_%=:\n" \
  "LOC_%=:\n" \
  "buffer_load_dword v104, v105, s[36:39], 0 offen sc1\n" \
  "s_waitcnt vmcnt(0)\n" \
  "v_cmp_gt_u32 vcc, 1, v104\n" \
  "s_cbranch_vccz LOXC_%=\n" \
  "s_add_u32 s60, s60, 1\n" \
  "s_cmp_gt_u32 s60, 20000\n" \
  "s_cbranch_scc1 LOXC_%=\n" \
  "s_sleep 4\n" \
  "s_branch LOC_%=\n" \
  "LOXC_%=:\n" \
  "s_mov_b32 s95, s62\n" \
  "s_mov_b32 s96, s86\n" \
  "s_memrealtime s[74:75]\n" \
  "s_memtime s[76:77]\n" \
  "s_waitcnt lgkmcnt(0)\n" \
  "s_sub_u32 s71, s76, s96\n" \
  "s_sub_u32 s72, s74, s95\n" \
  "s_mov_b32 s95, s74\n" \
  "s_mov_b32 s96, s76\n" \
  "v_cvt_f32_u32 v36, s71\n" \
  "v_cvt_f32_u32 v37, s72\n" \
  "v_rcp_f32 v37, v37\n" \
  "v_mul_f32 v36, 0x43810000, v36\n" \
  "v_mul_f32 v36, v36, v37\n" \
  "v_cvt_u32_f32 v36, v36\n" \
  "v_min_u32 v36, 6400, v36\n" \
  "v_max_u32 v36, 3072, v36\n" \
  "s_nop 0\n" \
  "v_readfirstlane_b32 s90, v36\n" \
  "s_nop 1\n" \
  "v_readfirstlane_b32 s88, v104\n" \
  "s_sub_u32 s71, s88, s74\n" \
  "s_add_u32 s71, s71, 100000\n" \
  "s_cmp_gt_u32 s71, 200000\n" \
  "s_cselect_b32 s88, s74, s88\n" \
  "s_mov_b32 s62, s88\n" \
  "s_lshr_b32 s53, s49, 5\n" \
  "s_add_u32 s53, s53, 1\n" \
  "LHD_%=:\n" \
  "s_memrealtime s[74:75]\n" \
  "s_waitcnt lgkmcnt(0)\n" \
  "s_sub_i32 s71, s88, s74\n" \
  "s_cmp_gt_i32 s71, 75\n" \
  "s_cbranch_scc1 LHD_%=\n" \
  "LOD_%=:\n" \
  "buffer_load_dword v36, v107, s[36:39], 0 offen sc1\n" \
  "s_waitcnt vmcnt(0)\n" \
  "v_cmp_gt_u32 vcc, 16, v36\n" \
  "s_cbranch_vccz LOXD_%=\n" \
  "s_add_u32 s60, s60, 1\n" \
  "s_cmp_gt_u32 s60, 20000\n" \
  "s_cbranch_scc1 LOXD_%=\n" \
  "s_sleep 2\n" \
  "s_branch LOD_%=\n" \
  "LOXD_%=:\n" \
  "LHB_%=:\n" \
  "s_barrier\n" \
  "s_branch LPASS_%=\n" \
  "LFIN_%=:\n" \
  "s_waitcnt vmcnt(0) lgkmcnt(0)\n" \
  "s_barrier\n" \
  "s_and_b32 s71, s48, 3\n" \
  "s_cmp_eq_u32 s71, 3\n" \
  "s_cbranch_scc0 LEND_%=\n" \
  "v_mov_b32 v34, 64\n" \
  "buffer_store_dword v34, v1, s[36:39], 0 offen sc1\n" \
  "LEND_%=:\n" \
  "s_mov_b32 s57, s35\n"
#define PROJQ_CLOBBERS \
  "v9", "v10", "v11", "v12", "v13", "v14", "v15", "v16", "v17", "v18", "v19", "v20", "v21", "v22", "v23", "v24", "v25", "v26", "v27", "v28", "v29", \
  "v30", "v31", "v32", "v33", "v34", "v35", "v36", "v37", "v38", "v39", "v40", "v41", "v42", "v43", "v44", "v45", "v46", "v47", "v48", "v49", "v50", \
  "v51", "v52", "v53", "v54", "v55", "v56", "v57", "v58", "v59", "v60", "v61", "v62", "v63", "v64", "v65", "v66", "v67", "v68", "v69", "v70", "v71", \
  "v72", "v73", "v74", "v75", "v76", "v77", "v78", "v79", "v80", "v81", "v82", "v83", "v84", "v85", "v86", "v87", "v88", "v89", "v90", "v91", "v92", \
  "v93", "v94", "v95", "v100", "v101", "v102", "v103", "v104", "v105", "v106", "v107", "v108", "v109", "v110", "v111", "v112", "v113", "v114", "v115", \
  "v116", "v117", "v118", "v119", "v120", "v121", "v122", "v123", "v144", "v145", "v146", "v147", "v148", "v149", "v150", "v151", "v152", "v153", \
  "v154", "v155", "v156", "v157", "v158", "v159", "v160", "v161", "v162", "v163", "v164", "v165", "v166", "v167", "v168", "v169", "v170", "v171", \
  "v172", "v173", "v174", "v175", "v176", "v177", "v178", "v179", "v180", "v181", "v182", "v183", "v184", "v185", "v186", "v187", "v188", "v189", \
  "v190", "v191", "v192", "v193", "v194", "v195", "v196", "v197", "v198", "v199", "v200", "v201", "v202", "v203", "v204", "v205", "v206", "v207", \
  "v208", "v209", "v210", "v211", "v212", "v213", "v214", "v215", "v216", "v217", "v218", "v219", "v220", "v221", "v222", "v223", "v224", "v225", \
  "v226", "v227", "v228", "v229", "v230", "v231", "v232", "v233", "v234", "v235", "v236", "v237", "v238", "v239", "v240", "v241", "v242", "v243", \
  "v244", "v245", "v246", "v247", "v248", "v249", "v250", "v251", "s52", "s56", "s57", "s58", "s59", "s60", "s61", "s62", "s63", "s64", "s65", "s68", \
  "s69", "s70", "s71", "s72", "s73"
#define PROJQ_ASM \
  "s_waitcnt vmcnt(0)\n" \
  "s_barrier\n" \
  "ds_read_b128 v[32:35], v0 offset:0\n" \
  "ds_read_b128 v[36:39], v0 offset:20480\n" \
  "ds_read_b128 v[40:43], v0 offset:1024\n" \
  "ds_read_b128 v[44:47], v0 offset:21504\n" \
  "v_mov_b32 v12, 2\n" \
  "v_add_u32 v9, 40960, v0\n" \
  "v_add_u32 v10, 81920, v0\n" \
  "s_mov_b32 s68, 81920\n" \
  "s_mov_b32 s52, 0\n" \
  "s_mov_b32 s65, 0\n" \
  "LL_%=:\n" \
  "s_waitcnt lgkmcnt(0)\n" \
  "v_mfma_f32_16x16x32_bf16 v[56:59], a[0:3], v[32:35], 0\n" \
  "s_add_u32 s63, s52, s54\n" \
  "ds_read_b128 v[48:51], v0 offset:2048\n" \
  "v_mfma_f32_16x16x32_bf16 v[68:71], a[0:3], v[36:39], 0\n" \
  "s_sub_u32 s64, s63, 2\n" \
  "ds_read_b128 v[52:55], v0 offset:22528\n" \
  "v_mfma_f32_16x16x32_bf16 v[60:63], a[80:83], v[32:35], 0\n" \
  "v_cvt_pk_bf16_f32 v16, v100, v101\n" \
  "v_cvt_pk_bf16_f32 v17, v102, v103\n" \
  "v_mfma_f32_16x16x32_bf16 v[72:75], a[80:83], v[36:39], 0\n" \
  "v_cvt_pk_bf16_f32 v18, v104, v105\n" \
  "v_cvt_pk_bf16_f32 v19, v106, v107\n" \
  "v_mfma_f32_16x16x32_bf16 v[64:67], a[160:163], v[32:35], 0\n" \
  "v_cvt_pk_bf16_f32 v20, v112, v113\n" \
  "v_cvt_pk_bf16_f32 v21, v114, v115\n" \
  "v_mfma_f32_16x16x32_bf16 v[76:79], a[160:163], v[36:39], 0\n" \
  "v_cvt_pk_bf16_f32 v22, v116, v117\n" \
  "v_cvt_pk_bf16_f32 v23, v118, v119\n" \
  "v_mfma_f32_16x16x32_bf16 v[56:59], a[4:7], v[40:43], v[56:59]\n" \
  "ds_read_b128 v[32:35], v0 offset:3072\n" \
  "v_mfma_f32_16x16x32_bf16 v[68:71], a[4:7], v[44:47], v[68:71]\n" \
  "s_barrier\n" \
  "ds_read_b128 v[36:39], v0 offset:23552\n" \
  "v_mfma_f32_16x16x32_bf16 v[60:63], a[84:87], v[40:43], v[60:63]\n" \
  "v_cvt_pk_bf16_f32 v24, v108, v109\n" \
  "v_cvt_pk_bf16_f32 v25, v110, v111\n" \
  "v_mfma_f32_16x16x32_bf16 v[72:75], a[84:87], v[44:47], v[72:75]\n" \
  "v_cvt_pk_bf16_f32 v26, v120, v121\n" \
  "v_cvt_pk_bf16_f32 v27, v122, v123\n" \
  "v_mfma_f32_16x16x32_bf16 v[64:67], a[164:167], v[40:43], v[64:67]\n" \
  "ds_write_b128 v2, v[16:19] offset:12288\n" \
  "v_mfma_f32_16x16x32_bf16 v[76:79], a[164:167], v[44:47], v[76:79]\n" \
  "s_waitcnt lgkmcnt(4)\n" \
  "v_mfma_f32_16x16x32_bf16 v[56:59], a[8:11], v[48:51], v[56:59]\n" \
  "s_waitcnt lgkmcnt(3)\n" \
  "ds_read_b128 v[40:43], v0 offset:4096\n" \
  "v_mfma_f32_16x16x32_bf16 v[68:71], a[8:11], v[52:55], v[68:71]\n" \
  "ds_read_b128 v[44:47], v0 offset:24576\n" \
  "v_mfma_f32_16x16x32_bf16 v[60:63], a[88:91], v[48:51], v[60:63]\n" \
  "ds_write_b128 v2, v[20:23] offset:13312\n" \
  "v_mfma_f32_16x16x32_bf16 v[72:75], a[88:91], v[52:55], v[72:75]\n" \
  "v_mfma_f32_16x16x32_bf16 v[64:67], a[168:171], v[48:51], v[64:67]\n" \
  "v_mfma_f32_16x16x32_bf16 v[76:79], a[168:171], v[52:55], v[76:79]\n" \
  "s_add_u32 s70, s68, 4096\n" \
  "s_add_u32 s71, s68, 8192\n" \
  "s_waitcnt lgkmcnt(5)\n" \
  "v_mfma_f32_16x16x32_bf16 v[56:59], a[12:15], v[32:35], v[56:59]\n" \
  "s_waitcnt lgkmcnt(4)\n" \
  "ds_write_b128 v2, v[24:27] offset:14336\n" \
  "ds_read_b128 v[48:51], v0 offset:5120\n" \
  "v_mfma_f32_16x16x32_bf16 v[68:71], a[12:15], v[36:39], v[68:71]\n" \
  "s_add_u32 m0, s53, 81920\n" \
  "ds_read_b128 v[52:55], v0 offset:25600\n" \
  "v_mfma_f32_16x16x32_bf16 v[60:63], a[92:95], v[32:35], v[60:63]\n" \
  "buffer_load_dwordx4 v1, s[36:39], s68 offen sc1 lds\n" \
  "v_mfma_f32_16x16x32_bf16 v[72:75], a[92:95], v[36:39], v[72:75]\n" \
  "v_mfma_f32_16x16x32_bf16 v[64:67], a[172:175], v[32:35], v[64:67]\n" \
  "v_mfma_f32_16x16x32_bf16 v[76:79], a[172:175], v[36:39], v[76:79]\n" \
  "s_waitcnt lgkmcnt(5)\n" \
  "v_mfma_f32_16x16x32_bf16 v[56:59], a[16:19], v[40:43], v[56:59]\n" \
  "s_waitcnt lgkmcnt(4)\n" \
  "ds_read_b128 v[32:35], v0 offset:6144\n" \
  "v_mfma_f32_16x16x32_bf16 v[68:71], a[16:19], v[44:47], v[68:71]\n" \
  "ds_read_b128 v[36:39], v0 offset:26624\n" \
  "v_mfma_f32_16x16x32_bf16 v[60:63], a[96:99], v[40:43], v[60:63]\n" \
  "buffer_load_dwordx4 v1, s[36:39], s68 offen offset:1024 sc1 lds\n" \
  "v_mfma_f32_16x16x32_bf16 v[72:75], a[96:99], v[44:47], v[72:75]\n" \
  "v_mfma_f32_16x16x32_bf16 v[64:67], a[176:179], v[40:43], v[64:67]\n" \
  "v_readlane_b32 s61, v126, s64\n" \
  "v_mfma_f32_16x16x32_bf16 v[76:79], a[176:179], v[44:47], v[76:79]\n" \
  "s_sub_u32 s64, s52, 2\n" \
  "s_cmp_lt_u32 s64, 56\n" \
  "s_waitcnt lgkmcnt(3)\n" \
  "v_mfma_f32_16x16x32_bf16 v[56:59], a[20:23], v[48:51], v[56:59]\n" \
  "s_cselect_b32 s61, s61, 1073741824\n" \
  "s_waitcnt lgkmcnt(2)\n" \
  "ds_read_b128 v[40:43], v0 offset:7168\n" \
  "v_mfma_f32_16x16x32_bf16 v[68:71], a[20:23], v[52:55], v[68:71]\n" \
  "ds_read_b128 v[44:47], v0 offset:27648\n" \
  "v_mfma_f32_16x16x32_bf16 v[60:63], a[100:103], v[48:51], v[60:63]\n" \
  "buffer_load_dwordx4 v1, s[36:39], s68 offen offset:2048 sc1 lds\n" \
  "v_mfma_f32_16x16x32_bf16 v[72:75], a[100:103], v[52:55], v[72:75]\n" \
  "s_cmp_ge_u32 s52, 4\n" \
  "s_cselect_b32 s62, 0, 1073741824\n" \
  "v_mfma_f32_16x16x32_bf16 v[64:67], a[180:183], v[48:51], v[64:67]\n" \
  "s_waitcnt vmcnt(3) lgkmcnt(4)\n" \
  "s_barrier\n" \
  "v_mfma_f32_16x16x32_bf16 v[76:79], a[180:183], v[52:55], v[76:79]\n" \
  "buffer_atomic_add v12, v129, s[40:43], s62 offen\n" \
  "ds_read_b128 v[216:219], v3 offset:0\n" \
  "s_waitcnt lgkmcnt(4)\n" \
  "v_mfma_f32_16x16x32_bf16 v[56:59], a[24:27], v[32:35], v[56:59]\n" \
  "s_waitcnt lgkmcnt(3)\n" \
  "ds_read_b128 v[48:51], v0 offset:8192\n" \
  "ds_read_b128 v[220:223], v3 offset:3072\n" \
  "v_mfma_f32_16x16x32_bf16 v[68:71], a[24:27], v[36:39], v[68:71]\n" \
  "ds_read_b128 v[52:55], v0 offset:28672\n" \
  "ds_read_b128 v[224:227], v3 offset:6144\n" \
  "v_mfma_f32_16x16x32_bf16 v[60:63], a[104:107], v[32:35], v[60:63]\n" \
  "buffer_load_dwordx4 v1, s[36:39], s68 offen offset:3072 sc1 lds\n" \
  "ds_read_b128 v[228:231], v3 offset:9216\n" \
  "v_mfma_f32_16x16x32_bf16 v[72:75], a[104:107], v[36:39], v[72:75]\n" \
  "ds_read_b64 v[232:233], v4 offset:0\n" \
  "ds_read_b64 v[234:235], v4 offset:3072\n" \
  "v_mfma_f32_16x16x32_bf16 v[64:67], a[184:187], v[32:35], v[64:67]\n" \
  "ds_read_b64 v[236:237], v4 offset:6144\n" \
  "ds_read_b64 v[238:239], v4 offset:9216\n" \
  "v_mfma_f32_16x16x32_bf16 v[76:79], a[184:187], v[36:39], v[76:79]\n" \
  "s_waitcnt lgkmcnt(11)\n" \
  "v_swap_b32 v162, v184\n" \
  "v_mfma_f32_16x16x32_bf16 v[56:59], a[28:31], v[40:43], v[56:59]\n" \
  "s_waitcnt lgkmcnt(10)\n" \
  "ds_read_b128 v[32:35], v0 offset:9216\n" \
  "v_mfma_f32_16x16x32_bf16 v[68:71], a[28:31], v[44:47], v[68:71]\n" \
  "s_add_u32 m0, s53, 86016\n" \
  "ds_read_b128 v[36:39], v0 offset:29696\n" \
  "v_mfma_f32_16x16x32_bf16 v[60:63], a[108:111], v[40:43], v[60:63]\n" \
  "buffer_load_dwordx4 v1, s[36:39], s70 offen sc1 lds\n" \
  "v_mfma_f32_16x16x32_bf16 v[72:75], a[108:111], v[44:47], v[72:75]\n" \
  "v_swap_b32 v163, v185\n" \
  "v_mfma_f32_16x16x32_bf16 v[64:67], a[188:191], v[40:43], v[64:67]\n" \
  "v_swap_b32 v166, v188\n" \
  "v_mfma_f32_16x16x32_bf16 v[76:79], a[188:191], v[44:47], v[76:79]\n" \
  "s_waitcnt lgkmcnt(10)\n" \
  "v_swap_b32 v167, v189\n" \
  "v_mfma_f32_16x16x32_bf16 v[56:59], a[32:35], v[48:51], v[56:59]\n" \
  "s_waitcnt lgkmcnt(8)\n" \
  "ds_read_b128 v[40:43], v0 offset:10240\n" \
  "v_mfma_f32_16x16x32_bf16 v[68:71], a[32:35], v[52:55], v[68:71]\n" \
  "ds_read_b128 v[44:47], v0 offset:30720\n" \
  "v_mfma_f32_16x16x32_bf16 v[240:243], v[192:195], v[216:219], v[132:135]\n" \
  "v_mfma_f32_16x16x32_bf16 v[244:247], v[144:147], v[216:219], v[136:139]\n" \
  "v_mfma_f32_16x16x32_bf16 v[248:251], v[168:171], v[216:219], v[140:143]\n" \
  "v_mfma_f32_16x16x32_bf16 v[60:63], a[112:115], v[48:51], v[60:63]\n" \
  "buffer_load_dwordx4 v1, s[36:39], s70 offen offset:1024 sc1 lds\n" \
  "v_mfma_f32_16x16x32_bf16 v[240:243], v[196:199], v[220:223], v[240:243]\n" \
  "v_mfma_f32_16x16x32_bf16 v[244:247], v[148:151], v[220:223], v[244:247]\n" \
  "v_mfma_f32_16x16x32_bf16 v[248:251], v[172:175], v[220:223], v[248:251]\n" \
  "v_mfma_f32_16x16x32_bf16 v[72:75], a[112:115], v[52:55], v[72:75]\n" \
  "s_waitcnt lgkmcnt(9)\n" \
  "v_mfma_f32_16x16x32_bf16 v[240:243], v[200:203], v[224:227], v[240:243]\n" \
  "v_mfma_f32_16x16x32_bf16 v[244:247], v[152:155], v[224:227], v[244:247]\n" \
  "v_mfma_f32_16x16x32_bf16 v[248:251], v[176:179], v[224:227], v[248:251]\n" \
  "v_mfma_f32_16x16x32_bf16 v[64:67], a[192:195], v[48:51], v[64:67]\n" \
  "s_waitcnt lgkmcnt(8)\n" \
  "v_mfma_f32_16x16x32_bf16 v[240:243], v[204:207], v[228:231], v[240:243]\n" \
  "v_mfma_f32_16x16x32_bf16 v[244:247], v[156:159], v[228:231], v[244:247]\n" \
  "v_mfma_f32_16x16x32_bf16 v[248:251], v[180:183], v[228:231], v[248:251]\n" \
  "v_mfma_f32_16x16x32_bf16 v[76:79], a[192:195], v[52:55], v[76:79]\n" \
  "s_waitcnt lgkmcnt(6)\n" \
  "v_mfma_f32_16x16x32_bf16 v[240:243], v[208:211], v[232:235], v[240:243]\n" \
  "v_mfma_f32_16x16x32_bf16 v[244:247], v[160:163], v[232:235], v[244:247]\n" \
  "v_mfma_f32_16x16x32_bf16 v[248:251], v[184:187], v[232:235], v[248:251]\n" \
  "s_waitcnt lgkmcnt(3)\n" \
  "v_mfma_f32_16x16x32_bf16 v[56:59], a[36:39], v[32:35], v[56:59]\n" \
  "ds_read_b128 v[48:51], v0 offset:11264\n" \
  "v_mfma_f32_16x16x32_bf16 v[240:243], v[212:215], v[236:239], v[240:243]\n" \
  "v_mfma_f32_16x16x32_bf16 v[244:247], v[164:167], v[236:239], v[244:247]\n" \
  "v_mfma_f32_16x16x32_bf16 v[248:251], v[188:191], v[236:239], v[248:251]\n" \
  "s_waitcnt lgkmcnt(3)\n" \
  "v_mfma_f32_16x16x32_bf16 v[68:71], a[36:39], v[36:39], v[68:71]\n" \
  "ds_read_b128 v[52:55], v0 offset:31744\n" \
  "v_mfma_f32_16x16x32_bf16 v[60:63], a[116:119], v[32:35], v[60:63]\n" \
  "buffer_load_dwordx4 v1, s[36:39], s70 offen offset:2048 sc1 lds\n" \
  "v_mfma_f32_16x16x32_bf16 v[72:75], a[116:119], v[36:39], v[72:75]\n" \
  "v_cvt_pk_bf16_f32 v80, v240, v241\n" \
  "v_cvt_pk_bf16_f32 v81, v242, v243\n" \
  "v_mfma_f32_16x16x32_bf16 v[64:67], a[196:199], v[32:35], v[64:67]\n" \
  "v_cvt_pk_bf16_f32 v82, v244, v245\n" \
  "v_cvt_pk_bf16_f32 v83, v246, v247\n" \
  "v_mfma_f32_16x16x32_bf16 v[76:79], a[196:199], v[36:39], v[76:79]\n" \
  "s_waitcnt lgkmcnt(3)\n" \
  "v_cvt_pk_bf16_f32 v84, v248, v249\n" \
  "v_cvt_pk_bf16_f32 v85, v250, v251\n" \
  "v_mfma_f32_16x16x32_bf16 v[56:59], a[40:43], v[40:43], v[56:59]\n" \
  "s_waitcnt lgkmcnt(2)\n" \
  "ds_read_b128 v[32:35], v0 offset:12288\n" \
  "v_lshlrev_b32 v240, 16, v80\n" \
  "v_mfma_f32_16x16x32_bf16 v[68:71], a[40:43], v[44:47], v[68:71]\n" \
  "ds_read_b128 v[36:39], v0 offset:32768\n" \
  "v_and_b32 v241, 0xffff0000, v80\n" \
  "v_mfma_f32_16x16x32_bf16 v[60:63], a[120:123], v[40:43], v[60:63]\n" \
  "buffer_load_dwordx4 v1, s[36:39], s70 offen offset:3072 sc1 lds\n" \
  "v_mul_f32 v240, 0x3d93cd3a, v240\n" \
  "v_mfma_f32_16x16x32_bf16 v[72:75], a[120:123], v[44:47], v[72:75]\n" \
  "v_mul_f32 v241, 0x3d93cd3a, v241\n" \
  "v_cvt_pk_bf16_f32 v80, v240, v241\n" \
  "v_mfma_f32_16x16x32_bf16 v[64:67], a[200:203], v[40:43], v[64:67]\n" \
  "v_lshlrev_b32 v240, 16, v80\n" \
  "v_and_b32 v241, 0xffff0000, v80\n" \
  "v_mfma_f32_16x16x32_bf16 v[76:79], a[200:203], v[44:47], v[76:79]\n" \
  "s_waitcnt lgkmcnt(3)\n" \
  "v_lshlrev_b32 v242, 16, v81\n" \
  "v_and_b32 v243, 0xffff0000, v81\n" \
  "v_mfma_f32_16x16x32_bf16 v[56:59], a[44:47], v[48:51], v[56:59]\n" \
  "s_waitcnt lgkmcnt(2)\n" \
  "ds_read_b128 v[40:43], v0 offset:13312\n" \
  "v_mul_f32 v242, 0x3d93cd3a, v242\n" \
  "v_mfma_f32_16x16x32_bf16 v[68:71], a[44:47], v[52:55], v[68:71]\n" \
  "s_add_u32 m0, s53, 90112\n" \
  "ds_read_b128 v[44:47], v0 offset:33792\n" \
  "v_mul_f32 v243, 0x3d93cd3a, v243\n" \
  "v_mfma_f32_16x16x32_bf16 v[60:63], a[124:127], v[48:51], v[60:63]\n" \
  "buffer_load_dwordx4 v1, s[36:39], s71 offen sc1 lds\n" \
  "v_cvt_pk_bf16_f32 v81, v242, v243\n" \
  "v_mfma_f32_16x16x32_bf16 v[72:75], a[124:127], v[52:55], v[72:75]\n" \
  "v_lshlrev_b32 v242, 16, v81\n" \
  "v_and_b32 v243, 0xffff0000, v81\n" \
  "v_mfma_f32_16x16x32_bf16 v[64:67], a[204:207], v[48:51], v[64:67]\n" \
  "v_readlane_b32 s56, v124, s63\n" \
  "v_mfma_f32_16x16x32_bf16 v[76:79], a[204:207], v[52:55], v[76:79]\n" \
  "s_waitcnt lgkmcnt(3)\n" \
  "v_lshlrev_b32 v244, 16, v82\n" \
  "v_and_b32 v245, 0xffff0000, v82\n" \
  "v_mfma_f32_16x16x32_bf16 v[56:59], a[48:51], v[32:35], v[56:59]\n" \
  "s_waitcnt lgkmcnt(2)\n" \
  "ds_read_b128 v[48:51], v0 offset:14336\n" \
  "v_mul_f32 v244, 0x3d93cd3a, v244\n" \
  "v_mfma_f32_16x16x32_bf16 v[68:71], a[48:51], v[36:39], v[68:71]\n" \
  "ds_read_b128 v[52:55], v0 offset:34816\n" \
  "v_mul_f32 v245, 0x3d93cd3a, v245\n" \
  "v_mfma_f32_16x16x32_bf16 v[60:63], a[128:131], v[32:35], v[60:63]\n" \
  "buffer_load_dwordx4 v1, s[36:39], s71 offen offset:1024 sc1 lds\n" \
  "v_cvt_pk_bf16_f32 v82, v244, v245\n" \
  "v_mfma_f32_16x16x32_bf16 v[72:75], a[128:131], v[36:39], v[72:75]\n" \
  "s_add_u32 s57, s56, 16384\n" \
  "v_lshlrev_b32 v244, 16, v82\n" \
  "v_and_b32 v245, 0xffff0000, v82\n" \
  "v_mfma_f32_16x16x32_bf16 v[64:67], a[208:211], v[32:35], v[64:67]\n" \
  "s_add_u32 s58, s56, 32768\n" \
  "v_lshlrev_b32 v246, 16, v83\n" \
  "v_and_b32 v247, 0xffff0000, v83\n" \
  "v_mfma_f32_16x16x32_bf16 v[76:79], a[208:211], v[36:39], v[76:79]\n" \
  "s_add_u32 s68, s68, 40960\n" \
  "s_add_u32 s59, s56, 49152\n" \
  "s_waitcnt lgkmcnt(3)\n" \
  "v_mul_f32 v246, 0x3d93cd3a, v246\n" \
  "v_mfma_f32_16x16x32_bf16 v[56:59], a[52:55], v[40:43], v[56:59]\n" \
  "s_waitcnt lgkmcnt(2)\n" \
  "ds_read_b128 v[32:35], v0 offset:15360\n" \
  "v_mul_f32 v247, 0x3d93cd3a, v247\n" \
  "v_mfma_f32_16x16x32_bf16 v[68:71], a[52:55], v[44:47], v[68:71]\n" \
  "ds_read_b128 v[36:39], v0 offset:35840\n" \
  "v_cvt_pk_bf16_f32 v83, v246, v247\n" \
  "v_mfma_f32_16x16x32_bf16 v[60:63], a[132:135], v[40:43], v[60:63]\n" \
  "v_lshlrev_b32 v246, 16, v83\n" \
  "v_and_b32 v247, 0xffff0000, v83\n" \
  "v_mfma_f32_16x16x32_bf16 v[72:75], a[132:135], v[44:47], v[72:75]\n" \
  "v_lshlrev_b32 v248, 16, v84\n" \
  "v_and_b32 v249, 0xffff0000, v84\n" \
  "v_mfma_f32_16x16x32_bf16 v[64:67], a[212:215], v[40:43], v[64:67]\n" \
  "v_mul_f32 v248, 0x3d93cd3a, v248\n" \
  "v_mul_f32 v249, 0x3d93cd3a, v249\n" \
  "v_mfma_f32_16x16x32_bf16 v[76:79], a[212:215], v[44:47], v[76:79]\n" \
  "buffer_load_dwordx4 v[144:147], v5, s[40:43], s56 offen offset:0 sc1\n" \
  "v_cvt_pk_bf16_f32 v84, v248, v249\n" \
  "s_waitcnt lgkmcnt(3)\n" \
  "v_mfma_f32_16x16x32_bf16 v[56:59], a[56:59], v[48:51], v[56:59]\n" \
  "s_waitcnt lgkmcnt(2)\n" \
  "ds_read_b128 v[40:43], v0 offset:16384\n" \
  "v_lshlrev_b32 v248, 16, v84\n" \
  "v_mfma_f32_16x16x32_bf16 v[68:71], a[56:59], v[52:55], v[68:71]\n" \
  "ds_read_b128 v[44:47], v0 offset:36864\n" \
  "v_and_b32 v249, 0xffff0000, v84\n" \
  "v_mfma_f32_16x16x32_bf16 v[60:63], a[136:139], v[48:51], v[60:63]\n" \
  "v_lshlrev_b32 v250, 16, v85\n" \
  "v_and_b32 v251, 0xffff0000, v85\n" \
  "v_mfma_f32_16x16x32_bf16 v[72:75], a[136:139], v[52:55], v[72:75]\n" \
  "v_mul_f32 v250, 0x3d93cd3a, v250\n" \
  "v_mul_f32 v251, 0x3d93cd3a, v251\n" \
  "v_mfma_f32_16x16x32_bf16 v[64:67], a[216:219], v[48:51], v[64:67]\n" \
  "v_cvt_pk_bf16_f32 v85, v250, v251\n" \
  "v_lshlrev_b32 v250, 16, v85\n" \
  "v_mfma_f32_16x16x32_bf16 v[76:79], a[216:219], v[52:55], v[76:79]\n" \
  "buffer_load_dwordx4 v[168:171], v5, s[40:43], s56 offen offset:1024 sc1\n" \
  "v_and_b32 v251, 0xffff0000, v85\n" \
  "s_waitcnt lgkmcnt(3)\n" \
  "v_mfma_f32_16x16x32_bf16 v[56:59], a[60:63], v[32:35], v[56:59]\n" \
  "s_waitcnt lgkmcnt(2)\n" \
  "ds_read_b128 v[48:51], v0 offset:17408\n" \
  "v_max3_f32 v80, v240, v241, v242\n" \
  "v_mfma_f32_16x16x32_bf16 v[68:71], a[60:63], v[36:39], v[68:71]\n" \
  "ds_read_b128 v[52:55], v0 offset:37888\n" \
  "v_max3_f32 v80, v80, v243, v244\n" \
  "v_mfma_f32_16x16x32_bf16 v[60:63], a[140:143], v[32:35], v[60:63]\n" \
  "v_max3_f32 v80, v80, v245, v246\n" \
  "v_max3_f32 v80, v80, v247, v248\n" \
  "v_mfma_f32_16x16x32_bf16 v[72:75], a[140:143], v[36:39], v[72:75]\n" \
  "v_max3_f32 v80, v80, v249, v250\n" \
  "v_max_f32 v80, v80, v251\n" \
  "v_mfma_f32_16x16x32_bf16 v[64:67], a[220:223], v[32:35], v[64:67]\n" \
  "v_mov_b32 v81, v80\n" \
  "v_mfma_f32_16x16x32_bf16 v[76:79], a[220:223], v[36:39], v[76:79]\n" \
  "buffer_load_dwordx4 v[148:151], v5, s[40:43], s57 offen offset:0 sc1\n" \
  "s_waitcnt lgkmcnt(3)\n" \
  "v_mfma_f32_16x16x32_bf16 v[56:59], a[64:67], v[40:43], v[56:59]\n" \
  "s_waitcnt lgkmcnt(2)\n" \
  "ds_read_b128 v[32:35], v0 offset:18432\n" \
  "v_mfma_f32_16x16x32_bf16 v[68:71], a[64:67], v[44:47], v[68:71]\n" \
  "ds_read_b128 v[36:39], v0 offset:38912\n" \
  "v_mfma_f32_16x16x32_bf16 v[60:63], a[144:147], v[40:43], v[60:63]\n" \
  "v_permlane16_swap_b32 v80, v81\n" \
  "v_mfma_f32_16x16x32_bf16 v[72:75], a[144:147], v[44:47], v[72:75]\n" \
  "v_max_f32 v80, v80, v81\n" \
  "v_mov_b32 v81, v80\n" \
  "v_mfma_f32_16x16x32_bf16 v[64:67], a[224:227], v[40:43], v[64:67]\n" \
  "v_mfma_f32_16x16x32_bf16 v[76:79], a[224:227], v[44:47], v[76:79]\n" \
  "buffer_load_dwordx4 v[172:175], v5, s[40:43], s57 offen offset:1024 sc1\n" \
  "s_waitcnt lgkmcnt(3)\n" \
  "v_mfma_f32_16x16x32_bf16 v[56:59], a[68:71], v[48:51], v[56:59]\n" \
  "s_waitcnt lgkmcnt(2)\n" \
  "ds_read_b128 v[40:43], v0 offset:19456\n" \
  "v_mfma_f32_16x16x32_bf16 v[68:71], a[68:71], v[52:55], v[68:71]\n" \
  "ds_read_b128 v[44:47], v0 offset:39936\n" \
  "v_mfma_f32_16x16x32_bf16 v[60:63], a[148:151], v[48:51], v[60:63]\n" \
  "v_permlane32_swap_b32 v80, v81\n" \
  "v_mfma_f32_16x16x32_bf16 v[72:75], a[148:151], v[52:55], v[72:75]\n" \
  "v_max_f32 v80, v80, v81\n" \
  "v_mul_f32 v80, 0x3fb8aa3b, v80\n" \
  "v_mfma_f32_16x16x32_bf16 v[64:67], a[228:231], v[48:51], v[64:67]\n" \
  "v_mov_b32 v82, 0x3fb8aa3b\n" \
  "v_fma_f32 v240, v240, v82, -v80\n" \
  "v_mfma_f32_16x16x32_bf16 v[76:79], a[228:231], v[52:55], v[76:79]\n" \
  "buffer_load_dwordx4 v[152:155], v5, s[40:43], s58 offen offset:0 sc1\n" \
  "v_fma_f32 v241, v241, v82, -v80\n" \
  "s_waitcnt vmcnt(24)\n" \
  "s_barrier\n" \
  "s_waitcnt lgkmcnt(3)\n" \
  "v_mfma_f32_16x16x32_bf16 v[56:59], a[72:75], v[32:35], v[56:59]\n" \
  "s_waitcnt lgkmcnt(2)\n" \
  "ds_read_b128 v[48:51], v9 offset:0\n" \
  "v_fma_f32 v242, v242, v82, -v80\n" \
  "v_mfma_f32_16x16x32_bf16 v[68:71], a[72:75], v[36:39], v[68:71]\n" \
  "ds_read_b128 v[52:55], v9 offset:20480\n" \
  "v_fma_f32 v243, v243, v82, -v80\n" \
  "v_mfma_f32_16x16x32_bf16 v[60:63], a[152:155], v[32:35], v[60:63]\n" \
  "v_fma_f32 v244, v244, v82, -v80\n" \
  "v_fma_f32 v245, v245, v82, -v80\n" \
  "v_mfma_f32_16x16x32_bf16 v[72:75], a[152:155], v[36:39], v[72:75]\n" \
  "v_fma_f32 v246, v246, v82, -v80\n" \
  "v_fma_f32 v247, v247, v82, -v80\n" \
  "v_mfma_f32_16x16x32_bf16 v[64:67], a[232:235], v[32:35], v[64:67]\n" \
  "v_fma_f32 v248, v248, v82, -v80\n" \
  "v_fma_f32 v249, v249, v82, -v80\n" \
  "v_mfma_f32_16x16x32_bf16 v[76:79], a[232:235], v[36:39], v[76:79]\n" \
  "buffer_load_dwordx4 v[176:179], v5, s[40:43], s58 offen offset:1024 sc1\n" \
  "v_fma_f32 v250, v250, v82, -v80\n" \
  "s_waitcnt lgkmcnt(3)\n" \
  "v_mfma_f32_16x16x32_bf16 v[56:59], a[76:79], v[40:43], v[56:59]\n" \
  "s_waitcnt lgkmcnt(2)\n" \
  "ds_read_b128 v[32:35], v9 offset:1024\n" \
  "v_fma_f32 v251, v251, v82, -v80\n" \
  "v_mfma_f32_16x16x32_bf16 v[68:71], a[76:79], v[44:47], v[68:71]\n" \
  "ds_read_b128 v[36:39], v9 offset:21504\n" \
  "v_mfma_f32_16x16x32_bf16 v[60:63], a[156:159], v[40:43], v[60:63]\n" \
  "v_exp_f32 v240, v240\n" \
  "v_mfma_f32_16x16x32_bf16 v[72:75], a[156:159], v[44:47], v[72:75]\n" \
  "v_exp_f32 v241, v241\n" \
  "v_mfma_f32_16x16x32_bf16 v[64:67], a[236:239], v[40:43], v[64:67]\n" \
  "v_exp_f32 v242, v242\n" \
  "v_mfma_f32_16x16x32_bf16 v[76:79], a[236:239], v[44:47], v[76:79]\n" \
  "v_exp_f32 v243, v243\n" \
  "s_add_u32 s52, s52, 1\n" \
  "s_waitcnt lgkmcnt(3)\n" \
  "v_mfma_f32_16x16x32_bf16 v[100:103], a[0:3], v[48:51], 0\n" \
  "s_waitcnt lgkmcnt(2)\n" \
  "ds_read_b128 v[40:43], v9 offset:2048\n" \
  "v_mfma_f32_16x16x32_bf16 v[112:115], a[0:3], v[52:55], 0\n" \
  "ds_read_b128 v[44:47], v9 offset:22528\n" \
  "v_mfma_f32_16x16x32_bf16 v[104:107], a[80:83], v[48:51], 0\n" \
  "v_cvt_pk_bf16_f32 v16, v56, v57\n" \
  "v_cvt_pk_bf16_f32 v17, v58, v59\n" \
  "v_mfma_f32_16x16x32_bf16 v[116:119], a[80:83], v[52:55], 0\n" \
  "v_cvt_pk_bf16_f32 v18, v60, v61\n" \
  "v_cvt_pk_bf16_f32 v19, v62, v63\n" \
  "v_mfma_f32_16x16x32_bf16 v[108:111], a[160:163], v[48:51], 0\n" \
  "v_cvt_pk_bf16_f32 v20, v68, v69\n" \
  "v_cvt_pk_bf16_f32 v21, v70, v71\n" \
  "v_mfma_f32_16x16x32_bf16 v[120:123], a[160:163], v[52:55], 0\n" \
  "v_cvt_pk_bf16_f32 v22, v72, v73\n" \
  "v_cvt_pk_bf16_f32 v23, v74, v75\n" \
  "buffer_load_dwordx4 v[156:159], v5, s[40:43], s59 offen offset:0 sc1\n" \
  "s_waitcnt lgkmcnt(3)\n" \
  "v_mfma_f32_16x16x32_bf16 v[100:103], a[4:7], v[32:35], v[100:103]\n" \
  "s_waitcnt lgkmcnt(2)\n" \
  "ds_read_b128 v[48:51], v9 offset:3072\n" \
  "v_mfma_f32_16x16x32_bf16 v[112:115], a[4:7], v[36:39], v[112:115]\n" \
  "s_barrier\n" \
  "ds_read_b128 v[52:55], v9 offset:23552\n" \
  "v_mfma_f32_16x16x32_bf16 v[104:107], a[84:87], v[32:35], v[104:107]\n" \
  "v_cvt_pk_bf16_f32 v24, v64, v65\n" \
  "v_cvt_pk_bf16_f32 v25, v66, v67\n" \
  "v_mfma_f32_16x16x32_bf16 v[116:119], a[84:87], v[36:39], v[116:119]\n" \
  "v_cvt_pk_bf16_f32 v26, v76, v77\n" \
  "v_cvt_pk_bf16_f32 v27, v78, v79\n" \
  "v_mfma_f32_16x16x32_bf16 v[108:111], a[164:167], v[32:35], v[108:111]\n" \
  "ds_write_b128 v2, v[16:19] offset:0\n" \
  "v_mfma_f32_16x16x32_bf16 v[120:123], a[164:167], v[36:39], v[120:123]\n" \
  "buffer_load_dwordx4 v[180:183], v5, s[40:43], s59 offen offset:1024 sc1\n" \
  "s_waitcnt lgkmcnt(4)\n" \
  "v_mfma_f32_16x16x32_bf16 v[100:103], a[8:11], v[40:43], v[100:103]\n" \
  "s_waitcnt lgkmcnt(3)\n" \
  "ds_read_b128 v[32:35], v9 offset:4096\n" \
  "v_mfma_f32_16x16x32_bf16 v[112:115], a[8:11], v[44:47], v[112:115]\n" \
  "ds_read_b128 v[36:39], v9 offset:24576\n" \
  "v_mfma_f32_16x16x32_bf16 v[104:107], a[88:91], v[40:43], v[104:107]\n" \
  "ds_write_b128 v2, v[20:23] offset:1024\n" \
  "v_mfma_f32_16x16x32_bf16 v[116:119], a[88:91], v[44:47], v[116:119]\n" \
  "v_exp_f32 v244, v244\n" \
  "v_mfma_f32_16x16x32_bf16 v[108:111], a[168:171], v[40:43], v[108:111]\n" \
  "v_exp_f32 v245, v245\n" \
  "v_mfma_f32_16x16x32_bf16 v[120:123], a[168:171], v[44:47], v[120:123]\n" \
  "s_add_u32 s70, s68, 4096\n" \
  "s_add_u32 s71, s68, 8192\n" \
  "buffer_load_dwordx4 v[160:163], v5, s[40:43], s56 offen offset:2048 sc1\n" \
  "s_waitcnt lgkmcnt(5)\n" \
  "v_mfma_f32_16x16x32_bf16 v[100:103], a[12:15], v[48:51], v[100:103]\n" \
  "s_waitcnt lgkmcnt(4)\n" \
  "ds_write_b128 v2, v[24:27] offset:2048\n" \
  "ds_read_b128 v[40:43], v9 offset:5120\n" \
  "v_mfma_f32_16x16x32_bf16 v[112:115], a[12:15], v[52:55], v[112:115]\n" \
  "s_add_u32 m0, s53, 0\n" \
  "ds_read_b128 v[44:47], v9 offset:25600\n" \
  "v_mfma_f32_16x16x32_bf16 v[104:107], a[92:95], v[48:51], v[104:107]\n" \
  "buffer_load_dwordx4 v1, s[36:39], s68 offen sc1 lds\n" \
  "v_mfma_f32_16x16x32_bf16 v[116:119], a[92:95], v[52:55], v[116:119]\n" \
  "v_exp_f32 v246, v246\n" \
  "v_mfma_f32_16x16x32_bf16 v[108:111], a[172:175], v[48:51], v[108:111]\n" \
  "v_exp_f32 v247, v247\n" \
  "v_mfma_f32_16x16x32_bf16 v[120:123], a[172:175], v[52:55], v[120:123]\n" \
  "buffer_load_dwordx4 v[184:187], v5, s[40:43], s57 offen offset:2048 sc1\n" \
  "s_waitcnt lgkmcnt(5)\n" \
  "v_mfma_f32_16x16x32_bf16 v[100:103], a[16:19], v[32:35], v[100:103]\n" \
  "s_waitcnt lgkmcnt(4)\n" \
  "ds_read_b128 v[48:51], v9 offset:6144\n" \
  "v_mfma_f32_16x16x32_bf16 v[112:115], a[16:19], v[36:39], v[112:115]\n" \
  "ds_read_b128 v[52:55], v9 offset:26624\n" \
  "v_mfma_f32_16x16x32_bf16 v[104:107], a[96:99], v[32:35], v[104:107]\n" \
  "buffer_load_dwordx4 v1, s[36:39], s68 offen offset:1024 sc1 lds\n" \
  "v_mfma_f32_16x16x32_bf16 v[116:119], a[96:99], v[36:39], v[116:119]\n" \
  "v_exp_f32 v248, v248\n" \
  "v_mfma_f32_16x16x32_bf16 v[108:111], a[176:179], v[32:35], v[108:111]\n" \
  "v_exp_f32 v249, v249\n" \
  "v_mfma_f32_16x16x32_bf16 v[120:123], a[176:179], v[36:39], v[120:123]\n" \
  "buffer_load_dwordx4 v[164:167], v5, s[40:43], s58 offen offset:2048 sc1\n" \
  "s_waitcnt lgkmcnt(3)\n" \
  "v_mfma_f32_16x16x32_bf16 v[100:103], a[20:23], v[40:43], v[100:103]\n" \
  "s_waitcnt lgkmcnt(2)\n" \
  "ds_read_b128 v[32:35], v9 offset:7168\n" \
  "v_mfma_f32_16x16x32_bf16 v[112:115], a[20:23], v[44:47], v[112:115]\n" \
  "ds_read_b128 v[36:39], v9 offset:27648\n" \
  "v_mfma_f32_16x16x32_bf16 v[104:107], a[100:103], v[40:43], v[104:107]\n" \
  "buffer_load_dwordx4 v1, s[36:39], s68 offen offset:2048 sc1 lds\n" \
  "v_mfma_f32_16x16x32_bf16 v[116:119], a[100:103], v[44:47], v[116:119]\n" \
  "v_exp_f32 v250, v250\n" \
  "v_mfma_f32_16x16x32_bf16 v[108:111], a[180:183], v[40:43], v[108:111]\n" \
  "v_exp_f32 v251, v251\n" \
  "v_mfma_f32_16x16x32_bf16 v[120:123], a[180:183], v[44:47], v[120:123]\n" \
  "buffer_load_dwordx4 v[188:191], v5, s[40:43], s59 offen offset:2048 sc1\n" \
  "v_add_f32 v80, v240, v241\n" \
  "s_waitcnt lgkmcnt(3)\n" \
  "v_mfma_f32_16x16x32_bf16 v[100:103], a[24:27], v[48:51], v[100:103]\n" \
  "s_waitcnt lgkmcnt(2)\n" \
  "ds_read_b128 v[40:43], v9 offset:8192\n" \
  "v_add_f32 v80, v80, v242\n" \
  "v_mfma_f32_16x16x32_bf16 v[112:115], a[24:27], v[52:55], v[112:115]\n" \
  "ds_read_b128 v[44:47], v9 offset:28672\n" \
  "v_add_f32 v80, v80, v243\n" \
  "v_mfma_f32_16x16x32_bf16 v[104:107], a[104:107], v[48:51], v[104:107]\n" \
  "buffer_load_dwordx4 v1, s[36:39], s68 offen offset:3072 sc1 lds\n" \
  "v_add_f32 v80, v80, v244\n" \
  "v_mfma_f32_16x16x32_bf16 v[116:119], a[104:107], v[52:55], v[116:119]\n" \
  "v_add_f32 v80, v80, v245\n" \
  "v_add_f32 v80, v80, v246\n" \
  "v_mfma_f32_16x16x32_bf16 v[108:111], a[184:187], v[48:51], v[108:111]\n" \
  "v_add_f32 v80, v80, v247\n" \
  "v_add_f32 v80, v80, v248\n" \
  "v_mfma_f32_16x16x32_bf16 v[120:123], a[184:187], v[52:55], v[120:123]\n" \
  "buffer_load_dword v11, v8, s[40:43], 0 offen sc1\n" \
  "v_add_f32 v80, v80, v249\n" \
  "s_waitcnt lgkmcnt(3)\n" \
  "v_mfma_f32_16x16x32_bf16 v[100:103], a[28:31], v[32:35], v[100:103]\n" \
  "s_waitcnt lgkmcnt(2)\n" \
  "ds_read_b128 v[48:51], v9 offset:9216\n" \
  "v_add_f32 v80, v80, v250\n" \
  "v_mfma_f32_16x16x32_bf16 v[112:115], a[28:31], v[36:39], v[112:115]\n" \
  "s_add_u32 m0, s53, 4096\n" \
  "ds_read_b128 v[52:55], v9 offset:29696\n" \
  "v_add_f32 v80, v80, v251\n" \
  "v_mfma_f32_16x16x32_bf16 v[104:107], a[108:111], v[32:35], v[104:107]\n" \
  "buffer_load_dwordx4 v1, s[36:39], s70 offen sc1 lds\n" \
  "v_mov_b32 v81, v80\n" \
  "v_mfma_f32_16x16x32_bf16 v[116:119], a[108:111], v[36:39], v[116:119]\n" \
  "v_mfma_f32_16x16x32_bf16 v[108:111], a[188:191], v[32:35], v[108:111]\n" \
  "v_readlane_b32 s60, v125, s63\n" \
  "v_mfma_f32_16x16x32_bf16 v[120:123], a[188:191], v[36:39], v[120:123]\n" \
  "s_waitcnt lgkmcnt(3)\n" \
  "v_permlane16_swap_b32 v80, v81\n" \
  "v_mfma_f32_16x16x32_bf16 v[100:103], a[32:35], v[40:43], v[100:103]\n" \
  "s_waitcnt lgkmcnt(2)\n" \
  "ds_read_b128 v[32:35], v9 offset:10240\n" \
  "v_add_f32 v80, v80, v81\n" \
  "v_mfma_f32_16x16x32_bf16 v[112:115], a[32:35], v[44:47], v[112:115]\n" \
  "ds_read_b128 v[36:39], v9 offset:30720\n" \
  "v_mov_b32 v81, v80\n" \
  "v_mfma_f32_16x16x32_bf16 v[104:107], a[112:115], v[40:43], v[104:107]\n" \
  "buffer_load_dwordx4 v1, s[36:39], s70 offen offset:1024 sc1 lds\n" \
  "v_mfma_f32_16x16x32_bf16 v[116:119], a[112:115], v[44:47], v[116:119]\n" \
  "v_permlane32_swap_b32 v80, v81\n" \
  "v_mfma_f32_16x16x32_bf16 v[108:111], a[192:195], v[40:43], v[108:111]\n" \
  "s_add_u32 s64, s52, 2\n" \
  "s_min_u32 s64, s64, 57\n" \
  "v_add_f32 v80, v80, v81\n" \
  "v_mfma_f32_16x16x32_bf16 v[120:123], a[192:195], v[44:47], v[120:123]\n" \
  "s_waitcnt lgkmcnt(3)\n" \
  "v_rcp_f32 v80, v80\n" \
  "v_mfma_f32_16x16x32_bf16 v[100:103], a[36:39], v[48:51], v[100:103]\n" \
  "s_waitcnt lgkmcnt(2)\n" \
  "ds_read_b128 v[40:43], v9 offset:11264\n" \
  "v_mul_f32 v244, v244, v80\n" \
  "v_mfma_f32_16x16x32_bf16 v[112:115], a[36:39], v[52:55], v[112:115]\n" \
  "ds_read_b128 v[44:47], v9 offset:31744\n" \
  "v_mul_f32 v245, v245, v80\n" \
  "v_mfma_f32_16x16x32_bf16 v[104:107], a[116:119], v[48:51], v[104:107]\n" \
  "buffer_load_dwordx4 v1, s[36:39], s70 offen offset:2048 sc1 lds\n" \
  "v_mul_f32 v246, v246, v80\n" \
  "v_mfma_f32_16x16x32_bf16 v[116:119], a[116:119], v[52:55], v[116:119]\n" \
  "v_mul_f32 v247, v247, v80\n" \
  "v_mul_f32 v248, v248, v80\n" \
  "v_mfma_f32_16x16x32_bf16 v[108:111], a[196:199], v[48:51], v[108:111]\n" \
  "v_mul_f32 v249, v249, v80\n" \
  "v_mul_f32 v250, v250, v80\n" \
  "v_mfma_f32_16x16x32_bf16 v[120:123], a[196:199], v[52:55], v[120:123]\n" \
  "s_waitcnt lgkmcnt(3)\n" \
  "v_mul_f32 v251, v251, v80\n" \
  "v_mul_f32 v240, v240, v80\n" \
  "v_mfma_f32_16x16x32_bf16 v[100:103], a[40:43], v[32:35], v[100:103]\n" \
  "s_waitcnt lgkmcnt(2)\n" \
  "ds_read_b128 v[48:51], v9 offset:12288\n" \
  "v_mul_f32 v241, v241, v80\n" \
  "v_mfma_f32_16x16x32_bf16 v[112:115], a[40:43], v[36:39], v[112:115]\n" \
  "ds_read_b128 v[52:55], v9 offset:32768\n" \
  "v_mul_f32 v242, v242, v80\n" \
  "v_mfma_f32_16x16x32_bf16 v[104:107], a[120:123], v[32:35], v[104:107]\n" \
  "buffer_load_dwordx4 v1, s[36:39], s70 offen offset:3072 sc1 lds\n" \
  "v_mul_f32 v243, v243, v80\n" \
  "v_mfma_f32_16x16x32_bf16 v[116:119], a[120:123], v[36:39], v[116:119]\n" \
  "v_cvt_pk_bf16_f32 v88, v244, v245\n" \
  "v_cvt_pk_bf16_f32 v89, v246, v247\n" \
  "v_mfma_f32_16x16x32_bf16 v[108:111], a[200:203], v[32:35], v[108:111]\n" \
  "v_cvt_pk_bf16_f32 v90, v248, v249\n" \
  "v_cvt_pk_bf16_f32 v91, v250, v251\n" \
  "v_mfma_f32_16x16x32_bf16 v[120:123], a[200:203], v[36:39], v[120:123]\n" \
  "s_waitcnt lgkmcnt(3)\n" \
  "v_cvt_pk_bf16_f32 v92, v240, v241\n" \
  "v_cvt_pk_bf16_f32 v93, v242, v243\n" \
  "v_mfma_f32_16x16x32_bf16 v[100:103], a[44:47], v[40:43], v[100:103]\n" \
  "ds_read_b128 v[32:35], v9 offset:13312\n" \
  "buffer_store_dwordx4 v[88:91], v127, s[40:43], s61 offen sc1\n" \
  "s_waitcnt lgkmcnt(3)\n" \
  "v_mfma_f32_16x16x32_bf16 v[112:115], a[44:47], v[44:47], v[112:115]\n" \
  "s_add_u32 m0, s53, 8192\n" \
  "ds_read_b128 v[36:39], v9 offset:33792\n" \
  "buffer_store_dwordx2 v[92:93], v128, s[40:43], s61 offen sc1\n" \
  "v_mfma_f32_16x16x32_bf16 v[104:107], a[124:127], v[40:43], v[104:107]\n" \
  "buffer_load_dwordx4 v1, s[36:39], s71 offen sc1 lds\n" \
  "v_mfma_f32_16x16x32_bf16 v[116:119], a[124:127], v[44:47], v[116:119]\n" \
  "v_mfma_f32_16x16x32_bf16 v[108:111], a[204:207], v[40:43], v[108:111]\n" \
  "v_mfma_f32_16x16x32_bf16 v[120:123], a[204:207], v[44:47], v[120:123]\n" \
  "s_waitcnt lgkmcnt(3)\n" \
  "v_mfma_f32_16x16x32_bf16 v[100:103], a[48:51], v[48:51], v[100:103]\n" \
  "s_waitcnt lgkmcnt(2)\n" \
  "ds_read_b128 v[40:43], v9 offset:14336\n" \
  "v_mfma_f32_16x16x32_bf16 v[112:115], a[48:51], v[52:55], v[112:115]\n" \
  "ds_read_b128 v[44:47], v9 offset:34816\n" \
  "v_mfma_f32_16x16x32_bf16 v[104:107], a[128:131], v[48:51], v[104:107]\n" \
  "buffer_load_dwordx4 v1, s[36:39], s71 offen offset:1024 sc1 lds\n" \
  "v_mfma_f32_16x16x32_bf16 v[116:119], a[128:131], v[52:55], v[116:119]\n" \
  "v_mfma_f32_16x16x32_bf16 v[108:111], a[208:211], v[48:51], v[108:111]\n" \
  "s_waitcnt vmcnt(8)\n" \
  "v_cmp_gt_u32 vcc, s64, v11\n" \
  "v_mfma_f32_16x16x32_bf16 v[120:123], a[208:211], v[52:55], v[120:123]\n" \
  "s_add_u32 s68, s68, 40960\n" \
  "s_waitcnt lgkmcnt(3)\n" \
  "v_mfma_f32_16x16x32_bf16 v[100:103], a[52:55], v[32:35], v[100:103]\n" \
  "s_waitcnt lgkmcnt(2)\n" \
  "ds_read_b128 v[48:51], v9 offset:15360\n" \
  "v_mfma_f32_16x16x32_bf16 v[112:115], a[52:55], v[36:39], v[112:115]\n" \
  "s_cbranch_vccnz LP1_%=\n" \
  "LK1_%=:\n" \
  "ds_read_b128 v[52:55], v9 offset:35840\n" \
  "v_mfma_f32_16x16x32_bf16 v[104:107], a[132:135], v[32:35], v[104:107]\n" \
  "v_mfma_f32_16x16x32_bf16 v[116:119], a[132:135], v[36:39], v[116:119]\n" \
  "v_mfma_f32_16x16x32_bf16 v[108:111], a[212:215], v[32:35], v[108:111]\n" \
  "v_mfma_f32_16x16x32_bf16 v[120:123], a[212:215], v[36:39], v[120:123]\n" \
  "buffer_load_dwordx4 v[192:195], v6, s[40:43], s60 offen offset:0 sc1\n" \
  "s_waitcnt lgkmcnt(3)\n" \
  "v_mfma_f32_16x16x32_bf16 v[100:103], a[56:59], v[40:43], v[100:103]\n" \
  "s_waitcnt lgkmcnt(2)\n" \
  "ds_read_b128 v[32:35], v9 offset:16384\n" \
  "v_mfma_f32_16x16x32_bf16 v[112:115], a[56:59], v[44:47], v[112:115]\n" \
  "ds_read_b128 v[36:39], v9 offset:36864\n" \
  "v_mfma_f32_16x16x32_bf16 v[104:107], a[136:139], v[40:43], v[104:107]\n" \
  "buffer_load_dwordx4 v[196:199], v6, s[40:43], s60 offen offset:1024 sc1\n" \
  "v_mfma_f32_16x16x32_bf16 v[116:119], a[136:139], v[44:47], v[116:119]\n" \
  "v_mfma_f32_16x16x32_bf16 v[108:111], a[216:219], v[40:43], v[108:111]\n" \
  "v_mfma_f32_16x16x32_bf16 v[120:123], a[216:219], v[44:47], v[120:123]\n" \
  "buffer_load_dwordx4 v[200:203], v6, s[40:43], s60 offen offset:2048 sc1\n" \
  "s_waitcnt lgkmcnt(3)\n" \
  "v_mfma_f32_16x16x32_bf16 v[100:103], a[60:63], v[48:51], v[100:103]\n" \
  "s_waitcnt lgkmcnt(2)\n" \
  "ds_read_b128 v[40:43], v9 offset:17408\n" \
  "v_mfma_f32_16x16x32_bf16 v[112:115], a[60:63], v[52:55], v[112:115]\n" \
  "ds_read_b128 v[44:47], v9 offset:37888\n" \
  "v_mfma_f32_16x16x32_bf16 v[104:107], a[140:143], v[48:51], v[104:107]\n" \
  "buffer_load_dwordx4 v[204:207], v6, s[40:43], s60 offen offset:3072 sc1\n" \
  "v_mfma_f32_16x16x32_bf16 v[116:119], a[140:143], v[52:55], v[116:119]\n" \
  "v_mfma_f32_16x16x32_bf16 v[108:111], a[220:223], v[48:51], v[108:111]\n" \
  "v_mfma_f32_16x16x32_bf16 v[120:123], a[220:223], v[52:55], v[120:123]\n" \
  "buffer_load_dwordx2 v[208:209], v7, s[40:43], s60 offen offset:0 sc1\n" \
  "s_waitcnt lgkmcnt(3)\n" \
  "v_mfma_f32_16x16x32_bf16 v[100:103], a[64:67], v[32:35], v[100:103]\n" \
  "s_waitcnt lgkmcnt(2)\n" \
  "ds_read_b128 v[48:51], v9 offset:18432\n" \
  "v_mfma_f32_16x16x32_bf16 v[112:115], a[64:67], v[36:39], v[112:115]\n" \
  "ds_read_b128 v[52:55], v9 offset:38912\n" \
  "v_mfma_f32_16x16x32_bf16 v[104:107], a[144:147], v[32:35], v[104:107]\n" \
  "buffer_load_dwordx2 v[210:211], v7, s[40:43], s60 offen offset:1024 sc1\n" \
  "v_mfma_f32_16x16x32_bf16 v[116:119], a[144:147], v[36:39], v[116:119]\n" \
  "v_mfma_f32_16x16x32_bf16 v[108:111], a[224:227], v[32:35], v[108:111]\n" \
  "v_mfma_f32_16x16x32_bf16 v[120:123], a[224:227], v[36:39], v[120:123]\n" \
  "buffer_load_dwordx2 v[212:213], v7, s[40:43], s60 offen offset:2048 sc1\n" \
  "s_waitcnt lgkmcnt(3)\n" \
  "v_mfma_f32_16x16x32_bf16 v[100:103], a[68:71], v[40:43], v[100:103]\n" \
  "s_waitcnt lgkmcnt(2)\n" \
  "ds_read_b128 v[32:35], v9 offset:19456\n" \
  "v_mfma_f32_16x16x32_bf16 v[112:115], a[68:71], v[44:47], v[112:115]\n" \
  "ds_read_b128 v[36:39], v9 offset:39936\n" \
  "v_mfma_f32_16x16x32_bf16 v[104:107], a[148:151], v[40:43], v[104:107]\n" \
  "buffer_load_dwordx2 v[214:215], v7, s[40:43], s60 offen offset:3072 sc1\n" \
  "v_mfma_f32_16x16x32_bf16 v[116:119], a[148:151], v[44:47], v[116:119]\n" \
  "v_mfma_f32_16x16x32_bf16 v[108:111], a[228:231], v[40:43], v[108:111]\n" \
  "v_mfma_f32_16x16x32_bf16 v[120:123], a[228:231], v[44:47], v[120:123]\n" \
  "s_waitcnt vmcnt(33)\n" \
  "s_barrier\n" \
  "s_waitcnt lgkmcnt(3)\n" \
  "v_mfma_f32_16x16x32_bf16 v[100:103], a[72:75], v[48:51], v[100:103]\n" \
  "s_waitcnt lgkmcnt(2)\n" \
  "ds_read_b128 v[40:43], v10 offset:0\n" \
  "v_mfma_f32_16x16x32_bf16 v[112:115], a[72:75], v[52:55], v[112:115]\n" \
  "ds_read_b128 v[44:47], v10 offset:20480\n" \
  "v_mfma_f32_16x16x32_bf16 v[104:107], a[152:155], v[48:51], v[104:107]\n" \
  "v_mfma_f32_16x16x32_bf16 v[116:119], a[152:155], v[52:55], v[116:119]\n" \
  "v_mfma_f32_16x16x32_bf16 v[108:111], a[232:235], v[48:51], v[108:111]\n" \
  "v_mfma_f32_16x16x32_bf16 v[120:123], a[232:235], v[52:55], v[120:123]\n" \
  "s_waitcnt lgkmcnt(3)\n" \
  "v_mfma_f32_16x16x32_bf16 v[100:103], a[76:79], v[32:35], v[100:103]\n" \
  "s_waitcnt lgkmcnt(2)\n" \
  "ds_read_b128 v[48:51], v10 offset:1024\n" \
  "v_mfma_f32_16x16x32_bf16 v[112:115], a[76:79], v[36:39], v[112:115]\n" \
  "ds_read_b128 v[52:55], v10 offset:21504\n" \
  "v_mfma_f32_16x16x32_bf16 v[104:107], a[156:159], v[32:35], v[104:107]\n" \
  "v_mfma_f32_16x16x32_bf16 v[116:119], a[156:159], v[36:39], v[116:119]\n" \
  "v_mfma_f32_16x16x32_bf16 v[108:111], a[236:239], v[32:35], v[108:111]\n" \
  "v_mfma_f32_16x16x32_bf16 v[120:123], a[236:239], v[36:39], v[120:123]\n" \
  "s_add_u32 s52, s52, 1\n" \
  "s_waitcnt lgkmcnt(3)\n" \
  "v_mfma_f32_16x16x32_bf16 v[56:59], a[0:3], v[40:43], 0\n" \
  "s_add_u32 s63, s52, s54\n" \
  "s_waitcnt lgkmcnt(2)\n" \
  "ds_read_b128 v[32:35], v10 offset:2048\n" \
  "v_mfma_f32_16x16x32_bf16 v[68:71], a[0:3], v[44:47], 0\n" \
  "s_sub_u32 s64, s63, 2\n" \
  "ds_read_b128 v[36:39], v10 offset:22528\n" \
  "v_mfma_f32_16x16x32_bf16 v[60:63], a[80:83], v[40:43], 0\n" \
  "v_cvt_pk_bf16_f32 v16, v100, v101\n" \
  "v_cvt_pk_bf16_f32 v17, v102, v103\n" \
  "v_mfma_f32_16x16x32_bf16 v[72:75], a[80:83], v[44:47], 0\n" \
  "v_cvt_pk_bf16_f32 v18, v104, v105\n" \
  "v_cvt_pk_bf16_f32 v19, v106, v107\n" \
  "v_mfma_f32_16x16x32_bf16 v[64:67], a[160:163], v[40:43], 0\n" \
  "v_cvt_pk_bf16_f32 v20, v112, v113\n" \
  "v_cvt_pk_bf16_f32 v21, v114, v115\n" \
  "v_mfma_f32_16x16x32_bf16 v[76:79], a[160:163], v[44:47], 0\n" \
  "s_waitcnt lgkmcnt(3)\n" \
  "v_cvt_pk_bf16_f32 v22, v116, v117\n" \
  "v_cvt_pk_bf16_f32 v23, v118, v119\n" \
  "v_mfma_f32_16x16x32_bf16 v[56:59], a[4:7], v[48:51], v[56:59]\n" \
  "s_waitcnt lgkmcnt(2)\n" \
  "ds_read_b128 v[40:43], v10 offset:3072\n" \
  "v_mfma_f32_16x16x32_bf16 v[68:71], a[4:7], v[52:55], v[68:71]\n" \
  "s_barrier\n" \
  "ds_read_b128 v[44:47], v10 offset:23552\n" \
  "v_mfma_f32_16x16x32_bf16 v[60:63], a[84:87], v[48:51], v[60:63]\n" \
  "v_cvt_pk_bf16_f32 v24, v108, v109\n" \
  "v_cvt_pk_bf16_f32 v25, v110, v111\n" \
  "v_mfma_f32_16x16x32_bf16 v[72:75], a[84:87], v[52:55], v[72:75]\n" \
  "v_cvt_pk_bf16_f32 v26, v120, v121\n" \
  "v_cvt_pk_bf16_f32 v27, v122, v123\n" \
  "v_mfma_f32_16x16x32_bf16 v[64:67], a[164:167], v[48:51], v[64:67]\n" \
  "ds_write_b128 v2, v[16:19] offset:12288\n" \
  "v_mfma_f32_16x16x32_bf16 v[76:79], a[164:167], v[52:55], v[76:79]\n" \
  "s_waitcnt lgkmcnt(4)\n" \
  "v_mfma_f32_16x16x32_bf16 v[56:59], a[8:11], v[32:35], v[56:59]\n" \
  "s_waitcnt lgkmcnt(3)\n" \
  "ds_read_b128 v[48:51], v10 offset:4096\n" \
  "v_mfma_f32_16x16x32_bf16 v[68:71], a[8:11], v[36:39], v[68:71]\n" \
  "ds_read_b128 v[52:55], v10 offset:24576\n" \
  "v_mfma_f32_16x16x32_bf16 v[60:63], a[88:91], v[32:35], v[60:63]\n" \
  "ds_write_b128 v2, v[20:23] offset:13312\n" \
  "v_mfma_f32_16x16x32_bf16 v[72:75], a[88:91], v[36:39], v[72:75]\n" \
  "v_mfma_f32_16x16x32_bf16 v[64:67], a[168:171], v[32:35], v[64:67]\n" \
  "v_mfma_f32_16x16x32_bf16 v[76:79], a[168:171], v[36:39], v[76:79]\n" \
  "s_add_u32 s70, s68, 4096\n" \
  "s_add_u32 s71, s68, 8192\n" \
  "s_waitcnt lgkmcnt(5)\n" \
  "v_mfma_f32_16x16x32_bf16 v[56:59], a[12:15], v[40:43], v[56:59]\n" \
  "s_waitcnt lgkmcnt(4)\n" \
  "ds_write_b128 v2, v[24:27] offset:14336\n" \
  "ds_read_b128 v[32:35], v10 offset:5120\n" \
  "v_mfma_f32_16x16x32_bf16 v[68:71], a[12:15], v[44:47], v[68:71]\n" \
  "s_add_u32 m0, s53, 40960\n" \
  "ds_read_b128 v[36:39], v10 offset:25600\n" \
  "v_mfma_f32_16x16x32_bf16 v[60:63], a[92:95], v[40:43], v[60:63]\n" \
  "buffer_load_dwordx4 v1, s[36:39], s68 offen sc1 lds\n" \
  "v_mfma_f32_16x16x32_bf16 v[72:75], a[92:95], v[44:47], v[72:75]\n" \
  "v_mfma_f32_16x16x32_bf16 v[64:67], a[172:175], v[40:43], v[64:67]\n" \
  "v_mfma_f32_16x16x32_bf16 v[76:79], a[172:175], v[44:47], v[76:79]\n" \
  "s_waitcnt lgkmcnt(5)\n" \
  "v_mfma_f32_16x16x32_bf16 v[56:59], a[16:19], v[48:51], v[56:59]\n" \
  "s_waitcnt lgkmcnt(4)\n" \
  "ds_read_b128 v[40:43], v10 offset:6144\n" \
  "v_mfma_f32_16x16x32_bf16 v[68:71], a[16:19], v[52:55], v[68:71]\n" \
  "ds_read_b128 v[44:47], v10 offset:26624\n" \
  "v_mfma_f32_16x16x32_bf16 v[60:63], a[96:99], v[48:51], v[60:63]\n" \
  "buffer_load_dwordx4 v1, s[36:39], s68 offen offset:1024 sc1 lds\n" \
  "v_mfma_f32_16x16x32_bf16 v[72:75], a[96:99], v[52:55], v[72:75]\n" \
  "v_mfma_f32_16x16x32_bf16 v[64:67], a[176:179], v[48:51], v[64:67]\n" \
  "v_readlane_b32 s61, v126, s64\n" \
  "v_mfma_f32_16x16x32_bf16 v[76:79], a[176:179], v[52:55], v[76:79]\n" \
  "s_sub_u32 s64, s52, 2\n" \
  "s_cmp_lt_u32 s64, 56\n" \
  "s_waitcnt lgkmcnt(3)\n" \
  "v_mfma_f32_16x16x32_bf16 v[56:59], a[20:23], v[32:35], v[56:59]\n" \
  "s_cselect_b32 s61, s61, 1073741824\n" \
  "s_waitcnt lgkmcnt(2)\n" \
  "ds_read_b128 v[48:51], v10 offset:7168\n" \
  "v_mfma_f32_16x16x32_bf16 v[68:71], a[20:23], v[36:39], v[68:71]\n" \
  "ds_read_b128 v[52:55], v10 offset:27648\n" \
  "v_mfma_f32_16x16x32_bf16 v[60:63], a[100:103], v[32:35], v[60:63]\n" \
  "buffer_load_dwordx4 v1, s[36:39], s68 offen offset:2048 sc1 lds\n" \
  "v_mfma_f32_16x16x32_bf16 v[72:75], a[100:103], v[36:39], v[72:75]\n" \
  "s_cmp_ge_u32 s52, 4\n" \
  "s_cselect_b32 s62, 0, 1073741824\n" \
  "v_mfma_f32_16x16x32_bf16 v[64:67], a[180:183], v[32:35], v[64:67]\n" \
  "s_waitcnt vmcnt(3) lgkmcnt(4)\n" \
  "s_barrier\n" \
  "v_mfma_f32_16x16x32_bf16 v[76:79], a[180:183], v[36:39], v[76:79]\n" \
  "buffer_atomic_add v12, v129, s[40:43], s62 offen\n" \
  "ds_read_b128 v[216:219], v3 offset:0\n" \
  "s_waitcnt lgkmcnt(4)\n" \
  "v_mfma_f32_16x16x32_bf16 v[56:59], a[24:27], v[40:43], v[56:59]\n" \
  "s_waitcnt lgkmcnt(3)\n" \
  "ds_read_b128 v[32:35], v10 offset:8192\n" \
  "ds_read_b128 v[220:223], v3 offset:3072\n" \
  "v_mfma_f32_16x16x32_bf16 v[68:71], a[24:27], v[44:47], v[68:71]\n" \
  "ds_read_b128 v[36:39], v10 offset:28672\n" \
  "ds_read_b128 v[224:227], v3 offset:6144\n" \
  "v_mfma_f32_16x16x32_bf16 v[60:63], a[104:107], v[40:43], v[60:63]\n" \
  "buffer_load_dwordx4 v1, s[36:39], s68 offen offset:3072 sc1 lds\n" \
  "ds_read_b128 v[228:231], v3 offset:9216\n" \
  "v_mfma_f32_16x16x32_bf16 v[72:75], a[104:107], v[44:47], v[72:75]\n" \
  "ds_read_b64 v[232:233], v4 offset:0\n" \
  "ds_read_b64 v[234:235], v4 offset:3072\n" \
  "v_mfma_f32_16x16x32_bf16 v[64:67], a[184:187], v[40:43], v[64:67]\n" \
  "ds_read_b64 v[236:237], v4 offset:6144\n" \
  "ds_read_b64 v[238:239], v4 offset:9216\n" \
  "v_mfma_f32_16x16x32_bf16 v[76:79], a[184:187], v[44:47], v[76:79]\n" \
  "s_waitcnt lgkmcnt(11)\n" \
  "v_swap_b32 v162, v184\n" \
  "v_mfma_f32_16x16x32_bf16 v[56:59], a[28:31], v[48:51], v[56:59]\n" \
  "s_waitcnt lgkmcnt(10)\n" \
  "ds_read_b128 v[40:43], v10 offset:9216\n" \
  "v_mfma_f32_16x16x32_bf16 v[68:71], a[28:31], v[52:55], v[68:71]\n" \
  "s_add_u32 m0, s53, 45056\n" \
  "ds_read_b128 v[44:47], v10 offset:29696\n" \
  "v_mfma_f32_16x16x32_bf16 v[60:63], a[108:111], v[48:51], v[60:63]\n" \
  "buffer_load_dwordx4 v1, s[36:39], s70 offen sc1 lds\n" \
  "v_mfma_f32_16x16x32_bf16 v[72:75], a[108:111], v[52:55], v[72:75]\n" \
  "v_swap_b32 v163, v185\n" \
  "v_mfma_f32_16x16x32_bf16 v[64:67], a[188:191], v[48:51], v[64:67]\n" \
  "v_swap_b32 v166, v188\n" \
  "v_mfma_f32_16x16x32_bf16 v[76:79], a[188:191], v[52:55], v[76:79]\n" \
  "s_waitcnt lgkmcnt(10)\n" \
  "v_swap_b32 v167, v189\n" \
  "v_mfma_f32_16x16x32_bf16 v[56:59], a[32:35], v[32:35], v[56:59]\n" \
  "s_waitcnt lgkmcnt(8)\n" \
  "ds_read_b128 v[48:51], v10 offset:10240\n" \
  "v_mfma_f32_16x16x32_bf16 v[68:71], a[32:35], v[36:39], v[68:71]\n" \
  "ds_read_b128 v[52:55], v10 offset:30720\n" \
  "v_mfma_f32_16x16x32_bf16 v[240:243], v[192:195], v[216:219], v[132:135]\n" \
  "v_mfma_f32_16x16x32_bf16 v[244:247], v[144:147], v[216:219], v[136:139]\n" \
  "v_mfma_f32_16x16x32_bf16 v[248:251], v[168:171], v[216:219], v[140:143]\n" \
  "v_mfma_f32_16x16x32_bf16 v[60:63], a[112:115], v[32:35], v[60:63]\n" \
  "buffer_load_dwordx4 v1, s[36:39], s70 offen offset:1024 sc1 lds\n" \
  "v_mfma_f32_16x16x32_bf16 v[240:243], v[196:199], v[220:223], v[240:243]\n" \
  "v_mfma_f32_16x16x32_bf16 v[244:247], v[148:151], v[220:223], v[244:247]\n" \
  "v_mfma_f32_16x16x32_bf16 v[248:251], v[172:175], v[220:223], v[248:251]\n" \
  "v_mfma_f32_16x16x32_bf16 v[72:75], a[112:115], v[36:39], v[72:75]\n" \
  "s_waitcnt lgkmcnt(9)\n" \
  "v_mfma_f32_16x16x32_bf16 v[240:243], v[200:203], v[224:227], v[240:243]\n" \
  "v_mfma_f32_16x16x32_bf16 v[244:247], v[152:155], v[224:227], v[244:247]\n" \
  "v_mfma_f32_16x16x32_bf16 v[248:251], v[176:179], v[224:227], v[248:251]\n" \
  "v_mfma_f32_16x16x32_bf16 v[64:67], a[192:195], v[32:35], v[64:67]\n" \
  "s_waitcnt lgkmcnt(8)\n" \
  "v_mfma_f32_16x16x32_bf16 v[240:243], v[204:207], v[228:231], v[240:243]\n" \
  "v_mfma_f32_16x16x32_bf16 v[244:247], v[156:159], v[228:231], v[244:247]\n" \
  "v_mfma_f32_16x16x32_bf16 v[248:251], v[180:183], v[228:231], v[248:251]\n" \
  "v_mfma_f32_16x16x32_bf16 v[76:79], a[192:195], v[36:39], v[76:79]\n" \
  "s_waitcnt lgkmcnt(6)\n" \
  "v_mfma_f32_16x16x32_bf16 v[240:243], v[208:211], v[232:235], v[240:243]\n" \
  "v_mfma_f32_16x16x32_bf16 v[244:247], v[160:163], v[232:235], v[244:247]\n" \
  "v_mfma_f32_16x16x32_bf16 v[248:251], v[184:187], v[232:235], v[248:251]\n" \
  "s_waitcnt lgkmcnt(3)\n" \
  "v_mfma_f32_16x16x32_bf16 v[56:59], a[36:39], v[40:43], v[56:59]\n" \
  "ds_read_b128 v[32:35], v10 offset:11264\n" \
  "v_mfma_f32_16x16x32_bf16 v[240:243], v[212:215], v[236:239], v[240:243]\n" \
  "v_mfma_f32_16x16x32_bf16 v[244:247], v[164:167], v[236:239], v[244:247]\n" \
  "v_mfma_f32_16x16x32_bf16 v[248:251], v[188:191], v[236:239], v[248:251]\n" \
  "s_waitcnt lgkmcnt(3)\n" \
  "v_mfma_f32_16x16x32_bf16 v[68:71], a[36:39], v[44:47], v[68:71]\n" \
  "ds_read_b128 v[36:39], v10 offset:31744\n" \
  "v_mfma_f32_16x16x32_bf16 v[60:63], a[116:119], v[40:43], v[60:63]\n" \
  "buffer_load_dwordx4 v1, s[36:39], s70 offen offset:2048 sc1 lds\n" \
  "v_mfma_f32_16x16x32_bf16 v[72:75], a[116:119], v[44:47], v[72:75]\n" \
  "v_cvt_pk_bf16_f32 v80, v240, v241\n" \
  "v_cvt_pk_bf16_f32 v81, v242, v243\n" \
  "v_mfma_f32_16x16x32_bf16 v[64:67], a[196:199], v[40:43], v[64:67]\n" \
  "v_cvt_pk_bf16_f32 v82, v244, v245\n" \
  "v_cvt_pk_bf16_f32 v83, v246, v247\n" \
  "v_mfma_f32_16x16x32_bf16 v[76:79], a[196:199], v[44:47], v[76:79]\n" \
  "s_waitcnt lgkmcnt(3)\n" \
  "v_cvt_pk_bf16_f32 v84, v248, v249\n" \
  "v_cvt_pk_bf16_f32 v85, v250, v251\n" \
  "v_mfma_f32_16x16x32_bf16 v[56:59], a[40:43], v[48:51], v[56:59]\n" \
  "s_waitcnt lgkmcnt(2)\n" \
  "ds_read_b128 v[40:43], v10 offset:12288\n" \
  "v_lshlrev_b32 v240, 16, v80\n" \
  "v_mfma_f32_16x16x32_bf16 v[68:71], a[40:43], v[52:55], v[68:71]\n" \
  "ds_read_b128 v[44:47], v10 offset:32768\n" \
  "v_and_b32 v241, 0xffff0000, v80\n" \
  "v_mfma_f32_16x16x32_bf16 v[60:63], a[120:123], v[48:51], v[60:63]\n" \
  "buffer_load_dwordx4 v1, s[36:39], s70 offen offset:3072 sc1 lds\n" \
  "v_mul_f32 v240, 0x3d93cd3a, v240\n" \
  "v_mfma_f32_16x16x32_bf16 v[72:75], a[120:123], v[52:55], v[72:75]\n" \
  "v_mul_f32 v241, 0x3d93cd3a, v241\n" \
  "v_cvt_pk_bf16_f32 v80, v240, v241\n" \
  "v_mfma_f32_16x16x32_bf16 v[64:67], a[200:203], v[48:51], v[64:67]\n" \
  "v_lshlrev_b32 v240, 16, v80\n" \
  "v_and_b32 v241, 0xffff0000, v80\n" \
  "v_mfma_f32_16x16x32_bf16 v[76:79], a[200:203], v[52:55], v[76:79]\n" \
  "s_waitcnt lgkmcnt(3)\n" \
  "v_lshlrev_b32 v242, 16, v81\n" \
  "v_and_b32 v243, 0xffff0000, v81\n" \
  "v_mfma_f32_16x16x32_bf16 v[56:59], a[44:47], v[32:35], v[56:59]\n" \
  "s_waitcnt lgkmcnt(2)\n" \
  "ds_read_b128 v[48:51], v10 offset:13312\n" \
  "v_mul_f32 v242, 0x3d93cd3a, v242\n" \
  "v_mfma_f32_16x16x32_bf16 v[68:71], a[44:47], v[36:39], v[68:71]\n" \
  "s_add_u32 m0, s53, 49152\n" \
  "ds_read_b128 v[52:55], v10 offset:33792\n" \
  "v_mul_f32 v243, 0x3d93cd3a, v243\n" \
  "v_mfma_f32_16x16x32_bf16 v[60:63], a[124:127], v[32:35], v[60:63]\n" \
  "buffer_load_dwordx4 v1, s[36:39], s71 offen sc1 lds\n" \
  "v_cvt_pk_bf16_f32 v81, v242, v243\n" \
  "v_mfma_f32_16x16x32_bf16 v[72:75], a[124:127], v[36:39], v[72:75]\n" \
  "v_lshlrev_b32 v242, 16, v81\n" \
  "v_and_b32 v243, 0xffff0000, v81\n" \
  "v_mfma_f32_16x16x32_bf16 v[64:67], a[204:207], v[32:35], v[64:67]\n" \
  "v_readlane_b32 s56, v124, s63\n" \
  "v_mfma_f32_16x16x32_bf16 v[76:79], a[204:207], v[36:39], v[76:79]\n" \
  "s_waitcnt lgkmcnt(3)\n" \
  "v_lshlrev_b32 v244, 16, v82\n" \
  "v_and_b32 v245, 0xffff0000, v82\n" \
  "v_mfma_f32_16x16x32_bf16 v[56:59], a[48:51], v[40:43], v[56:59]\n" \
  "s_waitcnt lgkmcnt(2)\n" \
  "ds_read_b128 v[32:35], v10 offset:14336\n" \
  "v_mul_f32 v244, 0x3d93cd3a, v244\n" \
  "v_mfma_f32_16x16x32_bf16 v[68:71], a[48:51], v[44:47], v[68:71]\n" \
  "ds_read_b128 v[36:39], v10 offset:34816\n" \
  "v_mul_f32 v245, 0x3d93cd3a, v245\n" \
  "v_mfma_f32_16x16x32_bf16 v[60:63], a[128:131], v[40:43], v[60:63]\n" \
  "buffer_load_dwordx4 v1, s[36:39], s71 offen offset:1024 sc1 lds\n" \
  "v_cvt_pk_bf16_f32 v82, v244, v245\n" \
  "v_mfma_f32_16x16x32_bf16 v[72:75], a[128:131], v[44:47], v[72:75]\n" \
  "s_add_u32 s57, s56, 16384\n" \
  "v_lshlrev_b32 v244, 16, v82\n" \
  "v_and_b32 v245, 0xffff0000, v82\n" \
  "v_mfma_f32_16x16x32_bf16 v[64:67], a[208:211], v[40:43], v[64:67]\n" \
  "s_add_u32 s58, s56, 32768\n" \
  "v_lshlrev_b32 v246, 16, v83\n" \
  "v_and_b32 v247, 0xffff0000, v83\n" \
  "v_mfma_f32_16x16x32_bf16 v[76:79], a[208:211], v[44:47], v[76:79]\n" \
  "s_add_u32 s68, s68, 40960\n" \
  "s_add_u32 s59, s56, 49152\n" \
  "s_waitcnt lgkmcnt(3)\n" \
  "v_mul_f32 v246, 0x3d93cd3a, v246\n" \
  "v_mfma_f32_16x16x32_bf16 v[56:59], a[52:55], v[48:51], v[56:59]\n" \
  "s_waitcnt lgkmcnt(2)\n" \
  "ds_read_b128 v[40:43], v10 offset:15360\n" \
  "v_mul_f32 v247, 0x3d93cd3a, v247\n" \
  "v_mfma_f32_16x16x32_bf16 v[68:71], a[52:55], v[52:55], v[68:71]\n" \
  "ds_read_b128 v[44:47], v10 offset:35840\n" \
  "v_cvt_pk_bf16_f32 v83, v246, v247\n" \
  "v_mfma_f32_16x16x32_bf16 v[60:63], a[132:135], v[48:51], v[60:63]\n" \
  "v_lshlrev_b32 v246, 16, v83\n" \
  "v_and_b32 v247, 0xffff0000, v83\n" \
  "v_mfma_f32_16x16x32_bf16 v[72:75], a[132:135], v[52:55], v[72:75]\n" \
  "v_lshlrev_b32 v248, 16, v84\n" \
  "v_and_b32 v249, 0xffff0000, v84\n" \
  "v_mfma_f32_16x16x32_bf16 v[64:67], a[212:215], v[48:51], v[64:67]\n" \
  "v_mul_f32 v248, 0x3d93cd3a, v248\n" \
  "v_mul_f32 v249, 0x3d93cd3a, v249\n" \
  "v_mfma_f32_16x16x32_bf16 v[76:79], a[212:215], v[52:55], v[76:79]\n" \
  "buffer_load_dwordx4 v[144:147], v5, s[40:43], s56 offen offset:0 sc1\n" \
  "v_cvt_pk_bf16_f32 v84, v248, v249\n" \
  "s_waitcnt lgkmcnt(3)\n" \
  "v_mfma_f32_16x16x32_bf16 v[56:59], a[56:59], v[32:35], v[56:59]\n" \
  "s_waitcnt lgkmcnt(2)\n" \
  "ds_read_b128 v[48:51], v10 offset:16384\n" \
  "v_lshlrev_b32 v248, 16, v84\n" \
  "v_mfma_f32_16x16x32_bf16 v[68:71], a[56:59], v[36:39], v[68:71]\n" \
  "ds_read_b128 v[52:55], v10 offset:36864\n" \
  "v_and_b32 v249, 0xffff0000, v84\n" \
  "v_mfma_f32_16x16x32_bf16 v[60:63], a[136:139], v[32:35], v[60:63]\n" \
  "v_lshlrev_b32 v250, 16, v85\n" \
  "v_and_b32 v251, 0xffff0000, v85\n" \
  "v_mfma_f32_16x16x32_bf16 v[72:75], a[136:139], v[36:39], v[72:75]\n" \
  "v_mul_f32 v250, 0x3d93cd3a, v250\n" \
  "v_mul_f32 v251, 0x3d93cd3a, v251\n" \
  "v_mfma_f32_16x16x32_bf16 v[64:67], a[216:219], v[32:35], v[64:67]\n" \
  "v_cvt_pk_bf16_f32 v85, v250, v251\n" \
  "v_lshlrev_b32 v250, 16, v85\n" \
  "v_mfma_f32_16x16x32_bf16 v[76:79], a[216:219], v[36:39], v[76:79]\n" \
  "buffer_load_dwordx4 v[168:171], v5, s[40:43], s56 offen offset:1024 sc1\n" \
  "v_and_b32 v251, 0xffff0000, v85\n" \
  "s_waitcnt lgkmcnt(3)\n" \
  "v_mfma_f32_16x16x32_bf16 v[56:59], a[60:63], v[40:43], v[56:59]\n" \
  "s_waitcnt lgkmcnt(2)\n" \
  "ds_read_b128 v[32:35], v10 offset:17408\n" \
  "v_max3_f32 v80, v240, v241, v242\n" \
  "v_mfma_f32_16x16x32_bf16 v[68:71], a[60:63], v[44:47], v[68:71]\n" \
  "ds_read_b128 v[36:39], v10 offset:37888\n" \
  "v_max3_f32 v80, v80, v243, v244\n" \
  "v_mfma_f32_16x16x32_bf16 v[60:63], a[140:143], v[40:43], v[60:63]\n" \
  "v_max3_f32 v80, v80, v245, v246\n" \
  "v_max3_f32 v80, v80, v247, v248\n" \
  "v_mfma_f32_16x16x32_bf16 v[72:75], a[140:143], v[44:47], v[72:75]\n" \
  "v_max3_f32 v80, v80, v249, v250\n" \
  "v_max_f32 v80, v80, v251\n" \
  "v_mfma_f32_16x16x32_bf16 v[64:67], a[220:223], v[40:43], v[64:67]\n" \
  "v_mov_b32 v81, v80\n" \
  "v_mfma_f32_16x16x32_bf16 v[76:79], a[220:223], v[44:47], v[76:79]\n" \
  "buffer_load_dwordx4 v[148:151], v5, s[40:43], s57 offen offset:0 sc1\n" \
  "s_waitcnt lgkmcnt(3)\n" \
  "v_mfma_f32_16x16x32_bf16 v[56:59], a[64:67], v[48:51], v[56:59]\n" \
  "s_waitcnt lgkmcnt(2)\n" \
  "ds_read_b128 v[40:43], v10 offset:18432\n" \
  "v_mfma_f32_16x16x32_bf16 v[68:71], a[64:67], v[52:55], v[68:71]\n" \
  "ds_read_b128 v[44:47], v10 offset:38912\n" \
  "v_mfma_f32_16x16x32_bf16 v[60:63], a[144:147], v[48:51], v[60:63]\n" \
  "v_permlane16_swap_b32 v80, v81\n" \
  "v_mfma_f32_16x16x32_bf16 v[72:75], a[144:147], v[52:55], v[72:75]\n" \
  "v_max_f32 v80, v80, v81\n" \
  "v_mov_b32 v81, v80\n" \
  "v_mfma_f32_16x16x32_bf16 v[64:67], a[224:227], v[48:51], v[64:67]\n" \
  "v_mfma_f32_16x16x32_bf16 v[76:79], a[224:227], v[52:55], v[76:79]\n" \
  "buffer_load_dwordx4 v[172:175], v5, s[40:43], s57 offen offset:1024 sc1\n" \
  "s_waitcnt lgkmcnt(3)\n" \
  "v_mfma_f32_16x16x32_bf16 v[56:59], a[68:71], v[32:35], v[56:59]\n" \
  "s_waitcnt lgkmcnt(2)\n" \
  "ds_read_b128 v[48:51], v10 offset:19456\n" \
  "v_mfma_f32_16x16x32_bf16 v[68:71], a[68:71], v[36:39], v[68:71]\n" \
  "ds_read_b128 v[52:55], v10 offset:39936\n" \
  "v_mfma_f32_16x16x32_bf16 v[60:63], a[148:151], v[32:35], v[60:63]\n" \
  "v_permlane32_swap_b32 v80, v81\n" \
  "v_mfma_f32_16x16x32_bf16 v[72:75], a[148:151], v[36:39], v[72:75]\n" \
  "v_max_f32 v80, v80, v81\n" \
  "v_mul_f32 v80, 0x3fb8aa3b, v80\n" \
  "v_mfma_f32_16x16x32_bf16 v[64:67], a[228:231], v[32:35], v[64:67]\n" \
  "v_mov_b32 v82, 0x3fb8aa3b\n" \
  "v_fma_f32 v240, v240, v82, -v80\n" \
  "v_mfma_f32_16x16x32_bf16 v[76:79], a[228:231], v[36:39], v[76:79]\n" \
  "buffer_load_dwordx4 v[152:155], v5, s[40:43], s58 offen offset:0 sc1\n" \
  "v_fma_f32 v241, v241, v82, -v80\n" \
  "s_waitcnt vmcnt(24)\n" \
  "s_barrier\n" \
  "s_waitcnt lgkmcnt(3)\n" \
  "v_mfma_f32_16x16x32_bf16 v[56:59], a[72:75], v[40:43], v[56:59]\n" \
  "s_waitcnt lgkmcnt(2)\n" \
  "ds_read_b128 v[32:35], v0 offset:0\n" \
  "v_fma_f32 v242, v242, v82, -v80\n" \
  "v_mfma_f32_16x16x32_bf16 v[68:71], a[72:75], v[44:47], v[68:71]\n" \
  "ds_read_b128 v[36:39], v0 offset:20480\n" \
  "v_fma_f32 v243, v243, v82, -v80\n" \
  "v_mfma_f32_16x16x32_bf16 v[60:63], a[152:155], v[40:43], v[60:63]\n" \
  "v_fma_f32 v244, v244, v82, -v80\n" \
  "v_fma_f32 v245, v245, v82, -v80\n" \
  "v_mfma_f32_16x16x32_bf16 v[72:75], a[152:155], v[44:47], v[72:75]\n" \
  "v_fma_f32 v246, v246, v82, -v80\n" \
  "v_fma_f32 v247, v247, v82, -v80\n" \
  "v_mfma_f32_16x16x32_bf16 v[64:67], a[232:235], v[40:43], v[64:67]\n" \
  "v_fma_f32 v248, v248, v82, -v80\n" \
  "v_fma_f32 v249, v249, v82, -v80\n" \
  "v_mfma_f32_16x16x32_bf16 v[76:79], a[232:235], v[44:47], v[76:79]\n" \
  "buffer_load_dwordx4 v[176:179], v5, s[40:43], s58 offen offset:1024 sc1\n" \
  "v_fma_f32 v250, v250, v82, -v80\n" \
  "s_waitcnt lgkmcnt(3)\n" \
  "v_mfma_f32_16x16x32_bf16 v[56:59], a[76:79], v[48:51], v[56:59]\n" \
  "s_waitcnt lgkmcnt(2)\n" \
  "ds_read_b128 v[40:43], v0 offset:1024\n" \
  "v_fma_f32 v251, v251, v82, -v80\n" \
  "v_mfma_f32_16x16x32_bf16 v[68:71], a[76:79], v[52:55], v[68:71]\n" \
  "ds_read_b128 v[44:47], v0 offset:21504\n" \
  "v_mfma_f32_16x16x32_bf16 v[60:63], a[156:159], v[48:51], v[60:63]\n" \
  "v_exp_f32 v240, v240\n" \
  "v_mfma_f32_16x16x32_bf16 v[72:75], a[156:159], v[52:55], v[72:75]\n" \
  "v_exp_f32 v241, v241\n" \
  "v_mfma_f32_16x16x32_bf16 v[64:67], a[236:239], v[48:51], v[64:67]\n" \
  "v_exp_f32 v242, v242\n" \
  "v_mfma_f32_16x16x32_bf16 v[76:79], a[236:239], v[52:55], v[76:79]\n" \
  "v_exp_f32 v243, v243\n" \
  "s_add_u32 s52, s52, 1\n" \
  "s_waitcnt lgkmcnt(3)\n" \
  "v_mfma_f32_16x16x32_bf16 v[100:103], a[0:3], v[32:35], 0\n" \
  "s_waitcnt lgkmcnt(2)\n" \
  "ds_read_b128 v[48:51], v0 offset:2048\n" \
  "v_mfma_f32_16x16x32_bf16 v[112:115], a[0:3], v[36:39], 0\n" \
  "ds_read_b128 v[52:55], v0 offset:22528\n" \
  "v_mfma_f32_16x16x32_bf16 v[104:107], a[80:83], v[32:35], 0\n" \
  "v_cvt_pk_bf16_f32 v16, v56, v57\n" \
  "v_cvt_pk_bf16_f32 v17, v58, v59\n" \
  "v_mfma_f32_16x16x32_bf16 v[116:119], a[80:83], v[36:39], 0\n" \
  "v_cvt_pk_bf16_f32 v18, v60, v61\n" \
  "v_cvt_pk_bf16_f32 v19, v62, v63\n" \
  "v_mfma_f32_16x16x32_bf16 v[108:111], a[160:163], v[32:35], 0\n" \
  "v_cvt_pk_bf16_f32 v20, v68, v69\n" \
  "v_cvt_pk_bf16_f32 v21, v70, v71\n" \
  "v_mfma_f32_16x16x32_bf16 v[120:123], a[160:163], v[36:39], 0\n" \
  "v_cvt_pk_bf16_f32 v22, v72, v73\n" \
  "v_cvt_pk_bf16_f32 v23, v74, v75\n" \
  "buffer_load_dwordx4 v[156:159], v5, s[40:43], s59 offen offset:0 sc1\n" \
  "s_waitcnt lgkmcnt(3)\n" \
  "v_mfma_f32_16x16x32_bf16 v[100:103], a[4:7], v[40:43], v[100:103]\n" \
  "s_waitcnt lgkmcnt(2)\n" \
  "ds_read_b128 v[32:35], v0 offset:3072\n" \
  "v_mfma_f32_16x16x32_bf16 v[112:115], a[4:7], v[44:47], v[112:115]\n" \
  "s_barrier\n" \
  "ds_read_b128 v[36:39], v0 offset:23552\n" \
  "v_mfma_f32_16x16x32_bf16 v[104:107], a[84:87], v[40:43], v[104:107]\n" \
  "v_cvt_pk_bf16_f32 v24, v64, v65\n" \
  "v_cvt_pk_bf16_f32 v25, v66, v67\n" \
  "v_mfma_f32_16x16x32_bf16 v[116:119], a[84:87], v[44:47], v[116:119]\n" \
  "v_cvt_pk_bf16_f32 v26, v76, v77\n" \
  "v_cvt_pk_bf16_f32 v27, v78, v79\n" \
  "v_mfma_f32_16x16x32_bf16 v[108:111], a[164:167], v[40:43], v[108:111]\n" \
  "ds_write_b128 v2, v[16:19] offset:0\n" \
  "v_mfma_f32_16x16x32_bf16 v[120:123], a[164:167], v[44:47], v[120:123]\n" \
  "buffer_load_dwordx4 v[180:183], v5, s[40:43], s59 offen offset:1024 sc1\n" \
  "s_waitcnt lgkmcnt(4)\n" \
  "v_mfma_f32_16x16x32_bf16 v[100:103], a[8:11], v[48:51], v[100:103]\n" \
  "s_waitcnt lgkmcnt(3)\n" \
  "ds_read_b128 v[40:43], v0 offset:4096\n" \
  "v_mfma_f32_16x16x32_bf16 v[112:115], a[8:11], v[52:55], v[112:115]\n" \
  "ds_read_b128 v[44:47], v0 offset:24576\n" \
  "v_mfma_f32_16x16x32_bf16 v[104:107], a[88:91], v[48:51], v[104:107]\n" \
  "ds_write_b128 v2, v[20:23] offset:1024\n" \
  "v_mfma_f32_16x16x32_bf16 v[116:119], a[88:91], v[52:55], v[116:119]\n" \
  "v_exp_f32 v244, v244\n" \
  "v_mfma_f32_16x16x32_bf16 v[108:111], a[168:171], v[48:51], v[108:111]\n" \
  "v_exp_f32 v245, v245\n" \
  "v_mfma_f32_16x16x32_bf16 v[120:123], a[168:171], v[52:55], v[120:123]\n" \
  "s_add_u32 s70, s68, 4096\n" \
  "s_add_u32 s71, s68, 8192\n" \
  "buffer_load_dwordx4 v[160:163], v5, s[40:43], s56 offen offset:2048 sc1\n" \
  "s_waitcnt lgkmcnt(5)\n" \
  "v_mfma_f32_16x16x32_bf16 v[100:103], a[12:15], v[32:35], v[100:103]\n" \
  "s_waitcnt lgkmcnt(4)\n" \
  "ds_write_b128 v2, v[24:27] offset:2048\n" \
  "ds_read_b128 v[48:51], v0 offset:5120\n" \
  "v_mfma_f32_16x16x32_bf16 v[112:115], a[12:15], v[36:39], v[112:115]\n" \
  "s_add_u32 m0, s53, 81920\n" \
  "ds_read_b128 v[52:55], v0 offset:25600\n" \
  "v_mfma_f32_16x16x32_bf16 v[104:107], a[92:95], v[32:35], v[104:107]\n" \
  "buffer_load_dwordx4 v1, s[36:39], s68 offen sc1 lds\n" \
  "v_mfma_f32_16x16x32_bf16 v[116:119], a[92:95], v[36:39], v[116:119]\n" \
  "v_exp_f32 v246, v246\n" \
  "v_mfma_f32_16x16x32_bf16 v[108:111], a[172:175], v[32:35], v[108:111]\n" \
  "v_exp_f32 v247, v247\n" \
  "v_mfma_f32_16x16x32_bf16 v[120:123], a[172:175], v[36:39], v[120:123]\n" \
  "buffer_load_dwordx4 v[184:187], v5, s[40:43], s57 offen offset:2048 sc1\n" \
  "s_waitcnt lgkmcnt(5)\n" \
  "v_mfma_f32_16x16x32_bf16 v[100:103], a[16:19], v[40:43], v[100:103]\n" \
  "s_waitcnt lgkmcnt(4)\n" \
  "ds_read_b128 v[32:35], v0 offset:6144\n" \
  "v_mfma_f32_16x16x32_bf16 v[112:115], a[16:19], v[44:47], v[112:115]\n" \
  "ds_read_b128 v[36:39], v0 offset:26624\n" \
  "v_mfma_f32_16x16x32_bf16 v[104:107], a[96:99], v[40:43], v[104:107]\n" \
  "buffer_load_dwordx4 v1, s[36:39], s68 offen offset:1024 sc1 lds\n" \
  "v_mfma_f32_16x16x32_bf16 v[116:119], a[96:99], v[44:47], v[116:119]\n" \
  "v_exp_f32 v248, v248\n" \
  "v_mfma_f32_16x16x32_bf16 v[108:111], a[176:179], v[40:43], v[108:111]\n" \
  "v_exp_f32 v249, v249\n" \
  "v_mfma_f32_16x16x32_bf16 v[120:123], a[176:179], v[44:47], v[120:123]\n" \
  "buffer_load_dwordx4 v[164:167], v5, s[40:43], s58 offen offset:2048 sc1\n" \
  "s_waitcnt lgkmcnt(3)\n" \
  "v_mfma_f32_16x16x32_bf16 v[100:103], a[20:23], v[48:51], v[100:103]\n" \
  "s_waitcnt lgkmcnt(2)\n" \
  "ds_read_b128 v[40:43], v0 offset:7168\n" \
  "v_mfma_f32_16x16x32_bf16 v[112:115], a[20:23], v[52:55], v[112:115]\n" \
  "ds_read_b128 v[44:47], v0 offset:27648\n" \
  "v_mfma_f32_16x16x32_bf16 v[104:107], a[100:103], v[48:51], v[104:107]\n" \
  "buffer_load_dwordx4 v1, s[36:39], s68 offen offset:2048 sc1 lds\n" \
  "v_mfma_f32_16x16x32_bf16 v[116:119], a[100:103], v[52:55], v[116:119]\n" \
  "v_exp_f32 v250, v250\n" \
  "v_mfma_f32_16x16x32_bf16 v[108:111], a[180:183], v[48:51], v[108:111]\n" \
  "v_exp_f32 v251, v251\n" \
  "v_mfma_f32_16x16x32_bf16 v[120:123], a[180:183], v[52:55], v[120:123]\n" \
  "buffer_load_dwordx4 v[188:191], v5, s[40:43], s59 offen offset:2048 sc1\n" \
  "v_add_f32 v80, v240, v241\n" \
  "s_waitcnt lgkmcnt(3)\n" \
  "v_mfma_f32_16x16x32_bf16 v[100:103], a[24:27], v[32:35], v[100:103]\n" \
  "s_waitcnt lgkmcnt(2)\n" \
  "ds_read_b128 v[48:51], v0 offset:8192\n" \
  "v_add_f32 v80, v80, v242\n" \
  "v_mfma_f32_16x16x32_bf16 v[112:115], a[24:27], v[36:39], v[112:115]\n" \
  "ds_read_b128 v[52:55], v0 offset:28672\n" \
  "v_add_f32 v80, v80, v243\n" \
  "v_mfma_f32_16x16x32_bf16 v[104:107], a[104:107], v[32:35], v[104:107]\n" \
  "buffer_load_dwordx4 v1, s[36:39], s68 offen offset:3072 sc1 lds\n" \
  "v_add_f32 v80, v80, v244\n" \
  "v_mfma_f32_16x16x32_bf16 v[116:119], a[104:107], v[36:39], v[116:119]\n" \
  "v_add_f32 v80, v80, v245\n" \
  "v_add_f32 v80, v80, v246\n" \
  "v_mfma_f32_16x16x32_bf16 v[108:111], a[184:187], v[32:35], v[108:111]\n" \
  "v_add_f32 v80, v80, v247\n" \
  "v_add_f32 v80, v80, v248\n" \
  "v_mfma_f32_16x16x32_bf16 v[120:123], a[184:187], v[36:39], v[120:123]\n" \
  "buffer_load_dword v11, v8, s[40:43], 0 offen sc1\n" \
  "v_add_f32 v80, v80, v249\n" \
  "s_waitcnt lgkmcnt(3)\n" \
  "v_mfma_f32_16x16x32_bf16 v[100:103], a[28:31], v[40:43], v[100:103]\n" \
  "s_waitcnt lgkmcnt(2)\n" \
  "ds_read_b128 v[32:35], v0 offset:9216\n" \
  "v_add_f32 v80, v80, v250\n" \
  "v_mfma_f32_16x16x32_bf16 v[112:115], a[28:31], v[44:47], v[112:115]\n" \
  "s_add_u32 m0, s53, 86016\n" \
  "ds_read_b128 v[36:39], v0 offset:29696\n" \
  "v_add_f32 v80, v80, v251\n" \
  "v_mfma_f32_16x16x32_bf16 v[104:107], a[108:111], v[40:43], v[104:107]\n" \
  "buffer_load_dwordx4 v1, s[36:39], s70 offen sc1 lds\n" \
  "v_mov_b32 v81, v80\n" \
  "v_mfma_f32_16x16x32_bf16 v[116:119], a[108:111], v[44:47], v[116:119]\n" \
  "v_mfma_f32_16x16x32_bf16 v[108:111], a[188:191], v[40:43], v[108:111]\n" \
  "v_readlane_b32 s60, v125, s63\n" \
  "v_mfma_f32_16x16x32_bf16 v[120:123], a[188:191], v[44:47], v[120:123]\n" \
  "s_waitcnt lgkmcnt(3)\n" \
  "v_permlane16_swap_b32 v80, v81\n" \
  "v_mfma_f32_16x16x32_bf16 v[100:103], a[32:35], v[48:51], v[100:103]\n" \
  "s_waitcnt lgkmcnt(2)\n" \
  "ds_read_b128 v[40:43], v0 offset:10240\n" \
  "v_add_f32 v80, v80, v81\n" \
  "v_mfma_f32_16x16x32_bf16 v[112:115], a[32:35], v[52:55], v[112:115]\n" \
  "ds_read_b128 v[44:47], v0 offset:30720\n" \
  "v_mov_b32 v81, v80\n" \
  "v_mfma_f32_16x16x32_bf16 v[104:107], a[112:115], v[48:51], v[104:107]\n" \
  "buffer_load_dwordx4 v1, s[36:39], s70 offen offset:1024 sc1 lds\n" \
  "v_mfma_f32_16x16x32_bf16 v[116:119], a[112:115], v[52:55], v[116:119]\n" \
  "v_permlane32_swap_b32 v80, v81\n" \
  "v_mfma_f32_16x16x32_bf16 v[108:111], a[192:195], v[48:51], v[108:111]\n" \
  "s_add_u32 s64, s52, 2\n" \
  "s_min_u32 s64, s64, 57\n" \
  "v_add_f32 v80, v80, v81\n" \
  "v_mfma_f32_16x16x32_bf16 v[120:123], a[192:195], v[52:55], v[120:123]\n" \
  "s_waitcnt lgkmcnt(3)\n" \
  "v_rcp_f32 v80, v80\n" \
  "v_mfma_f32_16x16x32_bf16 v[100:103], a[36:39], v[32:35], v[100:103]\n" \
  "s_waitcnt lgkmcnt(2)\n" \
  "ds_read_b128 v[48:51], v0 offset:11264\n" \
  "v_mul_f32 v244, v244, v80\n" \
  "v_mfma_f32_16x16x32_bf16 v[112:115], a[36:39], v[36:39], v[112:115]\n" \
  "ds_read_b128 v[52:55], v0 offset:31744\n" \
  "v_mul_f32 v245, v245, v80\n" \
  "v_mfma_f32_16x16x32_bf16 v[104:107], a[116:119], v[32:35], v[104:107]\n" \
  "buffer_load_dwordx4 v1, s[36:39], s70 offen offset:2048 sc1 lds\n" \
  "v_mul_f32 v246, v246, v80\n" \
  "v_mfma_f32_16x16x32_bf16 v[116:119], a[116:119], v[36:39], v[116:119]\n" \
  "v_mul_f32 v247, v247, v80\n" \
  "v_mul_f32 v248, v248, v80\n" \
  "v_mfma_f32_16x16x32_bf16 v[108:111], a[196:199], v[32:35], v[108:111]\n" \
  "v_mul_f32 v249, v249, v80\n" \
  "v_mul_f32 v250, v250, v80\n" \
  "v_mfma_f32_16x16x32_bf16 v[120:123], a[196:199], v[36:39], v[120:123]\n" \
  "s_waitcnt lgkmcnt(3)\n" \
  "v_mul_f32 v251, v251, v80\n" \
  "v_mul_f32 v240, v240, v80\n" \
  "v_mfma_f32_16x16x32_bf16 v[100:103], a[40:43], v[40:43], v[100:103]\n" \
  "s_waitcnt lgkmcnt(2)\n" \
  "ds_read_b128 v[32:35], v0 offset:12288\n" \
  "v_mul_f32 v241, v241, v80\n" \
  "v_mfma_f32_16x16x32_bf16 v[112:115], a[40:43], v[44:47], v[112:115]\n" \
  "ds_read_b128 v[36:39], v0 offset:32768\n" \
  "v_mul_f32 v242, v242, v80\n" \
  "v_mfma_f32_16x16x32_bf16 v[104:107], a[120:123], v[40:43], v[104:107]\n" \
  "buffer_load_dwordx4 v1, s[36:39], s70 offen offset:3072 sc1 lds\n" \
  "v_mul_f32 v243, v243, v80\n" \
  "v_mfma_f32_16x16x32_bf16 v[116:119], a[120:123], v[44:47], v[116:119]\n" \
  "v_cvt_pk_bf16_f32 v88, v244, v245\n" \
  "v_cvt_pk_bf16_f32 v89, v246, v247\n" \
  "v_mfma_f32_16x16x32_bf16 v[108:111], a[200:203], v[40:43], v[108:111]\n" \
  "v_cvt_pk_bf16_f32 v90, v248, v249\n" \
  "v_cvt_pk_bf16_f32 v91, v250, v251\n" \
  "v_mfma_f32_16x16x32_bf16 v[120:123], a[200:203], v[44:47], v[120:123]\n" \
  "s_waitcnt lgkmcnt(3)\n" \
  "v_cvt_pk_bf16_f32 v92, v240, v241\n" \
  "v_cvt_pk_bf16_f32 v93, v242, v243\n" \
  "v_mfma_f32_16x16x32_bf16 v[100:103], a[44:47], v[48:51], v[100:103]\n" \
  "ds_read_b128 v[40:43], v0 offset:13312\n" \
  "buffer_store_dwordx4 v[88:91], v127, s[40:43], s61 offen sc1\n" \
  "s_waitcnt lgkmcnt(3)\n" \
  "v_mfma_f32_16x16x32_bf16 v[112:115], a[44:47], v[52:55], v[112:115]\n" \
  "s_add_u32 m0, s53, 90112\n" \
  "ds_read_b128 v[44:47], v0 offset:33792\n" \
  "buffer_store_dwordx2 v[92:93], v128, s[40:43], s61 offen sc1\n" \
  "v_mfma_f32_16x16x32_bf16 v[104:107], a[124:127], v[48:51], v[104:107]\n" \
  "buffer_load_dwordx4 v1, s[36:39], s71 offen sc1 lds\n" \
  "v_mfma_f32_16x16x32_bf16 v[116:119], a[124:127], v[52:55], v[116:119]\n" \
  "v_mfma_f32_16x16x32_bf16 v[108:111], a[204:207], v[48:51], v[108:111]\n" \
  "v_mfma_f32_16x16x32_bf16 v[120:123], a[204:207], v[52:55], v[120:123]\n" \
  "s_waitcnt lgkmcnt(3)\n" \
  "v_mfma_f32_16x16x32_bf16 v[100:103], a[48:51], v[32:35], v[100:103]\n" \
  "s_waitcnt lgkmcnt(2)\n" \
  "ds_read_b128 v[48:51], v0 offset:14336\n" \
  "v_mfma_f32_16x16x32_bf16 v[112:115], a[48:51], v[36:39], v[112:115]\n" \
  "ds_read_b128 v[52:55], v0 offset:34816\n" \
  "v_mfma_f32_16x16x32_bf16 v[104:107], a[128:131], v[32:35], v[104:107]\n" \
  "buffer_load_dwordx4 v1, s[36:39], s71 offen offset:1024 sc1 lds\n" \
  "v_mfma_f32_16x16x32_bf16 v[116:119], a[128:131], v[36:39], v[116:119]\n" \
  "v_mfma_f32_16x16x32_bf16 v[108:111], a[208:211], v[32:35], v[108:111]\n" \
  "s_waitcnt vmcnt(8)\n" \
  "v_cmp_gt_u32 vcc, s64, v11\n" \
  "v_mfma_f32_16x16x32_bf16 v[120:123], a[208:211], v[36:39], v[120:123]\n" \
  "s_add_u32 s68, s68, 40960\n" \
  "s_waitcnt lgkmcnt(3)\n" \
  "v_mfma_f32_16x16x32_bf16 v[100:103], a[52:55], v[40:43], v[100:103]\n" \
  "s_waitcnt lgkmcnt(2)\n" \
  "ds_read_b128 v[32:35], v0 offset:15360\n" \
  "v_mfma_f32_16x16x32_bf16 v[112:115], a[52:55], v[44:47], v[112:115]\n" \
  "s_cbranch_vccnz LP3_%=\n" \
  "LK3_%=:\n" \
  "ds_read_b128 v[36:39], v0 offset:35840\n" \
  "v_mfma_f32_16x16x32_bf16 v[104:107], a[132:135], v[40:43], v[104:107]\n" \
  "v_mfma_f32_16x16x32_bf16 v[116:119], a[132:135], v[44:47], v[116:119]\n" \
  "v_mfma_f32_16x16x32_bf16 v[108:111], a[212:215], v[40:43], v[108:111]\n" \
  "v_mfma_f32_16x16x32_bf16 v[120:123], a[212:215], v[44:47], v[120:123]\n" \
  "buffer_load_dwordx4 v[192:195], v6, s[40:43], s60 offen offset:0 sc1\n" \
  "s_waitcnt lgkmcnt(3)\n" \
  "v_mfma_f32_16x16x32_bf16 v[100:103], a[56:59], v[48:51], v[100:103]\n" \
  "s_waitcnt lgkmcnt(2)\n" \
  "ds_read_b128 v[40:43], v0 offset:16384\n" \
  "v_mfma_f32_16x16x32_bf16 v[112:115], a[56:59], v[52:55], v[112:115]\n" \
  "ds_read_b128 v[44:47], v0 offset:36864\n" \
  "v_mfma_f32_16x16x32_bf16 v[104:107], a[136:139], v[48:51], v[104:107]\n" \
  "buffer_load_dwordx4 v[196:199], v6, s[40:43], s60 offen offset:1024 sc1\n" \
  "v_mfma_f32_16x16x32_bf16 v[116:119], a[136:139], v[52:55], v[116:119]\n" \
  "v_mfma_f32_16x16x32_bf16 v[108:111], a[216:219], v[48:51], v[108:111]\n" \
  "v_mfma_f32_16x16x32_bf16 v[120:123], a[216:219], v[52:55], v[120:123]\n" \
  "buffer_load_dwordx4 v[200:203], v6, s[40:43], s60 offen offset:2048 sc1\n" \
  "s_waitcnt lgkmcnt(3)\n" \
  "v_mfma_f32_16x16x32_bf16 v[100:103], a[60:63], v[32:35], v[100:103]\n" \
  "s_waitcnt lgkmcnt(2)\n" \
  "ds_read_b128 v[48:51], v0 offset:17408\n" \
  "v_mfma_f32_16x16x32_bf16 v[112:115], a[60:63], v[36:39], v[112:115]\n" \
  "ds_read_b128 v[52:55], v0 offset:37888\n" \
  "v_mfma_f32_16x16x32_bf16 v[104:107], a[140:143], v[32:35], v[104:107]\n" \
  "buffer_load_dwordx4 v[204:207], v6, s[40:43], s60 offen offset:3072 sc1\n" \
  "v_mfma_f32_16x16x32_bf16 v[116:119], a[140:143], v[36:39], v[116:119]\n" \
  "v_mfma_f32_16x16x32_bf16 v[108:111], a[220:223], v[32:35], v[108:111]\n" \
  "v_mfma_f32_16x16x32_bf16 v[120:123], a[220:223], v[36:39], v[120:123]\n" \
  "buffer_load_dwordx2 v[208:209], v7, s[40:43], s60 offen offset:0 sc1\n" \
  "s_waitcnt lgkmcnt(3)\n" \
  "v_mfma_f32_16x16x32_bf16 v[100:103], a[64:67], v[40:43], v[100:103]\n" \
  "s_waitcnt lgkmcnt(2)\n" \
  "ds_read_b128 v[32:35], v0 offset:18432\n" \
  "v_mfma_f32_16x16x32_bf16 v[112:115], a[64:67], v[44:47], v[112:115]\n" \
  "ds_read_b128 v[36:39], v0 offset:38912\n" \
  "v_mfma_f32_16x16x32_bf16 v[104:107], a[144:147], v[40:43], v[104:107]\n" \
  "buffer_load_dwordx2 v[210:211], v7, s[40:43], s60 offen offset:1024 sc1\n" \
  "v_mfma_f32_16x16x32_bf16 v[116:119], a[144:147], v[44:47], v[116:119]\n" \
  "v_mfma_f32_16x16x32_bf16 v[108:111], a[224:227], v[40:43], v[108:111]\n" \
  "v_mfma_f32_16x16x32_bf16 v[120:123], a[224:227], v[44:47], v[120:123]\n" \
  "buffer_load_dwordx2 v[212:213], v7, s[40:43], s60 offen offset:2048 sc1\n" \
  "s_waitcnt lgkmcnt(3)\n" \
  "v_mfma_f32_16x16x32_bf16 v[100:103], a[68:71], v[48:51], v[100:103]\n" \
  "s_waitcnt lgkmcnt(2)\n" \
  "ds_read_b128 v[40:43], v0 offset:19456\n" \
  "v_mfma_f32_16x16x32_bf16 v[112:115], a[68:71], v[52:55], v[112:115]\n" \
  "ds_read_b128 v[44:47], v0 offset:39936\n" \
  "v_mfma_f32_16x16x32_bf16 v[104:107], a[148:151], v[48:51], v[104:107]\n" \
  "buffer_load_dwordx2 v[214:215], v7, s[40:43], s60 offen offset:3072 sc1\n" \
  "v_mfma_f32_16x16x32_bf16 v[116:119], a[148:151], v[52:55], v[116:119]\n" \
  "v_mfma_f32_16x16x32_bf16 v[108:111], a[228:231], v[48:51], v[108:111]\n" \
  "v_mfma_f32_16x16x32_bf16 v[120:123], a[228:231], v[52:55], v[120:123]\n" \
  "s_waitcnt vmcnt(33)\n" \
  "s_barrier\n" \
  "s_waitcnt lgkmcnt(3)\n" \
  "v_mfma_f32_16x16x32_bf16 v[100:103], a[72:75], v[32:35], v[100:103]\n" \
  "s_waitcnt lgkmcnt(2)\n" \
  "ds_read_b128 v[48:51], v9 offset:0\n" \
  "v_mfma_f32_16x16x32_bf16 v[112:115], a[72:75], v[36:39], v[112:115]\n" \
  "ds_read_b128 v[52:55], v9 offset:20480\n" \
  "v_mfma_f32_16x16x32_bf16 v[104:107], a[152:155], v[32:35], v[104:107]\n" \
  "v_mfma_f32_16x16x32_bf16 v[116:119], a[152:155], v[36:39], v[116:119]\n" \
  "v_mfma_f32_16x16x32_bf16 v[108:111], a[232:235], v[32:35], v[108:111]\n" \
  "v_mfma_f32_16x16x32_bf16 v[120:123], a[232:235], v[36:39], v[120:123]\n" \
  "s_waitcnt lgkmcnt(3)\n" \
  "v_mfma_f32_16x16x32_bf16 v[100:103], a[76:79], v[40:43], v[100:103]\n" \
  "s_waitcnt lgkmcnt(2)\n" \
  "ds_read_b128 v[32:35], v9 offset:1024\n" \
  "v_mfma_f32_16x16x32_bf16 v[112:115], a[76:79], v[44:47], v[112:115]\n" \
  "ds_read_b128 v[36:39], v9 offset:21504\n" \
  "v_mfma_f32_16x16x32_bf16 v[104:107], a[156:159], v[40:43], v[104:107]\n" \
  "v_mfma_f32_16x16x32_bf16 v[116:119], a[156:159], v[44:47], v[116:119]\n" \
  "v_mfma_f32_16x16x32_bf16 v[108:111], a[236:239], v[40:43], v[108:111]\n" \
  "v_mfma_f32_16x16x32_bf16 v[120:123], a[236:239], v[44:47], v[120:123]\n" \
  "s_add_u32 s52, s52, 1\n" \
  "s_waitcnt lgkmcnt(3)\n" \
  "v_mfma_f32_16x16x32_bf16 v[56:59], a[0:3], v[48:51], 0\n" \
  "s_add_u32 s63, s52, s54\n" \
  "s_waitcnt lgkmcnt(2)\n" \
  "ds_read_b128 v[40:43], v9 offset:2048\n" \
  "v_mfma_f32_16x16x32_bf16 v[68:71], a[0:3], v[52:55], 0\n" \
  "s_sub_u32 s64, s63, 2\n" \
  "ds_read_b128 v[44:47], v9 offset:22528\n" \
  "v_mfma_f32_16x16x32_bf16 v[60:63], a[80:83], v[48:51], 0\n" \
  "v_cvt_pk_bf16_f32 v16, v100, v101\n" \
  "v_cvt_pk_bf16_f32 v17, v102, v103\n" \
  "v_mfma_f32_16x16x32_bf16 v[72:75], a[80:83], v[52:55], 0\n" \
  "v_cvt_pk_bf16_f32 v18, v104, v105\n" \
  "v_cvt_pk_bf16_f32 v19, v106, v107\n" \
  "v_mfma_f32_16x16x32_bf16 v[64:67], a[160:163], v[48:51], 0\n" \
  "v_cvt_pk_bf16_f32 v20, v112, v113\n" \
  "v_cvt_pk_bf16_f32 v21, v114, v115\n" \
  "v_mfma_f32_16x16x32_bf16 v[76:79], a[160:163], v[52:55], 0\n" \
  "s_waitcnt lgkmcnt(3)\n" \
  "v_cvt_pk_bf16_f32 v22, v116, v117\n" \
  "v_cvt_pk_bf16_f32 v23, v118, v119\n" \
  "v_mfma_f32_16x16x32_bf16 v[56:59], a[4:7], v[32:35], v[56:59]\n" \
  "s_waitcnt lgkmcnt(2)\n" \
  "ds_read_b128 v[48:51], v9 offset:3072\n" \
  "v_mfma_f32_16x16x32_bf16 v[68:71], a[4:7], v[36:39], v[68:71]\n" \
  "s_barrier\n" \
  "ds_read_b128 v[52:55], v9 offset:23552\n" \
  "v_mfma_f32_16x16x32_bf16 v[60:63], a[84:87], v[32:35], v[60:63]\n" \
  "v_cvt_pk_bf16_f32 v24, v108, v109\n" \
  "v_cvt_pk_bf16_f32 v25, v110, v111\n" \
  "v_mfma_f32_16x16x32_bf16 v[72:75], a[84:87], v[36:39], v[72:75]\n" \
  "v_cvt_pk_bf16_f32 v26, v120, v121\n" \
  "v_cvt_pk_bf16_f32 v27, v122, v123\n" \
  "v_mfma_f32_16x16x32_bf16 v[64:67], a[164:167], v[32:35], v[64:67]\n" \
  "ds_write_b128 v2, v[16:19] offset:12288\n" \
  "v_mfma_f32_16x16x32_bf16 v[76:79], a[164:167], v[36:39], v[76:79]\n" \
  "s_waitcnt lgkmcnt(4)\n" \
  "v_mfma_f32_16x16x32_bf16 v[56:59], a[8:11], v[40:43], v[56:59]\n" \
  "s_waitcnt lgkmcnt(3)\n" \
  "ds_read_b128 v[32:35], v9 offset:4096\n" \
  "v_mfma_f32_16x16x32_bf16 v[68:71], a[8:11], v[44:47], v[68:71]\n" \
  "ds_read_b128 v[36:39], v9 offset:24576\n" \
  "v_mfma_f32_16x16x32_bf16 v[60:63], a[88:91], v[40:43], v[60:63]\n" \
  "ds_write_b128 v2, v[20:23] offset:13312\n" \
  "v_mfma_f32_16x16x32_bf16 v[72:75], a[88:91], v[44:47], v[72:75]\n" \
  "v_mfma_f32_16x16x32_bf16 v[64:67], a[168:171], v[40:43], v[64:67]\n" \
  "v_mfma_f32_16x16x32_bf16 v[76:79], a[168:171], v[44:47], v[76:79]\n" \
  "s_add_u32 s70, s68, 4096\n" \
  "s_add_u32 s71, s68, 8192\n" \
  "s_waitcnt lgkmcnt(5)\n" \
  "v_mfma_f32_16x16x32_bf16 v[56:59], a[12:15], v[48:51], v[56:59]\n" \
  "s_waitcnt lgkmcnt(4)\n" \
  "ds_write_b128 v2, v[24:27] offset:14336\n" \
  "ds_read_b128 v[40:43], v9 offset:5120\n" \
  "v_mfma_f32_16x16x32_bf16 v[68:71], a[12:15], v[52:55], v[68:71]\n" \
  "s_add_u32 m0, s53, 0\n" \
  "ds_read_b128 v[44:47], v9 offset:25600\n" \
  "v_mfma_f32_16x16x32_bf16 v[60:63], a[92:95], v[48:51], v[60:63]\n" \
  "buffer_load_dwordx4 v1, s[36:39], s68 offen sc1 lds\n" \
  "v_mfma_f32_16x16x32_bf16 v[72:75], a[92:95], v[52:55], v[72:75]\n" \
  "v_mfma_f32_16x16x32_bf16 v[64:67], a[172:175], v[48:51], v[64:67]\n" \
  "v_mfma_f32_16x16x32_bf16 v[76:79], a[172:175], v[52:55], v[76:79]\n" \
  "s_waitcnt lgkmcnt(5)\n" \
  "v_mfma_f32_16x16x32_bf16 v[56:59], a[16:19], v[32:35], v[56:59]\n" \
  "s_waitcnt lgkmcnt(4)\n" \
  "ds_read_b128 v[48:51], v9 offset:6144\n" \
  "v_mfma_f32_16x16x32_bf16 v[68:71], a[16:19], v[36:39], v[68:71]\n" \
  "ds_read_b128 v[52:55], v9 offset:26624\n" \
  "v_mfma_f32_16x16x32_bf16 v[60:63], a[96:99], v[32:35], v[60:63]\n" \
  "buffer_load_dwordx4 v1, s[36:39], s68 offen offset:1024 sc1 lds\n" \
  "v_mfma_f32_16x16x32_bf16 v[72:75], a[96:99], v[36:39], v[72:75]\n" \
  "v_mfma_f32_16x16x32_bf16 v[64:67], a[176:179], v[32:35], v[64:67]\n" \
  "v_readlane_b32 s61, v126, s64\n" \
  "v_mfma_f32_16x16x32_bf16 v[76:79], a[176:179], v[36:39], v[76:79]\n" \
  "s_sub_u32 s64, s52, 2\n" \
  "s_cmp_lt_u32 s64, 56\n" \
  "s_waitcnt lgkmcnt(3)\n" \
  "v_mfma_f32_16x16x32_bf16 v[56:59], a[20:23], v[40:43], v[56:59]\n" \
  "s_cselect_b32 s61, s61, 1073741824\n" \
  "s_waitcnt lgkmcnt(2)\n" \
  "ds_read_b128 v[32:35], v9 offset:7168\n" \
  "v_mfma_f32_16x16x32_bf16 v[68:71], a[20:23], v[44:47], v[68:71]\n" \
  "ds_read_b128 v[36:39], v9 offset:27648\n" \
  "v_mfma_f32_16x16x32_bf16 v[60:63], a[100:103], v[40:43], v[60:63]\n" \
  "buffer_load_dwordx4 v1, s[36:39], s68 offen offset:2048 sc1 lds\n" \
  "v_mfma_f32_16x16x32_bf16 v[72:75], a[100:103], v[44:47], v[72:75]\n" \
  "s_cmp_ge_u32 s52, 4\n" \
  "s_cselect_b32 s62, 0, 1073741824\n" \
  "v_mfma_f32_16x16x32_bf16 v[64:67], a[180:183], v[40:43], v[64:67]\n" \
  "s_waitcnt vmcnt(3) lgkmcnt(4)\n" \
  "s_barrier\n" \
  "v_mfma_f32_16x16x32_bf16 v[76:79], a[180:183], v[44:47], v[76:79]\n" \
  "buffer_atomic_add v12, v129, s[40:43], s62 offen\n" \
  "ds_read_b128 v[216:219], v3 offset:0\n" \
  "s_waitcnt lgkmcnt(4)\n" \
  "v_mfma_f32_16x16x32_bf16 v[56:59], a[24:27], v[48:51], v[56:59]\n" \
  "s_waitcnt lgkmcnt(3)\n" \
  "ds_read_b128 v[40:43], v9 offset:8192\n" \
  "ds_read_b128 v[220:223], v3 offset:3072\n" \
  "v_mfma_f32_16x16x32_bf16 v[68:71], a[24:27], v[52:55], v[68:71]\n" \
  "ds_read_b128 v[44:47], v9 offset:28672\n" \
  "ds_read_b128 v[224:227], v3 offset:6144\n" \
  "v_mfma_f32_16x16x32_bf16 v[60:63], a[104:107], v[48:51], v[60:63]\n" \
  "buffer_load_dwordx4 v1, s[36:39], s68 offen offset:3072 sc1 lds\n" \
  "ds_read_b128 v[228:231], v3 offset:9216\n" \
  "v_mfma_f32_16x16x32_bf16 v[72:75], a[104:107], v[52:55], v[72:75]\n" \
  "ds_read_b64 v[232:233], v4 offset:0\n" \
  "ds_read_b64 v[234:235], v4 offset:3072\n" \
  "v_mfma_f32_16x16x32_bf16 v[64:67], a[184:187], v[48:51], v[64:67]\n" \
  "ds_read_b64 v[236:237], v4 offset:6144\n" \
  "ds_read_b64 v[238:239], v4 offset:9216\n" \
  "v_mfma_f32_16x16x32_bf16 v[76:79], a[184:187], v[52:55], v[76:79]\n" \
  "s_waitcnt lgkmcnt(11)\n" \
  "v_swap_b32 v162, v184\n" \
  "v_mfma_f32_16x16x32_bf16 v[56:59], a[28:31], v[32:35], v[56:59]\n" \
  "s_waitcnt lgkmcnt(10)\n" \
  "ds_read_b128 v[48:51], v9 offset:9216\n" \
  "v_mfma_f32_16x16x32_bf16 v[68:71], a[28:31], v[36:39], v[68:71]\n" \
  "s_add_u32 m0, s53, 4096\n" \
  "ds_read_b128 v[52:55], v9 offset:29696\n" \
  "v_mfma_f32_16x16x32_bf16 v[60:63], a[108:111], v[32:35], v[60:63]\n" \
  "buffer_load_dwordx4 v1, s[36:39], s70 offen sc1 lds\n" \
  "v_mfma_f32_16x16x32_bf16 v[72:75], a[108:111], v[36:39], v[72:75]\n" \
  "v_swap_b32 v163, v185\n" \
  "v_mfma_f32_16x16x32_bf16 v[64:67], a[188:191], v[32:35], v[64:67]\n" \
  "v_swap_b32 v166, v188\n" \
  "v_mfma_f32_16x16x32_bf16 v[76:79], a[188:191], v[36:39], v[76:79]\n" \
  "s_waitcnt lgkmcnt(10)\n" \
  "v_swap_b32 v167, v189\n" \
  "v_mfma_f32_16x16x32_bf16 v[56:59], a[32:35], v[40:43], v[56:59]\n" \
  "s_waitcnt lgkmcnt(8)\n" \
  "ds_read_b128 v[32:35], v9 offset:10240\n" \
  "v_mfma_f32_16x16x32_bf16 v[68:71], a[32:35], v[44:47], v[68:71]\n" \
  "ds_read_b128 v[36:39], v9 offset:30720\n" \
  "v_mfma_f32_16x16x32_bf16 v[240:243], v[192:195], v[216:219], v[132:135]\n" \
  "v_mfma_f32_16x16x32_bf16 v[244:247], v[144:147], v[216:219], v[136:139]\n" \
  "v_mfma_f32_16x16x32_bf16 v[248:251], v[168:171], v[216:219], v[140:143]\n" \
  "v_mfma_f32_16x16x32_bf16 v[60:63], a[112:115], v[40:43], v[60:63]\n" \
  "buffer_load_dwordx4 v1, s[36:39], s70 offen offset:1024 sc1 lds\n" \
  "v_mfma_f32_16x16x32_bf16 v[240:243], v[196:199], v[220:223], v[240:243]\n" \
  "v_mfma_f32_16x16x32_bf16 v[244:247], v[148:151], v[220:223], v[244:247]\n" \
  "v_mfma_f32_16x16x32_bf16 v[248:251], v[172:175], v[220:223], v[248:251]\n" \
  "v_mfma_f32_16x16x32_bf16 v[72:75], a[112:115], v[44:47], v[72:75]\n" \
  "s_waitcnt lgkmcnt(9)\n" \
  "v_mfma_f32_16x16x32_bf16 v[240:243], v[200:203], v[224:227], v[240:243]\n" \
  "v_mfma_f32_16x16x32_bf16 v[244:247], v[152:155], v[224:227], v[244:247]\n" \
  "v_mfma_f32_16x16x32_bf16 v[248:251], v[176:179], v[224:227], v[248:251]\n" \
  "v_mfma_f32_16x16x32_bf16 v[64:67], a[192:195], v[40:43], v[64:67]\n" \
  "s_waitcnt lgkmcnt(8)\n" \
  "v_mfma_f32_16x16x32_bf16 v[240:243], v[204:207], v[228:231], v[240:243]\n" \
  "v_mfma_f32_16x16x32_bf16 v[244:247], v[156:159], v[228:231], v[244:247]\n" \
  "v_mfma_f32_16x16x32_bf16 v[248:251], v[180:183], v[228:231], v[248:251]\n" \
  "v_mfma_f32_16x16x32_bf16 v[76:79], a[192:195], v[44:47], v[76:79]\n" \
  "s_waitcnt lgkmcnt(6)\n" \
  "v_mfma_f32_16x16x32_bf16 v[240:243], v[208:211], v[232:235], v[240:243]\n" \
  "v_mfma_f32_16x16x32_bf16 v[244:247], v[160:163], v[232:235], v[244:247]\n" \
  "v_mfma_f32_16x16x32_bf16 v[248:251], v[184:187], v[232:235], v[248:251]\n" \
  "s_waitcnt lgkmcnt(3)\n" \
  "v_mfma_f32_16x16x32_bf16 v[56:59], a[36:39], v[48:51], v[56:59]\n" \
  "ds_read_b128 v[40:43], v9 offset:11264\n" \
  "v_mfma_f32_16x16x32_bf16 v[240:243], v[212:215], v[236:239], v[240:243]\n" \
  "v_mfma_f32_16x16x32_bf16 v[244:247], v[164:167], v[236:239], v[244:247]\n" \
  "v_mfma_f32_16x16x32_bf16 v[248:251], v[188:191], v[236:239], v[248:251]\n" \
  "s_waitcnt lgkmcnt(3)\n" \
  "v_mfma_f32_16x16x32_bf16 v[68:71], a[36:39], v[52:55], v[68:71]\n" \
  "ds_read_b128 v[44:47], v9 offset:31744\n" \
  "v_mfma_f32_16x16x32_bf16 v[60:63], a[116:119], v[48:51], v[60:63]\n" \
  "buffer_load_dwordx4 v1, s[36:39], s70 offen offset:2048 sc1 lds\n" \
  "v_mfma_f32_16x16x32_bf16 v[72:75], a[116:119], v[52:55], v[72:75]\n" \
  "v_cvt_pk_bf16_f32 v80, v240, v241\n" \
  "v_cvt_pk_bf16_f32 v81, v242, v243\n" \
  "v_mfma_f32_16x16x32_bf16 v[64:67], a[196:199], v[48:51], v[64:67]\n" \
  "v_cvt_pk_bf16_f32 v82, v244, v245\n" \
  "v_cvt_pk_bf16_f32 v83, v246, v247\n" \
  "v_mfma_f32_16x16x32_bf16 v[76:79], a[196:199], v[52:55], v[76:79]\n" \
  "s_waitcnt lgkmcnt(3)\n" \
  "v_cvt_pk_bf16_f32 v84, v248, v249\n" \
  "v_cvt_pk_bf16_f32 v85, v250, v251\n" \
  "v_mfma_f32_16x16x32_bf16 v[56:59], a[40:43], v[32:35], v[56:59]\n" \
  "s_waitcnt lgkmcnt(2)\n" \
  "ds_read_b128 v[48:51], v9 offset:12288\n" \
  "v_lshlrev_b32 v240, 16, v80\n" \
  "v_mfma_f32_16x16x32_bf16 v[68:71], a[40:43], v[36:39], v[68:71]\n" \
  "ds_read_b128 v[52:55], v9 offset:32768\n" \
  "v_and_b32 v241, 0xffff0000, v80\n" \
  "v_mfma_f32_16x16x32_bf16 v[60:63], a[120:123], v[32:35], v[60:63]\n" \
  "buffer_load_dwordx4 v1, s[36:39], s70 offen offset:3072 sc1 lds\n" \
  "v_mul_f32 v240, 0x3d93cd3a, v240\n" \
  "v_mfma_f32_16x16x32_bf16 v[72:75], a[120:123], v[36:39], v[72:75]\n" \
  "v_mul_f32 v241, 0x3d93cd3a, v241\n" \
  "v_cvt_pk_bf16_f32 v80, v240, v241\n" \
  "v_mfma_f32_16x16x32_bf16 v[64:67], a[200:203], v[32:35], v[64:67]\n" \
  "v_lshlrev_b32 v240, 16, v80\n" \
  "v_and_b32 v241, 0xffff0000, v80\n" \
  "v_mfma_f32_16x16x32_bf16 v[76:79], a[200:203], v[36:39], v[76:79]\n" \
  "s_waitcnt lgkmcnt(3)\n" \
  "v_lshlrev_b32 v242, 16, v81\n" \
  "v_and_b32 v243, 0xffff0000, v81\n" \
  "v_mfma_f32_16x16x32_bf16 v[56:59], a[44:47], v[40:43], v[56:59]\n" \
  "s_waitcnt lgkmcnt(2)\n" \
  "ds_read_b128 v[32:35], v9 offset:13312\n" \
  "v_mul_f32 v242, 0x3d93cd3a, v242\n" \
  "v_mfma_f32_16x16x32_bf16 v[68:71], a[44:47], v[44:47], v[68:71]\n" \
  "s_add_u32 m0, s53, 8192\n" \
  "ds_read_b128 v[36:39], v9 offset:33792\n" \
  "v_mul_f32 v243, 0x3d93cd3a, v243\n" \
  "v_mfma_f32_16x16x32_bf16 v[60:63], a[124:127], v[40:43], v[60:63]\n" \
  "buffer_load_dwordx4 v1, s[36:39], s71 offen sc1 lds\n" \
  "v_cvt_pk_bf16_f32 v81, v242, v243\n" \
  "v_mfma_f32_16x16x32_bf16 v[72:75], a[124:127], v[44:47], v[72:75]\n" \
  "v_lshlrev_b32 v242, 16, v81\n" \
  "v_and_b32 v243, 0xffff0000, v81\n" \
  "v_mfma_f32_16x16x32_bf16 v[64:67], a[204:207], v[40:43], v[64:67]\n" \
  "v_readlane_b32 s56, v124, s63\n" \
  "v_mfma_f32_16x16x32_bf16 v[76:79], a[204:207], v[44:47], v[76:79]\n" \
  "s_waitcnt lgkmcnt(3)\n" \
  "v_lshlrev_b32 v244, 16, v82\n" \
  "v_and_b32 v245, 0xffff0000, v82\n" \
  "v_mfma_f32_16x16x32_bf16 v[56:59], a[48:51], v[48:51], v[56:59]\n" \
  "s_waitcnt lgkmcnt(2)\n" \
  "ds_read_b128 v[40:43], v9 offset:14336\n" \
  "v_mul_f32 v244, 0x3d93cd3a, v244\n" \
  "v_mfma_f32_16x16x32_bf16 v[68:71], a[48:51], v[52:55], v[68:71]\n" \
  "ds_read_b128 v[44:47], v9 offset:34816\n" \
  "v_mul_f32 v245, 0x3d93cd3a, v245\n" \
  "v_mfma_f32_16x16x32_bf16 v[60:63], a[128:131], v[48:51], v[60:63]\n" \
  "buffer_load_dwordx4 v1, s[36:39], s71 offen offset:1024 sc1 lds\n" \
  "v_cvt_pk_bf16_f32 v82, v244, v245\n" \
  "v_mfma_f32_16x16x32_bf16 v[72:75], a[128:131], v[52:55], v[72:75]\n" \
  "s_add_u32 s57, s56, 16384\n" \
  "v_lshlrev_b32 v244, 16, v82\n" \
  "v_and_b32 v245, 0xffff0000, v82\n" \
  "v_mfma_f32_16x16x32_bf16 v[64:67], a[208:211], v[48:51], v[64:67]\n" \
  "s_add_u32 s58, s56, 32768\n" \
  "v_lshlrev_b32 v246, 16, v83\n" \
  "v_and_b32 v247, 0xffff0000, v83\n" \
  "v_mfma_f32_16x16x32_bf16 v[76:79], a[208:211], v[52:55], v[76:79]\n" \
  "s_add_u32 s68, s68, 40960\n" \
  "s_add_u32 s59, s56, 49152\n" \
  "s_waitcnt lgkmcnt(3)\n" \
  "v_mul_f32 v246, 0x3d93cd3a, v246\n" \
  "v_mfma_f32_16x16x32_bf16 v[56:59], a[52:55], v[32:35], v[56:59]\n" \
  "s_waitcnt lgkmcnt(2)\n" \
  "ds_read_b128 v[48:51], v9 offset:15360\n" \
  "v_mul_f32 v247, 0x3d93cd3a, v247\n" \
  "v_mfma_f32_16x16x32_bf16 v[68:71], a[52:55], v[36:39], v[68:71]\n" \
  "ds_read_b128 v[52:55], v9 offset:35840\n" \
  "v_cvt_pk_bf16_f32 v83, v246, v247\n" \
  "v_mfma_f32_16x16x32_bf16 v[60:63], a[132:135], v[32:35], v[60:63]\n" \
  "v_lshlrev_b32 v246, 16, v83\n" \
  "v_and_b32 v247, 0xffff0000, v83\n" \
  "v_mfma_f32_16x16x32_bf16 v[72:75], a[132:135], v[36:39], v[72:75]\n" \
  "v_lshlrev_b32 v248, 16, v84\n" \
  "v_and_b32 v249, 0xffff0000, v84\n" \
  "v_mfma_f32_16x16x32_bf16 v[64:67], a[212:215], v[32:35], v[64:67]\n" \
  "v_mul_f32 v248, 0x3d93cd3a, v248\n" \
  "v_mul_f32 v249, 0x3d93cd3a, v249\n" \
  "v_mfma_f32_16x16x32_bf16 v[76:79], a[212:215], v[36:39], v[76:79]\n" \
  "buffer_load_dwordx4 v[144:147], v5, s[40:43], s56 offen offset:0 sc1\n" \
  "v_cvt_pk_bf16_f32 v84, v248, v249\n" \
  "s_waitcnt lgkmcnt(3)\n" \
  "v_mfma_f32_16x16x32_bf16 v[56:59], a[56:59], v[40:43], v[56:59]\n" \
  "s_waitcnt lgkmcnt(2)\n" \
  "ds_read_b128 v[32:35], v9 offset:16384\n" \
  "v_lshlrev_b32 v248, 16, v84\n" \
  "v_mfma_f32_16x16x32_bf16 v[68:71], a[56:59], v[44:47], v[68:71]\n" \
  "ds_read_b128 v[36:39], v9 offset:36864\n" \
  "v_and_b32 v249, 0xffff0000, v84\n" \
  "v_mfma_f32_16x16x32_bf16 v[60:63], a[136:139], v[40:43], v[60:63]\n" \
  "v_lshlrev_b32 v250, 16, v85\n" \
  "v_and_b32 v251, 0xffff0000, v85\n" \
  "v_mfma_f32_16x16x32_bf16 v[72:75], a[136:139], v[44:47], v[72:75]\n" \
  "v_mul_f32 v250, 0x3d93cd3a, v250\n" \
  "v_mul_f32 v251, 0x3d93cd3a, v251\n" \
  "v_mfma_f32_16x16x32_bf16 v[64:67], a[216:219], v[40:43], v[64:67]\n" \
  "v_cvt_pk_bf16_f32 v85, v250, v251\n" \
  "v_lshlrev_b32 v250, 16, v85\n" \
  "v_mfma_f32_16x16x32_bf16 v[76:79], a[216:219], v[44:47], v[76:79]\n" \
  "buffer_load_dwordx4 v[168:171], v5, s[40:43], s56 offen offset:1024 sc1\n" \
  "v_and_b32 v251, 0xffff0000, v85\n" \
  "s_waitcnt lgkmcnt(3)\n" \
  "v_mfma_f32_16x16x32_bf16 v[56:59], a[60:63], v[48:51], v[56:59]\n" \
  "s_waitcnt lgkmcnt(2)\n" \
  "ds_read_b128 v[40:43], v9 offset:17408\n" \
  "v_max3_f32 v80, v240, v241, v242\n" \
  "v_mfma_f32_16x16x32_bf16 v[68:71], a[60:63], v[52:55], v[68:71]\n" \
  "ds_read_b128 v[44:47], v9 offset:37888\n" \
  "v_max3_f32 v80, v80, v243, v244\n" \
  "v_mfma_f32_16x16x32_bf16 v[60:63], a[140:143], v[48:51], v[60:63]\n" \
  "v_max3_f32 v80, v80, v245, v246\n" \
  "v_max3_f32 v80, v80, v247, v248\n" \
  "v_mfma_f32_16x16x32_bf16 v[72:75], a[140:143], v[52:55], v[72:75]\n" \
  "v_max3_f32 v80, v80, v249, v250\n" \
  "v_max_f32 v80, v80, v251\n" \
  "v_mfma_f32_16x16x32_bf16 v[64:67], a[220:223], v[48:51], v[64:67]\n" \
  "v_mov_b32 v81, v80\n" \
  "v_mfma_f32_16x16x32_bf16 v[76:79], a[220:223], v[52:55], v[76:79]\n" \
  "buffer_load_dwordx4 v[148:151], v5, s[40:43], s57 offen offset:0 sc1\n" \
  "s_waitcnt lgkmcnt(3)\n" \
  "v_mfma_f32_16x16x32_bf16 v[56:59], a[64:67], v[32:35], v[56:59]\n" \
  "s_waitcnt lgkmcnt(2)\n" \
  "ds_read_b128 v[48:51], v9 offset:18432\n" \
  "v_mfma_f32_16x16x32_bf16 v[68:71], a[64:67], v[36:39], v[68:71]\n" \
  "ds_read_b128 v[52:55], v9 offset:38912\n" \
  "v_mfma_f32_16x16x32_bf16 v[60:63], a[144:147], v[32:35], v[60:63]\n" \
  "v_permlane16_swap_b32 v80, v81\n" \
  "v_mfma_f32_16x16x32_bf16 v[72:75], a[144:147], v[36:39], v[72:75]\n" \
  "v_max_f32 v80, v80, v81\n" \
  "v_mov_b32 v81, v80\n" \
  "v_mfma_f32_16x16x32_bf16 v[64:67], a[224:227], v[32:35], v[64:67]\n" \
  "v_mfma_f32_16x16x32_bf16 v[76:79], a[224:227], v[36:39], v[76:79]\n" \
  "buffer_load_dwordx4 v[172:175], v5, s[40:43], s57 offen offset:1024 sc1\n" \
  "s_waitcnt lgkmcnt(3)\n" \
  "v_mfma_f32_16x16x32_bf16 v[56:59], a[68:71], v[40:43], v[56:59]\n" \
  "s_waitcnt lgkmcnt(2)\n" \
  "ds_read_b128 v[32:35], v9 offset:19456\n" \
  "v_mfma_f32_16x16x32_bf16 v[68:71], a[68:71], v[44:47], v[68:71]\n" \
  "ds_read_b128 v[36:39], v9 offset:39936\n" \
  "v_mfma_f32_16x16x32_bf16 v[60:63], a[148:151], v[40:43], v[60:63]\n" \
  "v_permlane32_swap_b32 v80, v81\n" \
  "v_mfma_f32_16x16x32_bf16 v[72:75], a[148:151], v[44:47], v[72:75]\n" \
  "v_max_f32 v80, v80, v81\n" \
  "v_mul_f32 v80, 0x3fb8aa3b, v80\n" \
  "v_mfma_f32_16x16x32_bf16 v[64:67], a[228:231], v[40:43], v[64:67]\n" \
  "v_mov_b32 v82, 0x3fb8aa3b\n" \
  "v_fma_f32 v240, v240, v82, -v80\n" \
  "v_mfma_f32_16x16x32_bf16 v[76:79], a[228:231], v[44:47], v[76:79]\n" \
  "buffer_load_dwordx4 v[152:155], v5, s[40:43], s58 offen offset:0 sc1\n" \
  "v_fma_f32 v241, v241, v82, -v80\n" \
  "s_waitcnt vmcnt(24)\n" \
  "s_barrier\n" \
  "s_waitcnt lgkmcnt(3)\n" \
  "v_mfma_f32_16x16x32_bf16 v[56:59], a[72:75], v[48:51], v[56:59]\n" \
  "s_waitcnt lgkmcnt(2)\n" \
  "ds_read_b128 v[40:43], v10 offset:0\n" \
  "v_fma_f32 v242, v242, v82, -v80\n" \
  "v_mfma_f32_16x16x32_bf16 v[68:71], a[72:75], v[52:55], v[68:71]\n" \
  "ds_read_b128 v[44:47], v10 offset:20480\n" \
  "v_fma_f32 v243, v243, v82, -v80\n" \
  "v_mfma_f32_16x16x32_bf16 v[60:63], a[152:155], v[48:51], v[60:63]\n" \
  "v_fma_f32 v244, v244, v82, -v80\n" \
  "v_fma_f32 v245, v245, v82, -v80\n" \
  "v_mfma_f32_16x16x32_bf16 v[72:75], a[152:155], v[52:55], v[72:75]\n" \
  "v_fma_f32 v246, v246, v82, -v80\n" \
  "v_fma_f32 v247, v247, v82, -v80\n" \
  "v_mfma_f32_16x16x32_bf16 v[64:67], a[232:235], v[48:51], v[64:67]\n" \
  "v_fma_f32 v248, v248, v82, -v80\n" \
  "v_fma_f32 v249, v249, v82, -v80\n" \
  "v_mfma_f32_16x16x32_bf16 v[76:79], a[232:235], v[52:55], v[76:79]\n" \
  "buffer_load_dwordx4 v[176:179], v5, s[40:43], s58 offen offset:1024 sc1\n" \
  "v_fma_f32 v250, v250, v82, -v80\n" \
  "s_waitcnt lgkmcnt(3)\n" \
  "v_mfma_f32_16x16x32_bf16 v[56:59], a[76:79], v[32:35], v[56:59]\n" \
  "s_waitcnt lgkmcnt(2)\n" \
  "ds_read_b128 v[48:51], v10 offset:1024\n" \
  "v_fma_f32 v251, v251, v82, -v80\n" \
  "v_mfma_f32_16x16x32_bf16 v[68:71], a[76:79], v[36:39], v[68:71]\n" \
  "ds_read_b128 v[52:55], v10 offset:21504\n" \
  "v_mfma_f32_16x16x32_bf16 v[60:63], a[156:159], v[32:35], v[60:63]\n" \
  "v_exp_f32 v240, v240\n" \
  "v_mfma_f32_16x16x32_bf16 v[72:75], a[156:159], v[36:39], v[72:75]\n" \
  "v_exp_f32 v241, v241\n" \
  "v_mfma_f32_16x16x32_bf16 v[64:67], a[236:239], v[32:35], v[64:67]\n" \
  "v_exp_f32 v242, v242\n" \
  "v_mfma_f32_16x16x32_bf16 v[76:79], a[236:239], v[36:39], v[76:79]\n" \
  "v_exp_f32 v243, v243\n" \
  "s_add_u32 s52, s52, 1\n" \
  "s_waitcnt lgkmcnt(3)\n" \
  "v_mfma_f32_16x16x32_bf16 v[100:103], a[0:3], v[40:43], 0\n" \
  "s_waitcnt lgkmcnt(2)\n" \
  "ds_read_b128 v[32:35], v10 offset:2048\n" \
  "v_mfma_f32_16x16x32_bf16 v[112:115], a[0:3], v[44:47], 0\n" \
  "ds_read_b128 v[36:39], v10 offset:22528\n" \
  "v_mfma_f32_16x16x32_bf16 v[104:107], a[80:83], v[40:43], 0\n" \
  "v_cvt_pk_bf16_f32 v16, v56, v57\n" \
  "v_cvt_pk_bf16_f32 v17, v58, v59\n" \
  "v_mfma_f32_16x16x32_bf16 v[116:119], a[80:83], v[44:47], 0\n" \
  "v_cvt_pk_bf16_f32 v18, v60, v61\n" \
  "v_cvt_pk_bf16_f32 v19, v62, v63\n" \
  "v_mfma_f32_16x16x32_bf16 v[108:111], a[160:163], v[40:43], 0\n" \
  "v_cvt_pk_bf16_f32 v20, v68, v69\n" \
  "v_cvt_pk_bf16_f32 v21, v70, v71\n" \
  "v_mfma_f32_16x16x32_bf16 v[120:123], a[160:163], v[44:47], 0\n" \
  "v_cvt_pk_bf16_f32 v22, v72, v73\n" \
  "v_cvt_pk_bf16_f32 v23, v74, v75\n" \
  "buffer_load_dwordx4 v[156:159], v5, s[40:43], s59 offen offset:0 sc1\n" \
  "s_waitcnt lgkmcnt(3)\n" \
  "v_mfma_f32_16x16x32_bf16 v[100:103], a[4:7], v[48:51], v[100:103]\n" \
  "s_waitcnt lgkmcnt(2)\n" \
  "ds_read_b128 v[40:43], v10 offset:3072\n" \
  "v_mfma_f32_16x16x32_bf16 v[112:115], a[4:7], v[52:55], v[112:115]\n" \
  "s_barrier\n" \
  "ds_read_b128 v[44:47], v10 offset:23552\n" \
  "v_mfma_f32_16x16x32_bf16 v[104:107], a[84:87], v[48:51], v[104:107]\n" \
  "v_cvt_pk_bf16_f32 v24, v64, v65\n" \
  "v_cvt_pk_bf16_f32 v25, v66, v67\n" \
  "v_mfma_f32_16x16x32_bf16 v[116:119], a[84:87], v[52:55], v[116:119]\n" \
  "v_cvt_pk_bf16_f32 v26, v76, v77\n" \
  "v_cvt_pk_bf16_f32 v27, v78, v79\n" \
  "v_mfma_f32_16x16x32_bf16 v[108:111], a[164:167], v[48:51], v[108:111]\n" \
  "ds_write_b128 v2, v[16:19] offset:0\n" \
  "v_mfma_f32_16x16x32_bf16 v[120:123], a[164:167], v[52:55], v[120:123]\n" \
  "buffer_load_dwordx4 v[180:183], v5, s[40:43], s59 offen offset:1024 sc1\n" \
  "s_waitcnt lgkmcnt(4)\n" \
  "v_mfma_f32_16x16x32_bf16 v[100:103], a[8:11], v[32:35], v[100:103]\n" \
  "s_waitcnt lgkmcnt(3)\n" \
  "ds_read_b128 v[48:51], v10 offset:4096\n" \
  "v_mfma_f32_16x16x32_bf16 v[112:115], a[8:11], v[36:39], v[112:115]\n" \
  "ds_read_b128 v[52:55], v10 offset:24576\n" \
  "v_mfma_f32_16x16x32_bf16 v[104:107], a[88:91], v[32:35], v[104:107]\n" \
  "ds_write_b128 v2, v[20:23] offset:1024\n" \
  "v_mfma_f32_16x16x32_bf16 v[116:119], a[88:91], v[36:39], v[116:119]\n" \
  "v_exp_f32 v244, v244\n" \
  "v_mfma_f32_16x16x32_bf16 v[108:111], a[168:171], v[32:35], v[108:111]\n" \
  "v_exp_f32 v245, v245\n" \
  "v_mfma_f32_16x16x32_bf16 v[120:123], a[168:171], v[36:39], v[120:123]\n" \
  "s_add_u32 s70, s68, 4096\n" \
  "s_add_u32 s71, s68, 8192\n" \
  "buffer_load_dwordx4 v[160:163], v5, s[40:43], s56 offen offset:2048 sc1\n" \
  "s_waitcnt lgkmcnt(5)\n" \
  "v_mfma_f32_16x16x32_bf16 v[100:103], a[12:15], v[40:43], v[100:103]\n" \
  "s_waitcnt lgkmcnt(4)\n" \
  "ds_write_b128 v2, v[24:27] offset:2048\n" \
  "ds_read_b128 v[32:35], v10 offset:5120\n" \
  "v_mfma_f32_16x16x32_bf16 v[112:115], a[12:15], v[44:47], v[112:115]\n" \
  "s_add_u32 m0, s53, 40960\n" \
  "ds_read_b128 v[36:39], v10 offset:25600\n" \
  "v_mfma_f32_16x16x32_bf16 v[104:107], a[92:95], v[40:43], v[104:107]\n" \
  "buffer_load_dwordx4 v1, s[36:39], s68 offen sc1 lds\n" \
  "v_mfma_f32_16x16x32_bf16 v[116:119], a[92:95], v[44:47], v[116:119]\n" \
  "v_exp_f32 v246, v246\n" \
  "v_mfma_f32_16x16x32_bf16 v[108:111], a[172:175], v[40:43], v[108:111]\n" \
  "v_exp_f32 v247, v247\n" \
  "v_mfma_f32_16x16x32_bf16 v[120:123], a[172:175], v[44:47], v[120:123]\n" \
  "buffer_load_dwordx4 v[184:187], v5, s[40:43], s57 offen offset:2048 sc1\n" \
  "s_waitcnt lgkmcnt(5)\n" \
  "v_mfma_f32_16x16x32_bf16 v[100:103], a[16:19], v[48:51], v[100:103]\n" \
  "s_waitcnt lgkmcnt(4)\n" \
  "ds_read_b128 v[40:43], v10 offset:6144\n" \
  "v_mfma_f32_16x16x32_bf16 v[112:115], a[16:19], v[52:55], v[112:115]\n" \
  "ds_read_b128 v[44:47], v10 offset:26624\n" \
  "v_mfma_f32_16x16x32_bf16 v[104:107], a[96:99], v[48:51], v[104:107]\n" \
  "buffer_load_dwordx4 v1, s[36:39], s68 offen offset:1024 sc1 lds\n" \
  "v_mfma_f32_16x16x32_bf16 v[116:119], a[96:99], v[52:55], v[116:119]\n" \
  "v_exp_f32 v248, v248\n" \
  "v_mfma_f32_16x16x32_bf16 v[108:111], a[176:179], v[48:51], v[108:111]\n" \
  "v_exp_f32 v249, v249\n" \
  "v_mfma_f32_16x16x32_bf16 v[120:123], a[176:179], v[52:55], v[120:123]\n" \
  "buffer_load_dwordx4 v[164:167], v5, s[40:43], s58 offen offset:2048 sc1\n" \
  "s_waitcnt lgkmcnt(3)\n" \
  "v_mfma_f32_16x16x32_bf16 v[100:103], a[20:23], v[32:35], v[100:103]\n" \
  "s_waitcnt lgkmcnt(2)\n" \
  "ds_read_b128 v[48:51], v10 offset:7168\n" \
  "v_mfma_f32_16x16x32_bf16 v[112:115], a[20:23], v[36:39], v[112:115]\n" \
  "ds_read_b128 v[52:55], v10 offset:27648\n" \
  "v_mfma_f32_16x16x32_bf16 v[104:107], a[100:103], v[32:35], v[104:107]\n" \
  "buffer_load_dwordx4 v1, s[36:39], s68 offen offset:2048 sc1 lds\n" \
  "v_mfma_f32_16x16x32_bf16 v[116:119], a[100:103], v[36:39], v[116:119]\n" \
  "v_exp_f32 v250, v250\n" \
  "v_mfma_f32_16x16x32_bf16 v[108:111], a[180:183], v[32:35], v[108:111]\n" \
  "v_exp_f32 v251, v251\n" \
  "v_mfma_f32_16x16x32_bf16 v[120:123], a[180:183], v[36:39], v[120:123]\n" \
  "buffer_load_dwordx4 v[188:191], v5, s[40:43], s59 offen offset:2048 sc1\n" \
  "v_add_f32 v80, v240, v241\n" \
  "s_waitcnt lgkmcnt(3)\n" \
  "v_mfma_f32_16x16x32_bf16 v[100:103], a[24:27], v[40:43], v[100:103]\n" \
  "s_waitcnt lgkmcnt(2)\n" \
  "ds_read_b128 v[32:35], v10 offset:8192\n" \
  "v_add_f32 v80, v80, v242\n" \
  "v_mfma_f32_16x16x32_bf16 v[112:115], a[24:27], v[44:47], v[112:115]\n" \
  "ds_read_b128 v[36:39], v10 offset:28672\n" \
  "v_add_f32 v80, v80, v243\n" \
  "v_mfma_f32_16x16x32_bf16 v[104:107], a[104:107], v[40:43], v[104:107]\n" \
  "buffer_load_dwordx4 v1, s[36:39], s68 offen offset:3072 sc1 lds\n" \
  "v_add_f32 v80, v80, v244\n" \
  "v_mfma_f32_16x16x32_bf16 v[116:119], a[104:107], v[44:47], v[116:119]\n" \
  "v_add_f32 v80, v80, v245\n" \
  "v_add_f32 v80, v80, v246\n" \
  "v_mfma_f32_16x16x32_bf16 v[108:111], a[184:187], v[40:43], v[108:111]\n" \
  "v_add_f32 v80, v80, v247\n" \
  "v_add_f32 v80, v80, v248\n" \
  "v_mfma_f32_16x16x32_bf16 v[120:123], a[184:187], v[44:47], v[120:123]\n" \
  "buffer_load_dword v11, v8, s[40:43], 0 offen sc1\n" \
  "v_add_f32 v80, v80, v249\n" \
  "s_waitcnt lgkmcnt(3)\n" \
  "v_mfma_f32_16x16x32_bf16 v[100:103], a[28:31], v[48:51], v[100:103]\n" \
  "s_waitcnt lgkmcnt(2)\n" \
  "ds_read_b128 v[40:43], v10 offset:9216\n" \
  "v_add_f32 v80, v80, v250\n" \
  "v_mfma_f32_16x16x32_bf16 v[112:115], a[28:31], v[52:55], v[112:115]\n" \
  "s_add_u32 m0, s53, 45056\n" \
  "ds_read_b128 v[44:47], v10 offset:29696\n" \
  "v_add_f32 v80, v80, v251\n" \
  "v_mfma_f32_16x16x32_bf16 v[104:107], a[108:111], v[48:51], v[104:107]\n" \
  "buffer_load_dwordx4 v1, s[36:39], s70 offen sc1 lds\n" \
  "v_mov_b32 v81, v80\n" \
  "v_mfma_f32_16x16x32_bf16 v[116:119], a[108:111], v[52:55], v[116:119]\n" \
  "v_mfma_f32_16x16x32_bf16 v[108:111], a[188:191], v[48:51], v[108:111]\n" \
  "v_readlane_b32 s60, v125, s63\n" \
  "v_mfma_f32_16x16x32_bf16 v[120:123], a[188:191], v[52:55], v[120:123]\n" \
  "s_waitcnt lgkmcnt(3)\n" \
  "v_permlane16_swap_b32 v80, v81\n" \
  "v_mfma_f32_16x16x32_bf16 v[100:103], a[32:35], v[32:35], v[100:103]\n" \
  "s_waitcnt lgkmcnt(2)\n" \
  "ds_read_b128 v[48:51], v10 offset:10240\n" \
  "v_add_f32 v80, v80, v81\n" \
  "v_mfma_f32_16x16x32_bf16 v[112:115], a[32:35], v[36:39], v[112:115]\n" \
  "ds_read_b128 v[52:55], v10 offset:30720\n" \
  "v_mov_b32 v81, v80\n" \
  "v_mfma_f32_16x16x32_bf16 v[104:107], a[112:115], v[32:35], v[104:107]\n" \
  "buffer_load_dwordx4 v1, s[36:39], s70 offen offset:1024 sc1 lds\n" \
  "v_mfma_f32_16x16x32_bf16 v[116:119], a[112:115], v[36:39], v[116:119]\n" \
  "v_permlane32_swap_b32 v80, v81\n" \
  "v_mfma_f32_16x16x32_bf16 v[108:111], a[192:195], v[32:35], v[108:111]\n" \
  "s_add_u32 s64, s52, 2\n" \
  "s_min_u32 s64, s64, 57\n" \
  "v_add_f32 v80, v80, v81\n" \
  "v_mfma_f32_16x16x32_bf16 v[120:123], a[192:195], v[36:39], v[120:123]\n" \
  "s_waitcnt lgkmcnt(3)\n" \
  "v_rcp_f32 v80, v80\n" \
  "v_mfma_f32_16x16x32_bf16 v[100:103], a[36:39], v[40:43], v[100:103]\n" \
  "s_waitcnt lgkmcnt(2)\n" \
  "ds_read_b128 v[32:35], v10 offset:11264\n" \
  "v_mul_f32 v244, v244, v80\n" \
  "v_mfma_f32_16x16x32_bf16 v[112:115], a[36:39], v[44:47], v[112:115]\n" \
  "ds_read_b128 v[36:39], v10 offset:31744\n" \
  "v_mul_f32 v245, v245, v80\n" \
  "v_mfma_f32_16x16x32_bf16 v[104:107], a[116:119], v[40:43], v[104:107]\n" \
  "buffer_load_dwordx4 v1, s[36:39], s70 offen offset:2048 sc1 lds\n" \
  "v_mul_f32 v246, v246, v80\n" \
  "v_mfma_f32_16x16x32_bf16 v[116:119], a[116:119], v[44:47], v[116:119]\n" \
  "v_mul_f32 v247, v247, v80\n" \
  "v_mul_f32 v248, v248, v80\n" \
  "v_mfma_f32_16x16x32_bf16 v[108:111], a[196:199], v[40:43], v[108:111]\n" \
  "v_mul_f32 v249, v249, v80\n" \
  "v_mul_f32 v250, v250, v80\n" \
  "v_mfma_f32_16x16x32_bf16 v[120:123], a[196:199], v[44:47], v[120:123]\n" \
  "s_waitcnt lgkmcnt(3)\n" \
  "v_mul_f32 v251, v251, v80\n" \
  "v_mul_f32 v240, v240, v80\n" \
  "v_mfma_f32_16x16x32_bf16 v[100:103], a[40:43], v[48:51], v[100:103]\n" \
  "s_waitcnt lgkmcnt(2)\n" \
  "ds_read_b128 v[40:43], v10 offset:12288\n" \
  "v_mul_f32 v241, v241, v80\n" \
  "v_mfma_f32_16x16x32_bf16 v[112:115], a[40:43], v[52:55], v[112:115]\n" \
  "ds_read_b128 v[44:47], v10 offset:32768\n" \
  "v_mul_f32 v242, v242, v80\n" \
  "v_mfma_f32_16x16x32_bf16 v[104:107], a[120:123], v[48:51], v[104:107]\n" \
  "buffer_load_dwordx4 v1, s[36:39], s70 offen offset:3072 sc1 lds\n" \
  "v_mul_f32 v243, v243, v80\n" \
  "v_mfma_f32_16x16x32_bf16 v[116:119], a[120:123], v[52:55], v[116:119]\n" \
  "v_cvt_pk_bf16_f32 v88, v244, v245\n" \
  "v_cvt_pk_bf16_f32 v89, v246, v247\n" \
  "v_mfma_f32_16x16x32_bf16 v[108:111], a[200:203], v[48:51], v[108:111]\n" \
  "v_cvt_pk_bf16_f32 v90, v248, v249\n" \
  "v_cvt_pk_bf16_f32 v91, v250, v251\n" \
  "v_mfma_f32_16x16x32_bf16 v[120:123], a[200:203], v[52:55], v[120:123]\n" \
  "s_waitcnt lgkmcnt(3)\n" \
  "v_cvt_pk_bf16_f32 v92, v240, v241\n" \
  "v_cvt_pk_bf16_f32 v93, v242, v243\n" \
  "v_mfma_f32_16x16x32_bf16 v[100:103], a[44:47], v[32:35], v[100:103]\n" \
  "ds_read_b128 v[48:51], v10 offset:13312\n" \
  "buffer_store_dwordx4 v[88:91], v127, s[40:43], s61 offen sc1\n" \
  "s_waitcnt lgkmcnt(3)\n" \
  "v_mfma_f32_16x16x32_bf16 v[112:115], a[44:47], v[36:39], v[112:115]\n" \
  "s_add_u32 m0, s53, 49152\n" \
  "ds_read_b128 v[52:55], v10 offset:33792\n" \
  "buffer_store_dwordx2 v[92:93], v128, s[40:43], s61 offen sc1\n" \
  "v_mfma_f32_16x16x32_bf16 v[104:107], a[124:127], v[32:35], v[104:107]\n" \
  "buffer_load_dwordx4 v1, s[36:39], s71 offen sc1 lds\n" \
  "v_mfma_f32_16x16x32_bf16 v[116:119], a[124:127], v[36:39], v[116:119]\n" \
  "v_mfma_f32_16x16x32_bf16 v[108:111], a[204:207], v[32:35], v[108:111]\n" \
  "v_mfma_f32_16x16x32_bf16 v[120:123], a[204:207], v[36:39], v[120:123]\n" \
  "s_waitcnt lgkmcnt(3)\n" \
  "v_mfma_f32_16x16x32_bf16 v[100:103], a[48:51], v[40:43], v[100:103]\n" \
  "s_waitcnt lgkmcnt(2)\n" \
  "ds_read_b128 v[32:35], v10 offset:14336\n" \
  "v_mfma_f32_16x16x32_bf16 v[112:115], a[48:51], v[44:47], v[112:115]\n" \
  "ds_read_b128 v[36:39], v10 offset:34816\n" \
  "v_mfma_f32_16x16x32_bf16 v[104:107], a[128:131], v[40:43], v[104:107]\n" \
  "buffer_load_dwordx4 v1, s[36:39], s71 offen offset:1024 sc1 lds\n" \
  "v_mfma_f32_16x16x32_bf16 v[116:119], a[128:131], v[44:47], v[116:119]\n" \
  "v_mfma_f32_16x16x32_bf16 v[108:111], a[208:211], v[40:43], v[108:111]\n" \
  "s_waitcnt vmcnt(8)\n" \
  "v_cmp_gt_u32 vcc, s64, v11\n" \
  "v_mfma_f32_16x16x32_bf16 v[120:123], a[208:211], v[44:47], v[120:123]\n" \
  "s_add_u32 s68, s68, 40960\n" \
  "s_waitcnt lgkmcnt(3)\n" \
  "v_mfma_f32_16x16x32_bf16 v[100:103], a[52:55], v[48:51], v[100:103]\n" \
  "s_waitcnt lgkmcnt(2)\n" \
  "ds_read_b128 v[40:43], v10 offset:15360\n" \
  "v_mfma_f32_16x16x32_bf16 v[112:115], a[52:55], v[52:55], v[112:115]\n" \
  "s_cbranch_vccnz LP5_%=\n" \
  "LK5_%=:\n" \
  "ds_read_b128 v[44:47], v10 offset:35840\n" \
  "v_mfma_f32_16x16x32_bf16 v[104:107], a[132:135], v[48:51], v[104:107]\n" \
  "v_mfma_f32_16x16x32_bf16 v[116:119], a[132:135], v[52:55], v[116:119]\n" \
  "v_mfma_f32_16x16x32_bf16 v[108:111], a[212:215], v[48:51], v[108:111]\n" \
  "v_mfma_f32_16x16x32_bf16 v[120:123], a[212:215], v[52:55], v[120:123]\n" \
  "buffer_load_dwordx4 v[192:195], v6, s[40:43], s60 offen offset:0 sc1\n" \
  "s_waitcnt lgkmcnt(3)\n" \
  "v_mfma_f32_16x16x32_bf16 v[100:103], a[56:59], v[32:35], v[100:103]\n" \
  "s_waitcnt lgkmcnt(2)\n" \
  "ds_read_b128 v[48:51], v10 offset:16384\n" \
  "v_mfma_f32_16x16x32_bf16 v[112:115], a[56:59], v[36:39], v[112:115]\n" \
  "ds_read_b128 v[52:55], v10 offset:36864\n" \
  "v_mfma_f32_16x16x32_bf16 v[104:107], a[136:139], v[32:35], v[104:107]\n" \
  "buffer_load_dwordx4 v[196:199], v6, s[40:43], s60 offen offset:1024 sc1\n" \
  "v_mfma_f32_16x16x32_bf16 v[116:119], a[136:139], v[36:39], v[116:119]\n" \
  "v_mfma_f32_16x16x32_bf16 v[108:111], a[216:219], v[32:35], v[108:111]\n" \
  "v_mfma_f32_16x16x32_bf16 v[120:123], a[216:219], v[36:39], v[120:123]\n" \
  "buffer_load_dwordx4 v[200:203], v6, s[40:43], s60 offen offset:2048 sc1\n" \
  "s_waitcnt lgkmcnt(3)\n" \
  "v_mfma_f32_16x16x32_bf16 v[100:103], a[60:63], v[40:43], v[100:103]\n" \
  "s_waitcnt lgkmcnt(2)\n" \
  "ds_read_b128 v[32:35], v10 offset:17408\n" \
  "v_mfma_f32_16x16x32_bf16 v[112:115], a[60:63], v[44:47], v[112:115]\n" \
  "ds_read_b128 v[36:39], v10 offset:37888\n" \
  "v_mfma_f32_16x16x32_bf16 v[104:107], a[140:143], v[40:43], v[104:107]\n" \
  "buffer_load_dwordx4 v[204:207], v6, s[40:43], s60 offen offset:3072 sc1\n" \
  "v_mfma_f32_16x16x32_bf16 v[116:119], a[140:143], v[44:47], v[116:119]\n" \
  "v_mfma_f32_16x16x32_bf16 v[108:111], a[220:223], v[40:43], v[108:111]\n" \
  "v_mfma_f32_16x16x32_bf16 v[120:123], a[220:223], v[44:47], v[120:123]\n" \
  "buffer_load_dwordx2 v[208:209], v7, s[40:43], s60 offen offset:0 sc1\n" \
  "s_waitcnt lgkmcnt(3)\n" \
  "v_mfma_f32_16x16x32_bf16 v[100:103], a[64:67], v[48:51], v[100:103]\n" \
  "s_waitcnt lgkmcnt(2)\n" \
  "ds_read_b128 v[40:43], v10 offset:18432\n" \
  "v_mfma_f32_16x16x32_bf16 v[112:115], a[64:67], v[52:55], v[112:115]\n" \
  "ds_read_b128 v[44:47], v10 offset:38912\n" \
  "v_mfma_f32_16x16x32_bf16 v[104:107], a[144:147], v[48:51], v[104:107]\n" \
  "buffer_load_dwordx2 v[210:211], v7, s[40:43], s60 offen offset:1024 sc1\n" \
  "v_mfma_f32_16x16x32_bf16 v[116:119], a[144:147], v[52:55], v[116:119]\n" \
  "v_mfma_f32_16x16x32_bf16 v[108:111], a[224:227], v[48:51], v[108:111]\n" \
  "v_mfma_f32_16x16x32_bf16 v[120:123], a[224:227], v[52:55], v[120:123]\n" \
  "buffer_load_dwordx2 v[212:213], v7, s[40:43], s60 offen offset:2048 sc1\n" \
  "s_waitcnt lgkmcnt(3)\n" \
  "v_mfma_f32_16x16x32_bf16 v[100:103], a[68:71], v[32:35], v[100:103]\n" \
  "s_waitcnt lgkmcnt(2)\n" \
  "ds_read_b128 v[48:51], v10 offset:19456\n" \
  "v_mfma_f32_16x16x32_bf16 v[112:115], a[68:71], v[36:39], v[112:115]\n" \
  "ds_read_b128 v[52:55], v10 offset:39936\n" \
  "v_mfma_f32_16x16x32_bf16 v[104:107], a[148:151], v[32:35], v[104:107]\n" \
  "buffer_load_dwordx2 v[214:215], v7, s[40:43], s60 offen offset:3072 sc1\n" \
  "v_mfma_f32_16x16x32_bf16 v[116:119], a[148:151], v[36:39], v[116:119]\n" \
  "v_mfma_f32_16x16x32_bf16 v[108:111], a[228:231], v[32:35], v[108:111]\n" \
  "v_mfma_f32_16x16x32_bf16 v[120:123], a[228:231], v[36:39], v[120:123]\n" \
  "s_waitcnt vmcnt(33)\n" \
  "s_barrier\n" \
  "s_waitcnt lgkmcnt(3)\n" \
  "v_mfma_f32_16x16x32_bf16 v[100:103], a[72:75], v[40:43], v[100:103]\n" \
  "s_waitcnt lgkmcnt(2)\n" \
  "ds_read_b128 v[32:35], v0 offset:0\n" \
  "v_mfma_f32_16x16x32_bf16 v[112:115], a[72:75], v[44:47], v[112:115]\n" \
  "ds_read_b128 v[36:39], v0 offset:20480\n" \
  "v_mfma_f32_16x16x32_bf16 v[104:107], a[152:155], v[40:43], v[104:107]\n" \
  "v_mfma_f32_16x16x32_bf16 v[116:119], a[152:155], v[44:47], v[116:119]\n" \
  "v_mfma_f32_16x16x32_bf16 v[108:111], a[232:235], v[40:43], v[108:111]\n" \
  "v_mfma_f32_16x16x32_bf16 v[120:123], a[232:235], v[44:47], v[120:123]\n" \
  "s_waitcnt lgkmcnt(3)\n" \
  "v_mfma_f32_16x16x32_bf16 v[100:103], a[76:79], v[48:51], v[100:103]\n" \
  "s_waitcnt lgkmcnt(2)\n" \
  "ds_read_b128 v[40:43], v0 offset:1024\n" \
  "v_mfma_f32_16x16x32_bf16 v[112:115], a[76:79], v[52:55], v[112:115]\n" \
  "ds_read_b128 v[44:47], v0 offset:21504\n" \
  "v_mfma_f32_16x16x32_bf16 v[104:107], a[156:159], v[48:51], v[104:107]\n" \
  "v_mfma_f32_16x16x32_bf16 v[116:119], a[156:159], v[52:55], v[116:119]\n" \
  "v_mfma_f32_16x16x32_bf16 v[108:111], a[236:239], v[48:51], v[108:111]\n" \
  "v_mfma_f32_16x16x32_bf16 v[120:123], a[236:239], v[52:55], v[120:123]\n" \
  "s_add_u32 s52, s52, 1\n" \
  "s_cmp_lt_u32 s52, 60\n" \
  "s_cbranch_scc1 LL_%=\n" \
  "s_waitcnt vmcnt(0) lgkmcnt(0)\n" \
  "s_branch LX_%=\n" \
  "LP1_%=:\n" \
  "s_add_u32 s65, s65, 1\n" \
  "s_cmp_gt_u32 s65, 262144\n" \
  "s_cbranch_scc1 LK1_%=\n" \
  "s_sleep 6\n" \
  "buffer_load_dword v11, v8, s[40:43], 0 offen sc1\n" \
  "s_waitcnt vmcnt(0)\n" \
  "v_cmp_gt_u32 vcc, s64, v11\n" \
  "s_cbranch_vccnz LP1_%=\n" \
  "s_branch LK1_%=\n" \
  "LP3_%=:\n" \
  "s_add_u32 s65, s65, 1\n" \
  "s_cmp_gt_u32 s65, 262144\n" \
  "s_cbranch_scc1 LK3_%=\n" \
  "s_sleep 6\n" \
  "buffer_load_dword v11, v8, s[40:43], 0 offen sc1\n" \
  "s_waitcnt vmcnt(0)\n" \
  "v_cmp_gt_u32 vcc, s64, v11\n" \
  "s_cbranch_vccnz LP3_%=\n" \
  "s_branch LK3_%=\n" \
  "LP5_%=:\n" \
  "s_add_u32 s65, s65, 1\n" \
  "s_cmp_gt_u32 s65, 262144\n" \
  "s_cbranch_scc1 LK5_%=\n" \
  "s_sleep 6\n" \
  "buffer_load_dword v11, v8, s[40:43], 0 offen sc1\n" \
  "s_waitcnt vmcnt(0)\n" \
  "v_cmp_gt_u32 vcc, s64, v11\n" \
  "s_cbranch_vccnz LP5_%=\n" \
  "s_branch LK5_%=\n" \
  "LX_%=:\n" \
  "s_nop 7\n"
#define PROJK_CLOBBERS \
  "v9", "v10", "v11", "v12", "v13", "v14", "v15", "v16", "v17", "v18", "v19", "v20", "v21", "v22", "v23", "v24", "v25", "v26", "v27", "v28", "v29", \
  "v30", "v31", "v32", "v33", "v34", "v35", "v36", "v37", "v38", "v39", "v40", "v41", "v42", "v43", "v44", "v45", "v46", "v47", "v48", "v49", "v50", \
  "v51", "v52", "v53", "v54", "v55", "v56", "v57", "v58", "v59", "v60", "v61", "v62", "v63", "v64", "v65", "v66", "v67", "v68", "v69", "v70", "v71", \
  "v72", "v73", "v74", "v75", "v76", "v77", "v78", "v79", "v100", "v101", "v102", "v103", "v104", "v105", "v106", "v107", "v108", "v109", "v110", \
  "v111", "v112", "v113", "v114", "v115", "v116", "v117", "v118", "v119", "v120", "v121", "v122", "v123", "s68", "s69", "s70", "s71", "s72", "s73"
#define PROJK_ASM \
  "s_waitcnt vmcnt(10)\n" \
  "s_barrier\n" \
  "ds_read_b128 v[32:35], v0 offset:0\n" \
  "ds_read_b128 v[36:39], v0 offset:20480\n" \
  "ds_read_b128 v[40:43], v0 offset:1024\n" \
  "ds_read_b128 v[44:47], v0 offset:21504\n" \
  "v_mov_b32 v12, -1\n" \
  "v_mov_b32 v13, -1\n" \
  "v_mov_b32 v14, -1\n" \
  "v_mov_b32 v15, -1\n" \
  "v_mov_b32 v11, 1\n" \
  "v_add_u32 v9, 40960, v0\n" \
  "v_add_u32 v10, 81920, v0\n" \
  "s_mul_i32 s68, s52, 40960\n" \
  "s_add_u32 s68, s68, 81920\n" \
  "s_mov_b32 s72, s52\n" \
  "s_waitcnt lgkmcnt(2)\n" \
  "v_mfma_f32_16x16x32_bf16 v[56:59], a[0:3], v[32:35], 0\n" \
  "ds_read_b128 v[48:51], v0 offset:2048\n" \
  "v_mfma_f32_16x16x32_bf16 v[68:71], a[0:3], v[36:39], 0\n" \
  "ds_read_b128 v[52:55], v0 offset:22528\n" \
  "v_mfma_f32_16x16x32_bf16 v[60:63], a[80:83], v[32:35], 0\n" \
  "v_mfma_f32_16x16x32_bf16 v[72:75], a[80:83], v[36:39], 0\n" \
  "v_mfma_f32_16x16x32_bf16 v[64:67], a[160:163], v[32:35], 0\n" \
  "v_mfma_f32_16x16x32_bf16 v[76:79], a[160:163], v[36:39], 0\n" \
  "s_waitcnt lgkmcnt(2)\n" \
  "s_barrier\n" \
  "v_mfma_f32_16x16x32_bf16 v[56:59], a[4:7], v[40:43], v[56:59]\n" \
  "ds_read_b128 v[32:35], v0 offset:3072\n" \
  "v_mfma_f32_16x16x32_bf16 v[68:71], a[4:7], v[44:47], v[68:71]\n" \
  "ds_read_b128 v[36:39], v0 offset:23552\n" \
  "v_mfma_f32_16x16x32_bf16 v[60:63], a[84:87], v[40:43], v[60:63]\n" \
  "v_mfma_f32_16x16x32_bf16 v[72:75], a[84:87], v[44:47], v[72:75]\n" \
  "v_mfma_f32_16x16x32_bf16 v[64:67], a[164:167], v[40:43], v[64:67]\n" \
  "v_mfma_f32_16x16x32_bf16 v[76:79], a[164:167], v[44:47], v[76:79]\n" \
  "s_waitcnt lgkmcnt(2)\n" \
  "v_mfma_f32_16x16x32_bf16 v[56:59], a[8:11], v[48:51], v[56:59]\n" \
  "ds_read_b128 v[40:43], v0 offset:4096\n" \
  "v_mfma_f32_16x16x32_bf16 v[68:71], a[8:11], v[52:55], v[68:71]\n" \
  "ds_read_b128 v[44:47], v0 offset:24576\n" \
  "v_mfma_f32_16x16x32_bf16 v[60:63], a[88:91], v[48:51], v[60:63]\n" \
  "v_mfma_f32_16x16x32_bf16 v[72:75], a[88:91], v[52:55], v[72:75]\n" \
  "v_mfma_f32_16x16x32_bf16 v[64:67], a[168:171], v[48:51], v[64:67]\n" \
  "v_mfma_f32_16x16x32_bf16 v[76:79], a[168:171], v[52:55], v[76:79]\n" \
  "s_waitcnt lgkmcnt(2)\n" \
  "v_mfma_f32_16x16x32_bf16 v[56:59], a[12:15], v[32:35], v[56:59]\n" \
  "ds_read_b128 v[48:51], v0 offset:5120\n" \
  "v_mfma_f32_16x16x32_bf16 v[68:71], a[12:15], v[36:39], v[68:71]\n" \
  "ds_read_b128 v[52:55], v0 offset:25600\n" \
  "v_mfma_f32_16x16x32_bf16 v[60:63], a[92:95], v[32:35], v[60:63]\n" \
  "v_mfma_f32_16x16x32_bf16 v[72:75], a[92:95], v[36:39], v[72:75]\n" \
  "v_mfma_f32_16x16x32_bf16 v[64:67], a[172:175], v[32:35], v[64:67]\n" \
  "v_mfma_f32_16x16x32_bf16 v[76:79], a[172:175], v[36:39], v[76:79]\n" \
  "s_waitcnt lgkmcnt(2)\n" \
  "v_mfma_f32_16x16x32_bf16 v[56:59], a[16:19], v[40:43], v[56:59]\n" \
  "ds_read_b128 v[32:35], v0 offset:6144\n" \
  "v_mfma_f32_16x16x32_bf16 v[68:71], a[16:19], v[44:47], v[68:71]\n" \
  "ds_read_b128 v[36:39], v0 offset:26624\n" \
  "v_mfma_f32_16x16x32_bf16 v[60:63], a[96:99], v[40:43], v[60:63]\n" \
  "v_mfma_f32_16x16x32_bf16 v[72:75], a[96:99], v[44:47], v[72:75]\n" \
  "v_mfma_f32_16x16x32_bf16 v[64:67], a[176:179], v[40:43], v[64:67]\n" \
  "v_mfma_f32_16x16x32_bf16 v[76:79], a[176:179], v[44:47], v[76:79]\n" \
  "s_waitcnt lgkmcnt(2)\n" \
  "v_mfma_f32_16x16x32_bf16 v[56:59], a[20:23], v[48:51], v[56:59]\n" \
  "ds_read_b128 v[40:43], v0 offset:7168\n" \
  "v_mfma_f32_16x16x32_bf16 v[68:71], a[20:23], v[52:55], v[68:71]\n" \
  "ds_read_b128 v[44:47], v0 offset:27648\n" \
  "v_mfma_f32_16x16x32_bf16 v[60:63], a[100:103], v[48:51], v[60:63]\n" \
  "v_mfma_f32_16x16x32_bf16 v[72:75], a[100:103], v[52:55], v[72:75]\n" \
  "v_mfma_f32_16x16x32_bf16 v[64:67], a[180:183], v[48:51], v[64:67]\n" \
  "v_mfma_f32_16x16x32_bf16 v[76:79], a[180:183], v[52:55], v[76:79]\n" \
  "s_waitcnt lgkmcnt(2)\n" \
  "v_mfma_f32_16x16x32_bf16 v[56:59], a[24:27], v[32:35], v[56:59]\n" \
  "ds_read_b128 v[48:51], v0 offset:8192\n" \
  "v_mfma_f32_16x16x32_bf16 v[68:71], a[24:27], v[36:39], v[68:71]\n" \
  "ds_read_b128 v[52:55], v0 offset:28672\n" \
  "v_mfma_f32_16x16x32_bf16 v[60:63], a[104:107], v[32:35], v[60:63]\n" \
  "v_mfma_f32_16x16x32_bf16 v[72:75], a[104:107], v[36:39], v[72:75]\n" \
  "v_mfma_f32_16x16x32_bf16 v[64:67], a[184:187], v[32:35], v[64:67]\n" \
  "v_mfma_f32_16x16x32_bf16 v[76:79], a[184:187], v[36:39], v[76:79]\n" \
  "s_waitcnt lgkmcnt(2)\n" \
  "v_mfma_f32_16x16x32_bf16 v[56:59], a[28:31], v[40:43], v[56:59]\n" \
  "ds_read_b128 v[32:35], v0 offset:9216\n" \
  "v_mfma_f32_16x16x32_bf16 v[68:71], a[28:31], v[44:47], v[68:71]\n" \
  "ds_read_b128 v[36:39], v0 offset:29696\n" \
  "v_mfma_f32_16x16x32_bf16 v[60:63], a[108:111], v[40:43], v[60:63]\n" \
  "s_add_u32 s70, s68, 4096\n" \
  "v_mfma_f32_16x16x32_bf16 v[72:75], a[108:111], v[44:47], v[72:75]\n" \
  "s_add_u32 s71, s68, 8192\n" \
  "v_mfma_f32_16x16x32_bf16 v[64:67], a[188:191], v[40:43], v[64:67]\n" \
  "s_add_u32 m0, s53, 81920\n" \
  "v_mfma_f32_16x16x32_bf16 v[76:79], a[188:191], v[44:47], v[76:79]\n" \
  "buffer_load_dwordx4 v1, s[36:39], s68 offen sc1 lds\n" \
  "s_waitcnt lgkmcnt(2)\n" \
  "v_mfma_f32_16x16x32_bf16 v[56:59], a[32:35], v[48:51], v[56:59]\n" \
  "ds_read_b128 v[40:43], v0 offset:10240\n" \
  "v_mfma_f32_16x16x32_bf16 v[68:71], a[32:35], v[52:55], v[68:71]\n" \
  "ds_read_b128 v[44:47], v0 offset:30720\n" \
  "v_mfma_f32_16x16x32_bf16 v[60:63], a[112:115], v[48:51], v[60:63]\n" \
  "buffer_load_dwordx4 v1, s[36:39], s68 offen offset:1024 sc1 lds\n" \
  "v_mfma_f32_16x16x32_bf16 v[72:75], a[112:115], v[52:55], v[72:75]\n" \
  "v_mfma_f32_16x16x32_bf16 v[64:67], a[192:195], v[48:51], v[64:67]\n" \
  "v_mfma_f32_16x16x32_bf16 v[76:79], a[192:195], v[52:55], v[76:79]\n" \
  "s_waitcnt lgkmcnt(2)\n" \
  "v_mfma_f32_16x16x32_bf16 v[56:59], a[36:39], v[32:35], v[56:59]\n" \
  "ds_read_b128 v[48:51], v0 offset:11264\n" \
  "v_mfma_f32_16x16x32_bf16 v[68:71], a[36:39], v[36:39], v[68:71]\n" \
  "ds_read_b128 v[52:55], v0 offset:31744\n" \
  "v_mfma_f32_16x16x32_bf16 v[60:63], a[116:119], v[32:35], v[60:63]\n" \
  "buffer_load_dwordx4 v1, s[36:39], s68 offen offset:2048 sc1 lds\n" \
  "v_mfma_f32_16x16x32_bf16 v[72:75], a[116:119], v[36:39], v[72:75]\n" \
  "v_mfma_f32_16x16x32_bf16 v[64:67], a[196:199], v[32:35], v[64:67]\n" \
  "v_mfma_f32_16x16x32_bf16 v[76:79], a[196:199], v[36:39], v[76:79]\n" \
  "s_waitcnt lgkmcnt(2)\n" \
  "v_mfma_f32_16x16x32_bf16 v[56:59], a[40:43], v[40:43], v[56:59]\n" \
  "ds_read_b128 v[32:35], v0 offset:12288\n" \
  "v_mfma_f32_16x16x32_bf16 v[68:71], a[40:43], v[44:47], v[68:71]\n" \
  "ds_read_b128 v[36:39], v0 offset:32768\n" \
  "v_mfma_f32_16x16x32_bf16 v[60:63], a[120:123], v[40:43], v[60:63]\n" \
  "buffer_load_dwordx4 v1, s[36:39], s68 offen offset:3072 sc1 lds\n" \
  "v_mfma_f32_16x16x32_bf16 v[72:75], a[120:123], v[44:47], v[72:75]\n" \
  "v_mfma_f32_16x16x32_bf16 v[64:67], a[200:203], v[40:43], v[64:67]\n" \
  "v_mfma_f32_16x16x32_bf16 v[76:79], a[200:203], v[44:47], v[76:79]\n" \
  "s_waitcnt lgkmcnt(2)\n" \
  "v_mfma_f32_16x16x32_bf16 v[56:59], a[44:47], v[48:51], v[56:59]\n" \
  "ds_read_b128 v[40:43], v0 offset:13312\n" \
  "v_mfma_f32_16x16x32_bf16 v[68:71], a[44:47], v[52:55], v[68:71]\n" \
  "ds_read_b128 v[44:47], v0 offset:33792\n" \
  "v_mfma_f32_16x16x32_bf16 v[60:63], a[124:127], v[48:51], v[60:63]\n" \
  "s_add_u32 m0, s53, 86016\n" \
  "v_mfma_f32_16x16x32_bf16 v[72:75], a[124:127], v[52:55], v[72:75]\n" \
  "buffer_load_dwordx4 v1, s[36:39], s70 offen sc1 lds\n" \
  "v_mfma_f32_16x16x32_bf16 v[64:67], a[204:207], v[48:51], v[64:67]\n" \
  "v_mfma_f32_16x16x32_bf16 v[76:79], a[204:207], v[52:55], v[76:79]\n" \
  "s_waitcnt lgkmcnt(2)\n" \
  "v_mfma_f32_16x16x32_bf16 v[56:59], a[48:51], v[32:35], v[56:59]\n" \
  "ds_read_b128 v[48:51], v0 offset:14336\n" \
  "v_mfma_f32_16x16x32_bf16 v[68:71], a[48:51], v[36:39], v[68:71]\n" \
  "ds_read_b128 v[52:55], v0 offset:34816\n" \
  "v_mfma_f32_16x16x32_bf16 v[60:63], a[128:131], v[32:35], v[60:63]\n" \
  "buffer_load_dwordx4 v1, s[36:39], s70 offen offset:1024 sc1 lds\n" \
  "v_mfma_f32_16x16x32_bf16 v[72:75], a[128:131], v[36:39], v[72:75]\n" \
  "v_mfma_f32_16x16x32_bf16 v[64:67], a[208:211], v[32:35], v[64:67]\n" \
  "v_mfma_f32_16x16x32_bf16 v[76:79], a[208:211], v[36:39], v[76:79]\n" \
  "s_waitcnt lgkmcnt(2)\n" \
  "v_mfma_f32_16x16x32_bf16 v[56:59], a[52:55], v[40:43], v[56:59]\n" \
  "ds_read_b128 v[32:35], v0 offset:15360\n" \
  "v_mfma_f32_16x16x32_bf16 v[68:71], a[52:55], v[44:47], v[68:71]\n" \
  "ds_read_b128 v[36:39], v0 offset:35840\n" \
  "v_mfma_f32_16x16x32_bf16 v[60:63], a[132:135], v[40:43], v[60:63]\n" \
  "v_mfma_f32_16x16x32_bf16 v[72:75], a[132:135], v[44:47], v[72:75]\n" \
  "v_mfma_f32_16x16x32_bf16 v[64:67], a[212:215], v[40:43], v[64:67]\n" \
  "v_mfma_f32_16x16x32_bf16 v[76:79], a[212:215], v[44:47], v[76:79]\n" \
  "s_waitcnt lgkmcnt(2)\n" \
  "v_mfma_f32_16x16x32_bf16 v[56:59], a[56:59], v[48:51], v[56:59]\n" \
  "ds_read_b128 v[40:43], v0 offset:16384\n" \
  "v_mfma_f32_16x16x32_bf16 v[68:71], a[56:59], v[52:55], v[68:71]\n" \
  "ds_read_b128 v[44:47], v0 offset:36864\n" \
  "v_mfma_f32_16x16x32_bf16 v[60:63], a[136:139], v[48:51], v[60:63]\n" \
  "buffer_load_dwordx4 v1, s[36:39], s70 offen offset:2048 sc1 lds\n" \
  "v_mfma_f32_16x16x32_bf16 v[72:75], a[136:139], v[52:55], v[72:75]\n" \
  "v_readlane_b32 s69, v3, s72\n" \
  "v_mfma_f32_16x16x32_bf16 v[64:67], a[216:219], v[48:51], v[64:67]\n" \
  "s_add_u32 s72, s72, 1\n" \
  "v_mfma_f32_16x16x32_bf16 v[76:79], a[216:219], v[52:55], v[76:79]\n" \
  "s_waitcnt lgkmcnt(2)\n" \
  "v_mfma_f32_16x16x32_bf16 v[56:59], a[60:63], v[32:35], v[56:59]\n" \
  "ds_read_b128 v[48:51], v0 offset:17408\n" \
  "v_mfma_f32_16x16x32_bf16 v[68:71], a[60:63], v[36:39], v[68:71]\n" \
  "ds_read_b128 v[52:55], v0 offset:37888\n" \
  "v_mfma_f32_16x16x32_bf16 v[60:63], a[140:143], v[32:35], v[60:63]\n" \
  "buffer_load_dwordx4 v1, s[36:39], s70 offen offset:3072 sc1 lds\n" \
  "v_mfma_f32_16x16x32_bf16 v[72:75], a[140:143], v[36:39], v[72:75]\n" \
  "v_mfma_f32_16x16x32_bf16 v[64:67], a[220:223], v[32:35], v[64:67]\n" \
  "v_mfma_f32_16x16x32_bf16 v[76:79], a[220:223], v[36:39], v[76:79]\n" \
  "s_waitcnt lgkmcnt(2)\n" \
  "v_mfma_f32_16x16x32_bf16 v[56:59], a[64:67], v[40:43], v[56:59]\n" \
  "ds_read_b128 v[32:35], v0 offset:18432\n" \
  "v_mfma_f32_16x16x32_bf16 v[68:71], a[64:67], v[44:47], v[68:71]\n" \
  "ds_read_b128 v[36:39], v0 offset:38912\n" \
  "v_mfma_f32_16x16x32_bf16 v[60:63], a[144:147], v[40:43], v[60:63]\n" \
  "s_add_u32 m0, s53, 90112\n" \
  "v_mfma_f32_16x16x32_bf16 v[72:75], a[144:147], v[44:47], v[72:75]\n" \
  "buffer_load_dwordx4 v1, s[36:39], s71 offen sc1 lds\n" \
  "v_mfma_f32_16x16x32_bf16 v[64:67], a[224:227], v[40:43], v[64:67]\n" \
  "v_mfma_f32_16x16x32_bf16 v[76:79], a[224:227], v[44:47], v[76:79]\n" \
  "s_waitcnt lgkmcnt(2)\n" \
  "v_mfma_f32_16x16x32_bf16 v[56:59], a[68:71], v[48:51], v[56:59]\n" \
  "ds_read_b128 v[40:43], v0 offset:19456\n" \
  "v_mfma_f32_16x16x32_bf16 v[68:71], a[68:71], v[52:55], v[68:71]\n" \
  "ds_read_b128 v[44:47], v0 offset:39936\n" \
  "v_mfma_f32_16x16x32_bf16 v[60:63], a[148:151], v[48:51], v[60:63]\n" \
  "buffer_load_dwordx4 v1, s[36:39], s71 offen offset:1024 sc1 lds\n" \
  "v_mfma_f32_16x16x32_bf16 v[72:75], a[148:151], v[52:55], v[72:75]\n" \
  "s_add_u32 s68, s68, 40960\n" \
  "v_mfma_f32_16x16x32_bf16 v[64:67], a[228:231], v[48:51], v[64:67]\n" \
  "v_mfma_f32_16x16x32_bf16 v[76:79], a[228:231], v[52:55], v[76:79]\n" \
  "s_waitcnt vmcnt(10) lgkmcnt(2)\n" \
  "s_barrier\n" \
  "v_mfma_f32_16x16x32_bf16 v[56:59], a[72:75], v[32:35], v[56:59]\n" \
  "ds_read_b128 v[48:51], v9 offset:0\n" \
  "v_mfma_f32_16x16x32_bf16 v[68:71], a[72:75], v[36:39], v[68:71]\n" \
  "ds_read_b128 v[52:55], v9 offset:20480\n" \
  "v_mfma_f32_16x16x32_bf16 v[60:63], a[152:155], v[32:35], v[60:63]\n" \
  "v_mfma_f32_16x16x32_bf16 v[72:75], a[152:155], v[36:39], v[72:75]\n" \
  "v_mfma_f32_16x16x32_bf16 v[64:67], a[232:235], v[32:35], v[64:67]\n" \
  "v_mfma_f32_16x16x32_bf16 v[76:79], a[232:235], v[36:39], v[76:79]\n" \
  "s_waitcnt lgkmcnt(2)\n" \
  "v_mfma_f32_16x16x32_bf16 v[56:59], a[76:79], v[40:43], v[56:59]\n" \
  "ds_read_b128 v[32:35], v9 offset:1024\n" \
  "v_mfma_f32_16x16x32_bf16 v[68:71], a[76:79], v[44:47], v[68:71]\n" \
  "ds_read_b128 v[36:39], v9 offset:21504\n" \
  "v_mfma_f32_16x16x32_bf16 v[60:63], a[156:159], v[40:43], v[60:63]\n" \
  "s_cmp_eq_u32 s72, 64\n" \
  "v_mfma_f32_16x16x32_bf16 v[72:75], a[156:159], v[44:47], v[72:75]\n" \
  "v_mfma_f32_16x16x32_bf16 v[64:67], a[236:239], v[40:43], v[64:67]\n" \
  "v_mfma_f32_16x16x32_bf16 v[76:79], a[236:239], v[44:47], v[76:79]\n" \
  "s_waitcnt lgkmcnt(2)\n" \
  "v_mfma_f32_16x16x32_bf16 v[100:103], a[0:3], v[48:51], 0\n" \
  "ds_read_b128 v[40:43], v9 offset:2048\n" \
  "v_mfma_f32_16x16x32_bf16 v[112:115], a[0:3], v[52:55], 0\n" \
  "ds_read_b128 v[44:47], v9 offset:22528\n" \
  "v_mfma_f32_16x16x32_bf16 v[104:107], a[80:83], v[48:51], 0\n" \
  "v_cvt_pk_bf16_f32 v16, v56, v57\n" \
  "v_mfma_f32_16x16x32_bf16 v[116:119], a[80:83], v[52:55], 0\n" \
  "v_cvt_pk_bf16_f32 v17, v58, v59\n" \
  "v_mfma_f32_16x16x32_bf16 v[108:111], a[160:163], v[48:51], 0\n" \
  "v_cvt_pk_bf16_f32 v18, v60, v61\n" \
  "v_mfma_f32_16x16x32_bf16 v[120:123], a[160:163], v[52:55], 0\n" \
  "v_cvt_pk_bf16_f32 v19, v62, v63\n" \
  "s_waitcnt lgkmcnt(2)\n" \
  "s_barrier\n" \
  "v_mfma_f32_16x16x32_bf16 v[100:103], a[4:7], v[32:35], v[100:103]\n" \
  "ds_read_b128 v[48:51], v9 offset:3072\n" \
  "v_mfma_f32_16x16x32_bf16 v[112:115], a[4:7], v[36:39], v[112:115]\n" \
  "ds_read_b128 v[52:55], v9 offset:23552\n" \
  "v_mfma_f32_16x16x32_bf16 v[104:107], a[84:87], v[32:35], v[104:107]\n" \
  "buffer_store_dwordx4 v[16:19], v5, s[40:43], s69 offen offset:0 sc1\n" \
  "v_mfma_f32_16x16x32_bf16 v[116:119], a[84:87], v[36:39], v[116:119]\n" \
  "v_cvt_pk_bf16_f32 v20, v68, v69\n" \
  "v_mfma_f32_16x16x32_bf16 v[108:111], a[164:167], v[32:35], v[108:111]\n" \
  "v_cvt_pk_bf16_f32 v21, v70, v71\n" \
  "v_mfma_f32_16x16x32_bf16 v[120:123], a[164:167], v[36:39], v[120:123]\n" \
  "v_cvt_pk_bf16_f32 v22, v72, v73\n" \
  "s_waitcnt lgkmcnt(2)\n" \
  "v_mfma_f32_16x16x32_bf16 v[100:103], a[8:11], v[40:43], v[100:103]\n" \
  "ds_read_b128 v[32:35], v9 offset:4096\n" \
  "v_mfma_f32_16x16x32_bf16 v[112:115], a[8:11], v[44:47], v[112:115]\n" \
  "ds_read_b128 v[36:39], v9 offset:24576\n" \
  "v_mfma_f32_16x16x32_bf16 v[104:107], a[88:91], v[40:43], v[104:107]\n" \
  "v_cvt_pk_bf16_f32 v23, v74, v75\n" \
  "v_mfma_f32_16x16x32_bf16 v[116:119], a[88:91], v[44:47], v[116:119]\n" \
  "buffer_store_dwordx4 v[20:23], v5, s[40:43], s69 offen offset:1024 sc1\n" \
  "v_mfma_f32_16x16x32_bf16 v[108:111], a[168:171], v[40:43], v[108:111]\n" \
  "v_cvt_pk_bf16_f32 v24, v64, v65\n" \
  "v_mfma_f32_16x16x32_bf16 v[120:123], a[168:171], v[44:47], v[120:123]\n" \
  "v_cvt_pk_bf16_f32 v25, v66, v67\n" \
  "s_waitcnt lgkmcnt(2)\n" \
  "v_mfma_f32_16x16x32_bf16 v[100:103], a[12:15], v[48:51], v[100:103]\n" \
  "ds_read_b128 v[40:43], v9 offset:5120\n" \
  "v_mfma_f32_16x16x32_bf16 v[112:115], a[12:15], v[52:55], v[112:115]\n" \
  "ds_read_b128 v[44:47], v9 offset:25600\n" \
  "v_mfma_f32_16x16x32_bf16 v[104:107], a[92:95], v[48:51], v[104:107]\n" \
  "v_cvt_pk_bf16_f32 v26, v76, v77\n" \
  "v_mfma_f32_16x16x32_bf16 v[116:119], a[92:95], v[52:55], v[116:119]\n" \
  "v_cvt_pk_bf16_f32 v27, v78, v79\n" \
  "v_mfma_f32_16x16x32_bf16 v[108:111], a[172:175], v[48:51], v[108:111]\n" \
  "buffer_store_dwordx4 v[24:27], v5, s[40:43], s69 offen offset:2048 sc1\n" \
  "v_mfma_f32_16x16x32_bf16 v[120:123], a[172:175], v[52:55], v[120:123]\n" \
  "s_waitcnt lgkmcnt(2)\n" \
  "v_mfma_f32_16x16x32_bf16 v[100:103], a[16:19], v[32:35], v[100:103]\n" \
  "ds_read_b128 v[48:51], v9 offset:6144\n" \
  "v_mfma_f32_16x16x32_bf16 v[112:115], a[16:19], v[36:39], v[112:115]\n" \
  "ds_read_b128 v[52:55], v9 offset:26624\n" \
  "v_mfma_f32_16x16x32_bf16 v[104:107], a[96:99], v[32:35], v[104:107]\n" \
  "buffer_store_dwordx4 v[16:19], v2, s[40:43], s69 offen offset:0 sc1\n" \
  "v_mfma_f32_16x16x32_bf16 v[116:119], a[96:99], v[36:39], v[116:119]\n" \
  "v_mfma_f32_16x16x32_bf16 v[108:111], a[176:179], v[32:35], v[108:111]\n" \
  "v_mfma_f32_16x16x32_bf16 v[120:123], a[176:179], v[36:39], v[120:123]\n" \
  "s_waitcnt lgkmcnt(2)\n" \
  "v_mfma_f32_16x16x32_bf16 v[100:103], a[20:23], v[40:43], v[100:103]\n" \
  "ds_read_b128 v[32:35], v9 offset:7168\n" \
  "v_mfma_f32_16x16x32_bf16 v[112:115], a[20:23], v[44:47], v[112:115]\n" \
  "ds_read_b128 v[36:39], v9 offset:27648\n" \
  "v_mfma_f32_16x16x32_bf16 v[104:107], a[100:103], v[40:43], v[104:107]\n" \
  "buffer_store_dwordx4 v[20:23], v2, s[40:43], s69 offen offset:1024 sc1\n" \
  "v_mfma_f32_16x16x32_bf16 v[116:119], a[100:103], v[44:47], v[116:119]\n" \
  "v_mfma_f32_16x16x32_bf16 v[108:111], a[180:183], v[40:43], v[108:111]\n" \
  "v_mfma_f32_16x16x32_bf16 v[120:123], a[180:183], v[44:47], v[120:123]\n" \
  "s_waitcnt lgkmcnt(2)\n" \
  "v_mfma_f32_16x16x32_bf16 v[100:103], a[24:27], v[48:51], v[100:103]\n" \
  "ds_read_b128 v[40:43], v9 offset:8192\n" \
  "v_mfma_f32_16x16x32_bf16 v[112:115], a[24:27], v[52:55], v[112:115]\n" \
  "ds_read_b128 v[44:47], v9 offset:28672\n" \
  "v_mfma_f32_16x16x32_bf16 v[104:107], a[104:107], v[48:51], v[104:107]\n" \
  "buffer_store_dwordx4 v[24:27], v2, s[40:43], s69 offen offset:2048 sc1\n" \
  "v_mfma_f32_16x16x32_bf16 v[116:119], a[104:107], v[52:55], v[116:119]\n" \
  "s_nop 0\n" \
  "v_mfma_f32_16x16x32_bf16 v[108:111], a[184:187], v[48:51], v[108:111]\n" \
  "s_nop 0\n" \
  "v_mfma_f32_16x16x32_bf16 v[120:123], a[184:187], v[52:55], v[120:123]\n" \
  "s_waitcnt lgkmcnt(2)\n" \
  "v_mfma_f32_16x16x32_bf16 v[100:103], a[28:31], v[32:35], v[100:103]\n" \
  "ds_read_b128 v[48:51], v9 offset:9216\n" \
  "v_mfma_f32_16x16x32_bf16 v[112:115], a[28:31], v[36:39], v[112:115]\n" \
  "ds_read_b128 v[52:55], v9 offset:29696\n" \
  "v_mfma_f32_16x16x32_bf16 v[104:107], a[108:111], v[32:35], v[104:107]\n" \
  "s_add_u32 s70, s68, 4096\n" \
  "v_mfma_f32_16x16x32_bf16 v[116:119], a[108:111], v[36:39], v[116:119]\n" \
  "s_add_u32 s71, s68, 8192\n" \
  "v_mfma_f32_16x16x32_bf16 v[108:111], a[188:191], v[32:35], v[108:111]\n" \
  "s_add_u32 m0, s53, 0\n" \
  "v_mfma_f32_16x16x32_bf16 v[120:123], a[188:191], v[36:39], v[120:123]\n" \
  "buffer_load_dwordx4 v1, s[36:39], s68 offen sc1 lds\n" \
  "s_waitcnt lgkmcnt(2)\n" \
  "v_mfma_f32_16x16x32_bf16 v[100:103], a[32:35], v[40:43], v[100:103]\n" \
  "ds_read_b128 v[32:35], v9 offset:10240\n" \
  "v_mfma_f32_16x16x32_bf16 v[112:115], a[32:35], v[44:47], v[112:115]\n" \
  "ds_read_b128 v[36:39], v9 offset:30720\n" \
  "v_mfma_f32_16x16x32_bf16 v[104:107], a[112:115], v[40:43], v[104:107]\n" \
  "buffer_load_dwordx4 v1, s[36:39], s68 offen offset:1024 sc1 lds\n" \
  "v_mfma_f32_16x16x32_bf16 v[116:119], a[112:115], v[44:47], v[116:119]\n" \
  "v_mfma_f32_16x16x32_bf16 v[108:111], a[192:195], v[40:43], v[108:111]\n" \
  "v_mfma_f32_16x16x32_bf16 v[120:123], a[192:195], v[44:47], v[120:123]\n" \
  "s_waitcnt lgkmcnt(2)\n" \
  "v_mfma_f32_16x16x32_bf16 v[100:103], a[36:39], v[48:51], v[100:103]\n" \
  "ds_read_b128 v[40:43], v9 offset:11264\n" \
  "v_mfma_f32_16x16x32_bf16 v[112:115], a[36:39], v[52:55], v[112:115]\n" \
  "ds_read_b128 v[44:47], v9 offset:31744\n" \
  "v_mfma_f32_16x16x32_bf16 v[104:107], a[116:119], v[48:51], v[104:107]\n" \
  "buffer_load_dwordx4 v1, s[36:39], s68 offen offset:2048 sc1 lds\n" \
  "v_mfma_f32_16x16x32_bf16 v[116:119], a[116:119], v[52:55], v[116:119]\n" \
  "v_mfma_f32_16x16x32_bf16 v[108:111], a[196:199], v[48:51], v[108:111]\n" \
  "v_mfma_f32_16x16x32_bf16 v[120:123], a[196:199], v[52:55], v[120:123]\n" \
  "s_waitcnt lgkmcnt(2)\n" \
  "v_mfma_f32_16x16x32_bf16 v[100:103], a[40:43], v[32:35], v[100:103]\n" \
  "ds_read_b128 v[48:51], v9 offset:12288\n" \
  "v_mfma_f32_16x16x32_bf16 v[112:115], a[40:43], v[36:39], v[112:115]\n" \
  "ds_read_b128 v[52:55], v9 offset:32768\n" \
  "v_mfma_f32_16x16x32_bf16 v[104:107], a[120:123], v[32:35], v[104:107]\n" \
  "buffer_load_dwordx4 v1, s[36:39], s68 offen offset:3072 sc1 lds\n" \
  "v_mfma_f32_16x16x32_bf16 v[116:119], a[120:123], v[36:39], v[116:119]\n" \
  "v_mfma_f32_16x16x32_bf16 v[108:111], a[200:203], v[32:35], v[108:111]\n" \
  "v_mfma_f32_16x16x32_bf16 v[120:123], a[200:203], v[36:39], v[120:123]\n" \
  "s_waitcnt lgkmcnt(2)\n" \
  "v_mfma_f32_16x16x32_bf16 v[100:103], a[44:47], v[40:43], v[100:103]\n" \
  "ds_read_b128 v[32:35], v9 offset:13312\n" \
  "v_mfma_f32_16x16x32_bf16 v[112:115], a[44:47], v[44:47], v[112:115]\n" \
  "ds_read_b128 v[36:39], v9 offset:33792\n" \
  "v_mfma_f32_16x16x32_bf16 v[104:107], a[124:127], v[40:43], v[104:107]\n" \
  "s_add_u32 m0, s53, 4096\n" \
  "v_mfma_f32_16x16x32_bf16 v[116:119], a[124:127], v[44:47], v[116:119]\n" \
  "buffer_load_dwordx4 v1, s[36:39], s70 offen sc1 lds\n" \
  "v_mfma_f32_16x16x32_bf16 v[108:111], a[204:207], v[40:43], v[108:111]\n" \
  "v_mfma_f32_16x16x32_bf16 v[120:123], a[204:207], v[44:47], v[120:123]\n" \
  "s_waitcnt lgkmcnt(2)\n" \
  "v_mfma_f32_16x16x32_bf16 v[100:103], a[48:51], v[48:51], v[100:103]\n" \
  "ds_read_b128 v[40:43], v9 offset:14336\n" \
  "v_mfma_f32_16x16x32_bf16 v[112:115], a[48:51], v[52:55], v[112:115]\n" \
  "ds_read_b128 v[44:47], v9 offset:34816\n" \
  "v_mfma_f32_16x16x32_bf16 v[104:107], a[128:131], v[48:51], v[104:107]\n" \
  "buffer_load_dwordx4 v1, s[36:39], s70 offen offset:1024 sc1 lds\n" \
  "v_mfma_f32_16x16x32_bf16 v[116:119], a[128:131], v[52:55], v[116:119]\n" \
  "v_mfma_f32_16x16x32_bf16 v[108:111], a[208:211], v[48:51], v[108:111]\n" \
  "v_mfma_f32_16x16x32_bf16 v[120:123], a[208:211], v[52:55], v[120:123]\n" \
  "s_waitcnt vmcnt(6) lgkmcnt(2)\n" \
  "s_barrier\n" \
  "v_mfma_f32_16x16x32_bf16 v[100:103], a[52:55], v[32:35], v[100:103]\n" \
  "ds_read_b128 v[48:51], v9 offset:15360\n" \
  "v_mfma_f32_16x16x32_bf16 v[112:115], a[52:55], v[36:39], v[112:115]\n" \
  "ds_read_b128 v[52:55], v9 offset:35840\n" \
  "v_mfma_f32_16x16x32_bf16 v[104:107], a[132:135], v[32:35], v[104:107]\n" \
  "buffer_atomic_add v11, v4, s[40:43], s54 offen\n" \
  "v_mfma_f32_16x16x32_bf16 v[116:119], a[132:135], v[36:39], v[116:119]\n" \
  "s_add_u32 s54, s54, s55\n" \
  "v_mfma_f32_16x16x32_bf16 v[108:111], a[212:215], v[32:35], v[108:111]\n" \
  "s_and_b32 s54, s54, 0xffffdfff\n" \
  "v_mfma_f32_16x16x32_bf16 v[120:123], a[212:215], v[36:39], v[120:123]\n" \
  "s_waitcnt lgkmcnt(2)\n" \
  "v_mfma_f32_16x16x32_bf16 v[100:103], a[56:59], v[40:43], v[100:103]\n" \
  "ds_read_b128 v[32:35], v9 offset:16384\n" \
  "v_mfma_f32_16x16x32_bf16 v[112:115], a[56:59], v[44:47], v[112:115]\n" \
  "ds_read_b128 v[36:39], v9 offset:36864\n" \
  "v_mfma_f32_16x16x32_bf16 v[104:107], a[136:139], v[40:43], v[104:107]\n" \
  "buffer_load_dwordx4 v1, s[36:39], s70 offen offset:2048 sc1 lds\n" \
  "v_mfma_f32_16x16x32_bf16 v[116:119], a[136:139], v[44:47], v[116:119]\n" \
  "v_readlane_b32 s69, v3, s72\n" \
  "v_mfma_f32_16x16x32_bf16 v[108:111], a[216:219], v[40:43], v[108:111]\n" \
  "s_add_u32 s72, s72, 1\n" \
  "v_mfma_f32_16x16x32_bf16 v[120:123], a[216:219], v[44:47], v[120:123]\n" \
  "s_waitcnt lgkmcnt(2)\n" \
  "v_mfma_f32_16x16x32_bf16 v[100:103], a[60:63], v[48:51], v[100:103]\n" \
  "ds_read_b128 v[40:43], v9 offset:17408\n" \
  "v_mfma_f32_16x16x32_bf16 v[112:115], a[60:63], v[52:55], v[112:115]\n" \
  "ds_read_b128 v[44:47], v9 offset:37888\n" \
  "v_mfma_f32_16x16x32_bf16 v[104:107], a[140:143], v[48:51], v[104:107]\n" \
  "buffer_load_dwordx4 v1, s[36:39], s70 offen offset:3072 sc1 lds\n" \
  "v_mfma_f32_16x16x32_bf16 v[116:119], a[140:143], v[52:55], v[116:119]\n" \
  "v_mfma_f32_16x16x32_bf16 v[108:111], a[220:223], v[48:51], v[108:111]\n" \
  "v_mfma_f32_16x16x32_bf16 v[120:123], a[220:223], v[52:55], v[120:123]\n" \
  "s_waitcnt lgkmcnt(2)\n" \
  "v_mfma_f32_16x16x32_bf16 v[100:103], a[64:67], v[32:35], v[100:103]\n" \
  "ds_read_b128 v[48:51], v9 offset:18432\n" \
  "v_mfma_f32_16x16x32_bf16 v[112:115], a[64:67], v[36:39], v[112:115]\n" \
  "ds_read_b128 v[52:55], v9 offset:38912\n" \
  "v_mfma_f32_16x16x32_bf16 v[104:107], a[144:147], v[32:35], v[104:107]\n" \
  "s_add_u32 m0, s53, 8192\n" \
  "v_mfma_f32_16x16x32_bf16 v[116:119], a[144:147], v[36:39], v[116:119]\n" \
  "buffer_load_dwordx4 v1, s[36:39], s71 offen sc1 lds\n" \
  "v_mfma_f32_16x16x32_bf16 v[108:111], a[224:227], v[32:35], v[108:111]\n" \
  "v_mfma_f32_16x16x32_bf16 v[120:123], a[224:227], v[36:39], v[120:123]\n" \
  "s_waitcnt lgkmcnt(2)\n" \
  "v_mfma_f32_16x16x32_bf16 v[100:103], a[68:71], v[40:43], v[100:103]\n" \
  "ds_read_b128 v[32:35], v9 offset:19456\n" \
  "v_mfma_f32_16x16x32_bf16 v[112:115], a[68:71], v[44:47], v[112:115]\n" \
  "ds_read_b128 v[36:39], v9 offset:39936\n" \
  "v_mfma_f32_16x16x32_bf16 v[104:107], a[148:151], v[40:43], v[104:107]\n" \
  "buffer_load_dwordx4 v1, s[36:39], s71 offen offset:1024 sc1 lds\n" \
  "v_mfma_f32_16x16x32_bf16 v[116:119], a[148:151], v[44:47], v[116:119]\n" \
  "s_add_u32 s68, s68, 40960\n" \
  "v_mfma_f32_16x16x32_bf16 v[108:111], a[228:231], v[40:43], v[108:111]\n" \
  "v_mfma_f32_16x16x32_bf16 v[120:123], a[228:231], v[44:47], v[120:123]\n" \
  "s_waitcnt vmcnt(11) lgkmcnt(2)\n" \
  "s_barrier\n" \
  "v_mfma_f32_16x16x32_bf16 v[100:103], a[72:75], v[48:51], v[100:103]\n" \
  "ds_read_b128 v[40:43], v10 offset:0\n" \
  "v_mfma_f32_16x16x32_bf16 v[112:115], a[72:75], v[52:55], v[112:115]\n" \
  "ds_read_b128 v[44:47], v10 offset:20480\n" \
  "v_mfma_f32_16x16x32_bf16 v[104:107], a[152:155], v[48:51], v[104:107]\n" \
  "v_mfma_f32_16x16x32_bf16 v[116:119], a[152:155], v[52:55], v[116:119]\n" \
  "v_mfma_f32_16x16x32_bf16 v[108:111], a[232:235], v[48:51], v[108:111]\n" \
  "v_mfma_f32_16x16x32_bf16 v[120:123], a[232:235], v[52:55], v[120:123]\n" \
  "s_waitcnt lgkmcnt(2)\n" \
  "v_mfma_f32_16x16x32_bf16 v[100:103], a[76:79], v[32:35], v[100:103]\n" \
  "ds_read_b128 v[48:51], v10 offset:1024\n" \
  "v_mfma_f32_16x16x32_bf16 v[112:115], a[76:79], v[36:39], v[112:115]\n" \
  "ds_read_b128 v[52:55], v10 offset:21504\n" \
  "v_mfma_f32_16x16x32_bf16 v[104:107], a[156:159], v[32:35], v[104:107]\n" \
  "s_cmp_eq_u32 s72, 64\n" \
  "v_mfma_f32_16x16x32_bf16 v[116:119], a[156:159], v[36:39], v[116:119]\n" \
  "v_mfma_f32_16x16x32_bf16 v[108:111], a[236:239], v[32:35], v[108:111]\n" \
  "v_mfma_f32_16x16x32_bf16 v[120:123], a[236:239], v[36:39], v[120:123]\n" \
  "s_cbranch_scc1 LPE_%=\n" \
  "s_waitcnt lgkmcnt(2)\n" \
  "v_mfma_f32_16x16x32_bf16 v[56:59], a[0:3], v[40:43], 0\n" \
  "ds_read_b128 v[32:35], v10 offset:2048\n" \
  "v_mfma_f32_16x16x32_bf16 v[68:71], a[0:3], v[44:47], 0\n" \
  "ds_read_b128 v[36:39], v10 offset:22528\n" \
  "v_mfma_f32_16x16x32_bf16 v[60:63], a[80:83], v[40:43], 0\n" \
  "v_cvt_pk_bf16_f32 v16, v100, v101\n" \
  "v_mfma_f32_16x16x32_bf16 v[72:75], a[80:83], v[44:47], 0\n" \
  "v_cvt_pk_bf16_f32 v17, v102, v103\n" \
  "v_mfma_f32_16x16x32_bf16 v[64:67], a[160:163], v[40:43], 0\n" \
  "v_cvt_pk_bf16_f32 v18, v104, v105\n" \
  "v_mfma_f32_16x16x32_bf16 v[76:79], a[160:163], v[44:47], 0\n" \
  "v_cvt_pk_bf16_f32 v19, v106, v107\n" \
  "s_waitcnt lgkmcnt(2)\n" \
  "s_barrier\n" \
  "v_mfma_f32_16x16x32_bf16 v[56:59], a[4:7], v[48:51], v[56:59]\n" \
  "ds_read_b128 v[40:43], v10 offset:3072\n" \
  "v_mfma_f32_16x16x32_bf16 v[68:71], a[4:7], v[52:55], v[68:71]\n" \
  "ds_read_b128 v[44:47], v10 offset:23552\n" \
  "v_mfma_f32_16x16x32_bf16 v[60:63], a[84:87], v[48:51], v[60:63]\n" \
  "buffer_store_dwordx4 v[16:19], v5, s[40:43], s69 offen offset:0 sc1\n" \
  "v_mfma_f32_16x16x32_bf16 v[72:75], a[84:87], v[52:55], v[72:75]\n" \
  "v_cvt_pk_bf16_f32 v20, v112, v113\n" \
  "v_mfma_f32_16x16x32_bf16 v[64:67], a[164:167], v[48:51], v[64:67]\n" \
  "v_cvt_pk_bf16_f32 v21, v114, v115\n" \
  "v_mfma_f32_16x16x32_bf16 v[76:79], a[164:167], v[52:55], v[76:79]\n" \
  "v_cvt_pk_bf16_f32 v22, v116, v117\n" \
  "s_waitcnt lgkmcnt(2)\n" \
  "v_mfma_f32_16x16x32_bf16 v[56:59], a[8:11], v[32:35], v[56:59]\n" \
  "ds_read_b128 v[48:51], v10 offset:4096\n" \
  "v_mfma_f32_16x16x32_bf16 v[68:71], a[8:11], v[36:39], v[68:71]\n" \
  "ds_read_b128 v[52:55], v10 offset:24576\n" \
  "v_mfma_f32_16x16x32_bf16 v[60:63], a[88:91], v[32:35], v[60:63]\n" \
  "v_cvt_pk_bf16_f32 v23, v118, v119\n" \
  "v_mfma_f32_16x16x32_bf16 v[72:75], a[88:91], v[36:39], v[72:75]\n" \
  "buffer_store_dwordx4 v[20:23], v5, s[40:43], s69 offen offset:1024 sc1\n" \
  "v_mfma_f32_16x16x32_bf16 v[64:67], a[168:171], v[32:35], v[64:67]\n" \
  "v_cvt_pk_bf16_f32 v24, v108, v109\n" \
  "v_mfma_f32_16x16x32_bf16 v[76:79], a[168:171], v[36:39], v[76:79]\n" \
  "v_cvt_pk_bf16_f32 v25, v110, v111\n" \
  "s_waitcnt lgkmcnt(2)\n" \
  "v_mfma_f32_16x16x32_bf16 v[56:59], a[12:15], v[40:43], v[56:59]\n" \
  "ds_read_b128 v[32:35], v10 offset:5120\n" \
  "v_mfma_f32_16x16x32_bf16 v[68:71], a[12:15], v[44:47], v[68:71]\n" \
  "ds_read_b128 v[36:39], v10 offset:25600\n" \
  "v_mfma_f32_16x16x32_bf16 v[60:63], a[92:95], v[40:43], v[60:63]\n" \
  "v_cvt_pk_bf16_f32 v26, v120, v121\n" \
  "v_mfma_f32_16x16x32_bf16 v[72:75], a[92:95], v[44:47], v[72:75]\n" \
  "v_cvt_pk_bf16_f32 v27, v122, v123\n" \
  "v_mfma_f32_16x16x32_bf16 v[64:67], a[172:175], v[40:43], v[64:67]\n" \
  "buffer_store_dwordx4 v[24:27], v5, s[40:43], s69 offen offset:2048 sc1\n" \
  "v_mfma_f32_16x16x32_bf16 v[76:79], a[172:175], v[44:47], v[76:79]\n" \
  "s_waitcnt lgkmcnt(2)\n" \
  "v_mfma_f32_16x16x32_bf16 v[56:59], a[16:19], v[48:51], v[56:59]\n" \
  "ds_read_b128 v[40:43], v10 offset:6144\n" \
  "v_mfma_f32_16x16x32_bf16 v[68:71], a[16:19], v[52:55], v[68:71]\n" \
  "ds_read_b128 v[44:47], v10 offset:26624\n" \
  "v_mfma_f32_16x16x32_bf16 v[60:63], a[96:99], v[48:51], v[60:63]\n" \
  "buffer_store_dwordx4 v[16:19], v2, s[40:43], s69 offen offset:0 sc1\n" \
  "v_mfma_f32_16x16x32_bf16 v[72:75], a[96:99], v[52:55], v[72:75]\n" \
  "v_mfma_f32_16x16x32_bf16 v[64:67], a[176:179], v[48:51], v[64:67]\n" \
  "v_mfma_f32_16x16x32_bf16 v[76:79], a[176:179], v[52:55], v[76:79]\n" \
  "s_waitcnt lgkmcnt(2)\n" \
  "v_mfma_f32_16x16x32_bf16 v[56:59], a[20:23], v[32:35], v[56:59]\n" \
  "ds_read_b128 v[48:51], v10 offset:7168\n" \
  "v_mfma_f32_16x16x32_bf16 v[68:71], a[20:23], v[36:39], v[68:71]\n" \
  "ds_read_b128 v[52:55], v10 offset:27648\n" \
  "v_mfma_f32_16x16x32_bf16 v[60:63], a[100:103], v[32:35], v[60:63]\n" \
  "buffer_store_dwordx4 v[20:23], v2, s[40:43], s69 offen offset:1024 sc1\n" \
  "v_mfma_f32_16x16x32_bf16 v[72:75], a[100:103], v[36:39], v[72:75]\n" \
  "v_mfma_f32_16x16x32_bf16 v[64:67], a[180:183], v[32:35], v[64:67]\n" \
  "v_mfma_f32_16x16x32_bf16 v[76:79], a[180:183], v[36:39], v[76:79]\n" \
  "s_waitcnt lgkmcnt(2)\n" \
  "v_mfma_f32_16x16x32_bf16 v[56:59], a[24:27], v[40:43], v[56:59]\n" \
  "ds_read_b128 v[32:35], v10 offset:8192\n" \
  "v_mfma_f32_16x16x32_bf16 v[68:71], a[24:27], v[44:47], v[68:71]\n" \
  "ds_read_b128 v[36:39], v10 offset:28672\n" \
  "v_mfma_f32_16x16x32_bf16 v[60:63], a[104:107], v[40:43], v[60:63]\n" \
  "buffer_store_dwordx4 v[24:27], v2, s[40:43], s69 offen offset:2048 sc1\n" \
  "v_mfma_f32_16x16x32_bf16 v[72:75], a[104:107], v[44:47], v[72:75]\n" \
  "s_nop 0\n" \
  "v_mfma_f32_16x16x32_bf16 v[64:67], a[184:187], v[40:43], v[64:67]\n" \
  "s_nop 0\n" \
  "v_mfma_f32_16x16x32_bf16 v[76:79], a[184:187], v[44:47], v[76:79]\n" \
  "s_waitcnt lgkmcnt(2)\n" \
  "v_mfma_f32_16x16x32_bf16 v[56:59], a[28:31], v[48:51], v[56:59]\n" \
  "ds_read_b128 v[40:43], v10 offset:9216\n" \
  "v_mfma_f32_16x16x32_bf16 v[68:71], a[28:31], v[52:55], v[68:71]\n" \
  "ds_read_b128 v[44:47], v10 offset:29696\n" \
  "v_mfma_f32_16x16x32_bf16 v[60:63], a[108:111], v[48:51], v[60:63]\n" \
  "s_add_u32 s70, s68, 4096\n" \
  "v_mfma_f32_16x16x32_bf16 v[72:75], a[108:111], v[52:55], v[72:75]\n" \
  "s_add_u32 s71, s68, 8192\n" \
  "v_mfma_f32_16x16x32_bf16 v[64:67], a[188:191], v[48:51], v[64:67]\n" \
  "s_add_u32 m0, s53, 40960\n" \
  "v_mfma_f32_16x16x32_bf16 v[76:79], a[188:191], v[52:55], v[76:79]\n" \
  "buffer_load_dwordx4 v1, s[36:39], s68 offen sc1 lds\n" \
  "s_waitcnt lgkmcnt(2)\n" \
  "v_mfma_f32_16x16x32_bf16 v[56:59], a[32:35], v[32:35], v[56:59]\n" \
  "ds_read_b128 v[48:51], v10 offset:10240\n" \
  "v_mfma_f32_16x16x32_bf16 v[68:71], a[32:35], v[36:39], v[68:71]\n" \
  "ds_read_b128 v[52:55], v10 offset:30720\n" \
  "v_mfma_f32_16x16x32_bf16 v[60:63], a[112:115], v[32:35], v[60:63]\n" \
  "buffer_load_dwordx4 v1, s[36:39], s68 offen offset:1024 sc1 lds\n" \
  "v_mfma_f32_16x16x32_bf16 v[72:75], a[112:115], v[36:39], v[72:75]\n" \
  "v_mfma_f32_16x16x32_bf16 v[64:67], a[192:195], v[32:35], v[64:67]\n" \
  "v_mfma_f32_16x16x32_bf16 v[76:79], a[192:195], v[36:39], v[76:79]\n" \
  "s_waitcnt lgkmcnt(2)\n" \
  "v_mfma_f32_16x16x32_bf16 v[56:59], a[36:39], v[40:43], v[56:59]\n" \
  "ds_read_b128 v[32:35], v10 offset:11264\n" \
  "v_mfma_f32_16x16x32_bf16 v[68:71], a[36:39], v[44:47], v[68:71]\n" \
  "ds_read_b128 v[36:39], v10 offset:31744\n" \
  "v_mfma_f32_16x16x32_bf16 v[60:63], a[116:119], v[40:43], v[60:63]\n" \
  "buffer_load_dwordx4 v1, s[36:39], s68 offen offset:2048 sc1 lds\n" \
  "v_mfma_f32_16x16x32_bf16 v[72:75], a[116:119], v[44:47], v[72:75]\n" \
  "v_mfma_f32_16x16x32_bf16 v[64:67], a[196:199], v[40:43], v[64:67]\n" \
  "v_mfma_f32_16x16x32_bf16 v[76:79], a[196:199], v[44:47], v[76:79]\n" \
  "s_waitcnt lgkmcnt(2)\n" \
  "v_mfma_f32_16x16x32_bf16 v[56:59], a[40:43], v[48:51], v[56:59]\n" \
  "ds_read_b128 v[40:43], v10 offset:12288\n" \
  "v_mfma_f32_16x16x32_bf16 v[68:71], a[40:43], v[52:55], v[68:71]\n" \
  "ds_read_b128 v[44:47], v10 offset:32768\n" \
  "v_mfma_f32_16x16x32_bf16 v[60:63], a[120:123], v[48:51], v[60:63]\n" \
  "buffer_load_dwordx4 v1, s[36:39], s68 offen offset:3072 sc1 lds\n" \
  "v_mfma_f32_16x16x32_bf16 v[72:75], a[120:123], v[52:55], v[72:75]\n" \
  "v_mfma_f32_16x16x32_bf16 v[64:67], a[200:203], v[48:51], v[64:67]\n" \
  "v_mfma_f32_16x16x32_bf16 v[76:79], a[200:203], v[52:55], v[76:79]\n" \
  "s_waitcnt lgkmcnt(2)\n" \
  "v_mfma_f32_16x16x32_bf16 v[56:59], a[44:47], v[32:35], v[56:59]\n" \
  "ds_read_b128 v[48:51], v10 offset:13312\n" \
  "v_mfma_f32_16x16x32_bf16 v[68:71], a[44:47], v[36:39], v[68:71]\n" \
  "ds_read_b128 v[52:55], v10 offset:33792\n" \
  "v_mfma_f32_16x16x32_bf16 v[60:63], a[124:127], v[32:35], v[60:63]\n" \
  "s_add_u32 m0, s53, 45056\n" \
  "v_mfma_f32_16x16x32_bf16 v[72:75], a[124:127], v[36:39], v[72:75]\n" \
  "buffer_load_dwordx4 v1, s[36:39], s70 offen sc1 lds\n" \
  "v_mfma_f32_16x16x32_bf16 v[64:67], a[204:207], v[32:35], v[64:67]\n" \
  "v_mfma_f32_16x16x32_bf16 v[76:79], a[204:207], v[36:39], v[76:79]\n" \
  "s_waitcnt lgkmcnt(2)\n" \
  "v_mfma_f32_16x16x32_bf16 v[56:59], a[48:51], v[40:43], v[56:59]\n" \
  "ds_read_b128 v[32:35], v10 offset:14336\n" \
  "v_mfma_f32_16x16x32_bf16 v[68:71], a[48:51], v[44:47], v[68:71]\n" \
  "ds_read_b128 v[36:39], v10 offset:34816\n" \
  "v_mfma_f32_16x16x32_bf16 v[60:63], a[128:131], v[40:43], v[60:63]\n" \
  "buffer_load_dwordx4 v1, s[36:39], s70 offen offset:1024 sc1 lds\n" \
  "v_mfma_f32_16x16x32_bf16 v[72:75], a[128:131], v[44:47], v[72:75]\n" \
  "v_mfma_f32_16x16x32_bf16 v[64:67], a[208:211], v[40:43], v[64:67]\n" \
  "v_mfma_f32_16x16x32_bf16 v[76:79], a[208:211], v[44:47], v[76:79]\n" \
  "s_waitcnt vmcnt(6) lgkmcnt(2)\n" \
  "s_barrier\n" \
  "v_mfma_f32_16x16x32_bf16 v[56:59], a[52:55], v[48:51], v[56:59]\n" \
  "ds_read_b128 v[40:43], v10 offset:15360\n" \
  "v_mfma_f32_16x16x32_bf16 v[68:71], a[52:55], v[52:55], v[68:71]\n" \
  "ds_read_b128 v[44:47], v10 offset:35840\n" \
  "v_mfma_f32_16x16x32_bf16 v[60:63], a[132:135], v[48:51], v[60:63]\n" \
  "buffer_atomic_add v11, v4, s[40:43], s54 offen\n" \
  "v_mfma_f32_16x16x32_bf16 v[72:75], a[132:135], v[52:55], v[72:75]\n" \
  "s_add_u32 s54, s54, s55\n" \
  "v_mfma_f32_16x16x32_bf16 v[64:67], a[212:215], v[48:51], v[64:67]\n" \
  "s_and_b32 s54, s54, 0xffffdfff\n" \
  "v_mfma_f32_16x16x32_bf16 v[76:79], a[212:215], v[52:55], v[76:79]\n" \
  "s_waitcnt lgkmcnt(2)\n" \
  "v_mfma_f32_16x16x32_bf16 v[56:59], a[56:59], v[32:35], v[56:59]\n" \
  "ds_read_b128 v[48:51], v10 offset:16384\n" \
  "v_mfma_f32_16x16x32_bf16 v[68:71], a[56:59], v[36:39], v[68:71]\n" \
  "ds_read_b128 v[52:55], v10 offset:36864\n" \
  "v_mfma_f32_16x16x32_bf16 v[60:63], a[136:139], v[32:35], v[60:63]\n" \
  "buffer_load_dwordx4 v1, s[36:39], s70 offen offset:2048 sc1 lds\n" \
  "v_mfma_f32_16x16x32_bf16 v[72:75], a[136:139], v[36:39], v[72:75]\n" \
  "v_readlane_b32 s69, v3, s72\n" \
  "v_mfma_f32_16x16x32_bf16 v[64:67], a[216:219], v[32:35], v[64:67]\n" \
  "s_add_u32 s72, s72, 1\n" \
  "v_mfma_f32_16x16x32_bf16 v[76:79], a[216:219], v[36:39], v[76:79]\n" \
  "s_waitcnt lgkmcnt(2)\n" \
  "v_mfma_f32_16x16x32_bf16 v[56:59], a[60:63], v[40:43], v[56:59]\n" \
  "ds_read_b128 v[32:35], v10 offset:17408\n" \
  "v_mfma_f32_16x16x32_bf16 v[68:71], a[60:63], v[44:47], v[68:71]\n" \
  "ds_read_b128 v[36:39], v10 offset:37888\n" \
  "v_mfma_f32_16x16x32_bf16 v[60:63], a[140:143], v[40:43], v[60:63]\n" \
  "buffer_load_dwordx4 v1, s[36:39], s70 offen offset:3072 sc1 lds\n" \
  "v_mfma_f32_16x16x32_bf16 v[72:75], a[140:143], v[44:47], v[72:75]\n" \
  "v_mfma_f32_16x16x32_bf16 v[64:67], a[220:223], v[40:43], v[64:67]\n" \
  "v_mfma_f32_16x16x32_bf16 v[76:79], a[220:223], v[44:47], v[76:79]\n" \
  "s_waitcnt lgkmcnt(2)\n" \
  "v_mfma_f32_16x16x32_bf16 v[56:59], a[64:67], v[48:51], v[56:59]\n" \
  "ds_read_b128 v[40:43], v10 offset:18432\n" \
  "v_mfma_f32_16x16x32_bf16 v[68:71], a[64:67], v[52:55], v[68:71]\n" \
  "ds_read_b128 v[44:47], v10 offset:38912\n" \
  "v_mfma_f32_16x16x32_bf16 v[60:63], a[144:147], v[48:51], v[60:63]\n" \
  "s_add_u32 m0, s53, 49152\n" \
  "v_mfma_f32_16x16x32_bf16 v[72:75], a[144:147], v[52:55], v[72:75]\n" \
  "buffer_load_dwordx4 v1, s[36:39], s71 offen sc1 lds\n" \
  "v_mfma_f32_16x16x32_bf16 v[64:67], a[224:227], v[48:51], v[64:67]\n" \
  "v_mfma_f32_16x16x32_bf16 v[76:79], a[224:227], v[52:55], v[76:79]\n" \
  "s_waitcnt lgkmcnt(2)\n" \
  "v_mfma_f32_16x16x32_bf16 v[56:59], a[68:71], v[32:35], v[56:59]\n" \
  "ds_read_b128 v[48:51], v10 offset:19456\n" \
  "v_mfma_f32_16x16x32_bf16 v[68:71], a[68:71], v[36:39], v[68:71]\n" \
  "ds_read_b128 v[52:55], v10 offset:39936\n" \
  "v_mfma_f32_16x16x32_bf16 v[60:63], a[148:151], v[32:35], v[60:63]\n" \
  "buffer_load_dwordx4 v1, s[36:39], s71 offen offset:1024 sc1 lds\n" \
  "v_mfma_f32_16x16x32_bf16 v[72:75], a[148:151], v[36:39], v[72:75]\n" \
  "s_add_u32 s68, s68, 40960\n" \
  "v_mfma_f32_16x16x32_bf16 v[64:67], a[228:231], v[32:35], v[64:67]\n" \
  "v_mfma_f32_16x16x32_bf16 v[76:79], a[228:231], v[36:39], v[76:79]\n" \
  "s_waitcnt vmcnt(11) lgkmcnt(2)\n" \
  "s_barrier\n" \
  "v_mfma_f32_16x16x32_bf16 v[56:59], a[72:75], v[40:43], v[56:59]\n" \
  "ds_read_b128 v[32:35], v0 offset:0\n" \
  "v_mfma_f32_16x16x32_bf16 v[68:71], a[72:75], v[44:47], v[68:71]\n" \
  "ds_read_b128 v[36:39], v0 offset:20480\n" \
  "v_mfma_f32_16x16x32_bf16 v[60:63], a[152:155], v[40:43], v[60:63]\n" \
  "v_mfma_f32_16x16x32_bf16 v[72:75], a[152:155], v[44:47], v[72:75]\n" \
  "v_mfma_f32_16x16x32_bf16 v[64:67], a[232:235], v[40:43], v[64:67]\n" \
  "v_mfma_f32_16x16x32_bf16 v[76:79], a[232:235], v[44:47], v[76:79]\n" \
  "s_waitcnt lgkmcnt(2)\n" \
  "v_mfma_f32_16x16x32_bf16 v[56:59], a[76:79], v[48:51], v[56:59]\n" \
  "ds_read_b128 v[40:43], v0 offset:1024\n" \
  "v_mfma_f32_16x16x32_bf16 v[68:71], a[76:79], v[52:55], v[68:71]\n" \
  "ds_read_b128 v[44:47], v0 offset:21504\n" \
  "v_mfma_f32_16x16x32_bf16 v[60:63], a[156:159], v[48:51], v[60:63]\n" \
  "s_cmp_eq_u32 s72, 64\n" \
  "v_mfma_f32_16x16x32_bf16 v[72:75], a[156:159], v[52:55], v[72:75]\n" \
  "v_mfma_f32_16x16x32_bf16 v[64:67], a[236:239], v[48:51], v[64:67]\n" \
  "v_mfma_f32_16x16x32_bf16 v[76:79], a[236:239], v[52:55], v[76:79]\n" \
  "s_waitcnt lgkmcnt(2)\n" \
  "v_mfma_f32_16x16x32_bf16 v[100:103], a[0:3], v[32:35], 0\n" \
  "ds_read_b128 v[48:51], v0 offset:2048\n" \
  "v_mfma_f32_16x16x32_bf16 v[112:115], a[0:3], v[36:39], 0\n" \
  "ds_read_b128 v[52:55], v0 offset:22528\n" \
  "v_mfma_f32_16x16x32_bf16 v[104:107], a[80:83], v[32:35], 0\n" \
  "v_cvt_pk_bf16_f32 v16, v56, v57\n" \
  "v_mfma_f32_16x16x32_bf16 v[116:119], a[80:83], v[36:39], 0\n" \
  "v_cvt_pk_bf16_f32 v17, v58, v59\n" \
  "v_mfma_f32_16x16x32_bf16 v[108:111], a[160:163], v[32:35], 0\n" \
  "v_cvt_pk_bf16_f32 v18, v60, v61\n" \
  "v_mfma_f32_16x16x32_bf16 v[120:123], a[160:163], v[36:39], 0\n" \
  "v_cvt_pk_bf16_f32 v19, v62, v63\n" \
  "s_waitcnt lgkmcnt(2)\n" \
  "s_barrier\n" \
  "v_mfma_f32_16x16x32_bf16 v[100:103], a[4:7], v[40:43], v[100:103]\n" \
  "ds_read_b128 v[32:35], v0 offset:3072\n" \
  "v_mfma_f32_16x16x32_bf16 v[112:115], a[4:7], v[44:47], v[112:115]\n" \
  "ds_read_b128 v[36:39], v0 offset:23552\n" \
  "v_mfma_f32_16x16x32_bf16 v[104:107], a[84:87], v[40:43], v[104:107]\n" \
  "buffer_store_dwordx4 v[16:19], v5, s[40:43], s69 offen offset:0 sc1\n" \
  "v_mfma_f32_16x16x32_bf16 v[116:119], a[84:87], v[44:47], v[116:119]\n" \
  "v_cvt_pk_bf16_f32 v20, v68, v69\n" \
  "v_mfma_f32_16x16x32_bf16 v[108:111], a[164:167], v[40:43], v[108:111]\n" \
  "v_cvt_pk_bf16_f32 v21, v70, v71\n" \
  "v_mfma_f32_16x16x32_bf16 v[120:123], a[164:167], v[44:47], v[120:123]\n" \
  "v_cvt_pk_bf16_f32 v22, v72, v73\n" \
  "s_waitcnt lgkmcnt(2)\n" \
  "v_mfma_f32_16x16x32_bf16 v[100:103], a[8:11], v[48:51], v[100:103]\n" \
  "ds_read_b128 v[40:43], v0 offset:4096\n" \
  "v_mfma_f32_16x16x32_bf16 v[112:115], a[8:11], v[52:55], v[112:115]\n" \
  "ds_read_b128 v[44:47], v0 offset:24576\n" \
  "v_mfma_f32_16x16x32_bf16 v[104:107], a[88:91], v[48:51], v[104:107]\n" \
  "v_cvt_pk_bf16_f32 v23, v74, v75\n" \
  "v_mfma_f32_16x16x32_bf16 v[116:119], a[88:91], v[52:55], v[116:119]\n" \
  "buffer_store_dwordx4 v[20:23], v5, s[40:43], s69 offen offset:1024 sc1\n" \
  "v_mfma_f32_16x16x32_bf16 v[108:111], a[168:171], v[48:51], v[108:111]\n" \
  "v_cvt_pk_bf16_f32 v24, v64, v65\n" \
  "v_mfma_f32_16x16x32_bf16 v[120:123], a[168:171], v[52:55], v[120:123]\n" \
  "v_cvt_pk_bf16_f32 v25, v66, v67\n" \
  "s_waitcnt lgkmcnt(2)\n" \
  "v_mfma_f32_16x16x32_bf16 v[100:103], a[12:15], v[32:35], v[100:103]\n" \
  "ds_read_b128 v[48:51], v0 offset:5120\n" \
  "v_mfma_f32_16x16x32_bf16 v[112:115], a[12:15], v[36:39], v[112:115]\n" \
  "ds_read_b128 v[52:55], v0 offset:25600\n" \
  "v_mfma_f32_16x16x32_bf16 v[104:107], a[92:95], v[32:35], v[104:107]\n" \
  "v_cvt_pk_bf16_f32 v26, v76, v77\n" \
  "v_mfma_f32_16x16x32_bf16 v[116:119], a[92:95], v[36:39], v[116:119]\n" \
  "v_cvt_pk_bf16_f32 v27, v78, v79\n" \
  "v_mfma_f32_16x16x32_bf16 v[108:111], a[172:175], v[32:35], v[108:111]\n" \
  "buffer_store_dwordx4 v[24:27], v5, s[40:43], s69 offen offset:2048 sc1\n" \
  "v_mfma_f32_16x16x32_bf16 v[120:123], a[172:175], v[36:39], v[120:123]\n" \
  "s_waitcnt lgkmcnt(2)\n" \
  "v_mfma_f32_16x16x32_bf16 v[100:103], a[16:19], v[40:43], v[100:103]\n" \
  "ds_read_b128 v[32:35], v0 offset:6144\n" \
  "v_mfma_f32_16x16x32_bf16 v[112:115], a[16:19], v[44:47], v[112:115]\n" \
  "ds_read_b128 v[36:39], v0 offset:26624\n" \
  "v_mfma_f32_16x16x32_bf16 v[104:107], a[96:99], v[40:43], v[104:107]\n" \
  "buffer_store_dwordx4 v[16:19], v2, s[40:43], s69 offen offset:0 sc1\n" \
  "v_mfma_f32_16x16x32_bf16 v[116:119], a[96:99], v[44:47], v[116:119]\n" \
  "v_mfma_f32_16x16x32_bf16 v[108:111], a[176:179], v[40:43], v[108:111]\n" \
  "v_mfma_f32_16x16x32_bf16 v[120:123], a[176:179], v[44:47], v[120:123]\n" \
  "s_waitcnt lgkmcnt(2)\n" \
  "v_mfma_f32_16x16x32_bf16 v[100:103], a[20:23], v[48:51], v[100:103]\n" \
  "ds_read_b128 v[40:43], v0 offset:7168\n" \
  "v_mfma_f32_16x16x32_bf16 v[112:115], a[20:23], v[52:55], v[112:115]\n" \
  "ds_read_b128 v[44:47], v0 offset:27648\n" \
  "v_mfma_f32_16x16x32_bf16 v[104:107], a[100:103], v[48:51], v[104:107]\n" \
  "buffer_store_dwordx4 v[20:23], v2, s[40:43], s69 offen offset:1024 sc1\n" \
  "v_mfma_f32_16x16x32_bf16 v[116:119], a[100:103], v[52:55], v[116:119]\n" \
  "v_mfma_f32_16x16x32_bf16 v[108:111], a[180:183], v[48:51], v[108:111]\n" \
  "v_mfma_f32_16x16x32_bf16 v[120:123], a[180:183], v[52:55], v[120:123]\n" \
  "s_waitcnt lgkmcnt(2)\n" \
  "v_mfma_f32_16x16x32_bf16 v[100:103], a[24:27], v[32:35], v[100:103]\n" \
  "ds_read_b128 v[48:51], v0 offset:8192\n" \
  "v_mfma_f32_16x16x32_bf16 v[112:115], a[24:27], v[36:39], v[112:115]\n" \
  "ds_read_b128 v[52:55], v0 offset:28672\n" \
  "v_mfma_f32_16x16x32_bf16 v[104:107], a[104:107], v[32:35], v[104:107]\n" \
  "buffer_store_dwordx4 v[24:27], v2, s[40:43], s69 offen offset:2048 sc1\n" \
  "v_mfma_f32_16x16x32_bf16 v[116:119], a[104:107], v[36:39], v[116:119]\n" \
  "s_nop 0\n" \
  "v_mfma_f32_16x16x32_bf16 v[108:111], a[184:187], v[32:35], v[108:111]\n" \
  "s_nop 0\n" \
  "v_mfma_f32_16x16x32_bf16 v[120:123], a[184:187], v[36:39], v[120:123]\n" \
  "s_waitcnt lgkmcnt(2)\n" \
  "v_mfma_f32_16x16x32_bf16 v[100:103], a[28:31], v[40:43], v[100:103]\n" \
  "ds_read_b128 v[32:35], v0 offset:9216\n" \
  "v_mfma_f32_16x16x32_bf16 v[112:115], a[28:31], v[44:47], v[112:115]\n" \
  "ds_read_b128 v[36:39], v0 offset:29696\n" \
  "v_mfma_f32_16x16x32_bf16 v[104:107], a[108:111], v[40:43], v[104:107]\n" \
  "s_add_u32 s70, s68, 4096\n" \
  "v_mfma_f32_16x16x32_bf16 v[116:119], a[108:111], v[44:47], v[116:119]\n" \
  "s_add_u32 s71, s68, 8192\n" \
  "v_mfma_f32_16x16x32_bf16 v[108:111], a[188:191], v[40:43], v[108:111]\n" \
  "s_add_u32 m0, s53, 81920\n" \
  "v_mfma_f32_16x16x32_bf16 v[120:123], a[188:191], v[44:47], v[120:123]\n" \
  "buffer_load_dwordx4 v1, s[36:39], s68 offen sc1 lds\n" \
  "s_waitcnt lgkmcnt(2)\n" \
  "v_mfma_f32_16x16x32_bf16 v[100:103], a[32:35], v[48:51], v[100:103]\n" \
  "ds_read_b128 v[40:43], v0 offset:10240\n" \
  "v_mfma_f32_16x16x32_bf16 v[112:115], a[32:35], v[52:55], v[112:115]\n" \
  "ds_read_b128 v[44:47], v0 offset:30720\n" \
  "v_mfma_f32_16x16x32_bf16 v[104:107], a[112:115], v[48:51], v[104:107]\n" \
  "buffer_load_dwordx4 v1, s[36:39], s68 offen offset:1024 sc1 lds\n" \
  "v_mfma_f32_16x16x32_bf16 v[116:119], a[112:115], v[52:55], v[116:119]\n" \
  "v_mfma_f32_16x16x32_bf16 v[108:111], a[192:195], v[48:51], v[108:111]\n" \
  "v_mfma_f32_16x16x32_bf16 v[120:123], a[192:195], v[52:55], v[120:123]\n" \
  "s_waitcnt lgkmcnt(2)\n" \
  "v_mfma_f32_16x16x32_bf16 v[100:103], a[36:39], v[32:35], v[100:103]\n" \
  "ds_read_b128 v[48:51], v0 offset:11264\n" \
  "v_mfma_f32_16x16x32_bf16 v[112:115], a[36:39], v[36:39], v[112:115]\n" \
  "ds_read_b128 v[52:55], v0 offset:31744\n" \
  "v_mfma_f32_16x16x32_bf16 v[104:107], a[116:119], v[32:35], v[104:107]\n" \
  "buffer_load_dwordx4 v1, s[36:39], s68 offen offset:2048 sc1 lds\n" \
  "v_mfma_f32_16x16x32_bf16 v[116:119], a[116:119], v[36:39], v[116:119]\n" \
  "v_mfma_f32_16x16x32_bf16 v[108:111], a[196:199], v[32:35], v[108:111]\n" \
  "v_mfma_f32_16x16x32_bf16 v[120:123], a[196:199], v[36:39], v[120:123]\n" \
  "s_waitcnt lgkmcnt(2)\n" \
  "v_mfma_f32_16x16x32_bf16 v[100:103], a[40:43], v[40:43], v[100:103]\n" \
  "ds_read_b128 v[32:35], v0 offset:12288\n" \
  "v_mfma_f32_16x16x32_bf16 v[112:115], a[40:43], v[44:47], v[112:115]\n" \
  "ds_read_b128 v[36:39], v0 offset:32768\n" \
  "v_mfma_f32_16x16x32_bf16 v[104:107], a[120:123], v[40:43], v[104:107]\n" \
  "buffer_load_dwordx4 v1, s[36:39], s68 offen offset:3072 sc1 lds\n" \
  "v_mfma_f32_16x16x32_bf16 v[116:119], a[120:123], v[44:47], v[116:119]\n" \
  "v_mfma_f32_16x16x32_bf16 v[108:111], a[200:203], v[40:43], v[108:111]\n" \
  "v_mfma_f32_16x16x32_bf16 v[120:123], a[200:203], v[44:47], v[120:123]\n" \
  "s_waitcnt lgkmcnt(2)\n" \
  "v_mfma_f32_16x16x32_bf16 v[100:103], a[44:47], v[48:51], v[100:103]\n" \
  "ds_read_b128 v[40:43], v0 offset:13312\n" \
  "v_mfma_f32_16x16x32_bf16 v[112:115], a[44:47], v[52:55], v[112:115]\n" \
  "ds_read_b128 v[44:47], v0 offset:33792\n" \
  "v_mfma_f32_16x16x32_bf16 v[104:107], a[124:127], v[48:51], v[104:107]\n" \
  "s_add_u32 m0, s53, 86016\n" \
  "v_mfma_f32_16x16x32_bf16 v[116:119], a[124:127], v[52:55], v[116:119]\n" \
  "buffer_load_dwordx4 v1, s[36:39], s70 offen sc1 lds\n" \
  "v_mfma_f32_16x16x32_bf16 v[108:111], a[204:207], v[48:51], v[108:111]\n" \
  "v_mfma_f32_16x16x32_bf16 v[120:123], a[204:207], v[52:55], v[120:123]\n" \
  "s_waitcnt lgkmcnt(2)\n" \
  "v_mfma_f32_16x16x32_bf16 v[100:103], a[48:51], v[32:35], v[100:103]\n" \
  "ds_read_b128 v[48:51], v0 offset:14336\n" \
  "v_mfma_f32_16x16x32_bf16 v[112:115], a[48:51], v[36:39], v[112:115]\n" \
  "ds_read_b128 v[52:55], v0 offset:34816\n" \
  "v_mfma_f32_16x16x32_bf16 v[104:107], a[128:131], v[32:35], v[104:107]\n" \
  "buffer_load_dwordx4 v1, s[36:39], s70 offen offset:1024 sc1 lds\n" \
  "v_mfma_f32_16x16x32_bf16 v[116:119], a[128:131], v[36:39], v[116:119]\n" \
  "v_mfma_f32_16x16x32_bf16 v[108:111], a[208:211], v[32:35], v[108:111]\n" \
  "v_mfma_f32_16x16x32_bf16 v[120:123], a[208:211], v[36:39], v[120:123]\n" \
  "s_waitcnt vmcnt(6) lgkmcnt(2)\n" \
  "s_barrier\n" \
  "v_mfma_f32_16x16x32_bf16 v[100:103], a[52:55], v[40:43], v[100:103]\n" \
  "ds_read_b128 v[32:35], v0 offset:15360\n" \
  "v_mfma_f32_16x16x32_bf16 v[112:115], a[52:55], v[44:47], v[112:115]\n" \
  "ds_read_b128 v[36:39], v0 offset:35840\n" \
  "v_mfma_f32_16x16x32_bf16 v[104:107], a[132:135], v[40:43], v[104:107]\n" \
  "buffer_atomic_add v11, v4, s[40:43], s54 offen\n" \
  "v_mfma_f32_16x16x32_bf16 v[116:119], a[132:135], v[44:47], v[116:119]\n" \
  "s_add_u32 s54, s54, s55\n" \
  "v_mfma_f32_16x16x32_bf16 v[108:111], a[212:215], v[40:43], v[108:111]\n" \
  "s_and_b32 s54, s54, 0xffffdfff\n" \
  "v_mfma_f32_16x16x32_bf16 v[120:123], a[212:215], v[44:47], v[120:123]\n" \
  "s_waitcnt lgkmcnt(2)\n" \
  "v_mfma_f32_16x16x32_bf16 v[100:103], a[56:59], v[48:51], v[100:103]\n" \
  "ds_read_b128 v[40:43], v0 offset:16384\n" \
  "v_mfma_f32_16x16x32_bf16 v[112:115], a[56:59], v[52:55], v[112:115]\n" \
  "ds_read_b128 v[44:47], v0 offset:36864\n" \
  "v_mfma_f32_16x16x32_bf16 v[104:107], a[136:139], v[48:51], v[104:107]\n" \
  "buffer_load_dwordx4 v1, s[36:39], s70 offen offset:2048 sc1 lds\n" \
  "v_mfma_f32_16x16x32_bf16 v[116:119], a[136:139], v[52:55], v[116:119]\n" \
  "v_readlane_b32 s69, v3, s72\n" \
  "v_mfma_f32_16x16x32_bf16 v[108:111], a[216:219], v[48:51], v[108:111]\n" \
  "s_add_u32 s72, s72, 1\n" \
  "v_mfma_f32_16x16x32_bf16 v[120:123], a[216:219], v[52:55], v[120:123]\n" \
  "s_waitcnt lgkmcnt(2)\n" \
  "v_mfma_f32_16x16x32_bf16 v[100:103], a[60:63], v[32:35], v[100:103]\n" \
  "ds_read_b128 v[48:51], v0 offset:17408\n" \
  "v_mfma_f32_16x16x32_bf16 v[112:115], a[60:63], v[36:39], v[112:115]\n" \
  "ds_read_b128 v[52:55], v0 offset:37888\n" \
  "v_mfma_f32_16x16x32_bf16 v[104:107], a[140:143], v[32:35], v[104:107]\n" \
  "buffer_load_dwordx4 v1, s[36:39], s70 offen offset:3072 sc1 lds\n" \
  "v_mfma_f32_16x16x32_bf16 v[116:119], a[140:143], v[36:39], v[116:119]\n" \
  "v_mfma_f32_16x16x32_bf16 v[108:111], a[220:223], v[32:35], v[108:111]\n" \
  "v_mfma_f32_16x16x32_bf16 v[120:123], a[220:223], v[36:39], v[120:123]\n" \
  "s_waitcnt lgkmcnt(2)\n" \
  "v_mfma_f32_16x16x32_bf16 v[100:103], a[64:67], v[40:43], v[100:103]\n" \
  "ds_read_b128 v[32:35], v0 offset:18432\n" \
  "v_mfma_f32_16x16x32_bf16 v[112:115], a[64:67], v[44:47], v[112:115]\n" \
  "ds_read_b128 v[36:39], v0 offset:38912\n" \
  "v_mfma_f32_16x16x32_bf16 v[104:107], a[144:147], v[40:43], v[104:107]\n" \
  "s_add_u32 m0, s53, 90112\n" \
  "v_mfma_f32_16x16x32_bf16 v[116:119], a[144:147], v[44:47], v[116:119]\n" \
  "buffer_load_dwordx4 v1, s[36:39], s71 offen sc1 lds\n" \
  "v_mfma_f32_16x16x32_bf16 v[108:111], a[224:227], v[40:43], v[108:111]\n" \
  "v_mfma_f32_16x16x32_bf16 v[120:123], a[224:227], v[44:47], v[120:123]\n" \
  "s_waitcnt lgkmcnt(2)\n" \
  "v_mfma_f32_16x16x32_bf16 v[100:103], a[68:71], v[48:51], v[100:103]\n" \
  "ds_read_b128 v[40:43], v0 offset:19456\n" \
  "v_mfma_f32_16x16x32_bf16 v[112:115], a[68:71], v[52:55], v[112:115]\n" \
  "ds_read_b128 v[44:47], v0 offset:39936\n" \
  "v_mfma_f32_16x16x32_bf16 v[104:107], a[148:151], v[48:51], v[104:107]\n" \
  "buffer_load_dwordx4 v1, s[36:39], s71 offen offset:1024 sc1 lds\n" \
  "v_mfma_f32_16x16x32_bf16 v[116:119], a[148:151], v[52:55], v[116:119]\n" \
  "s_add_u32 s68, s68, 40960\n" \
  "v_mfma_f32_16x16x32_bf16 v[108:111], a[228:231], v[48:51], v[108:111]\n" \
  "v_mfma_f32_16x16x32_bf16 v[120:123], a[228:231], v[52:55], v[120:123]\n" \
  "s_waitcnt vmcnt(11) lgkmcnt(2)\n" \
  "s_barrier\n" \
  "v_mfma_f32_16x16x32_bf16 v[100:103], a[72:75], v[32:35], v[100:103]\n" \
  "ds_read_b128 v[48:51], v9 offset:0\n" \
  "v_mfma_f32_16x16x32_bf16 v[112:115], a[72:75], v[36:39], v[112:115]\n" \
  "ds_read_b128 v[52:55], v9 offset:20480\n" \
  "v_mfma_f32_16x16x32_bf16 v[104:107], a[152:155], v[32:35], v[104:107]\n" \
  "v_mfma_f32_16x16x32_bf16 v[116:119], a[152:155], v[36:39], v[116:119]\n" \
  "v_mfma_f32_16x16x32_bf16 v[108:111], a[232:235], v[32:35], v[108:111]\n" \
  "v_mfma_f32_16x16x32_bf16 v[120:123], a[232:235], v[36:39], v[120:123]\n" \
  "s_waitcnt lgkmcnt(2)\n" \
  "v_mfma_f32_16x16x32_bf16 v[100:103], a[76:79], v[40:43], v[100:103]\n" \
  "ds_read_b128 v[32:35], v9 offset:1024\n" \
  "v_mfma_f32_16x16x32_bf16 v[112:115], a[76:79], v[44:47], v[112:115]\n" \
  "ds_read_b128 v[36:39], v9 offset:21504\n" \
  "v_mfma_f32_16x16x32_bf16 v[104:107], a[156:159], v[40:43], v[104:107]\n" \
  "s_cmp_eq_u32 s72, 64\n" \
  "v_mfma_f32_16x16x32_bf16 v[116:119], a[156:159], v[44:47], v[116:119]\n" \
  "v_mfma_f32_16x16x32_bf16 v[108:111], a[236:239], v[40:43], v[108:111]\n" \
  "v_mfma_f32_16x16x32_bf16 v[120:123], a[236:239], v[44:47], v[120:123]\n" \
  "s_cbranch_scc1 LPE_%=\n" \
  "s_waitcnt lgkmcnt(2)\n" \
  "v_mfma_f32_16x16x32_bf16 v[56:59], a[0:3], v[48:51], 0\n" \
  "ds_read_b128 v[40:43], v9 offset:2048\n" \
  "v_mfma_f32_16x16x32_bf16 v[68:71], a[0:3], v[52:55], 0\n" \
  "ds_read_b128 v[44:47], v9 offset:22528\n" \
  "v_mfma_f32_16x16x32_bf16 v[60:63], a[80:83], v[48:51], 0\n" \
  "v_cvt_pk_bf16_f32 v16, v100, v101\n" \
  "v_mfma_f32_16x16x32_bf16 v[72:75], a[80:83], v[52:55], 0\n" \
  "v_cvt_pk_bf16_f32 v17, v102, v103\n" \
  "v_mfma_f32_16x16x32_bf16 v[64:67], a[160:163], v[48:51], 0\n" \
  "v_cvt_pk_bf16_f32 v18, v104, v105\n" \
  "v_mfma_f32_16x16x32_bf16 v[76:79], a[160:163], v[52:55], 0\n" \
  "v_cvt_pk_bf16_f32 v19, v106, v107\n" \
  "s_waitcnt lgkmcnt(2)\n" \
  "s_barrier\n" \
  "v_mfma_f32_16x16x32_bf16 v[56:59], a[4:7], v[32:35], v[56:59]\n" \
  "ds_read_b128 v[48:51], v9 offset:3072\n" \
  "v_mfma_f32_16x16x32_bf16 v[68:71], a[4:7], v[36:39], v[68:71]\n" \
  "ds_read_b128 v[52:55], v9 offset:23552\n" \
  "v_mfma_f32_16x16x32_bf16 v[60:63], a[84:87], v[32:35], v[60:63]\n" \
  "buffer_store_dwordx4 v[16:19], v5, s[40:43], s69 offen offset:0 sc1\n" \
  "v_mfma_f32_16x16x32_bf16 v[72:75], a[84:87], v[36:39], v[72:75]\n" \
  "v_cvt_pk_bf16_f32 v20, v112, v113\n" \
  "v_mfma_f32_16x16x32_bf16 v[64:67], a[164:167], v[32:35], v[64:67]\n" \
  "v_cvt_pk_bf16_f32 v21, v114, v115\n" \
  "v_mfma_f32_16x16x32_bf16 v[76:79], a[164:167], v[36:39], v[76:79]\n" \
  "v_cvt_pk_bf16_f32 v22, v116, v117\n" \
  "s_waitcnt lgkmcnt(2)\n" \
  "v_mfma_f32_16x16x32_bf16 v[56:59], a[8:11], v[40:43], v[56:59]\n" \
  "ds_read_b128 v[32:35], v9 offset:4096\n" \
  "v_mfma_f32_16x16x32_bf16 v[68:71], a[8:11], v[44:47], v[68:71]\n" \
  "ds_read_b128 v[36:39], v9 offset:24576\n" \
  "v_mfma_f32_16x16x32_bf16 v[60:63], a[88:91], v[40:43], v[60:63]\n" \
  "v_cvt_pk_bf16_f32 v23, v118, v119\n" \
  "v_mfma_f32_16x16x32_bf16 v[72:75], a[88:91], v[44:47], v[72:75]\n" \
  "buffer_store_dwordx4 v[20:23], v5, s[40:43], s69 offen offset:1024 sc1\n" \
  "v_mfma_f32_16x16x32_bf16 v[64:67], a[168:171], v[40:43], v[64:67]\n" \
  "v_cvt_pk_bf16_f32 v24, v108, v109\n" \
  "v_mfma_f32_16x16x32_bf16 v[76:79], a[168:171], v[44:47], v[76:79]\n" \
  "v_cvt_pk_bf16_f32 v25, v110, v111\n" \
  "s_waitcnt lgkmcnt(2)\n" \
  "v_mfma_f32_16x16x32_bf16 v[56:59], a[12:15], v[48:51], v[56:59]\n" \
  "ds_read_b128 v[40:43], v9 offset:5120\n" \
  "v_mfma_f32_16x16x32_bf16 v[68:71], a[12:15], v[52:55], v[68:71]\n" \
  "ds_read_b128 v[44:47], v9 offset:25600\n" \
  "v_mfma_f32_16x16x32_bf16 v[60:63], a[92:95], v[48:51], v[60:63]\n" \
  "v_cvt_pk_bf16_f32 v26, v120, v121\n" \
  "v_mfma_f32_16x16x32_bf16 v[72:75], a[92:95], v[52:55], v[72:75]\n" \
  "v_cvt_pk_bf16_f32 v27, v122, v123\n" \
  "v_mfma_f32_16x16x32_bf16 v[64:67], a[172:175], v[48:51], v[64:67]\n" \
  "buffer_store_dwordx4 v[24:27], v5, s[40:43], s69 offen offset:2048 sc1\n" \
  "v_mfma_f32_16x16x32_bf16 v[76:79], a[172:175], v[52:55], v[76:79]\n" \
  "s_waitcnt lgkmcnt(2)\n" \
  "v_mfma_f32_16x16x32_bf16 v[56:59], a[16:19], v[32:35], v[56:59]\n" \
  "ds_read_b128 v[48:51], v9 offset:6144\n" \
  "v_mfma_f32_16x16x32_bf16 v[68:71], a[16:19], v[36:39], v[68:71]\n" \
  "ds_read_b128 v[52:55], v9 offset:26624\n" \
  "v_mfma_f32_16x16x32_bf16 v[60:63], a[96:99], v[32:35], v[60:63]\n" \
  "buffer_store_dwordx4 v[16:19], v2, s[40:43], s69 offen offset:0 sc1\n" \
  "v_mfma_f32_16x16x32_bf16 v[72:75], a[96:99], v[36:39], v[72:75]\n" \
  "v_mfma_f32_16x16x32_bf16 v[64:67], a[176:179], v[32:35], v[64:67]\n" \
  "v_mfma_f32_16x16x32_bf16 v[76:79], a[176:179], v[36:39], v[76:79]\n" \
  "s_waitcnt lgkmcnt(2)\n" \
  "v_mfma_f32_16x16x32_bf16 v[56:59], a[20:23], v[40:43], v[56:59]\n" \
  "ds_read_b128 v[32:35], v9 offset:7168\n" \
  "v_mfma_f32_16x16x32_bf16 v[68:71], a[20:23], v[44:47], v[68:71]\n" \
  "ds_read_b128 v[36:39], v9 offset:27648\n" \
  "v_mfma_f32_16x16x32_bf16 v[60:63], a[100:103], v[40:43], v[60:63]\n" \
  "buffer_store_dwordx4 v[20:23], v2, s[40:43], s69 offen offset:1024 sc1\n" \
  "v_mfma_f32_16x16x32_bf16 v[72:75], a[100:103], v[44:47], v[72:75]\n" \
  "v_mfma_f32_16x16x32_bf16 v[64:67], a[180:183], v[40:43], v[64:67]\n" \
  "v_mfma_f32_16x16x32_bf16 v[76:79], a[180:183], v[44:47], v[76:79]\n" \
  "s_waitcnt lgkmcnt(2)\n" \
  "v_mfma_f32_16x16x32_bf16 v[56:59], a[24:27], v[48:51], v[56:59]\n" \
  "ds_read_b128 v[40:43], v9 offset:8192\n" \
  "v_mfma_f32_16x16x32_bf16 v[68:71], a[24:27], v[52:55], v[68:71]\n" \
  "ds_read_b128 v[44:47], v9 offset:28672\n" \
  "v_mfma_f32_16x16x32_bf16 v[60:63], a[104:107], v[48:51], v[60:63]\n" \
  "buffer_store_dwordx4 v[24:27], v2, s[40:43], s69 offen offset:2048 sc1\n" \
  "v_mfma_f32_16x16x32_bf16 v[72:75], a[104:107], v[52:55], v[72:75]\n" \
  "s_nop 0\n" \
  "v_mfma_f32_16x16x32_bf16 v[64:67], a[184:187], v[48:51], v[64:67]\n" \
  "s_nop 0\n" \
  "v_mfma_f32_16x16x32_bf16 v[76:79], a[184:187], v[52:55], v[76:79]\n" \
  "s_waitcnt lgkmcnt(2)\n" \
  "v_mfma_f32_16x16x32_bf16 v[56:59], a[28:31], v[32:35], v[56:59]\n" \
  "ds_read_b128 v[48:51], v9 offset:9216\n" \
  "v_mfma_f32_16x16x32_bf16 v[68:71], a[28:31], v[36:39], v[68:71]\n" \
  "ds_read_b128 v[52:55], v9 offset:29696\n" \
  "v_mfma_f32_16x16x32_bf16 v[60:63], a[108:111], v[32:35], v[60:63]\n" \
  "s_add_u32 s70, s68, 4096\n" \
  "v_mfma_f32_16x16x32_bf16 v[72:75], a[108:111], v[36:39], v[72:75]\n" \
  "s_add_u32 s71, s68, 8192\n" \
  "v_mfma_f32_16x16x32_bf16 v[64:67], a[188:191], v[32:35], v[64:67]\n" \
  "s_add_u32 m0, s53, 0\n" \
  "v_mfma_f32_16x16x32_bf16 v[76:79], a[188:191], v[36:39], v[76:79]\n" \
  "buffer_load_dwordx4 v1, s[36:39], s68 offen sc1 lds\n" \
  "s_waitcnt lgkmcnt(2)\n" \
  "v_mfma_f32_16x16x32_bf16 v[56:59], a[32:35], v[40:43], v[56:59]\n" \
  "ds_read_b128 v[32:35], v9 offset:10240\n" \
  "v_mfma_f32_16x16x32_bf16 v[68:71], a[32:35], v[44:47], v[68:71]\n" \
  "ds_read_b128 v[36:39], v9 offset:30720\n" \
  "v_mfma_f32_16x16x32_bf16 v[60:63], a[112:115], v[40:43], v[60:63]\n" \
  "buffer_load_dwordx4 v1, s[36:39], s68 offen offset:1024 sc1 lds\n" \
  "v_mfma_f32_16x16x32_bf16 v[72:75], a[112:115], v[44:47], v[72:75]\n" \
  "v_mfma_f32_16x16x32_bf16 v[64:67], a[192:195], v[40:43], v[64:67]\n" \
  "v_mfma_f32_16x16x32_bf16 v[76:79], a[192:195], v[44:47], v[76:79]\n" \
  "s_waitcnt lgkmcnt(2)\n" \
  "v_mfma_f32_16x16x32_bf16 v[56:59], a[36:39], v[48:51], v[56:59]\n" \
  "ds_read_b128 v[40:43], v9 offset:11264\n" \
  "v_mfma_f32_16x16x32_bf16 v[68:71], a[36:39], v[52:55], v[68:71]\n" \
  "ds_read_b128 v[44:47], v9 offset:31744\n" \
  "v_mfma_f32_16x16x32_bf16 v[60:63], a[116:119], v[48:51], v[60:63]\n" \
  "buffer_load_dwordx4 v1, s[36:39], s68 offen offset:2048 sc1 lds\n" \
  "v_mfma_f32_16x16x32_bf16 v[72:75], a[116:119], v[52:55], v[72:75]\n" \
  "v_mfma_f32_16x16x32_bf16 v[64:67], a[196:199], v[48:51], v[64:67]\n" \
  "v_mfma_f32_16x16x32_bf16 v[76:79], a[196:199], v[52:55], v[76:79]\n" \
  "s_waitcnt lgkmcnt(2)\n" \
  "v_mfma_f32_16x16x32_bf16 v[56:59], a[40:43], v[32:35], v[56:59]\n" \
  "ds_read_b128 v[48:51], v9 offset:12288\n" \
  "v_mfma_f32_16x16x32_bf16 v[68:71], a[40:43], v[36:39], v[68:71]\n" \
  "ds_read_b128 v[52:55], v9 offset:32768\n" \
  "v_mfma_f32_16x16x32_bf16 v[60:63], a[120:123], v[32:35], v[60:63]\n" \
  "buffer_load_dwordx4 v1, s[36:39], s68 offen offset:3072 sc1 lds\n" \
  "v_mfma_f32_16x16x32_bf16 v[72:75], a[120:123], v[36:39], v[72:75]\n" \
  "v_mfma_f32_16x16x32_bf16 v[64:67], a[200:203], v[32:35], v[64:67]\n" \
  "v_mfma_f32_16x16x32_bf16 v[76:79], a[200:203], v[36:39], v[76:79]\n" \
  "s_waitcnt lgkmcnt(2)\n" \
  "v_mfma_f32_16x16x32_bf16 v[56:59], a[44:47], v[40:43], v[56:59]\n" \
  "ds_read_b128 v[32:35], v9 offset:13312\n" \
  "v_mfma_f32_16x16x32_bf16 v[68:71], a[44:47], v[44:47], v[68:71]\n" \
  "ds_read_b128 v[36:39], v9 offset:33792\n" \
  "v_mfma_f32_16x16x32_bf16 v[60:63], a[124:127], v[40:43], v[60:63]\n" \
  "s_add_u32 m0, s53, 4096\n" \
  "v_mfma_f32_16x16x32_bf16 v[72:75], a[124:127], v[44:47], v[72:75]\n" \
  "buffer_load_dwordx4 v1, s[36:39], s70 offen sc1 lds\n" \
  "v_mfma_f32_16x16x32_bf16 v[64:67], a[204:207], v[40:43], v[64:67]\n" \
  "v_mfma_f32_16x16x32_bf16 v[76:79], a[204:207], v[44:47], v[76:79]\n" \
  "s_waitcnt lgkmcnt(2)\n" \
  "v_mfma_f32_16x16x32_bf16 v[56:59], a[48:51], v[48:51], v[56:59]\n" \
  "ds_read_b128 v[40:43], v9 offset:14336\n" \
  "v_mfma_f32_16x16x32_bf16 v[68:71], a[48:51], v[52:55], v[68:71]\n" \
  "ds_read_b128 v[44:47], v9 offset:34816\n" \
  "v_mfma_f32_16x16x32_bf16 v[60:63], a[128:131], v[48:51], v[60:63]\n" \
  "buffer_load_dwordx4 v1, s[36:39], s70 offen offset:1024 sc1 lds\n" \
  "v_mfma_f32_16x16x32_bf16 v[72:75], a[128:131], v[52:55], v[72:75]\n" \
  "v_mfma_f32_16x16x32_bf16 v[64:67], a[208:211], v[48:51], v[64:67]\n" \
  "v_mfma_f32_16x16x32_bf16 v[76:79], a[208:211], v[52:55], v[76:79]\n" \
  "s_waitcnt vmcnt(6) lgkmcnt(2)\n" \
  "s_barrier\n" \
  "v_mfma_f32_16x16x32_bf16 v[56:59], a[52:55], v[32:35], v[56:59]\n" \
  "ds_read_b128 v[48:51], v9 offset:15360\n" \
  "v_mfma_f32_16x16x32_bf16 v[68:71], a[52:55], v[36:39], v[68:71]\n" \
  "ds_read_b128 v[52:55], v9 offset:35840\n" \
  "v_mfma_f32_16x16x32_bf16 v[60:63], a[132:135], v[32:35], v[60:63]\n" \
  "buffer_atomic_add v11, v4, s[40:43], s54 offen\n" \
  "v_mfma_f32_16x16x32_bf16 v[72:75], a[132:135], v[36:39], v[72:75]\n" \
  "s_add_u32 s54, s54, s55\n" \
  "v_mfma_f32_16x16x32_bf16 v[64:67], a[212:215], v[32:35], v[64:67]\n" \
  "s_and_b32 s54, s54, 0xffffdfff\n" \
  "v_mfma_f32_16x16x32_bf16 v[76:79], a[212:215], v[36:39], v[76:79]\n" \
  "s_waitcnt lgkmcnt(2)\n" \
  "v_mfma_f32_16x16x32_bf16 v[56:59], a[56:59], v[40:43], v[56:59]\n" \
  "ds_read_b128 v[32:35], v9 offset:16384\n" \
  "v_mfma_f32_16x16x32_bf16 v[68:71], a[56:59], v[44:47], v[68:71]\n" \
  "ds_read_b128 v[36:39], v9 offset:36864\n" \
  "v_mfma_f32_16x16x32_bf16 v[60:63], a[136:139], v[40:43], v[60:63]\n" \
  "buffer_load_dwordx4 v1, s[36:39], s70 offen offset:2048 sc1 lds\n" \
  "v_mfma_f32_16x16x32_bf16 v[72:75], a[136:139], v[44:47], v[72:75]\n" \
  "v_readlane_b32 s69, v3, s72\n" \
  "v_mfma_f32_16x16x32_bf16 v[64:67], a[216:219], v[40:43], v[64:67]\n" \
  "s_add_u32 s72, s72, 1\n" \
  "v_mfma_f32_16x16x32_bf16 v[76:79], a[216:219], v[44:47], v[76:79]\n" \
  "s_waitcnt lgkmcnt(2)\n" \
  "v_mfma_f32_16x16x32_bf16 v[56:59], a[60:63], v[48:51], v[56:59]\n" \
  "ds_read_b128 v[40:43], v9 offset:17408\n" \
  "v_mfma_f32_16x16x32_bf16 v[68:71], a[60:63], v[52:55], v[68:71]\n" \
  "ds_read_b128 v[44:47], v9 offset:37888\n" \
  "v_mfma_f32_16x16x32_bf16 v[60:63], a[140:143], v[48:51], v[60:63]\n" \
  "buffer_load_dwordx4 v1, s[36:39], s70 offen offset:3072 sc1 lds\n" \
  "v_mfma_f32_16x16x32_bf16 v[72:75], a[140:143], v[52:55], v[72:75]\n" \
  "v_mfma_f32_16x16x32_bf16 v[64:67], a[220:223], v[48:51], v[64:67]\n" \
  "v_mfma_f32_16x16x32_bf16 v[76:79], a[220:223], v[52:55], v[76:79]\n" \
  "s_waitcnt lgkmcnt(2)\n" \
  "v_mfma_f32_16x16x32_bf16 v[56:59], a[64:67], v[32:35], v[56:59]\n" \
  "ds_read_b128 v[48:51], v9 offset:18432\n" \
  "v_mfma_f32_16x16x32_bf16 v[68:71], a[64:67], v[36:39], v[68:71]\n" \
  "ds_read_b128 v[52:55], v9 offset:38912\n" \
  "v_mfma_f32_16x16x32_bf16 v[60:63], a[144:147], v[32:35], v[60:63]\n" \
  "s_add_u32 m0, s53, 8192\n" \
  "v_mfma_f32_16x16x32_bf16 v[72:75], a[144:147], v[36:39], v[72:75]\n" \
  "buffer_load_dwordx4 v1, s[36:39], s71 offen sc1 lds\n" \
  "v_mfma_f32_16x16x32_bf16 v[64:67], a[224:227], v[32:35], v[64:67]\n" \
  "v_mfma_f32_16x16x32_bf16 v[76:79], a[224:227], v[36:39], v[76:79]\n" \
  "s_waitcnt lgkmcnt(2)\n" \
  "v_mfma_f32_16x16x32_bf16 v[56:59], a[68:71], v[40:43], v[56:59]\n" \
  "ds_read_b128 v[32:35], v9 offset:19456\n" \
  "v_mfma_f32_16x16x32_bf16 v[68:71], a[68:71], v[44:47], v[68:71]\n" \
  "ds_read_b128 v[36:39], v9 offset:39936\n" \
  "v_mfma_f32_16x16x32_bf16 v[60:63], a[148:151], v[40:43], v[60:63]\n" \
  "buffer_load_dwordx4 v1, s[36:39], s71 offen offset:1024 sc1 lds\n" \
  "v_mfma_f32_16x16x32_bf16 v[72:75], a[148:151], v[44:47], v[72:75]\n" \
  "s_add_u32 s68, s68, 40960\n" \
  "v_mfma_f32_16x16x32_bf16 v[64:67], a[228:231], v[40:43], v[64:67]\n" \
  "v_mfma_f32_16x16x32_bf16 v[76:79], a[228:231], v[44:47], v[76:79]\n" \
  "s_waitcnt vmcnt(11) lgkmcnt(2)\n" \
  "s_barrier\n" \
  "v_mfma_f32_16x16x32_bf16 v[56:59], a[72:75], v[48:51], v[56:59]\n" \
  "ds_read_b128 v[40:43], v10 offset:0\n" \
  "v_mfma_f32_16x16x32_bf16 v[68:71], a[72:75], v[52:55], v[68:71]\n" \
  "ds_read_b128 v[44:47], v10 offset:20480\n" \
  "v_mfma_f32_16x16x32_bf16 v[60:63], a[152:155], v[48:51], v[60:63]\n" \
  "v_mfma_f32_16x16x32_bf16 v[72:75], a[152:155], v[52:55], v[72:75]\n" \
  "v_mfma_f32_16x16x32_bf16 v[64:67], a[232:235], v[48:51], v[64:67]\n" \
  "v_mfma_f32_16x16x32_bf16 v[76:79], a[232:235], v[52:55], v[76:79]\n" \
  "s_waitcnt lgkmcnt(2)\n" \
  "v_mfma_f32_16x16x32_bf16 v[56:59], a[76:79], v[32:35], v[56:59]\n" \
  "ds_read_b128 v[48:51], v10 offset:1024\n" \
  "v_mfma_f32_16x16x32_bf16 v[68:71], a[76:79], v[36:39], v[68:71]\n" \
  "ds_read_b128 v[52:55], v10 offset:21504\n" \
  "v_mfma_f32_16x16x32_bf16 v[60:63], a[156:159], v[32:35], v[60:63]\n" \
  "s_cmp_eq_u32 s72, 64\n" \
  "v_mfma_f32_16x16x32_bf16 v[72:75], a[156:159], v[36:39], v[72:75]\n" \
  "v_mfma_f32_16x16x32_bf16 v[64:67], a[236:239], v[32:35], v[64:67]\n" \
  "v_mfma_f32_16x16x32_bf16 v[76:79], a[236:239], v[36:39], v[76:79]\n" \
  "s_waitcnt lgkmcnt(2)\n" \
  "v_mfma_f32_16x16x32_bf16 v[100:103], a[0:3], v[40:43], 0\n" \
  "ds_read_b128 v[32:35], v10 offset:2048\n" \
  "v_mfma_f32_16x16x32_bf16 v[112:115], a[0:3], v[44:47], 0\n" \
  "ds_read_b128 v[36:39], v10 offset:22528\n" \
  "v_mfma_f32_16x16x32_bf16 v[104:107], a[80:83], v[40:43], 0\n" \
  "v_cvt_pk_bf16_f32 v16, v56, v57\n" \
  "v_mfma_f32_16x16x32_bf16 v[116:119], a[80:83], v[44:47], 0\n" \
  "v_cvt_pk_bf16_f32 v17, v58, v59\n" \
  "v_mfma_f32_16x16x32_bf16 v[108:111], a[160:163], v[40:43], 0\n" \
  "v_cvt_pk_bf16_f32 v18, v60, v61\n" \
  "v_mfma_f32_16x16x32_bf16 v[120:123], a[160:163], v[44:47], 0\n" \
  "v_cvt_pk_bf16_f32 v19, v62, v63\n" \
  "s_waitcnt lgkmcnt(2)\n" \
  "s_barrier\n" \
  "v_mfma_f32_16x16x32_bf16 v[100:103], a[4:7], v[48:51], v[100:103]\n" \
  "ds_read_b128 v[40:43], v10 offset:3072\n" \
  "v_mfma_f32_16x16x32_bf16 v[112:115], a[4:7], v[52:55], v[112:115]\n" \
  "ds_read_b128 v[44:47], v10 offset:23552\n" \
  "v_mfma_f32_16x16x32_bf16 v[104:107], a[84:87], v[48:51], v[104:107]\n" \
  "buffer_store_dwordx4 v[16:19], v5, s[40:43], s69 offen offset:0 sc1\n" \
  "v_mfma_f32_16x16x32_bf16 v[116:119], a[84:87], v[52:55], v[116:119]\n" \
  "v_cvt_pk_bf16_f32 v20, v68, v69\n" \
  "v_mfma_f32_16x16x32_bf16 v[108:111], a[164:167], v[48:51], v[108:111]\n" \
  "v_cvt_pk_bf16_f32 v21, v70, v71\n" \
  "v_mfma_f32_16x16x32_bf16 v[120:123], a[164:167], v[52:55], v[120:123]\n" \
  "v_cvt_pk_bf16_f32 v22, v72, v73\n" \
  "s_waitcnt lgkmcnt(2)\n" \
  "v_mfma_f32_16x16x32_bf16 v[100:103], a[8:11], v[32:35], v[100:103]\n" \
  "ds_read_b128 v[48:51], v10 offset:4096\n" \
  "v_mfma_f32_16x16x32_bf16 v[112:115], a[8:11], v[36:39], v[112:115]\n" \
  "ds_read_b128 v[52:55], v10 offset:24576\n" \
  "v_mfma_f32_16x16x32_bf16 v[104:107], a[88:91], v[32:35], v[104:107]\n" \
  "v_cvt_pk_bf16_f32 v23, v74, v75\n" \
  "v_mfma_f32_16x16x32_bf16 v[116:119], a[88:91], v[36:39], v[116:119]\n" \
  "buffer_store_dwordx4 v[20:23], v5, s[40:43], s69 offen offset:1024 sc1\n" \
  "v_mfma_f32_16x16x32_bf16 v[108:111], a[168:171], v[32:35], v[108:111]\n" \
  "v_cvt_pk_bf16_f32 v24, v64, v65\n" \
  "v_mfma_f32_16x16x32_bf16 v[120:123], a[168:171], v[36:39], v[120:123]\n" \
  "v_cvt_pk_bf16_f32 v25, v66, v67\n" \
  "s_waitcnt lgkmcnt(2)\n" \
  "v_mfma_f32_16x16x32_bf16 v[100:103], a[12:15], v[40:43], v[100:103]\n" \
  "ds_read_b128 v[32:35], v10 offset:5120\n" \
  "v_mfma_f32_16x16x32_bf16 v[112:115], a[12:15], v[44:47], v[112:115]\n" \
  "ds_read_b128 v[36:39], v10 offset:25600\n" \
  "v_mfma_f32_16x16x32_bf16 v[104:107], a[92:95], v[40:43], v[104:107]\n" \
  "v_cvt_pk_bf16_f32 v26, v76, v77\n" \
  "v_mfma_f32_16x16x32_bf16 v[116:119], a[92:95], v[44:47], v[116:119]\n" \
  "v_cvt_pk_bf16_f32 v27, v78, v79\n" \
  "v_mfma_f32_16x16x32_bf16 v[108:111], a[172:175], v[40:43], v[108:111]\n" \
  "buffer_store_dwordx4 v[24:27], v5, s[40:43], s69 offen offset:2048 sc1\n" \
  "v_mfma_f32_16x16x32_bf16 v[120:123], a[172:175], v[44:47], v[120:123]\n" \
  "s_waitcnt lgkmcnt(2)\n" \
  "v_mfma_f32_16x16x32_bf16 v[100:103], a[16:19], v[48:51], v[100:103]\n" \
  "ds_read_b128 v[40:43], v10 offset:6144\n" \
  "v_mfma_f32_16x16x32_bf16 v[112:115], a[16:19], v[52:55], v[112:115]\n" \
  "ds_read_b128 v[44:47], v10 offset:26624\n" \
  "v_mfma_f32_16x16x32_bf16 v[104:107], a[96:99], v[48:51], v[104:107]\n" \
  "buffer_store_dwordx4 v[16:19], v2, s[40:43], s69 offen offset:0 sc1\n" \
  "v_mfma_f32_16x16x32_bf16 v[116:119], a[96:99], v[52:55], v[116:119]\n" \
  "v_mfma_f32_16x16x32_bf16 v[108:111], a[176:179], v[48:51], v[108:111]\n" \
  "v_mfma_f32_16x16x32_bf16 v[120:123], a[176:179], v[52:55], v[120:123]\n" \
  "s_waitcnt lgkmcnt(2)\n" \
  "v_mfma_f32_16x16x32_bf16 v[100:103], a[20:23], v[32:35], v[100:103]\n" \
  "ds_read_b128 v[48:51], v10 offset:7168\n" \
  "v_mfma_f32_16x16x32_bf16 v[112:115], a[20:23], v[36:39], v[112:115]\n" \
  "ds_read_b128 v[52:55], v10 offset:27648\n" \
  "v_mfma_f32_16x16x32_bf16 v[104:107], a[100:103], v[32:35], v[104:107]\n" \
  "buffer_store_dwordx4 v[20:23], v2, s[40:43], s69 offen offset:1024 sc1\n" \
  "v_mfma_f32_16x16x32_bf16 v[116:119], a[100:103], v[36:39], v[116:119]\n" \
  "v_mfma_f32_16x16x32_bf16 v[108:111], a[180:183], v[32:35], v[108:111]\n" \
  "v_mfma_f32_16x16x32_bf16 v[120:123], a[180:183], v[36:39], v[120:123]\n" \
  "s_waitcnt lgkmcnt(2)\n" \
  "v_mfma_f32_16x16x32_bf16 v[100:103], a[24:27], v[40:43], v[100:103]\n" \
  "ds_read_b128 v[32:35], v10 offset:8192\n" \
  "v_mfma_f32_16x16x32_bf16 v[112:115], a[24:27], v[44:47], v[112:115]\n" \
  "ds_read_b128 v[36:39], v10 offset:28672\n" \
  "v_mfma_f32_16x16x32_bf16 v[104:107], a[104:107], v[40:43], v[104:107]\n" \
  "buffer_store_dwordx4 v[24:27], v2, s[40:43], s69 offen offset:2048 sc1\n" \
  "v_mfma_f32_16x16x32_bf16 v[116:119], a[104:107], v[44:47], v[116:119]\n" \
  "s_nop 0\n" \
  "v_mfma_f32_16x16x32_bf16 v[108:111], a[184:187], v[40:43], v[108:111]\n" \
  "s_nop 0\n" \
  "v_mfma_f32_16x16x32_bf16 v[120:123], a[184:187], v[44:47], v[120:123]\n" \
  "s_waitcnt lgkmcnt(2)\n" \
  "v_mfma_f32_16x16x32_bf16 v[100:103], a[28:31], v[48:51], v[100:103]\n" \
  "ds_read_b128 v[40:43], v10 offset:9216\n" \
  "v_mfma_f32_16x16x32_bf16 v[112:115], a[28:31], v[52:55], v[112:115]\n" \
  "ds_read_b128 v[44:47], v10 offset:29696\n" \
  "v_mfma_f32_16x16x32_bf16 v[104:107], a[108:111], v[48:51], v[104:107]\n" \
  "s_add_u32 s70, s68, 4096\n" \
  "v_mfma_f32_16x16x32_bf16 v[116:119], a[108:111], v[52:55], v[116:119]\n" \
  "s_add_u32 s71, s68, 8192\n" \
  "v_mfma_f32_16x16x32_bf16 v[108:111], a[188:191], v[48:51], v[108:111]\n" \
  "s_add_u32 m0, s53, 40960\n" \
  "v_mfma_f32_16x16x32_bf16 v[120:123], a[188:191], v[52:55], v[120:123]\n" \
  "buffer_load_dwordx4 v1, s[36:39], s68 offen sc1 lds\n" \
  "s_waitcnt lgkmcnt(2)\n" \
  "v_mfma_f32_16x16x32_bf16 v[100:103], a[32:35], v[32:35], v[100:103]\n" \
  "ds_read_b128 v[48:51], v10 offset:10240\n" \
  "v_mfma_f32_16x16x32_bf16 v[112:115], a[32:35], v[36:39], v[112:115]\n" \
  "ds_read_b128 v[52:55], v10 offset:30720\n" \
  "v_mfma_f32_16x16x32_bf16 v[104:107], a[112:115], v[32:35], v[104:107]\n" \
  "buffer_load_dwordx4 v1, s[36:39], s68 offen offset:1024 sc1 lds\n" \
  "v_mfma_f32_16x16x32_bf16 v[116:119], a[112:115], v[36:39], v[116:119]\n" \
  "v_mfma_f32_16x16x32_bf16 v[108:111], a[192:195], v[32:35], v[108:111]\n" \
  "v_mfma_f32_16x16x32_bf16 v[120:123], a[192:195], v[36:39], v[120:123]\n" \
  "s_waitcnt lgkmcnt(2)\n" \
  "v_mfma_f32_16x16x32_bf16 v[100:103], a[36:39], v[40:43], v[100:103]\n" \
  "ds_read_b128 v[32:35], v10 offset:11264\n" \
  "v_mfma_f32_16x16x32_bf16 v[112:115], a[36:39], v[44:47], v[112:115]\n" \
  "ds_read_b128 v[36:39], v10 offset:31744\n" \
  "v_mfma_f32_16x16x32_bf16 v[104:107], a[116:119], v[40:43], v[104:107]\n" \
  "buffer_load_dwordx4 v1, s[36:39], s68 offen offset:2048 sc1 lds\n" \
  "v_mfma_f32_16x16x32_bf16 v[116:119], a[116:119], v[44:47], v[116:119]\n" \
  "v_mfma_f32_16x16x32_bf16 v[108:111], a[196:199], v[40:43], v[108:111]\n" \
  "v_mfma_f32_16x16x32_bf16 v[120:123], a[196:199], v[44:47], v[120:123]\n" \
  "s_waitcnt lgkmcnt(2)\n" \
  "v_mfma_f32_16x16x32_bf16 v[100:103], a[40:43], v[48:51], v[100:103]\n" \
  "ds_read_b128 v[40:43], v10 offset:12288\n" \
  "v_mfma_f32_16x16x32_bf16 v[112:115], a[40:43], v[52:55], v[112:115]\n" \
  "ds_read_b128 v[44:47], v10 offset:32768\n" \
  "v_mfma_f32_16x16x32_bf16 v[104:107], a[120:123], v[48:51], v[104:107]\n" \
  "buffer_load_dwordx4 v1, s[36:39], s68 offen offset:3072 sc1 lds\n" \
  "v_mfma_f32_16x16x32_bf16 v[116:119], a[120:123], v[52:55], v[116:119]\n" \
  "v_mfma_f32_16x16x32_bf16 v[108:111], a[200:203], v[48:51], v[108:111]\n" \
  "v_mfma_f32_16x16x32_bf16 v[120:123], a[200:203], v[52:55], v[120:123]\n" \
  "s_waitcnt lgkmcnt(2)\n" \
  "v_mfma_f32_16x16x32_bf16 v[100:103], a[44:47], v[32:35], v[100:103]\n" \
  "ds_read_b128 v[48:51], v10 offset:13312\n" \
  "v_mfma_f32_16x16x32_bf16 v[112:115], a[44:47], v[36:39], v[112:115]\n" \
  "ds_read_b128 v[52:55], v10 offset:33792\n" \
  "v_mfma_f32_16x16x32_bf16 v[104:107], a[124:127], v[32:35], v[104:107]\n" \
  "s_add_u32 m0, s53, 45056\n" \
  "v_mfma_f32_16x16x32_bf16 v[116:119], a[124:127], v[36:39], v[116:119]\n" \
  "buffer_load_dwordx4 v1, s[36:39], s70 offen sc1 lds\n" \
  "v_mfma_f32_16x16x32_bf16 v[108:111], a[204:207], v[32:35], v[108:111]\n" \
  "v_mfma_f32_16x16x32_bf16 v[120:123], a[204:207], v[36:39], v[120:123]\n" \
  "s_waitcnt lgkmcnt(2)\n" \
  "v_mfma_f32_16x16x32_bf16 v[100:103], a[48:51], v[40:43], v[100:103]\n" \
  "ds_read_b128 v[32:35], v10 offset:14336\n" \
  "v_mfma_f32_16x16x32_bf16 v[112:115], a[48:51], v[44:47], v[112:115]\n" \
  "ds_read_b128 v[36:39], v10 offset:34816\n" \
  "v_mfma_f32_16x16x32_bf16 v[104:107], a[128:131], v[40:43], v[104:107]\n" \
  "buffer_load_dwordx4 v1, s[36:39], s70 offen offset:1024 sc1 lds\n" \
  "v_mfma_f32_16x16x32_bf16 v[116:119], a[128:131], v[44:47], v[116:119]\n" \
  "v_mfma_f32_16x16x32_bf16 v[108:111], a[208:211], v[40:43], v[108:111]\n" \
  "v_mfma_f32_16x16x32_bf16 v[120:123], a[208:211], v[44:47], v[120:123]\n" \
  "s_waitcnt vmcnt(6) lgkmcnt(2)\n" \
  "s_barrier\n" \
  "v_mfma_f32_16x16x32_bf16 v[100:103], a[52:55], v[48:51], v[100:103]\n" \
  "ds_read_b128 v[40:43], v10 offset:15360\n" \
  "v_mfma_f32_16x16x32_bf16 v[112:115], a[52:55], v[52:55], v[112:115]\n" \
  "ds_read_b128 v[44:47], v10 offset:35840\n" \
  "v_mfma_f32_16x16x32_bf16 v[104:107], a[132:135], v[48:51], v[104:107]\n" \
  "buffer_atomic_add v11, v4, s[40:43], s54 offen\n" \
  "v_mfma_f32_16x16x32_bf16 v[116:119], a[132:135], v[52:55], v[116:119]\n" \
  "s_add_u32 s54, s54, s55\n" \
  "v_mfma_f32_16x16x32_bf16 v[108:111], a[212:215], v[48:51], v[108:111]\n" \
  "s_and_b32 s54, s54, 0xffffdfff\n" \
  "v_mfma_f32_16x16x32_bf16 v[120:123], a[212:215], v[52:55], v[120:123]\n" \
  "s_waitcnt lgkmcnt(2)\n" \
  "v_mfma_f32_16x16x32_bf16 v[100:103], a[56:59], v[32:35], v[100:103]\n" \
  "ds_read_b128 v[48:51], v10 offset:16384\n" \
  "v_mfma_f32_16x16x32_bf16 v[112:115], a[56:59], v[36:39], v[112:115]\n" \
  "ds_read_b128 v[52:55], v10 offset:36864\n" \
  "v_mfma_f32_16x16x32_bf16 v[104:107], a[136:139], v[32:35], v[104:107]\n" \
  "buffer_load_dwordx4 v1, s[36:39], s70 offen offset:2048 sc1 lds\n" \
  "v_mfma_f32_16x16x32_bf16 v[116:119], a[136:139], v[36:39], v[116:119]\n" \
  "v_readlane_b32 s69, v3, s72\n" \
  "v_mfma_f32_16x16x32_bf16 v[108:111], a[216:219], v[32:35], v[108:111]\n" \
  "s_add_u32 s72, s72, 1\n" \
  "v_mfma_f32_16x16x32_bf16 v[120:123], a[216:219], v[36:39], v[120:123]\n" \
  "s_waitcnt lgkmcnt(2)\n" \
  "v_mfma_f32_16x16x32_bf16 v[100:103], a[60:63], v[40:43], v[100:103]\n" \
  "ds_read_b128 v[32:35], v10 offset:17408\n" \
  "v_mfma_f32_16x16x32_bf16 v[112:115], a[60:63], v[44:47], v[112:115]\n" \
  "ds_read_b128 v[36:39], v10 offset:37888\n" \
  "v_mfma_f32_16x16x32_bf16 v[104:107], a[140:143], v[40:43], v[104:107]\n" \
  "buffer_load_dwordx4 v1, s[36:39], s70 offen offset:3072 sc1 lds\n" \
  "v_mfma_f32_16x16x32_bf16 v[116:119], a[140:143], v[44:47], v[116:119]\n" \
  "v_mfma_f32_16x16x32_bf16 v[108:111], a[220:223], v[40:43], v[108:111]\n" \
  "v_mfma_f32_16x16x32_bf16 v[120:123], a[220:223], v[44:47], v[120:123]\n" \
  "s_waitcnt lgkmcnt(2)\n" \
  "v_mfma_f32_16x16x32_bf16 v[100:103], a[64:67], v[48:51], v[100:103]\n" \
  "ds_read_b128 v[40:43], v10 offset:18432\n" \
  "v_mfma_f32_16x16x32_bf16 v[112:115], a[64:67], v[52:55], v[112:115]\n" \
  "ds_read_b128 v[44:47], v10 offset:38912\n" \
  "v_mfma_f32_16x16x32_bf16 v[104:107], a[144:147], v[48:51], v[104:107]\n" \
  "s_add_u32 m0, s53, 49152\n" \
  "v_mfma_f32_16x16x32_bf16 v[116:119], a[144:147], v[52:55], v[116:119]\n" \
  "buffer_load_dwordx4 v1, s[36:39], s71 offen sc1 lds\n" \
  "v_mfma_f32_16x16x32_bf16 v[108:111], a[224:227], v[48:51], v[108:111]\n" \
  "v_mfma_f32_16x16x32_bf16 v[120:123], a[224:227], v[52:55], v[120:123]\n" \
  "s_waitcnt lgkmcnt(2)\n" \
  "v_mfma_f32_16x16x32_bf16 v[100:103], a[68:71], v[32:35], v[100:103]\n" \
  "ds_read_b128 v[48:51], v10 offset:19456\n" \
  "v_mfma_f32_16x16x32_bf16 v[112:115], a[68:71], v[36:39], v[112:115]\n" \
  "ds_read_b128 v[52:55], v10 offset:39936\n" \
  "v_mfma_f32_16x16x32_bf16 v[104:107], a[148:151], v[32:35], v[104:107]\n" \
  "buffer_load_dwordx4 v1, s[36:39], s71 offen offset:1024 sc1 lds\n" \
  "v_mfma_f32_16x16x32_bf16 v[116:119], a[148:151], v[36:39], v[116:119]\n" \
  "s_add_u32 s68, s68, 40960\n" \
  "v_mfma_f32_16x16x32_bf16 v[108:111], a[228:231], v[32:35], v[108:111]\n" \
  "v_mfma_f32_16x16x32_bf16 v[120:123], a[228:231], v[36:39], v[120:123]\n" \
  "s_waitcnt vmcnt(11) lgkmcnt(2)\n" \
  "s_barrier\n" \
  "v_mfma_f32_16x16x32_bf16 v[100:103], a[72:75], v[40:43], v[100:103]\n" \
  "v_mfma_f32_16x16x32_bf16 v[112:115], a[72:75], v[44:47], v[112:115]\n" \
  "v_mfma_f32_16x16x32_bf16 v[104:107], a[152:155], v[40:43], v[104:107]\n" \
  "v_mfma_f32_16x16x32_bf16 v[116:119], a[152:155], v[44:47], v[116:119]\n" \
  "v_mfma_f32_16x16x32_bf16 v[108:111], a[232:235], v[40:43], v[108:111]\n" \
  "v_mfma_f32_16x16x32_bf16 v[120:123], a[232:235], v[44:47], v[120:123]\n" \
  "s_waitcnt lgkmcnt(0)\n" \
  "v_mfma_f32_16x16x32_bf16 v[100:103], a[76:79], v[48:51], v[100:103]\n" \
  "s_cmp_eq_u32 s72, 64\n" \
  "v_mfma_f32_16x16x32_bf16 v[112:115], a[76:79], v[52:55], v[112:115]\n" \
  "v_mfma_f32_16x16x32_bf16 v[104:107], a[156:159], v[48:51], v[104:107]\n" \
  "v_mfma_f32_16x16x32_bf16 v[116:119], a[156:159], v[52:55], v[116:119]\n" \
  "v_mfma_f32_16x16x32_bf16 v[108:111], a[236:239], v[48:51], v[108:111]\n" \
  "v_mfma_f32_16x16x32_bf16 v[120:123], a[236:239], v[52:55], v[120:123]\n" \
  "LPE_%=:\n" \
  "s_nop 6\n" \
  "v_cvt_pk_bf16_f32 v16, v100, v101\n" \
  "v_cvt_pk_bf16_f32 v17, v102, v103\n" \
  "v_cvt_pk_bf16_f32 v18, v104, v105\n" \
  "v_cvt_pk_bf16_f32 v19, v106, v107\n" \
  "buffer_store_dwordx4 v[16:19], v5, s[40:43], s69 offen offset:0 sc1\n" \
  "v_cvt_pk_bf16_f32 v20, v112, v113\n" \
  "v_cvt_pk_bf16_f32 v21, v114, v115\n" \
  "v_cvt_pk_bf16_f32 v22, v116, v117\n" \
  "v_cvt_pk_bf16_f32 v23, v118, v119\n" \
  "buffer_store_dwordx4 v[20:23], v5, s[40:43], s69 offen offset:1024 sc1\n" \
  "v_cvt_pk_bf16_f32 v24, v108, v109\n" \
  "v_cvt_pk_bf16_f32 v25, v110, v111\n" \
  "v_cvt_pk_bf16_f32 v26, v120, v121\n" \
  "v_cvt_pk_bf16_f32 v27, v122, v123\n" \
  "buffer_store_dwordx4 v[24:27], v5, s[40:43], s69 offen offset:2048 sc1\n" \
  "buffer_store_dwordx4 v[16:19], v2, s[40:43], s69 offen offset:0 sc1\n" \
  "buffer_store_dwordx4 v[20:23], v2, s[40:43], s69 offen offset:1024 sc1\n" \
  "buffer_store_dwordx4 v[24:27], v2, s[40:43], s69 offen offset:2048 sc1\n" \
  "s_nop 0\n" \
  "s_waitcnt vmcnt(0) lgkmcnt(0)\n" \
  "s_barrier\n" \
  "buffer_atomic_add v11, v4, s[40:43], s54 offen\n" \
  "s_add_u32 s54, s54, s55\n" \
  "s_and_b32 s54, s54, 0xffffdfff\n"
#define PROJV_CLOBBERS \
  "v9", "v10", "v11", "v12", "v13", "v14", "v15", "v16", "v17", "v18", "v19", "v20", "v21", "v22", "v23", "v24", "v25", "v26", "v27", "v28", "v29", \
  "v30", "v31", "v32", "v33", "v34", "v35", "v36", "v37", "v38", "v39", "v40", "v41", "v42", "v43", "v44", "v45", "v46", "v47", "v48", "v49", "v50", \
  "v51", "v52", "v53", "v54", "v55", "v56", "v57", "v58", "v59", "v60", "v61", "v62", "v63", "v64", "v65", "v66", "v67", "v68", "v69", "v70", "v71", \
  "v72", "v73", "v74", "v75", "v76", "v77", "v78", "v79", "v100", "v101", "v102", "v103", "v104", "v105", "v106", "v107", "v108", "v109", "v110", \
  "v111", "v112", "v113", "v114", "v115", "v116", "v117", "v118", "v119", "v120", "v121", "v122", "v123", "s68", "s69", "s70", "s71", "s72", "s73"
#define PROJV_ASM \
  "s_waitcnt vmcnt(10)\n" \
  "s_barrier\n" \
  "ds_read_b128 v[32:35], v0 offset:0\n" \
  "ds_read_b128 v[36:39], v0 offset:20480\n" \
  "ds_read_b128 v[40:43], v0 offset:1024\n" \
  "ds_read_b128 v[44:47], v0 offset:21504\n" \
  "v_mov_b32 v12, -1\n" \
  "v_mov_b32 v13, -1\n" \
  "v_mov_b32 v14, -1\n" \
  "v_mov_b32 v15, -1\n" \
  "v_mov_b32 v11, 1\n" \
  "v_add_u32 v9, 40960, v0\n" \
  "v_add_u32 v10, 81920, v0\n" \
  "s_mul_i32 s68, s52, 40960\n" \
  "s_add_u32 s68, s68, 81920\n" \
  "s_mov_b32 s72, s52\n" \
  "s_waitcnt lgkmcnt(2)\n" \
  "v_mfma_f32_16x16x32_bf16 v[56:59], a[0:3], v[32:35], 0\n" \
  "ds_read_b128 v[48:51], v0 offset:2048\n" \
  "v_mfma_f32_16x16x32_bf16 v[68:71], a[0:3], v[36:39], 0\n" \
  "ds_read_b128 v[52:55], v0 offset:22528\n" \
  "v_mfma_f32_16x16x32_bf16 v[60:63], a[80:83], v[32:35], 0\n" \
  "v_mfma_f32_16x16x32_bf16 v[72:75], a[80:83], v[36:39], 0\n" \
  "v_mfma_f32_16x16x32_bf16 v[64:67], a[160:163], v[32:35], 0\n" \
  "v_mfma_f32_16x16x32_bf16 v[76:79], a[160:163], v[36:39], 0\n" \
  "s_waitcnt lgkmcnt(2)\n" \
  "s_barrier\n" \
  "v_mfma_f32_16x16x32_bf16 v[56:59], a[4:7], v[40:43], v[56:59]\n" \
  "ds_read_b128 v[32:35], v0 offset:3072\n" \
  "v_mfma_f32_16x16x32_bf16 v[68:71], a[4:7], v[44:47], v[68:71]\n" \
  "ds_read_b128 v[36:39], v0 offset:23552\n" \
  "v_mfma_f32_16x16x32_bf16 v[60:63], a[84:87], v[40:43], v[60:63]\n" \
  "v_mfma_f32_16x16x32_bf16 v[72:75], a[84:87], v[44:47], v[72:75]\n" \
  "v_mfma_f32_16x16x32_bf16 v[64:67], a[164:167], v[40:43], v[64:67]\n" \
  "v_mfma_f32_16x16x32_bf16 v[76:79], a[164:167], v[44:47], v[76:79]\n" \
  "s_waitcnt lgkmcnt(2)\n" \
  "v_mfma_f32_16x16x32_bf16 v[56:59], a[8:11], v[48:51], v[56:59]\n" \
  "ds_read_b128 v[40:43], v0 offset:4096\n" \
  "v_mfma_f32_16x16x32_bf16 v[68:71], a[8:11], v[52:55], v[68:71]\n" \
  "ds_read_b128 v[44:47], v0 offset:24576\n" \
  "v_mfma_f32_16x16x32_bf16 v[60:63], a[88:91], v[48:51], v[60:63]\n" \
  "v_mfma_f32_16x16x32_bf16 v[72:75], a[88:91], v[52:55], v[72:75]\n" \
  "v_mfma_f32_16x16x32_bf16 v[64:67], a[168:171], v[48:51], v[64:67]\n" \
  "v_mfma_f32_16x16x32_bf16 v[76:79], a[168:171], v[52:55], v[76:79]\n" \
  "s_waitcnt lgkmcnt(2)\n" \
  "v_mfma_f32_16x16x32_bf16 v[56:59], a[12:15], v[32:35], v[56:59]\n" \
  "ds_read_b128 v[48:51], v0 offset:5120\n" \
  "v_mfma_f32_16x16x32_bf16 v[68:71], a[12:15], v[36:39], v[68:71]\n" \
  "ds_read_b128 v[52:55], v0 offset:25600\n" \
  "v_mfma_f32_16x16x32_bf16 v[60:63], a[92:95], v[32:35], v[60:63]\n" \
  "v_mfma_f32_16x16x32_bf16 v[72:75], a[92:95], v[36:39], v[72:75]\n" \
  "v_mfma_f32_16x16x32_bf16 v[64:67], a[172:175], v[32:35], v[64:67]\n" \
  "v_mfma_f32_16x16x32_bf16 v[76:79], a[172:175], v[36:39], v[76:79]\n" \
  "s_waitcnt lgkmcnt(2)\n" \
  "v_mfma_f32_16x16x32_bf16 v[56:59], a[16:19], v[40:43], v[56:59]\n" \
  "ds_read_b128 v[32:35], v0 offset:6144\n" \
  "v_mfma_f32_16x16x32_bf16 v[68:71], a[16:19], v[44:47], v[68:71]\n" \
  "ds_read_b128 v[36:39], v0 offset:26624\n" \
  "v_mfma_f32_16x16x32_bf16 v[60:63], a[96:99], v[40:43], v[60:63]\n" \
  "v_mfma_f32_16x16x32_bf16 v[72:75], a[96:99], v[44:47], v[72:75]\n" \
  "v_mfma_f32_16x16x32_bf16 v[64:67], a[176:179], v[40:43], v[64:67]\n" \
  "v_mfma_f32_16x16x32_bf16 v[76:79], a[176:179], v[44:47], v[76:79]\n" \
  "s_waitcnt lgkmcnt(2)\n" \
  "v_mfma_f32_16x16x32_bf16 v[56:59], a[20:23], v[48:51], v[56:59]\n" \
  "ds_read_b128 v[40:43], v0 offset:7168\n" \
  "v_mfma_f32_16x16x32_bf16 v[68:71], a[20:23], v[52:55], v[68:71]\n" \
  "ds_read_b128 v[44:47], v0 offset:27648\n" \
  "v_mfma_f32_16x16x32_bf16 v[60:63], a[100:103], v[48:51], v[60:63]\n" \
  "v_mfma_f32_16x16x32_bf16 v[72:75], a[100:103], v[52:55], v[72:75]\n" \
  "v_mfma_f32_16x16x32_bf16 v[64:67], a[180:183], v[48:51], v[64:67]\n" \
  "v_mfma_f32_16x16x32_bf16 v[76:79], a[180:183], v[52:55], v[76:79]\n" \
  "s_waitcnt lgkmcnt(2)\n" \
  "v_mfma_f32_16x16x32_bf16 v[56:59], a[24:27], v[32:35], v[56:59]\n" \
  "ds_read_b128 v[48:51], v0 offset:8192\n" \
  "v_mfma_f32_16x16x32_bf16 v[68:71], a[24:27], v[36:39], v[68:71]\n" \
  "ds_read_b128 v[52:55], v0 offset:28672\n" \
  "v_mfma_f32_16x16x32_bf16 v[60:63], a[104:107], v[32:35], v[60:63]\n" \
  "v_mfma_f32_16x16x32_bf16 v[72:75], a[104:107], v[36:39], v[72:75]\n" \
  "v_mfma_f32_16x16x32_bf16 v[64:67], a[184:187], v[32:35], v[64:67]\n" \
  "v_mfma_f32_16x16x32_bf16 v[76:79], a[184:187], v[36:39], v[76:79]\n" \
  "s_waitcnt lgkmcnt(2)\n" \
  "v_mfma_f32_16x16x32_bf16 v[56:59], a[28:31], v[40:43], v[56:59]\n" \
  "ds_read_b128 v[32:35], v0 offset:9216\n" \
  "v_mfma_f32_16x16x32_bf16 v[68:71], a[28:31], v[44:47], v[68:71]\n" \
  "ds_read_b128 v[36:39], v0 offset:29696\n" \
  "v_mfma_f32_16x16x32_bf16 v[60:63], a[108:111], v[40:43], v[60:63]\n" \
  "s_add_u32 s70, s68, 4096\n" \
  "v_mfma_f32_16x16x32_bf16 v[72:75], a[108:111], v[44:47], v[72:75]\n" \
  "s_add_u32 s71, s68, 8192\n" \
  "v_mfma_f32_16x16x32_bf16 v[64:67], a[188:191], v[40:43], v[64:67]\n" \
  "s_add_u32 m0, s53, 81920\n" \
  "v_mfma_f32_16x16x32_bf16 v[76:79], a[188:191], v[44:47], v[76:79]\n" \
  "buffer_load_dwordx4 v1, s[36:39], s68 offen sc1 lds\n" \
  "s_waitcnt lgkmcnt(2)\n" \
  "v_mfma_f32_16x16x32_bf16 v[56:59], a[32:35], v[48:51], v[56:59]\n" \
  "ds_read_b128 v[40:43], v0 offset:10240\n" \
  "v_mfma_f32_16x16x32_bf16 v[68:71], a[32:35], v[52:55], v[68:71]\n" \
  "ds_read_b128 v[44:47], v0 offset:30720\n" \
  "v_mfma_f32_16x16x32_bf16 v[60:63], a[112:115], v[48:51], v[60:63]\n" \
  "buffer_load_dwordx4 v1, s[36:39], s68 offen offset:1024 sc1 lds\n" \
  "v_mfma_f32_16x16x32_bf16 v[72:75], a[112:115], v[52:55], v[72:75]\n" \
  "v_mfma_f32_16x16x32_bf16 v[64:67], a[192:195], v[48:51], v[64:67]\n" \
  "v_mfma_f32_16x16x32_bf16 v[76:79], a[192:195], v[52:55], v[76:79]\n" \
  "s_waitcnt lgkmcnt(2)\n" \
  "v_mfma_f32_16x16x32_bf16 v[56:59], a[36:39], v[32:35], v[56:59]\n" \
  "ds_read_b128 v[48:51], v0 offset:11264\n" \
  "v_mfma_f32_16x16x32_bf16 v[68:71], a[36:39], v[36:39], v[68:71]\n" \
  "ds_read_b128 v[52:55], v0 offset:31744\n" \
  "v_mfma_f32_16x16x32_bf16 v[60:63], a[116:119], v[32:35], v[60:63]\n" \
  "buffer_load_dwordx4 v1, s[36:39], s68 offen offset:2048 sc1 lds\n" \
  "v_mfma_f32_16x16x32_bf16 v[72:75], a[116:119], v[36:39], v[72:75]\n" \
  "v_mfma_f32_16x16x32_bf16 v[64:67], a[196:199], v[32:35], v[64:67]\n" \
  "v_mfma_f32_16x16x32_bf16 v[76:79], a[196:199], v[36:39], v[76:79]\n" \
  "s_waitcnt lgkmcnt(2)\n" \
  "v_mfma_f32_16x16x32_bf16 v[56:59], a[40:43], v[40:43], v[56:59]\n" \
  "ds_read_b128 v[32:35], v0 offset:12288\n" \
  "v_mfma_f32_16x16x32_bf16 v[68:71], a[40:43], v[44:47], v[68:71]\n" \
  "ds_read_b128 v[36:39], v0 offset:32768\n" \
  "v_mfma_f32_16x16x32_bf16 v[60:63], a[120:123], v[40:43], v[60:63]\n" \
  "buffer_load_dwordx4 v1, s[36:39], s68 offen offset:3072 sc1 lds\n" \
  "v_mfma_f32_16x16x32_bf16 v[72:75], a[120:123], v[44:47], v[72:75]\n" \
  "v_mfma_f32_16x16x32_bf16 v[64:67], a[200:203], v[40:43], v[64:67]\n" \
  "v_mfma_f32_16x16x32_bf16 v[76:79], a[200:203], v[44:47], v[76:79]\n" \
  "s_waitcnt lgkmcnt(2)\n" \
  "v_mfma_f32_16x16x32_bf16 v[56:59], a[44:47], v[48:51], v[56:59]\n" \
  "ds_read_b128 v[40:43], v0 offset:13312\n" \
  "v_mfma_f32_16x16x32_bf16 v[68:71], a[44:47], v[52:55], v[68:71]\n" \
  "ds_read_b128 v[44:47], v0 offset:33792\n" \
  "v_mfma_f32_16x16x32_bf16 v[60:63], a[124:127], v[48:51], v[60:63]\n" \
  "s_add_u32 m0, s53, 86016\n" \
  "v_mfma_f32_16x16x32_bf16 v[72:75], a[124:127], v[52:55], v[72:75]\n" \
  "buffer_load_dwordx4 v1, s[36:39], s70 offen sc1 lds\n" \
  "v_mfma_f32_16x16x32_bf16 v[64:67], a[204:207], v[48:51], v[64:67]\n" \
  "v_mfma_f32_16x16x32_bf16 v[76:79], a[204:207], v[52:55], v[76:79]\n" \
  "s_waitcnt lgkmcnt(2)\n" \
  "v_mfma_f32_16x16x32_bf16 v[56:59], a[48:51], v[32:35], v[56:59]\n" \
  "ds_read_b128 v[48:51], v0 offset:14336\n" \
  "v_mfma_f32_16x16x32_bf16 v[68:71], a[48:51], v[36:39], v[68:71]\n" \
  "ds_read_b128 v[52:55], v0 offset:34816\n" \
  "v_mfma_f32_16x16x32_bf16 v[60:63], a[128:131], v[32:35], v[60:63]\n" \
  "buffer_load_dwordx4 v1, s[36:39], s70 offen offset:1024 sc1 lds\n" \
  "v_mfma_f32_16x16x32_bf16 v[72:75], a[128:131], v[36:39], v[72:75]\n" \
  "v_mfma_f32_16x16x32_bf16 v[64:67], a[208:211], v[32:35], v[64:67]\n" \
  "v_mfma_f32_16x16x32_bf16 v[76:79], a[208:211], v[36:39], v[76:79]\n" \
  "s_waitcnt lgkmcnt(2)\n" \
  "v_mfma_f32_16x16x32_bf16 v[56:59], a[52:55], v[40:43], v[56:59]\n" \
  "ds_read_b128 v[32:35], v0 offset:15360\n" \
  "v_mfma_f32_16x16x32_bf16 v[68:71], a[52:55], v[44:47], v[68:71]\n" \
  "ds_read_b128 v[36:39], v0 offset:35840\n" \
  "v_mfma_f32_16x16x32_bf16 v[60:63], a[132:135], v[40:43], v[60:63]\n" \
  "v_mfma_f32_16x16x32_bf16 v[72:75], a[132:135], v[44:47], v[72:75]\n" \
  "v_mfma_f32_16x16x32_bf16 v[64:67], a[212:215], v[40:43], v[64:67]\n" \
  "v_mfma_f32_16x16x32_bf16 v[76:79], a[212:215], v[44:47], v[76:79]\n" \
  "s_waitcnt lgkmcnt(2)\n" \
  "v_mfma_f32_16x16x32_bf16 v[56:59], a[56:59], v[48:51], v[56:59]\n" \
  "ds_read_b128 v[40:43], v0 offset:16384\n" \
  "v_mfma_f32_16x16x32_bf16 v[68:71], a[56:59], v[52:55], v[68:71]\n" \
  "ds_read_b128 v[44:47], v0 offset:36864\n" \
  "v_mfma_f32_16x16x32_bf16 v[60:63], a[136:139], v[48:51], v[60:63]\n" \
  "buffer_load_dwordx4 v1, s[36:39], s70 offen offset:2048 sc1 lds\n" \
  "v_mfma_f32_16x16x32_bf16 v[72:75], a[136:139], v[52:55], v[72:75]\n" \
  "v_readlane_b32 s69, v3, s72\n" \
  "v_mfma_f32_16x16x32_bf16 v[64:67], a[216:219], v[48:51], v[64:67]\n" \
  "v_readlane_b32 s73, v6, s72\n" \
  "v_mfma_f32_16x16x32_bf16 v[76:79], a[216:219], v[52:55], v[76:79]\n" \
  "s_add_u32 s72, s72, 1\n" \
  "s_waitcnt lgkmcnt(2)\n" \
  "v_mfma_f32_16x16x32_bf16 v[56:59], a[60:63], v[32:35], v[56:59]\n" \
  "ds_read_b128 v[48:51], v0 offset:17408\n" \
  "v_mfma_f32_16x16x32_bf16 v[68:71], a[60:63], v[36:39], v[68:71]\n" \
  "ds_read_b128 v[52:55], v0 offset:37888\n" \
  "v_mfma_f32_16x16x32_bf16 v[60:63], a[140:143], v[32:35], v[60:63]\n" \
  "buffer_load_dwordx4 v1, s[36:39], s70 offen offset:3072 sc1 lds\n" \
  "v_mfma_f32_16x16x32_bf16 v[72:75], a[140:143], v[36:39], v[72:75]\n" \
  "v_mfma_f32_16x16x32_bf16 v[64:67], a[220:223], v[32:35], v[64:67]\n" \
  "v_mfma_f32_16x16x32_bf16 v[76:79], a[220:223], v[36:39], v[76:79]\n" \
  "s_waitcnt lgkmcnt(2)\n" \
  "v_mfma_f32_16x16x32_bf16 v[56:59], a[64:67], v[40:43], v[56:59]\n" \
  "ds_read_b128 v[32:35], v0 offset:18432\n" \
  "v_mfma_f32_16x16x32_bf16 v[68:71], a[64:67], v[44:47], v[68:71]\n" \
  "ds_read_b128 v[36:39], v0 offset:38912\n" \
  "v_mfma_f32_16x16x32_bf16 v[60:63], a[144:147], v[40:43], v[60:63]\n" \
  "s_add_u32 m0, s53, 90112\n" \
  "v_mfma_f32_16x16x32_bf16 v[72:75], a[144:147], v[44:47], v[72:75]\n" \
  "buffer_load_dwordx4 v1, s[36:39], s71 offen sc1 lds\n" \
  "v_mfma_f32_16x16x32_bf16 v[64:67], a[224:227], v[40:43], v[64:67]\n" \
  "v_mfma_f32_16x16x32_bf16 v[76:79], a[224:227], v[44:47], v[76:79]\n" \
  "s_waitcnt lgkmcnt(2)\n" \
  "v_mfma_f32_16x16x32_bf16 v[56:59], a[68:71], v[48:51], v[56:59]\n" \
  "ds_read_b128 v[40:43], v0 offset:19456\n" \
  "v_mfma_f32_16x16x32_bf16 v[68:71], a[68:71], v[52:55], v[68:71]\n" \
  "ds_read_b128 v[44:47], v0 offset:39936\n" \
  "v_mfma_f32_16x16x32_bf16 v[60:63], a[148:151], v[48:51], v[60:63]\n" \
  "buffer_load_dwordx4 v1, s[36:39], s71 offen offset:1024 sc1 lds\n" \
  "v_mfma_f32_16x16x32_bf16 v[72:75], a[148:151], v[52:55], v[72:75]\n" \
  "s_add_u32 s68, s68, 40960\n" \
  "v_mfma_f32_16x16x32_bf16 v[64:67], a[228:231], v[48:51], v[64:67]\n" \
  "v_mfma_f32_16x16x32_bf16 v[76:79], a[228:231], v[52:55], v[76:79]\n" \
  "s_waitcnt vmcnt(10) lgkmcnt(2)\n" \
  "s_barrier\n" \
  "v_mfma_f32_16x16x32_bf16 v[56:59], a[72:75], v[32:35], v[56:59]\n" \
  "ds_read_b128 v[48:51], v9 offset:0\n" \
  "v_mfma_f32_16x16x32_bf16 v[68:71], a[72:75], v[36:39], v[68:71]\n" \
  "ds_read_b128 v[52:55], v9 offset:20480\n" \
  "v_mfma_f32_16x16x32_bf16 v[60:63], a[152:155], v[32:35], v[60:63]\n" \
  "v_mfma_f32_16x16x32_bf16 v[72:75], a[152:155], v[36:39], v[72:75]\n" \
  "v_mfma_f32_16x16x32_bf16 v[64:67], a[232:235], v[32:35], v[64:67]\n" \
  "v_mfma_f32_16x16x32_bf16 v[76:79], a[232:235], v[36:39], v[76:79]\n" \
  "s_waitcnt lgkmcnt(2)\n" \
  "v_mfma_f32_16x16x32_bf16 v[56:59], a[76:79], v[40:43], v[56:59]\n" \
  "ds_read_b128 v[32:35], v9 offset:1024\n" \
  "v_mfma_f32_16x16x32_bf16 v[68:71], a[76:79], v[44:47], v[68:71]\n" \
  "ds_read_b128 v[36:39], v9 offset:21504\n" \
  "v_mfma_f32_16x16x32_bf16 v[60:63], a[156:159], v[40:43], v[60:63]\n" \
  "s_cmp_eq_u32 s72, 64\n" \
  "v_mfma_f32_16x16x32_bf16 v[72:75], a[156:159], v[44:47], v[72:75]\n" \
  "v_mfma_f32_16x16x32_bf16 v[64:67], a[236:239], v[40:43], v[64:67]\n" \
  "v_mfma_f32_16x16x32_bf16 v[76:79], a[236:239], v[44:47], v[76:79]\n" \
  "s_waitcnt lgkmcnt(2)\n" \
  "v_mfma_f32_16x16x32_bf16 v[100:103], a[0:3], v[48:51], 0\n" \
  "ds_read_b128 v[40:43], v9 offset:2048\n" \
  "v_mfma_f32_16x16x32_bf16 v[112:115], a[0:3], v[52:55], 0\n" \
  "ds_read_b128 v[44:47], v9 offset:22528\n" \
  "v_mfma_f32_16x16x32_bf16 v[104:107], a[80:83], v[48:51], 0\n" \
  "v_cvt_pk_bf16_f32 v16, v56, v57\n" \
  "v_mfma_f32_16x16x32_bf16 v[116:119], a[80:83], v[52:55], 0\n" \
  "v_cvt_pk_bf16_f32 v17, v58, v59\n" \
  "v_mfma_f32_16x16x32_bf16 v[108:111], a[160:163], v[48:51], 0\n" \
  "v_cvt_pk_bf16_f32 v18, v60, v61\n" \
  "v_mfma_f32_16x16x32_bf16 v[120:123], a[160:163], v[52:55], 0\n" \
  "v_cvt_pk_bf16_f32 v19, v62, v63\n" \
  "s_waitcnt lgkmcnt(2)\n" \
  "s_barrier\n" \
  "v_mfma_f32_16x16x32_bf16 v[100:103], a[4:7], v[32:35], v[100:103]\n" \
  "ds_read_b128 v[48:51], v9 offset:3072\n" \
  "v_mfma_f32_16x16x32_bf16 v[112:115], a[4:7], v[36:39], v[112:115]\n" \
  "ds_read_b128 v[52:55], v9 offset:23552\n" \
  "v_mfma_f32_16x16x32_bf16 v[104:107], a[84:87], v[32:35], v[104:107]\n" \
  "buffer_store_dwordx4 v[16:19], v2, s[40:43], s69 offen offset:0 sc1\n" \
  "v_mfma_f32_16x16x32_bf16 v[116:119], a[84:87], v[36:39], v[116:119]\n" \
  "v_cvt_pk_bf16_f32 v20, v68, v69\n" \
  "v_mfma_f32_16x16x32_bf16 v[108:111], a[164:167], v[32:35], v[108:111]\n" \
  "v_cvt_pk_bf16_f32 v21, v70, v71\n" \
  "v_mfma_f32_16x16x32_bf16 v[120:123], a[164:167], v[36:39], v[120:123]\n" \
  "v_cvt_pk_bf16_f32 v22, v72, v73\n" \
  "s_waitcnt lgkmcnt(2)\n" \
  "v_mfma_f32_16x16x32_bf16 v[100:103], a[8:11], v[40:43], v[100:103]\n" \
  "ds_read_b128 v[32:35], v9 offset:4096\n" \
  "v_mfma_f32_16x16x32_bf16 v[112:115], a[8:11], v[44:47], v[112:115]\n" \
  "ds_read_b128 v[36:39], v9 offset:24576\n" \
  "v_mfma_f32_16x16x32_bf16 v[104:107], a[88:91], v[40:43], v[104:107]\n" \
  "v_cvt_pk_bf16_f32 v23, v74, v75\n" \
  "v_mfma_f32_16x16x32_bf16 v[116:119], a[88:91], v[44:47], v[116:119]\n" \
  "buffer_store_dwordx4 v[20:23], v2, s[40:43], s69 offen offset:1024 sc1\n" \
  "v_mfma_f32_16x16x32_bf16 v[108:111], a[168:171], v[40:43], v[108:111]\n" \
  "v_cvt_pk_bf16_f32 v24, v64, v65\n" \
  "v_mfma_f32_16x16x32_bf16 v[120:123], a[168:171], v[44:47], v[120:123]\n" \
  "v_cvt_pk_bf16_f32 v25, v66, v67\n" \
  "s_waitcnt lgkmcnt(2)\n" \
  "v_mfma_f32_16x16x32_bf16 v[100:103], a[12:15], v[48:51], v[100:103]\n" \
  "ds_read_b128 v[40:43], v9 offset:5120\n" \
  "v_mfma_f32_16x16x32_bf16 v[112:115], a[12:15], v[52:55], v[112:115]\n" \
  "ds_read_b128 v[44:47], v9 offset:25600\n" \
  "v_mfma_f32_16x16x32_bf16 v[104:107], a[92:95], v[48:51], v[104:107]\n" \
  "v_cvt_pk_bf16_f32 v26, v76, v77\n" \
  "v_mfma_f32_16x16x32_bf16 v[116:119], a[92:95], v[52:55], v[116:119]\n" \
  "v_cvt_pk_bf16_f32 v27, v78, v79\n" \
  "v_mfma_f32_16x16x32_bf16 v[108:111], a[172:175], v[48:51], v[108:111]\n" \
  "buffer_store_dwordx4 v[24:27], v2, s[40:43], s69 offen offset:2048 sc1\n" \
  "v_mfma_f32_16x16x32_bf16 v[120:123], a[172:175], v[52:55], v[120:123]\n" \
  "s_waitcnt lgkmcnt(2)\n" \
  "v_mfma_f32_16x16x32_bf16 v[100:103], a[16:19], v[32:35], v[100:103]\n" \
  "ds_read_b128 v[48:51], v9 offset:6144\n" \
  "v_mfma_f32_16x16x32_bf16 v[112:115], a[16:19], v[36:39], v[112:115]\n" \
  "ds_read_b128 v[52:55], v9 offset:26624\n" \
  "v_mfma_f32_16x16x32_bf16 v[104:107], a[96:99], v[32:35], v[104:107]\n" \
  "buffer_store_dwordx4 v[12:15], v5, s[40:43], s73 offen offset:0 sc1\n" \
  "v_mfma_f32_16x16x32_bf16 v[116:119], a[96:99], v[36:39], v[116:119]\n" \
  "v_mfma_f32_16x16x32_bf16 v[108:111], a[176:179], v[32:35], v[108:111]\n" \
  "v_mfma_f32_16x16x32_bf16 v[120:123], a[176:179], v[36:39], v[120:123]\n" \
  "s_waitcnt lgkmcnt(2)\n" \
  "v_mfma_f32_16x16x32_bf16 v[100:103], a[20:23], v[40:43], v[100:103]\n" \
  "ds_read_b128 v[32:35], v9 offset:7168\n" \
  "v_mfma_f32_16x16x32_bf16 v[112:115], a[20:23], v[44:47], v[112:115]\n" \
  "ds_read_b128 v[36:39], v9 offset:27648\n" \
  "v_mfma_f32_16x16x32_bf16 v[104:107], a[100:103], v[40:43], v[104:107]\n" \
  "buffer_store_dwordx4 v[12:15], v5, s[40:43], s73 offen offset:1024 sc1\n" \
  "v_mfma_f32_16x16x32_bf16 v[116:119], a[100:103], v[44:47], v[116:119]\n" \
  "v_mfma_f32_16x16x32_bf16 v[108:111], a[180:183], v[40:43], v[108:111]\n" \
  "v_mfma_f32_16x16x32_bf16 v[120:123], a[180:183], v[44:47], v[120:123]\n" \
  "s_waitcnt lgkmcnt(2)\n" \
  "v_mfma_f32_16x16x32_bf16 v[100:103], a[24:27], v[48:51], v[100:103]\n" \
  "ds_read_b128 v[40:43], v9 offset:8192\n" \
  "v_mfma_f32_16x16x32_bf16 v[112:115], a[24:27], v[52:55], v[112:115]\n" \
  "ds_read_b128 v[44:47], v9 offset:28672\n" \
  "v_mfma_f32_16x16x32_bf16 v[104:107], a[104:107], v[48:51], v[104:107]\n" \
  "buffer_store_dwordx4 v[12:15], v5, s[40:43], s73 offen offset:2048 sc1\n" \
  "v_mfma_f32_16x16x32_bf16 v[116:119], a[104:107], v[52:55], v[116:119]\n" \
  "s_nop 0\n" \
  "v_mfma_f32_16x16x32_bf16 v[108:111], a[184:187], v[48:51], v[108:111]\n" \
  "buffer_store_dwordx4 v[12:15], v5, s[40:43], s73 offen offset:3072 sc1\n" \
  "v_mfma_f32_16x16x32_bf16 v[120:123], a[184:187], v[52:55], v[120:123]\n" \
  "s_waitcnt lgkmcnt(2)\n" \
  "v_mfma_f32_16x16x32_bf16 v[100:103], a[28:31], v[32:35], v[100:103]\n" \
  "ds_read_b128 v[48:51], v9 offset:9216\n" \
  "v_mfma_f32_16x16x32_bf16 v[112:115], a[28:31], v[36:39], v[112:115]\n" \
  "ds_read_b128 v[52:55], v9 offset:29696\n" \
  "v_mfma_f32_16x16x32_bf16 v[104:107], a[108:111], v[32:35], v[104:107]\n" \
  "s_add_u32 s70, s68, 4096\n" \
  "v_mfma_f32_16x16x32_bf16 v[116:119], a[108:111], v[36:39], v[116:119]\n" \
  "s_add_u32 s71, s68, 8192\n" \
  "v_mfma_f32_16x16x32_bf16 v[108:111], a[188:191], v[32:35], v[108:111]\n" \
  "s_add_u32 m0, s53, 0\n" \
  "v_mfma_f32_16x16x32_bf16 v[120:123], a[188:191], v[36:39], v[120:123]\n" \
  "buffer_load_dwordx4 v1, s[36:39], s68 offen sc1 lds\n" \
  "s_waitcnt lgkmcnt(2)\n" \
  "v_mfma_f32_16x16x32_bf16 v[100:103], a[32:35], v[40:43], v[100:103]\n" \
  "ds_read_b128 v[32:35], v9 offset:10240\n" \
  "v_mfma_f32_16x16x32_bf16 v[112:115], a[32:35], v[44:47], v[112:115]\n" \
  "ds_read_b128 v[36:39], v9 offset:30720\n" \
  "v_mfma_f32_16x16x32_bf16 v[104:107], a[112:115], v[40:43], v[104:107]\n" \
  "buffer_load_dwordx4 v1, s[36:39], s68 offen offset:1024 sc1 lds\n" \
  "v_mfma_f32_16x16x32_bf16 v[116:119], a[112:115], v[44:47], v[116:119]\n" \
  "v_mfma_f32_16x16x32_bf16 v[108:111], a[192:195], v[40:43], v[108:111]\n" \
  "v_mfma_f32_16x16x32_bf16 v[120:123], a[192:195], v[44:47], v[120:123]\n" \
  "s_waitcnt lgkmcnt(2)\n" \
  "v_mfma_f32_16x16x32_bf16 v[100:103], a[36:39], v[48:51], v[100:103]\n" \
  "ds_read_b128 v[40:43], v9 offset:11264\n" \
  "v_mfma_f32_16x16x32_bf16 v[112:115], a[36:39], v[52:55], v[112:115]\n" \
  "ds_read_b128 v[44:47], v9 offset:31744\n" \
  "v_mfma_f32_16x16x32_bf16 v[104:107], a[116:119], v[48:51], v[104:107]\n" \
  "buffer_load_dwordx4 v1, s[36:39], s68 offen offset:2048 sc1 lds\n" \
  "v_mfma_f32_16x16x32_bf16 v[116:119], a[116:119], v[52:55], v[116:119]\n" \
  "v_mfma_f32_16x16x32_bf16 v[108:111], a[196:199], v[48:51], v[108:111]\n" \
  "v_mfma_f32_16x16x32_bf16 v[120:123], a[196:199], v[52:55], v[120:123]\n" \
  "s_waitcnt lgkmcnt(2)\n" \
  "v_mfma_f32_16x16x32_bf16 v[100:103], a[40:43], v[32:35], v[100:103]\n" \
  "ds_read_b128 v[48:51], v9 offset:12288\n" \
  "v_mfma_f32_16x16x32_bf16 v[112:115], a[40:43], v[36:39], v[112:115]\n" \
  "ds_read_b128 v[52:55], v9 offset:32768\n" \
  "v_mfma_f32_16x16x32_bf16 v[104:107], a[120:123], v[32:35], v[104:107]\n" \
  "buffer_load_dwordx4 v1, s[36:39], s68 offen offset:3072 sc1 lds\n" \
  "v_mfma_f32_16x16x32_bf16 v[116:119], a[120:123], v[36:39], v[116:119]\n" \
  "v_mfma_f32_16x16x32_bf16 v[108:111], a[200:203], v[32:35], v[108:111]\n" \
  "v_mfma_f32_16x16x32_bf16 v[120:123], a[200:203], v[36:39], v[120:123]\n" \
  "s_waitcnt lgkmcnt(2)\n" \
  "v_mfma_f32_16x16x32_bf16 v[100:103], a[44:47], v[40:43], v[100:103]\n" \
  "ds_read_b128 v[32:35], v9 offset:13312\n" \
  "v_mfma_f32_16x16x32_bf16 v[112:115], a[44:47], v[44:47], v[112:115]\n" \
  "ds_read_b128 v[36:39], v9 offset:33792\n" \
  "v_mfma_f32_16x16x32_bf16 v[104:107], a[124:127], v[40:43], v[104:107]\n" \
  "s_add_u32 m0, s53, 4096\n" \
  "v_mfma_f32_16x16x32_bf16 v[116:119], a[124:127], v[44:47], v[116:119]\n" \
  "buffer_load_dwordx4 v1, s[36:39], s70 offen sc1 lds\n" \
  "v_mfma_f32_16x16x32_bf16 v[108:111], a[204:207], v[40:43], v[108:111]\n" \
  "v_mfma_f32_16x16x32_bf16 v[120:123], a[204:207], v[44:47], v[120:123]\n" \
  "s_waitcnt lgkmcnt(2)\n" \
  "v_mfma_f32_16x16x32_bf16 v[100:103], a[48:51], v[48:51], v[100:103]\n" \
  "ds_read_b128 v[40:43], v9 offset:14336\n" \
  "v_mfma_f32_16x16x32_bf16 v[112:115], a[48:51], v[52:55], v[112:115]\n" \
  "ds_read_b128 v[44:47], v9 offset:34816\n" \
  "v_mfma_f32_16x16x32_bf16 v[104:107], a[128:131], v[48:51], v[104:107]\n" \
  "buffer_load_dwordx4 v1, s[36:39], s70 offen offset:1024 sc1 lds\n" \
  "v_mfma_f32_16x16x32_bf16 v[116:119], a[128:131], v[52:55], v[116:119]\n" \
  "v_mfma_f32_16x16x32_bf16 v[108:111], a[208:211], v[48:51], v[108:111]\n" \
  "v_mfma_f32_16x16x32_bf16 v[120:123], a[208:211], v[52:55], v[120:123]\n" \
  "s_waitcnt vmcnt(6) lgkmcnt(2)\n" \
  "s_barrier\n" \
  "v_mfma_f32_16x16x32_bf16 v[100:103], a[52:55], v[32:35], v[100:103]\n" \
  "ds_read_b128 v[48:51], v9 offset:15360\n" \
  "v_mfma_f32_16x16x32_bf16 v[112:115], a[52:55], v[36:39], v[112:115]\n" \
  "ds_read_b128 v[52:55], v9 offset:35840\n" \
  "v_mfma_f32_16x16x32_bf16 v[104:107], a[132:135], v[32:35], v[104:107]\n" \
  "buffer_atomic_add v11, v4, s[40:43], s54 offen\n" \
  "v_mfma_f32_16x16x32_bf16 v[116:119], a[132:135], v[36:39], v[116:119]\n" \
  "s_add_u32 s54, s54, s55\n" \
  "v_mfma_f32_16x16x32_bf16 v[108:111], a[212:215], v[32:35], v[108:111]\n" \
  "s_and_b32 s54, s54, 0xffffdfff\n" \
  "v_mfma_f32_16x16x32_bf16 v[120:123], a[212:215], v[36:39], v[120:123]\n" \
  "s_waitcnt lgkmcnt(2)\n" \
  "v_mfma_f32_16x16x32_bf16 v[100:103], a[56:59], v[40:43], v[100:103]\n" \
  "ds_read_b128 v[32:35], v9 offset:16384\n" \
  "v_mfma_f32_16x16x32_bf16 v[112:115], a[56:59], v[44:47], v[112:115]\n" \
  "ds_read_b128 v[36:39], v9 offset:36864\n" \
  "v_mfma_f32_16x16x32_bf16 v[104:107], a[136:139], v[40:43], v[104:107]\n" \
  "buffer_load_dwordx4 v1, s[36:39], s70 offen offset:2048 sc1 lds\n" \
  "v_mfma_f32_16x16x32_bf16 v[116:119], a[136:139], v[44:47], v[116:119]\n" \
  "v_readlane_b32 s69, v3, s72\n" \
  "v_mfma_f32_16x16x32_bf16 v[108:111], a[216:219], v[40:43], v[108:111]\n" \
  "v_readlane_b32 s73, v6, s72\n" \
  "v_mfma_f32_16x16x32_bf16 v[120:123], a[216:219], v[44:47], v[120:123]\n" \
  "s_add_u32 s72, s72, 1\n" \
  "s_waitcnt lgkmcnt(2)\n" \
  "v_mfma_f32_16x16x32_bf16 v[100:103], a[60:63], v[48:51], v[100:103]\n" \
  "ds_read_b128 v[40:43], v9 offset:17408\n" \
  "v_mfma_f32_16x16x32_bf16 v[112:115], a[60:63], v[52:55], v[112:115]\n" \
  "ds_read_b128 v[44:47], v9 offset:37888\n" \
  "v_mfma_f32_16x16x32_bf16 v[104:107], a[140:143], v[48:51], v[104:107]\n" \
  "buffer_load_dwordx4 v1, s[36:39], s70 offen offset:3072 sc1 lds\n" \
  "v_mfma_f32_16x16x32_bf16 v[116:119], a[140:143], v[52:55], v[116:119]\n" \
  "v_mfma_f32_16x16x32_bf16 v[108:111], a[220:223], v[48:51], v[108:111]\n" \
  "v_mfma_f32_16x16x32_bf16 v[120:123], a[220:223], v[52:55], v[120:123]\n" \
  "s_waitcnt lgkmcnt(2)\n" \
  "v_mfma_f32_16x16x32_bf16 v[100:103], a[64:67], v[32:35], v[100:103]\n" \
  "ds_read_b128 v[48:51], v9 offset:18432\n" \
  "v_mfma_f32_16x16x32_bf16 v[112:115], a[64:67], v[36:39], v[112:115]\n" \
  "ds_read_b128 v[52:55], v9 offset:38912\n" \
  "v_mfma_f32_16x16x32_bf16 v[104:107], a[144:147], v[32:35], v[104:107]\n" \
  "s_add_u32 m0, s53, 8192\n" \
  "v_mfma_f32_16x16x32_bf16 v[116:119], a[144:147], v[36:39], v[116:119]\n" \
  "buffer_load_dwordx4 v1, s[36:39], s71 offen sc1 lds\n" \
  "v_mfma_f32_16x16x32_bf16 v[108:111], a[224:227], v[32:35], v[108:111]\n" \
  "v_mfma_f32_16x16x32_bf16 v[120:123], a[224:227], v[36:39], v[120:123]\n" \
  "s_waitcnt lgkmcnt(2)\n" \
  "v_mfma_f32_16x16x32_bf16 v[100:103], a[68:71], v[40:43], v[100:103]\n" \
  "ds_read_b128 v[32:35], v9 offset:19456\n" \
  "v_mfma_f32_16x16x32_bf16 v[112:115], a[68:71], v[44:47], v[112:115]\n" \
  "ds_read_b128 v[36:39], v9 offset:39936\n" \
  "v_mfma_f32_16x16x32_bf16 v[104:107], a[148:151], v[40:43], v[104:107]\n" \
  "buffer_load_dwordx4 v1, s[36:39], s71 offen offset:1024 sc1 lds\n" \
  "v_mfma_f32_16x16x32_bf16 v[116:119], a[148:151], v[44:47], v[116:119]\n" \
  "s_add_u32 s68, s68, 40960\n" \
  "v_mfma_f32_16x16x32_bf16 v[108:111], a[228:231], v[40:43], v[108:111]\n" \
  "v_mfma_f32_16x16x32_bf16 v[120:123], a[228:231], v[44:47], v[120:123]\n" \
  "s_waitcnt vmcnt(11) lgkmcnt(2)\n" \
  "s_barrier\n" \
  "v_mfma_f32_16x16x32_bf16 v[100:103], a[72:75], v[48:51], v[100:103]\n" \
  "ds_read_b128 v[40:43], v10 offset:0\n" \
  "v_mfma_f32_16x16x32_bf16 v[112:115], a[72:75], v[52:55], v[112:115]\n" \
  "ds_read_b128 v[44:47], v10 offset:20480\n" \
  "v_mfma_f32_16x16x32_bf16 v[104:107], a[152:155], v[48:51], v[104:107]\n" \
  "v_mfma_f32_16x16x32_bf16 v[116:119], a[152:155], v[52:55], v[116:119]\n" \
  "v_mfma_f32_16x16x32_bf16 v[108:111], a[232:235], v[48:51], v[108:111]\n" \
  "v_mfma_f32_16x16x32_bf16 v[120:123], a[232:235], v[52:55], v[120:123]\n" \
  "s_waitcnt lgkmcnt(2)\n" \
  "v_mfma_f32_16x16x32_bf16 v[100:103], a[76:79], v[32:35], v[100:103]\n" \
  "ds_read_b128 v[48:51], v10 offset:1024\n" \
  "v_mfma_f32_16x16x32_bf16 v[112:115], a[76:79], v[36:39], v[112:115]\n" \
  "ds_read_b128 v[52:55], v10 offset:21504\n" \
  "v_mfma_f32_16x16x32_bf16 v[104:107], a[156:159], v[32:35], v[104:107]\n" \
  "s_cmp_eq_u32 s72, 64\n" \
  "v_mfma_f32_16x16x32_bf16 v[116:119], a[156:159], v[36:39], v[116:119]\n" \
  "v_mfma_f32_16x16x32_bf16 v[108:111], a[236:239], v[32:35], v[108:111]\n" \
  "v_mfma_f32_16x16x32_bf16 v[120:123], a[236:239], v[36:39], v[120:123]\n" \
  "s_cbranch_scc1 LPE_%=\n" \
  "s_waitcnt lgkmcnt(2)\n" \
  "v_mfma_f32_16x16x32_bf16 v[56:59], a[0:3], v[40:43], 0\n" \
  "ds_read_b128 v[32:35], v10 offset:2048\n" \
  "v_mfma_f32_16x16x32_bf16 v[68:71], a[0:3], v[44:47], 0\n" \
  "ds_read_b128 v[36:39], v10 offset:22528\n" \
  "v_mfma_f32_16x16x32_bf16 v[60:63], a[80:83], v[40:43], 0\n" \
  "v_cvt_pk_bf16_f32 v16, v100, v101\n" \
  "v_mfma_f32_16x16x32_bf16 v[72:75], a[80:83], v[44:47], 0\n" \
  "v_cvt_pk_bf16_f32 v17, v102, v103\n" \
  "v_mfma_f32_16x16x32_bf16 v[64:67], a[160:163], v[40:43], 0\n" \
  "v_cvt_pk_bf16_f32 v18, v104, v105\n" \
  "v_mfma_f32_16x16x32_bf16 v[76:79], a[160:163], v[44:47], 0\n" \
  "v_cvt_pk_bf16_f32 v19, v106, v107\n" \
  "s_waitcnt lgkmcnt(2)\n" \
  "s_barrier\n" \
  "v_mfma_f32_16x16x32_bf16 v[56:59], a[4:7], v[48:51], v[56:59]\n" \
  "ds_read_b128 v[40:43], v10 offset:3072\n" \
  "v_mfma_f32_16x16x32_bf16 v[68:71], a[4:7], v[52:55], v[68:71]\n" \
  "ds_read_b128 v[44:47], v10 offset:23552\n" \
  "v_mfma_f32_16x16x32_bf16 v[60:63], a[84:87], v[48:51], v[60:63]\n" \
  "buffer_store_dwordx4 v[16:19], v2, s[40:43], s69 offen offset:0 sc1\n" \
  "v_mfma_f32_16x16x32_bf16 v[72:75], a[84:87], v[52:55], v[72:75]\n" \
  "v_cvt_pk_bf16_f32 v20, v112, v113\n" \
  "v_mfma_f32_16x16x32_bf16 v[64:67], a[164:167], v[48:51], v[64:67]\n" \
  "v_cvt_pk_bf16_f32 v21, v114, v115\n" \
  "v_mfma_f32_16x16x32_bf16 v[76:79], a[164:167], v[52:55], v[76:79]\n" \
  "v_cvt_pk_bf16_f32 v22, v116, v117\n" \
  "s_waitcnt lgkmcnt(2)\n" \
  "v_mfma_f32_16x16x32_bf16 v[56:59], a[8:11], v[32:35], v[56:59]\n" \
  "ds_read_b128 v[48:51], v10 offset:4096\n" \
  "v_mfma_f32_16x16x32_bf16 v[68:71], a[8:11], v[36:39], v[68:71]\n" \
  "ds_read_b128 v[52:55], v10 offset:24576\n" \
  "v_mfma_f32_16x16x32_bf16 v[60:63], a[88:91], v[32:35], v[60:63]\n" \
  "v_cvt_pk_bf16_f32 v23, v118, v119\n" \
  "v_mfma_f32_16x16x32_bf16 v[72:75], a[88:91], v[36:39], v[72:75]\n" \
  "buffer_store_dwordx4 v[20:23], v2, s[40:43], s69 offen offset:1024 sc1\n" \
  "v_mfma_f32_16x16x32_bf16 v[64:67], a[168:171], v[32:35], v[64:67]\n" \
  "v_cvt_pk_bf16_f32 v24, v108, v109\n" \
  "v_mfma_f32_16x16x32_bf16 v[76:79], a[168:171], v[36:39], v[76:79]\n" \
  "v_cvt_pk_bf16_f32 v25, v110, v111\n" \
  "s_waitcnt lgkmcnt(2)\n" \
  "v_mfma_f32_16x16x32_bf16 v[56:59], a[12:15], v[40:43], v[56:59]\n" \
  "ds_read_b128 v[32:35], v10 offset:5120\n" \
  "v_mfma_f32_16x16x32_bf16 v[68:71], a[12:15], v[44:47], v[68:71]\n" \
  "ds_read_b128 v[36:39], v10 offset:25600\n" \
  "v_mfma_f32_16x16x32_bf16 v[60:63], a[92:95], v[40:43], v[60:63]\n" \
  "v_cvt_pk_bf16_f32 v26, v120, v121\n" \
  "v_mfma_f32_16x16x32_bf16 v[72:75], a[92:95], v[44:47], v[72:75]\n" \
  "v_cvt_pk_bf16_f32 v27, v122, v123\n" \
  "v_mfma_f32_16x16x32_bf16 v[64:67], a[172:175], v[40:43], v[64:67]\n" \
  "buffer_store_dwordx4 v[24:27], v2, s[40:43], s69 offen offset:2048 sc1\n" \
  "v_mfma_f32_16x16x32_bf16 v[76:79], a[172:175], v[44:47], v[76:79]\n" \
  "s_waitcnt lgkmcnt(2)\n" \
  "v_mfma_f32_16x16x32_bf16 v[56:59], a[16:19], v[48:51], v[56:59]\n" \
  "ds_read_b128 v[40:43], v10 offset:6144\n" \
  "v_mfma_f32_16x16x32_bf16 v[68:71], a[16:19], v[52:55], v[68:71]\n" \
  "ds_read_b128 v[44:47], v10 offset:26624\n" \
  "v_mfma_f32_16x16x32_bf16 v[60:63], a[96:99], v[48:51], v[60:63]\n" \
  "buffer_store_dwordx4 v[12:15], v5, s[40:43], s73 offen offset:0 sc1\n" \
  "v_mfma_f32_16x16x32_bf16 v[72:75], a[96:99], v[52:55], v[72:75]\n" \
  "v_mfma_f32_16x16x32_bf16 v[64:67], a[176:179], v[48:51], v[64:67]\n" \
  "v_mfma_f32_16x16x32_bf16 v[76:79], a[176:179], v[52:55], v[76:79]\n" \
  "s_waitcnt lgkmcnt(2)\n" \
  "v_mfma_f32_16x16x32_bf16 v[56:59], a[20:23], v[32:35], v[56:59]\n" \
  "ds_read_b128 v[48:51], v10 offset:7168\n" \
  "v_mfma_f32_16x16x32_bf16 v[68:71], a[20:23], v[36:39], v[68:71]\n" \
  "ds_read_b128 v[52:55], v10 offset:27648\n" \
  "v_mfma_f32_16x16x32_bf16 v[60:63], a[100:103], v[32:35], v[60:63]\n" \
  "buffer_store_dwordx4 v[12:15], v5, s[40:43], s73 offen offset:1024 sc1\n" \
  "v_mfma_f32_16x16x32_bf16 v[72:75], a[100:103], v[36:39], v[72:75]\n" \
  "v_mfma_f32_16x16x32_bf16 v[64:67], a[180:183], v[32:35], v[64:67]\n" \
  "v_mfma_f32_16x16x32_bf16 v[76:79], a[180:183], v[36:39], v[76:79]\n" \
  "s_waitcnt lgkmcnt(2)\n" \
  "v_mfma_f32_16x16x32_bf16 v[56:59], a[24:27], v[40:43], v[56:59]\n" \
  "ds_read_b128 v[32:35], v10 offset:8192\n" \
  "v_mfma_f32_16x16x32_bf16 v[68:71], a[24:27], v[44:47], v[68:71]\n" \
  "ds_read_b128 v[36:39], v10 offset:28672\n" \
  "v_mfma_f32_16x16x32_bf16 v[60:63], a[104:107], v[40:43], v[60:63]\n" \
  "buffer_store_dwordx4 v[12:15], v5, s[40:43], s73 offen offset:2048 sc1\n" \
  "v_mfma_f32_16x16x32_bf16 v[72:75], a[104:107], v[44:47], v[72:75]\n" \
  "s_nop 0\n" \
  "v_mfma_f32_16x16x32_bf16 v[64:67], a[184:187], v[40:43], v[64:67]\n" \
  "buffer_store_dwordx4 v[12:15], v5, s[40:43], s73 offen offset:3072 sc1\n" \
  "v_mfma_f32_16x16x32_bf16 v[76:79], a[184:187], v[44:47], v[76:79]\n" \
  "s_waitcnt lgkmcnt(2)\n" \
  "v_mfma_f32_16x16x32_bf16 v[56:59], a[28:31], v[48:51], v[56:59]\n" \
  "ds_read_b128 v[40:43], v10 offset:9216\n" \
  "v_mfma_f32_16x16x32_bf16 v[68:71], a[28:31], v[52:55], v[68:71]\n" \
  "ds_read_b128 v[44:47], v10 offset:29696\n" \
  "v_mfma_f32_16x16x32_bf16 v[60:63], a[108:111], v[48:51], v[60:63]\n" \
  "s_add_u32 s70, s68, 4096\n" \
  "v_mfma_f32_16x16x32_bf16 v[72:75], a[108:111], v[52:55], v[72:75]\n" \
  "s_add_u32 s71, s68, 8192\n" \
  "v_mfma_f32_16x16x32_bf16 v[64:67], a[188:191], v[48:51], v[64:67]\n" \
  "s_add_u32 m0, s53, 40960\n" \
  "v_mfma_f32_16x16x32_bf16 v[76:79], a[188:191], v[52:55], v[76:79]\n" \
  "buffer_load_dwordx4 v1, s[36:39], s68 offen sc1 lds\n" \
  "s_waitcnt lgkmcnt(2)\n" \
  "v_mfma_f32_16x16x32_bf16 v[56:59], a[32:35], v[32:35], v[56:59]\n" \
  "ds_read_b128 v[48:51], v10 offset:10240\n" \
  "v_mfma_f32_16x16x32_bf16 v[68:71], a[32:35], v[36:39], v[68:71]\n" \
  "ds_read_b128 v[52:55], v10 offset:30720\n" \
  "v_mfma_f32_16x16x32_bf16 v[60:63], a[112:115], v[32:35], v[60:63]\n" \
  "buffer_load_dwordx4 v1, s[36:39], s68 offen offset:1024 sc1 lds\n" \
  "v_mfma_f32_16x16x32_bf16 v[72:75], a[112:115], v[36:39], v[72:75]\n" \
  "v_mfma_f32_16x16x32_bf16 v[64:67], a[192:195], v[32:35], v[64:67]\n" \
  "v_mfma_f32_16x16x32_bf16 v[76:79], a[192:195], v[36:39], v[76:79]\n" \
  "s_waitcnt lgkmcnt(2)\n" \
  "v_mfma_f32_16x16x32_bf16 v[56:59], a[36:39], v[40:43], v[56:59]\n" \
  "ds_read_b128 v[32:35], v10 offset:11264\n" \
  "v_mfma_f32_16x16x32_bf16 v[68:71], a[36:39], v[44:47], v[68:71]\n" \
  "ds_read_b128 v[36:39], v10 offset:31744\n" \
  "v_mfma_f32_16x16x32_bf16 v[60:63], a[116:119], v[40:43], v[60:63]\n" \
  "buffer_load_dwordx4 v1, s[36:39], s68 offen offset:2048 sc1 lds\n" \
  "v_mfma_f32_16x16x32_bf16 v[72:75], a[116:119], v[44:47], v[72:75]\n" \
  "v_mfma_f32_16x16x32_bf16 v[64:67], a[196:199], v[40:43], v[64:67]\n" \
  "v_mfma_f32_16x16x32_bf16 v[76:79], a[196:199], v[44:47], v[76:79]\n" \
  "s_waitcnt lgkmcnt(2)\n" \
  "v_mfma_f32_16x16x32_bf16 v[56:59], a[40:43], v[48:51], v[56:59]\n" \
  "ds_read_b128 v[40:43], v10 offset:12288\n" \
  "v_mfma_f32_16x16x32_bf16 v[68:71], a[40:43], v[52:55], v[68:71]\n" \
  "ds_read_b128 v[44:47], v10 offset:32768\n" \
  "v_mfma_f32_16x16x32_bf16 v[60:63], a[120:123], v[48:51], v[60:63]\n" \
  "buffer_load_dwordx4 v1, s[36:39], s68 offen offset:3072 sc1 lds\n" \
  "v_mfma_f32_16x16x32_bf16 v[72:75], a[120:123], v[52:55], v[72:75]\n" \
  "v_mfma_f32_16x16x32_bf16 v[64:67], a[200:203], v[48:51], v[64:67]\n" \
  "v_mfma_f32_16x16x32_bf16 v[76:79], a[200:203], v[52:55], v[76:79]\n" \
  "s_waitcnt lgkmcnt(2)\n" \
  "v_mfma_f32_16x16x32_bf16 v[56:59], a[44:47], v[32:35], v[56:59]\n" \
  "ds_read_b128 v[48:51], v10 offset:13312\n" \
  "v_mfma_f32_16x16x32_bf16 v[68:71], a[44:47], v[36:39], v[68:71]\n" \
  "ds_read_b128 v[52:55], v10 offset:33792\n" \
  "v_mfma_f32_16x16x32_bf16 v[60:63], a[124:127], v[32:35], v[60:63]\n" \
  "s_add_u32 m0, s53, 45056\n" \
  "v_mfma_f32_16x16x32_bf16 v[72:75], a[124:127], v[36:39], v[72:75]\n" \
  "buffer_load_dwordx4 v1, s[36:39], s70 offen sc1 lds\n" \
  "v_mfma_f32_16x16x32_bf16 v[64:67], a[204:207], v[32:35], v[64:67]\n" \
  "v_mfma_f32_16x16x32_bf16 v[76:79], a[204:207], v[36:39], v[76:79]\n" \
  "s_waitcnt lgkmcnt(2)\n" \
  "v_mfma_f32_16x16x32_bf16 v[56:59], a[48:51], v[40:43], v[56:59]\n" \
  "ds_read_b128 v[32:35], v10 offset:14336\n" \
  "v_mfma_f32_16x16x32_bf16 v[68:71], a[48:51], v[44:47], v[68:71]\n" \
  "ds_read_b128 v[36:39], v10 offset:34816\n" \
  "v_mfma_f32_16x16x32_bf16 v[60:63], a[128:131], v[40:43], v[60:63]\n" \
  "buffer_load_dwordx4 v1, s[36:39], s70 offen offset:1024 sc1 lds\n" \
  "v_mfma_f32_16x16x32_bf16 v[72:75], a[128:131], v[44:47], v[72:75]\n" \
  "v_mfma_f32_16x16x32_bf16 v[64:67], a[208:211], v[40:43], v[64:67]\n" \
  "v_mfma_f32_16x16x32_bf16 v[76:79], a[208:211], v[44:47], v[76:79]\n" \
  "s_waitcnt vmcnt(6) lgkmcnt(2)\n" \
  "s_barrier\n" \
  "v_mfma_f32_16x16x32_bf16 v[56:59], a[52:55], v[48:51], v[56:59]\n" \
  "ds_read_b128 v[40:43], v10 offset:15360\n" \
  "v_mfma_f32_16x16x32_bf16 v[68:71], a[52:55], v[52:55], v[68:71]\n" \
  "ds_read_b128 v[44:47], v10 offset:35840\n" \
  "v_mfma_f32_16x16x32_bf16 v[60:63], a[132:135], v[48:51], v[60:63]\n" \
  "buffer_atomic_add v11, v4, s[40:43], s54 offen\n" \
  "v_mfma_f32_16x16x32_bf16 v[72:75], a[132:135], v[52:55], v[72:75]\n" \
  "s_add_u32 s54, s54, s55\n" \
  "v_mfma_f32_16x16x32_bf16 v[64:67], a[212:215], v[48:51], v[64:67]\n" \
  "s_and_b32 s54, s54, 0xffffdfff\n" \
  "v_mfma_f32_16x16x32_bf16 v[76:79], a[212:215], v[52:55], v[76:79]\n" \
  "s_waitcnt lgkmcnt(2)\n" \
  "v_mfma_f32_16x16x32_bf16 v[56:59], a[56:59], v[32:35], v[56:59]\n" \
  "ds_read_b128 v[48:51], v10 offset:16384\n" \
  "v_mfma_f32_16x16x32_bf16 v[68:71], a[56:59], v[36:39], v[68:71]\n" \
  "ds_read_b128 v[52:55], v10 offset:36864\n" \
  "v_mfma_f32_16x16x32_bf16 v[60:63], a[136:139], v[32:35], v[60:63]\n" \
  "buffer_load_dwordx4 v1, s[36:39], s70 offen offset:2048 sc1 lds\n" \
  "v_mfma_f32_16x16x32_bf16 v[72:75], a[136:139], v[36:39], v[72:75]\n" \
  "v_readlane_b32 s69, v3, s72\n" \
  "v_mfma_f32_16x16x32_bf16 v[64:67], a[216:219], v[32:35], v[64:67]\n" \
  "v_readlane_b32 s73, v6, s72\n" \
  "v_mfma_f32_16x16x32_bf16 v[76:79], a[216:219], v[36:39], v[76:79]\n" \
  "s_add_u32 s72, s72, 1\n" \
  "s_waitcnt lgkmcnt(2)\n" \
  "v_mfma_f32_16x16x32_bf16 v[56:59], a[60:63], v[40:43], v[56:59]\n" \
  "ds_read_b128 v[32:35], v10 offset:17408\n" \
  "v_mfma_f32_16x16x32_bf16 v[68:71], a[60:63], v[44:47], v[68:71]\n" \
  "ds_read_b128 v[36:39], v10 offset:37888\n" \
  "v_mfma_f32_16x16x32_bf16 v[60:63], a[140:143], v[40:43], v[60:63]\n" \
  "buffer_load_dwordx4 v1, s[36:39], s70 offen offset:3072 sc1 lds\n" \
  "v_mfma_f32_16x16x32_bf16 v[72:75], a[140:143], v[44:47], v[72:75]\n" \
  "v_mfma_f32_16x16x32_bf16 v[64:67], a[220:223], v[40:43], v[64:67]\n" \
  "v_mfma_f32_16x16x32_bf16 v[76:79], a[220:223], v[44:47], v[76:79]\n" \
  "s_waitcnt lgkmcnt(2)\n" \
  "v_mfma_f32_16x16x32_bf16 v[56:59], a[64:67], v[48:51], v[56:59]\n" \
  "ds_read_b128 v[40:43], v10 offset:18432\n" \
  "v_mfma_f32_16x16x32_bf16 v[68:71], a[64:67], v[52:55], v[68:71]\n" \
  "ds_read_b128 v[44:47], v10 offset:38912\n" \
  "v_mfma_f32_16x16x32_bf16 v[60:63], a[144:147], v[48:51], v[60:63]\n" \
  "s_add_u32 m0, s53, 49152\n" \
  "v_mfma_f32_16x16x32_bf16 v[72:75], a[144:147], v[52:55], v[72:75]\n" \
  "buffer_load_dwordx4 v1, s[36:39], s71 offen sc1 lds\n" \
  "v_mfma_f32_16x16x32_bf16 v[64:67], a[224:227], v[48:51], v[64:67]\n" \
  "v_mfma_f32_16x16x32_bf16 v[76:79], a[224:227], v[52:55], v[76:79]\n" \
  "s_waitcnt lgkmcnt(2)\n" \
  "v_mfma_f32_16x16x32_bf16 v[56:59], a[68:71], v[32:35], v[56:59]\n" \
  "ds_read_b128 v[48:51], v10 offset:19456\n" \
  "v_mfma_f32_16x16x32_bf16 v[68:71], a[68:71], v[36:39], v[68:71]\n" \
  "ds_read_b128 v[52:55], v10 offset:39936\n" \
  "v_mfma_f32_16x16x32_bf16 v[60:63], a[148:151], v[32:35], v[60:63]\n" \
  "buffer_load_dwordx4 v1, s[36:39], s71 offen offset:1024 sc1 lds\n" \
  "v_mfma_f32_16x16x32_bf16 v[72:75], a[148:151], v[36:39], v[72:75]\n" \
  "s_add_u32 s68, s68, 40960\n" \
  "v_mfma_f32_16x16x32_bf16 v[64:67], a[228:231], v[32:35], v[64:67]\n" \
  "v_mfma_f32_16x16x32_bf16 v[76:79], a[228:231], v[36:39], v[76:79]\n" \
  "s_waitcnt vmcnt(11) lgkmcnt(2)\n" \
  "s_barrier\n" \
  "v_mfma_f32_16x16x32_bf16 v[56:59], a[72:75], v[40:43], v[56:59]\n" \
  "ds_read_b128 v[32:35], v0 offset:0\n" \
  "v_mfma_f32_16x16x32_bf16 v[68:71], a[72:75], v[44:47], v[68:71]\n" \
  "ds_read_b128 v[36:39], v0 offset:20480\n" \
  "v_mfma_f32_16x16x32_bf16 v[60:63], a[152:155], v[40:43], v[60:63]\n" \
  "v_mfma_f32_16x16x32_bf16 v[72:75], a[152:155], v[44:47], v[72:75]\n" \
  "v_mfma_f32_16x16x32_bf16 v[64:67], a[232:235], v[40:43], v[64:67]\n" \
  "v_mfma_f32_16x16x32_bf16 v[76:79], a[232:235], v[44:47], v[76:79]\n" \
  "s_waitcnt lgkmcnt(2)\n" \
  "v_mfma_f32_16x16x32_bf16 v[56:59], a[76:79], v[48:51], v[56:59]\n" \
  "ds_read_b128 v[40:43], v0 offset:1024\n" \
  "v_mfma_f32_16x16x32_bf16 v[68:71], a[76:79], v[52:55], v[68:71]\n" \
  "ds_read_b128 v[44:47], v0 offset:21504\n" \
  "v_mfma_f32_16x16x32_bf16 v[60:63], a[156:159], v[48:51], v[60:63]\n" \
  "s_cmp_eq_u32 s72, 64\n" \
  "v_mfma_f32_16x16x32_bf16 v[72:75], a[156:159], v[52:55], v[72:75]\n" \
  "v_mfma_f32_16x16x32_bf16 v[64:67], a[236:239], v[48:51], v[64:67]\n" \
  "v_mfma_f32_16x16x32_bf16 v[76:79], a[236:239], v[52:55], v[76:79]\n" \
  "s_waitcnt lgkmcnt(2)\n" \
  "v_mfma_f32_16x16x32_bf16 v[100:103], a[0:3], v[32:35], 0\n" \
  "ds_read_b128 v[48:51], v0 offset:2048\n" \
  "v_mfma_f32_16x16x32_bf16 v[112:115], a[0:3], v[36:39], 0\n" \
  "ds_read_b128 v[52:55], v0 offset:22528\n" \
  "v_mfma_f32_16x16x32_bf16 v[104:107], a[80:83], v[32:35], 0\n" \
  "v_cvt_pk_bf16_f32 v16, v56, v57\n" \
  "v_mfma_f32_16x16x32_bf16 v[116:119], a[80:83], v[36:39], 0\n" \
  "v_cvt_pk_bf16_f32 v17, v58, v59\n" \
  "v_mfma_f32_16x16x32_bf16 v[108:111], a[160:163], v[32:35], 0\n" \
  "v_cvt_pk_bf16_f32 v18, v60, v61\n" \
  "v_mfma_f32_16x16x32_bf16 v[120:123], a[160:163], v[36:39], 0\n" \
  "v_cvt_pk_bf16_f32 v19, v62, v63\n" \
  "s_waitcnt lgkmcnt(2)\n" \
  "s_barrier\n" \
  "v_mfma_f32_16x16x32_bf16 v[100:103], a[4:7], v[40:43], v[100:103]\n" \
  "ds_read_b128 v[32:35], v0 offset:3072\n" \
  "v_mfma_f32_16x16x32_bf16 v[112:115], a[4:7], v[44:47], v[112:115]\n" \
  "ds_read_b128 v[36:39], v0 offset:23552\n" \
  "v_mfma_f32_16x16x32_bf16 v[104:107], a[84:87], v[40:43], v[104:107]\n" \
  "buffer_store_dwordx4 v[16:19], v2, s[40:43], s69 offen offset:0 sc1\n" \
  "v_mfma_f32_16x16x32_bf16 v[116:119], a[84:87], v[44:47], v[116:119]\n" \
  "v_cvt_pk_bf16_f32 v20, v68, v69\n" \
  "v_mfma_f32_16x16x32_bf16 v[108:111], a[164:167], v[40:43], v[108:111]\n" \
  "v_cvt_pk_bf16_f32 v21, v70, v71\n" \
  "v_mfma_f32_16x16x32_bf16 v[120:123], a[164:167], v[44:47], v[120:123]\n" \
  "v_cvt_pk_bf16_f32 v22, v72, v73\n" \
  "s_waitcnt lgkmcnt(2)\n" \
  "v_mfma_f32_16x16x32_bf16 v[100:103], a[8:11], v[48:51], v[100:103]\n" \
  "ds_read_b128 v[40:43], v0 offset:4096\n" \
  "v_mfma_f32_16x16x32_bf16 v[112:115], a[8:11], v[52:55], v[112:115]\n" \
  "ds_read_b128 v[44:47], v0 offset:24576\n" \
  "v_mfma_f32_16x16x32_bf16 v[104:107], a[88:91], v[48:51], v[104:107]\n" \
  "v_cvt_pk_bf16_f32 v23, v74, v75\n" \
  "v_mfma_f32_16x16x32_bf16 v[116:119], a[88:91], v[52:55], v[116:119]\n" \
  "buffer_store_dwordx4 v[20:23], v2, s[40:43], s69 offen offset:1024 sc1\n" \
  "v_mfma_f32_16x16x32_bf16 v[108:111], a[168:171], v[48:51], v[108:111]\n" \
  "v_cvt_pk_bf16_f32 v24, v64, v65\n" \
  "v_mfma_f32_16x16x32_bf16 v[120:123], a[168:171], v[52:55], v[120:123]\n" \
  "v_cvt_pk_bf16_f32 v25, v66, v67\n" \
  "s_waitcnt lgkmcnt(2)\n" \
  "v_mfma_f32_16x16x32_bf16 v[100:103], a[12:15], v[32:35], v[100:103]\n" \
  "ds_read_b128 v[48:51], v0 offset:5120\n" \
  "v_mfma_f32_16x16x32_bf16 v[112:115], a[12:15], v[36:39], v[112:115]\n" \
  "ds_read_b128 v[52:55], v0 offset:25600\n" \
  "v_mfma_f32_16x16x32_bf16 v[104:107], a[92:95], v[32:35], v[104:107]\n" \
  "v_cvt_pk_bf16_f32 v26, v76, v77\n" \
  "v_mfma_f32_16x16x32_bf16 v[116:119], a[92:95], v[36:39], v[116:119]\n" \
  "v_cvt_pk_bf16_f32 v27, v78, v79\n" \
  "v_mfma_f32_16x16x32_bf16 v[108:111], a[172:175], v[32:35], v[108:111]\n" \
  "buffer_store_dwordx4 v[24:27], v2, s[40:43], s69 offen offset:2048 sc1\n" \
  "v_mfma_f32_16x16x32_bf16 v[120:123], a[172:175], v[36:39], v[120:123]\n" \
  "s_waitcnt lgkmcnt(2)\n" \
  "v_mfma_f32_16x16x32_bf16 v[100:103], a[16:19], v[40:43], v[100:103]\n" \
  "ds_read_b128 v[32:35], v0 offset:6144\n" \
  "v_mfma_f32_16x16x32_bf16 v[112:115], a[16:19], v[44:47], v[112:115]\n" \
  "ds_read_b128 v[36:39], v0 offset:26624\n" \
  "v_mfma_f32_16x16x32_bf16 v[104:107], a[96:99], v[40:43], v[104:107]\n" \
  "buffer_store_dwordx4 v[12:15], v5, s[40:43], s73 offen offset:0 sc1\n" \
  "v_mfma_f32_16x16x32_bf16 v[116:119], a[96:99], v[44:47], v[116:119]\n" \
  "v_mfma_f32_16x16x32_bf16 v[108:111], a[176:179], v[40:43], v[108:111]\n" \
  "v_mfma_f32_16x16x32_bf16 v[120:123], a[176:179], v[44:47], v[120:123]\n" \
  "s_waitcnt lgkmcnt(2)\n" \
  "v_mfma_f32_16x16x32_bf16 v[100:103], a[20:23], v[48:51], v[100:103]\n" \
  "ds_read_b128 v[40:43], v0 offset:7168\n" \
  "v_mfma_f32_16x16x32_bf16 v[112:115], a[20:23], v[52:55], v[112:115]\n" \
  "ds_read_b128 v[44:47], v0 offset:27648\n" \
  "v_mfma_f32_16x16x32_bf16 v[104:107], a[100:103], v[48:51], v[104:107]\n" \
  "buffer_store_dwordx4 v[12:15], v5, s[40:43], s73 offen offset:1024 sc1\n" \
  "v_mfma_f32_16x16x32_bf16 v[116:119], a[100:103], v[52:55], v[116:119]\n" \
  "v_mfma_f32_16x16x32_bf16 v[108:111], a[180:183], v[48:51], v[108:111]\n" \
  "v_mfma_f32_16x16x32_bf16 v[120:123], a[180:183], v[52:55], v[120:123]\n" \
  "s_waitcnt lgkmcnt(2)\n" \
  "v_mfma_f32_16x16x32_bf16 v[100:103], a[24:27], v[32:35], v[100:103]\n" \
  "ds_read_b128 v[48:51], v0 offset:8192\n" \
  "v_mfma_f32_16x16x32_bf16 v[112:115], a[24:27], v[36:39], v[112:115]\n" \
  "ds_read_b128 v[52:55], v0 offset:28672\n" \
  "v_mfma_f32_16x16x32_bf16 v[104:107], a[104:107], v[32:35], v[104:107]\n" \
  "buffer_store_dwordx4 v[12:15], v5, s[40:43], s73 offen offset:2048 sc1\n" \
  "v_mfma_f32_16x16x32_bf16 v[116:119], a[104:107], v[36:39], v[116:119]\n" \
  "s_nop 0\n" \
  "v_mfma_f32_16x16x32_bf16 v[108:111], a[184:187], v[32:35], v[108:111]\n" \
  "buffer_store_dwordx4 v[12:15], v5, s[40:43], s73 offen offset:3072 sc1\n" \
  "v_mfma_f32_16x16x32_bf16 v[120:123], a[184:187], v[36:39], v[120:123]\n" \
  "s_waitcnt lgkmcnt(2)\n" \
  "v_mfma_f32_16x16x32_bf16 v[100:103], a[28:31], v[40:43], v[100:103]\n" \
  "ds_read_b128 v[32:35], v0 offset:9216\n" \
  "v_mfma_f32_16x16x32_bf16 v[112:115], a[28:31], v[44:47], v[112:115]\n" \
  "ds_read_b128 v[36:39], v0 offset:29696\n" \
  "v_mfma_f32_16x16x32_bf16 v[104:107], a[108:111], v[40:43], v[104:107]\n" \
  "s_add_u32 s70, s68, 4096\n" \
  "v_mfma_f32_16x16x32_bf16 v[116:119], a[108:111], v[44:47], v[116:119]\n" \
  "s_add_u32 s71, s68, 8192\n" \
  "v_mfma_f32_16x16x32_bf16 v[108:111], a[188:191], v[40:43], v[108:111]\n" \
  "s_add_u32 m0, s53, 81920\n" \
  "v_mfma_f32_16x16x32_bf16 v[120:123], a[188:191], v[44:47], v[120:123]\n" \
  "buffer_load_dwordx4 v1, s[36:39], s68 offen sc1 lds\n" \
  "s_waitcnt lgkmcnt(2)\n" \
  "v_mfma_f32_16x16x32_bf16 v[100:103], a[32:35], v[48:51], v[100:103]\n" \
  "ds_read_b128 v[40:43], v0 offset:10240\n" \
  "v_mfma_f32_16x16x32_bf16 v[112:115], a[32:35], v[52:55], v[112:115]\n" \
  "ds_read_b128 v[44:47], v0 offset:30720\n" \
  "v_mfma_f32_16x16x32_bf16 v[104:107], a[112:115], v[48:51], v[104:107]\n" \
  "buffer_load_dwordx4 v1, s[36:39], s68 offen offset:1024 sc1 lds\n" \
  "v_mfma_f32_16x16x32_bf16 v[116:119], a[112:115], v[52:55], v[116:119]\n" \
  "v_mfma_f32_16x16x32_bf16 v[108:111], a[192:195], v[48:51], v[108:111]\n" \
  "v_mfma_f32_16x16x32_bf16 v[120:123], a[192:195], v[52:55], v[120:123]\n" \
  "s_waitcnt lgkmcnt(2)\n" \
  "v_mfma_f32_16x16x32_bf16 v[100:103], a[36:39], v[32:35], v[100:103]\n" \
  "ds_read_b128 v[48:51], v0 offset:11264\n" \
  "v_mfma_f32_16x16x32_bf16 v[112:115], a[36:39], v[36:39], v[112:115]\n" \
  "ds_read_b128 v[52:55], v0 offset:31744\n" \
  "v_mfma_f32_16x16x32_bf16 v[104:107], a[116:119], v[32:35], v[104:107]\n" \
  "buffer_load_dwordx4 v1, s[36:39], s68 offen offset:2048 sc1 lds\n" \
  "v_mfma_f32_16x16x32_bf16 v[116:119], a[116:119], v[36:39], v[116:119]\n" \
  "v_mfma_f32_16x16x32_bf16 v[108:111], a[196:199], v[32:35], v[108:111]\n" \
  "v_mfma_f32_16x16x32_bf16 v[120:123], a[196:199], v[36:39], v[120:123]\n" \
  "s_waitcnt lgkmcnt(2)\n" \
  "v_mfma_f32_16x16x32_bf16 v[100:103], a[40:43], v[40:43], v[100:103]\n" \
  "ds_read_b128 v[32:35], v0 offset:12288\n" \
  "v_mfma_f32_16x16x32_bf16 v[112:115], a[40:43], v[44:47], v[112:115]\n" \
  "ds_read_b128 v[36:39], v0 offset:32768\n" \
  "v_mfma_f32_16x16x32_bf16 v[104:107], a[120:123], v[40:43], v[104:107]\n" \
  "buffer_load_dwordx4 v1, s[36:39], s68 offen offset:3072 sc1 lds\n" \
  "v_mfma_f32_16x16x32_bf16 v[116:119], a[120:123], v[44:47], v[116:119]\n" \
  "v_mfma_f32_16x16x32_bf16 v[108:111], a[200:203], v[40:43], v[108:111]\n" \
  "v_mfma_f32_16x16x32_bf16 v[120:123], a[200:203], v[44:47], v[120:123]\n" \
  "s_waitcnt lgkmcnt(2)\n" \
  "v_mfma_f32_16x16x32_bf16 v[100:103], a[44:47], v[48:51], v[100:103]\n" \
  "ds_read_b128 v[40:43], v0 offset:13312\n" \
  "v_mfma_f32_16x16x32_bf16 v[112:115], a[44:47], v[52:55], v[112:115]\n" \
  "ds_read_b128 v[44:47], v0 offset:33792\n" \
  "v_mfma_f32_16x16x32_bf16 v[104:107], a[124:127], v[48:51], v[104:107]\n" \
  "s_add_u32 m0, s53, 86016\n" \
  "v_mfma_f32_16x16x32_bf16 v[116:119], a[124:127], v[52:55], v[116:119]\n" \
  "buffer_load_dwordx4 v1, s[36:39], s70 offen sc1 lds\n" \
  "v_mfma_f32_16x16x32_bf16 v[108:111], a[204:207], v[48:51], v[108:111]\n" \
  "v_mfma_f32_16x16x32_bf16 v[120:123], a[204:207], v[52:55], v[120:123]\n" \
  "s_waitcnt lgkmcnt(2)\n" \
  "v_mfma_f32_16x16x32_bf16 v[100:103], a[48:51], v[32:35], v[100:103]\n" \
  "ds_read_b128 v[48:51], v0 offset:14336\n" \
  "v_mfma_f32_16x16x32_bf16 v[112:115], a[48:51], v[36:39], v[112:115]\n" \
  "ds_read_b128 v[52:55], v0 offset:34816\n" \
  "v_mfma_f32_16x16x32_bf16 v[104:107], a[128:131], v[32:35], v[104:107]\n" \
  "buffer_load_dwordx4 v1, s[36:39], s70 offen offset:1024 sc1 lds\n" \
  "v_mfma_f32_16x16x32_bf16 v[116:119], a[128:131], v[36:39], v[116:119]\n" \
  "v_mfma_f32_16x16x32_bf16 v[108:111], a[208:211], v[32:35], v[108:111]\n" \
  "v_mfma_f32_16x16x32_bf16 v[120:123], a[208:211], v[36:39], v[120:123]\n" \
  "s_waitcnt vmcnt(6) lgkmcnt(2)\n" \
  "s_barrier\n" \
  "v_mfma_f32_16x16x32_bf16 v[100:103], a[52:55], v[40:43], v[100:103]\n" \
  "ds_read_b128 v[32:35], v0 offset:15360\n" \
  "v_mfma_f32_16x16x32_bf16 v[112:115], a[52:55], v[44:47], v[112:115]\n" \
  "ds_read_b128 v[36:39], v0 offset:35840\n" \
  "v_mfma_f32_16x16x32_bf16 v[104:107], a[132:135], v[40:43], v[104:107]\n" \
  "buffer_atomic_add v11, v4, s[40:43], s54 offen\n" \
  "v_mfma_f32_16x16x32_bf16 v[116:119], a[132:135], v[44:47], v[116:119]\n" \
  "s_add_u32 s54, s54, s55\n" \
  "v_mfma_f32_16x16x32_bf16 v[108:111], a[212:215], v[40:43], v[108:111]\n" \
  "s_and_b32 s54, s54, 0xffffdfff\n" \
  "v_mfma_f32_16x16x32_bf16 v[120:123], a[212:215], v[44:47], v[120:123]\n" \
  "s_waitcnt lgkmcnt(2)\n" \
  "v_mfma_f32_16x16x32_bf16 v[100:103], a[56:59], v[48:51], v[100:103]\n" \
  "ds_read_b128 v[40:43], v0 offset:16384\n" \
  "v_mfma_f32_16x16x32_bf16 v[112:115], a[56:59], v[52:55], v[112:115]\n" \
  "ds_read_b128 v[44:47], v0 offset:36864\n" \
  "v_mfma_f32_16x16x32_bf16 v[104:107], a[136:139], v[48:51], v[104:107]\n" \
  "buffer_load_dwordx4 v1, s[36:39], s70 offen offset:2048 sc1 lds\n" \
  "v_mfma_f32_16x16x32_bf16 v[116:119], a[136:139], v[52:55], v[116:119]\n" \
  "v_readlane_b32 s69, v3, s72\n" \
  "v_mfma_f32_16x16x32_bf16 v[108:111], a[216:219], v[48:51], v[108:111]\n" \
  "v_readlane_b32 s73, v6, s72\n" \
  "v_mfma_f32_16x16x32_bf16 v[120:123], a[216:219], v[52:55], v[120:123]\n" \
  "s_add_u32 s72, s72, 1\n" \
  "s_waitcnt lgkmcnt(2)\n" \
  "v_mfma_f32_16x16x32_bf16 v[100:103], a[60:63], v[32:35], v[100:103]\n" \
  "ds_read_b128 v[48:51], v0 offset:17408\n" \
  "v_mfma_f32_16x16x32_bf16 v[112:115], a[60:63], v[36:39], v[112:115]\n" \
  "ds_read_b128 v[52:55], v0 offset:37888\n" \
  "v_mfma_f32_16x16x32_bf16 v[104:107], a[140:143], v[32:35], v[104:107]\n" \
  "buffer_load_dwordx4 v1, s[36:39], s70 offen offset:3072 sc1 lds\n" \
  "v_mfma_f32_16x16x32_bf16 v[116:119], a[140:143], v[36:39], v[116:119]\n" \
  "v_mfma_f32_16x16x32_bf16 v[108:111], a[220:223], v[32:35], v[108:111]\n" \
  "v_mfma_f32_16x16x32_bf16 v[120:123], a[220:223], v[36:39], v[120:123]\n" \
  "s_waitcnt lgkmcnt(2)\n" \
  "v_mfma_f32_16x16x32_bf16 v[100:103], a[64:67], v[40:43], v[100:103]\n" \
  "ds_read_b128 v[32:35], v0 offset:18432\n" \
  "v_mfma_f32_16x16x32_bf16 v[112:115], a[64:67], v[44:47], v[112:115]\n" \
  "ds_read_b128 v[36:39], v0 offset:38912\n" \
  "v_mfma_f32_16x16x32_bf16 v[104:107], a[144:147], v[40:43], v[104:107]\n" \
  "s_add_u32 m0, s53, 90112\n" \
  "v_mfma_f32_16x16x32_bf16 v[116:119], a[144:147], v[44:47], v[116:119]\n" \
  "buffer_load_dwordx4 v1, s[36:39], s71 offen sc1 lds\n" \
  "v_mfma_f32_16x16x32_bf16 v[108:111], a[224:227], v[40:43], v[108:111]\n" \
  "v_mfma_f32_16x16x32_bf16 v[120:123], a[224:227], v[44:47], v[120:123]\n" \
  "s_waitcnt lgkmcnt(2)\n" \
  "v_mfma_f32_16x16x32_bf16 v[100:103], a[68:71], v[48:51], v[100:103]\n" \
  "ds_read_b128 v[40:43], v0 offset:19456\n" \
  "v_mfma_f32_16x16x32_bf16 v[112:115], a[68:71], v[52:55], v[112:115]\n" \
  "ds_read_b128 v[44:47], v0 offset:39936\n" \
  "v_mfma_f32_16x16x32_bf16 v[104:107], a[148:151], v[48:51], v[104:107]\n" \
  "buffer_load_dwordx4 v1, s[36:39], s71 offen offset:1024 sc1 lds\n" \
  "v_mfma_f32_16x16x32_bf16 v[116:119], a[148:151], v[52:55], v[116:119]\n" \
  "s_add_u32 s68, s68, 40960\n" \
  "v_mfma_f32_16x16x32_bf16 v[108:111], a[228:231], v[48:51], v[108:111]\n" \
  "v_mfma_f32_16x16x32_bf16 v[120:123], a[228:231], v[52:55], v[120:123]\n" \
  "s_waitcnt vmcnt(11) lgkmcnt(2)\n" \
  "s_barrier\n" \
  "v_mfma_f32_16x16x32_bf16 v[100:103], a[72:75], v[32:35], v[100:103]\n" \
  "ds_read_b128 v[48:51], v9 offset:0\n" \
  "v_mfma_f32_16x16x32_bf16 v[112:115], a[72:75], v[36:39], v[112:115]\n" \
  "ds_read_b128 v[52:55], v9 offset:20480\n" \
  "v_mfma_f32_16x16x32_bf16 v[104:107], a[152:155], v[32:35], v[104:107]\n" \
  "v_mfma_f32_16x16x32_bf16 v[116:119], a[152:155], v[36:39], v[116:119]\n" \
  "v_mfma_f32_16x16x32_bf16 v[108:111], a[232:235], v[32:35], v[108:111]\n" \
  "v_mfma_f32_16x16x32_bf16 v[120:123], a[232:235], v[36:39], v[120:123]\n" \
  "s_waitcnt lgkmcnt(2)\n" \
  "v_mfma_f32_16x16x32_bf16 v[100:103], a[76:79], v[40:43], v[100:103]\n" \
  "ds_read_b128 v[32:35], v9 offset:1024\n" \
  "v_mfma_f32_16x16x32_bf16 v[112:115], a[76:79], v[44:47], v[112:115]\n" \
  "ds_read_b128 v[36:39], v9 offset:21504\n" \
  "v_mfma_f32_16x16x32_bf16 v[104:107], a[156:159], v[40:43], v[104:107]\n" \
  "s_cmp_eq_u32 s72, 64\n" \
  "v_mfma_f32_16x16x32_bf16 v[116:119], a[156:159], v[44:47], v[116:119]\n" \
  "v_mfma_f32_16x16x32_bf16 v[108:111], a[236:239], v[40:43], v[108:111]\n" \
  "v_mfma_f32_16x16x32_bf16 v[120:123], a[236:239], v[44:47], v[120:123]\n" \
  "s_cbranch_scc1 LPE_%=\n" \
  "s_waitcnt lgkmcnt(2)\n" \
  "v_mfma_f32_16x16x32_bf16 v[56:59], a[0:3], v[48:51], 0\n" \
  "ds_read_b128 v[40:43], v9 offset:2048\n" \
  "v_mfma_f32_16x16x32_bf16 v[68:71], a[0:3], v[52:55], 0\n" \
  "ds_read_b128 v[44:47], v9 offset:22528\n" \
  "v_mfma_f32_16x16x32_bf16 v[60:63], a[80:83], v[48:51], 0\n" \
  "v_cvt_pk_bf16_f32 v16, v100, v101\n" \
  "v_mfma_f32_16x16x32_bf16 v[72:75], a[80:83], v[52:55], 0\n" \
  "v_cvt_pk_bf16_f32 v17, v102, v103\n" \
  "v_mfma_f32_16x16x32_bf16 v[64:67], a[160:163], v[48:51], 0\n" \
  "v_cvt_pk_bf16_f32 v18, v104, v105\n" \
  "v_mfma_f32_16x16x32_bf16 v[76:79], a[160:163], v[52:55], 0\n" \
  "v_cvt_pk_bf16_f32 v19, v106, v107\n" \
  "s_waitcnt lgkmcnt(2)\n" \
  "s_barrier\n" \
  "v_mfma_f32_16x16x32_bf16 v[56:59], a[4:7], v[32:35], v[56:59]\n" \
  "ds_read_b128 v[48:51], v9 offset:3072\n" \
  "v_mfma_f32_16x16x32_bf16 v[68:71], a[4:7], v[36:39], v[68:71]\n" \
  "ds_read_b128 v[52:55], v9 offset:23552\n" \
  "v_mfma_f32_16x16x32_bf16 v[60:63], a[84:87], v[32:35], v[60:63]\n" \
  "buffer_store_dwordx4 v[16:19], v2, s[40:43], s69 offen offset:0 sc1\n" \
  "v_mfma_f32_16x16x32_bf16 v[72:75], a[84:87], v[36:39], v[72:75]\n" \
  "v_cvt_pk_bf16_f32 v20, v112, v113\n" \
  "v_mfma_f32_16x16x32_bf16 v[64:67], a[164:167], v[32:35], v[64:67]\n" \
  "v_cvt_pk_bf16_f32 v21, v114, v115\n" \
  "v_mfma_f32_16x16x32_bf16 v[76:79], a[164:167], v[36:39], v[76:79]\n" \
  "v_cvt_pk_bf16_f32 v22, v116, v117\n" \
  "s_waitcnt lgkmcnt(2)\n" \
  "v_mfma_f32_16x16x32_bf16 v[56:59], a[8:11], v[40:43], v[56:59]\n" \
  "ds_read_b128 v[32:35], v9 offset:4096\n" \
  "v_mfma_f32_16x16x32_bf16 v[68:71], a[8:11], v[44:47], v[68:71]\n" \
  "ds_read_b128 v[36:39], v9 offset:24576\n" \
  "v_mfma_f32_16x16x32_bf16 v[60:63], a[88:91], v[40:43], v[60:63]\n" \
  "v_cvt_pk_bf16_f32 v23, v118, v119\n" \
  "v_mfma_f32_16x16x32_bf16 v[72:75], a[88:91], v[44:47], v[72:75]\n" \
  "buffer_store_dwordx4 v[20:23], v2, s[40:43], s69 offen offset:1024 sc1\n" \
  "v_mfma_f32_16x16x32_bf16 v[64:67], a[168:171], v[40:43], v[64:67]\n" \
  "v_cvt_pk_bf16_f32 v24, v108, v109\n" \
  "v_mfma_f32_16x16x32_bf16 v[76:79], a[168:171], v[44:47], v[76:79]\n" \
  "v_cvt_pk_bf16_f32 v25, v110, v111\n" \
  "s_waitcnt lgkmcnt(2)\n" \
  "v_mfma_f32_16x16x32_bf16 v[56:59], a[12:15], v[48:51], v[56:59]\n" \
  "ds_read_b128 v[40:43], v9 offset:5120\n" \
  "v_mfma_f32_16x16x32_bf16 v[68:71], a[12:15], v[52:55], v[68:71]\n" \
  "ds_read_b128 v[44:47], v9 offset:25600\n" \
  "v_mfma_f32_16x16x32_bf16 v[60:63], a[92:95], v[48:51], v[60:63]\n" \
  "v_cvt_pk_bf16_f32 v26, v120, v121\n" \
  "v_mfma_f32_16x16x32_bf16 v[72:75], a[92:95], v[52:55], v[72:75]\n" \
  "v_cvt_pk_bf16_f32 v27, v122, v123\n" \
  "v_mfma_f32_16x16x32_bf16 v[64:67], a[172:175], v[48:51], v[64:67]\n" \
  "buffer_store_dwordx4 v[24:27], v2, s[40:43], s69 offen offset:2048 sc1\n" \
  "v_mfma_f32_16x16x32_bf16 v[76:79], a[172:175], v[52:55], v[76:79]\n" \
  "s_waitcnt lgkmcnt(2)\n" \
  "v_mfma_f32_16x16x32_bf16 v[56:59], a[16:19], v[32:35], v[56:59]\n" \
  "ds_read_b128 v[48:51], v9 offset:6144\n" \
  "v_mfma_f32_16x16x32_bf16 v[68:71], a[16:19], v[36:39], v[68:71]\n" \
  "ds_read_b128 v[52:55], v9 offset:26624\n" \
  "v_mfma_f32_16x16x32_bf16 v[60:63], a[96:99], v[32:35], v[60:63]\n" \
  "buffer_store_dwordx4 v[12:15], v5, s[40:43], s73 offen offset:0 sc1\n" \
  "v_mfma_f32_16x16x32_bf16 v[72:75], a[96:99], v[36:39], v[72:75]\n" \
  "v_mfma_f32_16x16x32_bf16 v[64:67], a[176:179], v[32:35], v[64:67]\n" \
  "v_mfma_f32_16x16x32_bf16 v[76:79], a[176:179], v[36:39], v[76:79]\n" \
  "s_waitcnt lgkmcnt(2)\n" \
  "v_mfma_f32_16x16x32_bf16 v[56:59], a[20:23], v[40:43], v[56:59]\n" \
  "ds_read_b128 v[32:35], v9 offset:7168\n" \
  "v_mfma_f32_16x16x32_bf16 v[68:71], a[20:23], v[44:47], v[68:71]\n" \
  "ds_read_b128 v[36:39], v9 offset:27648\n" \
  "v_mfma_f32_16x16x32_bf16 v[60:63], a[100:103], v[40:43], v[60:63]\n" \
  "buffer_store_dwordx4 v[12:15], v5, s[40:43], s73 offen offset:1024 sc1\n" \
  "v_mfma_f32_16x16x32_bf16 v[72:75], a[100:103], v[44:47], v[72:75]\n" \
  "v_mfma_f32_16x16x32_bf16 v[64:67], a[180:183], v[40:43], v[64:67]\n" \
  "v_mfma_f32_16x16x32_bf16 v[76:79], a[180:183], v[44:47], v[76:79]\n" \
  "s_waitcnt lgkmcnt(2)\n" \
  "v_mfma_f32_16x16x32_bf16 v[56:59], a[24:27], v[48:51], v[56:59]\n" \
  "ds_read_b128 v[40:43], v9 offset:8192\n" \
  "v_mfma_f32_16x16x32_bf16 v[68:71], a[24:27], v[52:55], v[68:71]\n" \
  "ds_read_b128 v[44:47], v9 offset:28672\n" \
  "v_mfma_f32_16x16x32_bf16 v[60:63], a[104:107], v[48:51], v[60:63]\n" \
  "buffer_store_dwordx4 v[12:15], v5, s[40:43], s73 offen offset:2048 sc1\n" \
  "v_mfma_f32_16x16x32_bf16 v[72:75], a[104:107], v[52:55], v[72:75]\n" \
  "s_nop 0\n" \
  "v_mfma_f32_16x16x32_bf16 v[64:67], a[184:187], v[48:51], v[64:67]\n" \
  "buffer_store_dwordx4 v[12:15], v5, s[40:43], s73 offen offset:3072 sc1\n" \
  "v_mfma_f32_16x16x32_bf16 v[76:79], a[184:187], v[52:55], v[76:79]\n" \
  "s_waitcnt lgkmcnt(2)\n" \
  "v_mfma_f32_16x16x32_bf16 v[56:59], a[28:31], v[32:35], v[56:59]\n" \
  "ds_read_b128 v[48:51], v9 offset:9216\n" \
  "v_mfma_f32_16x16x32_bf16 v[68:71], a[28:31], v[36:39], v[68:71]\n" \
  "ds_read_b128 v[52:55], v9 offset:29696\n" \
  "v_mfma_f32_16x16x32_bf16 v[60:63], a[108:111], v[32:35], v[60:63]\n" \
  "s_add_u32 s70, s68, 4096\n" \
  "v_mfma_f32_16x16x32_bf16 v[72:75], a[108:111], v[36:39], v[72:75]\n" \
  "s_add_u32 s71, s68, 8192\n" \
  "v_mfma_f32_16x16x32_bf16 v[64:67], a[188:191], v[32:35], v[64:67]\n" \
  "s_add_u32 m0, s53, 0\n" \
  "v_mfma_f32_16x16x32_bf16 v[76:79], a[188:191], v[36:39], v[76:79]\n" \
  "buffer_load_dwordx4 v1, s[36:39], s68 offen sc1 lds\n" \
  "s_waitcnt lgkmcnt(2)\n" \
  "v_mfma_f32_16x16x32_bf16 v[56:59], a[32:35], v[40:43], v[56:59]\n" \
  "ds_read_b128 v[32:35], v9 offset:10240\n" \
  "v_mfma_f32_16x16x32_bf16 v[68:71], a[32:35], v[44:47], v[68:71]\n" \
  "ds_read_b128 v[36:39], v9 offset:30720\n" \
  "v_mfma_f32_16x16x32_bf16 v[60:63], a[112:115], v[40:43], v[60:63]\n" \
  "buffer_load_dwordx4 v1, s[36:39], s68 offen offset:1024 sc1 lds\n" \
  "v_mfma_f32_16x16x32_bf16 v[72:75], a[112:115], v[44:47], v[72:75]\n" \
  "v_mfma_f32_16x16x32_bf16 v[64:67], a[192:195], v[40:43], v[64:67]\n" \
  "v_mfma_f32_16x16x32_bf16 v[76:79], a[192:195], v[44:47], v[76:79]\n" \
  "s_waitcnt lgkmcnt(2)\n" \
  "v_mfma_f32_16x16x32_bf16 v[56:59], a[36:39], v[48:51], v[56:59]\n" \
  "ds_read_b128 v[40:43], v9 offset:11264\n" \
  "v_mfma_f32_16x16x32_bf16 v[68:71], a[36:39], v[52:55], v[68:71]\n" \
  "ds_read_b128 v[44:47], v9 offset:31744\n" \
  "v_mfma_f32_16x16x32_bf16 v[60:63], a[116:119], v[48:51], v[60:63]\n" \
  "buffer_load_dwordx4 v1, s[36:39], s68 offen offset:2048 sc1 lds\n" \
  "v_mfma_f32_16x16x32_bf16 v[72:75], a[116:119], v[52:55], v[72:75]\n" \
  "v_mfma_f32_16x16x32_bf16 v[64:67], a[196:199], v[48:51], v[64:67]\n" \
  "v_mfma_f32_16x16x32_bf16 v[76:79], a[196:199], v[52:55], v[76:79]\n" \
  "s_waitcnt lgkmcnt(2)\n" \
  "v_mfma_f32_16x16x32_bf16 v[56:59], a[40:43], v[32:35], v[56:59]\n" \
  "ds_read_b128 v[48:51], v9 offset:12288\n" \
  "v_mfma_f32_16x16x32_bf16 v[68:71], a[40:43], v[36:39], v[68:71]\n" \
  "ds_read_b128 v[52:55], v9 offset:32768\n" \
  "v_mfma_f32_16x16x32_bf16 v[60:63], a[120:123], v[32:35], v[60:63]\n" \
  "buffer_load_dwordx4 v1, s[36:39], s68 offen offset:3072 sc1 lds\n" \
  "v_mfma_f32_16x16x32_bf16 v[72:75], a[120:123], v[36:39], v[72:75]\n" \
  "v_mfma_f32_16x16x32_bf16 v[64:67], a[200:203], v[32:35], v[64:67]\n" \
  "v_mfma_f32_16x16x32_bf16 v[76:79], a[200:203], v[36:39], v[76:79]\n" \
  "s_waitcnt lgkmcnt(2)\n" \
  "v_mfma_f32_16x16x32_bf16 v[56:59], a[44:47], v[40:43], v[56:59]\n" \
  "ds_read_b128 v[32:35], v9 offset:13312\n" \
  "v_mfma_f32_16x16x32_bf16 v[68:71], a[44:47], v[44:47], v[68:71]\n" \
  "ds_read_b128 v[36:39], v9 offset:33792\n" \
  "v_mfma_f32_16x16x32_bf16 v[60:63], a[124:127], v[40:43], v[60:63]\n" \
  "s_add_u32 m0, s53, 4096\n" \
  "v_mfma_f32_16x16x32_bf16 v[72:75], a[124:127], v[44:47], v[72:75]\n" \
  "buffer_load_dwordx4 v1, s[36:39], s70 offen sc1 lds\n" \
  "v_mfma_f32_16x16x32_bf16 v[64:67], a[204:207], v[40:43], v[64:67]\n" \
  "v_mfma_f32_16x16x32_bf16 v[76:79], a[204:207], v[44:47], v[76:79]\n" \
  "s_waitcnt lgkmcnt(2)\n" \
  "v_mfma_f32_16x16x32_bf16 v[56:59], a[48:51], v[48:51], v[56:59]\n" \
  "ds_read_b128 v[40:43], v9 offset:14336\n" \
  "v_mfma_f32_16x16x32_bf16 v[68:71], a[48:51], v[52:55], v[68:71]\n" \
  "ds_read_b128 v[44:47], v9 offset:34816\n" \
  "v_mfma_f32_16x16x32_bf16 v[60:63], a[128:131], v[48:51], v[60:63]\n" \
  "buffer_load_dwordx4 v1, s[36:39], s70 offen offset:1024 sc1 lds\n" \
  "v_mfma_f32_16x16x32_bf16 v[72:75], a[128:131], v[52:55], v[72:75]\n" \
  "v_mfma_f32_16x16x32_bf16 v[64:67], a[208:211], v[48:51], v[64:67]\n" \
  "v_mfma_f32_16x16x32_bf16 v[76:79], a[208:211], v[52:55], v[76:79]\n" \
  "s_waitcnt vmcnt(6) lgkmcnt(2)\n" \
  "s_barrier\n" \
  "v_mfma_f32_16x16x32_bf16 v[56:59], a[52:55], v[32:35], v[56:59]\n" \
  "ds_read_b128 v[48:51], v9 offset:15360\n" \
  "v_mfma_f32_16x16x32_bf16 v[68:71], a[52:55], v[36:39], v[68:71]\n" \
  "ds_read_b128 v[52:55], v9 offset:35840\n" \
  "v_mfma_f32_16x16x32_bf16 v[60:63], a[132:135], v[32:35], v[60:63]\n" \
  "buffer_atomic_add v11, v4, s[40:43], s54 offen\n" \
  "v_mfma_f32_16x16x32_bf16 v[72:75], a[132:135], v[36:39], v[72:75]\n" \
  "s_add_u32 s54, s54, s55\n" \
  "v_mfma_f32_16x16x32_bf16 v[64:67], a[212:215], v[32:35], v[64:67]\n" \
  "s_and_b32 s54, s54, 0xffffdfff\n" \
  "v_mfma_f32_16x16x32_bf16 v[76:79], a[212:215], v[36:39], v[76:79]\n" \
  "s_waitcnt lgkmcnt(2)\n" \
  "v_mfma_f32_16x16x32_bf16 v[56:59], a[56:59], v[40:43], v[56:59]\n" \
  "ds_read_b128 v[32:35], v9 offset:16384\n" \
  "v_mfma_f32_16x16x32_bf16 v[68:71], a[56:59], v[44:47], v[68:71]\n" \
  "ds_read_b128 v[36:39], v9 offset:36864\n" \
  "v_mfma_f32_16x16x32_bf16 v[60:63], a[136:139], v[40:43], v[60:63]\n" \
  "buffer_load_dwordx4 v1, s[36:39], s70 offen offset:2048 sc1 lds\n" \
  "v_mfma_f32_16x16x32_bf16 v[72:75], a[136:139], v[44:47], v[72:75]\n" \
  "v_readlane_b32 s69, v3, s72\n" \
  "v_mfma_f32_16x16x32_bf16 v[64:67], a[216:219], v[40:43], v[64:67]\n" \
  "v_readlane_b32 s73, v6, s72\n" \
  "v_mfma_f32_16x16x32_bf16 v[76:79], a[216:219], v[44:47], v[76:79]\n" \
  "s_add_u32 s72, s72, 1\n" \
  "s_waitcnt lgkmcnt(2)\n" \
  "v_mfma_f32_16x16x32_bf16 v[56:59], a[60:63], v[48:51], v[56:59]\n" \
  "ds_read_b128 v[40:43], v9 offset:17408\n" \
  "v_mfma_f32_16x16x32_bf16 v[68:71], a[60:63], v[52:55], v[68:71]\n" \
  "ds_read_b128 v[44:47], v9 offset:37888\n" \
  "v_mfma_f32_16x16x32_bf16 v[60:63], a[140:143], v[48:51], v[60:63]\n" \
  "buffer_load_dwordx4 v1, s[36:39], s70 offen offset:3072 sc1 lds\n" \
  "v_mfma_f32_16x16x32_bf16 v[72:75], a[140:143], v[52:55], v[72:75]\n" \
  "v_mfma_f32_16x16x32_bf16 v[64:67], a[220:223], v[48:51], v[64:67]\n" \
  "v_mfma_f32_16x16x32_bf16 v[76:79], a[220:223], v[52:55], v[76:79]\n" \
  "s_waitcnt lgkmcnt(2)\n" \
  "v_mfma_f32_16x16x32_bf16 v[56:59], a[64:67], v[32:35], v[56:59]\n" \
  "ds_read_b128 v[48:51], v9 offset:18432\n" \
  "v_mfma_f32_16x16x32_bf16 v[68:71], a[64:67], v[36:39], v[68:71]\n" \
  "ds_read_b128 v[52:55], v9 offset:38912\n" \
  "v_mfma_f32_16x16x32_bf16 v[60:63], a[144:147], v[32:35], v[60:63]\n" \
  "s_add_u32 m0, s53, 8192\n" \
  "v_mfma_f32_16x16x32_bf16 v[72:75], a[144:147], v[36:39], v[72:75]\n" \
  "buffer_load_dwordx4 v1, s[36:39], s71 offen sc1 lds\n" \
  "v_mfma_f32_16x16x32_bf16 v[64:67], a[224:227], v[32:35], v[64:67]\n" \
  "v_mfma_f32_16x16x32_bf16 v[76:79], a[224:227], v[36:39], v[76:79]\n" \
  "s_waitcnt lgkmcnt(2)\n" \
  "v_mfma_f32_16x16x32_bf16 v[56:59], a[68:71], v[40:43], v[56:59]\n" \
  "ds_read_b128 v[32:35], v9 offset:19456\n" \
  "v_mfma_f32_16x16x32_bf16 v[68:71], a[68:71], v[44:47], v[68:71]\n" \
  "ds_read_b128 v[36:39], v9 offset:39936\n" \
  "v_mfma_f32_16x16x32_bf16 v[60:63], a[148:151], v[40:43], v[60:63]\n" \
  "buffer_load_dwordx4 v1, s[36:39], s71 offen offset:1024 sc1 lds\n" \
  "v_mfma_f32_16x16x32_bf16 v[72:75], a[148:151], v[44:47], v[72:75]\n" \
  "s_add_u32 s68, s68, 40960\n" \
  "v_mfma_f32_16x16x32_bf16 v[64:67], a[228:231], v[40:43], v[64:67]\n" \
  "v_mfma_f32_16x16x32_bf16 v[76:79], a[228:231], v[44:47], v[76:79]\n" \
  "s_waitcnt vmcnt(11) lgkmcnt(2)\n" \
  "s_barrier\n" \
  "v_mfma_f32_16x16x32_bf16 v[56:59], a[72:75], v[48:51], v[56:59]\n" \
  "ds_read_b128 v[40:43], v10 offset:0\n" \
  "v_mfma_f32_16x16x32_bf16 v[68:71], a[72:75], v[52:55], v[68:71]\n" \
  "ds_read_b128 v[44:47], v10 offset:20480\n" \
  "v_mfma_f32_16x16x32_bf16 v[60:63], a[152:155], v[48:51], v[60:63]\n" \
  "v_mfma_f32_16x16x32_bf16 v[72:75], a[152:155], v[52:55], v[72:75]\n" \
  "v_mfma_f32_16x16x32_bf16 v[64:67], a[232:235], v[48:51], v[64:67]\n" \
  "v_mfma_f32_16x16x32_bf16 v[76:79], a[232:235], v[52:55], v[76:79]\n" \
  "s_waitcnt lgkmcnt(2)\n" \
  "v_mfma_f32_16x16x32_bf16 v[56:59], a[76:79], v[32:35], v[56:59]\n" \
  "ds_read_b128 v[48:51], v10 offset:1024\n" \
  "v_mfma_f32_16x16x32_bf16 v[68:71], a[76:79], v[36:39], v[68:71]\n" \
  "ds_read_b128 v[52:55], v10 offset:21504\n" \
  "v_mfma_f32_16x16x32_bf16 v[60:63], a[156:159], v[32:35], v[60:63]\n" \
  "s_cmp_eq_u32 s72, 64\n" \
  "v_mfma_f32_16x16x32_bf16 v[72:75], a[156:159], v[36:39], v[72:75]\n" \
  "v_mfma_f32_16x16x32_bf16 v[64:67], a[236:239], v[32:35], v[64:67]\n" \
  "v_mfma_f32_16x16x32_bf16 v[76:79], a[236:239], v[36:39], v[76:79]\n" \
  "s_waitcnt lgkmcnt(2)\n" \
  "v_mfma_f32_16x16x32_bf16 v[100:103], a[0:3], v[40:43], 0\n" \
  "ds_read_b128 v[32:35], v10 offset:2048\n" \
  "v_mfma_f32_16x16x32_bf16 v[112:115], a[0:3], v[44:47], 0\n" \
  "ds_read_b128 v[36:39], v10 offset:22528\n" \
  "v_mfma_f32_16x16x32_bf16 v[104:107], a[80:83], v[40:43], 0\n" \
  "v_cvt_pk_bf16_f32 v16, v56, v57\n" \
  "v_mfma_f32_16x16x32_bf16 v[116:119], a[80:83], v[44:47], 0\n" \
  "v_cvt_pk_bf16_f32 v17, v58, v59\n" \
  "v_mfma_f32_16x16x32_bf16 v[108:111], a[160:163], v[40:43], 0\n" \
  "v_cvt_pk_bf16_f32 v18, v60, v61\n" \
  "v_mfma_f32_16x16x32_bf16 v[120:123], a[160:163], v[44:47], 0\n" \
  "v_cvt_pk_bf16_f32 v19, v62, v63\n" \
  "s_waitcnt lgkmcnt(2)\n" \
  "s_barrier\n" \
  "v_mfma_f32_16x16x32_bf16 v[100:103], a[4:7], v[48:51], v[100:103]\n" \
  "ds_read_b128 v[40:43], v10 offset:3072\n" \
  "v_mfma_f32_16x16x32_bf16 v[112:115], a[4:7], v[52:55], v[112:115]\n" \
  "ds_read_b128 v[44:47], v10 offset:23552\n" \
  "v_mfma_f32_16x16x32_bf16 v[104:107], a[84:87], v[48:51], v[104:107]\n" \
  "buffer_store_dwordx4 v[16:19], v2, s[40:43], s69 offen offset:0 sc1\n" \
  "v_mfma_f32_16x16x32_bf16 v[116:119], a[84:87], v[52:55], v[116:119]\n" \
  "v_cvt_pk_bf16_f32 v20, v68, v69\n" \
  "v_mfma_f32_16x16x32_bf16 v[108:111], a[164:167], v[48:51], v[108:111]\n" \
  "v_cvt_pk_bf16_f32 v21, v70, v71\n" \
  "v_mfma_f32_16x16x32_bf16 v[120:123], a[164:167], v[52:55], v[120:123]\n" \
  "v_cvt_pk_bf16_f32 v22, v72, v73\n" \
  "s_waitcnt lgkmcnt(2)\n" \
  "v_mfma_f32_16x16x32_bf16 v[100:103], a[8:11], v[32:35], v[100:103]\n" \
  "ds_read_b128 v[48:51], v10 offset:4096\n" \
  "v_mfma_f32_16x16x32_bf16 v[112:115], a[8:11], v[36:39], v[112:115]\n" \
  "ds_read_b128 v[52:55], v10 offset:24576\n" \
  "v_mfma_f32_16x16x32_bf16 v[104:107], a[88:91], v[32:35], v[104:107]\n" \
  "v_cvt_pk_bf16_f32 v23, v74, v75\n" \
  "v_mfma_f32_16x16x32_bf16 v[116:119], a[88:91], v[36:39], v[116:119]\n" \
  "buffer_store_dwordx4 v[20:23], v2, s[40:43], s69 offen offset:1024 sc1\n" \
  "v_mfma_f32_16x16x32_bf16 v[108:111], a[168:171], v[32:35], v[108:111]\n" \
  "v_cvt_pk_bf16_f32 v24, v64, v65\n" \
  "v_mfma_f32_16x16x32_bf16 v[120:123], a[168:171], v[36:39], v[120:123]\n" \
  "v_cvt_pk_bf16_f32 v25, v66, v67\n" \
  "s_waitcnt lgkmcnt(2)\n" \
  "v_mfma_f32_16x16x32_bf16 v[100:103], a[12:15], v[40:43], v[100:103]\n" \
  "ds_read_b128 v[32:35], v10 offset:5120\n" \
  "v_mfma_f32_16x16x32_bf16 v[112:115], a[12:15], v[44:47], v[112:115]\n" \
  "ds_read_b128 v[36:39], v10 offset:25600\n" \
  "v_mfma_f32_16x16x32_bf16 v[104:107], a[92:95], v[40:43], v[104:107]\n" \
  "v_cvt_pk_bf16_f32 v26, v76, v77\n" \
  "v_mfma_f32_16x16x32_bf16 v[116:119], a[92:95], v[44:47], v[116:119]\n" \
  "v_cvt_pk_bf16_f32 v27, v78, v79\n" \
  "v_mfma_f32_16x16x32_bf16 v[108:111], a[172:175], v[40:43], v[108:111]\n" \
  "buffer_store_dwordx4 v[24:27], v2, s[40:43], s69 offen offset:2048 sc1\n" \
  "v_mfma_f32_16x16x32_bf16 v[120:123], a[172:175], v[44:47], v[120:123]\n" \
  "s_waitcnt lgkmcnt(2)\n" \
  "v_mfma_f32_16x16x32_bf16 v[100:103], a[16:19], v[48:51], v[100:103]\n" \
  "ds_read_b128 v[40:43], v10 offset:6144\n" \
  "v_mfma_f32_16x16x32_bf16 v[112:115], a[16:19], v[52:55], v[112:115]\n" \
  "ds_read_b128 v[44:47], v10 offset:26624\n" \
  "v_mfma_f32_16x16x32_bf16 v[104:107], a[96:99], v[48:51], v[104:107]\n" \
  "buffer_store_dwordx4 v[12:15], v5, s[40:43], s73 offen offset:0 sc1\n" \
  "v_mfma_f32_16x16x32_bf16 v[116:119], a[96:99], v[52:55], v[116:119]\n" \
  "v_mfma_f32_16x16x32_bf16 v[108:111], a[176:179], v[48:51], v[108:111]\n" \
  "v_mfma_f32_16x16x32_bf16 v[120:123], a[176:179], v[52:55], v[120:123]\n" \
  "s_waitcnt lgkmcnt(2)\n" \
  "v_mfma_f32_16x16x32_bf16 v[100:103], a[20:23], v[32:35], v[100:103]\n" \
  "ds_read_b128 v[48:51], v10 offset:7168\n" \
  "v_mfma_f32_16x16x32_bf16 v[112:115], a[20:23], v[36:39], v[112:115]\n" \
  "ds_read_b128 v[52:55], v10 offset:27648\n" \
  "v_mfma_f32_16x16x32_bf16 v[104:107], a[100:103], v[32:35], v[104:107]\n" \
  "buffer_store_dwordx4 v[12:15], v5, s[40:43], s73 offen offset:1024 sc1\n" \
  "v_mfma_f32_16x16x32_bf16 v[116:119], a[100:103], v[36:39], v[116:119]\n" \
  "v_mfma_f32_16x16x32_bf16 v[108:111], a[180:183], v[32:35], v[108:111]\n" \
  "v_mfma_f32_16x16x32_bf16 v[120:123], a[180:183], v[36:39], v[120:123]\n" \
  "s_waitcnt lgkmcnt(2)\n" \
  "v_mfma_f32_16x16x32_bf16 v[100:103], a[24:27], v[40:43], v[100:103]\n" \
  "ds_read_b128 v[32:35], v10 offset:8192\n" \
  "v_mfma_f32_16x16x32_bf16 v[112:115], a[24:27], v[44:47], v[112:115]\n" \
  "ds_read_b128 v[36:39], v10 offset:28672\n" \
  "v_mfma_f32_16x16x32_bf16 v[104:107], a[104:107], v[40:43], v[104:107]\n" \
  "buffer_store_dwordx4 v[12:15], v5, s[40:43], s73 offen offset:2048 sc1\n" \
  "v_mfma_f32_16x16x32_bf16 v[116:119], a[104:107], v[44:47], v[116:119]\n" \
  "s_nop 0\n" \
  "v_mfma_f32_16x16x32_bf16 v[108:111], a[184:187], v[40:43], v[108:111]\n" \
  "buffer_store_dwordx4 v[12:15], v5, s[40:43], s73 offen offset:3072 sc1\n" \
  "v_mfma_f32_16x16x32_bf16 v[120:123], a[184:187], v[44:47], v[120:123]\n" \
  "s_waitcnt lgkmcnt(2)\n" \
  "v_mfma_f32_16x16x32_bf16 v[100:103], a[28:31], v[48:51], v[100:103]\n" \
  "ds_read_b128 v[40:43], v10 offset:9216\n" \
  "v_mfma_f32_16x16x32_bf16 v[112:115], a[28:31], v[52:55], v[112:115]\n" \
  "ds_read_b128 v[44:47], v10 offset:29696\n" \
  "v_mfma_f32_16x16x32_bf16 v[104:107], a[108:111], v[48:51], v[104:107]\n" \
  "s_add_u32 s70, s68, 4096\n" \
  "v_mfma_f32_16x16x32_bf16 v[116:119], a[108:111], v[52:55], v[116:119]\n" \
  "s_add_u32 s71, s68, 8192\n" \
  "v_mfma_f32_16x16x32_bf16 v[108:111], a[188:191], v[48:51], v[108:111]\n" \
  "s_add_u32 m0, s53, 40960\n" \
  "v_mfma_f32_16x16x32_bf16 v[120:123], a[188:191], v[52:55], v[120:123]\n" \
  "buffer_load_dwordx4 v1, s[36:39], s68 offen sc1 lds\n" \
  "s_waitcnt lgkmcnt(2)\n" \
  "v_mfma_f32_16x16x32_bf16 v[100:103], a[32:35], v[32:35], v[100:103]\n" \
  "ds_read_b128 v[48:51], v10 offset:10240\n" \
  "v_mfma_f32_16x16x32_bf16 v[112:115], a[32:35], v[36:39], v[112:115]\n" \
  "ds_read_b128 v[52:55], v10 offset:30720\n" \
  "v_mfma_f32_16x16x32_bf16 v[104:107], a[112:115], v[32:35], v[104:107]\n" \
  "buffer_load_dwordx4 v1, s[36:39], s68 offen offset:1024 sc1 lds\n" \
  "v_mfma_f32_16x16x32_bf16 v[116:119], a[112:115], v[36:39], v[116:119]\n" \
  "v_mfma_f32_16x16x32_bf16 v[108:111], a[192:195], v[32:35], v[108:111]\n" \
  "v_mfma_f32_16x16x32_bf16 v[120:123], a[192:195], v[36:39], v[120:123]\n" \
  "s_waitcnt lgkmcnt(2)\n" \
  "v_mfma_f32_16x16x32_bf16 v[100:103], a[36:39], v[40:43], v[100:103]\n" \
  "ds_read_b128 v[32:35], v10 offset:11264\n" \
  "v_mfma_f32_16x16x32_bf16 v[112:115], a[36:39], v[44:47], v[112:115]\n" \
  "ds_read_b128 v[36:39], v10 offset:31744\n" \
  "v_mfma_f32_16x16x32_bf16 v[104:107], a[116:119], v[40:43], v[104:107]\n" \
  "buffer_load_dwordx4 v1, s[36:39], s68 offen offset:2048 sc1 lds\n" \
  "v_mfma_f32_16x16x32_bf16 v[116:119], a[116:119], v[44:47], v[116:119]\n" \
  "v_mfma_f32_16x16x32_bf16 v[108:111], a[196:199], v[40:43], v[108:111]\n" \
  "v_mfma_f32_16x16x32_bf16 v[120:123], a[196:199], v[44:47], v[120:123]\n" \
  "s_waitcnt lgkmcnt(2)\n" \
  "v_mfma_f32_16x16x32_bf16 v[100:103], a[40:43], v[48:51], v[100:103]\n" \
  "ds_read_b128 v[40:43], v10 offset:12288\n" \
  "v_mfma_f32_16x16x32_bf16 v[112:115], a[40:43], v[52:55], v[112:115]\n" \
  "ds_read_b128 v[44:47], v10 offset:32768\n" \
  "v_mfma_f32_16x16x32_bf16 v[104:107], a[120:123], v[48:51], v[104:107]\n" \
  "buffer_load_dwordx4 v1, s[36:39], s68 offen offset:3072 sc1 lds\n" \
  "v_mfma_f32_16x16x32_bf16 v[116:119], a[120:123], v[52:55], v[116:119]\n" \
  "v_mfma_f32_16x16x32_bf16 v[108:111], a[200:203], v[48:51], v[108:111]\n" \
  "v_mfma_f32_16x16x32_bf16 v[120:123], a[200:203], v[52:55], v[120:123]\n" \
  "s_waitcnt lgkmcnt(2)\n" \
  "v_mfma_f32_16x16x32_bf16 v[100:103], a[44:47], v[32:35], v[100:103]\n" \
  "ds_read_b128 v[48:51], v10 offset:13312\n" \
  "v_mfma_f32_16x16x32_bf16 v[112:115], a[44:47], v[36:39], v[112:115]\n" \
  "ds_read_b128 v[52:55], v10 offset:33792\n" \
  "v_mfma_f32_16x16x32_bf16 v[104:107], a[124:127], v[32:35], v[104:107]\n" \
  "s_add_u32 m0, s53, 45056\n" \
  "v_mfma_f32_16x16x32_bf16 v[116:119], a[124:127], v[36:39], v[116:119]\n" \
  "buffer_load_dwordx4 v1, s[36:39], s70 offen sc1 lds\n" \
  "v_mfma_f32_16x16x32_bf16 v[108:111], a[204:207], v[32:35], v[108:111]\n" \
  "v_mfma_f32_16x16x32_bf16 v[120:123], a[204:207], v[36:39], v[120:123]\n" \
  "s_waitcnt lgkmcnt(2)\n" \
  "v_mfma_f32_16x16x32_bf16 v[100:103], a[48:51], v[40:43], v[100:103]\n" \
  "ds_read_b128 v[32:35], v10 offset:14336\n" \
  "v_mfma_f32_16x16x32_bf16 v[112:115], a[48:51], v[44:47], v[112:115]\n" \
  "ds_read_b128 v[36:39], v10 offset:34816\n" \
  "v_mfma_f32_16x16x32_bf16 v[104:107], a[128:131], v[40:43], v[104:107]\n" \
  "buffer_load_dwordx4 v1, s[36:39], s70 offen offset:1024 sc1 lds\n" \
  "v_mfma_f32_16x16x32_bf16 v[116:119], a[128:131], v[44:47], v[116:119]\n" \
  "v_mfma_f32_16x16x32_bf16 v[108:111], a[208:211], v[40:43], v[108:111]\n" \
  "v_mfma_f32_16x16x32_bf16 v[120:123], a[208:211], v[44:47], v[120:123]\n" \
  "s_waitcnt vmcnt(6) lgkmcnt(2)\n" \
  "s_barrier\n" \
  "v_mfma_f32_16x16x32_bf16 v[100:103], a[52:55], v[48:51], v[100:103]\n" \
  "ds_read_b128 v[40:43], v10 offset:15360\n" \
  "v_mfma_f32_16x16x32_bf16 v[112:115], a[52:55], v[52:55], v[112:115]\n" \
  "ds_read_b128 v[44:47], v10 offset:35840\n" \
  "v_mfma_f32_16x16x32_bf16 v[104:107], a[132:135], v[48:51], v[104:107]\n" \
  "buffer_atomic_add v11, v4, s[40:43], s54 offen\n" \
  "v_mfma_f32_16x16x32_bf16 v[116:119], a[132:135], v[52:55], v[116:119]\n" \
  "s_add_u32 s54, s54, s55\n" \
  "v_mfma_f32_16x16x32_bf16 v[108:111], a[212:215], v[48:51], v[108:111]\n" \
  "s_and_b32 s54, s54, 0xffffdfff\n" \
  "v_mfma_f32_16x16x32_bf16 v[120:123], a[212:215], v[52:55], v[120:123]\n" \
  "s_waitcnt lgkmcnt(2)\n" \
  "v_mfma_f32_16x16x32_bf16 v[100:103], a[56:59], v[32:35], v[100:103]\n" \
  "ds_read_b128 v[48:51], v10 offset:16384\n" \
  "v_mfma_f32_16x16x32_bf16 v[112:115], a[56:59], v[36:39], v[112:115]\n" \
  "ds_read_b128 v[52:55], v10 offset:36864\n" \
  "v_mfma_f32_16x16x32_bf16 v[104:107], a[136:139], v[32:35], v[104:107]\n" \
  "buffer_load_dwordx4 v1, s[36:39], s70 offen offset:2048 sc1 lds\n" \
  "v_mfma_f32_16x16x32_bf16 v[116:119], a[136:139], v[36:39], v[116:119]\n" \
  "v_readlane_b32 s69, v3, s72\n" \
  "v_mfma_f32_16x16x32_bf16 v[108:111], a[216:219], v[32:35], v[108:111]\n" \
  "v_readlane_b32 s73, v6, s72\n" \
  "v_mfma_f32_16x16x32_bf16 v[120:123], a[216:219], v[36:39], v[120:123]\n" \
  "s_add_u32 s72, s72, 1\n" \
  "s_waitcnt lgkmcnt(2)\n" \
  "v_mfma_f32_16x16x32_bf16 v[100:103], a[60:63], v[40:43], v[100:103]\n" \
  "ds_read_b128 v[32:35], v10 offset:17408\n" \
  "v_mfma_f32_16x16x32_bf16 v[112:115], a[60:63], v[44:47], v[112:115]\n" \
  "ds_read_b128 v[36:39], v10 offset:37888\n" \
  "v_mfma_f32_16x16x32_bf16 v[104:107], a[140:143], v[40:43], v[104:107]\n" \
  "buffer_load_dwordx4 v1, s[36:39], s70 offen offset:3072 sc1 lds\n" \
  "v_mfma_f32_16x16x32_bf16 v[116:119], a[140:143], v[44:47], v[116:119]\n" \
  "v_mfma_f32_16x16x32_bf16 v[108:111], a[220:223], v[40:43], v[108:111]\n" \
  "v_mfma_f32_16x16x32_bf16 v[120:123], a[220:223], v[44:47], v[120:123]\n" \
  "s_waitcnt lgkmcnt(2)\n" \
  "v_mfma_f32_16x16x32_bf16 v[100:103], a[64:67], v[48:51], v[100:103]\n" \
  "ds_read_b128 v[40:43], v10 offset:18432\n" \
  "v_mfma_f32_16x16x32_bf16 v[112:115], a[64:67], v[52:55], v[112:115]\n" \
  "ds_read_b128 v[44:47], v10 offset:38912\n" \
  "v_mfma_f32_16x16x32_bf16 v[104:107], a[144:147], v[48:51], v[104:107]\n" \
  "s_add_u32 m0, s53, 49152\n" \
  "v_mfma_f32_16x16x32_bf16 v[116:119], a[144:147], v[52:55], v[116:119]\n" \
  "buffer_load_dwordx4 v1, s[36:39], s71 offen sc1 lds\n" \
  "v_mfma_f32_16x16x32_bf16 v[108:111], a[224:227], v[48:51], v[108:111]\n" \
  "v_mfma_f32_16x16x32_bf16 v[120:123], a[224:227], v[52:55], v[120:123]\n" \
  "s_waitcnt lgkmcnt(2)\n" \
  "v_mfma_f32_16x16x32_bf16 v[100:103], a[68:71], v[32:35], v[100:103]\n" \
  "ds_read_b128 v[48:51], v10 offset:19456\n" \
  "v_mfma_f32_16x16x32_bf16 v[112:115], a[68:71], v[36:39], v[112:115]\n" \
  "ds_read_b128 v[52:55], v10 offset:39936\n" \
  "v_mfma_f32_16x16x32_bf16 v[104:107], a[148:151], v[32:35], v[104:107]\n" \
  "buffer_load_dwordx4 v1, s[36:39], s71 offen offset:1024 sc1 lds\n" \
  "v_mfma_f32_16x16x32_bf16 v[116:119], a[148:151], v[36:39], v[116:119]\n" \
  "s_add_u32 s68, s68, 40960\n" \
  "v_mfma_f32_16x16x32_bf16 v[108:111], a[228:231], v[32:35], v[108:111]\n" \
  "v_mfma_f32_16x16x32_bf16 v[120:123], a[228:231], v[36:39], v[120:123]\n" \
  "s_waitcnt vmcnt(11) lgkmcnt(2)\n" \
  "s_barrier\n" \
  "v_mfma_f32_16x16x32_bf16 v[100:103], a[72:75], v[40:43], v[100:103]\n" \
  "v_mfma_f32_16x16x32_bf16 v[112:115], a[72:75], v[44:47], v[112:115]\n" \
  "v_mfma_f32_16x16x32_bf16 v[104:107], a[152:155], v[40:43], v[104:107]\n" \
  "v_mfma_f32_16x16x32_bf16 v[116:119], a[152:155], v[44:47], v[116:119]\n" \
  "v_mfma_f32_16x16x32_bf16 v[108:111], a[232:235], v[40:43], v[108:111]\n" \
  "v_mfma_f32_16x16x32_bf16 v[120:123], a[232:235], v[44:47], v[120:123]\n" \
  "s_waitcnt lgkmcnt(0)\n" \
  "v_mfma_f32_16x16x32_bf16 v[100:103], a[76:79], v[48:51], v[100:103]\n" \
  "s_cmp_eq_u32 s72, 64\n" \
  "v_mfma_f32_16x16x32_bf16 v[112:115], a[76:79], v[52:55], v[112:115]\n" \
  "v_mfma_f32_16x16x32_bf16 v[104:107], a[156:159], v[48:51], v[104:107]\n" \
  "v_mfma_f32_16x16x32_bf16 v[116:119], a[156:159], v[52:55], v[116:119]\n" \
  "v_mfma_f32_16x16x32_bf16 v[108:111], a[236:239], v[48:51], v[108:111]\n" \
  "v_mfma_f32_16x16x32_bf16 v[120:123], a[236:239], v[52:55], v[120:123]\n" \
  "LPE_%=:\n" \
  "s_nop 6\n" \
  "v_cvt_pk_bf16_f32 v16, v100, v101\n" \
  "v_cvt_pk_bf16_f32 v17, v102, v103\n" \
  "v_cvt_pk_bf16_f32 v18, v104, v105\n" \
  "v_cvt_pk_bf16_f32 v19, v106, v107\n" \
  "buffer_store_dwordx4 v[16:19], v2, s[40:43], s69 offen offset:0 sc1\n" \
  "v_cvt_pk_bf16_f32 v20, v112, v113\n" \
  "v_cvt_pk_bf16_f32 v21, v114, v115\n" \
  "v_cvt_pk_bf16_f32 v22, v116, v117\n" \
  "v_cvt_pk_bf16_f32 v23, v118, v119\n" \
  "buffer_store_dwordx4 v[20:23], v2, s[40:43], s69 offen offset:1024 sc1\n" \
  "v_cvt_pk_bf16_f32 v24, v108, v109\n" \
  "v_cvt_pk_bf16_f32 v25, v110, v111\n" \
  "v_cvt_pk_bf16_f32 v26, v120, v121\n" \
  "v_cvt_pk_bf16_f32 v27, v122, v123\n" \
  "buffer_store_dwordx4 v[24:27], v2, s[40:43], s69 offen offset:2048 sc1\n" \
  "buffer_store_dwordx4 v[12:15], v5, s[40:43], s73 offen offset:0 sc1\n" \
  "buffer_store_dwordx4 v[12:15], v5, s[40:43], s73 offen offset:1024 sc1\n" \
  "buffer_store_dwordx4 v[12:15], v5, s[40:43], s73 offen offset:2048 sc1\n" \
  "buffer_store_dwordx4 v[12:15], v5, s[40:43], s73 offen offset:3072 sc1\n" \
  "s_waitcnt vmcnt(0) lgkmcnt(0)\n" \
  "s_barrier\n" \
  "buffer_atomic_add v11, v4, s[40:43], s54 offen\n" \
  "s_add_u32 s54, s54, s55\n" \
  "s_and_b32 s54, s54, 0xffffdfff\n"
#define VO_CLOBBERS \
  "v16", "v17", "v18", "v19", "v20", "v21", "v22", "v23", "v24", "v25", "v26", "v27", "v28", "v29", "v30", "v31", "v32", "v33", "v34", "v35", "v36", \
  "v37", "v38", "v39", "v40", "v41", "v42", "v43", "v44", "v45", "v46", "v47", "v48", "v49", "v50", "v51", "v52", "v53", "v54", "v55", "v56", "v57", \
  "v58", "v59", "v60", "v61", "v62", "v63", "v64", "v65", "v66", "v67", "v68", "v69", "v70", "v71", "v72", "v73", "v74", "v75", "v76", "v77", "v78", \
  "v79", "v80", "v81", "v82", "v83", "v84", "v85", "v86", "v87", "v88", "v89", "v90", "v91", "v100", "v101", "v102", "v103", "v104", "v105", "v106", \
  "v107", "v108", "v109", "v110", "v111", "v112", "v113", "v114", "v115", "v116", "v117", "v118", "v119", "v120", "v121", "v122", "v123", "v124", \
  "v125", "v126", "v127", "v128", "v129", "v130", "v131", "v132", "v133", "v134", "v135", "v136", "v137", "v138", "v139", "v140", "v141", "v142", \
  "v143", "v144", "v145", "v146", "v147", "s41", "s42", "s43", "s44", "s45", "s46", "s47", "s48", "s49", "s50"
#define VO_ASM \
  "v_mov_b32 v91, 1\n" \
  "s_mov_b32 s41, 0\n" \
  "s_mov_b32 s48, 0\n" \
  "s_mov_b32 s47, 1073741824\n" \
  "s_mov_b32 s44, 1073741824\n" \
  "s_mov_b32 s49, 1073741824\n" \
  "v_readlane_b32 s42, v4, 0\n" \
  "s_add_u32 s46, s41, 0\n" \
  "s_min_u32 s46, s46, 63\n" \
  "v_mad_u32_u24 v88, v8, s46, v7\n" \
  "v_mad_u32_u24 v89, v9, s46, v10\n" \
  "buffer_load_dword v90, v88, s[36:39], 0 offen sc1\n" \
  "s_waitcnt vmcnt(0)\n" \
  "v_cmp_gt_u32 vcc, v89, v90\n" \
  "s_cbranch_vccnz LPS_%=\n" \
  "LKS_%=:\n" \
  "s_add_u32 m0, s40, 0\n" \
  "s_nop 0\n" \
  "buffer_load_dwordx4 v1, s[36:39], s42 offen offset:0 sc1 lds\n" \
  "buffer_load_dwordx4 v1, s[36:39], s42 offen offset:1024 sc1 lds\n" \
  "buffer_load_dwordx4 v1, s[36:39], s42 offen offset:2048 sc1 lds\n" \
  "s_waitcnt vmcnt(0)\n" \
  "s_barrier\n" \
  "ds_read_b128 v[40:43], v0 offset:0\n" \
  "ds_read_b128 v[44:47], v0 offset:3072\n" \
  "ds_read_b128 v[48:51], v0 offset:6144\n" \
  "ds_read_b128 v[52:55], v0 offset:9216\n" \
  "ds_read_b64 v[56:57], v0 offset:2048\n" \
  "ds_read_b64 v[58:59], v0 offset:5120\n" \
  "ds_read_b64 v[60:61], v0 offset:8192\n" \
  "ds_read_b64 v[62:63], v0 offset:11264\n" \
  "s_waitcnt lgkmcnt(0)\n" \
  "ds_read_b128 v[64:67], v0 offset:1024\n" \
  "ds_read_b128 v[68:71], v0 offset:4096\n" \
  "ds_read_b128 v[72:75], v0 offset:7168\n" \
  "ds_read_b128 v[76:79], v0 offset:10240\n" \
  "ds_read_b64 v[80:81], v0 offset:2056\n" \
  "ds_read_b64 v[82:83], v0 offset:5128\n" \
  "ds_read_b64 v[84:85], v0 offset:8200\n" \
  "ds_read_b64 v[86:87], v0 offset:11272\n" \
  "s_waitcnt lgkmcnt(0)\n" \
  "LL_%=:\n" \
  "v_mfma_f32_16x16x32_bf16 v[24:27], v[40:43], a[0:3], 0\n" \
  "s_add_u32 s46, s41, 1\n" \
  "s_min_u32 s46, s46, 63\n" \
  "v_mad_u32_u24 v88, v8, s46, v7\n" \
  "v_mfma_f32_16x16x32_bf16 v[28:31], v[64:67], a[0:3], 0\n" \
  "v_mad_u32_u24 v89, v9, s46, v10\n" \
  "v_mfma_f32_16x16x32_bf16 v[24:27], v[44:47], a[4:7], v[24:27]\n" \
  "v_cvt_pk_bf16_f32 v20, v32, v33\n" \
  "v_cvt_pk_bf16_f32 v21, v34, v35\n" \
  "v_mfma_f32_16x16x32_bf16 v[28:31], v[68:71], a[4:7], v[28:31]\n" \
  "v_cvt_pk_bf16_f32 v22, v36, v37\n" \
  "v_cvt_pk_bf16_f32 v23, v38, v39\n" \
  "v_mfma_f32_16x16x32_bf16 v[24:27], v[48:51], a[8:11], v[24:27]\n" \
  "buffer_store_dwordx4 v[20:23], v3, s[36:39], s44 offen sc1\n" \
  "v_mfma_f32_16x16x32_bf16 v[28:31], v[72:75], a[8:11], v[28:31]\n" \
  "s_xor_b32 s50, s44, 0x2000\n" \
  "s_add_u32 s44, s44, 0x40000\n" \
  "v_mfma_f32_16x16x32_bf16 v[24:27], v[52:55], a[12:15], v[24:27]\n" \
  "v_readlane_b32 s43, v5, s41\n" \
  "v_mfma_f32_16x16x32_bf16 v[28:31], v[76:79], a[12:15], v[28:31]\n" \
  "buffer_load_dword v90, v88, s[36:39], 0 offen sc1\n" \
  "v_mfma_f32_16x16x32_bf16 v[24:27], v[56:59], a[16:19], v[24:27]\n" \
  "v_readlane_b32 s44, v6, s41\n" \
  "v_mfma_f32_16x16x32_bf16 v[28:31], v[80:83], a[16:19], v[28:31]\n" \
  "v_readlane_b32 s42, v4, s46\n" \
  "v_mfma_f32_16x16x32_bf16 v[24:27], v[60:63], a[20:23], v[24:27]\n" \
  "buffer_store_dwordx4 v[20:23], v3, s[36:39], s50 offen sc1\n" \
  "v_mfma_f32_16x16x32_bf16 v[28:31], v[84:87], a[20:23], v[28:31]\n" \
  "v_mfma_f32_16x16x32_bf16 v[32:35], v[40:43], a[24:27], 0\n" \
  "v_mfma_f32_16x16x32_bf16 v[36:39], v[64:67], a[24:27], 0\n" \
  "v_mfma_f32_16x16x32_bf16 v[32:35], v[44:47], a[28:31], v[32:35]\n" \
  "v_cvt_pk_bf16_f32 v16, v24, v25\n" \
  "v_cvt_pk_bf16_f32 v17, v26, v27\n" \
  "v_mfma_f32_16x16x32_bf16 v[36:39], v[68:71], a[28:31], v[36:39]\n" \
  "v_cvt_pk_bf16_f32 v18, v28, v29\n" \
  "v_cvt_pk_bf16_f32 v19, v30, v31\n" \
  "v_mfma_f32_16x16x32_bf16 v[32:35], v[48:51], a[32:35], v[32:35]\n" \
  "buffer_store_dwordx4 v[16:19], v2, s[36:39], s43 offen sc1\n" \
  "v_mfma_f32_16x16x32_bf16 v[36:39], v[72:75], a[32:35], v[36:39]\n" \
  "s_xor_b32 s50, s43, 0x2000\n" \
  "s_add_u32 s43, s43, 0x40000\n" \
  "v_mfma_f32_16x16x32_bf16 v[32:35], v[52:55], a[36:39], v[32:35]\n" \
  "v_mfma_f32_16x16x32_bf16 v[36:39], v[76:79], a[36:39], v[36:39]\n" \
  "v_mfma_f32_16x16x32_bf16 v[32:35], v[56:59], a[40:43], v[32:35]\n" \
  "v_mfma_f32_16x16x32_bf16 v[36:39], v[80:83], a[40:43], v[36:39]\n" \
  "v_mfma_f32_16x16x32_bf16 v[32:35], v[60:63], a[44:47], v[32:35]\n" \
  "buffer_store_dwordx4 v[16:19], v2, s[36:39], s50 offen sc1\n" \
  "v_mfma_f32_16x16x32_bf16 v[36:39], v[84:87], a[44:47], v[36:39]\n" \
  "v_mfma_f32_16x16x32_bf16 v[24:27], v[40:43], a[48:51], 0\n" \
  "v_mfma_f32_16x16x32_bf16 v[28:31], v[64:67], a[48:51], 0\n" \
  "v_mfma_f32_16x16x32_bf16 v[24:27], v[44:47], a[52:55], v[24:27]\n" \
  "v_cvt_pk_bf16_f32 v20, v32, v33\n" \
  "v_cvt_pk_bf16_f32 v21, v34, v35\n" \
  "v_mfma_f32_16x16x32_bf16 v[28:31], v[68:71], a[52:55], v[28:31]\n" \
  "v_cvt_pk_bf16_f32 v22, v36, v37\n" \
  "v_cvt_pk_bf16_f32 v23, v38, v39\n" \
  "v_mfma_f32_16x16x32_bf16 v[24:27], v[48:51], a[56:59], v[24:27]\n" \
  "buffer_store_dwordx4 v[20:23], v2, s[36:39], s43 offen sc1\n" \
  "v_mfma_f32_16x16x32_bf16 v[28:31], v[72:75], a[56:59], v[28:31]\n" \
  "s_xor_b32 s50, s43, 0x2000\n" \
  "s_add_u32 s43, s43, 0x40000\n" \
  "v_mfma_f32_16x16x32_bf16 v[24:27], v[52:55], a[60:63], v[24:27]\n" \
  "v_mfma_f32_16x16x32_bf16 v[28:31], v[76:79], a[60:63], v[28:31]\n" \
  "v_mfma_f32_16x16x32_bf16 v[24:27], v[56:59], a[64:67], v[24:27]\n" \
  "v_mfma_f32_16x16x32_bf16 v[28:31], v[80:83], a[64:67], v[28:31]\n" \
  "v_mfma_f32_16x16x32_bf16 v[24:27], v[60:63], a[68:71], v[24:27]\n" \
  "buffer_store_dwordx4 v[20:23], v2, s[36:39], s50 offen sc1\n" \
  "v_mfma_f32_16x16x32_bf16 v[28:31], v[84:87], a[68:71], v[28:31]\n" \
  "v_mfma_f32_16x16x32_bf16 v[32:35], v[40:43], a[72:75], 0\n" \
  "v_mfma_f32_16x16x32_bf16 v[36:39], v[64:67], a[72:75], 0\n" \
  "v_mfma_f32_16x16x32_bf16 v[32:35], v[44:47], a[76:79], v[32:35]\n" \
  "v_cvt_pk_bf16_f32 v16, v24, v25\n" \
  "v_cvt_pk_bf16_f32 v17, v26, v27\n" \
  "v_mfma_f32_16x16x32_bf16 v[36:39], v[68:71], a[76:79], v[36:39]\n" \
  "v_cvt_pk_bf16_f32 v18, v28, v29\n" \
  "v_cvt_pk_bf16_f32 v19, v30, v31\n" \
  "v_mfma_f32_16x16x32_bf16 v[32:35], v[48:51], a[80:83], v[32:35]\n" \
  "buffer_store_dwordx4 v[16:19], v2, s[36:39], s43 offen sc1\n" \
  "v_mfma_f32_16x16x32_bf16 v[36:39], v[72:75], a[80:83], v[36:39]\n" \
  "s_xor_b32 s50, s43, 0x2000\n" \
  "s_add_u32 s43, s43, 0x40000\n" \
  "v_mfma_f32_16x16x32_bf16 v[32:35], v[52:55], a[84:87], v[32:35]\n" \
  "v_mfma_f32_16x16x32_bf16 v[36:39], v[76:79], a[84:87], v[36:39]\n" \
  "v_mfma_f32_16x16x32_bf16 v[32:35], v[56:59], a[88:91], v[32:35]\n" \
  "v_mfma_f32_16x16x32_bf16 v[36:39], v[80:83], a[88:91], v[36:39]\n" \
  "v_mfma_f32_16x16x32_bf16 v[32:35], v[60:63], a[92:95], v[32:35]\n" \
  "buffer_store_dwordx4 v[16:19], v2, s[36:39], s50 offen sc1\n" \
  "v_mfma_f32_16x16x32_bf16 v[36:39], v[84:87], a[92:95], v[36:39]\n" \
  "v_mfma_f32_16x16x32_bf16 v[24:27], v[40:43], a[96:99], 0\n" \
  "s_waitcnt vmcnt(6)\n" \
  "s_barrier\n" \
  "v_cmp_gt_u32 vcc, v89, v90\n" \
  "v_mfma_f32_16x16x32_bf16 v[28:31], v[64:67], a[96:99], 0\n" \
  "buffer_atomic_add v91, v11, s[36:39], s47 offen\n" \
  "v_mfma_f32_16x16x32_bf16 v[24:27], v[44:47], a[100:103], v[24:27]\n" \
  "v_cvt_pk_bf16_f32 v20, v32, v33\n" \
  "v_cvt_pk_bf16_f32 v21, v34, v35\n" \
  "v_mfma_f32_16x16x32_bf16 v[28:31], v[68:71], a[100:103], v[28:31]\n" \
  "v_cvt_pk_bf16_f32 v22, v36, v37\n" \
  "v_cvt_pk_bf16_f32 v23, v38, v39\n" \
  "v_mfma_f32_16x16x32_bf16 v[24:27], v[48:51], a[104:107], v[24:27]\n" \
  "buffer_store_dwordx4 v[20:23], v2, s[36:39], s43 offen sc1\n" \
  "v_mfma_f32_16x16x32_bf16 v[28:31], v[72:75], a[104:107], v[28:31]\n" \
  "s_xor_b32 s50, s43, 0x2000\n" \
  "s_add_u32 s43, s43, 0x40000\n" \
  "v_mfma_f32_16x16x32_bf16 v[24:27], v[52:55], a[108:111], v[24:27]\n" \
  "s_cbranch_vccnz LP0_%=\n" \
  "LK0_%=:\n" \
  "s_add_u32 m0, s40, 12288\n" \
  "v_mfma_f32_16x16x32_bf16 v[28:31], v[76:79], a[108:111], v[28:31]\n" \
  "buffer_load_dwordx4 v1, s[36:39], s42 offen offset:0 sc1 lds\n" \
  "v_mfma_f32_16x16x32_bf16 v[24:27], v[56:59], a[112:115], v[24:27]\n" \
  "v_mfma_f32_16x16x32_bf16 v[28:31], v[80:83], a[112:115], v[28:31]\n" \
  "buffer_load_dwordx4 v1, s[36:39], s42 offen offset:1024 sc1 lds\n" \
  "v_mfma_f32_16x16x32_bf16 v[24:27], v[60:63], a[116:119], v[24:27]\n" \
  "buffer_store_dwordx4 v[20:23], v2, s[36:39], s50 offen sc1\n" \
  "v_mfma_f32_16x16x32_bf16 v[28:31], v[84:87], a[116:119], v[28:31]\n" \
  "buffer_load_dwordx4 v1, s[36:39], s42 offen offset:2048 sc1 lds\n" \
  "v_mfma_f32_16x16x32_bf16 v[32:35], v[40:43], a[120:123], 0\n" \
  "v_mfma_f32_16x16x32_bf16 v[36:39], v[64:67], a[120:123], 0\n" \
  "buffer_atomic_add v91, v12, s[36:39], s49 offen\n" \
  "v_mfma_f32_16x16x32_bf16 v[32:35], v[44:47], a[124:127], v[32:35]\n" \
  "v_cvt_pk_bf16_f32 v16, v24, v25\n" \
  "v_cvt_pk_bf16_f32 v17, v26, v27\n" \
  "v_mfma_f32_16x16x32_bf16 v[36:39], v[68:71], a[124:127], v[36:39]\n" \
  "v_cvt_pk_bf16_f32 v18, v28, v29\n" \
  "v_cvt_pk_bf16_f32 v19, v30, v31\n" \
  "v_mfma_f32_16x16x32_bf16 v[32:35], v[48:51], a[128:131], v[32:35]\n" \
  "buffer_store_dwordx4 v[16:19], v2, s[36:39], s43 offen sc1\n" \
  "v_mfma_f32_16x16x32_bf16 v[36:39], v[72:75], a[128:131], v[36:39]\n" \
  "s_xor_b32 s50, s43, 0x2000\n" \
  "s_add_u32 s43, s43, 0x40000\n" \
  "v_mfma_f32_16x16x32_bf16 v[32:35], v[52:55], a[132:135], v[32:35]\n" \
  "s_mov_b32 s49, 0\n" \
  "s_lshr_b32 s45, s41, 4\n" \
  "s_lshl_b32 s45, s45, 13\n" \
  "v_mfma_f32_16x16x32_bf16 v[36:39], v[76:79], a[132:135], v[36:39]\n" \
  "s_lshl_b32 s47, s41, 9\n" \
  "s_add_u32 s47, s47, s45\n" \
  "v_mfma_f32_16x16x32_bf16 v[32:35], v[56:59], a[136:139], v[32:35]\n" \
  "v_mfma_f32_16x16x32_bf16 v[36:39], v[80:83], a[136:139], v[36:39]\n" \
  "v_mfma_f32_16x16x32_bf16 v[32:35], v[60:63], a[140:143], v[32:35]\n" \
  "buffer_store_dwordx4 v[16:19], v2, s[36:39], s50 offen sc1\n" \
  "v_mfma_f32_16x16x32_bf16 v[36:39], v[84:87], a[140:143], v[36:39]\n" \
  "v_mfma_f32_16x16x32_bf16 v[24:27], v[40:43], a[144:147], 0\n" \
  "v_mfma_f32_16x16x32_bf16 v[28:31], v[64:67], a[144:147], 0\n" \
  "v_mfma_f32_16x16x32_bf16 v[24:27], v[44:47], a[148:151], v[24:27]\n" \
  "v_cvt_pk_bf16_f32 v20, v32, v33\n" \
  "v_cvt_pk_bf16_f32 v21, v34, v35\n" \
  "v_mfma_f32_16x16x32_bf16 v[28:31], v[68:71], a[148:151], v[28:31]\n" \
  "v_cvt_pk_bf16_f32 v22, v36, v37\n" \
  "v_cvt_pk_bf16_f32 v23, v38, v39\n" \
  "v_mfma_f32_16x16x32_bf16 v[24:27], v[48:51], a[152:155], v[24:27]\n" \
  "buffer_store_dwordx4 v[20:23], v2, s[36:39], s43 offen sc1\n" \
  "v_mfma_f32_16x16x32_bf16 v[28:31], v[72:75], a[152:155], v[28:31]\n" \
  "s_xor_b32 s50, s43, 0x2000\n" \
  "s_add_u32 s43, s43, 0x40000\n" \
  "v_mfma_f32_16x16x32_bf16 v[24:27], v[52:55], a[156:159], v[24:27]\n" \
  "v_mfma_f32_16x16x32_bf16 v[28:31], v[76:79], a[156:159], v[28:31]\n" \
  "v_mfma_f32_16x16x32_bf16 v[24:27], v[56:59], a[160:163], v[24:27]\n" \
  "v_mfma_f32_16x16x32_bf16 v[28:31], v[80:83], a[160:163], v[28:31]\n" \
  "v_mfma_f32_16x16x32_bf16 v[24:27], v[60:63], a[164:167], v[24:27]\n" \
  "buffer_store_dwordx4 v[20:23], v2, s[36:39], s50 offen sc1\n" \
  "v_mfma_f32_16x16x32_bf16 v[28:31], v[84:87], a[164:167], v[28:31]\n" \
  "v_mfma_f32_16x16x32_bf16 v[32:35], v[40:43], a[168:171], 0\n" \
  "v_mfma_f32_16x16x32_bf16 v[36:39], v[64:67], a[168:171], 0\n" \
  "v_mfma_f32_16x16x32_bf16 v[32:35], v[44:47], a[172:175], v[32:35]\n" \
  "v_cvt_pk_bf16_f32 v16, v24, v25\n" \
  "v_cvt_pk_bf16_f32 v17, v26, v27\n" \
  "v_mfma_f32_16x16x32_bf16 v[36:39], v[68:71], a[172:175], v[36:39]\n" \
  "v_cvt_pk_bf16_f32 v18, v28, v29\n" \
  "v_cvt_pk_bf16_f32 v19, v30, v31\n" \
  "v_mfma_f32_16x16x32_bf16 v[32:35], v[48:51], a[176:179], v[32:35]\n" \
  "buffer_store_dwordx4 v[16:19], v2, s[36:39], s43 offen sc1\n" \
  "v_mfma_f32_16x16x32_bf16 v[36:39], v[72:75], a[176:179], v[36:39]\n" \
  "s_xor_b32 s50, s43, 0x2000\n" \
  "s_add_u32 s43, s43, 0x40000\n" \
  "v_mfma_f32_16x16x32_bf16 v[32:35], v[52:55], a[180:183], v[32:35]\n" \
  "v_mfma_f32_16x16x32_bf16 v[36:39], v[76:79], a[180:183], v[36:39]\n" \
  "v_mfma_f32_16x16x32_bf16 v[32:35], v[56:59], a[184:187], v[32:35]\n" \
  "v_mfma_f32_16x16x32_bf16 v[36:39], v[80:83], a[184:187], v[36:39]\n" \
  "v_mfma_f32_16x16x32_bf16 v[32:35], v[60:63], a[188:191], v[32:35]\n" \
  "buffer_store_dwordx4 v[16:19], v2, s[36:39], s50 offen sc1\n" \
  "v_mfma_f32_16x16x32_bf16 v[36:39], v[84:87], a[188:191], v[36:39]\n" \
  "v_mfma_f32_16x16x32_bf16 v[24:27], v[40:43], a[192:195], 0\n" \
  "v_mfma_f32_16x16x32_bf16 v[28:31], v[64:67], a[192:195], 0\n" \
  "v_mfma_f32_16x16x32_bf16 v[24:27], v[44:47], a[196:199], v[24:27]\n" \
  "v_cvt_pk_bf16_f32 v20, v32, v33\n" \
  "v_cvt_pk_bf16_f32 v21, v34, v35\n" \
  "v_mfma_f32_16x16x32_bf16 v[28:31], v[68:71], a[196:199], v[28:31]\n" \
  "v_cvt_pk_bf16_f32 v22, v36, v37\n" \
  "v_cvt_pk_bf16_f32 v23, v38, v39\n" \
  "v_mfma_f32_16x16x32_bf16 v[24:27], v[48:51], a[200:203], v[24:27]\n" \
  "buffer_store_dwordx4 v[20:23], v2, s[36:39], s43 offen sc1\n" \
  "v_mfma_f32_16x16x32_bf16 v[28:31], v[72:75], a[200:203], v[28:31]\n" \
  "s_xor_b32 s50, s43, 0x2000\n" \
  "s_add_u32 s43, s43, 0x40000\n" \
  "v_mfma_f32_16x16x32_bf16 v[24:27], v[52:55], a[204:207], v[24:27]\n" \
  "s_waitcnt vmcnt(8)\n" \
  "s_barrier\n" \
  "v_mfma_f32_16x16x32_bf16 v[28:31], v[76:79], a[204:207], v[28:31]\n" \
  "ds_read_b128 v[100:103], v0 offset:12288\n" \
  "ds_read_b128 v[104:107], v0 offset:15360\n" \
  "v_mfma_f32_16x16x32_bf16 v[24:27], v[56:59], a[208:211], v[24:27]\n" \
  "ds_read_b128 v[108:111], v0 offset:18432\n" \
  "ds_read_b128 v[112:115], v0 offset:21504\n" \
  "v_mfma_f32_16x16x32_bf16 v[28:31], v[80:83], a[208:211], v[28:31]\n" \
  "ds_read_b64 v[116:117], v0 offset:14336\n" \
  "ds_read_b64 v[118:119], v0 offset:17408\n" \
  "v_mfma_f32_16x16x32_bf16 v[24:27], v[60:63], a[212:215], v[24:27]\n" \
  "buffer_store_dwordx4 v[20:23], v2, s[36:39], s50 offen sc1\n" \
  "ds_read_b64 v[120:121], v0 offset:20480\n" \
  "v_mfma_f32_16x16x32_bf16 v[28:31], v[84:87], a[212:215], v[28:31]\n" \
  "ds_read_b64 v[122:123], v0 offset:23552\n" \
  "ds_read_b128 v[124:127], v0 offset:13312\n" \
  "v_mfma_f32_16x16x32_bf16 v[32:35], v[40:43], a[216:219], 0\n" \
  "ds_read_b128 v[128:131], v0 offset:16384\n" \
  "ds_read_b128 v[132:135], v0 offset:19456\n" \
  "v_mfma_f32_16x16x32_bf16 v[36:39], v[64:67], a[216:219], 0\n" \
  "ds_read_b128 v[136:139], v0 offset:22528\n" \
  "ds_read_b64 v[140:141], v0 offset:14344\n" \
  "v_mfma_f32_16x16x32_bf16 v[32:35], v[44:47], a[220:223], v[32:35]\n" \
  "v_cvt_pk_bf16_f32 v16, v24, v25\n" \
  "v_cvt_pk_bf16_f32 v17, v26, v27\n" \
  "v_mfma_f32_16x16x32_bf16 v[36:39], v[68:71], a[220:223], v[36:39]\n" \
  "v_cvt_pk_bf16_f32 v18, v28, v29\n" \
  "v_cvt_pk_bf16_f32 v19, v30, v31\n" \
  "v_mfma_f32_16x16x32_bf16 v[32:35], v[48:51], a[224:227], v[32:35]\n" \
  "buffer_store_dwordx4 v[16:19], v3, s[36:39], s44 offen sc1\n" \
  "ds_read_b64 v[142:143], v0 offset:17416\n" \
  "v_mfma_f32_16x16x32_bf16 v[36:39], v[72:75], a[224:227], v[36:39]\n" \
  "s_xor_b32 s50, s44, 0x2000\n" \
  "s_add_u32 s44, s44, 0x40000\n" \
  "ds_read_b64 v[144:145], v0 offset:20488\n" \
  "v_mfma_f32_16x16x32_bf16 v[32:35], v[52:55], a[228:231], v[32:35]\n" \
  "ds_read_b64 v[146:147], v0 offset:23560\n" \
  "v_mfma_f32_16x16x32_bf16 v[36:39], v[76:79], a[228:231], v[36:39]\n" \
  "v_mfma_f32_16x16x32_bf16 v[32:35], v[56:59], a[232:235], v[32:35]\n" \
  "v_mfma_f32_16x16x32_bf16 v[36:39], v[80:83], a[232:235], v[36:39]\n" \
  "v_mfma_f32_16x16x32_bf16 v[32:35], v[60:63], a[236:239], v[32:35]\n" \
  "buffer_store_dwordx4 v[16:19], v3, s[36:39], s50 offen sc1\n" \
  "v_mfma_f32_16x16x32_bf16 v[36:39], v[84:87], a[236:239], v[36:39]\n" \
  "s_add_u32 s41, s41, 1\n" \
  "s_waitcnt lgkmcnt(0)\n" \
  "v_mfma_f32_16x16x32_bf16 v[24:27], v[100:103], a[0:3], 0\n" \
  "s_add_u32 s46, s41, 1\n" \
  "s_min_u32 s46, s46, 63\n" \
  "v_mad_u32_u24 v88, v8, s46, v7\n" \
  "v_mfma_f32_16x16x32_bf16 v[28:31], v[124:127], a[0:3], 0\n" \
  "v_mad_u32_u24 v89, v9, s46, v10\n" \
  "v_mfma_f32_16x16x32_bf16 v[24:27], v[104:107], a[4:7], v[24:27]\n" \
  "v_cvt_pk_bf16_f32 v20, v32, v33\n" \
  "v_cvt_pk_bf16_f32 v21, v34, v35\n" \
  "v_mfma_f32_16x16x32_bf16 v[28:31], v[128:131], a[4:7], v[28:31]\n" \
  "v_cvt_pk_bf16_f32 v22, v36, v37\n" \
  "v_cvt_pk_bf16_f32 v23, v38, v39\n" \
  "v_mfma_f32_16x16x32_bf16 v[24:27], v[108:111], a[8:11], v[24:27]\n" \
  "buffer_store_dwordx4 v[20:23], v3, s[36:39], s44 offen sc1\n" \
  "v_mfma_f32_16x16x32_bf16 v[28:31], v[132:135], a[8:11], v[28:31]\n" \
  "s_xor_b32 s50, s44, 0x2000\n" \
  "s_add_u32 s44, s44, 0x40000\n" \
  "v_mfma_f32_16x16x32_bf16 v[24:27], v[112:115], a[12:15], v[24:27]\n" \
  "v_readlane_b32 s43, v5, s41\n" \
  "v_mfma_f32_16x16x32_bf16 v[28:31], v[136:139], a[12:15], v[28:31]\n" \
  "buffer_load_dword v90, v88, s[36:39], 0 offen sc1\n" \
  "v_mfma_f32_16x16x32_bf16 v[24:27], v[116:119], a[16:19], v[24:27]\n" \
  "v_readlane_b32 s44, v6, s41\n" \
  "v_mfma_f32_16x16x32_bf16 v[28:31], v[140:143], a[16:19], v[28:31]\n" \
  "v_readlane_b32 s42, v4, s46\n" \
  "v_mfma_f32_16x16x32_bf16 v[24:27], v[120:123], a[20:23], v[24:27]\n" \
  "buffer_store_dwordx4 v[20:23], v3, s[36:39], s50 offen sc1\n" \
  "v_mfma_f32_16x16x32_bf16 v[28:31], v[144:147], a[20:23], v[28:31]\n" \
  "v_mfma_f32_16x16x32_bf16 v[32:35], v[100:103], a[24:27], 0\n" \
  "v_mfma_f32_16x16x32_bf16 v[36:39], v[124:127], a[24:27], 0\n" \
  "v_mfma_f32_16x16x32_bf16 v[32:35], v[104:107], a[28:31], v[32:35]\n" \
  "v_cvt_pk_bf16_f32 v16, v24, v25\n" \
  "v_cvt_pk_bf16_f32 v17, v26, v27\n" \
  "v_mfma_f32_16x16x32_bf16 v[36:39], v[128:131], a[28:31], v[36:39]\n" \
  "v_cvt_pk_bf16_f32 v18, v28, v29\n" \
  "v_cvt_pk_bf16_f32 v19, v30, v31\n" \
  "v_mfma_f32_16x16x32_bf16 v[32:35], v[108:111], a[32:35], v[32:35]\n" \
  "buffer_store_dwordx4 v[16:19], v2, s[36:39], s43 offen sc1\n" \
  "v_mfma_f32_16x16x32_bf16 v[36:39], v[132:135], a[32:35], v[36:39]\n" \
  "s_xor_b32 s50, s43, 0x2000\n" \
  "s_add_u32 s43, s43, 0x40000\n" \
  "v_mfma_f32_16x16x32_bf16 v[32:35], v[112:115], a[36:39], v[32:35]\n" \
  "v_mfma_f32_16x16x32_bf16 v[36:39], v[136:139], a[36:39], v[36:39]\n" \
  "v_mfma_f32_16x16x32_bf16 v[32:35], v[116:119], a[40:43], v[32:35]\n" \
  "v_mfma_f32_16x16x32_bf16 v[36:39], v[140:143], a[40:43], v[36:39]\n" \
  "v_mfma_f32_16x16x32_bf16 v[32:35], v[120:123], a[44:47], v[32:35]\n" \
  "buffer_store_dwordx4 v[16:19], v2, s[36:39], s50 offen sc1\n" \
  "v_mfma_f32_16x16x32_bf16 v[36:39], v[144:147], a[44:47], v[36:39]\n" \
  "v_mfma_f32_16x16x32_bf16 v[24:27], v[100:103], a[48:51], 0\n" \
  "v_mfma_f32_16x16x32_bf16 v[28:31], v[124:127], a[48:51], 0\n" \
  "v_mfma_f32_16x16x32_bf16 v[24:27], v[104:107], a[52:55], v[24:27]\n" \
  "v_cvt_pk_bf16_f32 v20, v32, v33\n" \
  "v_cvt_pk_bf16_f32 v21, v34, v35\n" \
  "v_mfma_f32_16x16x32_bf16 v[28:31], v[128:131], a[52:55], v[28:31]\n" \
  "v_cvt_pk_bf16_f32 v22, v36, v37\n" \
  "v_cvt_pk_bf16_f32 v23, v38, v39\n" \
  "v_mfma_f32_16x16x32_bf16 v[24:27], v[108:111], a[56:59], v[24:27]\n" \
  "buffer_store_dwordx4 v[20:23], v2, s[36:39], s43 offen sc1\n" \
  "v_mfma_f32_16x16x32_bf16 v[28:31], v[132:135], a[56:59], v[28:31]\n" \
  "s_xor_b32 s50, s43, 0x2000\n" \
  "s_add_u32 s43, s43, 0x40000\n" \
  "v_mfma_f32_16x16x32_bf16 v[24:27], v[112:115], a[60:63], v[24:27]\n" \
  "v_mfma_f32_16x16x32_bf16 v[28:31], v[136:139], a[60:63], v[28:31]\n" \
  "v_mfma_f32_16x16x32_bf16 v[24:27], v[116:119], a[64:67], v[24:27]\n" \
  "v_mfma_f32_16x16x32_bf16 v[28:31], v[140:143], a[64:67], v[28:31]\n" \
  "v_mfma_f32_16x16x32_bf16 v[24:27], v[120:123], a[68:71], v[24:27]\n" \
  "buffer_store_dwordx4 v[20:23], v2, s[36:39], s50 offen sc1\n" \
  "v_mfma_f32_16x16x32_bf16 v[28:31], v[144:147], a[68:71], v[28:31]\n" \
  "v_mfma_f32_16x16x32_bf16 v[32:35], v[100:103], a[72:75], 0\n" \
  "v_mfma_f32_16x16x32_bf16 v[36:39], v[124:127], a[72:75], 0\n" \
  "v_mfma_f32_16x16x32_bf16 v[32:35], v[104:107], a[76:79], v[32:35]\n" \
  "v_cvt_pk_bf16_f32 v16, v24, v25\n" \
  "v_cvt_pk_bf16_f32 v17, v26, v27\n" \
  "v_mfma_f32_16x16x32_bf16 v[36:39], v[128:131], a[76:79], v[36:39]\n" \
  "v_cvt_pk_bf16_f32 v18, v28, v29\n" \
  "v_cvt_pk_bf16_f32 v19, v30, v31\n" \
  "v_mfma_f32_16x16x32_bf16 v[32:35], v[108:111], a[80:83], v[32:35]\n" \
  "buffer_store_dwordx4 v[16:19], v2, s[36:39], s43 offen sc1\n" \
  "v_mfma_f32_16x16x32_bf16 v[36:39], v[132:135], a[80:83], v[36:39]\n" \
  "s_xor_b32 s50, s43, 0x2000\n" \
  "s_add_u32 s43, s43, 0x40000\n" \
  "v_mfma_f32_16x16x32_bf16 v[32:35], v[112:115], a[84:87], v[32:35]\n" \
  "v_mfma_f32_16x16x32_bf16 v[36:39], v[136:139], a[84:87], v[36:39]\n" \
  "v_mfma_f32_16x16x32_bf16 v[32:35], v[116:119], a[88:91], v[32:35]\n" \
  "v_mfma_f32_16x16x32_bf16 v[36:39], v[140:143], a[88:91], v[36:39]\n" \
  "v_mfma_f32_16x16x32_bf16 v[32:35], v[120:123], a[92:95], v[32:35]\n" \
  "buffer_store_dwordx4 v[16:19], v2, s[36:39], s50 offen sc1\n" \
  "v_mfma_f32_16x16x32_bf16 v[36:39], v[144:147], a[92:95], v[36:39]\n" \
  "v_mfma_f32_16x16x32_bf16 v[24:27], v[100:103], a[96:99], 0\n" \
  "s_waitcnt vmcnt(6)\n" \
  "s_barrier\n" \
  "v_cmp_gt_u32 vcc, v89, v90\n" \
  "v_mfma_f32_16x16x32_bf16 v[28:31], v[124:127], a[96:99], 0\n" \
  "buffer_atomic_add v91, v11, s[36:39], s47 offen\n" \
  "v_mfma_f32_16x16x32_bf16 v[24:27], v[104:107], a[100:103], v[24:27]\n" \
  "v_cvt_pk_bf16_f32 v20, v32, v33\n" \
  "v_cvt_pk_bf16_f32 v21, v34, v35\n" \
  "v_mfma_f32_16x16x32_bf16 v[28:31], v[128:131], a[100:103], v[28:31]\n" \
  "v_cvt_pk_bf16_f32 v22, v36, v37\n" \
  "v_cvt_pk_bf16_f32 v23, v38, v39\n" \
  "v_mfma_f32_16x16x32_bf16 v[24:27], v[108:111], a[104:107], v[24:27]\n" \
  "buffer_store_dwordx4 v[20:23], v2, s[36:39], s43 offen sc1\n" \
  "v_mfma_f32_16x16x32_bf16 v[28:31], v[132:135], a[104:107], v[28:31]\n" \
  "s_xor_b32 s50, s43, 0x2000\n" \
  "s_add_u32 s43, s43, 0x40000\n" \
  "v_mfma_f32_16x16x32_bf16 v[24:27], v[112:115], a[108:111], v[24:27]\n" \
  "s_cbranch_vccnz LP1_%=\n" \
  "LK1_%=:\n" \
  "s_add_u32 m0, s40, 0\n" \
  "v_mfma_f32_16x16x32_bf16 v[28:31], v[136:139], a[108:111], v[28:31]\n" \
  "buffer_load_dwordx4 v1, s[36:39], s42 offen offset:0 sc1 lds\n" \
  "v_mfma_f32_16x16x32_bf16 v[24:27], v[116:119], a[112:115], v[24:27]\n" \
  "v_mfma_f32_16x16x32_bf16 v[28:31], v[140:143], a[112:115], v[28:31]\n" \
  "buffer_load_dwordx4 v1, s[36:39], s42 offen offset:1024 sc1 lds\n" \
  "v_mfma_f32_16x16x32_bf16 v[24:27], v[120:123], a[116:119], v[24:27]\n" \
  "buffer_store_dwordx4 v[20:23], v2, s[36:39], s50 offen sc1\n" \
  "v_mfma_f32_16x16x32_bf16 v[28:31], v[144:147], a[116:119], v[28:31]\n" \
  "buffer_load_dwordx4 v1, s[36:39], s42 offen offset:2048 sc1 lds\n" \
  "v_mfma_f32_16x16x32_bf16 v[32:35], v[100:103], a[120:123], 0\n" \
  "v_mfma_f32_16x16x32_bf16 v[36:39], v[124:127], a[120:123], 0\n" \
  "buffer_atomic_add v91, v12, s[36:39], s49 offen\n" \
  "v_mfma_f32_16x16x32_bf16 v[32:35], v[104:107], a[124:127], v[32:35]\n" \
  "v_cvt_pk_bf16_f32 v16, v24, v25\n" \
  "v_cvt_pk_bf16_f32 v17, v26, v27\n" \
  "v_mfma_f32_16x16x32_bf16 v[36:39], v[128:131], a[124:127], v[36:39]\n" \
  "v_cvt_pk_bf16_f32 v18, v28, v29\n" \
  "v_cvt_pk_bf16_f32 v19, v30, v31\n" \
  "v_mfma_f32_16x16x32_bf16 v[32:35], v[108:111], a[128:131], v[32:35]\n" \
  "buffer_store_dwordx4 v[16:19], v2, s[36:39], s43 offen sc1\n" \
  "v_mfma_f32_16x16x32_bf16 v[36:39], v[132:135], a[128:131], v[36:39]\n" \
  "s_xor_b32 s50, s43, 0x2000\n" \
  "s_add_u32 s43, s43, 0x40000\n" \
  "v_mfma_f32_16x16x32_bf16 v[32:35], v[112:115], a[132:135], v[32:35]\n" \
  "s_mov_b32 s49, 0\n" \
  "s_lshr_b32 s45, s41, 4\n" \
  "s_lshl_b32 s45, s45, 13\n" \
  "v_mfma_f32_16x16x32_bf16 v[36:39], v[136:139], a[132:135], v[36:39]\n" \
  "s_lshl_b32 s47, s41, 9\n" \
  "s_add_u32 s47, s47, s45\n" \
  "v_mfma_f32_16x16x32_bf16 v[32:35], v[116:119], a[136:139], v[32:35]\n" \
  "v_mfma_f32_16x16x32_bf16 v[36:39], v[140:143], a[136:139], v[36:39]\n" \
  "v_mfma_f32_16x16x32_bf16 v[32:35], v[120:123], a[140:143], v[32:35]\n" \
  "buffer_store_dwordx4 v[16:19], v2, s[36:39], s50 offen sc1\n" \
  "v_mfma_f32_16x16x32_bf16 v[36:39], v[144:147], a[140:143], v[36:39]\n" \
  "v_mfma_f32_16x16x32_bf16 v[24:27], v[100:103], a[144:147], 0\n" \
  "v_mfma_f32_16x16x32_bf16 v[28:31], v[124:127], a[144:147], 0\n" \
  "v_mfma_f32_16x16x32_bf16 v[24:27], v[104:107], a[148:151], v[24:27]\n" \
  "v_cvt_pk_bf16_f32 v20, v32, v33\n" \
  "v_cvt_pk_bf16_f32 v21, v34, v35\n" \
  "v_mfma_f32_16x16x32_bf16 v[28:31], v[128:131], a[148:151], v[28:31]\n" \
  "v_cvt_pk_bf16_f32 v22, v36, v37\n" \
  "v_cvt_pk_bf16_f32 v23, v38, v39\n" \
  "v_mfma_f32_16x16x32_bf16 v[24:27], v[108:111], a[152:155], v[24:27]\n" \
  "buffer_store_dwordx4 v[20:23], v2, s[36:39], s43 offen sc1\n" \
  "v_mfma_f32_16x16x32_bf16 v[28:31], v[132:135], a[152:155], v[28:31]\n" \
  "s_xor_b32 s50, s43, 0x2000\n" \
  "s_add_u32 s43, s43, 0x40000\n" \
  "v_mfma_f32_16x16x32_bf16 v[24:27], v[112:115], a[156:159], v[24:27]\n" \
  "v_mfma_f32_16x16x32_bf16 v[28:31], v[136:139], a[156:159], v[28:31]\n" \
  "v_mfma_f32_16x16x32_bf16 v[24:27], v[116:119], a[160:163], v[24:27]\n" \
  "v_mfma_f32_16x16x32_bf16 v[28:31], v[140:143], a[160:163], v[28:31]\n" \
  "v_mfma_f32_16x16x32_bf16 v[24:27], v[120:123], a[164:167], v[24:27]\n" \
  "buffer_store_dwordx4 v[20:23], v2, s[36:39], s50 offen sc1\n" \
  "v_mfma_f32_16x16x32_bf16 v[28:31], v[144:147], a[164:167], v[28:31]\n" \
  "v_mfma_f32_16x16x32_bf16 v[32:35], v[100:103], a[168:171], 0\n" \
  "v_mfma_f32_16x16x32_bf16 v[36:39], v[124:127], a[168:171], 0\n" \
  "v_mfma_f32_16x16x32_bf16 v[32:35], v[104:107], a[172:175], v[32:35]\n" \
  "v_cvt_pk_bf16_f32 v16, v24, v25\n" \
  "v_cvt_pk_bf16_f32 v17, v26, v27\n" \
  "v_mfma_f32_16x16x32_bf16 v[36:39], v[128:131], a[172:175], v[36:39]\n" \
  "v_cvt_pk_bf16_f32 v18, v28, v29\n" \
  "v_cvt_pk_bf16_f32 v19, v30, v31\n" \
  "v_mfma_f32_16x16x32_bf16 v[32:35], v[108:111], a[176:179], v[32:35]\n" \
  "buffer_store_dwordx4 v[16:19], v2, s[36:39], s43 offen sc1\n" \
  "v_mfma_f32_16x16x32_bf16 v[36:39], v[132:135], a[176:179], v[36:39]\n" \
  "s_xor_b32 s50, s43, 0x2000\n" \
  "s_add_u32 s43, s43, 0x40000\n" \
  "v_mfma_f32_16x16x32_bf16 v[32:35], v[112:115], a[180:183], v[32:35]\n" \
  "v_mfma_f32_16x16x32_bf16 v[36:39], v[136:139], a[180:183], v[36:39]\n" \
  "v_mfma_f32_16x16x32_bf16 v[32:35], v[116:119], a[184:187], v[32:35]\n" \
  "v_mfma_f32_16x16x32_bf16 v[36:39], v[140:143], a[184:187], v[36:39]\n" \
  "v_mfma_f32_16x16x32_bf16 v[32:35], v[120:123], a[188:191], v[32:35]\n" \
  "buffer_store_dwordx4 v[16:19], v2, s[36:39], s50 offen sc1\n" \
  "v_mfma_f32_16x16x32_bf16 v[36:39], v[144:147], a[188:191], v[36:39]\n" \
  "v_mfma_f32_16x16x32_bf16 v[24:27], v[100:103], a[192:195], 0\n" \
  "v_mfma_f32_16x16x32_bf16 v[28:31], v[124:127], a[192:195], 0\n" \
  "v_mfma_f32_16x16x32_bf16 v[24:27], v[104:107], a[196:199], v[24:27]\n" \
  "v_cvt_pk_bf16_f32 v20, v32, v33\n" \
  "v_cvt_pk_bf16_f32 v21, v34, v35\n" \
  "v_mfma_f32_16x16x32_bf16 v[28:31], v[128:131], a[196:199], v[28:31]\n" \
  "v_cvt_pk_bf16_f32 v22, v36, v37\n" \
  "v_cvt_pk_bf16_f32 v23, v38, v39\n" \
  "v_mfma_f32_16x16x32_bf16 v[24:27], v[108:111], a[200:203], v[24:27]\n" \
  "buffer_store_dwordx4 v[20:23], v2, s[36:39], s43 offen sc1\n" \
  "v_mfma_f32_16x16x32_bf16 v[28:31], v[132:135], a[200:203], v[28:31]\n" \
  "s_xor_b32 s50, s43, 0x2000\n" \
  "s_add_u32 s43, s43, 0x40000\n" \
  "v_mfma_f32_16x16x32_bf16 v[24:27], v[112:115], a[204:207], v[24:27]\n" \
  "s_waitcnt vmcnt(8)\n" \
  "s_barrier\n" \
  "v_mfma_f32_16x16x32_bf16 v[28:31], v[136:139], a[204:207], v[28:31]\n" \
  "ds_read_b128 v[40:43], v0 offset:0\n" \
  "ds_read_b128 v[44:47], v0 offset:3072\n" \
  "v_mfma_f32_16x16x32_bf16 v[24:27], v[116:119], a[208:211], v[24:27]\n" \
  "ds_read_b128 v[48:51], v0 offset:6144\n" \
  "ds_read_b128 v[52:55], v0 offset:9216\n" \
  "v_mfma_f32_16x16x32_bf16 v[28:31], v[140:143], a[208:211], v[28:31]\n" \
  "ds_read_b64 v[56:57], v0 offset:2048\n" \
  "ds_read_b64 v[58:59], v0 offset:5120\n" \
  "v_mfma_f32_16x16x32_bf16 v[24:27], v[120:123], a[212:215], v[24:27]\n" \
  "buffer_store_dwordx4 v[20:23], v2, s[36:39], s50 offen sc1\n" \
  "ds_read_b64 v[60:61], v0 offset:8192\n" \
  "v_mfma_f32_16x16x32_bf16 v[28:31], v[144:147], a[212:215], v[28:31]\n" \
  "ds_read_b64 v[62:63], v0 offset:11264\n" \
  "ds_read_b128 v[64:67], v0 offset:1024\n" \
  "v_mfma_f32_16x16x32_bf16 v[32:35], v[100:103], a[216:219], 0\n" \
  "ds_read_b128 v[68:71], v0 offset:4096\n" \
  "ds_read_b128 v[72:75], v0 offset:7168\n" \
  "v_mfma_f32_16x16x32_bf16 v[36:39], v[124:127], a[216:219], 0\n" \
  "ds_read_b128 v[76:79], v0 offset:10240\n" \
  "ds_read_b64 v[80:81], v0 offset:2056\n" \
  "v_mfma_f32_16x16x32_bf16 v[32:35], v[104:107], a[220:223], v[32:35]\n" \
  "v_cvt_pk_bf16_f32 v16, v24, v25\n" \
  "v_cvt_pk_bf16_f32 v17, v26, v27\n" \
  "v_mfma_f32_16x16x32_bf16 v[36:39], v[128:131], a[220:223], v[36:39]\n" \
  "v_cvt_pk_bf16_f32 v18, v28, v29\n" \
  "v_cvt_pk_bf16_f32 v19, v30, v31\n" \
  "v_mfma_f32_16x16x32_bf16 v[32:35], v[108:111], a[224:227], v[32:35]\n" \
  "buffer_store_dwordx4 v[16:19], v3, s[36:39], s44 offen sc1\n" \
  "ds_read_b64 v[82:83], v0 offset:5128\n" \
  "v_mfma_f32_16x16x32_bf16 v[36:39], v[132:135], a[224:227], v[36:39]\n" \
  "s_xor_b32 s50, s44, 0x2000\n" \
  "s_add_u32 s44, s44, 0x40000\n" \
  "ds_read_b64 v[84:85], v0 offset:8200\n" \
  "v_mfma_f32_16x16x32_bf16 v[32:35], v[112:115], a[228:231], v[32:35]\n" \
  "ds_read_b64 v[86:87], v0 offset:11272\n" \
  "v_mfma_f32_16x16x32_bf16 v[36:39], v[136:139], a[228:231], v[36:39]\n" \
  "v_mfma_f32_16x16x32_bf16 v[32:35], v[116:119], a[232:235], v[32:35]\n" \
  "v_mfma_f32_16x16x32_bf16 v[36:39], v[140:143], a[232:235], v[36:39]\n" \
  "v_mfma_f32_16x16x32_bf16 v[32:35], v[120:123], a[236:239], v[32:35]\n" \
  "buffer_store_dwordx4 v[16:19], v3, s[36:39], s50 offen sc1\n" \
  "v_mfma_f32_16x16x32_bf16 v[36:39], v[144:147], a[236:239], v[36:39]\n" \
  "s_add_u32 s41, s41, 1\n" \
  "s_waitcnt lgkmcnt(0)\n" \
  "s_cmp_lt_u32 s41, 64\n" \
  "s_cbranch_scc1 LL_%=\n" \
  "s_nop 7\n" \
  "v_cvt_pk_bf16_f32 v20, v32, v33\n" \
  "v_cvt_pk_bf16_f32 v21, v34, v35\n" \
  "v_cvt_pk_bf16_f32 v22, v36, v37\n" \
  "v_cvt_pk_bf16_f32 v23, v38, v39\n" \
  "buffer_store_dwordx4 v[20:23], v3, s[36:39], s44 offen sc1\n" \
  "s_xor_b32 s50, s44, 0x2000\n" \
  "s_add_u32 s44, s44, 0x40000\n" \
  "buffer_store_dwordx4 v[20:23], v3, s[36:39], s50 offen sc1\n" \
  "s_waitcnt vmcnt(0)\n" \
  "s_barrier\n" \
  "buffer_atomic_add v91, v11, s[36:39], s47 offen\n" \
  "buffer_atomic_add v91, v12, s[36:39], s49 offen\n" \
  "s_branch LX_%=\n" \
  "LPS_%=:\n" \
  "s_add_u32 s48, s48, 1\n" \
  "s_cmp_gt_u32 s48, 262144\n" \
  "s_cbranch_scc1 LKS_%=\n" \
  "s_sleep 4\n" \
  "buffer_load_dword v90, v88, s[36:39], 0 offen sc1\n" \
  "s_waitcnt vmcnt(0)\n" \
  "v_cmp_gt_u32 vcc, v89, v90\n" \
  "s_cbranch_vccnz LPS_%=\n" \
  "s_branch LKS_%=\n" \
  "LP0_%=:\n" \
  "s_add_u32 s48, s48, 1\n" \
  "s_cmp_gt_u32 s48, 262144\n" \
  "s_cbranch_scc1 LK0_%=\n" \
  "s_sleep 4\n" \
  "buffer_load_dword v90, v88, s[36:39], 0 offen sc1\n" \
  "s_waitcnt vmcnt(0)\n" \
  "v_cmp_gt_u32 vcc, v89, v90\n" \
  "s_cbranch_vccnz LP0_%=\n" \
  "s_branch LK0_%=\n" \
  "LP1_%=:\n" \
  "s_add_u32 s48, s48, 1\n" \
  "s_cmp_gt_u32 s48, 262144\n" \
  "s_cbranch_scc1 LK1_%=\n" \
  "s_sleep 4\n" \
  "buffer_load_dword v90, v88, s[36:39], 0 offen sc1\n" \
  "s_waitcnt vmcnt(0)\n" \
  "v_cmp_gt_u32 vcc, v89, v90\n" \
  "s_cbranch_vccnz LP1_%=\n" \
  "s_branch LK1_%=\n" \
  "LX_%=:\n"

__device__ __forceinline__ int fresh_lane() {
  int ln;
  asm volatile("v_mbcnt_lo_u32_b32 %0, -1, 0\n\tv_mbcnt_hi_u32_b32 %0, -1, %0" : "=v"(ln));
  __builtin_assume(ln >= 0 && ln < 64);
  return ln;
}
__device__ __forceinline__ unsigned now_ticks() { return (unsigned)__builtin_amdgcn_s_memrealtime(); }
template <int SLEEP>
__device__ __forceinline__ void wait_ge(gu32* ctrl, long word, unsigned need) {
#pragma unroll 1
  for (int spin = 0;; ++spin) {
    const unsigned x = __hip_atomic_load(ctrl + word, RLX_AGENT);
    if (__builtin_amdgcn_ballot_w64(x < need) == 0) return;
    if (spin > SPIN_CAP || ((spin & 1023) == 1023 && rfl(__hip_atomic_load(ctrl + C_ERR, RLX_AGENT)))) { __hip_atomic_store(ctrl + C_ERR, 1u, RLX_AGENT); return; }
    __builtin_amdgcn_s_sleep(SLEEP);
  }
}
__device__ __forceinline__ void wait_tag(gu32* ctrl, int word, unsigned tag) {
#pragma unroll 1
  for (int spin = 0; spin <= SPIN_CAP; ++spin) {
    if (__builtin_amdgcn_ballot_w64(__hip_atomic_load(ctrl + word, RLX_AGENT) != tag) == 0) return;
    __builtin_amdgcn_s_sleep(3);
  }
}
__device__ __forceinline__ unsigned long long wait_init(gu32* ctrl, unsigned tag, int lane) {
  unsigned x;
#pragma unroll 1
  for (int spin = 0;; ++spin) {
    x = __hip_atomic_load(ctrl + C_INIT + (lane & 31) * 32, RLX_AGENT);
    if (__builtin_amdgcn_ballot_w64((x & ~3u) != tag) == 0 || spin > SPIN_CAP) break;
    __builtin_amdgcn_s_sleep(4);
  }
  const unsigned long long m = __builtin_amdgcn_ballot_w64((x & (1u + (lane >> 5))) != 0), alt = 0x5555555555555555ull;
  const int d = __builtin_popcountll(m ^ alt);
  return d <= 26 ? alt : d >= 38 ? ~alt : m;
}
template <typename VT>
__device__ __forceinline__ void set_rsrc(VT& sr, int i, const void* ptr, unsigned bytes) {
  const unsigned long long a = uni64((unsigned long long)ptr);
  sr[4 * i] = (unsigned)a; sr[4 * i + 1] = (unsigned)(a >> 32) & 0xffffu; sr[4 * i + 2] = bytes; sr[4 * i + 3] = 0x00020000u;
}
__device__ __forceinline__ int so_near(unsigned long long par, int blk) { return (blk << 21) | ((int)(par >> (blk & 63) & 1) << 13); }
__device__ __forceinline__ int so_hand(unsigned long long par, int s) { return so_near(par, HB + (s >> 1)) + (s & 1) * 0x100000; }
__device__ __forceinline__ int so_far(unsigned long long par, int blk) { return so_near(par, blk) ^ 0x2000; }
__device__ __forceinline__ long nword(unsigned long long par, int off) { return ((long)so_near(par, XB) + lmap(off) - (long)CTRL_OFF) / 4; }
__device__ __forceinline__ v4u ld16s(rsrc_t r, int vo, int so) { return __builtin_amdgcn_raw_buffer_load_b128(r, vo, so, 16); }
__device__ __forceinline__ v4u ld16n(rsrc_t r, int vo, int so) { return __builtin_amdgcn_raw_buffer_load_b128(r, vo, so, TPOL); }
__device__ __forceinline__ v2u ld8s(rsrc_t r, int vo, int so) { return __builtin_amdgcn_raw_buffer_load_b64(r, vo, so, 16); }
__device__ __forceinline__ void st16s(v4u x, rsrc_t r, int vo, int so) { __builtin_amdgcn_raw_buffer_store_b128(x, r, vo, so, 16); }
#define TS(cond, i)
#define OPAQUE(v) asm volatile("" : "=v"(v))
#define W240_IN(w) "{a[0:31]}"(w[0]), "{a[32:63]}"(w[1]), "{a[64:95]}"(w[2]), "{a[96:127]}"(w[3]), "{a[128:159]}"(w[4]), "{a[160:191]}"(w[5]), \
                   "{a[192:223]}"(w[6]), "{a[224:239]}"(w##7)
#define W240_SET(w, i, k, val) do { if ((i) < 224) w[(i) >> 5][((i) & 31) + (k)] = (val); else w##7[(i) - 224 + (k)] = (val); } while (0)

__device__ __forceinline__ unsigned wave_max_u32(unsigned m) {
  m = max(m, (unsigned)__builtin_amdgcn_update_dpp(0, (int)m, 0xb1, 0xf, 0xf, true)); m = max(m, (unsigned)__builtin_amdgcn_update_dpp(0, (int)m, 0x4e, 0xf, 0xf, true));
  m = max(m, (unsigned)__builtin_amdgcn_update_dpp(0, (int)m, 0x124, 0xf, 0xf, true)); m = max(m, ror8u(m));
  auto a = __builtin_amdgcn_permlane16_swap(m, m, false, false); m = max(a[0], a[1]);
  auto b = __builtin_amdgcn_permlane32_swap(m, m, false, false); return max(b[0], b[1]);
}
__device__ __forceinline__ void select_rows(gu32* ctrl, rsrc_t rT, int lane) {
  unsigned key[32];
  v4u rr[8];
#pragma unroll
  for (int i = 0; i < 8; ++i) rr[i] = ld16s(rT, lane * 16 + i * 1024, 2 * (int)HF_BYTES);
  __builtin_amdgcn_sched_barrier(0);
#pragma unroll
  for (int i = 0; i < 8; ++i) {
    const v4u r = rr[i];
#pragma unroll
    for (int k = 0; k < 4; ++k) {
      unsigned u = f2u(u2f(r[k]) + 0.f);
      u = (u & 0x80000000u) ? ~u : (u | 0x80000000u);
      key[4 * i + k] = (u & 0xffff0000u) | (unsigned)(S - 1 - (i * 256 + lane * 4 + k));
    }
  }
  unsigned mine = 0, bound = 0;
#pragma unroll 1
  for (int it = 0; it < TOPK; ++it) {
    unsigned m = 0;
#pragma unroll
    for (int i = 0; i < 32; ++i) m = max(m, key[i] - bound);
    bound += wave_max_u32(m);
    if ((lane & 7) == it) mine = S - (bound & 0xffffu);
  }
#pragma unroll
  for (int i = 0; i < 2; ++i) __hip_atomic_store(ctrl + C_IDX + ((lane >> 3) + 8 * i) * 32 + (lane & 7), mine, RLX_AGENT);
}

__device__ __forceinline__ void ffn_role(const Params& p, int role, int lane, int wave) {
  gu32* ctrl = (gu32*)(p.ws + CTRL_OFF);
  const int g = lane >> 4, n = lane & 15, q = (lane >> 2) & 3, pp = lane & 3;
  const bool up = role < 96, down = role >= 88;
  const int c = role - 88;
  v4u wg[5], wu[5], wd[12];
  {
    const rsrc_t rg = mkrsrc(p.wg, DM * FF * 2), ru = mkrsrc(p.wu, DM * FF * 2), rd = mkrsrc(p.wd, FF * DM * 2);
#pragma unroll
    for (int i = 0; i < 5; ++i) {
      const int vo = up ? ((wave * 160 + i * 32 + (lane >> 1)) * FF + role * 16) * 2 + (lane & 1) * 16 : OOB;
      dma16(rg, vo, 0, wave * 22528 + i * 1024); dma16(ru, vo, 0, wave * 22528 + 5120 + i * 1024);
    }
#pragma unroll
    for (int i = 0; i < 12; ++i) dma16(rd, down ? ((wave * 384 + i * 32 + (lane >> 1)) * DM + c * 16) * 2 + (lane & 1) * 16 : OOB, 0, wave * 22528 + 10240 + i * 1024);
    VMCNT(0);
    const unsigned a = wave * 22528 + (8 * g + q) * 32 + pp * 8;
#pragma unroll
    for (int i = 0; i < 22; ++i) {
      const v2u lo = trread(a + i * 1024), hi = trread(a + i * 1024 + 128);
      const v4u x = {lo[0], lo[1], hi[0], hi[1]};
      if (i < 5) wg[i] = x; else if (i < 10) wu[i - 5] = x; else wd[i - 10] = x;
    }
  }
  const rsrc_t rT = mkrsrc(p.ws + X1_OFF, (unsigned)(WS_END - X1_OFF));
  constexpr int SO_H2 = DM * 2, SO_R = 2 * (int)HF_BYTES, SO_ACT = (int)(ACT_OFF - X1_OFF), RED = 98304;
  if (wave == 0) {
    wait_ge<3>(ctrl, C_IDX + (role & 15) * 32 + (lane & 7), 1u);
    if (lane < 8) *(LAS unsigned*)(L_MISC + lane * 4) = __hip_atomic_load(ctrl + C_IDX + (role & 15) * 32 + lane, RLX_AGENT);
  }
  __syncthreads();
  const unsigned t0 = now_ticks();
  const int row = (*(LAS int*)(L_MISC + (n & 7) * 4) - 1) & (S - 1);
  v2u x1, h2, gb; unsigned rv;
  if (down && wave == 0) { { const v4u t = ld16s(mkrsrc(p.ws + CTRL_OFF + C_MOD * 4, 15360), (5 * DM + c * 16 + 4 * g) * 4, 0); gb = v2u{t[0], t[2]}; } x1 = ld8s(rT, (row * 2 * DM + c * 16 + 4 * g) * 2, 0); h2 = ld8s(rT, (row * 2 * DM + c * 16 + 4 * g) * 2, SO_H2); rv = __builtin_amdgcn_raw_buffer_load_b32(rT, row * 4, SO_R, 16); }
  if (up) {
    v4u xb[5];
#pragma unroll
    for (int i = 0; i < 5; ++i) xb[i] = ld16s(rT, (row * 2 * DM + wave * 160 + i * 32 + 8 * g) * 2, SO_H2);
    __builtin_amdgcn_sched_barrier(0);
    f4 ag = {}, au = {};
#pragma unroll
    for (int i = 0; i < 5; ++i) { ag = mfma32(wg[i], xb[i], ag); au = mfma32(wu[i], xb[i], au); }
    *(LAS f4*)(RED + wave * 2048 + lane * 16) = ag; *(LAS f4*)(RED + wave * 2048 + 1024 + lane * 16) = au;
    __syncthreads();
    if (wave == 0) {
#pragma unroll
      for (int w = 1; w < 4; ++w) { ag += *(LAS f4*)(RED + w * 2048 + lane * 16); au += *(LAS f4*)(RED + w * 2048 + 1024 + lane * 16); }
      float a[4];
#pragma unroll
      for (int r = 0; r < 4; ++r) { const float gs = rbf(ag[r]); a[r] = rbf(gs / (1.f + __expf(-gs))) * rbf(au[r]); }
      __builtin_amdgcn_raw_buffer_store_b64(v2u{unsent(pack2(a[0], a[1])), unsent(pack2(a[2], a[3]))}, rT, n < 8 ? role * 256 + (g >> 1) * 128 + n * 16 + (g & 1) * 8 : OOB, SO_ACT, 16);
    }
  }
  if (!down) return;
  while ((int)(now_ticks() - t0) < FFN_WAIT) __builtin_amdgcn_s_sleep(1);
  v4u ab[12];
  for (int spin = 0;; ++spin) {
    asm volatile("" ::: "memory");
    unsigned m = 0;
#pragma unroll
    for (int i = 0; i < 12; ++i) ab[i] = ld16s(rT, (wave * 12 + i) * 512 + g * 128 + (n & 7) * 16, SO_ACT);
    __builtin_amdgcn_sched_barrier(0);
#pragma unroll
    for (int i = 0; i < 12; ++i) m = max(max(m, max(ab[i][0], ab[i][1])), max(ab[i][2], ab[i][3]));
    if (__builtin_amdgcn_ballot_w64(m == ~0u) == 0) break;
    if (spin > SPIN_CAP) { __hip_atomic_store(ctrl + C_ERR, 1u, RLX_AGENT); break; }
    __builtin_amdgcn_s_sleep(4);
  }
  f4 y = {};
#pragma unroll
  for (int i = 0; i < 12; ++i) y = mfma32(wd[i], ab[i], y);
  *(LAS f4*)(RED + 8192 + wave * 1024 + lane * 16) = y;
  __syncthreads();
  if (wave == 0 && n < 8) {
#pragma unroll
    for (int w = 1; w < 4; ++w) y += *(LAS f4*)(RED + 8192 + w * 1024 + lane * 16);
    const float rs = rbf(1.f / (1.f + __expf(-u2f(rv))));
    float o[4];
#pragma unroll
    for (int r = 0; r < 4; ++r) {
      const float hv = (r & 1) ? hi2f(h2[r >> 1]) : lo2f(h2[r >> 1]), xv = (r & 1) ? hi2f(x1[r >> 1]) : lo2f(x1[r >> 1]);
      o[r] = xv + rbf(((r & 1) ? hi2f(gb[r >> 1]) : lo2f(gb[r >> 1])) * rbf(hv + rbf(rbf(y[r]) * rs)));
    }
    *(v2u*)(p.out + (size_t)row * DM + c * 16 + 4 * g) = v2u{pack2(o[0], o[1]), pack2(o[2], o[3])};
  }
}

constexpr int LP = 0, L_PR = 2560, L_MOD = 16384;
__device__ __forceinline__ v4u ln1_piece(const Params& p, int row0, int i, int lane) {
  return __builtin_amdgcn_raw_buffer_load_b128(mkrsrc(p.x, S * DM * 2), (row0 + (lane & 7)) * 1280 + (lane >> 3) * 16, i * 128, 0);
}
__device__ __forceinline__ void ln1_stats(const v4u (&a)[10], float& mb, float& isd) {
  float s = 0.f, q = 0.f;
  const bf2 one = {(__bf16)1.f, (__bf16)1.f};
#pragma unroll
  for (int i = 0; i < 10; ++i)
#pragma unroll
    for (int k = 0; k < 4; ++k) { const unsigned w = a[i][k]; s = __builtin_amdgcn_fdot2_f32_bf16(__builtin_bit_cast(bf2, w), one, s, false); }
  s += ror8f(s); s = swapsum16(s, s); s = swapsum32(s, s);
  const float mean = s * (1.f / DM);
#pragma unroll
  for (int i = 0; i < 10; ++i)
#pragma unroll
    for (int k = 0; k < 4; ++k) { const float d0 = lo2f(a[i][k]) - mean, d1 = hi2f(a[i][k]) - mean; q = fmaf(d0, d0, fmaf(d1, d1, q)); }
  q += ror8f(q); q = swapsum16(q, q); q = swapsum32(q, q);
  mb = rbf(mean); isd = __builtin_amdgcn_rcpf(rbf(__builtin_amdgcn_sqrtf(rbf(rbf(q * (1.f / DM)) + 1e-5f))));
}
__device__ __forceinline__ unsigned ln_norm(unsigned a, float mb, float isd) { const unsigned d = pack2(lo2f(a) - mb, hi2f(a) - mb); return pack2(lo2f(d) * isd, hi2f(d) * isd); }
__device__ __forceinline__ unsigned ln_mod(unsigned n, unsigned sc, unsigned sh) { const unsigned m = pack2(lo2f(n) * lo2f(sc), hi2f(n) * hi2f(sc)); return pack2(lo2f(m) + lo2f(sh), hi2f(m) + hi2f(sh)); }
__device__ __forceinline__ void ln1_store(const Params& p, v4u o, int row0, int i, int lane) {
  st16s(o, mkrsrc(p.ws + HF_OFF, (unsigned)HF_BYTES), (row0 >> 4) * 20480 + (lane >> 3) * 256 + ((row0 & 8) + (lane & 7)) * 16, i * 2048);
}
template <typename B>
__device__ __forceinline__ void mod_unit(const Params& p, int u, int tid, unsigned tag, B between) {
  const rsrc_t rw = mkrsrc(p.wmod, DM * 3840 * 2), rc = mkrsrc(p.c, DM * 2);
  LAS float* sv = (LAS float*)L_MOD; LAS float* red = sv + 768;
  u16 cv[3]; v4u w[20];
#pragma unroll
  for (int i = 0; i < 3; ++i) cv[i] = __builtin_amdgcn_raw_buffer_load_b16(rc, tid * 2, i * 512, 0);
#pragma unroll
  for (int i = 0; i < 20; ++i) w[i] = __builtin_amdgcn_raw_buffer_load_b128(rw, u < 60 ? (tid >> 3) * 7680 + u * 128 + (tid & 7) * 16 : OOB, i * 32 * 7680, 0);
  between();
  __builtin_amdgcn_sched_barrier(0);
#pragma unroll
  for (int i = 0; i < 3; ++i) { const float c = bf2f(cv[i]); sv[i * 256 + tid] = rbf(c / (1.f + __expf(-c))); }
  LGKM0(); BAR();
  float acc[8] = {};
#pragma unroll
  for (int i = 0; i < 20; ++i) {
    const float x = sv[i * 32 + (tid >> 3)];
#pragma unroll
    for (int k = 0; k < 4; ++k) { acc[2 * k] += x * lo2f(w[i][k]); acc[2 * k + 1] += x * hi2f(w[i][k]); }
  }
#pragma unroll
  for (int k = 0; k < 8; ++k) red[(tid >> 3) * 65 + (tid & 7) * 8 + k] = acc[k];
  LGKM0(); BAR();
  float sum = 0.f;
#pragma unroll
  for (int i = 0; i < 8; ++i) sum += red[((tid & 3) * 8 + i) * 65 + (tid >> 2)];
  sum += u2f(__builtin_amdgcn_update_dpp(0, (int)f2u(sum), 0xb1, 0xf, 0xf, true)); sum += u2f(__builtin_amdgcn_update_dpp(0, (int)f2u(sum), 0x4e, 0xf, 0xf, true));
  const unsigned y = pack2((u / 10) % 3 == 1 ? 1.f + rbf(sum) : sum, 0.f), z = (unsigned)__builtin_amdgcn_update_dpp(0, (int)y, 0x104, 0xf, 0xf, true);
  __builtin_amdgcn_raw_buffer_store_b64(v2u{y | z << 16, tag}, mkrsrc(p.ws + CTRL_OFF + C_MOD * 4, 15360), (tid & 7) == 0 ? u * 256 + tid : OOB, 0, 16);
  LGKM0(); BAR();
}
template <typename I, typename T>
__device__ __forceinline__ void stage4(I issue, T take) {
  issue(0, 0); issue(1, 1);
  VMCNT(15); BAR(); take(0, 0); LGKM0(); BAR(); issue(2, 0);
  VMCNT(15); BAR(); take(1, 1); LGKM0(); BAR(); issue(3, 1);
  VMCNT(15); BAR(); take(2, 0);
  VMCNT(0); BAR(); take(3, 1); LGKM0();
}

__device__ __forceinline__ void chain_role(const Params& p, int role, int lane, int w, unsigned tag) {
  const int ty = role % 3, h = role / 3, r = 2 * h + ty;
  const int g = lane >> 4, n = lane & 15, q = (lane >> 2) & 3, pp = lane & 3, par = n >> 3, i = n & 7, sel = q >> 1;
  constexpr int soX = XB << 21;
  v32u vc, vi;
  OPAQUE(vc); OPAQUE(vi);
#pragma unroll
  for (int k = 0; k < 10; ++k) {
    int o;
    if (ty == 1) { const int u = ((w & 1) * 8 + k) * 64 + lane, row = u >> 4; o = row * 12288 + ((u & 15) ^ (((row & 1) | ((row >> 1) & 2) | ((w >> 1) << 2)) << 1)) * 16; }
    else if (ty == 0 && w == 3) { const int u = k * 64 + lane, row = u >> 3; o = row * 12288 + 256 + ((u & 7) ^ (row & 4) ^ 2) * 16; }
    else { const int u = ((ty ? 6 : 8) * w + k) * 64 + lane, row = u / 24; o = row * 12288 + ((u % 24) ^ (row & 4)) * 16; }
    vi[k] = o + h * 384;
    const int u = (10 * w + k) * 64 + lane, row = u / 80;
    vi[10 + k] = (h * DH + row) * 1280 + ((u % 80) ^ (((row & 1) | ((row >> 1) & 2)) << 2)) * 16;
  }
  const int r0 = 4 * g + (q & 1), sw = (g & 1) << 6;
  if (ty == 0) {
    vi[20] = r0 * 384 + ((64 * w + 32 * sel + 8 * pp) ^ sw); vi[22] = 384;
    vi[21] = sel ? 24576 + r0 * 128 + ((32 * w + 8 * pp) ^ sw ^ 32) : r0 * 384 + ((256 + 32 * w + 8 * pp) ^ sw); vi[23] = sel ? 128 : 384;
  } else if (ty == 1) {
#pragma unroll
    for (int k = 0; k < 2; ++k) { vi[20 + k] = sel * 16384 + r0 * 256 + ((64 * w + 32 * k + 8 * pp) ^ (((q & 1) | ((g & 1) << 1) | (sel << 2)) << 5)); vi[22 + k] = 256; }
  } else {
    const int rv = 16 * (g >> 1) + 8 * sel + 4 * (g & 1) + (q & 1);
    vi[20] = rv * 384 + ((64 * w + 8 * pp) ^ sw); vi[21] = rv * 384 + ((256 + 32 * w + 8 * pp) ^ sw);
    vi[22] = r0 * 1280 + ((64 * w + 32 * sel + 8 * pp) ^ (((q & 1) | ((g & 1) << 1)) << 6));
  }
  vi[24] = (int)CTRL_OFF + (C_ANCH + 128) * 4;
  vi[25] = (int)CTRL_OFF + (C_ANCH + (role >> 5) * 32) * 4;
  vi[26] = lane < 4 ? (int)CTRL_OFF + (C_ANCH + lane * 32) * 4 : OOB;
  vi[27] = soX + lmap(N_PP) + pp * 128;
  vi[28] = w == 0 && lane == 0 ? vi[24] : OOB;
  vi[29] = (int)CTRL_OFF + (C_INIT + (lane & 31) * 32) * 4; vi[30] = 1 + (lane >> 5);
  vi[31] = lane == 0 ? soX + lmap(N_CF) + h * 128 + ty * 4 : OOB;
  vc[0] = n < 8 ? 0x4444 : 0xEEEE;
  const int tok = 16 * ((n >> 2) & 1) + 4 * (2 * w + par) + (n & 3);
  const int ko = h * 65536 + 3072 + (g * 16 + (tok & 15)) * 16;
  const int own = (role >> 5) * CPY;
  vc[1] = w == 2 ? lmap(own + M_OFF) + lane * 16 : ko + (tok >> 4) * 1024;
  vc[2] = w == 2 ? lmap(own + M_OFF) + 4096 + lane * 16 : ko + 2048 + (tok >> 4) * 8;
  vc[3] = (XP & 8) && w != 2 ? OOB : lmap(w == 2 ? own + M_OFF + 8192 : w == 3 ? V2_OFF + r * 4096 : r * 16384 + w * 8192) + lane * 16;
  vc[4] = lmap(P2_OFF + (16 * w + 8 * (g >> 1) + (n >> 1)) * 1024 + h * 32 + (n & 1) * 16 + (g & 1) * 8);
  vc[5] = L_MEM + (ty < 2 ? g * 128 + i * 16 : lane * 16);
  vc[6] = L_MEM + 8192 + (ty < 2 ? g : 16 * (g >> 1) + 2 * (g & 1) + par) * 32 + i * 4; vc[7] = vc[6] + 1024;
  const int kmo = lmap(h * 4096 + w * 1024);
  auto im4 = [](int u, int d) { return u * 128 + ((d ^ ((u & 2) << 2)) * 8); };
  if (ty == 1) {
    vc[8] = L_Q + w * 1024 + im4(g, n); vc[9] = vc[8] + 512;
    vc[10] = L_Q + im4(pp, 4 * g + q); vc[11] = vc[10] + 512; vc[12] = L_Q + w * 1024 + (g >> 1) * 512 + im4(pp, 8 * (g & 1) + q);
    vc[14] = par ? kmo + (g * 8 + i) * 16 : OOB;
  } else {
    vc[8] = L_Q + w * 512 + g * 128 + n * 8;
    vc[9] = ty ? L_Q + 2048 + w * 256 + (g & 1) * 128 + n * 8 + (g >> 1) * 4 : L_Q2 + w * 512 + im4(g, n);
    vc[10] = L_Q + (g >> 1) * 256 + (pp & 1) * 128 + (8 * (g & 1) + q) * 8;
    vc[11] = ty ? L_WO + w * 10240 + lane * 16 : L_Q2 + im4(w < 2 ? pp & 1 : pp, 4 * g + q);
    vc[12] = L_Q2 + w * 512 + im4(pp, 4 * g + q);
    vc[14] = par ? kmo + 512 + (g * 8 + i) * 8 : OOB;
  }
  vc[13] = ty == 2 ? lmap(P_OFF + ((4 * w + 2 * (g >> 1) + par) * 32 + h) * 128 + i * 16 + (g & 1) * 8)
         : ty == 0 && w < 2 ? lmap(E_OFF + h * 512 + (2 * w + (g >> 1)) * 128 + i * 16 + (g & 1) * 8 + par * 4)
         : g >= 2 && !par ? lmap(S_OFF + h * 256 + ty * 128 + i * 16 + (g - 2) * 8) : OOB;
  const int h0 = 8 * w + 4 * par;
  vc[15] = lmap(own + S_OFF + (h0 + g) * 256 + i * 16);
  vc[16] = lmap(own + E_OFF + h0 * 512 + g * 128 + i * 16);
  vc[17] = lmap(P_OFF + (r * 32 + h0 + g) * 128 + i * 16);
  vc[18] = i < 2 ? lmap(P2_OFF + r * 1024 + (h0 + g) * 32 + i * 16) : OOB;
  vc[19] = L_VO + h0 * 512 + g * 128 + i * 16;
  vc[20] = L_VO + 16384 + h0 * 128 + g * 32 + (i & 1) * 16;
  vc[21] = L_RED + w * 768 + (g & 1) * 256 + i * 16 + par * 8;
  vc[22] = L_RED + w * 768 + 512 + (g & 1) * 128 + i * 16 + par * 8;
  vc[23] = L_RED + min(lane, 47) * 16;
  vc[24] = lane < 32 ? lmap(M_OFF + r * 128 + i * 16 + (lane >> 3) * 4) : lane < 40 ? lmap(M_OFF + 8192 + tslot(r) * 32 + i * 4) : OOB;
  vc[25] = so_hand(0, lane); vc[26] = (3 * (lane % RING) + 2) << 21; vc[27] = (3 * (lane % RING) + (w != 3)) << 21;
  vc[28] = soX + lmap(N_PP) + pp * 128;
  vc[29] = soX + lmap(N_REC) + pp * 32; vc[30] = (lane == 0 && role < 3) || (lane == 1 + (role >> 5) && (role == 2 || role == 35 || role == 65)) ? vc[29] : OOB;
  vc[31] = lane == 0 ? (role * 4 + w) * 16 : OOB;
  v4u sh, s0, s1; v16u sc;
  set_rsrc(sh, 0, p.ws, (unsigned)HAND_BYTES + CTRL_BYTES);
  set_rsrc(s0, 0, ty == 2 ? p.wv : w >= 3 - ty ? p.wk : p.wq, DM * HD * 2); set_rsrc(s1, 0, p.wo, DM * HD * 2);
  sc[0] = ty * 4 + w; sc[1] = role; sc[2] = p.dB; sc[3] = p.dM; sc[4] = p.dS; sc[5] = p.lead; sc[6] = soX; sc[7] = (unsigned)HAND_BYTES + CTRL_BYTES;
  sc[8] = w * (ty == 2 ? 6144 : 8192); sc[9] = w * 10240; sc[10] = tag;
#pragma unroll
  for (int k = 0; k < 16; ++k) sc[k] = k < 11 ? rfl(sc[k]) : 0;
  asm volatile(CHAIN_ASM : "+{s[48:63]}"(sc), "+{v[0:31]}"(vc), "+{v[192:223]}"(vi), "+{s[36:39]}"(sh), "+{s[40:43]}"(s0), "+{s[44:47]}"(s1) :: "memory", "vcc", "scc", CHAIN_CLOBBERS);
  lane = fresh_lane();
  gu32* ct = (gu32*)(((unsigned long long)sh[0] | (unsigned long long)sh[1] << 32) + CTRL_OFF);
  if (lane == 0) {
    if (sc[12] > 20000u || sc[10] > 4000u || sc[11] > 4000u) __hip_atomic_store(ct + C_ERR, 1u, RLX_AGENT);
  }
}

__device__ __forceinline__ void load_wo(const Params& p, int h, int col0, int cstep, int lane, int wave, v32u (&wa)[7], v16u& wa7) {
  const int g = lane >> 4, q = (lane >> 2) & 3, pp = lane & 3;
  const rsrc_t ro = mkrsrc(p.wo, DM * HD * 2);
  stage4(
    [&](int m, int slot) {
#pragma unroll 1
      for (int i = 0; i < 15; ++i) {
        const int c = wave * 15 + i;
        dma16(ro, lane * 16, (h * DH + (c < 40 ? 32 * m : 96 + 16 * m)) * DM * 2 + c * 1024, slot * 61440 + c * 1024);
      }
    },
    [&](int m, int slot) {
#pragma unroll
      for (int c = 0; c < 10; ++c) {
        const unsigned col = slot * 61440 + (col0 + cstep * c + 4 * pp) * 2;
        const v2u lo = trread((8 * g + q) * 1280 + col), hi = trread((8 * g + 4 + q) * 1280 + col), tl = trread((32 + 4 * g + q) * 1280 + col);
        const int r = (c * 6 + m) * 4, r2 = (c * 6 + 4 + (m >> 1)) * 4 + 2 * (m & 1);
        W240_SET(wa, r, 0, lo[0]); W240_SET(wa, r, 1, lo[1]); W240_SET(wa, r, 2, hi[0]); W240_SET(wa, r, 3, hi[1]);
        W240_SET(wa, r2, 0, tl[0]); W240_SET(wa, r2, 1, tl[1]);
      }
    });
  BAR();
}

__device__ __forceinline__ void vo_role(const Params& p, int h, int lane, int wave, unsigned tag) {
  v32u wa[7]; v16u wa7;
  load_wo(p, h, 16 * wave, 64, lane, wave, wa, wa7);
  TS(lane == 0 && wave == 0, 256 + 96 + h)
  const unsigned long long parmask = wait_init((gu32*)(p.ws + CTRL_OFF), tag, lane);
  const int g = lane >> 4, n = lane & 15, b = 3 * (lane % RING);
  v16u vc; v4u sh;
  OPAQUE(vc);
  vc[0] = lane * 16; vc[1] = h * 65536 + wave * 16384 + lane * 16;
  vc[2] = (XP & 4) ? OOB : lmap((2 * wave + (n >> 3)) * 16384 + h * 512 + g * 128 + (n & 7) * 16);
  vc[3] = lmap(V2_OFF + (8 * wave + (n >> 1)) * 4096 + h * 128 + g * 32 + (n & 1) * 16);
  vc[4] = so_far(parmask, b + 2); vc[5] = so_near(parmask, b + 1); vc[6] = so_near(parmask, b);
  const bool cl = lane & 1;
  vc[7] = (int)CTRL_OFF + (cl ? C_CL : C_VP + h * 32) * 4;
  vc[8] = cl ? 128 : 0; vc[9] = !cl; vc[10] = cl ? 8 : 1;
  vc[11] = wave == 0 && lane == 0 ? so_near(parmask, XB) + lmap(N_PP) + (h & 3) * 128 : OOB;
  vc[12] = wave == 0 && lane == 0 ? (int)CTRL_OFF + (C_WP + h * 32) * 4 : OOB;
  set_rsrc(sh, 0, p.ws, (unsigned)HAND_BYTES + CTRL_BYTES);
  asm volatile(VO_ASM :: "{v[0:15]}"(vc), "{s[36:39]}"(sh), "{s40}"(wave * 3072), W240_IN(wa) : "memory", "vcc", "scc", VO_CLOBBERS);
  wait_ge<32>((gu32*)(p.ws + CTRL_OFF), nword(parmask, N_CF + h * 128 + 8), NSEG - 1);
  TS(fresh_lane() == 0 && wave == 0, 992 + h)
}

__device__ __forceinline__ void set_up(const Params& p, int j, int blk, int lane, int wave, unsigned tag) {
  const rsrc_t rH = mkrsrc(p.ws, (unsigned)HAND_BYTES + CTRL_BYTES);
  const int tid = wave * 64 + lane;
  if (wave < 2) {
    unsigned best[2] = {1u << 30, 1u << 30};
#pragma unroll 1
    for (int rep = 0; rep < CALREP; ++rep) {
      const int off = lane < 8 ? ((j + 32 * wave) << 21) + 0x1FC000 + (rep & 1) * 8192 + lane * 16 : OOB;
      const unsigned long long t1 = __builtin_amdgcn_s_memtime();
      LGKM0();
      st16s(v4u{1u, 2u, 3u, 4u}, rH, off, 0);
      VMCNT(0);
      const unsigned d = (unsigned)(__builtin_amdgcn_s_memtime() - t1);
      if (rep & 1) best[1] = min(best[1], d); else best[0] = min(best[0], d);
    }
    const unsigned x = xcc_id();
    if (lane == 0) *(LAS unsigned*)(L_MISC + wave * 4) = ((best[1] < best[0] ? 1u : 0u) ^ (x >> 2) ^ ((x + 1 - blk) >> 2)) & 1u;
  } else {
    const int t = tid - 128, i = j * 128 + t;
    const v4u z = {0u, 0u, 0u, 0u};
    const int k = j * 80 + t, row = k / 320, cp = k % 320;
    const unsigned x = unsent(__builtin_amdgcn_raw_buffer_load_b32(mkrsrc(p.mem0, 8 * DM * 2), k * 4, 0, 0));
    st16s(v4u{(unsigned)(i * 4 + C_Z0 - C_CL) < RING * 32u ? 8u : 0u, 0u, 0u, 0u}, rH, i < (C_Z1 - C_Z0) / 4 ? (int)CTRL_OFF + C_Z0 * 4 + i * 16 : OOB, 0);
#pragma unroll
    for (int u = 0; u < 2; ++u) st16s(z, rH, i + u * 4096 < (N_END - N_PP) / 8 ? (XB << 21) + 2 * N_PP + (i + u * 4096) * 16 : OOB, 0);
    st16s(v4u{~0u, ~0u, ~0u, ~0u}, mkrsrc(p.ws + ACT_OFF, TOPK * FF * 2), i * 16, 0);
    const int mo = t < 80 ? lmap(cp < 256 ? (cp >> 2) * 128 + row * 16 + (cp & 3) * 4 : 8192 + tslot(cp - 256) * 32 + row * 4) : OOB;
#pragma unroll
    for (int u = 0; u < 6; ++u) __builtin_amdgcn_raw_buffer_store_b32(x, rH, mo, (XB << 21) + (u >> 1) * 2 * CPY + (u & 1) * 8192, 16);
  }
  VMCNT(0); LGKM0(); BAR();
  if (tid == 0) __hip_atomic_store((gu32*)(p.ws + CTRL_OFF) + C_INIT + j * 32, tag | *(LAS unsigned*)L_MISC | *(LAS unsigned*)(L_MISC + 4) << 1, RLX_AGENT);
  TS(tid == 0, 768 + j)
  mod_unit(p, 28 + j, tid, tag, [] {});
}

__device__ __forceinline__ void proj_role(const Params& p, int type, int head, int blk, int lane, int wave, unsigned tag) {
  gu32* ctrl = (gu32*)(p.ws + CTRL_OFF);
  const int g = lane >> 4, q = (lane >> 2) & 3, pp = lane & 3;
  v32u wa[7]; v16u wa7;
  {
    if (type == 0) { set_up(p, head, blk, lane, wave, tag); __builtin_amdgcn_s_sleep(QSLEEP); }
    const rsrc_t rw = mkrsrc(type == 0 ? p.wq : type == 1 ? p.wk : p.wv, DM * HD * 2);
    stage4(
      [&](int m, int slot) {
#pragma unroll 1
        for (int i = 0; i < 15; ++i) {
          const int c = (wave * 15 + i) * 64 + lane;
          dma16(rw, ((m * 160 + c / 24) * HD + head * DH) * 2 + (c % 24) * 16, 0, slot * 61440 + (wave * 15 + i) * 1024);
        }
      },
      [&](int m, int slot) {
#pragma unroll
        for (int ks = 0; ks < 5; ++ks)
#pragma unroll
          for (int t = 0; t < 3; ++t) {
            const unsigned a = slot * 61440 + (32 * ks + 8 * g + q) * 384 + (t < 2 ? 32 * wave + 8 * pp + 4 * t : 128 + 16 * wave + 4 * pp) * 2;
            const v2u lo = trread(a), hi = trread(a + 4 * 384);
            const int r = (t * 20 + m * 5 + ks) * 4;
            W240_SET(wa, r, 0, lo[0]); W240_SET(wa, r, 1, lo[1]); W240_SET(wa, r, 2, hi[0]); W240_SET(wa, r, 3, hi[1]);
          }
      });
  }
  TS(lane == 0 && wave == 0, 256 + 160 + type * 32 + head)
  const unsigned long long parmask = wait_init(ctrl, tag, lane);
  const int k0 = type ? 0 : HS;
  if (wave < 2) wait_tag(ctrl, C_LNF + (type ? wave * 64 + lane : (24 + HS) * 4 + (lane & 31)) * 8, tag);
  BAR();
  const rsrc_t rh = mkrsrc(p.ws + HF_OFF, (unsigned)HF_BYTES);
  auto first_rows = [&](int seg) {
#pragma unroll
    for (int k = 0; k < 20; ++k) dma16_sc1(rh, wave * 10240 + lane * 16, (seg + k / 10) * 40960 + (k % 10) * 1024, (k / 10) * 40960 + wave * 10240 + (k % 10) * 1024);
  };
  first_rows(k0);
  v8u vc, sr; v4u sc;
  vc[0] = lane * 16; vc[1] = wave * 10240 + lane * 16;
  vc[2] = (head * 4 + wave) * (type ? 16384 : 4096) + lane * 16;
  vc[3] = type ? so_near(parmask, 3 * (lane % RING) + 2) ^ (type == 1 ? 0 : 0x2000) : (QB << 21) + (lane & 7) * 524288;
  vc[4] = wave || lane ? OOB : type == 1 ? so_near(parmask, XB) + lmap(N_PP) + (head & 3) * 128 : (int)CTRL_OFF + ((type ? C_VP : C_QP) + head * 32) * 4;
  vc[5] = type == 1 ? vc[2] + 3072 : type && head < 30 ? lmap((head * 4 + wave) * 4096) + lane * 16 : OOB;
  vc[6] = so_hand(parmask, lane); vc[7] = 0;
  set_rsrc(sr, 0, p.ws + HF_OFF, (unsigned)HF_BYTES); set_rsrc(sr, 1, p.ws, (unsigned)HAND_BYTES + CTRL_BYTES);
  sc[1] = wave * 10240; sc[2] = 0; sc[3] = type == 1 ? 0x2200 : 0;
#pragma unroll 1
  for (int k = k0; k < NSEG; k += CHUNK) {
    if (wave == 3 && type && k + CHUNK > RING) wait_ge<8>(ctrl, C_OP + head * 32, max(k + CHUNK - RING - 2, 1) + STAG * (2 * (2 - type) + (head & 1)));
    if (wave == 2 && k) wait_tag(ctrl, C_LNF + (24 + min(k + 2 + (lane >> 2) % CHUNK, NSEG - 1)) * 32 + (lane & 3) * 8, tag);
    sc[0] = k;
    TS(lane == 0 && wave == 0 && head == 0, 3100 + type * 16 + k / CHUNK)
    if (type == 1) asm volatile(PROJK_ASM : "+{s[52:55]}"(sc) : "{v[0:7]}"(vc), "{s[36:43]}"(sr), W240_IN(wa) : "memory", "vcc", "scc", PROJK_CLOBBERS);
    else asm volatile(PROJV_ASM : "+{s[52:55]}"(sc) : "{v[0:7]}"(vc), "{s[36:43]}"(sr), W240_IN(wa) : "memory", "vcc", "scc", PROJV_CLOBBERS);
  }
  if (type) return;
  lane = fresh_lane();
  wait_tag(ctrl, C_LNF + (wave * 64 + lane) * 8, tag); wait_tag(ctrl, C_LNF + (256 + (wave & 1) * 32 + (lane & 31)) * 8, tag);
  if (wave == 0) wait_ge<8>(ctrl, nword(parmask, N_CF + head * 128 + 8), 1u);
  BAR();
  first_rows(0);
  TS(lane == 0 && wave == 0 && head == 0, 3100)
  const int g2 = lane >> 4, n = lane & 15, qt = wave & 1, lq = L_MISC + (wave >> 1) * 12288;
  v8u va, vb; v12u init;
  OPAQUE(va); OPAQUE(vb);
  va[0] = lane * 16; va[1] = wave * 10240 + lane * 16; va[2] = L_MISC + wave * 3072 + lane * 16;
  va[3] = lq + qt * 1024 + lane * 16; va[4] = lq + 2048 + lane * 16 + qt * 8; va[5] = head * 65536 + lane * 16;
  va[6] = lmap(head * 4096) + (g2 * 8 + (n & 7)) * 16; va[7] = lmap(head * 4096) + 512 + (g2 * 8 + (n & 7)) * 8;
  vb[0] = so_near(parmask, 3 * (lane % RING) + 2); vb[1] = so_hand(parmask, lane) ^ 0x2000; vb[2] = so_far(parmask, 3 * (lane % RING));
  vb[3] = lmap(PR_OFF) + (head >> 2) * 32768 + ((head & 3) * 2 + qt) * 1024 + lane * 16;
  vb[4] = g2 < 2 ? lmap(PR_OFF) + (head >> 2) * 32768 + 16384 + qt * 1024 + (head & 3) * 256 + n * 16 + g2 * 8 : OOB;
  vb[5] = wave || lane ? OOB : (int)CTRL_OFF + (C_OP + head * 32) * 4;
  const unsigned neg = f2u(-1e30f);
#pragma unroll
  for (int r = 0; r < 4; ++r) { init[r] = g2 < 2 ? 0u : neg; init[4 + r] = 4 * g2 + r <= n + 16 * qt ? 0u : neg; init[8 + r] = 4 * g2 + r + 16 <= n + 16 * qt ? 0u : neg; }
  asm volatile(PROJQ_ASM :: "{v[0:7]}"(va), "{v8}"(so_near(parmask, XB) + lmap(N_CF) + head * 128 + 8), "{v[124:131]}"(vb), "{v[132:143]}"(init), "{s[36:43]}"(sr),
               "{s53}"(wave * 10240), "{s54}"(wave >> 1), W240_IN(wa) : "memory", "vcc", "scc", PROJQ_CLOBBERS);
}

constexpr int L_P = 16384;
struct Row { v4u a; unsigned b; __device__ __forceinline__ unsigned operator[](int i) const { return i < 4 ? a[i] : b; } };
__device__ __forceinline__ Row row_ld(rsrc_t r, int o, int so, int lane) { return Row{ld16s(r, o + lane * 16, so), __builtin_amdgcn_raw_buffer_load_b32(r, o + 1024 + lane * 4, so, 16)}; }
__device__ __forceinline__ void row_st(const unsigned (&x)[5], rsrc_t r, int o, int lane) {
  st16s(v4u{x[0], x[1], x[2], x[3]}, r, o + lane * 16, 0); __builtin_amdgcn_raw_buffer_store_b32(x[4], r, o + 1024 + lane * 4, 0, 16);
}
__device__ __forceinline__ void row_load(const Params& p, rsrc_t rH, int so, int row, int lane, Row (&r)[9]) {
  r[8] = row_ld(mkrsrc(p.x, S * DM * 2), row * 1280, 0, lane);
#pragma unroll
  for (int k = 0; k < 8; ++k) r[k] = row_ld(rH, ((row & 31) / 6) * 16384 + ((row & 31) % 6) * 1280, so + k * 98304, lane);
}
__device__ __forceinline__ void row_finish(const Params& p, int row, int lane, const Row (&r)[9]) {
  const rsrc_t rT = mkrsrc(p.ws + X1_OFF, (unsigned)(WS_END - X1_OFF)), rO = mkrsrc(p.out, S * DM * 2);
  Row pr[5];
#pragma unroll
  for (int k = 0; k < 5; ++k) pr[k] = Row{lds128(L_PR + k * 1280 + lane * 16), *(LAS unsigned*)(L_PR + k * 1280 + 1024 + lane * 4)};
  unsigned ul = 0x3f80u, uh = 0x3f800000u;
  asm("" : "+s"(ul), "+s"(uh));
  const bf2 lo = __builtin_bit_cast(bf2, ul), hi = __builtin_bit_cast(bf2, uh);
  unsigned xs[5], oh[5], o[5];
  float x1[10], sum = 0.f;
#pragma unroll
  for (int i = 0; i < 5; ++i) {
    float a = 0.f, c = 0.f;
#pragma unroll
    for (int k = 0; k < 8; ++k) { const unsigned w = r[k][i]; a = __builtin_amdgcn_fdot2_f32_bf16(__builtin_bit_cast(bf2, w), lo, a, false); c = __builtin_amdgcn_fdot2_f32_bf16(__builtin_bit_cast(bf2, w), hi, c, false); }
    const unsigned yb = pack2(a, c), ga = pack2(lo2f(pr[0][i]) * lo2f(yb), hi2f(pr[0][i]) * hi2f(yb));
    xs[i] = pack2(lo2f(r[8][i]) + lo2f(ga), hi2f(r[8][i]) + hi2f(ga));
    x1[2 * i] = lo2f(xs[i]); x1[2 * i + 1] = hi2f(xs[i]);
    sum += x1[2 * i] + x1[2 * i + 1];
  }
  const float mean = wave_sum(sum) * (1.f / DM);
  float sq = 0.f;
#pragma unroll
  for (int i = 0; i < 10; ++i) sq = fmaf(x1[i] - mean, x1[i] - mean, sq);
  const float mb = rbf(mean), isd = __builtin_amdgcn_rcpf(rbf(__builtin_amdgcn_sqrtf(rbf(rbf(wave_sum(sq) * (1.f / DM)) + 1e-5f))));
  float rs = 0.f;
#pragma unroll
  for (int i = 0; i < 5; ++i) {
    const unsigned d = pack2(x1[2 * i] - mb, x1[2 * i + 1] - mb), nn = pack2(lo2f(d) * isd, hi2f(d) * isd);
    const unsigned m = pack2(lo2f(nn) * lo2f(pr[2][i]), hi2f(nn) * hi2f(pr[2][i]));
    oh[i] = pack2(lo2f(m) + lo2f(pr[1][i]), hi2f(m) + hi2f(pr[1][i]));
    const float h0 = lo2f(oh[i]), h1 = hi2f(oh[i]);
    rs = fmaf(h0, lo2f(pr[4][i]), fmaf(h1, hi2f(pr[4][i]), rs));
    const unsigned gh = pack2(lo2f(pr[3][i]) * h0, hi2f(pr[3][i]) * h1);
    o[i] = pack2(x1[2 * i] + lo2f(gh), x1[2 * i + 1] + hi2f(gh));
  }
  rs = wave_sum(rs);
  __builtin_amdgcn_raw_buffer_store_b32(f2u(rbf(rs)), rT, lane == 0 ? row * 4 : OOB, 2 * (int)HF_BYTES, 16);
  row_st(oh, rT, row * 2560 + 1280, lane); row_st(xs, rT, row * 2560, lane); row_st(o, rO, row * 1280, lane);
}
template <bool LAST>
__device__ __forceinline__ void tok_unit(const Params& p, unsigned long long parmask, int s, int hq, int wave, v4u (&pre)[4][4]) {
  gu32* ctrl = (gu32*)(p.ws + CTRL_OFF);
  const rsrc_t rH = mkrsrc(p.ws, (unsigned)HAND_BYTES + CTRL_BYTES);
  const int lane = fresh_lane(), g = lane >> 4, n = lane & 15, h = hq * 4 + wave, b = 3 * (s % RING);
  const int sH = so_near(parmask, b), sV = so_near(parmask, b + 1) ^ TFAR, sK = so_near(parmask, b + 2), sP = so_far(parmask, 3 * ((s + RING - 4) % RING));
  const int k4 = (lane >> 2) & 3;
  if (wave == 0)
    wait_ge<LAST ? 32 : 8>(ctrl, k4 >= 2 ? C_AF + max(s + 4 - RING, 0) * 32 : !LAST ? (k4 ? C_WP : C_OP) + (hq * 4 + g) * 32 : k4 ? C_QP + (hq * 4 + g) * 32 : nword(parmask, N_PP + s * 512 + (lane & 3) * 128),
                           k4 >= 2 ? (s + 4 < RING ? 0 : 8) : !LAST ? s + 1 + 4 * k4 : k4 ? 8 : 16);
  BAR();
  TS(lane == 0 && wave == 0 && hq == 0, 1024 + s * 8)
  v4u um[10], vo[10][4], pb[2][5];
  Row ri[9];
  const int row = s < 4 ? 0x80000 : (s - 4) * 32 + h;
  auto load_um = [&](int c) {
    um[c] = c < 8 ? UML(rH, lmap(P_OFF + (16 * wave + (n >> 3)) * 4096 + (hq * 4 + g) * 128 + (n & 7) * 16), so_hand(parmask, s) + c * 16384)
                  : UML(rH, lmap(P2_OFF + (16 * wave + (n >> 1)) * 1024 + (hq * 4 + g) * 32 + (n & 1) * 16), so_hand(parmask, s) + (c - 8) * 16384);
  };
  const int vm = (XP & 1) ? OOB : lmap((16 * wave + (n >> 3)) * 16384 + hq * 2048 + g * 128 + (n & 7) * 16), vt = lmap(V2_OFF + (16 * wave + (n >> 1)) * 4096 + hq * 512 + g * 32 + (n & 1) * 16);
  auto load_vo = [&](int c0, int c1) {
#pragma unroll
    for (int c = c0; c < c1; ++c) {
      if (LAST || c >= 4)
#pragma unroll
      for (int k = 0; k < 4; ++k) vo[c][k] = __builtin_amdgcn_raw_buffer_load_b128(rH, c < 8 ? vm + k * 512 : vt + k * 128, c < 8 ? sV + c * 65536 : (sH ^ TFAR) + (c - 8) * 65536, LAST ? LPOL : TPOL);
      if (!LAST) load_um(c);
    }
  };
  auto load_pre = [&](int so, int c0) {
#pragma unroll
    for (int i = 0; i < 8; ++i) pre[c0 + (i >> 2)][i & 3] = __builtin_amdgcn_raw_buffer_load_b128(rH, vm + (i & 3) * 512, so + (c0 + (i >> 2)) * 65536, TPOL);
  };
  if (LAST) {
    v4u qf[2][6], kf[2][6], qx[4], kx[4], km[6];
    const int sQ = (QB << 21) + (s - HS) * 524288;
#pragma unroll
    for (int w = 0; w < 4; ++w) {
#pragma unroll
      for (int t = 0; t < 2; ++t) { qf[t][w] = ld16s(rH, h * 16384 + w * 4096 + t * 1024 + lane * 16, sQ); kf[t][w] = ld16s(rH, h * 65536 + w * 16384 + t * 1024 + lane * 16, sK); }
      qx[w] = ld16s(rH, h * 16384 + w * 4096 + 2048 + lane * 16, sQ); kx[w] = ld16s(rH, h * 65536 + w * 16384 + 2048 + lane * 16, sK);
    }
    load_vo(0, 10);
    __builtin_amdgcn_sched_barrier(0);
#pragma unroll
    for (int w = 0; w < 4; ++w)
#pragma unroll
      for (int t = 0; t < 2; ++t)
#pragma unroll
        for (int k = 0; k < 2; ++k) { qf[t][4 + (w >> 1)][2 * (w & 1) + k] = qx[w][2 * t + k]; kf[t][4 + (w >> 1)][2 * (w & 1) + k] = kx[w][2 * t + k]; }
    f4 sc[2][3] = {};
#pragma unroll
    for (int f = 0; f < 6; ++f) { sc[0][1] = mfma32(kf[0][f], qf[0][f], sc[0][1]); sc[1][1] = mfma32(kf[0][f], qf[1][f], sc[1][1]); sc[1][2] = mfma32(kf[1][f], qf[1][f], sc[1][2]); }
    if (wave == 0) wait_ge<LPOLL>(ctrl, nword(parmask, N_CF + (hq * 4 + g) * 128 + (s >= NSEG - 2 ? min(lane & 3, 2) : 2) * 4), min(s + 2, NSEG));
    BAR();
#pragma unroll
    for (int w = 0; w < 4; ++w) {
      km[w] = ld16s(rH, lmap(h * 4096) + (g * 8 + (n & 7)) * 16 + w * 1024, so_hand(parmask, s) ^ 0x2000);
      const v2u a = ld8s(rH, lmap(h * 4096) + 512 + (g * 8 + (n & 7)) * 8 + w * 1024, so_hand(parmask, s) ^ 0x2000);
      km[4 + (w >> 1)][2 * (w & 1)] = a[0]; km[4 + (w >> 1)][2 * (w & 1) + 1] = a[1];
    }
#pragma unroll
    for (int c = 0; c < 10; ++c) load_um(c);
    __builtin_amdgcn_sched_barrier(0);
    TS(lane == 0 && wave == 0 && hq == 0, 1025 + s * 8)
#pragma unroll
    for (int f = 0; f < 6; ++f) { sc[0][0] = mfma32(km[f], qf[0][f], sc[0][0]); sc[1][0] = mfma32(km[f], qf[1][f], sc[1][0]); }
#pragma unroll
    for (int t = 0; t < 2; ++t) {
      float e[12], mx = -3e38f, sum = 0.f;
#pragma unroll
      for (int k = 0; k < 8 + 4 * t; k += 2) {
        const unsigned u = pack2(sc[t][k >> 2][k & 3], sc[t][k >> 2][(k & 3) + 1]), w = pack2(lo2f(u) * 0.07216878364870322f, hi2f(u) * 0.07216878364870322f);
#pragma unroll
        for (int i = 0; i < 2; ++i) {
          const bool on = k < 4 ? g < 2 : (k >> 2) - 1 < t || 4 * g + (k & 3) + i <= n;
          e[k + i] = on ? (i ? hi2f(w) : lo2f(w)) : -3e38f;
          mx = fmaxf(mx, e[k + i]);
        }
      }
      mx = max_x32(max_x16(mx));
#pragma unroll
      for (int k = 0; k < 8 + 4 * t; ++k) { e[k] = __builtin_amdgcn_exp2f((e[k] - mx) * 1.4426950408889634f); sum += e[k]; }
      sum = swapsum16(sum, sum); sum = swapsum32(sum, sum);
      const float r = __builtin_amdgcn_rcpf(sum);
      *(LAS v4u*)(L_P + (wave * 2 + t) * 1024 + lane * 16) = v4u{pack2(e[4] * r, e[5] * r), pack2(e[6] * r, e[7] * r), t ? pack2(e[8] * r, e[9] * r) : 0u, t ? pack2(e[10] * r, e[11] * r) : 0u};
      *(LAS v2u*)(L_P + 8192 + (g >> 1) * 2048 + t * 1024 + wave * 256 + n * 16 + (g & 1) * 8) = v2u{pack2(e[0] * r, e[1] * r), pack2(e[2] * r, e[3] * r)};
    }
    LGKM0();
  } else {
#pragma unroll
    for (int k = 0; k < 3; ++k) dma16_sc1(rH, lmap(PR_OFF) + hq * 32768 + lane * 16, (sH ^ 0x2000) + (k < 2 ? k * 4096 : 16384) + wave * 1024, L_P + k * 4096 + wave * 1024);
    if (s < 4) { load_pre(sV, 0); load_pre(sV, 2); VMCNT(0); }
    load_vo(0, 6);
    __builtin_amdgcn_sched_barrier(0);
    VMCNT(14);
#pragma unroll
    for (int i = 0; i < 16; ++i) vo[i >> 2][i & 3] = pre[i >> 2][i & 3];
  }
  BAR();
#pragma unroll
  for (int k = 0; k < 10; ++k) pb[k & 1][k >> 1] = lds128(L_P + k * 1024 + lane * 16);
  TS(lane == 0 && wave == 0 && hq == 0, 1026 + s * 8)
  int so[2];
#pragma unroll
  for (int t = 0; t < 2; ++t) so[t] = hq * 98304 + ((16 * t + n) / 6) * 16384 + ((16 * t + n) % 6) * 1280 + (g & 1) * 32 + (g >> 1) * 16;
  f4 d[2][2][2];
  unsigned af = 0;
#pragma unroll
  for (int c = 0; c < 12; c += 2) {
    if (!LAST) {
      if (c == 2) af = __hip_atomic_load(ctrl + C_AF + max(s - 4, 0) * 32, RLX_AGENT);
      if (c == 2 || c == 4) load_vo(c + 4, c + 6);
      if (c == 6) {
        if (s >= 4 && rfl(af) < 8) wait_ge<2>(ctrl, C_AF + (s - 4) * 32, 8u);
        row_load(p, rH, sP, row, lane, ri);
      }
      __builtin_amdgcn_sched_barrier(0);
    }
    if (c < 10) {
#pragma unroll
      for (int k = 0; k < 4; ++k) asm volatile("" : "+a"(vo[c][k]), "+a"(vo[c + 1][k]));
      asm volatile("" : "+a"(um[c]), "+a"(um[c + 1]));
#pragma unroll
      for (int t = 0; t < 2; ++t)
#pragma unroll
        for (int k = 0; k < 5; ++k)
#pragma unroll
          for (int u = 0; u < 2; ++u) d[(c >> 1) & 1][t][u] = mfma32(k < 4 ? vo[c + u][k] : um[c + u], pb[t][k], k ? d[(c >> 1) & 1][t][u] : f4{});
    }
    if (c > 0)
#pragma unroll
      for (int t = 0; t < 2; ++t) {
        const f4 x = d[~(c >> 1) & 1][t][0], y = d[~(c >> 1) & 1][t][1];
        auto lo = __builtin_amdgcn_permlane16_swap(pack2(x[0], x[1]), pack2(y[0], y[1]), false, false);
        auto hi = __builtin_amdgcn_permlane16_swap(pack2(x[2], x[3]), pack2(y[2], y[3]), false, false);
        st16s(v4u{lo[0], hi[0], lo[1], hi[1]}, rH, so[t] + (c < 10 ? 32 * (c - 2) : 1024), (sH ^ 0x2000) + (c < 10 ? 256 : 64) * wave);
      }
    __builtin_amdgcn_sched_barrier(0);
  }
  TS(lane == 0 && wave == 0 && hq == 0, 1027 + s * 8)
  const bool rel = !LAST && s + RING < NSEG;
  if (!LAST) {
    const int sN = s + 4 < HS ? so_near(parmask, 3 * ((s + 4) % RING) + 1) ^ TFAR : OOB;
    load_pre(sN, 0);
    __builtin_amdgcn_sched_barrier(0);
    row_finish(p, row, lane, ri);
    __builtin_amdgcn_sched_barrier(0);
    load_pre(sN, 2);
    __builtin_amdgcn_sched_barrier(0);
    VMCNT(23);
  } else VMCNT(0);
  BAR();
  if (wave == 0 && lane < (rel ? 2 : 1)) __hip_atomic_fetch_add(ctrl + (lane ? C_CL + (s + RING) * 32 : C_AF + s * 32), 1u, RLX_AGENT);
  TS(lane == 0 && wave == 0 && hq == 0, 1028 + s * 8)
}

__device__ __forceinline__ void mod_gather(const Params& p, int n0, int n1, int tid, unsigned tag) {
  const rsrc_t rm = mkrsrc(p.ws + CTRL_OFF + C_MOD * 4, 15360);
  if (tid < 80) *(LAS v4u*)(LP + 7680 + tid * 16) = ((const v4u*)p.wr)[tid];
#pragma unroll 1
  for (int n = n0; n < n1; ++n)
#pragma unroll 1
    for (int spin = 0; spin <= SPIN_CAP; ++spin) {
      const int o = min(n * 256 + tid, 959);
      const v4u a = ld16s(rm, o * 16, 0);
      *(LAS v2u*)(LP + o * 8) = v2u{a[0], a[2]};
      if (__builtin_amdgcn_ballot_w64(a[1] != tag || a[3] != tag) == 0) break;
      __builtin_amdgcn_s_sleep(8);
    }
  LGKM0(); BAR();
}
__device__ __forceinline__ void tok_role(const Params& p, int j, int lane, int wave, unsigned tag) {
  gu32* ctrl = (gu32*)(p.ws + CTRL_OFF);
  const rsrc_t rH = mkrsrc(p.ws, (unsigned)HAND_BYTES + CTRL_BYTES);
  const int tid = wave * 64 + lane, hq = j & 7, grp = j >> 3;
  const rsrc_t rm = mkrsrc(p.ws + CTRL_OFF + C_MOD * 4, 15360);
  {
    v4u a[10], own[3];
    float mb, isd;
    mod_unit(p, j < 28 ? j : 60, tid, tag, [&] {
#pragma unroll
      for (int i = 0; i < 10; ++i) a[i] = ln1_piece(p, j * 8, i, lane);
#pragma unroll
      for (int i = 0; i < 3; ++i) own[i] = ln1_piece(p, j * 8, min(wave + 4 * i, 9), lane);
    });
    TS(tid == 0, 896 + j)
    ln1_stats(a, mb, isd);
#pragma unroll
    for (int i = 0; i < 3; ++i)
#pragma unroll
      for (int k = 0; k < 4; ++k) own[i][k] = ln_norm(own[i][k], mb, isd);
    v4u g[3][4];
#pragma unroll 1
    for (int spin = 0; spin <= SPIN_CAP; ++spin) {
      bool bad = false;
#pragma unroll
      for (int i = 0; i < 3; ++i)
#pragma unroll
        for (int k = 0; k < 4; ++k) g[i][k] = ld16s(rm, (lane >> 3) * 32 + (k & 1) * 16 + (k >> 1) * 2560, min(wave + 4 * i, 9) * 256);
#pragma unroll
      for (int i = 0; i < 12; ++i) bad |= g[i >> 2][i & 3][1] != tag || g[i >> 2][i & 3][3] != tag;
      if (__builtin_amdgcn_ballot_w64(bad) == 0) break;
      __builtin_amdgcn_s_sleep(2);
    }
    TS(tid == 0, 928 + j)
#pragma unroll
    for (int i = 0; i < 3; ++i) {
#pragma unroll
      for (int k = 0; k < 4; ++k) own[i][k] = ln_mod(own[i][k], g[i][2 + (k >> 1)][2 * (k & 1)], g[i][k >> 1][2 * (k & 1)]);
      if (wave + 4 * i < 10) ln1_store(p, own[i], j * 8, wave + 4 * i, lane);
    }
    VMCNT(0);
    if (lane == 0) __hip_atomic_store(ctrl + C_LNF + j * 32 + wave * 8, tag, RLX_AGENT);
    TS(tid == 0, 800 + j)
  }
  mod_gather(p, 0, 2, tid, tag);
  const unsigned long long parmask = wait_init(ctrl, tag, lane);
#pragma unroll 1
  for (int seg = j < 24 ? 8 + j : 32 + j; seg < (j < 24 ? HS : NSEG); seg += 24) {
    const int row0 = seg * 32 + wave * 8, lane = fresh_lane();
    v4u a[10];
#pragma unroll
    for (int i = 0; i < 10; ++i) a[i] = ln1_piece(p, row0, i, lane);
    float mb, isd;
    ln1_stats(a, mb, isd);
#pragma unroll
    for (int i = 0; i < 10; ++i) {
      const v4u sh = lds128(LP + (lane >> 3) * 16 + i * 128), sc = lds128(LP + 1280 + (lane >> 3) * 16 + i * 128);
#pragma unroll
      for (int k = 0; k < 4; ++k) a[i][k] = ln_mod(ln_norm(a[i][k], mb, isd), sc[k], sh[k]);
      ln1_store(p, a[i], row0, i, lane);
    }
    VMCNT(0);
    if (lane == 0) __hip_atomic_store(ctrl + C_LNF + (24 + seg) * 32 + wave * 8, tag, RLX_AGENT);
  }
  mod_gather(p, 2, 4, tid, tag);
  TS(tid == 0, 832 + j)
  v4u pre[4][4] = {};
#pragma unroll 1
  for (int s = grp; s < HS; s += 4) tok_unit<false>(p, parmask, s, hq, wave, pre);
}
__device__ __forceinline__ void last_row(const Params& p, unsigned long long parmask, int s, int hq, int wave, unsigned tag) {
  gu32* ctrl = (gu32*)(p.ws + CTRL_OFF);
  const int lane = fresh_lane(), row = s * 32 + hq * 4 + wave;
  wait_ge<1>(ctrl, C_AF + s * 32, 8u);
  Row ri[9];
  row_load(p, mkrsrc(p.ws, (unsigned)HAND_BYTES), so_far(parmask, 3 * (s % RING)), row, lane, ri);
  row_finish(p, row, lane, ri);
  VMCNT(0);
  unsigned t = 0;
  if (lane == 0) t = __hip_atomic_fetch_add(ctrl + C_FIN, 1u, RLX_AGENT);
  if (rfl(t) == 3 * 128 - 1) {
    TS(lane == 0, 3074)
    select_rows(ctrl, mkrsrc(p.ws + X1_OFF, (unsigned)(WS_END - X1_OFF)), lane);
    TS(lane == 0, 3075)
    if (lane == 0) __hip_atomic_store(ctrl + C_EPOCH, tag, RLX_AGENT);
  }
}

__global__ void __launch_bounds__(256, 1) k_main(Params p) {
  extern __shared__ __attribute__((aligned(16))) unsigned char smem[];
  const int tid = threadIdx.x, wave = rfl(tid >> 6), blk = blockIdx.x;
  {
    unsigned long long pc;
    asm volatile("s_getpc_b64 s[40:41]\n\ts_add_u32 s40, s40, _Z6k_main6Params@rel32@lo+4\n\ts_addc_u32 s41, s41, _Z6k_main6Params@rel32@hi+12" : "={s[40:41]}"(pc) :: "scc");
    dma16(mkrsrc((const void*)pc, CODE_BYTES), (((blk >> 3) % 5) * 256 + tid) * 128, 0, ((blk & 7) < 3 ? 12288 : L_MISC + 1024) + wave * 1024);
  }
  const unsigned tag = (*(const __attribute__((address_space(4))) unsigned*)uni64((unsigned long long)(p.ws + CTRL_OFF)) + 4u) & ~3u;
  const int x = blk & 7, t = blk >> 3;
  const int idx = (x - 3) * 32 + t, role = x < 3 ? x * 32 + t : 96 + (idx % 5) * 32 + idx / 5;
  TS(tid == 0, role)
  if (role < 128) {
    if (role < 96) chain_role(p, role, fresh_lane(), wave, tag);
    else vo_role(p, role - 96, fresh_lane(), wave, tag);
    const __attribute__((address_space(4))) Params* pk = (const __attribute__((address_space(4))) Params*)__builtin_amdgcn_kernarg_segment_ptr();
    asm volatile("" : "+s"(pk) :: "memory");
    const int ln = fresh_lane();
    Params p2 = {};
    p2.wg = pk->wg; p2.wu = pk->wu; p2.wd = pk->wd; p2.out = pk->out; p2.ws = pk->ws;
    ffn_role(p2, role, ln, wave);
  }
  else {
    if (role < 160) tok_role(p, role - 128, fresh_lane(), wave, tag);
    else proj_role(p, (role - 160) >> 5, role & 31, blk, fresh_lane(), wave, tag);
    if (role < 160 || role >= 192) {
      const unsigned long long parmask = wait_init((gu32*)(p.ws + CTRL_OFF), tag, fresh_lane());
      const int s = (role < 160 ? HS - 4 : role < 224 ? HS + 4 : HS) + ((role >> 3) & 3);
      if (role >= 192) { mod_gather(p, 1, 4, wave * 64 + fresh_lane(), tag); v4u pre[4][4]; tok_unit<true>(p, parmask, s, role & 7, wave, pre); }
      last_row(p, parmask, s, role & 7, wave, tag);
    }
  }
  TS(wave == 0 && fresh_lane() == 0, 2048 + role)
}

#define SCHED_DB 82
#define SCHED_DM 73
#define SCHED_DS 115
#define SCHED_LEAD 250

extern "C" void kernel_launch(void* const* d_in, const int* in_sizes, int n_in, void* d_out, int out_size, void* d_ws, size_t ws_size,
                              hipStream_t stream) {
  Params p;
  p.x = (const u16*)d_in[0]; p.mem0 = (const u16*)d_in[1]; p.wq = (const u16*)d_in[2]; p.wk = (const u16*)d_in[3];
  p.wv = (const u16*)d_in[4]; p.wo = (const u16*)d_in[5]; p.wr = (const u16*)d_in[8]; p.wg = (const u16*)d_in[9];
  p.wu = (const u16*)d_in[10]; p.wd = (const u16*)d_in[11]; p.c = (const u16*)d_in[12]; p.wmod = (const u16*)d_in[13];
  p.out = (u16*)d_out;
  p.ws = (unsigned char*)(((uintptr_t)d_ws + 2 * MB - 1) & ~(uintptr_t)(2 * MB - 1));
  p.dB = SCHED_DB; p.dM = SCHED_DM; p.dS = SCHED_DS; p.lead = SCHED_LEAD;
  (void)hipFuncSetAttribute((const void*)k_main, hipFuncAttributeMaxDynamicSharedMemorySize, MAIN_LDS);
  k_main<<<256, 256, MAIN_LDS, stream>>>(p);
}
```
